```python
import torch

def _softmax(x, axis=-1):
    return torch.softmax(x, dim=axis)
def _gelu(x):
    return torch.nn.functional.gelu(x, approximate="tanh")
def _silu(x):
    return torch.nn.functional.silu(x)
def _sigmoid(x):
    return torch.sigmoid(x)
def _layernorm(x, axis=-1, eps=1e-5):
    m = x.mean(dim=axis, keepdim=True)
    v = x.var(dim=axis, keepdim=True, unbiased=False)
    return (x - m) / torch.sqrt(v + eps)
def _scatter_add(tgt, idx, src, axis=0):
    return tgt.clone().index_add_(axis, idx, src)
def _segment_sum(src, seg, n):
    out = torch.zeros((n,) + src.shape[1:], dtype=src.dtype)
    return out.index_add_(0, seg, src)
def _pad(x, pw):
    flat = tuple(v for pair in reversed(pw) for v in pair)
    return torch.nn.functional.pad(x, flat)

kernel_name = 'recurrent_scan__mha_block_cached__moe_arch_mod__prompt'

def setup_inputs():

    _w = (lambda a: (lambda t: t.to(torch.bfloat16) if t.is_floating_point() else t)(torch.as_tensor(a)))
    def _A_setup():
        B, Sq, D = 1, 2048, 640
        H, Sk, Dh = 32, 2048, 128
        return {
            "x": _w((lambda *s: torch.randn(*s, dtype=torch.bfloat16) * 0.05)(B, Sq, D)),
            "k_cache": _w((lambda *s: torch.randn(*s, dtype=torch.bfloat16) * 1.6)(B, H, Sk, Dh)),
            "v_cache": _w((lambda *s: torch.randn(*s, dtype=torch.bfloat16) * 0.2)(B, H, Sk, Dh)),
            "wq": _w((lambda *s: torch.randn(*s, dtype=torch.bfloat16) * 1.6)(D, H * Dh) * (D ** -0.5)),
            "wo": _w((lambda *s: torch.randn(*s, dtype=torch.bfloat16) * 0.2)(H * Dh, D) * ((H * Dh) ** -0.5)),
        }
    _A_in = _A_setup()

    def _B_setup():
        B, S, D, F, C = 1, 2048, 640, 1728, 8
        return {
            "x": _w((lambda *s: torch.randn(*s, dtype=torch.bfloat16) * 0.05)(B, S, D)),
            "w_r": _w((lambda *s: torch.randn(*s, dtype=torch.bfloat16) * 0.2)(D)),
            "w_gate": _w((lambda *s: torch.randn(*s, dtype=torch.bfloat16) * 0.1)(D, F)),
            "w_up": _w((lambda *s: torch.randn(*s, dtype=torch.bfloat16) * 0.1)(D, F)),
            "w_down": _w((lambda *s: torch.randn(*s, dtype=torch.bfloat16) * 0.1)(F, D)),
        }
    _B_in = _B_setup()

    return {
        "A_x": _A_in["x"],
        "A_k_cache": _A_in["k_cache"],
        "A_v_cache": _A_in["v_cache"],
        "A_wq": _A_in["wq"],
        "A_wo": _A_in["wo"],
        "B_x": _B_in["x"],
        "B_w_r": _B_in["w_r"],
        "B_w_gate": _B_in["w_gate"],
        "B_w_up": _B_in["w_up"],
        "B_w_down": _B_in["w_down"],
    }

def reference(A_x, A_k_cache, A_v_cache, A_wq, A_wo, B_x, B_w_r, B_w_gate, B_w_up, B_w_down):
    def _A_ref(x, k_cache, v_cache, wq, wo):
        B, Sq, D = x.shape
        H, Dh = 32, 128
        q = (lambda x, *d: x.permute(*d))((x @ wq).reshape(B, Sq, H, Dh), 0, 2, 1, 3)
        scores = torch.einsum("bhqd,bhkd->bhqk", q, k_cache) * (Dh ** -0.5)
        p = _softmax(scores, axis=-1)
        o = torch.einsum("bhqk,bhkd->bhqd", p, v_cache)
        return (lambda x, *d: x.permute(*d))(o, 0, 2, 1, 3).reshape(B, Sq, H * Dh) @ wo

    def _B_ref(x, w_r, w_gate, w_up, w_down):
        B, S, D = x.shape
        C = 8
        r = x @ w_r
        idx = torch.argsort(-r, axis=-1)[:, :C]
        idx_e = (lambda x, n, axis: torch.repeat_interleave(x, n, dim=axis))(idx.reshape(B, C, 1), D, axis=2)
        x_sel = (lambda x, i, axis: torch.take_along_dim(x, i, dim=axis))(x, idx_e, axis=1)
        g = _silu(x_sel @ w_gate)
        u = x_sel @ w_up
        y = (g * u) @ w_down
        r_sel = _sigmoid((lambda x, i, axis: torch.take_along_dim(x, i, dim=axis))(r, idx, axis=1))
        y = y * r_sel.reshape(B, C, 1)
        b_off = (torch.arange(B) * S).reshape(B, 1)
        flat_idx = (idx + b_off).reshape(B * C)
        zero = (x * 0.0).reshape(B * S, D)
        upd = _scatter_add(zero, flat_idx, y.reshape(B * C, D), axis=0)
        return x + upd.reshape(B, S, D)

    x = A_x
    xn = _layernorm(x, axis=-1)
    s = _A_ref(xn, A_k_cache, A_v_cache, A_wq, A_wo)
    g = _silu(_B_ref(xn, B_w_r, B_w_gate, B_w_up, B_w_down))
    return x + s * g
```

```cpp
#include <hip/hip_runtime.h>
#include <stdint.h>

using f32x4  = __attribute__((ext_vector_type(4))) float;
using u32x4  = __attribute__((ext_vector_type(4))) unsigned;
using u32x2  = __attribute__((ext_vector_type(2))) unsigned;

constexpr int SEQ = 2048, DM = 640, NH = 32, DH = 128, HD = NH * DH, FF = 1728, TOPC = 8;

constexpr unsigned WS_FO  = 0;
constexpr unsigned WS_EP  = 32768;
constexpr unsigned WS_G1  = 36864;
constexpr unsigned WS_G2  = 53248;
constexpr unsigned WS_FH  = 69632;
constexpr unsigned WS_FY  = 73728;
constexpr unsigned WS_FG  = 77824;
constexpr unsigned WS_FR  = 81920;
constexpr unsigned WS_XN  = 131072;
constexpr unsigned WS_OF  = WS_XN + SEQ * DM * 2;
constexpr unsigned WS_H   = WS_OF + (18u << 20);
constexpr unsigned WS_Y   = WS_H + 32768;
constexpr unsigned WS_P   = WS_Y + 1048576;
constexpr unsigned WS_SPAN = 64u << 20;
constexpr int DN_FIRST = 76, YP = 18, SPIN = 30000;

#define CLOB_V "v0","v1","v2","v3","v4","v5","v6","v7","v8","v9","v10","v11","v12","v13","v14","v15","v16","v17","v18","v19","v20","v21","v22","v23","v24","v25","v26","v27","v28","v29","v30","v31","v32","v33","v34","v35","v36","v37","v38","v39","v40","v41","v42","v43","v44","v45","v46","v47","v48","v49","v50","v51","v52","v53","v54","v55","v56","v57","v58","v59","v60","v61","v62","v63","v64","v65","v66","v67","v68","v69","v70","v71","v72","v73","v74","v75","v76","v77","v78","v79","v80","v81","v82","v83","v84","v85","v86","v87","v88","v89","v90","v91","v92","v93","v94","v95","v96","v97","v98","v99","v100","v101","v102","v103","v104","v105","v106","v107","v108","v109","v110","v111","v112","v113","v114","v115","v116","v117","v118","v119","v120","v121","v122","v123","v124","v125","v126","v127","v128","v129","v130","v131","v132","v133","v134","v135","v136","v137","v138","v139","v140","v141","v142","v143","v144","v145","v146","v147","v148","v149","v150","v151","v152","v153","v154","v155","v156","v157","v158","v159","v160","v161","v162","v163","v164","v165","v166","v167","v168","v169","v170","v171","v172","v173","v174","v175","v176","v177","v178","v179","v180","v181","v182","v183","v184","v185","v186","v187","v188","v189","v190","v191","v192","v193","v194","v195","v196","v197","v198","v199","v200","v201","v202","v203","v204","v205","v206","v207","v208","v209","v210","v211","v212","v213","v214","v215","v216","v217","v218","v219","v220","v221","v222","v223","v224","v225","v226","v227","v228","v229","v230","v231","v232","v233","v234","v235","v236","v237","v238","v239","v240","v241","v242","v243","v244","v245","v246","v247","v248","v249","v250","v251","v252","v253","v254","v255"
#define CLOB_A "a0","a1","a2","a3","a4","a5","a6","a7","a8","a9","a10","a11","a12","a13","a14","a15","a16","a17","a18","a19","a20","a21","a22","a23","a24","a25","a26","a27","a28","a29","a30","a31","a32","a33","a34","a35","a36","a37","a38","a39","a40","a41","a42","a43","a44","a45","a46","a47","a48","a49","a50","a51","a52","a53","a54","a55","a56","a57","a58","a59","a60","a61","a62","a63","a64","a65","a66","a67","a68","a69","a70","a71","a72","a73","a74","a75","a76","a77","a78","a79","a80","a81","a82","a83","a84","a85","a86","a87","a88","a89","a90","a91","a92","a93","a94","a95","a96","a97","a98","a99","a100","a101","a102","a103","a104","a105","a106","a107","a108","a109","a110","a111","a112","a113","a114","a115","a116","a117","a118","a119","a120","a121","a122","a123","a124","a125","a126","a127","a128","a129","a130","a131","a132","a133","a134","a135","a136","a137","a138","a139","a140","a141","a142","a143","a144","a145","a146","a147","a148","a149","a150","a151","a152","a153","a154","a155","a156","a157","a158","a159","a160","a161","a162","a163","a164","a165","a166","a167","a168","a169","a170","a171","a172","a173","a174","a175","a176","a177","a178","a179","a180","a181","a182","a183","a184","a185","a186","a187","a188","a189","a190","a191","a192","a193","a194","a195","a196","a197","a198","a199","a200","a201","a202","a203","a204","a205","a206","a207","a208","a209","a210","a211","a212","a213","a214","a215","a216","a217","a218","a219","a220","a221","a222","a223","a224","a225","a226","a227","a228","a229","a230","a231","a232","a233","a234","a235","a236","a237","a238","a239","a240","a241","a242","a243","a244","a245","a246","a247","a248","a249","a250","a251","a252","a253","a254","a255"
#define CLOB_S "s8","s9","s10","s11","s12","s13","s14","s15","s16","s17","s18","s19","s20","s21","s22","s23","s24","s25","s26","s27","s28","s29","s30","s31","s32","s33","s34","s35","s36","s37","s38","s39","s40","s41","s42","s43","s44","s45","s46","s47","s48","s49","s50","s51","s52","s53","s54","s55","s56","s57","s58","s59","s60","s61","s62","s63","s64","s65","s66","s67","s68","s69","s70","s71","s72","s73","s74","s75","s76","s77","s78","s79","s80","s81","s82","s83","s84","s85","s86","s87","s88","s89","s90","s91","s92","s93","s94","s95","s96","s97","s98","s99"
__global__ __launch_bounds__(256, 1) __attribute__((aligned(4096))) void ka_fused(const unsigned short* __restrict__ x, const unsigned short* __restrict__ Kc, const unsigned short* __restrict__ Vc,
                                                   const unsigned short* __restrict__ wq, const unsigned short* __restrict__ wo,
                                                   const unsigned short* __restrict__ wr, const unsigned short* __restrict__ wg, const unsigned short* __restrict__ wu,
                                                   const unsigned short* __restrict__ wd,
                                                   char* __restrict__ ws, unsigned short* __restrict__ out
                                                   ) {
  extern __shared__ __attribute__((aligned(16))) char lds[];
  const unsigned w = __builtin_amdgcn_readfirstlane(threadIdx.x >> 6);
  const unsigned slds = __builtin_amdgcn_readfirstlane((unsigned)(size_t)lds);
  asm volatile(
R"ASM(
.macro MM a, b, c, d:vararg
v_mfma_f32_16x16x32_bf16 \a, \b, \c, \d
.endm
.macro TR a, b:vararg
ds_read_b64_tr_b16 \a, \b
.endm
.macro CV a, b, c:vararg
v_cvt_pk_bf16_f32 \a, \b, \c
.endm
.macro AR a, b:vararg
v_accvgpr_read_b32 \a, \b
.endm
.macro AW a, b:vararg
v_accvgpr_write_b32 \a, \b
.endm
.macro EX a, b:vararg
v_exp_f32 \a, \b
.endm
.macro RD a, b:vararg
ds_read_b128 \a, \b
.endm
s_branch LPG0_%=
.p2align 6
LPG0_%=:
s_load_dwordx2 s[16:17], %[ka], 24
s_load_dwordx2 s[32:33], %[ka], 0
s_load_dwordx2 s[20:21], %[ka], 72
s_load_dwordx2 s[8:9], %[ka], 8
s_load_dwordx2 s[12:13], %[ka], 16
s_load_dwordx2 s[24:25], %[ka], 32
s_load_dwordx2 s[44:45], %[ka], 40
s_load_dwordx2 s[28:29], %[ka], 64
s_load_dwordx2 s[40:41], %[ka], 80
s_lshl_b32 s77, %[swave], 2
s_and_b32 s77, s77, 8
s_add_u32 s77, s77, 48
s_load_dwordx2 s[36:37], %[ka], s77
v_mbcnt_lo_u32_b32 v127, -1, 0
v_mbcnt_hi_u32_b32 v127, -1, v127
v_lshlrev_b32 v248, 4, v127
s_lshr_b32 s77, %[sb], 3
s_lshl_b32 s77, s77, 2
s_add_u32 s77, s77, %[swave]
s_lshl_b32 s77, s77, 10
v_add_u32 v118, s77, v248
s_mov_b32 s77, LKEND_ka-LPC_%=-16
v_min_u32 v118, s77, v118
s_getpc_b64 s[98:99]
LPC_%=:
global_load_dwordx4 a[252:255], v118, s[98:99]
s_and_b32 s77, %[sb], 7
s_lshl_b32 s77, s77, 2
s_bfe_u32 s84, %[sb], 131075
s_add_u32 s49, s77, s84
s_lshr_b32 s50, %[sb], 5
s_lshr_b32 s76, %[sb], 8
s_lshl_b32 s76, s76, 7
s_and_b32 s77, %[sb], 127
s_or_b32 s76, s76, s77
s_lshl_b32 s76, s76, 2
s_add_u32 s76, s76, %[swave]
s_lshl_b32 s76, s76, 1
s_bitcmp1_b32 %[sb], 7
s_cselect_b64 s[52:53], -1, 0
s_and_b32 s54, s52, 1
s_mov_b32 s55, s54
s_and_b32 s54, s52, 0x10001
s_mov_b32 s55, s54
v_lshrrev_b32 v180, 5, v127
s_mul_i32 s82, s76, 2560
v_and_b32 v182, 240, v248
v_lshrrev_b32 v181, 4, v127
v_mul_u32_u24 v168, 1280, v181
v_add_u32 v168, v168, v182
s_lshl_b32 s77, %[swave], 12
s_add_u32 s65, s77, %[slds]
s_lshl_b32 s89, %[swave], 17
v_bfe_u32 v210, v127, 2, 3
v_and_b32 v212, 3, v127
v_lshlrev_b32 v213, 4, v212
v_lshl_add_u32 v213, v180, 6, v213
v_lshl_add_u32 v253, v210, 13, v213
v_add_u32 v254, 128, v253
s_mul_i32 s96, %[swave], 4112
s_add_u32 s96, s96, %[slds]
s_lshl_b32 s77, s50, 2
s_add_u32 s77, s77, %[swave]
s_mul_i32 s68, s77, 81920
s_mul_i32 s83, s77, 560128
s_add_u32 s83, s83, 2752512
s_lshl_b32 s93, s77, 9
s_lshl_b32 s94, %[swave], 13
s_add_u32 s94, s94, %[slds]
v_lshrrev_b32 v211, 3, v127
v_mul_u32_u24 v220, 1280, v211
v_lshrrev_b32 v212, 4, v127
v_and_b32 v211, 7, v127
v_xor_b32 v211, v211, v212
v_lshl_add_u32 v220, v211, 4, v220
v_xor_b32 v221, 64, v220
s_bitcmp1_b32 s84, 0
s_mov_b32 s77, 0x77552200
s_cselect_b32 s77, 0x77552200, s77
s_lshl_b32 s91, s50, 2
s_lshr_b32 s77, s77, s91
s_and_b32 s77, s77, 15
s_lshl_b32 s86, s77, 7
s_lshl_b32 s85, s77, 19
s_lshl_b32 s90, s49, 8
s_waitcnt lgkmcnt(0)
s_add_u32 s16, s16, s90
s_addc_u32 s17, s17, 0
s_sub_u32 s18, 5242880, s90
s_mov_b32 s19, 131072
s_mov_b32 s34, 2621440
s_mov_b32 s35, 131072
s_mov_b32 s22, 67108864
s_mov_b32 s23, 131072
s_bitcmp1_b32 %[sb], 7
s_cbranch_scc1 LCA_%=
s_add_u32 s87, s89, s85
s_add_u32 s85, s85, 524288
s_cmp_ge_u32 s85, 5242880
s_cselect_b32 s85, 0, s85
s_add_u32 m0, s96, 65536
s_nop 0
buffer_load_dwordx4 v253, s[16:19], s87 offen offset:0 lds
s_add_u32 m0, s96, 66560
s_nop 0
buffer_load_dwordx4 v254, s[16:19], s87 offen offset:0 lds
s_add_u32 s87, s87, 65536
s_add_u32 m0, s96, 67592
s_nop 0
buffer_load_dwordx4 v253, s[16:19], s87 offen offset:0 lds
s_add_u32 m0, s96, 68616
s_nop 0
buffer_load_dwordx4 v254, s[16:19], s87 offen offset:0 lds
s_add_u32 s88, s68, s86
s_add_u32 s86, s86, 128
s_cmp_ge_u32 s86, 1280
s_cselect_b32 s86, 0, s86
s_add_u32 m0, s94, 0
s_add_u32 s95, s88, 0
buffer_load_dwordx4 v220, s[32:35], s95 offen offset:0 lds
s_add_u32 s95, s88, 9216
buffer_load_dwordx4 v221, s[32:35], s95 offen offset:1024 lds
s_add_u32 s95, s88, 18432
buffer_load_dwordx4 v220, s[32:35], s95 offen offset:2048 lds
s_add_u32 s95, s88, 27648
buffer_load_dwordx4 v221, s[32:35], s95 offen offset:3072 lds
s_add_u32 m0, s94, 4096
s_add_u32 s95, s88, 40960
buffer_load_dwordx4 v220, s[32:35], s95 offen offset:0 lds
s_add_u32 s95, s88, 50176
buffer_load_dwordx4 v221, s[32:35], s95 offen offset:1024 lds
s_add_u32 s95, s88, 59392
buffer_load_dwordx4 v220, s[32:35], s95 offen offset:2048 lds
s_add_u32 s95, s88, 68608
buffer_load_dwordx4 v221, s[32:35], s95 offen offset:3072 lds
s_add_u32 s87, s89, s85
s_add_u32 s85, s85, 524288
s_cmp_ge_u32 s85, 5242880
s_cselect_b32 s85, 0, s85
s_add_u32 m0, s96, 81984
s_nop 0
buffer_load_dwordx4 v253, s[16:19], s87 offen offset:0 lds
s_add_u32 m0, s96, 83008
s_nop 0
buffer_load_dwordx4 v254, s[16:19], s87 offen offset:0 lds
s_add_u32 s87, s87, 65536
s_add_u32 m0, s96, 84040
s_nop 0
buffer_load_dwordx4 v253, s[16:19], s87 offen offset:0 lds
s_add_u32 m0, s96, 85064
s_nop 0
buffer_load_dwordx4 v254, s[16:19], s87 offen offset:0 lds
s_branch LCT_%=
LCA_%=:
buffer_load_dwordx4 v[128:131], v168, s[32:35], s82 offen offset:0
buffer_load_dwordx4 v[132:135], v168, s[32:35], s82 offen offset:256
buffer_load_dwordx4 v[136:139], v168, s[32:35], s82 offen offset:512
buffer_load_dwordx4 v[140:143], v168, s[32:35], s82 offen offset:768
buffer_load_dwordx4 v[144:147], v168, s[32:35], s82 offen offset:1024
global_load_dwordx4 v[100:103], v182, s[44:45] offset:0
global_load_dwordx4 v[104:107], v182, s[44:45] offset:256
global_load_dwordx4 v[108:111], v182, s[44:45] offset:512
global_load_dwordx4 v[112:115], v182, s[44:45] offset:768
global_load_dwordx4 v[116:119], v182, s[44:45] offset:1024
LCT_%=:
v_and_b32 v211, 15, v127
v_bfe_u32 v222, v127, 1, 3
v_xor_b32 v222, v222, v212
v_lshlrev_b32 v222, 4, v222
v_lshl_add_u32 v222, v211, 7, v222
s_lshl_b32 s77, %[sb], 11
s_and_b32 s77, s77, 2048
v_add_u32 v222, s77, v222
v_xor_b32 v223, 64, v222
v_xor_b32 v217, 2048, v222
v_add_u32 v217, s94, v217
v_xor_b32 v218, 2048, v223
v_add_u32 v218, s94, v218
v_add_u32 v222, s94, v222
v_add_u32 v223, s94, v223
v_and_b32 v214, 1, v212
v_lshlrev_b32 v215, 5, v214
v_xor_b32 v215, v215, v213
v_lshl_add_u32 v235, v210, 8, v215
v_add_u32 v235, 128, v235
v_and_b32 v234, 1, v127
v_mul_u32_u24 v234, 43690, v234
v_add_u32 v234, 17476, v234
v_bfe_u32 v215, v127, 2, 2
v_and_b32 v216, 3, v127
v_mul_u32_u24 v252, 2056, v212
v_lshl_add_u32 v252, v215, 6, v252
v_lshl_add_u32 v252, v216, 4, v252
s_xor_b32 s77, %[swave], 0
s_lshl_b32 s77, s77, 9
s_add_u32 s77, s77, %[slds]
s_add_u32 s77, s77, 65536
v_add_u32 v192, s77, v252
v_add_u32 v196, 49344, v192
s_xor_b32 s77, %[swave], 1
s_lshl_b32 s77, s77, 9
s_add_u32 s77, s77, %[slds]
s_add_u32 s77, s77, 65536
v_add_u32 v193, s77, v252
v_add_u32 v197, 49344, v193
s_xor_b32 s77, %[swave], 2
s_lshl_b32 s77, s77, 9
s_add_u32 s77, s77, %[slds]
s_add_u32 s77, s77, 65536
v_add_u32 v194, s77, v252
v_add_u32 v198, 49344, v194
s_xor_b32 s77, %[swave], 3
s_lshl_b32 s77, s77, 9
s_add_u32 s77, s77, %[slds]
s_add_u32 s77, s77, 65536
v_add_u32 v195, s77, v252
v_add_u32 v199, 49344, v195
v_mov_b32 v236, 0x3f803f80
v_mov_b32 v184, 0
v_mov_b32 v237, 0x3f803f80
v_mov_b32 v185, 0
v_mov_b32 v238, 0x3f803f80
v_mov_b32 v188, 0
v_mov_b32 v239, 0x3f803f80
v_mov_b32 v189, 0
s_getpc_b64 s[98:99]
LTA0_%=:
s_lshl_b32 s73, %[swave], 12
s_add_u32 s73, s73, LPG1_%=-LTA0_%=
s_add_u32 s98, s98, s73
s_addc_u32 s99, s99, 0
s_mov_b64 exec, 0
s_setpc_b64 s[98:99]
LTR0_%=:
s_mov_b64 exec, -1
s_nop 0
s_bitcmp1_b32 %[sb], 7
s_cbranch_scc1 LCB_%=
s_add_u32 s88, s68, s86
s_add_u32 s86, s86, 128
s_cmp_ge_u32 s86, 1280
s_cselect_b32 s86, 0, s86
s_add_u32 m0, s94, 32768
s_add_u32 s95, s88, 0
buffer_load_dwordx4 v220, s[32:35], s95 offen offset:0 lds
s_add_u32 s95, s88, 9216
buffer_load_dwordx4 v221, s[32:35], s95 offen offset:1024 lds
s_add_u32 s95, s88, 18432
buffer_load_dwordx4 v220, s[32:35], s95 offen offset:2048 lds
s_add_u32 s95, s88, 27648
buffer_load_dwordx4 v221, s[32:35], s95 offen offset:3072 lds
s_add_u32 m0, s94, 36864
s_add_u32 s95, s88, 40960
buffer_load_dwordx4 v220, s[32:35], s95 offen offset:0 lds
s_add_u32 s95, s88, 50176
buffer_load_dwordx4 v221, s[32:35], s95 offen offset:1024 lds
s_add_u32 s95, s88, 59392
buffer_load_dwordx4 v220, s[32:35], s95 offen offset:2048 lds
s_add_u32 s95, s88, 68608
buffer_load_dwordx4 v221, s[32:35], s95 offen offset:3072 lds
s_branch LCM_%=
LCB_%=:
s_waitcnt vmcnt(5)
s_mov_b32 s61, 0x3acccccd
s_add_u32 s87, s89, s85
s_add_u32 s85, s85, 524288
s_cmp_ge_u32 s85, 5242880
s_cselect_b32 s85, 0, s85
s_add_u32 m0, s96, 65536
s_nop 0
buffer_load_dwordx4 v253, s[16:19], s87 offen offset:0 lds
s_lshl_b32 s60, s76, 1
s_sub_u32 s60, 65535, s60
v_lshlrev_b32 v0, 16, v128
v_and_b32 v1, 0xffff0000, v128
v_lshlrev_b32 v2, 16, v129
v_and_b32 v3, 0xffff0000, v129
v_lshlrev_b32 v4, 16, v130
v_and_b32 v5, 0xffff0000, v130
s_add_u32 m0, s96, 66560
s_nop 0
buffer_load_dwordx4 v254, s[16:19], s87 offen offset:0 lds
v_lshlrev_b32 v6, 16, v131
v_and_b32 v7, 0xffff0000, v131
v_lshlrev_b32 v8, 16, v132
v_and_b32 v9, 0xffff0000, v132
v_lshlrev_b32 v10, 16, v133
v_and_b32 v11, 0xffff0000, v133
v_lshlrev_b32 v12, 16, v134
v_and_b32 v13, 0xffff0000, v134
s_add_u32 s87, s87, 65536
s_add_u32 m0, s96, 67592
s_nop 0
buffer_load_dwordx4 v253, s[16:19], s87 offen offset:0 lds
v_lshlrev_b32 v14, 16, v135
v_and_b32 v15, 0xffff0000, v135
v_lshlrev_b32 v16, 16, v136
v_and_b32 v17, 0xffff0000, v136
v_lshlrev_b32 v18, 16, v137
v_and_b32 v19, 0xffff0000, v137
v_lshlrev_b32 v20, 16, v138
v_and_b32 v21, 0xffff0000, v138
s_add_u32 m0, s96, 68616
s_nop 0
buffer_load_dwordx4 v254, s[16:19], s87 offen offset:0 lds
v_lshlrev_b32 v22, 16, v139
v_and_b32 v23, 0xffff0000, v139
v_lshlrev_b32 v24, 16, v140
v_and_b32 v25, 0xffff0000, v140
v_lshlrev_b32 v26, 16, v141
v_and_b32 v27, 0xffff0000, v141
v_lshlrev_b32 v28, 16, v142
v_and_b32 v29, 0xffff0000, v142
s_add_u32 s88, s68, s86
s_add_u32 s86, s86, 128
s_cmp_ge_u32 s86, 1280
s_cselect_b32 s86, 0, s86
s_add_u32 m0, s94, 0
s_add_u32 s95, s88, 0
buffer_load_dwordx4 v220, s[32:35], s95 offen offset:0 lds
v_lshlrev_b32 v30, 16, v143
v_and_b32 v31, 0xffff0000, v143
v_lshlrev_b32 v32, 16, v144
v_and_b32 v33, 0xffff0000, v144
v_lshlrev_b32 v34, 16, v145
v_and_b32 v35, 0xffff0000, v145
v_lshlrev_b32 v36, 16, v146
v_and_b32 v37, 0xffff0000, v146
s_add_u32 s95, s88, 9216
buffer_load_dwordx4 v221, s[32:35], s95 offen offset:1024 lds
v_lshlrev_b32 v38, 16, v147
v_and_b32 v39, 0xffff0000, v147
v_pk_add_f32 v[40:41], v[0:1], v[2:3]
v_pk_add_f32 v[42:43], v[4:5], v[6:7]
v_pk_add_f32 v[40:41], v[40:41], v[8:9]
v_pk_add_f32 v[42:43], v[42:43], v[10:11]
v_pk_add_f32 v[40:41], v[40:41], v[12:13]
v_pk_add_f32 v[42:43], v[42:43], v[14:15]
s_add_u32 s95, s88, 18432
buffer_load_dwordx4 v220, s[32:35], s95 offen offset:2048 lds
v_pk_add_f32 v[40:41], v[40:41], v[16:17]
v_pk_add_f32 v[42:43], v[42:43], v[18:19]
v_pk_add_f32 v[40:41], v[40:41], v[20:21]
v_pk_add_f32 v[42:43], v[42:43], v[22:23]
v_pk_add_f32 v[40:41], v[40:41], v[24:25]
v_pk_add_f32 v[42:43], v[42:43], v[26:27]
v_pk_add_f32 v[40:41], v[40:41], v[28:29]
v_pk_add_f32 v[42:43], v[42:43], v[30:31]
s_add_u32 s95, s88, 27648
buffer_load_dwordx4 v221, s[32:35], s95 offen offset:3072 lds
v_pk_add_f32 v[40:41], v[40:41], v[32:33]
v_pk_add_f32 v[42:43], v[42:43], v[34:35]
v_pk_add_f32 v[40:41], v[40:41], v[36:37]
v_pk_add_f32 v[42:43], v[42:43], v[38:39]
v_pk_add_f32 v[40:41], v[40:41], v[42:43]
v_add_f32 v48, v40, v41
s_nop 1
v_add_f32_dpp v48, v48, v48 row_ror:8 row_mask:0xf bank_mask:0xf
s_nop 1
v_add_f32_dpp v48, v48, v48 row_ror:4 row_mask:0xf bank_mask:0xf
s_add_u32 m0, s94, 4096
s_add_u32 s95, s88, 40960
buffer_load_dwordx4 v220, s[32:35], s95 offen offset:0 lds
v_add_f32_dpp v48, v48, v48 row_ror:2 row_mask:0xf bank_mask:0xf
s_nop 1
v_add_f32_dpp v48, v48, v48 row_ror:1 row_mask:0xf bank_mask:0xf
v_mul_f32 v48, 0x3acccccd, v48
CV v44,v48,v48
v_and_b32 v44, 0xffff0000, v44
v_sub_f32 v48, v48, v44
v_pk_add_f32 v[0:1], v[0:1], v[44:45] op_sel_hi:[1,0] neg_lo:[0,1] neg_hi:[0,1]
v_pk_add_f32 v[2:3], v[2:3], v[44:45] op_sel_hi:[1,0] neg_lo:[0,1] neg_hi:[0,1]
s_add_u32 s95, s88, 50176
buffer_load_dwordx4 v221, s[32:35], s95 offen offset:1024 lds
v_pk_add_f32 v[4:5], v[4:5], v[44:45] op_sel_hi:[1,0] neg_lo:[0,1] neg_hi:[0,1]
v_pk_add_f32 v[6:7], v[6:7], v[44:45] op_sel_hi:[1,0] neg_lo:[0,1] neg_hi:[0,1]
v_pk_add_f32 v[8:9], v[8:9], v[44:45] op_sel_hi:[1,0] neg_lo:[0,1] neg_hi:[0,1]
v_pk_add_f32 v[10:11], v[10:11], v[44:45] op_sel_hi:[1,0] neg_lo:[0,1] neg_hi:[0,1]
v_pk_add_f32 v[12:13], v[12:13], v[44:45] op_sel_hi:[1,0] neg_lo:[0,1] neg_hi:[0,1]
)ASM"
R"ASM(
v_pk_add_f32 v[14:15], v[14:15], v[44:45] op_sel_hi:[1,0] neg_lo:[0,1] neg_hi:[0,1]
v_pk_add_f32 v[16:17], v[16:17], v[44:45] op_sel_hi:[1,0] neg_lo:[0,1] neg_hi:[0,1]
v_pk_add_f32 v[18:19], v[18:19], v[44:45] op_sel_hi:[1,0] neg_lo:[0,1] neg_hi:[0,1]
s_add_u32 s95, s88, 59392
buffer_load_dwordx4 v220, s[32:35], s95 offen offset:2048 lds
v_pk_add_f32 v[20:21], v[20:21], v[44:45] op_sel_hi:[1,0] neg_lo:[0,1] neg_hi:[0,1]
v_pk_add_f32 v[22:23], v[22:23], v[44:45] op_sel_hi:[1,0] neg_lo:[0,1] neg_hi:[0,1]
v_pk_add_f32 v[24:25], v[24:25], v[44:45] op_sel_hi:[1,0] neg_lo:[0,1] neg_hi:[0,1]
v_pk_add_f32 v[26:27], v[26:27], v[44:45] op_sel_hi:[1,0] neg_lo:[0,1] neg_hi:[0,1]
v_pk_add_f32 v[28:29], v[28:29], v[44:45] op_sel_hi:[1,0] neg_lo:[0,1] neg_hi:[0,1]
v_pk_add_f32 v[30:31], v[30:31], v[44:45] op_sel_hi:[1,0] neg_lo:[0,1] neg_hi:[0,1]
v_pk_add_f32 v[32:33], v[32:33], v[44:45] op_sel_hi:[1,0] neg_lo:[0,1] neg_hi:[0,1]
v_pk_add_f32 v[34:35], v[34:35], v[44:45] op_sel_hi:[1,0] neg_lo:[0,1] neg_hi:[0,1]
s_add_u32 s95, s88, 68608
buffer_load_dwordx4 v221, s[32:35], s95 offen offset:3072 lds
v_pk_add_f32 v[36:37], v[36:37], v[44:45] op_sel_hi:[1,0] neg_lo:[0,1] neg_hi:[0,1]
v_pk_add_f32 v[38:39], v[38:39], v[44:45] op_sel_hi:[1,0] neg_lo:[0,1] neg_hi:[0,1]
v_pk_mul_f32 v[40:41], v[0:1], v[0:1]
v_pk_mul_f32 v[42:43], v[2:3], v[2:3]
v_pk_fma_f32 v[40:41], v[4:5], v[4:5], v[40:41]
v_pk_fma_f32 v[42:43], v[6:7], v[6:7], v[42:43]
v_pk_fma_f32 v[40:41], v[8:9], v[8:9], v[40:41]
v_pk_fma_f32 v[42:43], v[10:11], v[10:11], v[42:43]
s_add_u32 s87, s89, s85
s_add_u32 s85, s85, 524288
s_cmp_ge_u32 s85, 5242880
s_cselect_b32 s85, 0, s85
s_add_u32 m0, s96, 81984
s_nop 0
buffer_load_dwordx4 v253, s[16:19], s87 offen offset:0 lds
v_pk_fma_f32 v[40:41], v[12:13], v[12:13], v[40:41]
v_pk_fma_f32 v[42:43], v[14:15], v[14:15], v[42:43]
v_pk_fma_f32 v[40:41], v[16:17], v[16:17], v[40:41]
v_pk_fma_f32 v[42:43], v[18:19], v[18:19], v[42:43]
v_pk_fma_f32 v[40:41], v[20:21], v[20:21], v[40:41]
v_pk_fma_f32 v[42:43], v[22:23], v[22:23], v[42:43]
v_pk_fma_f32 v[40:41], v[24:25], v[24:25], v[40:41]
v_pk_fma_f32 v[42:43], v[26:27], v[26:27], v[42:43]
s_add_u32 m0, s96, 83008
s_nop 0
buffer_load_dwordx4 v254, s[16:19], s87 offen offset:0 lds
v_pk_fma_f32 v[40:41], v[28:29], v[28:29], v[40:41]
v_pk_fma_f32 v[42:43], v[30:31], v[30:31], v[42:43]
v_pk_fma_f32 v[40:41], v[32:33], v[32:33], v[40:41]
v_pk_fma_f32 v[42:43], v[34:35], v[34:35], v[42:43]
v_pk_fma_f32 v[40:41], v[36:37], v[36:37], v[40:41]
v_pk_fma_f32 v[42:43], v[38:39], v[38:39], v[42:43]
v_mul_f32 v48, v48, v48
v_pk_add_f32 v[40:41], v[40:41], v[42:43]
s_add_u32 s87, s87, 65536
s_add_u32 m0, s96, 84040
s_nop 0
buffer_load_dwordx4 v253, s[16:19], s87 offen offset:0 lds
v_add_f32 v49, v40, v41
CV v128,v0,v1
CV v129,v2,v3
v_add_f32_dpp v49, v49, v49 row_ror:8 row_mask:0xf bank_mask:0xf
CV v130,v4,v5
CV v131,v6,v7
v_add_f32_dpp v49, v49, v49 row_ror:4 row_mask:0xf bank_mask:0xf
CV v132,v8,v9
s_add_u32 m0, s96, 85064
s_nop 0
buffer_load_dwordx4 v254, s[16:19], s87 offen offset:0 lds
CV v133,v10,v11
v_add_f32_dpp v49, v49, v49 row_ror:2 row_mask:0xf bank_mask:0xf
CV v134,v12,v13
CV v135,v14,v15
v_add_f32_dpp v49, v49, v49 row_ror:1 row_mask:0xf bank_mask:0xf
v_fma_f32 v49, v49, s61, -v48
CV v136,v16,v17
v_max_f32 v49, 0, v49
s_add_u32 s88, s68, s86
s_add_u32 s86, s86, 128
s_cmp_ge_u32 s86, 1280
s_cselect_b32 s86, 0, s86
s_add_u32 m0, s94, 32768
s_add_u32 s95, s88, 0
buffer_load_dwordx4 v220, s[32:35], s95 offen offset:0 lds
CV v137,v18,v19
CV v49,v49,v49
CV v138,v20,v21
v_and_b32 v49, 0xffff0000, v49
CV v139,v22,v23
v_add_f32 v49, 0x3727c5ac, v49
CV v140,v24,v25
CV v49,v49,v49
s_add_u32 s95, s88, 9216
buffer_load_dwordx4 v221, s[32:35], s95 offen offset:1024 lds
CV v141,v26,v27
v_and_b32 v49, 0xffff0000, v49
CV v142,v28,v29
v_sqrt_f32 v49, v49
CV v143,v30,v31
CV v178,v44,v49
CV v144,v32,v33
v_and_b32 v49, 0xffff0000, v178
s_add_u32 s95, s88, 18432
buffer_load_dwordx4 v220, s[32:35], s95 offen offset:2048 lds
CV v145,v34,v35
v_rcp_f32 v46, v49
CV v146,v36,v37
CV v147,v38,v39
v_lshlrev_b32 v0, 16, v128
v_and_b32 v1, 0xffff0000, v128
v_lshlrev_b32 v2, 16, v129
v_and_b32 v3, 0xffff0000, v129
s_add_u32 s95, s88, 27648
buffer_load_dwordx4 v221, s[32:35], s95 offen offset:3072 lds
v_lshlrev_b32 v4, 16, v130
v_and_b32 v5, 0xffff0000, v130
v_lshlrev_b32 v6, 16, v131
v_and_b32 v7, 0xffff0000, v131
v_lshlrev_b32 v8, 16, v132
v_and_b32 v9, 0xffff0000, v132
v_lshlrev_b32 v10, 16, v133
v_and_b32 v11, 0xffff0000, v133
s_add_u32 m0, s94, 36864
s_add_u32 s95, s88, 40960
buffer_load_dwordx4 v220, s[32:35], s95 offen offset:0 lds
v_lshlrev_b32 v12, 16, v134
v_and_b32 v13, 0xffff0000, v134
v_lshlrev_b32 v14, 16, v135
v_and_b32 v15, 0xffff0000, v135
v_lshlrev_b32 v16, 16, v136
v_and_b32 v17, 0xffff0000, v136
v_lshlrev_b32 v18, 16, v137
v_and_b32 v19, 0xffff0000, v137
s_add_u32 s95, s88, 50176
buffer_load_dwordx4 v221, s[32:35], s95 offen offset:1024 lds
v_lshlrev_b32 v20, 16, v138
v_and_b32 v21, 0xffff0000, v138
v_lshlrev_b32 v22, 16, v139
v_and_b32 v23, 0xffff0000, v139
v_lshlrev_b32 v24, 16, v140
v_and_b32 v25, 0xffff0000, v140
v_lshlrev_b32 v26, 16, v141
v_and_b32 v27, 0xffff0000, v141
s_add_u32 s95, s88, 59392
buffer_load_dwordx4 v220, s[32:35], s95 offen offset:2048 lds
v_lshlrev_b32 v28, 16, v142
v_and_b32 v29, 0xffff0000, v142
v_lshlrev_b32 v30, 16, v143
v_and_b32 v31, 0xffff0000, v143
v_lshlrev_b32 v32, 16, v144
v_and_b32 v33, 0xffff0000, v144
v_lshlrev_b32 v34, 16, v145
v_and_b32 v35, 0xffff0000, v145
s_add_u32 s95, s88, 68608
buffer_load_dwordx4 v221, s[32:35], s95 offen offset:3072 lds
v_lshlrev_b32 v36, 16, v146
v_and_b32 v37, 0xffff0000, v146
v_lshlrev_b32 v38, 16, v147
v_and_b32 v39, 0xffff0000, v147
v_pk_mul_f32 v[0:1], v[0:1], v[46:47] op_sel_hi:[1,0]
v_pk_mul_f32 v[2:3], v[2:3], v[46:47] op_sel_hi:[1,0]
v_pk_mul_f32 v[4:5], v[4:5], v[46:47] op_sel_hi:[1,0]
v_pk_mul_f32 v[6:7], v[6:7], v[46:47] op_sel_hi:[1,0]
v_pk_mul_f32 v[8:9], v[8:9], v[46:47] op_sel_hi:[1,0]
v_pk_mul_f32 v[10:11], v[10:11], v[46:47] op_sel_hi:[1,0]
v_pk_mul_f32 v[12:13], v[12:13], v[46:47] op_sel_hi:[1,0]
v_pk_mul_f32 v[14:15], v[14:15], v[46:47] op_sel_hi:[1,0]
v_pk_mul_f32 v[16:17], v[16:17], v[46:47] op_sel_hi:[1,0]
v_pk_mul_f32 v[18:19], v[18:19], v[46:47] op_sel_hi:[1,0]
v_pk_mul_f32 v[20:21], v[20:21], v[46:47] op_sel_hi:[1,0]
v_pk_mul_f32 v[22:23], v[22:23], v[46:47] op_sel_hi:[1,0]
v_pk_mul_f32 v[24:25], v[24:25], v[46:47] op_sel_hi:[1,0]
v_pk_mul_f32 v[26:27], v[26:27], v[46:47] op_sel_hi:[1,0]
v_pk_mul_f32 v[28:29], v[28:29], v[46:47] op_sel_hi:[1,0]
v_pk_mul_f32 v[30:31], v[30:31], v[46:47] op_sel_hi:[1,0]
v_pk_mul_f32 v[32:33], v[32:33], v[46:47] op_sel_hi:[1,0]
v_pk_mul_f32 v[34:35], v[34:35], v[46:47] op_sel_hi:[1,0]
v_pk_mul_f32 v[36:37], v[36:37], v[46:47] op_sel_hi:[1,0]
v_pk_mul_f32 v[38:39], v[38:39], v[46:47] op_sel_hi:[1,0]
CV v128,v0,v1
CV v129,v2,v3
CV v130,v4,v5
CV v131,v6,v7
CV v132,v8,v9
CV v133,v10,v11
CV v134,v12,v13
CV v135,v14,v15
CV v136,v16,v17
CV v137,v18,v19
CV v138,v20,v21
CV v139,v22,v23
CV v140,v24,v25
CV v141,v26,v27
CV v142,v28,v29
CV v143,v30,v31
CV v144,v32,v33
CV v145,v34,v35
CV v146,v36,v37
CV v147,v38,v39
s_waitcnt vmcnt(24)
v_dot2_f32_bf16 v40, v128, v100, 0
v_dot2_f32_bf16 v41, v129, v101, 0
v_dot2_f32_bf16 v42, v130, v102, 0
v_dot2_f32_bf16 v43, v131, v103, 0
v_dot2_f32_bf16 v40, v132, v104, v40
v_dot2_f32_bf16 v41, v133, v105, v41
v_dot2_f32_bf16 v42, v134, v106, v42
v_dot2_f32_bf16 v43, v135, v107, v43
v_dot2_f32_bf16 v40, v136, v108, v40
v_dot2_f32_bf16 v41, v137, v109, v41
v_dot2_f32_bf16 v42, v138, v110, v42
v_dot2_f32_bf16 v43, v139, v111, v43
v_dot2_f32_bf16 v40, v140, v112, v40
v_dot2_f32_bf16 v41, v141, v113, v41
v_dot2_f32_bf16 v42, v142, v114, v42
v_dot2_f32_bf16 v43, v143, v115, v43
v_dot2_f32_bf16 v40, v144, v116, v40
v_dot2_f32_bf16 v41, v145, v117, v41
v_dot2_f32_bf16 v42, v146, v118, v42
v_dot2_f32_bf16 v43, v147, v119, v43
s_nop 2
v_pk_add_f32 v[40:41], v[40:41], v[42:43]
v_add_f32 v160, v40, v41
s_nop 1
v_add_f32_dpp v160, v160, v160 row_ror:8 row_mask:0xf bank_mask:0xf
s_nop 1
v_add_f32_dpp v160, v160, v160 row_ror:4 row_mask:0xf bank_mask:0xf
s_nop 1
v_add_f32_dpp v160, v160, v160 row_ror:2 row_mask:0xf bank_mask:0xf
s_nop 1
v_add_f32_dpp v160, v160, v160 row_ror:1 row_mask:0xf bank_mask:0xf
CV v160,v160,v160
v_ashrrev_i32 v50, 31, v160
v_or_b32 v50, 0x80000000, v50
v_xor_b32 v160, v160, v50
v_and_b32 v160, 0xffff0000, v160
v_sub_u32 v50, s60, v181
v_or_b32 v160, v160, v50
LCM_%=:
s_waitcnt vmcnt(12)
LPG1_%=:
s_cbranch_execz LPG5_%=
RD a[128:131],v222 offset:0
RD a[132:135],v223 offset:0
RD a[136:139],v217 offset:0
RD a[140:143],v218 offset:0
RD a[144:147],v222 offset:4096
RD a[148:151],v223 offset:4096
RD a[152:155],v217 offset:4096
RD a[156:159],v218 offset:4096
s_barrier
TR a[0:1],v192 offset:0
TR a[2:3],v192 offset:256
TR a[4:5],v192 offset:8224
TR a[6:7],v192 offset:8480
TR a[8:9],v192 offset:8
TR a[10:11],v192 offset:264
TR a[12:13],v192 offset:8232
TR a[14:15],v192 offset:8488
s_waitcnt lgkmcnt(8)
s_waitcnt lgkmcnt(4)
MM v[0:3],a[0:3],a[128:131],0
s_mov_b32 s66, 32768
TR a[16:17],v193 offset:0
TR a[18:19],v193 offset:256
MM v[0:3],a[4:7],a[132:135],v[0:3]
s_lshl_b32 s90, s76, 1
TR a[20:21],v193 offset:8224
TR a[22:23],v193 offset:8480
MM v[8:11],a[4:7],a[140:143],0
s_and_b32 s77, s90, 15
TR a[24:25],v193 offset:8
TR a[26:27],v193 offset:264
buffer_load_dword v169, off, s[20:23], s66 sc1
MM v[8:11],a[0:3],a[136:139],v[8:11]
s_add_u32 s82, s82, 131072
TR a[28:29],v193 offset:8232
TR a[30:31],v193 offset:8488
MM v[16:19],a[0:3],a[144:147],0
s_add_u32 s87, s89, s85
TR a[32:33],v194 offset:0
TR a[34:35],v194 offset:256
MM v[16:19],a[4:7],a[148:151],v[16:19]
s_add_u32 s85, s85, 524288
TR a[36:37],v194 offset:8224
TR a[38:39],v194 offset:8480
MM v[24:27],a[4:7],a[156:159],0
s_lshl_b32 s77, s77, 1
TR a[40:41],v194 offset:8
TR a[42:43],v194 offset:264
MM v[24:27],a[0:3],a[152:155],v[24:27]
s_waitcnt lgkmcnt(14)
TR a[44:45],v194 offset:8232
TR a[46:47],v194 offset:8488
s_cmp_ge_u32 s85, 5242880
s_cselect_b32 s85, 0, s85
MM v[4:7],a[8:11],a[128:131],0
s_add_u32 m0, s96, 98432
TR a[48:49],v195 offset:0
TR a[50:51],v195 offset:256
MM v[4:7],a[12:15],a[132:135],v[4:7]
s_bfe_u32 s91, s90, 65540
TR a[52:53],v195 offset:8224
TR a[54:55],v195 offset:8480
MM v[12:15],a[12:15],a[140:143],0
s_add_u32 s77, s77, s91
TR a[56:57],v195 offset:8
TR a[58:59],v195 offset:264
buffer_load_dwordx4 v253, s[16:19], s87 offen offset:0 lds
MM v[12:15],a[8:11],a[136:139],v[12:15]
s_add_u32 m0, s96, 99456
TR a[60:61],v195 offset:8232
TR a[62:63],v195 offset:8488
MM v[20:23],a[8:11],a[144:147],0
s_and_b32 s91, s90, 0xffffffe0
buffer_load_dwordx4 v254, s[16:19], s87 offen offset:0 lds
MM v[20:23],a[12:15],a[148:151],v[20:23]
s_add_u32 s87, s87, 65536
s_add_u32 m0, s96, 100488
s_add_u32 s77, s77, s91
MM v[28:31],a[12:15],a[156:159],0
s_lshl_b32 s77, s77, 3
s_add_u32 s97, s77, 36864
s_sub_u32 s79, 65535, s90
MM v[28:31],a[8:11],a[152:155],v[28:31]
s_lshl_b32 s78, s90, 2
s_add_u32 s78, s78, 53248
s_lshl_b32 s80, s76, 2
v_smfmac_f32_16x16x64_bf16 v[184:187], v[236:239], a[0:7], v234
s_add_u32 s80, s80, 81920
v_lshlrev_b32 v232, 11, v180
v_smfmac_f32_16x16x64_bf16 v[188:191], v[236:239], a[8:15], v234
s_waitcnt lgkmcnt(15)
v_lshl_add_u32 v232, v214, 8, v232
MM v[32:35],a[16:19],a[128:131],0
buffer_load_dwordx4 v253, s[16:19], s87 offen offset:0 lds
MM v[32:35],a[20:23],a[132:135],v[32:35]
s_add_u32 m0, s96, 101512
v_lshl_add_u32 v232, v215, 6, v232
v_lshl_add_u32 v232, v216, 3, v232
MM v[40:43],a[20:23],a[140:143],0
s_waitcnt vmcnt(12)
s_barrier
v_lshl_add_u32 v232, v214, 5, v232
MM v[40:43],a[16:19],a[136:139],v[40:43]
TR a[0:1],v192 offset:16448
TR a[2:3],v192 offset:16704
MM v[48:51],a[16:19],a[144:147],0
TR a[4:5],v192 offset:24672
TR a[6:7],v192 offset:24928
buffer_load_dwordx4 v254, s[16:19], s87 offen offset:0 lds
MM v[48:51],a[20:23],a[148:151],v[48:51]
TR a[8:9],v192 offset:16456
TR a[10:11],v192 offset:16712
MM v[56:59],a[20:23],a[156:159],0
s_add_u32 s88, s68, s86
)ASM"
R"ASM(
TR a[12:13],v192 offset:24680
TR a[14:15],v192 offset:24936
MM v[56:59],a[16:19],a[152:155],v[56:59]
s_add_u32 s86, s86, 128
s_cmp_ge_u32 s86, 1280
s_cselect_b32 s86, 0, s86
MM v[36:39],a[24:27],a[128:131],0
s_add_u32 m0, s94, 0
TR a[16:17],v193 offset:16448
TR a[18:19],v193 offset:16704
MM v[36:39],a[28:31],a[132:135],v[36:39]
s_add_u32 s95, s88, 0
TR a[20:21],v193 offset:24672
TR a[22:23],v193 offset:24928
MM v[44:47],a[28:31],a[140:143],0
buffer_load_dwordx4 v220, s[32:35], s95 offen offset:0 lds
MM v[44:47],a[24:27],a[136:139],v[44:47]
s_add_u32 s95, s88, 9216
v_xor_b32 v233, 32, v232
s_add_u32 s77, %[slds], 65536
MM v[52:55],a[24:27],a[144:147],0
buffer_load_dwordx4 v221, s[32:35], s95 offen offset:1024 lds
MM v[52:55],a[28:31],a[148:151],v[52:55]
s_add_u32 s95, s88, 18432
v_add_u32 v232, s77, v232
v_add_u32 v233, s77, v233
MM v[60:63],a[28:31],a[156:159],0
v_lshlrev_b32 v214, 8, v211
v_add_u32 v214, %[slds], v214
MM v[60:63],a[24:27],a[152:155],v[60:63]
s_waitcnt lgkmcnt(15)
v_xor_b32 v215, v211, v212
s_lshl_b32 s77, %[swave], 2
MM v[64:67],a[32:35],a[128:131],0
TR a[24:25],v193 offset:16456
TR a[26:27],v193 offset:16712
MM v[64:67],a[36:39],a[132:135],v[64:67]
TR a[28:29],v193 offset:24680
TR a[30:31],v193 offset:24936
MM v[72:75],a[36:39],a[140:143],0
buffer_load_dwordx4 v220, s[32:35], s95 offen offset:2048 lds
MM v[72:75],a[32:35],a[136:139],v[72:75]
s_add_u32 s95, s88, 27648
v_xor_b32 v213, s77, v215
v_xor_b32 v216, 0, v213
MM v[80:83],a[32:35],a[144:147],0
buffer_load_dwordx4 v221, s[32:35], s95 offen offset:3072 lds
MM v[80:83],a[36:39],a[148:151],v[80:83]
s_add_u32 m0, s94, 4096
s_add_u32 s95, s88, 40960
v_lshl_add_u32 v224, v216, 4, v214
MM v[88:91],a[36:39],a[156:159],0
v_xor_b32 v216, 4, v213
v_lshl_add_u32 v225, v216, 4, v214
MM v[88:91],a[32:35],a[152:155],v[88:91]
v_xor_b32 v216, 8, v213
v_lshl_add_u32 v226, v216, 4, v214
MM v[68:71],a[40:43],a[128:131],0
TR a[32:33],v194 offset:16448
TR a[34:35],v194 offset:16704
MM v[68:71],a[44:47],a[132:135],v[68:71]
s_waitcnt vmcnt(8)
TR a[36:37],v194 offset:24672
TR a[38:39],v194 offset:24928
MM v[76:79],a[44:47],a[140:143],0
buffer_load_dwordx4 v220, s[32:35], s95 offen offset:0 lds
MM v[76:79],a[40:43],a[136:139],v[76:79]
s_add_u32 s95, s88, 50176
v_readfirstlane_b32 s69, v169
v_lshlrev_b32 v173, 4, v181
MM v[84:87],a[40:43],a[144:147],0
s_add_u32 s69, s69, 1
buffer_load_dwordx4 v221, s[32:35], s95 offen offset:1024 lds
MM v[84:87],a[44:47],a[148:151],v[84:87]
s_add_u32 s95, s88, 59392
v_mov_b32 v179, s69
v_xor_b32 v216, 12, v213
MM v[92:95],a[44:47],a[156:159],0
s_mov_b64 exec, s[54:55]
buffer_store_dwordx2 v[178:179], v173, s[20:23], s97 offen offset:0 sc1
s_mov_b64 exec, -1
MM v[92:95],a[40:43],a[152:155],v[92:95]
v_lshl_add_u32 v227, v216, 4, v214
v_lshlrev_b32 v214, 8, v212
MM v[96:99],a[48:51],a[128:131],0
TR a[40:41],v194 offset:16456
TR a[42:43],v194 offset:16712
MM v[96:99],a[52:55],a[132:135],v[96:99]
TR a[44:45],v194 offset:24680
TR a[46:47],v194 offset:24936
MM v[104:107],a[52:55],a[140:143],0
buffer_load_dwordx4 v220, s[32:35], s95 offen offset:2048 lds
MM v[104:107],a[48:51],a[136:139],v[104:107]
s_add_u32 s95, s88, 68608
v_xor_b32 v216, 0, v215
v_lshl_add_u32 v228, v216, 4, v214
MM v[112:115],a[48:51],a[144:147],0
buffer_load_dwordx4 v221, s[32:35], s95 offen offset:3072 lds
MM v[112:115],a[52:55],a[148:151],v[112:115]
s_add_u32 s87, s89, s85
s_add_u32 s85, s85, 524288
v_xor_b32 v216, 4, v215
MM v[120:123],a[52:55],a[156:159],0
s_cmp_ge_u32 s85, 5242880
s_cselect_b32 s85, 0, s85
s_add_u32 m0, s96, 114880
MM v[120:123],a[48:51],a[152:155],v[120:123]
s_waitcnt lgkmcnt(15)
v_lshl_add_u32 v229, v216, 4, v214
v_xor_b32 v216, 8, v215
MM v[100:103],a[56:59],a[128:131],0
TR a[48:49],v195 offset:16448
TR a[50:51],v195 offset:16704
s_mov_b64 exec, s[52:53]
buffer_store_dwordx4 v[128:131], v168, s[20:23], s82 offen offset:0 sc1
s_mov_b64 exec, -1
MM v[100:103],a[60:63],a[132:135],v[100:103]
TR a[52:53],v195 offset:24672
TR a[54:55],v195 offset:24928
MM v[108:111],a[60:63],a[140:143],0
v_lshl_add_u32 v230, v216, 4, v214
v_xor_b32 v216, 12, v215
MM v[108:111],a[56:59],a[136:139],v[108:111]
v_lshl_add_u32 v231, v216, 4, v214
s_lshl_b32 s90, s49, 19
MM v[116:119],a[56:59],a[144:147],0
buffer_load_dwordx4 v253, s[16:19], s87 offen offset:0 lds
MM v[116:119],a[60:63],a[148:151],v[116:119]
s_add_u32 m0, s96, 115904
s_add_u32 s8, s8, s90
s_addc_u32 s9, s9, 0
MM v[124:127],a[60:63],a[156:159],0
s_mov_b32 s10, 524288
s_mov_b32 s11, 131072
MM v[124:127],a[56:59],a[152:155],v[124:127]
s_add_u32 s12, s12, s90
s_addc_u32 s13, s13, 0
s_mov_b32 s14, 525312
buffer_load_dwordx4 v254, s[16:19], s87 offen offset:0 lds
s_add_u32 s87, s87, 65536
s_add_u32 m0, s96, 116936
s_nop 0
buffer_load_dwordx4 v253, s[16:19], s87 offen offset:0 lds
s_add_u32 m0, s96, 117960
s_nop 0
buffer_load_dwordx4 v254, s[16:19], s87 offen offset:0 lds
RD a[160:163],v222 offset:32768
RD a[164:167],v223 offset:32768
RD a[168:171],v217 offset:32768
RD a[172:175],v218 offset:32768
RD a[176:179],v222 offset:36864
RD a[180:183],v223 offset:36864
RD a[184:187],v217 offset:36864
RD a[188:191],v218 offset:36864
s_waitcnt vmcnt(14) lgkmcnt(0)
s_barrier
MM v[0:3],a[0:3],a[160:163],v[0:3]
s_mov_b32 s15, 131072
TR a[56:57],v195 offset:16456
TR a[58:59],v195 offset:16712
MM v[0:3],a[4:7],a[164:167],v[0:3]
TR a[60:61],v195 offset:24680
TR a[62:63],v195 offset:24936
MM v[8:11],a[4:7],a[172:175],v[8:11]
s_mov_b64 exec, s[52:53]
buffer_store_dwordx4 v[132:135], v168, s[20:23], s82 offen offset:256 sc1
s_mov_b64 exec, -1
MM v[8:11],a[0:3],a[168:171],v[8:11]
s_add_u32 s88, s68, s86
s_add_u32 s86, s86, 128
MM v[16:19],a[0:3],a[176:179],v[16:19]
s_cmp_ge_u32 s86, 1280
s_cselect_b32 s86, 0, s86
s_add_u32 m0, s94, 32768
MM v[16:19],a[4:7],a[180:183],v[16:19]
s_add_u32 s95, s88, 0
buffer_load_dwordx4 v220, s[32:35], s95 offen offset:0 lds
MM v[24:27],a[4:7],a[188:191],v[24:27]
s_add_u32 s95, s88, 9216
s_sub_u32 s12, s12, 1024
s_subb_u32 s13, s13, 0
MM v[24:27],a[0:3],a[184:187],v[24:27]
s_lshr_b32 s90, s49, 2
s_mul_i32 s90, s90, 655360
s_mul_i32 s77, s84, 320
MM v[4:7],a[8:11],a[160:163],v[4:7]
s_add_u32 s90, s90, s77
buffer_load_dwordx4 v221, s[32:35], s95 offen offset:1024 lds
MM v[4:7],a[12:15],a[164:167],v[4:7]
s_add_u32 s95, s88, 18432
s_add_u32 s24, s24, s90
s_addc_u32 s25, s25, 0
MM v[12:15],a[12:15],a[172:175],v[12:15]
s_sub_u32 s26, 5242880, s90
s_mov_b32 s27, 131072
s_sub_u32 s77, %[sb], 76
MM v[12:15],a[8:11],a[168:171],v[12:15]
s_min_u32 s47, s77, %[sb]
s_mul_i32 s77, s47, 205
s_lshr_b32 s46, s77, 11
MM v[20:23],a[8:11],a[176:179],v[20:23]
s_mul_i32 s90, s46, 121600
buffer_load_dwordx4 v220, s[32:35], s95 offen offset:2048 lds
MM v[20:23],a[12:15],a[180:183],v[20:23]
s_add_u32 s95, s88, 27648
s_lshl_b32 s77, s47, 7
s_add_u32 s90, s90, s77
MM v[28:31],a[12:15],a[188:191],v[28:31]
s_add_u32 s28, s28, s90
s_addc_u32 s29, s29, 0
s_sub_u32 s30, 2211840, s90
MM v[28:31],a[8:11],a[184:187],v[28:31]
s_mov_b32 s31, 131072
s_mov_b32 s38, 2211840
s_mov_b32 s39, 131072
v_smfmac_f32_16x16x64_bf16 v[184:187], v[236:239], a[0:7], v234
s_mov_b32 s42, 2621440
s_mov_b32 s43, 131072
v_smfmac_f32_16x16x64_bf16 v[188:191], v[236:239], a[8:15], v234
s_lshr_b32 s77, %[sb], 6
s_lshl_b32 s77, s77, 3
MM v[32:35],a[16:19],a[160:163],v[32:35]
s_and_b32 s90, %[sb], 7
TR a[0:1],v192 offset:32896
TR a[2:3],v192 offset:33152
buffer_load_dwordx4 v221, s[32:35], s95 offen offset:3072 lds
MM v[32:35],a[20:23],a[164:167],v[32:35]
s_add_u32 m0, s94, 36864
TR a[4:5],v192 offset:41120
TR a[6:7],v192 offset:41376
MM v[40:43],a[20:23],a[172:175],v[40:43]
s_add_u32 s95, s88, 40960
TR a[8:9],v192 offset:32904
TR a[10:11],v192 offset:33160
s_mov_b64 exec, s[52:53]
buffer_store_dwordx4 v[136:139], v168, s[20:23], s82 offen offset:512 sc1
s_mov_b64 exec, -1
MM v[40:43],a[16:19],a[168:171],v[40:43]
s_add_u32 s77, s77, s90
TR a[12:13],v192 offset:41128
TR a[14:15],v192 offset:41384
MM v[48:51],a[16:19],a[176:179],v[48:51]
s_sub_u32 s90, s77, 8
s_cmp_ge_u32 s77, 27
s_cselect_b32 s77, s90, s77
MM v[48:51],a[20:23],a[180:183],v[48:51]
s_lshl_b32 s77, s77, 7
s_lshl_b32 s90, %[sb], 1
s_and_b32 s90, s90, 112
MM v[56:59],a[20:23],a[188:191],v[56:59]
s_add_u32 s48, s77, s90
buffer_load_dwordx4 v220, s[32:35], s95 offen offset:0 lds
MM v[56:59],a[16:19],a[184:187],v[56:59]
s_add_u32 s95, s88, 50176
s_mov_b32 s71, 0x4e800000
s_mov_b32 s72, 0x3e0293ee
MM v[36:39],a[24:27],a[160:163],v[36:39]
s_lshl_b32 s64, %[swave], 12
TR a[16:17],v193 offset:32896
TR a[18:19],v193 offset:33152
MM v[36:39],a[28:31],a[164:167],v[36:39]
TR a[20:21],v193 offset:41120
TR a[22:23],v193 offset:41376
MM v[44:47],a[28:31],a[172:175],v[44:47]
buffer_load_dwordx4 v221, s[32:35], s95 offen offset:1024 lds
MM v[44:47],a[24:27],a[168:171],v[44:47]
s_add_u32 s95, s88, 59392
MM v[52:55],a[24:27],a[176:179],v[52:55]
MM v[52:55],a[28:31],a[180:183],v[52:55]
MM v[60:63],a[28:31],a[188:191],v[60:63]
buffer_load_dwordx4 v220, s[32:35], s95 offen offset:2048 lds
MM v[60:63],a[24:27],a[184:187],v[60:63]
s_add_u32 s95, s88, 68608
MM v[64:67],a[32:35],a[160:163],v[64:67]
TR a[24:25],v193 offset:32904
TR a[26:27],v193 offset:33160
MM v[64:67],a[36:39],a[164:167],v[64:67]
TR a[28:29],v193 offset:41128
TR a[30:31],v193 offset:41384
MM v[72:75],a[36:39],a[172:175],v[72:75]
buffer_load_dwordx4 v221, s[32:35], s95 offen offset:3072 lds
MM v[72:75],a[32:35],a[168:171],v[72:75]
s_add_u32 s87, s89, s85
s_add_u32 s85, s85, 524288
MM v[80:83],a[32:35],a[176:179],v[80:83]
s_cmp_ge_u32 s85, 5242880
s_cselect_b32 s85, 0, s85
s_add_u32 m0, s96, 131328
MM v[80:83],a[36:39],a[180:183],v[80:83]
MM v[88:91],a[36:39],a[188:191],v[88:91]
buffer_load_dwordx4 v253, s[16:19], s87 offen offset:0 lds
MM v[88:91],a[32:35],a[184:187],v[88:91]
s_add_u32 m0, s96, 132352
MM v[68:71],a[40:43],a[160:163],v[68:71]
TR a[32:33],v194 offset:32896
TR a[34:35],v194 offset:33152
MM v[68:71],a[44:47],a[164:167],v[68:71]
TR a[36:37],v194 offset:41120
TR a[38:39],v194 offset:41376
MM v[76:79],a[44:47],a[172:175],v[76:79]
buffer_load_dwordx4 v254, s[16:19], s87 offen offset:0 lds
MM v[76:79],a[40:43],a[168:171],v[76:79]
s_add_u32 s87, s87, 65536
s_add_u32 m0, s96, 133384
MM v[84:87],a[40:43],a[176:179],v[84:87]
MM v[84:87],a[44:47],a[180:183],v[84:87]
MM v[92:95],a[44:47],a[188:191],v[92:95]
buffer_load_dwordx4 v253, s[16:19], s87 offen offset:0 lds
MM v[92:95],a[40:43],a[184:187],v[92:95]
s_add_u32 m0, s96, 134408
MM v[96:99],a[48:51],a[160:163],v[96:99]
TR a[40:41],v194 offset:32904
TR a[42:43],v194 offset:33160
MM v[96:99],a[52:55],a[164:167],v[96:99]
TR a[44:45],v194 offset:41128
TR a[46:47],v194 offset:41384
MM v[104:107],a[52:55],a[172:175],v[104:107]
buffer_load_dwordx4 v254, s[16:19], s87 offen offset:0 lds
MM v[104:107],a[48:51],a[168:171],v[104:107]
s_waitcnt vmcnt(19)
RD a[192:195],v222 offset:0
MM v[112:115],a[48:51],a[176:179],v[112:115]
RD a[196:199],v223 offset:0
MM v[112:115],a[52:55],a[180:183],v[112:115]
RD a[200:203],v217 offset:0
MM v[120:123],a[52:55],a[188:191],v[120:123]
s_mov_b64 exec, s[52:53]
buffer_store_dwordx4 v[140:143], v168, s[20:23], s82 offen offset:768 sc1
s_mov_b64 exec, -1
MM v[120:123],a[48:51],a[184:187],v[120:123]
s_waitcnt lgkmcnt(15)
RD a[204:207],v218 offset:0
MM v[100:103],a[56:59],a[160:163],v[100:103]
TR a[48:49],v195 offset:32896
TR a[50:51],v195 offset:33152
MM v[100:103],a[60:63],a[164:167],v[100:103]
TR a[52:53],v195 offset:41120
TR a[54:55],v195 offset:41376
MM v[108:111],a[60:63],a[172:175],v[108:111]
RD a[208:211],v222 offset:4096
MM v[108:111],a[56:59],a[168:171],v[108:111]
RD a[212:215],v223 offset:4096
MM v[116:119],a[56:59],a[176:179],v[116:119]
s_mov_b64 exec, s[52:53]
buffer_store_dwordx4 v[144:147], v168, s[20:23], s82 offen offset:1024 sc1
s_mov_b64 exec, -1
MM v[116:119],a[60:63],a[180:183],v[116:119]
RD a[216:219],v217 offset:4096
MM v[124:127],a[60:63],a[188:191],v[124:127]
RD a[220:223],v218 offset:4096
MM v[124:127],a[56:59],a[184:187],v[124:127]
s_waitcnt vmcnt(16) lgkmcnt(0)
s_barrier
MM v[0:3],a[0:3],a[192:195],v[0:3]
TR a[56:57],v195 offset:32904
)ASM"
R"ASM(
TR a[58:59],v195 offset:33160
MM v[0:3],a[4:7],a[196:199],v[0:3]
TR a[60:61],v195 offset:41128
TR a[62:63],v195 offset:41384
MM v[8:11],a[4:7],a[204:207],v[8:11]
s_add_u32 s88, s68, s86
s_add_u32 s86, s86, 128
MM v[8:11],a[0:3],a[200:203],v[8:11]
s_cmp_ge_u32 s86, 1280
s_cselect_b32 s86, 0, s86
s_add_u32 m0, s94, 0
MM v[16:19],a[0:3],a[208:211],v[16:19]
s_add_u32 s95, s88, 0
buffer_load_dwordx4 v220, s[32:35], s95 offen offset:0 lds
v_lshlrev_b32 v173, 2, v181
MM v[16:19],a[4:7],a[212:215],v[16:19]
s_add_u32 s95, s88, 9216
s_mov_b64 exec, s[54:55]
buffer_store_dword v160, v173, s[20:23], s78 offen offset:0 sc1
s_mov_b64 exec, -1
MM v[24:27],a[4:7],a[220:223],v[24:27]
buffer_load_dwordx4 v221, s[32:35], s95 offen offset:1024 lds
MM v[24:27],a[0:3],a[216:219],v[24:27]
s_add_u32 s95, s88, 18432
v_mov_b32 v170, s69
v_mov_b32 v171, s69
MM v[4:7],a[8:11],a[192:195],v[4:7]
MM v[4:7],a[12:15],a[196:199],v[4:7]
MM v[12:15],a[12:15],a[204:207],v[12:15]
buffer_load_dwordx4 v220, s[32:35], s95 offen offset:2048 lds
MM v[12:15],a[8:11],a[200:203],v[12:15]
s_add_u32 s95, s88, 27648
MM v[20:23],a[8:11],a[208:211],v[20:23]
MM v[20:23],a[12:15],a[212:215],v[20:23]
MM v[28:31],a[12:15],a[220:223],v[28:31]
buffer_load_dwordx4 v221, s[32:35], s95 offen offset:3072 lds
MM v[28:31],a[8:11],a[216:219],v[28:31]
s_add_u32 m0, s94, 4096
s_add_u32 s95, s88, 40960
v_smfmac_f32_16x16x64_bf16 v[184:187], v[236:239], a[0:7], v234
v_smfmac_f32_16x16x64_bf16 v[188:191], v[236:239], a[8:15], v234
MM v[32:35],a[16:19],a[192:195],v[32:35]
TR a[0:1],v196 offset:0
TR a[2:3],v196 offset:256
MM v[32:35],a[20:23],a[196:199],v[32:35]
TR a[4:5],v196 offset:8224
TR a[6:7],v196 offset:8480
MM v[40:43],a[20:23],a[204:207],v[40:43]
TR a[8:9],v196 offset:8
TR a[10:11],v196 offset:264
buffer_load_dwordx4 v220, s[32:35], s95 offen offset:0 lds
MM v[40:43],a[16:19],a[200:203],v[40:43]
s_add_u32 s95, s88, 50176
TR a[12:13],v196 offset:8232
TR a[14:15],v196 offset:8488
MM v[48:51],a[16:19],a[208:211],v[48:51]
buffer_load_dwordx4 v221, s[32:35], s95 offen offset:1024 lds
MM v[48:51],a[20:23],a[212:215],v[48:51]
s_add_u32 s95, s88, 59392
MM v[56:59],a[20:23],a[220:223],v[56:59]
MM v[56:59],a[16:19],a[216:219],v[56:59]
MM v[36:39],a[24:27],a[192:195],v[36:39]
TR a[16:17],v197 offset:0
TR a[18:19],v197 offset:256
MM v[36:39],a[28:31],a[196:199],v[36:39]
TR a[20:21],v197 offset:8224
TR a[22:23],v197 offset:8480
MM v[44:47],a[28:31],a[204:207],v[44:47]
buffer_load_dwordx4 v220, s[32:35], s95 offen offset:2048 lds
MM v[44:47],a[24:27],a[200:203],v[44:47]
s_add_u32 s95, s88, 68608
MM v[52:55],a[24:27],a[208:211],v[52:55]
buffer_load_dwordx4 v221, s[32:35], s95 offen offset:3072 lds
MM v[52:55],a[28:31],a[212:215],v[52:55]
s_add_u32 s87, s89, s85
s_add_u32 s85, s85, 524288
MM v[60:63],a[28:31],a[220:223],v[60:63]
s_cmp_ge_u32 s85, 5242880
s_cselect_b32 s85, 0, s85
s_add_u32 m0, s96, 65536
MM v[60:63],a[24:27],a[216:219],v[60:63]
MM v[64:67],a[32:35],a[192:195],v[64:67]
TR a[24:25],v197 offset:8
TR a[26:27],v197 offset:264
MM v[64:67],a[36:39],a[196:199],v[64:67]
TR a[28:29],v197 offset:8232
TR a[30:31],v197 offset:8488
MM v[72:75],a[36:39],a[204:207],v[72:75]
buffer_load_dwordx4 v253, s[16:19], s87 offen offset:0 lds
MM v[72:75],a[32:35],a[200:203],v[72:75]
s_add_u32 m0, s96, 66560
MM v[80:83],a[32:35],a[208:211],v[80:83]
buffer_load_dwordx4 v254, s[16:19], s87 offen offset:0 lds
MM v[80:83],a[36:39],a[212:215],v[80:83]
s_add_u32 s87, s87, 65536
s_add_u32 m0, s96, 67592
MM v[88:91],a[36:39],a[220:223],v[88:91]
MM v[88:91],a[32:35],a[216:219],v[88:91]
LPG2_%=:
s_cbranch_execz LPG6_%=
MM v[68:71],a[40:43],a[192:195],v[68:71]
TR a[32:33],v198 offset:0
TR a[34:35],v198 offset:256
MM v[68:71],a[44:47],a[196:199],v[68:71]
TR a[36:37],v198 offset:8224
TR a[38:39],v198 offset:8480
MM v[76:79],a[44:47],a[204:207],v[76:79]
buffer_load_dwordx4 v253, s[16:19], s87 offen offset:0 lds
MM v[76:79],a[40:43],a[200:203],v[76:79]
s_add_u32 m0, s96, 68616
MM v[84:87],a[40:43],a[208:211],v[84:87]
buffer_load_dwordx4 v254, s[16:19], s87 offen offset:0 lds
MM v[84:87],a[44:47],a[212:215],v[84:87]
s_waitcnt vmcnt(19)
RD a[224:227],v222 offset:32768
MM v[92:95],a[44:47],a[220:223],v[92:95]
RD a[228:231],v223 offset:32768
MM v[92:95],a[40:43],a[216:219],v[92:95]
RD a[232:235],v217 offset:32768
MM v[96:99],a[48:51],a[192:195],v[96:99]
TR a[40:41],v198 offset:8
TR a[42:43],v198 offset:264
MM v[96:99],a[52:55],a[196:199],v[96:99]
TR a[44:45],v198 offset:8232
TR a[46:47],v198 offset:8488
MM v[104:107],a[52:55],a[204:207],v[104:107]
RD a[236:239],v218 offset:32768
MM v[104:107],a[48:51],a[200:203],v[104:107]
RD a[240:243],v222 offset:36864
MM v[112:115],a[48:51],a[208:211],v[112:115]
RD a[244:247],v223 offset:36864
MM v[112:115],a[52:55],a[212:215],v[112:115]
RD a[248:251],v217 offset:36864
MM v[120:123],a[52:55],a[220:223],v[120:123]
RD a[252:255],v218 offset:36864
MM v[120:123],a[48:51],a[216:219],v[120:123]
s_waitcnt lgkmcnt(15)
MM v[100:103],a[56:59],a[192:195],v[100:103]
TR a[48:49],v199 offset:0
TR a[50:51],v199 offset:256
MM v[100:103],a[60:63],a[196:199],v[100:103]
TR a[52:53],v199 offset:8224
TR a[54:55],v199 offset:8480
MM v[108:111],a[60:63],a[204:207],v[108:111]
s_waitcnt lgkmcnt(4)
s_add_u32 s88, s68, s86
s_add_u32 s86, s86, 128
MM v[108:111],a[56:59],a[200:203],v[108:111]
s_cmp_ge_u32 s86, 1280
s_cselect_b32 s86, 0, s86
s_add_u32 m0, s94, 32768
MM v[116:119],a[56:59],a[208:211],v[116:119]
s_add_u32 s95, s88, 0
buffer_load_dwordx4 v220, s[32:35], s95 offen offset:0 lds
MM v[116:119],a[60:63],a[212:215],v[116:119]
s_add_u32 s95, s88, 9216
MM v[124:127],a[60:63],a[220:223],v[124:127]
buffer_load_dwordx4 v221, s[32:35], s95 offen offset:1024 lds
MM v[124:127],a[56:59],a[216:219],v[124:127]
s_add_u32 s95, s88, 18432
s_waitcnt vmcnt(17)
s_barrier
MM v[0:3],a[0:3],a[224:227],v[0:3]
TR a[56:57],v199 offset:8
TR a[58:59],v199 offset:264
MM v[0:3],a[4:7],a[228:231],v[0:3]
TR a[60:61],v199 offset:8232
TR a[62:63],v199 offset:8488
MM v[8:11],a[4:7],a[236:239],v[8:11]
buffer_load_dwordx4 v220, s[32:35], s95 offen offset:2048 lds
MM v[8:11],a[0:3],a[232:235],v[8:11]
s_add_u32 s95, s88, 27648
MM v[16:19],a[0:3],a[240:243],v[16:19]
MM v[16:19],a[4:7],a[244:247],v[16:19]
MM v[24:27],a[4:7],a[252:255],v[24:27]
buffer_load_dwordx4 v221, s[32:35], s95 offen offset:3072 lds
MM v[24:27],a[0:3],a[248:251],v[24:27]
s_add_u32 m0, s94, 36864
s_add_u32 s95, s88, 40960
MM v[4:7],a[8:11],a[224:227],v[4:7]
MM v[4:7],a[12:15],a[228:231],v[4:7]
MM v[12:15],a[12:15],a[236:239],v[12:15]
buffer_load_dwordx4 v220, s[32:35], s95 offen offset:0 lds
MM v[12:15],a[8:11],a[232:235],v[12:15]
s_add_u32 s95, s88, 50176
MM v[20:23],a[8:11],a[240:243],v[20:23]
MM v[20:23],a[12:15],a[244:247],v[20:23]
MM v[28:31],a[12:15],a[252:255],v[28:31]
buffer_load_dwordx4 v221, s[32:35], s95 offen offset:1024 lds
MM v[28:31],a[8:11],a[248:251],v[28:31]
s_add_u32 s95, s88, 59392
v_smfmac_f32_16x16x64_bf16 v[184:187], v[236:239], a[0:7], v234
v_smfmac_f32_16x16x64_bf16 v[188:191], v[236:239], a[8:15], v234
MM v[32:35],a[16:19],a[224:227],v[32:35]
TR a[0:1],v196 offset:16448
TR a[2:3],v196 offset:16704
MM v[32:35],a[20:23],a[228:231],v[32:35]
TR a[4:5],v196 offset:24672
TR a[6:7],v196 offset:24928
MM v[40:43],a[20:23],a[236:239],v[40:43]
TR a[8:9],v196 offset:16456
TR a[10:11],v196 offset:16712
buffer_load_dwordx4 v220, s[32:35], s95 offen offset:2048 lds
MM v[40:43],a[16:19],a[232:235],v[40:43]
s_add_u32 s95, s88, 68608
TR a[12:13],v196 offset:24680
TR a[14:15],v196 offset:24936
MM v[48:51],a[16:19],a[240:243],v[48:51]
buffer_load_dwordx4 v221, s[32:35], s95 offen offset:3072 lds
MM v[48:51],a[20:23],a[244:247],v[48:51]
s_add_u32 s87, s89, s85
s_add_u32 s85, s85, 524288
MM v[56:59],a[20:23],a[252:255],v[56:59]
s_cmp_ge_u32 s85, 5242880
s_cselect_b32 s85, 0, s85
s_add_u32 m0, s96, 81984
MM v[56:59],a[16:19],a[248:251],v[56:59]
MM v[36:39],a[24:27],a[224:227],v[36:39]
TR a[16:17],v197 offset:16448
TR a[18:19],v197 offset:16704
MM v[36:39],a[28:31],a[228:231],v[36:39]
TR a[20:21],v197 offset:24672
TR a[22:23],v197 offset:24928
MM v[44:47],a[28:31],a[236:239],v[44:47]
buffer_load_dwordx4 v253, s[16:19], s87 offen offset:0 lds
MM v[44:47],a[24:27],a[232:235],v[44:47]
s_add_u32 m0, s96, 83008
MM v[52:55],a[24:27],a[240:243],v[52:55]
buffer_load_dwordx4 v254, s[16:19], s87 offen offset:0 lds
MM v[52:55],a[28:31],a[244:247],v[52:55]
s_add_u32 s87, s87, 65536
s_add_u32 m0, s96, 84040
MM v[60:63],a[28:31],a[252:255],v[60:63]
MM v[60:63],a[24:27],a[248:251],v[60:63]
MM v[64:67],a[32:35],a[224:227],v[64:67]
TR a[24:25],v197 offset:16456
TR a[26:27],v197 offset:16712
MM v[64:67],a[36:39],a[228:231],v[64:67]
TR a[28:29],v197 offset:24680
TR a[30:31],v197 offset:24936
MM v[72:75],a[36:39],a[236:239],v[72:75]
buffer_load_dwordx4 v253, s[16:19], s87 offen offset:0 lds
MM v[72:75],a[32:35],a[232:235],v[72:75]
s_add_u32 m0, s96, 85064
MM v[80:83],a[32:35],a[240:243],v[80:83]
buffer_load_dwordx4 v254, s[16:19], s87 offen offset:0 lds
MM v[80:83],a[36:39],a[244:247],v[80:83]
s_waitcnt vmcnt(16)
RD a[128:131],v222 offset:0
MM v[88:91],a[36:39],a[252:255],v[88:91]
RD a[132:135],v223 offset:0
MM v[88:91],a[32:35],a[248:251],v[88:91]
RD a[136:139],v217 offset:0
MM v[68:71],a[40:43],a[224:227],v[68:71]
TR a[32:33],v198 offset:16448
TR a[34:35],v198 offset:16704
MM v[68:71],a[44:47],a[228:231],v[68:71]
TR a[36:37],v198 offset:24672
TR a[38:39],v198 offset:24928
MM v[76:79],a[44:47],a[236:239],v[76:79]
RD a[140:143],v218 offset:0
MM v[76:79],a[40:43],a[232:235],v[76:79]
RD a[144:147],v222 offset:4096
MM v[84:87],a[40:43],a[240:243],v[84:87]
RD a[148:151],v223 offset:4096
MM v[84:87],a[44:47],a[244:247],v[84:87]
RD a[152:155],v217 offset:4096
MM v[92:95],a[44:47],a[252:255],v[92:95]
RD a[156:159],v218 offset:4096
MM v[92:95],a[40:43],a[248:251],v[92:95]
s_waitcnt lgkmcnt(15)
MM v[96:99],a[48:51],a[224:227],v[96:99]
TR a[40:41],v198 offset:16456
TR a[42:43],v198 offset:16712
MM v[96:99],a[52:55],a[228:231],v[96:99]
TR a[44:45],v198 offset:24680
TR a[46:47],v198 offset:24936
MM v[104:107],a[52:55],a[236:239],v[104:107]
s_waitcnt lgkmcnt(4)
s_add_u32 s88, s68, s86
s_add_u32 s86, s86, 128
MM v[104:107],a[48:51],a[232:235],v[104:107]
s_cmp_ge_u32 s86, 1280
s_cselect_b32 s86, 0, s86
s_add_u32 m0, s94, 0
MM v[112:115],a[48:51],a[240:243],v[112:115]
s_add_u32 s95, s88, 0
buffer_load_dwordx4 v220, s[32:35], s95 offen offset:0 lds
MM v[112:115],a[52:55],a[244:247],v[112:115]
s_add_u32 s95, s88, 9216
MM v[120:123],a[52:55],a[252:255],v[120:123]
buffer_load_dwordx4 v221, s[32:35], s95 offen offset:1024 lds
MM v[120:123],a[48:51],a[248:251],v[120:123]
s_add_u32 s95, s88, 18432
MM v[100:103],a[56:59],a[224:227],v[100:103]
TR a[48:49],v199 offset:16448
TR a[50:51],v199 offset:16704
MM v[100:103],a[60:63],a[228:231],v[100:103]
TR a[52:53],v199 offset:24672
TR a[54:55],v199 offset:24928
MM v[108:111],a[60:63],a[236:239],v[108:111]
buffer_load_dwordx4 v220, s[32:35], s95 offen offset:2048 lds
MM v[108:111],a[56:59],a[232:235],v[108:111]
s_add_u32 s95, s88, 27648
MM v[116:119],a[56:59],a[240:243],v[116:119]
MM v[116:119],a[60:63],a[244:247],v[116:119]
MM v[124:127],a[60:63],a[252:255],v[124:127]
buffer_load_dwordx4 v221, s[32:35], s95 offen offset:3072 lds
MM v[124:127],a[56:59],a[248:251],v[124:127]
s_add_u32 m0, s94, 4096
s_add_u32 s95, s88, 40960
s_waitcnt vmcnt(16)
s_barrier
MM v[0:3],a[0:3],a[128:131],v[0:3]
TR a[56:57],v199 offset:16456
TR a[58:59],v199 offset:16712
MM v[0:3],a[4:7],a[132:135],v[0:3]
TR a[60:61],v199 offset:24680
TR a[62:63],v199 offset:24936
MM v[8:11],a[4:7],a[140:143],v[8:11]
buffer_load_dwordx4 v220, s[32:35], s95 offen offset:0 lds
MM v[8:11],a[0:3],a[136:139],v[8:11]
s_add_u32 s95, s88, 50176
MM v[16:19],a[0:3],a[144:147],v[16:19]
MM v[16:19],a[4:7],a[148:151],v[16:19]
MM v[24:27],a[4:7],a[156:159],v[24:27]
buffer_load_dwordx4 v221, s[32:35], s95 offen offset:1024 lds
MM v[24:27],a[0:3],a[152:155],v[24:27]
s_add_u32 s95, s88, 59392
MM v[4:7],a[8:11],a[128:131],v[4:7]
MM v[4:7],a[12:15],a[132:135],v[4:7]
MM v[12:15],a[12:15],a[140:143],v[12:15]
)ASM"
R"ASM(
buffer_load_dwordx4 v220, s[32:35], s95 offen offset:2048 lds
MM v[12:15],a[8:11],a[136:139],v[12:15]
s_add_u32 s95, s88, 68608
MM v[20:23],a[8:11],a[144:147],v[20:23]
MM v[20:23],a[12:15],a[148:151],v[20:23]
MM v[28:31],a[12:15],a[156:159],v[28:31]
buffer_load_dwordx4 v221, s[32:35], s95 offen offset:3072 lds
MM v[28:31],a[8:11],a[152:155],v[28:31]
s_add_u32 s87, s89, s85
s_add_u32 s85, s85, 524288
v_smfmac_f32_16x16x64_bf16 v[184:187], v[236:239], a[0:7], v234
s_cmp_ge_u32 s85, 5242880
s_cselect_b32 s85, 0, s85
v_smfmac_f32_16x16x64_bf16 v[188:191], v[236:239], a[8:15], v234
s_add_u32 m0, s96, 98432
MM v[32:35],a[16:19],a[128:131],v[32:35]
TR a[0:1],v192 offset:0
TR a[2:3],v192 offset:256
MM v[32:35],a[20:23],a[132:135],v[32:35]
TR a[4:5],v192 offset:8224
TR a[6:7],v192 offset:8480
MM v[40:43],a[20:23],a[140:143],v[40:43]
TR a[8:9],v192 offset:8
TR a[10:11],v192 offset:264
buffer_load_dwordx4 v253, s[16:19], s87 offen offset:0 lds
MM v[40:43],a[16:19],a[136:139],v[40:43]
s_add_u32 m0, s96, 99456
TR a[12:13],v192 offset:8232
TR a[14:15],v192 offset:8488
MM v[48:51],a[16:19],a[144:147],v[48:51]
buffer_load_dwordx4 v254, s[16:19], s87 offen offset:0 lds
MM v[48:51],a[20:23],a[148:151],v[48:51]
s_add_u32 s87, s87, 65536
s_add_u32 m0, s96, 100488
MM v[56:59],a[20:23],a[156:159],v[56:59]
MM v[56:59],a[16:19],a[152:155],v[56:59]
MM v[36:39],a[24:27],a[128:131],v[36:39]
TR a[16:17],v193 offset:0
TR a[18:19],v193 offset:256
MM v[36:39],a[28:31],a[132:135],v[36:39]
TR a[20:21],v193 offset:8224
TR a[22:23],v193 offset:8480
MM v[44:47],a[28:31],a[140:143],v[44:47]
buffer_load_dwordx4 v253, s[16:19], s87 offen offset:0 lds
MM v[44:47],a[24:27],a[136:139],v[44:47]
s_add_u32 m0, s96, 101512
MM v[52:55],a[24:27],a[144:147],v[52:55]
buffer_load_dwordx4 v254, s[16:19], s87 offen offset:0 lds
MM v[52:55],a[28:31],a[148:151],v[52:55]
s_waitcnt vmcnt(16)
RD a[160:163],v222 offset:32768
MM v[60:63],a[28:31],a[156:159],v[60:63]
RD a[164:167],v223 offset:32768
MM v[60:63],a[24:27],a[152:155],v[60:63]
RD a[168:171],v217 offset:32768
MM v[64:67],a[32:35],a[128:131],v[64:67]
TR a[24:25],v193 offset:8
TR a[26:27],v193 offset:264
MM v[64:67],a[36:39],a[132:135],v[64:67]
TR a[28:29],v193 offset:8232
TR a[30:31],v193 offset:8488
MM v[72:75],a[36:39],a[140:143],v[72:75]
RD a[172:175],v218 offset:32768
MM v[72:75],a[32:35],a[136:139],v[72:75]
RD a[176:179],v222 offset:36864
MM v[80:83],a[32:35],a[144:147],v[80:83]
RD a[180:183],v223 offset:36864
MM v[80:83],a[36:39],a[148:151],v[80:83]
RD a[184:187],v217 offset:36864
MM v[88:91],a[36:39],a[156:159],v[88:91]
RD a[188:191],v218 offset:36864
MM v[88:91],a[32:35],a[152:155],v[88:91]
s_waitcnt lgkmcnt(15)
MM v[68:71],a[40:43],a[128:131],v[68:71]
TR a[32:33],v194 offset:0
TR a[34:35],v194 offset:256
MM v[68:71],a[44:47],a[132:135],v[68:71]
TR a[36:37],v194 offset:8224
TR a[38:39],v194 offset:8480
MM v[76:79],a[44:47],a[140:143],v[76:79]
s_waitcnt lgkmcnt(4)
s_add_u32 s88, s68, s86
s_add_u32 s86, s86, 128
MM v[76:79],a[40:43],a[136:139],v[76:79]
s_cmp_ge_u32 s86, 1280
s_cselect_b32 s86, 0, s86
s_add_u32 m0, s94, 32768
MM v[84:87],a[40:43],a[144:147],v[84:87]
s_add_u32 s95, s88, 0
buffer_load_dwordx4 v220, s[32:35], s95 offen offset:0 lds
MM v[84:87],a[44:47],a[148:151],v[84:87]
s_add_u32 s95, s88, 9216
MM v[92:95],a[44:47],a[156:159],v[92:95]
buffer_load_dwordx4 v221, s[32:35], s95 offen offset:1024 lds
MM v[92:95],a[40:43],a[152:155],v[92:95]
s_add_u32 s95, s88, 18432
MM v[96:99],a[48:51],a[128:131],v[96:99]
TR a[40:41],v194 offset:8
TR a[42:43],v194 offset:264
MM v[96:99],a[52:55],a[132:135],v[96:99]
TR a[44:45],v194 offset:8232
TR a[46:47],v194 offset:8488
MM v[104:107],a[52:55],a[140:143],v[104:107]
buffer_load_dwordx4 v220, s[32:35], s95 offen offset:2048 lds
MM v[104:107],a[48:51],a[136:139],v[104:107]
s_add_u32 s95, s88, 27648
MM v[112:115],a[48:51],a[144:147],v[112:115]
MM v[112:115],a[52:55],a[148:151],v[112:115]
MM v[120:123],a[52:55],a[156:159],v[120:123]
buffer_load_dwordx4 v221, s[32:35], s95 offen offset:3072 lds
MM v[120:123],a[48:51],a[152:155],v[120:123]
s_add_u32 m0, s94, 36864
s_add_u32 s95, s88, 40960
MM v[100:103],a[56:59],a[128:131],v[100:103]
TR a[48:49],v195 offset:0
TR a[50:51],v195 offset:256
MM v[100:103],a[60:63],a[132:135],v[100:103]
TR a[52:53],v195 offset:8224
TR a[54:55],v195 offset:8480
MM v[108:111],a[60:63],a[140:143],v[108:111]
buffer_load_dwordx4 v220, s[32:35], s95 offen offset:0 lds
MM v[108:111],a[56:59],a[136:139],v[108:111]
s_add_u32 s95, s88, 50176
MM v[116:119],a[56:59],a[144:147],v[116:119]
MM v[116:119],a[60:63],a[148:151],v[116:119]
MM v[124:127],a[60:63],a[156:159],v[124:127]
buffer_load_dwordx4 v221, s[32:35], s95 offen offset:1024 lds
MM v[124:127],a[56:59],a[152:155],v[124:127]
s_add_u32 s95, s88, 59392
s_waitcnt vmcnt(18)
s_barrier
MM v[0:3],a[0:3],a[160:163],v[0:3]
TR a[56:57],v195 offset:8
TR a[58:59],v195 offset:264
MM v[0:3],a[4:7],a[164:167],v[0:3]
TR a[60:61],v195 offset:8232
TR a[62:63],v195 offset:8488
MM v[8:11],a[4:7],a[172:175],v[8:11]
buffer_load_dwordx4 v220, s[32:35], s95 offen offset:2048 lds
MM v[8:11],a[0:3],a[168:171],v[8:11]
s_add_u32 s95, s88, 68608
MM v[16:19],a[0:3],a[176:179],v[16:19]
MM v[16:19],a[4:7],a[180:183],v[16:19]
MM v[24:27],a[4:7],a[188:191],v[24:27]
buffer_load_dwordx4 v221, s[32:35], s95 offen offset:3072 lds
MM v[24:27],a[0:3],a[184:187],v[24:27]
s_add_u32 s87, s89, s85
s_add_u32 s85, s85, 524288
MM v[4:7],a[8:11],a[160:163],v[4:7]
s_cmp_ge_u32 s85, 5242880
s_cselect_b32 s85, 0, s85
s_add_u32 m0, s96, 114880
MM v[4:7],a[12:15],a[164:167],v[4:7]
MM v[12:15],a[12:15],a[172:175],v[12:15]
buffer_load_dwordx4 v253, s[16:19], s87 offen offset:0 lds
MM v[12:15],a[8:11],a[168:171],v[12:15]
s_add_u32 m0, s96, 115904
MM v[20:23],a[8:11],a[176:179],v[20:23]
MM v[20:23],a[12:15],a[180:183],v[20:23]
MM v[28:31],a[12:15],a[188:191],v[28:31]
buffer_load_dwordx4 v254, s[16:19], s87 offen offset:0 lds
MM v[28:31],a[8:11],a[184:187],v[28:31]
s_add_u32 s87, s87, 65536
s_add_u32 m0, s96, 116936
v_smfmac_f32_16x16x64_bf16 v[184:187], v[236:239], a[0:7], v234
v_smfmac_f32_16x16x64_bf16 v[188:191], v[236:239], a[8:15], v234
MM v[32:35],a[16:19],a[160:163],v[32:35]
TR a[0:1],v192 offset:16448
TR a[2:3],v192 offset:16704
MM v[32:35],a[20:23],a[164:167],v[32:35]
TR a[4:5],v192 offset:24672
TR a[6:7],v192 offset:24928
MM v[40:43],a[20:23],a[172:175],v[40:43]
TR a[8:9],v192 offset:16456
TR a[10:11],v192 offset:16712
buffer_load_dwordx4 v253, s[16:19], s87 offen offset:0 lds
MM v[40:43],a[16:19],a[168:171],v[40:43]
s_add_u32 m0, s96, 117960
TR a[12:13],v192 offset:24680
TR a[14:15],v192 offset:24936
MM v[48:51],a[16:19],a[176:179],v[48:51]
buffer_load_dwordx4 v254, s[16:19], s87 offen offset:0 lds
MM v[48:51],a[20:23],a[180:183],v[48:51]
s_waitcnt vmcnt(16)
RD a[192:195],v222 offset:0
MM v[56:59],a[20:23],a[188:191],v[56:59]
RD a[196:199],v223 offset:0
MM v[56:59],a[16:19],a[184:187],v[56:59]
RD a[200:203],v217 offset:0
MM v[36:39],a[24:27],a[160:163],v[36:39]
TR a[16:17],v193 offset:16448
TR a[18:19],v193 offset:16704
MM v[36:39],a[28:31],a[164:167],v[36:39]
TR a[20:21],v193 offset:24672
TR a[22:23],v193 offset:24928
MM v[44:47],a[28:31],a[172:175],v[44:47]
RD a[204:207],v218 offset:0
MM v[44:47],a[24:27],a[168:171],v[44:47]
RD a[208:211],v222 offset:4096
MM v[52:55],a[24:27],a[176:179],v[52:55]
RD a[212:215],v223 offset:4096
MM v[52:55],a[28:31],a[180:183],v[52:55]
RD a[216:219],v217 offset:4096
MM v[60:63],a[28:31],a[188:191],v[60:63]
RD a[220:223],v218 offset:4096
MM v[60:63],a[24:27],a[184:187],v[60:63]
s_waitcnt lgkmcnt(15)
MM v[64:67],a[32:35],a[160:163],v[64:67]
TR a[24:25],v193 offset:16456
TR a[26:27],v193 offset:16712
MM v[64:67],a[36:39],a[164:167],v[64:67]
TR a[28:29],v193 offset:24680
TR a[30:31],v193 offset:24936
MM v[72:75],a[36:39],a[172:175],v[72:75]
s_waitcnt lgkmcnt(4)
s_add_u32 s88, s68, s86
s_add_u32 s86, s86, 128
MM v[72:75],a[32:35],a[168:171],v[72:75]
s_cmp_ge_u32 s86, 1280
s_cselect_b32 s86, 0, s86
s_add_u32 m0, s94, 0
MM v[80:83],a[32:35],a[176:179],v[80:83]
s_add_u32 s95, s88, 0
buffer_load_dwordx4 v220, s[32:35], s95 offen offset:0 lds
MM v[80:83],a[36:39],a[180:183],v[80:83]
s_add_u32 s95, s88, 9216
MM v[88:91],a[36:39],a[188:191],v[88:91]
buffer_load_dwordx4 v221, s[32:35], s95 offen offset:1024 lds
MM v[88:91],a[32:35],a[184:187],v[88:91]
s_add_u32 s95, s88, 18432
MM v[68:71],a[40:43],a[160:163],v[68:71]
TR a[32:33],v194 offset:16448
TR a[34:35],v194 offset:16704
MM v[68:71],a[44:47],a[164:167],v[68:71]
TR a[36:37],v194 offset:24672
TR a[38:39],v194 offset:24928
MM v[76:79],a[44:47],a[172:175],v[76:79]
buffer_load_dwordx4 v220, s[32:35], s95 offen offset:2048 lds
MM v[76:79],a[40:43],a[168:171],v[76:79]
s_add_u32 s95, s88, 27648
MM v[84:87],a[40:43],a[176:179],v[84:87]
MM v[84:87],a[44:47],a[180:183],v[84:87]
MM v[92:95],a[44:47],a[188:191],v[92:95]
buffer_load_dwordx4 v221, s[32:35], s95 offen offset:3072 lds
MM v[92:95],a[40:43],a[184:187],v[92:95]
s_add_u32 m0, s94, 4096
s_add_u32 s95, s88, 40960
MM v[96:99],a[48:51],a[160:163],v[96:99]
TR a[40:41],v194 offset:16456
TR a[42:43],v194 offset:16712
MM v[96:99],a[52:55],a[164:167],v[96:99]
TR a[44:45],v194 offset:24680
TR a[46:47],v194 offset:24936
MM v[104:107],a[52:55],a[172:175],v[104:107]
buffer_load_dwordx4 v220, s[32:35], s95 offen offset:0 lds
MM v[104:107],a[48:51],a[168:171],v[104:107]
s_add_u32 s95, s88, 50176
MM v[112:115],a[48:51],a[176:179],v[112:115]
MM v[112:115],a[52:55],a[180:183],v[112:115]
MM v[120:123],a[52:55],a[188:191],v[120:123]
buffer_load_dwordx4 v221, s[32:35], s95 offen offset:1024 lds
MM v[120:123],a[48:51],a[184:187],v[120:123]
s_add_u32 s95, s88, 59392
MM v[100:103],a[56:59],a[160:163],v[100:103]
TR a[48:49],v195 offset:16448
TR a[50:51],v195 offset:16704
MM v[100:103],a[60:63],a[164:167],v[100:103]
TR a[52:53],v195 offset:24672
TR a[54:55],v195 offset:24928
MM v[108:111],a[60:63],a[172:175],v[108:111]
buffer_load_dwordx4 v220, s[32:35], s95 offen offset:2048 lds
MM v[108:111],a[56:59],a[168:171],v[108:111]
s_add_u32 s95, s88, 68608
MM v[116:119],a[56:59],a[176:179],v[116:119]
MM v[116:119],a[60:63],a[180:183],v[116:119]
MM v[124:127],a[60:63],a[188:191],v[124:127]
buffer_load_dwordx4 v221, s[32:35], s95 offen offset:3072 lds
MM v[124:127],a[56:59],a[184:187],v[124:127]
s_add_u32 s87, s89, s85
s_add_u32 s85, s85, 524288
s_waitcnt vmcnt(20)
s_barrier
MM v[0:3],a[0:3],a[192:195],v[0:3]
TR a[56:57],v195 offset:16456
TR a[58:59],v195 offset:16712
MM v[0:3],a[4:7],a[196:199],v[0:3]
TR a[60:61],v195 offset:24680
TR a[62:63],v195 offset:24936
s_cmp_ge_u32 s85, 5242880
s_cselect_b32 s85, 0, s85
MM v[8:11],a[4:7],a[204:207],v[8:11]
s_add_u32 m0, s96, 131328
s_nop 0
buffer_load_dwordx4 v253, s[16:19], s87 offen offset:0 lds
v_and_b32 v208, 240, v248
MM v[8:11],a[0:3],a[200:203],v[8:11]
s_add_u32 m0, s96, 132352
s_add_u32 s93, s93, 36864
MM v[16:19],a[0:3],a[208:211],v[16:19]
MM v[16:19],a[4:7],a[212:215],v[16:19]
MM v[24:27],a[4:7],a[220:223],v[24:27]
LPG3_%=:
s_cbranch_execz LPG7_%=
buffer_load_dwordx4 v254, s[16:19], s87 offen offset:0 lds
MM v[24:27],a[0:3],a[216:219],v[24:27]
s_add_u32 s87, s87, 65536
s_add_u32 m0, s96, 133384
MM v[4:7],a[8:11],a[192:195],v[4:7]
MM v[4:7],a[12:15],a[196:199],v[4:7]
MM v[12:15],a[12:15],a[204:207],v[12:15]
buffer_load_dwordx4 v253, s[16:19], s87 offen offset:0 lds
MM v[12:15],a[8:11],a[200:203],v[12:15]
s_add_u32 m0, s96, 134408
MM v[20:23],a[8:11],a[208:211],v[20:23]
MM v[20:23],a[12:15],a[212:215],v[20:23]
MM v[28:31],a[12:15],a[220:223],v[28:31]
buffer_load_dwordx4 v254, s[16:19], s87 offen offset:0 lds
MM v[28:31],a[8:11],a[216:219],v[28:31]
s_waitcnt vmcnt(16)
RD a[224:227],v222 offset:32768
v_smfmac_f32_16x16x64_bf16 v[184:187], v[236:239], a[0:7], v234
RD a[228:231],v223 offset:32768
v_smfmac_f32_16x16x64_bf16 v[188:191], v[236:239], a[8:15], v234
RD a[232:235],v217 offset:32768
MM v[32:35],a[16:19],a[192:195],v[32:35]
TR a[0:1],v192 offset:32896
TR a[2:3],v192 offset:33152
MM v[32:35],a[20:23],a[196:199],v[32:35]
TR a[4:5],v192 offset:41120
TR a[6:7],v192 offset:41376
)ASM"
R"ASM(
MM v[40:43],a[20:23],a[204:207],v[40:43]
TR a[8:9],v192 offset:32904
TR a[10:11],v192 offset:33160
RD a[236:239],v218 offset:32768
buffer_load_dwordx4 v[200:203], v208, s[20:23], s93 offen offset:0 sc1
MM v[40:43],a[16:19],a[200:203],v[40:43]
TR a[12:13],v192 offset:41128
TR a[14:15],v192 offset:41384
MM v[48:51],a[16:19],a[208:211],v[48:51]
buffer_load_dwordx4 v[204:207], v208, s[20:23], s93 offen offset:256 sc1
MM v[48:51],a[20:23],a[212:215],v[48:51]
RD a[240:243],v222 offset:36864
MM v[56:59],a[20:23],a[220:223],v[56:59]
RD a[244:247],v223 offset:36864
MM v[56:59],a[16:19],a[216:219],v[56:59]
s_waitcnt lgkmcnt(15)
RD a[248:251],v217 offset:36864
MM v[36:39],a[24:27],a[192:195],v[36:39]
TR a[16:17],v193 offset:32896
TR a[18:19],v193 offset:33152
MM v[36:39],a[28:31],a[196:199],v[36:39]
TR a[20:21],v193 offset:41120
TR a[22:23],v193 offset:41376
MM v[44:47],a[28:31],a[204:207],v[44:47]
RD a[252:255],v218 offset:36864
MM v[44:47],a[24:27],a[200:203],v[44:47]
MM v[52:55],a[24:27],a[208:211],v[52:55]
MM v[52:55],a[28:31],a[212:215],v[52:55]
MM v[60:63],a[28:31],a[220:223],v[60:63]
s_waitcnt lgkmcnt(0)
s_add_u32 s88, s68, s86
s_add_u32 s86, s86, 128
MM v[60:63],a[24:27],a[216:219],v[60:63]
s_cmp_ge_u32 s86, 1280
s_cselect_b32 s86, 0, s86
s_add_u32 m0, s94, 32768
MM v[64:67],a[32:35],a[192:195],v[64:67]
s_add_u32 s95, s88, 0
TR a[24:25],v193 offset:32904
TR a[26:27],v193 offset:33160
MM v[64:67],a[36:39],a[196:199],v[64:67]
TR a[28:29],v193 offset:41128
TR a[30:31],v193 offset:41384
MM v[72:75],a[36:39],a[204:207],v[72:75]
buffer_load_dwordx4 v220, s[32:35], s95 offen offset:0 lds
MM v[72:75],a[32:35],a[200:203],v[72:75]
s_add_u32 s95, s88, 9216
MM v[80:83],a[32:35],a[208:211],v[80:83]
buffer_load_dwordx4 v221, s[32:35], s95 offen offset:1024 lds
MM v[80:83],a[36:39],a[212:215],v[80:83]
s_add_u32 s95, s88, 18432
MM v[88:91],a[36:39],a[220:223],v[88:91]
MM v[88:91],a[32:35],a[216:219],v[88:91]
MM v[68:71],a[40:43],a[192:195],v[68:71]
TR a[32:33],v194 offset:32896
TR a[34:35],v194 offset:33152
MM v[68:71],a[44:47],a[196:199],v[68:71]
TR a[36:37],v194 offset:41120
TR a[38:39],v194 offset:41376
MM v[76:79],a[44:47],a[204:207],v[76:79]
buffer_load_dwordx4 v220, s[32:35], s95 offen offset:2048 lds
MM v[76:79],a[40:43],a[200:203],v[76:79]
s_add_u32 s95, s88, 27648
MM v[84:87],a[40:43],a[208:211],v[84:87]
buffer_load_dwordx4 v221, s[32:35], s95 offen offset:3072 lds
MM v[84:87],a[44:47],a[212:215],v[84:87]
s_waitcnt vmcnt(10)
RD a[128:131],v222 offset:0
MM v[92:95],a[44:47],a[220:223],v[92:95]
RD a[132:135],v223 offset:0
MM v[92:95],a[40:43],a[216:219],v[92:95]
RD a[136:139],v217 offset:0
MM v[96:99],a[48:51],a[192:195],v[96:99]
TR a[40:41],v194 offset:32904
TR a[42:43],v194 offset:33160
MM v[96:99],a[52:55],a[196:199],v[96:99]
TR a[44:45],v194 offset:41128
TR a[46:47],v194 offset:41384
MM v[104:107],a[52:55],a[204:207],v[104:107]
RD a[140:143],v218 offset:0
MM v[104:107],a[48:51],a[200:203],v[104:107]
RD a[144:147],v222 offset:4096
MM v[112:115],a[48:51],a[208:211],v[112:115]
RD a[148:151],v223 offset:4096
MM v[112:115],a[52:55],a[212:215],v[112:115]
RD a[152:155],v217 offset:4096
MM v[120:123],a[52:55],a[220:223],v[120:123]
RD a[156:159],v218 offset:4096
MM v[120:123],a[48:51],a[216:219],v[120:123]
s_add_u32 m0, s94, 36864
s_add_u32 s95, s88, 40960
MM v[100:103],a[56:59],a[192:195],v[100:103]
TR a[48:49],v195 offset:32896
TR a[50:51],v195 offset:33152
MM v[100:103],a[60:63],a[196:199],v[100:103]
TR a[52:53],v195 offset:41120
TR a[54:55],v195 offset:41376
buffer_load_dwordx4 v220, s[32:35], s95 offen offset:0 lds
MM v[108:111],a[60:63],a[204:207],v[108:111]
s_add_u32 s95, s88, 50176
MM v[108:111],a[56:59],a[200:203],v[108:111]
buffer_load_dwordx4 v221, s[32:35], s95 offen offset:1024 lds
MM v[116:119],a[56:59],a[208:211],v[116:119]
s_add_u32 s95, s88, 59392
MM v[116:119],a[60:63],a[212:215],v[116:119]
MM v[124:127],a[60:63],a[220:223],v[124:127]
MM v[124:127],a[56:59],a[216:219],v[124:127]
buffer_load_dwordx4 v220, s[32:35], s95 offen offset:2048 lds
s_add_u32 s95, s88, 68608
buffer_load_dwordx4 v221, s[32:35], s95 offen offset:3072 lds
s_waitcnt lgkmcnt(4)
s_waitcnt vmcnt(10)
s_barrier
MM v[0:3],a[0:3],a[224:227],v[0:3]
s_add_u32 s77, s64, 0
TR a[56:57],v195 offset:32904
TR a[58:59],v195 offset:33160
MM v[0:3],a[4:7],a[228:231],v[0:3]
s_add_u32 m0, s65, 0
TR a[60:61],v195 offset:41128
TR a[62:63],v195 offset:41384
MM v[8:11],a[4:7],a[236:239],v[8:11]
s_waitcnt vmcnt(8)
TR a[64:65],v196 offset:0
TR a[66:67],v196 offset:256
MM v[8:11],a[0:3],a[232:235],v[8:11]
s_lshl_b32 s76, %[swave], 7
v_lshrrev_b32 v253, 8, v248
v_lshlrev_b32 v253, 5, v253
MM v[16:19],a[0:3],a[240:243],v[16:19]
s_add_u32 s76, s76, %[slds]
TR a[68:69],v196 offset:8224
TR a[70:71],v196 offset:8480
v_xor_b32 v201, s69, v201
MM v[16:19],a[4:7],a[244:247],v[16:19]
buffer_load_dwordx4 v228, s[8:11], s77 offen offset:0 lds
s_mov_b64 exec, s[54:55]
buffer_store_dwordx2 v[170:171], off, s[20:23], s80 sc1
s_mov_b64 exec, -1
v_add_u32 v253, s76, v253
v_add_u32 v253, 151552, v253
MM v[24:27],a[4:7],a[252:255],v[24:27]
TR a[72:73],v196 offset:16448
TR a[74:75],v196 offset:16704
MM v[24:27],a[0:3],a[248:251],v[24:27]
TR a[76:77],v196 offset:24672
TR a[78:79],v196 offset:24928
v_xor_b32 v203, s69, v203
MM v[4:7],a[8:11],a[224:227],v[4:7]
TR a[80:81],v196 offset:8
TR a[82:83],v196 offset:264
MM v[4:7],a[12:15],a[228:231],v[4:7]
TR a[84:85],v196 offset:8232
TR a[86:87],v196 offset:8488
buffer_load_dwordx4 v229, s[8:11], s77 offen offset:1024 lds
MM v[12:15],a[12:15],a[236:239],v[12:15]
TR a[88:89],v196 offset:16456
TR a[90:91],v196 offset:16712
v_xor_b32 v205, s69, v205
MM v[12:15],a[8:11],a[232:235],v[12:15]
TR a[92:93],v196 offset:24680
TR a[94:95],v196 offset:24936
MM v[20:23],a[8:11],a[240:243],v[20:23]
TR a[96:97],v197 offset:0
TR a[98:99],v197 offset:256
MM v[20:23],a[12:15],a[244:247],v[20:23]
TR a[100:101],v197 offset:8224
TR a[102:103],v197 offset:8480
buffer_load_dwordx4 v230, s[8:11], s77 offen offset:2048 lds
v_xor_b32 v207, s69, v207
MM v[28:31],a[12:15],a[252:255],v[28:31]
TR a[104:105],v197 offset:16448
TR a[106:107],v197 offset:16704
MM v[28:31],a[8:11],a[248:251],v[28:31]
TR a[108:109],v197 offset:24672
TR a[110:111],v197 offset:24928
v_smfmac_f32_16x16x64_bf16 v[184:187], v[236:239], a[0:7], v234
TR a[112:113],v197 offset:8
v_or3_b32 v209, v201, v203, v205
v_smfmac_f32_16x16x64_bf16 v[188:191], v[236:239], a[8:15], v234
TR a[114:115],v197 offset:264
buffer_load_dwordx4 v231, s[8:11], s77 offen offset:3072 lds
MM v[32:35],a[16:19],a[224:227],v[32:35]
s_add_u32 s77, s64, 16384
TR a[116:117],v197 offset:8232
TR a[118:119],v197 offset:8488
MM v[32:35],a[20:23],a[228:231],v[32:35]
s_add_u32 m0, s65, 16384
TR a[120:121],v197 offset:16456
TR a[122:123],v197 offset:16712
v_or_b32 v209, v209, v207
MM v[40:43],a[20:23],a[236:239],v[40:43]
TR a[124:125],v197 offset:24680
TR a[126:127],v197 offset:24936
MM v[40:43],a[16:19],a[232:235],v[40:43]
TR a[0:1],v198 offset:0
TR a[2:3],v198 offset:256
MM v[48:51],a[16:19],a[240:243],v[48:51]
TR a[4:5],v198 offset:8224
TR a[6:7],v198 offset:8480
v_cmp_ne_u32 vcc, 0, v209
s_cbranch_vccnz LRG1o_%=
LRG1ob_%=:
MM v[48:51],a[20:23],a[244:247],v[48:51]
TR a[8:9],v198 offset:16448
TR a[10:11],v198 offset:16704
buffer_load_dwordx4 v228, s[8:11], s77 offen offset:0 lds
MM v[56:59],a[20:23],a[252:255],v[56:59]
TR a[12:13],v198 offset:24672
TR a[14:15],v198 offset:24928
MM v[56:59],a[16:19],a[248:251],v[56:59]
s_bitcmp1_b32 %[sb], 0
s_cselect_b64 exec, -1, 0
v_swap_b32 v200, v202
v_swap_b32 v204, v206
s_mov_b64 exec, -1
MM v[36:39],a[24:27],a[224:227],v[36:39]
v_lshlrev_b32 v240, 16, v200
v_and_b32 v200, 0xffff0000, v200
MM v[36:39],a[28:31],a[228:231],v[36:39]
v_rcp_f32 v246, v200
MM v[44:47],a[28:31],a[236:239],v[44:47]
v_xor_b32 v240, 0x80000000, v240
v_mul_f32 v246, s72, v246
MM v[44:47],a[24:27],a[232:235],v[44:47]
v_lshlrev_b32 v241, 16, v202
v_and_b32 v202, 0xffff0000, v202
MM v[52:55],a[24:27],a[240:243],v[52:55]
v_rcp_f32 v247, v202
MM v[52:55],a[28:31],a[244:247],v[52:55]
buffer_load_dwordx4 v229, s[8:11], s77 offen offset:1024 lds
MM v[60:63],a[28:31],a[252:255],v[60:63]
v_xor_b32 v241, 0x80000000, v241
v_mul_f32 v247, s72, v247
MM v[60:63],a[24:27],a[248:251],v[60:63]
v_lshlrev_b32 v242, 16, v204
v_and_b32 v204, 0xffff0000, v204
MM v[64:67],a[32:35],a[224:227],v[64:67]
TR a[16:17],v198 offset:8
TR a[18:19],v198 offset:264
MM v[64:67],a[36:39],a[228:231],v[64:67]
TR a[20:21],v198 offset:8232
TR a[22:23],v198 offset:8488
v_rcp_f32 v244, v204
MM v[72:75],a[36:39],a[236:239],v[72:75]
TR a[24:25],v198 offset:16456
TR a[26:27],v198 offset:16712
MM v[72:75],a[32:35],a[232:235],v[72:75]
TR a[28:29],v198 offset:24680
TR a[30:31],v198 offset:24936
MM v[80:83],a[32:35],a[240:243],v[80:83]
v_xor_b32 v242, 0x80000000, v242
v_mul_f32 v244, s72, v244
MM v[80:83],a[36:39],a[244:247],v[80:83]
v_lshlrev_b32 v243, 16, v206
v_and_b32 v206, 0xffff0000, v206
MM v[88:91],a[36:39],a[252:255],v[88:91]
v_rcp_f32 v245, v206
MM v[88:91],a[32:35],a[248:251],v[88:91]
s_waitcnt lgkmcnt(15)
buffer_load_dwordx4 v230, s[8:11], s77 offen offset:2048 lds
v_smfmac_f32_16x16x64_bf16 v[184:187], v[236:239], a[64:71], v234
v_xor_b32 v243, 0x80000000, v243
v_smfmac_f32_16x16x64_bf16 v[184:187], v[236:239], a[72:79], v234
v_mul_f32 v245, s72, v245
v_smfmac_f32_16x16x64_bf16 v[188:191], v[236:239], a[80:87], v234
v_smfmac_f32_16x16x64_bf16 v[188:191], v[236:239], a[88:95], v234
MM v[68:71],a[40:43],a[224:227],v[68:71]
v_bfe_u32 v183, v248, 6, 2
v_lshlrev_b32 v183, 5, v183
MM v[68:71],a[44:47],a[228:231],v[68:71]
v_bfe_u32 v182, v248, 4, 2
v_lshl_add_u32 v183, v182, 2, v183
MM v[76:79],a[44:47],a[236:239],v[76:79]
v_add_u32 v183, s76, v183
v_add_u32 v183, 151552, v183
MM v[76:79],a[40:43],a[232:235],v[76:79]
MM v[84:87],a[40:43],a[240:243],v[84:87]
MM v[84:87],a[44:47],a[244:247],v[84:87]
buffer_load_dwordx4 v231, s[8:11], s77 offen offset:3072 lds
MM v[92:95],a[44:47],a[252:255],v[92:95]
s_waitcnt vmcnt(9)
RD a[160:163],v222 offset:32768
MM v[92:95],a[40:43],a[248:251],v[92:95]
v_add_f32 v184, v184, v185
v_add_f32 v188, v188, v189
MM v[96:99],a[48:51],a[224:227],v[96:99]
TR a[32:33],v199 offset:0
TR a[34:35],v199 offset:256
MM v[96:99],a[52:55],a[228:231],v[96:99]
RD a[164:167],v223 offset:32768
ds_write_b32 v183, v184 offset:0
ds_write_b32 v183, v188 offset:16
MM v[104:107],a[52:55],a[236:239],v[104:107]
TR a[36:37],v199 offset:8224
TR a[38:39],v199 offset:8480
MM v[104:107],a[48:51],a[232:235],v[104:107]
TR a[40:41],v199 offset:16448
TR a[42:43],v199 offset:16704
MM v[112:115],a[48:51],a[240:243],v[112:115]
TR a[44:45],v199 offset:24672
TR a[46:47],v199 offset:24928
RD a[168:171],v217 offset:32768
MM v[112:115],a[52:55],a[244:247],v[112:115]
RD a[172:175],v218 offset:32768
MM v[120:123],a[52:55],a[252:255],v[120:123]
RD a[176:179],v222 offset:36864
MM v[120:123],a[48:51],a[248:251],v[120:123]
RD a[180:183],v223 offset:36864
MM v[100:103],a[56:59],a[224:227],v[100:103]
RD a[184:187],v217 offset:36864
MM v[100:103],a[60:63],a[228:231],v[100:103]
RD a[188:191],v218 offset:36864
MM v[108:111],a[60:63],a[236:239],v[108:111]
s_add_u32 s77, s64, 0
s_add_u32 s81, s77, 896
s_add_u32 m0, s65, 65536
MM v[108:111],a[56:59],a[232:235],v[108:111]
buffer_load_dwordx4 v235, s[12:15], s81 offen offset:0 lds
MM v[116:119],a[56:59],a[240:243],v[116:119]
MM v[116:119],a[60:63],a[244:247],v[116:119]
MM v[124:127],a[60:63],a[252:255],v[124:127]
MM v[124:127],a[56:59],a[248:251],v[124:127]
s_waitcnt lgkmcnt(0)
s_barrier
MM v[0:3],a[64:67],a[128:131],v[0:3]
s_mov_b32 s56, 0x000f000f
TR a[48:49],v199 offset:8
TR a[50:51],v199 offset:264
MM v[0:3],a[68:71],a[132:135],v[0:3]
s_mov_b32 s57, s56
TR a[52:53],v199 offset:8232
TR a[54:55],v199 offset:8488
MM v[0:3],a[72:75],a[160:163],v[0:3]
s_lshl_b64 s[58:59], s[56:57], 4
TR a[56:57],v199 offset:16456
TR a[58:59],v199 offset:16712
MM v[0:3],a[76:79],a[164:167],v[0:3]
TR a[60:61],v199 offset:24680
TR a[62:63],v199 offset:24936
buffer_load_dwordx4 v235, s[12:15], s77 offen offset:1024 lds
MM v[8:11],a[76:79],a[172:175],v[8:11]
)ASM"
R"ASM(
v_xor_b32 v151, 0, v253
v_xor_b32 v143, 0, v253
MM v[8:11],a[72:75],a[168:171],v[8:11]
RD v[144:147],v151 offset:0
RD v[148:151],v151 offset:16
MM v[8:11],a[68:71],a[140:143],v[8:11]
RD v[136:139],v143 offset:0
RD v[140:143],v143 offset:16
MM v[8:11],a[64:67],a[136:139],v[8:11]
MM v[4:7],a[80:83],a[128:131],v[4:7]
MM v[4:7],a[84:87],a[132:135],v[4:7]
s_waitcnt lgkmcnt(2)
MM v[4:7],a[88:91],a[160:163],v[4:7]
s_waitcnt lgkmcnt(0)
MM v[4:7],a[92:95],a[164:167],v[4:7]
buffer_load_dwordx4 v235, s[12:15], s81 offen offset:2048 lds
MM v[12:15],a[92:95],a[172:175],v[12:15]
MM v[12:15],a[88:91],a[168:171],v[12:15]
MM v[12:15],a[84:87],a[140:143],v[12:15]
MM v[12:15],a[80:83],a[136:139],v[12:15]
MM v[32:35],a[96:99],a[128:131],v[32:35]
MM v[32:35],a[100:103],a[132:135],v[32:35]
MM v[32:35],a[104:107],a[160:163],v[32:35]
MM v[32:35],a[108:111],a[164:167],v[32:35]
v_pk_fma_f32 v[0:1], v[240:241], v[144:145], v[0:1] op_sel:[0,0,0] op_sel_hi:[0,1,1]
v_pk_fma_f32 v[2:3], v[240:241], v[146:147], v[2:3] op_sel:[0,0,0] op_sel_hi:[0,1,1]
v_pk_fma_f32 v[4:5], v[240:241], v[148:149], v[4:5] op_sel:[0,0,0] op_sel_hi:[0,1,1]
v_pk_fma_f32 v[6:7], v[240:241], v[150:151], v[6:7] op_sel:[0,0,0] op_sel_hi:[0,1,1]
v_pk_mul_f32 v[0:1], v[246:247], v[0:1] op_sel:[0,0] op_sel_hi:[0,1]
v_pk_mul_f32 v[2:3], v[246:247], v[2:3] op_sel:[0,0] op_sel_hi:[0,1]
v_pk_mul_f32 v[4:5], v[246:247], v[4:5] op_sel:[0,0] op_sel_hi:[0,1]
v_pk_mul_f32 v[6:7], v[246:247], v[6:7] op_sel:[0,0] op_sel_hi:[0,1]
MM v[40:43],a[108:111],a[172:175],v[40:43]
CV v160,v0,v1
CV v161,v2,v3
MM v[40:43],a[104:107],a[168:171],v[40:43]
buffer_load_dwordx4 v235, s[12:15], s77 offen offset:3072 lds
MM v[40:43],a[100:103],a[140:143],v[40:43]
s_add_u32 s77, s64, 32768
s_add_u32 m0, s65, 32768
CV v162,v4,v5
MM v[40:43],a[96:99],a[136:139],v[40:43]
CV v163,v6,v7
MM v[36:39],a[112:115],a[128:131],v[36:39]
v_pk_fma_f32 v[8:9], v[240:241], v[144:145], v[8:9] op_sel:[1,0,0] op_sel_hi:[1,1,1]
v_pk_fma_f32 v[10:11], v[240:241], v[146:147], v[10:11] op_sel:[1,0,0] op_sel_hi:[1,1,1]
v_pk_fma_f32 v[12:13], v[240:241], v[148:149], v[12:13] op_sel:[1,0,0] op_sel_hi:[1,1,1]
v_pk_fma_f32 v[14:15], v[240:241], v[150:151], v[14:15] op_sel:[1,0,0] op_sel_hi:[1,1,1]
v_pk_mul_f32 v[8:9], v[246:247], v[8:9] op_sel:[1,0] op_sel_hi:[1,1]
v_pk_mul_f32 v[10:11], v[246:247], v[10:11] op_sel:[1,0] op_sel_hi:[1,1]
v_pk_mul_f32 v[12:13], v[246:247], v[12:13] op_sel:[1,0] op_sel_hi:[1,1]
v_pk_mul_f32 v[14:15], v[246:247], v[14:15] op_sel:[1,0] op_sel_hi:[1,1]
MM v[36:39],a[116:119],a[132:135],v[36:39]
CV v164,v8,v9
CV v165,v10,v11
MM v[36:39],a[120:123],a[160:163],v[36:39]
CV v166,v12,v13
CV v167,v14,v15
MM v[36:39],a[124:127],a[164:167],v[36:39]
v_xor_b32 v159, 128, v253
MM v[44:47],a[124:127],a[172:175],v[44:47]
RD v[152:155],v159 offset:0
RD v[156:159],v159 offset:16
MM v[44:47],a[120:123],a[168:171],v[44:47]
MM v[44:47],a[116:119],a[140:143],v[44:47]
MM v[44:47],a[112:115],a[136:139],v[44:47]
buffer_load_dwordx4 v228, s[8:11], s77 offen offset:0 lds
MM v[64:67],a[0:3],a[128:131],v[64:67]
s_waitcnt lgkmcnt(0)
MM v[64:67],a[4:7],a[132:135],v[64:67]
MM v[64:67],a[8:11],a[160:163],v[64:67]
MM v[64:67],a[12:15],a[164:167],v[64:67]
v_pk_fma_f32 v[32:33], v[240:241], v[152:153], v[32:33] op_sel:[0,0,0] op_sel_hi:[0,1,1]
v_pk_fma_f32 v[34:35], v[240:241], v[154:155], v[34:35] op_sel:[0,0,0] op_sel_hi:[0,1,1]
v_pk_fma_f32 v[36:37], v[240:241], v[156:157], v[36:37] op_sel:[0,0,0] op_sel_hi:[0,1,1]
v_pk_fma_f32 v[38:39], v[240:241], v[158:159], v[38:39] op_sel:[0,0,0] op_sel_hi:[0,1,1]
v_pk_mul_f32 v[32:33], v[246:247], v[32:33] op_sel:[0,0] op_sel_hi:[0,1]
v_pk_mul_f32 v[34:35], v[246:247], v[34:35] op_sel:[0,0] op_sel_hi:[0,1]
v_pk_mul_f32 v[36:37], v[246:247], v[36:37] op_sel:[0,0] op_sel_hi:[0,1]
v_pk_mul_f32 v[38:39], v[246:247], v[38:39] op_sel:[0,0] op_sel_hi:[0,1]
MM v[72:75],a[12:15],a[172:175],v[72:75]
CV v176,v32,v33
CV v177,v34,v35
MM v[72:75],a[8:11],a[168:171],v[72:75]
CV v178,v36,v37
CV v179,v38,v39
MM v[72:75],a[4:7],a[140:143],v[72:75]
v_pk_fma_f32 v[40:41], v[240:241], v[152:153], v[40:41] op_sel:[1,0,0] op_sel_hi:[1,1,1]
v_pk_fma_f32 v[42:43], v[240:241], v[154:155], v[42:43] op_sel:[1,0,0] op_sel_hi:[1,1,1]
v_pk_fma_f32 v[44:45], v[240:241], v[156:157], v[44:45] op_sel:[1,0,0] op_sel_hi:[1,1,1]
v_pk_fma_f32 v[46:47], v[240:241], v[158:159], v[46:47] op_sel:[1,0,0] op_sel_hi:[1,1,1]
v_pk_mul_f32 v[40:41], v[246:247], v[40:41] op_sel:[1,0] op_sel_hi:[1,1]
v_pk_mul_f32 v[42:43], v[246:247], v[42:43] op_sel:[1,0] op_sel_hi:[1,1]
v_pk_mul_f32 v[44:45], v[246:247], v[44:45] op_sel:[1,0] op_sel_hi:[1,1]
v_pk_mul_f32 v[46:47], v[246:247], v[46:47] op_sel:[1,0] op_sel_hi:[1,1]
MM v[72:75],a[0:3],a[136:139],v[72:75]
CV v180,v40,v41
CV v181,v42,v43
MM v[68:71],a[16:19],a[128:131],v[68:71]
CV v182,v44,v45
CV v183,v46,v47
MM v[68:71],a[20:23],a[132:135],v[68:71]
buffer_load_dwordx4 v229, s[8:11], s77 offen offset:1024 lds
MM v[68:71],a[24:27],a[160:163],v[68:71]
v_xor_b32 v151, 256, v253
MM v[68:71],a[28:31],a[164:167],v[68:71]
RD v[144:147],v151 offset:0
RD v[148:151],v151 offset:16
MM v[76:79],a[28:31],a[172:175],v[76:79]
MM v[76:79],a[24:27],a[168:171],v[76:79]
MM v[76:79],a[20:23],a[140:143],v[76:79]
MM v[76:79],a[16:19],a[136:139],v[76:79]
s_waitcnt lgkmcnt(0)
MM v[96:99],a[32:35],a[128:131],v[96:99]
MM v[96:99],a[36:39],a[132:135],v[96:99]
MM v[96:99],a[40:43],a[160:163],v[96:99]
MM v[96:99],a[44:47],a[164:167],v[96:99]
buffer_load_dwordx4 v230, s[8:11], s77 offen offset:2048 lds
MM v[104:107],a[44:47],a[172:175],v[104:107]
v_pk_fma_f32 v[64:65], v[240:241], v[144:145], v[64:65] op_sel:[0,0,0] op_sel_hi:[0,1,1]
v_pk_fma_f32 v[66:67], v[240:241], v[146:147], v[66:67] op_sel:[0,0,0] op_sel_hi:[0,1,1]
v_pk_fma_f32 v[68:69], v[240:241], v[148:149], v[68:69] op_sel:[0,0,0] op_sel_hi:[0,1,1]
v_pk_fma_f32 v[70:71], v[240:241], v[150:151], v[70:71] op_sel:[0,0,0] op_sel_hi:[0,1,1]
v_pk_mul_f32 v[64:65], v[246:247], v[64:65] op_sel:[0,0] op_sel_hi:[0,1]
v_pk_mul_f32 v[66:67], v[246:247], v[66:67] op_sel:[0,0] op_sel_hi:[0,1]
v_pk_mul_f32 v[68:69], v[246:247], v[68:69] op_sel:[0,0] op_sel_hi:[0,1]
v_pk_mul_f32 v[70:71], v[246:247], v[70:71] op_sel:[0,0] op_sel_hi:[0,1]
MM v[104:107],a[40:43],a[168:171],v[104:107]
CV v192,v64,v65
CV v193,v66,v67
MM v[104:107],a[36:39],a[140:143],v[104:107]
CV v194,v68,v69
CV v195,v70,v71
MM v[104:107],a[32:35],a[136:139],v[104:107]
v_pk_fma_f32 v[72:73], v[240:241], v[144:145], v[72:73] op_sel:[1,0,0] op_sel_hi:[1,1,1]
v_pk_fma_f32 v[74:75], v[240:241], v[146:147], v[74:75] op_sel:[1,0,0] op_sel_hi:[1,1,1]
v_pk_fma_f32 v[76:77], v[240:241], v[148:149], v[76:77] op_sel:[1,0,0] op_sel_hi:[1,1,1]
v_pk_fma_f32 v[78:79], v[240:241], v[150:151], v[78:79] op_sel:[1,0,0] op_sel_hi:[1,1,1]
v_pk_mul_f32 v[72:73], v[246:247], v[72:73] op_sel:[1,0] op_sel_hi:[1,1]
v_pk_mul_f32 v[74:75], v[246:247], v[74:75] op_sel:[1,0] op_sel_hi:[1,1]
v_pk_mul_f32 v[76:77], v[246:247], v[76:77] op_sel:[1,0] op_sel_hi:[1,1]
v_pk_mul_f32 v[78:79], v[246:247], v[78:79] op_sel:[1,0] op_sel_hi:[1,1]
MM v[100:103],a[48:51],a[128:131],v[100:103]
CV v196,v72,v73
CV v197,v74,v75
MM v[100:103],a[52:55],a[132:135],v[100:103]
CV v198,v76,v77
CV v199,v78,v79
MM v[100:103],a[56:59],a[160:163],v[100:103]
v_xor_b32 v159, 384, v253
MM v[100:103],a[60:63],a[164:167],v[100:103]
RD v[152:155],v159 offset:0
RD v[156:159],v159 offset:16
MM v[108:111],a[60:63],a[172:175],v[108:111]
MM v[108:111],a[56:59],a[168:171],v[108:111]
buffer_load_dwordx4 v231, s[8:11], s77 offen offset:3072 lds
MM v[108:111],a[52:55],a[140:143],v[108:111]
s_add_u32 s77, s64, 16384
s_add_u32 s81, s77, 896
s_add_u32 m0, s65, 81920
MM v[108:111],a[48:51],a[136:139],v[108:111]
s_waitcnt lgkmcnt(0)
MM v[16:19],a[64:67],a[144:147],v[16:19]
MM v[16:19],a[68:71],a[148:151],v[16:19]
MM v[16:19],a[72:75],a[176:179],v[16:19]
MM v[16:19],a[76:79],a[180:183],v[16:19]
v_pk_fma_f32 v[96:97], v[240:241], v[152:153], v[96:97] op_sel:[0,0,0] op_sel_hi:[0,1,1]
v_pk_fma_f32 v[98:99], v[240:241], v[154:155], v[98:99] op_sel:[0,0,0] op_sel_hi:[0,1,1]
v_pk_fma_f32 v[100:101], v[240:241], v[156:157], v[100:101] op_sel:[0,0,0] op_sel_hi:[0,1,1]
v_pk_fma_f32 v[102:103], v[240:241], v[158:159], v[102:103] op_sel:[0,0,0] op_sel_hi:[0,1,1]
v_pk_mul_f32 v[96:97], v[246:247], v[96:97] op_sel:[0,0] op_sel_hi:[0,1]
v_pk_mul_f32 v[98:99], v[246:247], v[98:99] op_sel:[0,0] op_sel_hi:[0,1]
v_pk_mul_f32 v[100:101], v[246:247], v[100:101] op_sel:[0,0] op_sel_hi:[0,1]
v_pk_mul_f32 v[102:103], v[246:247], v[102:103] op_sel:[0,0] op_sel_hi:[0,1]
s_nop 0
LPG4_%=:
s_cbranch_execz LPG8_%=
MM v[24:27],a[76:79],a[188:191],v[24:27]
CV v208,v96,v97
CV v209,v98,v99
MM v[24:27],a[72:75],a[184:187],v[24:27]
CV v210,v100,v101
CV v211,v102,v103
MM v[24:27],a[68:71],a[156:159],v[24:27]
s_waitcnt vmcnt(12)
s_barrier
RD a[192:195],v224 offset:0
MM v[24:27],a[64:67],a[152:155],v[24:27]
buffer_load_dwordx4 v235, s[12:15], s81 offen offset:0 lds
MM v[20:23],a[80:83],a[144:147],v[20:23]
RD a[196:199],v225 offset:0
RD a[200:203],v226 offset:0
MM v[20:23],a[84:87],a[148:151],v[20:23]
RD a[204:207],v227 offset:0
RD a[208:211],v224 offset:4096
MM v[20:23],a[88:91],a[176:179],v[20:23]
RD a[212:215],v225 offset:4096
RD a[216:219],v226 offset:4096
MM v[20:23],a[92:95],a[180:183],v[20:23]
RD a[220:223],v227 offset:4096
RD a[224:227],v224 offset:8192
MM v[28:31],a[92:95],a[188:191],v[28:31]
RD a[228:231],v225 offset:8192
RD a[232:235],v226 offset:8192
MM v[28:31],a[88:91],a[184:187],v[28:31]
RD a[236:239],v227 offset:8192
RD a[240:243],v224 offset:12288
MM v[28:31],a[84:87],a[156:159],v[28:31]
RD a[244:247],v225 offset:12288
RD a[248:251],v226 offset:12288
MM v[28:31],a[80:83],a[152:155],v[28:31]
RD a[252:255],v227 offset:12288
MM v[48:51],a[96:99],a[144:147],v[48:51]
v_pk_fma_f32 v[104:105], v[240:241], v[152:153], v[104:105] op_sel:[1,0,0] op_sel_hi:[1,1,1]
v_pk_fma_f32 v[106:107], v[240:241], v[154:155], v[106:107] op_sel:[1,0,0] op_sel_hi:[1,1,1]
v_pk_fma_f32 v[108:109], v[240:241], v[156:157], v[108:109] op_sel:[1,0,0] op_sel_hi:[1,1,1]
v_pk_fma_f32 v[110:111], v[240:241], v[158:159], v[110:111] op_sel:[1,0,0] op_sel_hi:[1,1,1]
v_pk_mul_f32 v[104:105], v[246:247], v[104:105] op_sel:[1,0] op_sel_hi:[1,1]
v_pk_mul_f32 v[106:107], v[246:247], v[106:107] op_sel:[1,0] op_sel_hi:[1,1]
v_pk_mul_f32 v[108:109], v[246:247], v[108:109] op_sel:[1,0] op_sel_hi:[1,1]
v_pk_mul_f32 v[110:111], v[246:247], v[110:111] op_sel:[1,0] op_sel_hi:[1,1]
MM v[48:51],a[100:103],a[148:151],v[48:51]
buffer_load_dwordx4 v235, s[12:15], s77 offen offset:1024 lds
MM v[48:51],a[104:107],a[176:179],v[48:51]
CV v212,v104,v105
CV v213,v106,v107
MM v[48:51],a[108:111],a[180:183],v[48:51]
CV v214,v108,v109
CV v215,v110,v111
MM v[56:59],a[108:111],a[188:191],v[56:59]
v_mov_b32 v247, 0
v_mov_b32 v236, 0
MM v[56:59],a[104:107],a[184:187],v[56:59]
v_mov_b32 v237, 0
v_cndmask_b32 v144, 0, v238, s[56:57]
MM v[56:59],a[100:103],a[156:159],v[56:59]
v_cndmask_b32 v145, 0, v238, s[56:57]
v_cndmask_b32 v146, 0, v238, s[56:57]
MM v[56:59],a[96:99],a[152:155],v[56:59]
v_cndmask_b32 v147, 0, v238, s[56:57]
v_cndmask_b32 v148, 0, v238, s[58:59]
MM v[52:55],a[112:115],a[144:147],v[52:55]
v_cndmask_b32 v149, 0, v238, s[58:59]
v_cndmask_b32 v150, 0, v238, s[58:59]
MM v[52:55],a[116:119],a[148:151],v[52:55]
v_pk_fma_f32 v[16:17], v[242:243], v[136:137], v[16:17] op_sel:[0,0,0] op_sel_hi:[0,1,1]
v_pk_fma_f32 v[18:19], v[242:243], v[138:139], v[18:19] op_sel:[0,0,0] op_sel_hi:[0,1,1]
v_pk_fma_f32 v[20:21], v[242:243], v[140:141], v[20:21] op_sel:[0,0,0] op_sel_hi:[0,1,1]
v_pk_fma_f32 v[22:23], v[242:243], v[142:143], v[22:23] op_sel:[0,0,0] op_sel_hi:[0,1,1]
v_pk_mul_f32 v[16:17], v[244:245], v[16:17] op_sel:[0,0] op_sel_hi:[0,1]
v_pk_mul_f32 v[18:19], v[244:245], v[18:19] op_sel:[0,0] op_sel_hi:[0,1]
v_pk_mul_f32 v[20:21], v[244:245], v[20:21] op_sel:[0,0] op_sel_hi:[0,1]
v_pk_mul_f32 v[22:23], v[244:245], v[22:23] op_sel:[0,0] op_sel_hi:[0,1]
MM v[52:55],a[120:123],a[176:179],v[52:55]
v_cndmask_b32 v151, 0, v238, s[58:59]
CV v168,v16,v17
)ASM"
R"ASM(
MM v[52:55],a[124:127],a[180:183],v[52:55]
buffer_load_dwordx4 v235, s[12:15], s81 offen offset:2048 lds
s_waitcnt lgkmcnt(0)
MM v[0:3],a[192:195],v[160:163],0
CV v169,v18,v19
CV v170,v20,v21
MM v[0:3],a[196:199],v[176:179],v[0:3]
CV v171,v22,v23
MM v[0:3],a[200:203],v[192:195],v[0:3]
v_pk_fma_f32 v[24:25], v[242:243], v[136:137], v[24:25] op_sel:[1,0,0] op_sel_hi:[1,1,1]
v_pk_fma_f32 v[26:27], v[242:243], v[138:139], v[26:27] op_sel:[1,0,0] op_sel_hi:[1,1,1]
v_pk_fma_f32 v[28:29], v[242:243], v[140:141], v[28:29] op_sel:[1,0,0] op_sel_hi:[1,1,1]
v_pk_fma_f32 v[30:31], v[242:243], v[142:143], v[30:31] op_sel:[1,0,0] op_sel_hi:[1,1,1]
v_pk_mul_f32 v[24:25], v[244:245], v[24:25] op_sel:[1,0] op_sel_hi:[1,1]
v_pk_mul_f32 v[26:27], v[244:245], v[26:27] op_sel:[1,0] op_sel_hi:[1,1]
v_pk_mul_f32 v[28:29], v[244:245], v[28:29] op_sel:[1,0] op_sel_hi:[1,1]
v_pk_mul_f32 v[30:31], v[244:245], v[30:31] op_sel:[1,0] op_sel_hi:[1,1]
MM v[0:3],a[204:207],v[208:211],v[0:3]
CV v172,v24,v25
CV v173,v26,v27
MM v[8:11],a[192:195],v[164:167],0
CV v174,v28,v29
CV v175,v30,v31
MM v[8:11],a[196:199],v[180:183],v[8:11]
v_xor_b32 v143, 128, v253
MM v[8:11],a[200:203],v[196:199],v[8:11]
RD v[136:139],v143 offset:0
RD v[140:143],v143 offset:16
MM v[8:11],a[204:207],v[212:215],v[8:11]
MM v[4:7],a[208:211],v[160:163],0
v_max3_f32 v152, v0, v1, v2
v_max_f32 v152, v152, v3
MM v[4:7],a[212:215],v[176:179],v[4:7]
buffer_load_dwordx4 v235, s[12:15], s77 offen offset:3072 lds
MM v[4:7],a[216:219],v[192:195],v[4:7]
s_add_u32 s77, s64, 32768
s_add_u32 s81, s77, 896
s_add_u32 m0, s65, 98304
v_mov_b32 v153, v152
MM v[4:7],a[220:223],v[208:211],v[4:7]
s_waitcnt lgkmcnt(0)
v_permlane16_swap_b32 v152, v153
v_max_f32 v152, v152, v153
MM v[12:15],a[208:211],v[164:167],0
s_waitcnt vmcnt(8)
s_barrier
RD a[192:195],v224 offset:16384
v_mov_b32 v153, v152
MM v[12:15],a[212:215],v[180:183],v[12:15]
RD a[196:199],v225 offset:16384
RD a[200:203],v226 offset:16384
v_permlane32_swap_b32 v152, v153
v_max_f32 v152, v152, v153
v_sub_f32 v128, 0, v152
MM v[12:15],a[216:219],v[196:199],v[12:15]
RD a[204:207],v227 offset:16384
v_sub_f32 v129, 0, v152
v_sub_f32 v130, 0, v152
MM v[12:15],a[220:223],v[212:215],v[12:15]
v_sub_f32 v131, 0, v152
v_max3_f32 v154, v8, v9, v10
MM v[32:35],a[224:227],v[160:163],0
RD a[208:211],v224 offset:20480
RD a[212:215],v225 offset:20480
v_max_f32 v154, v154, v11
v_mov_b32 v155, v154
MM v[32:35],a[228:231],v[176:179],v[32:35]
RD a[216:219],v226 offset:20480
RD a[220:223],v227 offset:20480
v_permlane16_swap_b32 v154, v155
v_max_f32 v154, v154, v155
MM v[32:35],a[232:235],v[192:195],v[32:35]
v_mov_b32 v155, v154
v_sub_f32 v0, v0, v152
MM v[32:35],a[236:239],v[208:211],v[32:35]
buffer_load_dwordx4 v235, s[12:15], s81 offen offset:0 lds
v_permlane32_swap_b32 v154, v155
v_max_f32 v154, v154, v155
v_sub_f32 v132, 0, v154
MM v[40:43],a[224:227],v[164:167],0
v_sub_f32 v133, 0, v154
v_sub_f32 v134, 0, v154
v_sub_f32 v135, 0, v154
v_sub_f32 v1, v1, v152
EX v0,v0
MM v[40:43],a[228:231],v[180:183],v[40:43]
EX v1,v1
MM v[40:43],a[232:235],v[196:199],v[40:43]
v_sub_f32 v2, v2, v152
v_sub_f32 v3, v3, v152
MM v[40:43],a[236:239],v[212:215],v[40:43]
EX v2,v2
MM v[36:39],a[240:243],v[160:163],v[128:131]
RD a[224:227],v224 offset:24576
RD a[228:231],v225 offset:24576
EX v3,v3
MM v[36:39],a[244:247],v[176:179],v[36:39]
RD a[232:235],v226 offset:24576
RD a[236:239],v227 offset:24576
CV v0,v0,v1
v_sub_f32 v4, v4, v152
MM v[36:39],a[248:251],v[192:195],v[36:39]
v_sub_f32 v5, v5, v152
EX v4,v4
MM v[36:39],a[252:255],v[208:211],v[36:39]
EX v5,v5
MM v[44:47],a[240:243],v[164:167],v[132:135]
CV v1,v2,v3
v_sub_f32 v6, v6, v152
MM v[44:47],a[244:247],v[180:183],v[44:47]
buffer_load_dwordx4 v235, s[12:15], s77 offen offset:1024 lds
v_sub_f32 v7, v7, v152
MM v[44:47],a[248:251],v[196:199],v[44:47]
EX v6,v6
EX v7,v7
MM v[44:47],a[252:255],v[212:215],v[44:47]
CV v2,v4,v5
v_sub_f32 v8, v8, v154
MM v[60:63],a[124:127],a[188:191],v[60:63]
RD a[240:243],v224 offset:28672
RD a[244:247],v225 offset:28672
v_sub_f32 v9, v9, v154
MM v[60:63],a[120:123],a[184:187],v[60:63]
RD a[248:251],v226 offset:28672
RD a[252:255],v227 offset:28672
EX v8,v8
MM v[60:63],a[116:119],a[156:159],v[60:63]
EX v9,v9
MM v[60:63],a[112:115],a[152:155],v[60:63]
CV v3,v6,v7
v_sub_f32 v10, v10, v154
MM v[80:83],a[0:3],a[144:147],v[80:83]
v_sub_f32 v11, v11, v154
EX v10,v10
MM v[80:83],a[4:7],a[148:151],v[80:83]
EX v11,v11
MM v[80:83],a[8:11],a[176:179],v[80:83]
CV v8,v8,v9
v_sub_f32 v12, v12, v154
MM v[80:83],a[12:15],a[180:183],v[80:83]
buffer_load_dwordx4 v235, s[12:15], s81 offen offset:2048 lds
v_sub_f32 v13, v13, v154
EX v12,v12
MM v[88:91],a[12:15],a[188:191],v[88:91]
EX v13,v13
MM v[88:91],a[8:11],a[184:187],v[88:91]
v_pk_fma_f32 v[48:49], v[242:243], v[136:137], v[48:49] op_sel:[0,0,0] op_sel_hi:[0,1,1]
v_pk_fma_f32 v[50:51], v[242:243], v[138:139], v[50:51] op_sel:[0,0,0] op_sel_hi:[0,1,1]
v_pk_fma_f32 v[52:53], v[242:243], v[140:141], v[52:53] op_sel:[0,0,0] op_sel_hi:[0,1,1]
v_pk_fma_f32 v[54:55], v[242:243], v[142:143], v[54:55] op_sel:[0,0,0] op_sel_hi:[0,1,1]
v_pk_mul_f32 v[48:49], v[244:245], v[48:49] op_sel:[0,0] op_sel_hi:[0,1]
v_pk_mul_f32 v[50:51], v[244:245], v[50:51] op_sel:[0,0] op_sel_hi:[0,1]
v_pk_mul_f32 v[52:53], v[244:245], v[52:53] op_sel:[0,0] op_sel_hi:[0,1]
v_pk_mul_f32 v[54:55], v[244:245], v[54:55] op_sel:[0,0] op_sel_hi:[0,1]
CV v9,v10,v11
MM v[88:91],a[4:7],a[156:159],v[88:91]
v_sub_f32 v14, v14, v154
v_sub_f32 v15, v15, v154
EX v14,v14
MM v[88:91],a[0:3],a[152:155],v[88:91]
EX v15,v15
MM v[84:87],a[16:19],a[144:147],v[84:87]
CV v10,v12,v13
v_sub_f32 v32, v32, v152
MM v[84:87],a[20:23],a[148:151],v[84:87]
v_sub_f32 v33, v33, v152
CV v184,v48,v49
MM v[84:87],a[24:27],a[176:179],v[84:87]
EX v32,v32
MM v[84:87],a[28:31],a[180:183],v[84:87]
EX v33,v33
MM v[92:95],a[28:31],a[188:191],v[92:95]
CV v11,v14,v15
v_sub_f32 v34, v34, v152
v_sub_f32 v35, v35, v152
MM v[92:95],a[24:27],a[184:187],v[92:95]
buffer_load_dwordx4 v235, s[12:15], s77 offen offset:3072 lds
EX v34,v34
MM v[92:95],a[20:23],a[156:159],v[92:95]
s_add_u32 s77, s64, 49152
s_add_u32 m0, s65, 49152
CV v185,v50,v51
EX v35,v35
MM v[92:95],a[16:19],a[152:155],v[92:95]
buffer_load_dwordx4 v228, s[8:11], s77 offen offset:0 lds
CV v4,v32,v33
EX v36,v36
MM v[112:115],a[32:35],a[144:147],v[112:115]
EX v37,v37
MM v[112:115],a[36:39],a[148:151],v[112:115]
CV v5,v34,v35
CV v186,v52,v53
MM v[112:115],a[40:43],a[176:179],v[112:115]
EX v38,v38
EX v39,v39
MM v[112:115],a[44:47],a[180:183],v[112:115]
buffer_load_dwordx4 v229, s[8:11], s77 offen offset:1024 lds
CV v6,v36,v37
MM v[120:123],a[44:47],a[188:191],v[120:123]
v_sub_f32 v40, v40, v154
v_sub_f32 v41, v41, v154
MM v[120:123],a[40:43],a[184:187],v[120:123]
EX v40,v40
MM v[120:123],a[36:39],a[156:159],v[120:123]
EX v41,v41
MM v[120:123],a[32:35],a[152:155],v[120:123]
buffer_load_dwordx4 v230, s[8:11], s77 offen offset:2048 lds
CV v7,v38,v39
v_sub_f32 v42, v42, v154
MM v[116:119],a[48:51],a[144:147],v[116:119]
v_sub_f32 v43, v43, v154
CV v187,v54,v55
EX v42,v42
MM v[116:119],a[52:55],a[148:151],v[116:119]
EX v43,v43
MM v[116:119],a[56:59],a[176:179],v[116:119]
v_pk_fma_f32 v[56:57], v[242:243], v[136:137], v[56:57] op_sel:[1,0,0] op_sel_hi:[1,1,1]
v_pk_fma_f32 v[58:59], v[242:243], v[138:139], v[58:59] op_sel:[1,0,0] op_sel_hi:[1,1,1]
v_pk_fma_f32 v[60:61], v[242:243], v[140:141], v[60:61] op_sel:[1,0,0] op_sel_hi:[1,1,1]
v_pk_fma_f32 v[62:63], v[242:243], v[142:143], v[62:63] op_sel:[1,0,0] op_sel_hi:[1,1,1]
v_pk_mul_f32 v[56:57], v[244:245], v[56:57] op_sel:[1,0] op_sel_hi:[1,1]
v_pk_mul_f32 v[58:59], v[244:245], v[58:59] op_sel:[1,0] op_sel_hi:[1,1]
v_pk_mul_f32 v[60:61], v[244:245], v[60:61] op_sel:[1,0] op_sel_hi:[1,1]
v_pk_mul_f32 v[62:63], v[244:245], v[62:63] op_sel:[1,0] op_sel_hi:[1,1]
CV v12,v40,v41
EX v44,v44
MM v[116:119],a[60:63],a[180:183],v[116:119]
buffer_load_dwordx4 v231, s[8:11], s77 offen offset:3072 lds
EX v45,v45
MM v[124:127],a[60:63],a[188:191],v[124:127]
s_add_u32 s77, s64, 49152
s_add_u32 s81, s77, 896
s_add_u32 m0, s65, 114688
CV v13,v42,v43
MM v[124:127],a[56:59],a[184:187],v[124:127]
EX v46,v46
MM v[124:127],a[52:55],a[156:159],v[124:127]
EX v47,v47
CV v14,v44,v45
CV v15,v46,v47
MM v[124:127],a[48:51],a[152:155],v[124:127]
buffer_load_dwordx4 v235, s[12:15], s81 offen offset:0 lds
v_smfmac_f32_16x16x64_bf16 v[236:239], v[144:147], v[0:7], v234
CV v188,v56,v57
v_smfmac_f32_16x16x64_bf16 v[236:239], v[148:151], v[8:15], v234
s_waitcnt lgkmcnt(0)
CV v189,v58,v59
buffer_load_dwordx4 v235, s[12:15], s77 offen offset:1024 lds
buffer_load_dwordx4 v235, s[12:15], s81 offen offset:2048 lds
buffer_load_dwordx4 v235, s[12:15], s77 offen offset:3072 lds
s_add_u32 s64, s64, 49152
MM v[64:67],a[192:195],v[160:163],v[128:131]
s_add_u32 s64, s64, 16384
TR a[128:129],v232 offset:0
CV v190,v60,v61
MM v[64:67],a[196:199],v[176:179],v[64:67]
s_add_u32 s81, s64, 896
TR a[130:131],v232 offset:4096
CV v191,v62,v63
MM v[64:67],a[200:203],v[192:195],v[64:67]
TR a[132:133],v232 offset:8192
TR a[134:135],v232 offset:12288
MM v[64:67],a[204:207],v[208:211],v[64:67]
v_add_f32 v246, v236, v237
v_xor_b32 v143, 256, v253
MM v[68:71],a[220:223],v[208:211],v[128:131]
TR a[136:137],v233 offset:0
v_cmp_nge_f32 vcc, s71, v246
MM v[68:71],a[216:219],v[192:195],v[68:71]
s_cbranch_vccnz LSITE0_%=
LBACK0_%=:
TR a[138:139],v233 offset:4096
RD v[136:139],v143 offset:0
MM v[68:71],a[212:215],v[176:179],v[68:71]
TR a[140:141],v233 offset:8192
RD v[140:143],v143 offset:16
MM v[68:71],a[208:211],v[160:163],v[68:71]
TR a[142:143],v233 offset:12288
TR a[144:145],v232 offset:512
MM v[76:79],a[208:211],v[164:167],v[132:135]
TR a[146:147],v232 offset:4608
TR a[148:149],v232 offset:8704
MM v[76:79],a[212:215],v[180:183],v[76:79]
TR a[150:151],v232 offset:12800
TR a[152:153],v233 offset:512
MM v[76:79],a[216:219],v[196:199],v[76:79]
s_waitcnt lgkmcnt(6)
v_pk_fma_f32 v[80:81], v[242:243], v[136:137], v[80:81] op_sel:[0,0,0] op_sel_hi:[0,1,1]
v_pk_fma_f32 v[82:83], v[242:243], v[138:139], v[82:83] op_sel:[0,0,0] op_sel_hi:[0,1,1]
v_pk_fma_f32 v[84:85], v[242:243], v[140:141], v[84:85] op_sel:[0,0,0] op_sel_hi:[0,1,1]
v_pk_fma_f32 v[86:87], v[242:243], v[142:143], v[86:87] op_sel:[0,0,0] op_sel_hi:[0,1,1]
v_pk_mul_f32 v[80:81], v[244:245], v[80:81] op_sel:[0,0] op_sel_hi:[0,1]
v_pk_mul_f32 v[82:83], v[244:245], v[82:83] op_sel:[0,0] op_sel_hi:[0,1]
v_pk_mul_f32 v[84:85], v[244:245], v[84:85] op_sel:[0,0] op_sel_hi:[0,1]
v_pk_mul_f32 v[86:87], v[244:245], v[86:87] op_sel:[0,0] op_sel_hi:[0,1]
MM v[76:79],a[220:223],v[212:215],v[76:79]
EX v64,v64
MM v[72:75],a[204:207],v[212:215],v[132:135]
EX v65,v65
MM v[72:75],a[200:203],v[196:199],v[72:75]
TR a[154:155],v233 offset:4608
TR a[156:157],v233 offset:8704
MM v[72:75],a[196:199],v[180:183],v[72:75]
TR a[158:159],v233 offset:12800
TR a[160:161],v232 offset:1024
MM v[72:75],a[192:195],v[164:167],v[72:75]
EX v66,v66
MM v[96:99],a[224:227],v[160:163],v[128:131]
EX v67,v67
MM v[96:99],a[228:231],v[176:179],v[96:99]
TR a[162:163],v232 offset:5120
TR a[164:165],v232 offset:9216
MM v[96:99],a[232:235],v[192:195],v[96:99]
TR a[166:167],v232 offset:13312
CV v200,v80,v81
EX v68,v68
MM v[96:99],a[236:239],v[208:211],v[96:99]
TR a[168:169],v233 offset:1024
CV v201,v82,v83
MM v[100:103],a[252:255],v[208:211],v[128:131]
TR a[170:171],v233 offset:5120
EX v69,v69
MM v[100:103],a[248:251],v[192:195],v[100:103]
TR a[172:173],v233 offset:9216
TR a[174:175],v233 offset:13312
CV v202,v84,v85
MM v[100:103],a[244:247],v[176:179],v[100:103]
TR a[176:177],v232 offset:1536
EX v70,v70
CV v203,v86,v87
MM v[100:103],a[240:243],v[160:163],v[100:103]
TR a[178:179],v232 offset:5632
v_pk_fma_f32 v[88:89], v[242:243], v[136:137], v[88:89] op_sel:[1,0,0] op_sel_hi:[1,1,1]
v_pk_fma_f32 v[90:91], v[242:243], v[138:139], v[90:91] op_sel:[1,0,0] op_sel_hi:[1,1,1]
v_pk_fma_f32 v[92:93], v[242:243], v[140:141], v[92:93] op_sel:[1,0,0] op_sel_hi:[1,1,1]
v_pk_fma_f32 v[94:95], v[242:243], v[142:143], v[94:95] op_sel:[1,0,0] op_sel_hi:[1,1,1]
v_pk_mul_f32 v[88:89], v[244:245], v[88:89] op_sel:[1,0] op_sel_hi:[1,1]
)ASM"
R"ASM(
v_pk_mul_f32 v[90:91], v[244:245], v[90:91] op_sel:[1,0] op_sel_hi:[1,1]
v_pk_mul_f32 v[92:93], v[244:245], v[92:93] op_sel:[1,0] op_sel_hi:[1,1]
v_pk_mul_f32 v[94:95], v[244:245], v[94:95] op_sel:[1,0] op_sel_hi:[1,1]
EX v71,v71
MM v[108:111],a[240:243],v[164:167],v[132:135]
TR a[180:181],v232 offset:9728
CV v64,v64,v65
MM v[108:111],a[244:247],v[180:183],v[108:111]
TR a[182:183],v232 offset:13824
TR a[184:185],v233 offset:1536
EX v72,v72
MM v[108:111],a[248:251],v[196:199],v[108:111]
TR a[186:187],v233 offset:5632
TR a[188:189],v233 offset:9728
TR a[190:191],v233 offset:13824
EX v73,v73
MM v[108:111],a[252:255],v[212:215],v[108:111]
CV v65,v66,v67
CV v66,v68,v69
MM v[104:107],a[236:239],v[212:215],v[132:135]
EX v74,v74
MM v[104:107],a[232:235],v[196:199],v[104:107]
EX v75,v75
MM v[104:107],a[228:231],v[180:183],v[104:107]
CV v67,v70,v71
CV v204,v88,v89
MM v[104:107],a[224:227],v[164:167],v[104:107]
s_waitcnt vmcnt(12) lgkmcnt(0)
s_barrier
CV v205,v90,v91
EX v76,v76
MM a[0:3],a[128:131],v[0:3],0
s_add_u32 m0, s65, 0
EX v77,v77
MM a[0:3],a[132:135],v[4:7],a[0:3]
CV v206,v92,v93
CV v72,v72,v73
MM a[8:11],a[132:135],v[12:15],0
CV v207,v94,v95
v_xor_b32 v143, 384, v253
MM a[8:11],a[128:131],v[8:11],a[8:11]
EX v78,v78
MM a[12:15],a[136:139],v[8:11],0
EX v79,v79
MM a[12:15],a[140:143],v[12:15],a[12:15]
RD v[136:139],v143 offset:0
RD v[140:143],v143 offset:16
MM a[4:7],a[140:143],v[4:7],0
EX v96,v96
MM a[4:7],a[136:139],v[0:3],a[4:7]
RD a[192:195],v224 offset:32768
CV v73,v74,v75
MM a[32:35],a[144:147],v[0:3],0
EX v97,v97
MM a[32:35],a[148:151],v[4:7],a[32:35]
s_waitcnt lgkmcnt(1)
buffer_load_dwordx4 v228, s[8:11], s64 offen offset:0 lds
v_pk_fma_f32 v[112:113], v[242:243], v[136:137], v[112:113] op_sel:[0,0,0] op_sel_hi:[0,1,1]
v_pk_fma_f32 v[114:115], v[242:243], v[138:139], v[114:115] op_sel:[0,0,0] op_sel_hi:[0,1,1]
v_pk_fma_f32 v[116:117], v[242:243], v[140:141], v[116:117] op_sel:[0,0,0] op_sel_hi:[0,1,1]
v_pk_fma_f32 v[118:119], v[242:243], v[142:143], v[118:119] op_sel:[0,0,0] op_sel_hi:[0,1,1]
v_pk_mul_f32 v[112:113], v[244:245], v[112:113] op_sel:[0,0] op_sel_hi:[0,1]
v_pk_mul_f32 v[114:115], v[244:245], v[114:115] op_sel:[0,0] op_sel_hi:[0,1]
v_pk_mul_f32 v[116:117], v[244:245], v[116:117] op_sel:[0,0] op_sel_hi:[0,1]
v_pk_mul_f32 v[118:119], v[244:245], v[118:119] op_sel:[0,0] op_sel_hi:[0,1]
EX v98,v98
MM a[40:43],a[148:151],v[12:15],0
RD a[196:199],v225 offset:32768
CV v74,v76,v77
MM a[40:43],a[144:147],v[8:11],a[40:43]
RD a[200:203],v226 offset:32768
EX v99,v99
MM a[44:47],a[152:155],v[8:11],0
buffer_load_dwordx4 v229, s[8:11], s64 offen offset:1024 lds
MM a[44:47],a[156:159],v[12:15],a[44:47]
RD a[204:207],v227 offset:32768
EX v100,v100
MM a[36:39],a[156:159],v[4:7],0
EX v101,v101
CV v75,v78,v79
MM a[36:39],a[152:155],v[0:3],a[36:39]
RD a[208:211],v224 offset:36864
CV v216,v112,v113
MM a[64:67],a[160:163],v[0:3],0
EX v102,v102
MM a[64:67],a[164:167],v[4:7],a[64:67]
buffer_load_dwordx4 v230, s[8:11], s64 offen offset:2048 lds
RD a[212:215],v225 offset:36864
CV v217,v114,v115
MM a[72:75],a[164:167],v[12:15],0
EX v103,v103
CV v68,v96,v97
CV v218,v116,v117
MM a[72:75],a[160:163],v[8:11],a[72:75]
RD a[216:219],v226 offset:36864
CV v219,v118,v119
EX v104,v104
MM a[76:79],a[168:171],v[8:11],0
buffer_load_dwordx4 v231, s[8:11], s64 offen offset:3072 lds
RD a[220:223],v227 offset:36864
MM a[76:79],a[172:175],v[12:15],a[76:79]
s_add_u32 m0, s65, 65536
v_pk_fma_f32 v[120:121], v[242:243], v[136:137], v[120:121] op_sel:[1,0,0] op_sel_hi:[1,1,1]
v_pk_fma_f32 v[122:123], v[242:243], v[138:139], v[122:123] op_sel:[1,0,0] op_sel_hi:[1,1,1]
v_pk_fma_f32 v[124:125], v[242:243], v[140:141], v[124:125] op_sel:[1,0,0] op_sel_hi:[1,1,1]
v_pk_fma_f32 v[126:127], v[242:243], v[142:143], v[126:127] op_sel:[1,0,0] op_sel_hi:[1,1,1]
v_pk_mul_f32 v[120:121], v[244:245], v[120:121] op_sel:[1,0] op_sel_hi:[1,1]
v_pk_mul_f32 v[122:123], v[244:245], v[122:123] op_sel:[1,0] op_sel_hi:[1,1]
v_pk_mul_f32 v[124:125], v[244:245], v[124:125] op_sel:[1,0] op_sel_hi:[1,1]
v_pk_mul_f32 v[126:127], v[244:245], v[126:127] op_sel:[1,0] op_sel_hi:[1,1]
EX v105,v105
CV v69,v98,v99
MM a[68:71],a[172:175],v[4:7],0
RD a[224:227],v224 offset:40960
CV v70,v100,v101
MM a[68:71],a[168:171],v[0:3],a[68:71]
EX v106,v106
MM a[96:99],a[176:179],v[0:3],0
RD a[228:231],v225 offset:40960
buffer_load_dwordx4 v235, s[12:15], s81 offen offset:0 lds
EX v107,v107
MM a[96:99],a[180:183],v[4:7],a[96:99]
RD a[232:235],v226 offset:40960
CV v71,v102,v103
MM a[104:107],a[180:183],v[12:15],0
EX v108,v108
MM a[104:107],a[176:179],v[8:11],a[104:107]
EX v109,v109
MM a[108:111],a[184:187],v[8:11],0
RD a[236:239],v227 offset:40960
RD a[240:243],v224 offset:45056
buffer_load_dwordx4 v235, s[12:15], s64 offen offset:1024 lds
MM a[108:111],a[188:191],v[12:15],a[108:111]
EX v110,v110
CV v220,v120,v121
MM a[100:103],a[188:191],v[4:7],0
RD a[244:247],v225 offset:45056
RD a[248:251],v226 offset:45056
RD a[252:255],v227 offset:45056
CV v221,v122,v123
EX v111,v111
CV v222,v124,v125
CV v223,v126,v127
MM a[100:103],a[184:187],v[0:3],a[100:103]
buffer_load_dwordx4 v235, s[12:15], s81 offen offset:2048 lds
CV v76,v104,v105
v_smfmac_f32_16x16x64_bf16 v[236:239], v[144:147], v[64:71], v234
CV v77,v106,v107
CV v78,v108,v109
CV v79,v110,v111
s_nop 1
v_smfmac_f32_16x16x64_bf16 v[236:239], v[148:151], v[72:79], v234
s_waitcnt lgkmcnt(0)
buffer_load_dwordx4 v235, s[12:15], s64 offen offset:3072 lds
MM v[0:3],a[192:195],v[160:163],v[128:131]
s_add_u32 s64, s64, 16384
TR a[128:129],v232 offset:16384
TR a[130:131],v232 offset:20480
MM v[0:3],a[196:199],v[176:179],v[0:3]
s_add_u32 s81, s64, 896
TR a[132:133],v232 offset:24576
s_nop 0
LPG5_%=:
s_cbranch_execz LPG9_%=
TR a[134:135],v232 offset:28672
MM v[0:3],a[200:203],v[192:195],v[0:3]
TR a[136:137],v233 offset:16384
TR a[138:139],v233 offset:20480
MM v[0:3],a[204:207],v[208:211],v[0:3]
TR a[140:141],v233 offset:24576
v_add_f32 v247, v236, v237
MM v[4:7],a[220:223],v[208:211],v[128:131]
TR a[142:143],v233 offset:28672
v_cmp_nge_f32 vcc, s71, v247
MM v[4:7],a[216:219],v[192:195],v[4:7]
s_cbranch_vccnz LSITE1_%=
LBACK1_%=:
TR a[144:145],v232 offset:16896
TR a[146:147],v232 offset:20992
MM v[4:7],a[212:215],v[176:179],v[4:7]
s_lshl_b32 s90, %[swave], 10
s_add_u32 s90, s90, 81920
TR a[148:149],v232 offset:25088
MM v[4:7],a[208:211],v[160:163],v[4:7]
buffer_load_dwordx4 v[48:51], v248, s[20:23], s90 offen offset:0 sc1
MM v[12:15],a[208:211],v[164:167],v[132:135]
EX v0,v0
MM v[12:15],a[212:215],v[180:183],v[12:15]
EX v1,v1
MM v[12:15],a[216:219],v[196:199],v[12:15]
TR a[150:151],v232 offset:29184
CV v0,v0,v1
MM v[12:15],a[220:223],v[212:215],v[12:15]
TR a[152:153],v233 offset:16896
TR a[154:155],v233 offset:20992
MM v[8:11],a[204:207],v[212:215],v[132:135]
EX v2,v2
MM v[8:11],a[200:203],v[196:199],v[8:11]
EX v3,v3
MM v[8:11],a[196:199],v[180:183],v[8:11]
TR a[156:157],v233 offset:25088
CV v1,v2,v3
MM v[8:11],a[192:195],v[164:167],v[8:11]
TR a[158:159],v233 offset:29184
TR a[160:161],v232 offset:17408
MM v[32:35],a[224:227],v[160:163],v[128:131]
EX v4,v4
MM v[32:35],a[228:231],v[176:179],v[32:35]
TR a[162:163],v232 offset:21504
TR a[164:165],v232 offset:25600
MM v[32:35],a[232:235],v[192:195],v[32:35]
EX v5,v5
MM v[32:35],a[236:239],v[208:211],v[32:35]
TR a[166:167],v232 offset:29696
CV v2,v4,v5
MM v[36:39],a[252:255],v[208:211],v[128:131]
EX v6,v6
MM v[36:39],a[248:251],v[192:195],v[36:39]
TR a[168:169],v233 offset:17408
TR a[170:171],v233 offset:21504
MM v[36:39],a[244:247],v[176:179],v[36:39]
EX v7,v7
MM v[36:39],a[240:243],v[160:163],v[36:39]
TR a[172:173],v233 offset:25600
CV v3,v6,v7
MM v[44:47],a[240:243],v[164:167],v[132:135]
EX v8,v8
MM v[44:47],a[244:247],v[180:183],v[44:47]
TR a[174:175],v233 offset:29696
TR a[176:177],v232 offset:17920
MM v[44:47],a[248:251],v[196:199],v[44:47]
EX v9,v9
MM v[44:47],a[252:255],v[212:215],v[44:47]
TR a[178:179],v232 offset:22016
TR a[180:181],v232 offset:26112
MM v[40:43],a[236:239],v[212:215],v[132:135]
EX v10,v10
MM v[40:43],a[232:235],v[196:199],v[40:43]
TR a[182:183],v232 offset:30208
CV v8,v8,v9
MM v[40:43],a[228:231],v[180:183],v[40:43]
EX v11,v11
MM v[40:43],a[224:227],v[164:167],v[40:43]
TR a[184:185],v233 offset:17920
TR a[186:187],v233 offset:22016
MM v[16:19],a[192:195],v[168:171],0
EX v12,v12
MM v[16:19],a[196:199],v[184:187],v[16:19]
TR a[188:189],v233 offset:26112
TR a[190:191],v233 offset:30208
MM v[16:19],a[200:203],v[200:203],v[16:19]
EX v13,v13
MM v[16:19],a[204:207],v[216:219],v[16:19]
EX v14,v14
MM v[24:27],a[192:195],v[172:175],0
EX v15,v15
MM v[24:27],a[196:199],v[188:191],v[24:27]
EX v32,v32
MM v[24:27],a[200:203],v[204:207],v[24:27]
EX v33,v33
MM v[24:27],a[204:207],v[220:223],v[24:27]
s_waitcnt vmcnt(13) lgkmcnt(0)
s_barrier
CV v9,v10,v11
MM a[0:3],a[128:131],v[64:67],a[0:3]
s_add_u32 m0, s65, 16384
CV v10,v12,v13
CV v11,v14,v15
MM a[0:3],a[132:135],v[68:71],a[0:3]
RD a[192:195],v224 offset:49152
CV v4,v32,v33
MM a[8:11],a[132:135],v[76:79],a[8:11]
v_max3_f32 v154, v16, v17, v18
v_max_f32 v154, v154, v19
MM a[8:11],a[128:131],v[72:75],a[8:11]
buffer_load_dwordx4 v228, s[8:11], s64 offen offset:0 lds
MM a[12:15],a[136:139],v[72:75],a[12:15]
EX v34,v34
MM a[12:15],a[140:143],v[76:79],a[12:15]
RD a[196:199],v225 offset:49152
v_mov_b32 v155, v154
MM a[4:7],a[140:143],v[68:71],a[4:7]
s_nop 0
v_permlane16_swap_b32 v154, v155
v_max_f32 v154, v154, v155
MM a[4:7],a[136:139],v[64:67],a[4:7]
EX v35,v35
MM a[32:35],a[144:147],v[64:67],a[32:35]
RD a[200:203],v226 offset:49152
v_mov_b32 v155, v154
MM a[32:35],a[148:151],v[68:71],a[32:35]
s_nop 0
v_permlane32_swap_b32 v154, v155
v_max_f32 v154, v154, v155
MM a[40:43],a[148:151],v[76:79],a[40:43]
EX v36,v36
MM a[40:43],a[144:147],v[72:75],a[40:43]
v_sub_f32 v136, 0, v154
v_sub_f32 v137, 0, v154
MM a[44:47],a[152:155],v[72:75],a[44:47]
EX v37,v37
MM a[44:47],a[156:159],v[76:79],a[44:47]
RD a[204:207],v227 offset:49152
buffer_load_dwordx4 v229, s[8:11], s64 offen offset:1024 lds
v_sub_f32 v138, 0, v154
MM a[36:39],a[156:159],v[68:71],a[36:39]
v_sub_f32 v139, 0, v154
v_mov_b32_dpp v152, v144 row_ror:8 row_mask:0xf bank_mask:0xf
EX v38,v38
MM a[36:39],a[152:155],v[64:67],a[36:39]
RD a[208:211],v224 offset:53248
v_mov_b32_dpp v153, v145 row_ror:8 row_mask:0xf bank_mask:0xf
MM a[64:67],a[160:163],v[64:67],a[64:67]
EX v39,v39
v_mov_b32_dpp v154, v146 row_ror:8 row_mask:0xf bank_mask:0xf
MM a[64:67],a[164:167],v[68:71],a[64:67]
RD a[212:215],v225 offset:53248
buffer_load_dwordx4 v230, s[8:11], s64 offen offset:2048 lds
v_mov_b32_dpp v155, v147 row_ror:8 row_mask:0xf bank_mask:0xf
MM a[72:75],a[164:167],v[76:79],a[72:75]
RD a[216:219],v226 offset:53248
v_max3_f32 v158, v24, v25, v26
EX v40,v40
MM a[72:75],a[160:163],v[72:75],a[72:75]
v_max_f32 v158, v158, v27
v_mov_b32 v159, v158
CV v5,v34,v35
MM a[76:79],a[168:171],v[72:75],a[76:79]
buffer_load_dwordx4 v231, s[8:11], s64 offen offset:3072 lds
RD a[220:223],v227 offset:53248
EX v41,v41
v_permlane16_swap_b32 v158, v159
MM a[76:79],a[172:175],v[76:79],a[76:79]
s_add_u32 m0, s65, 81920
v_max_f32 v158, v158, v159
CV v6,v36,v37
MM a[68:71],a[172:175],v[68:71],a[68:71]
RD a[224:227],v224 offset:57344
EX v42,v42
v_mov_b32 v159, v158
MM a[68:71],a[168:171],v[64:67],a[68:71]
s_nop 0
v_permlane32_swap_b32 v158, v159
v_max_f32 v158, v158, v159
EX v43,v43
MM a[96:99],a[176:179],v[64:67],a[96:99]
RD a[228:231],v225 offset:57344
buffer_load_dwordx4 v235, s[12:15], s81 offen offset:0 lds
v_sub_f32 v140, 0, v158
MM a[96:99],a[180:183],v[68:71],a[96:99]
v_sub_f32 v141, 0, v158
CV v7,v38,v39
EX v44,v44
MM a[104:107],a[180:183],v[76:79],a[104:107]
RD a[232:235],v226 offset:57344
v_sub_f32 v142, 0, v158
MM a[104:107],a[176:179],v[72:75],a[104:107]
v_sub_f32 v143, 0, v158
v_mov_b32_dpp v156, v148 row_ror:8 row_mask:0xf bank_mask:0xf
EX v45,v45
MM a[108:111],a[184:187],v[72:75],a[108:111]
RD a[236:239],v227 offset:57344
buffer_load_dwordx4 v235, s[12:15], s64 offen offset:1024 lds
v_mov_b32_dpp v157, v149 row_ror:8 row_mask:0xf bank_mask:0xf
v_mov_b32_dpp v158, v150 row_ror:8 row_mask:0xf bank_mask:0xf
)ASM"
R"ASM(
v_mov_b32_dpp v159, v151 row_ror:8 row_mask:0xf bank_mask:0xf
MM a[108:111],a[188:191],v[76:79],a[108:111]
EX v46,v46
MM a[100:103],a[188:191],v[68:71],a[100:103]
RD a[240:243],v224 offset:61440
RD a[244:247],v225 offset:61440
RD a[248:251],v226 offset:61440
RD a[252:255],v227 offset:61440
EX v47,v47
MM a[100:103],a[184:187],v[64:67],a[100:103]
buffer_load_dwordx4 v235, s[12:15], s81 offen offset:2048 lds
CV v12,v40,v41
v_smfmac_f32_16x16x64_bf16 v[236:239], v[144:147], v[0:7], v234
CV v13,v42,v43
CV v14,v44,v45
CV v15,v46,v47
s_nop 1
v_smfmac_f32_16x16x64_bf16 v[236:239], v[148:151], v[8:15], v234
s_waitcnt lgkmcnt(0)
buffer_load_dwordx4 v235, s[12:15], s64 offen offset:3072 lds
MM v[64:67],a[192:195],v[160:163],v[128:131]
s_add_u32 s64, s64, 16384
TR a[128:129],v232 offset:32768
s_add_u32 s81, s64, 896
MM v[64:67],a[196:199],v[176:179],v[64:67]
TR a[130:131],v232 offset:36864
TR a[132:133],v232 offset:40960
MM v[64:67],a[200:203],v[192:195],v[64:67]
TR a[134:135],v232 offset:45056
TR a[136:137],v233 offset:32768
MM v[64:67],a[204:207],v[208:211],v[64:67]
TR a[138:139],v233 offset:36864
v_add_f32 v246, v236, v237
MM v[68:71],a[220:223],v[208:211],v[128:131]
TR a[140:141],v233 offset:40960
v_cmp_nge_f32 vcc, s71, v246
MM v[68:71],a[216:219],v[192:195],v[68:71]
s_cbranch_vccnz LSITE2_%=
LBACK2_%=:
TR a[142:143],v233 offset:45056
TR a[144:145],v232 offset:33280
MM v[68:71],a[212:215],v[176:179],v[68:71]
TR a[146:147],v232 offset:37376
TR a[148:149],v232 offset:41472
MM v[68:71],a[208:211],v[160:163],v[68:71]
TR a[150:151],v232 offset:45568
TR a[152:153],v233 offset:33280
MM v[76:79],a[208:211],v[164:167],v[132:135]
EX v64,v64
MM v[76:79],a[212:215],v[180:183],v[76:79]
EX v65,v65
MM v[76:79],a[216:219],v[196:199],v[76:79]
TR a[154:155],v233 offset:37376
CV v64,v64,v65
MM v[76:79],a[220:223],v[212:215],v[76:79]
EX v66,v66
MM v[72:75],a[204:207],v[212:215],v[132:135]
EX v67,v67
MM v[72:75],a[200:203],v[196:199],v[72:75]
TR a[156:157],v233 offset:41472
CV v65,v66,v67
MM v[72:75],a[196:199],v[180:183],v[72:75]
EX v68,v68
MM v[72:75],a[192:195],v[164:167],v[72:75]
EX v69,v69
MM v[80:83],a[192:195],v[168:171],v[136:139]
TR a[158:159],v233 offset:45568
CV v66,v68,v69
MM v[80:83],a[196:199],v[184:187],v[80:83]
EX v70,v70
MM v[80:83],a[200:203],v[200:203],v[80:83]
EX v71,v71
MM v[80:83],a[204:207],v[216:219],v[80:83]
TR a[160:161],v232 offset:33792
CV v67,v70,v71
MM v[84:87],a[220:223],v[216:219],v[136:139]
s_waitcnt vmcnt(8)
v_xor_b32 v48, s69, v48
v_xor_b32 v49, s69, v49
MM v[84:87],a[216:219],v[200:203],v[84:87]
v_xor_b32 v50, s69, v50
v_xor_b32 v51, s69, v51
MM v[84:87],a[212:215],v[184:187],v[84:87]
v_or3_b32 v52, v48, v49, v50
v_or_b32 v52, v52, v51
EX v72,v72
MM v[84:87],a[208:211],v[168:171],v[84:87]
v_cmp_ne_u32 vcc, 0, v52
s_cbranch_vccnz LRFR_%=
LRFRb_%=:
EX v73,v73
MM v[92:95],a[208:211],v[172:175],v[140:143]
s_lshl_b32 s90, %[swave], 11
s_add_u32 s78, s90, %[slds]
s_add_u32 s78, s78, 155648
MM v[92:95],a[212:215],v[188:191],v[92:95]
s_add_u32 s90, s90, 53248
s_mov_b32 m0, s78
CV v72,v72,v73
EX v74,v74
MM v[92:95],a[216:219],v[204:207],v[92:95]
buffer_load_dwordx4 v248, s[20:23], s90 offen offset:0 sc1 lds
EX v75,v75
MM v[92:95],a[220:223],v[220:223],v[92:95]
buffer_load_dwordx4 v248, s[20:23], s90 offen offset:1024 sc1 lds
EX v76,v76
MM v[88:91],a[204:207],v[220:223],v[140:143]
EX v77,v77
MM v[88:91],a[200:203],v[204:207],v[88:91]
EX v78,v78
MM v[88:91],a[196:199],v[188:191],v[88:91]
EX v79,v79
MM v[88:91],a[192:195],v[172:175],v[88:91]
CV v73,v74,v75
CV v74,v76,v77
MM v[96:99],a[224:227],v[160:163],v[128:131]
EX v80,v80
MM v[96:99],a[228:231],v[176:179],v[96:99]
EX v81,v81
MM v[96:99],a[232:235],v[192:195],v[96:99]
EX v82,v82
MM v[96:99],a[236:239],v[208:211],v[96:99]
TR a[162:163],v232 offset:37888
CV v75,v78,v79
MM v[100:103],a[252:255],v[208:211],v[128:131]
EX v83,v83
MM v[100:103],a[248:251],v[192:195],v[100:103]
TR a[164:165],v232 offset:41984
TR a[166:167],v232 offset:46080
MM v[100:103],a[244:247],v[176:179],v[100:103]
EX v84,v84
MM v[100:103],a[240:243],v[160:163],v[100:103]
EX v85,v85
MM v[108:111],a[240:243],v[164:167],v[132:135]
EX v86,v86
MM v[108:111],a[244:247],v[180:183],v[108:111]
TR a[168:169],v233 offset:33792
CV v80,v80,v81
MM v[108:111],a[248:251],v[196:199],v[108:111]
EX v87,v87
MM v[108:111],a[252:255],v[212:215],v[108:111]
EX v88,v88
MM v[104:107],a[236:239],v[212:215],v[132:135]
TR a[170:171],v233 offset:37888
CV v81,v82,v83
MM v[104:107],a[232:235],v[196:199],v[104:107]
EX v89,v89
MM v[104:107],a[228:231],v[180:183],v[104:107]
EX v90,v90
MM v[104:107],a[224:227],v[164:167],v[104:107]
TR a[172:173],v233 offset:41984
CV v82,v84,v85
MM v[112:115],a[224:227],v[168:171],v[136:139]
TR a[174:175],v233 offset:46080
EX v91,v91
MM v[112:115],a[228:231],v[184:187],v[112:115]
EX v92,v92
MM v[112:115],a[232:235],v[200:203],v[112:115]
TR a[176:177],v232 offset:34304
EX v93,v93
MM v[112:115],a[236:239],v[216:219],v[112:115]
TR a[178:179],v232 offset:38400
CV v83,v86,v87
MM v[116:119],a[252:255],v[216:219],v[136:139]
EX v94,v94
MM v[116:119],a[248:251],v[200:203],v[116:119]
EX v95,v95
MM v[116:119],a[244:247],v[184:187],v[116:119]
TR a[180:181],v232 offset:42496
CV v88,v88,v89
MM v[116:119],a[240:243],v[168:171],v[116:119]
EX v96,v96
MM v[124:127],a[240:243],v[172:175],v[140:143]
TR a[182:183],v232 offset:46592
EX v97,v97
CV v89,v90,v91
MM v[124:127],a[244:247],v[188:191],v[124:127]
EX v98,v98
MM v[124:127],a[248:251],v[204:207],v[124:127]
TR a[184:185],v233 offset:34304
TR a[186:187],v233 offset:38400
TR a[188:189],v233 offset:42496
TR a[190:191],v233 offset:46592
MM v[124:127],a[252:255],v[220:223],v[124:127]
EX v99,v99
CV v90,v92,v93
MM v[120:123],a[236:239],v[220:223],v[140:143]
EX v100,v100
MM v[120:123],a[232:235],v[204:207],v[120:123]
EX v101,v101
CV v91,v94,v95
MM v[120:123],a[228:231],v[188:191],v[120:123]
EX v102,v102
MM v[120:123],a[224:227],v[172:175],v[120:123]
s_waitcnt lgkmcnt(0)
s_barrier
CV v68,v96,v97
MM a[0:3],a[128:131],v[0:3],a[0:3]
s_add_u32 m0, s65, 32768
EX v103,v103
MM a[0:3],a[132:135],v[4:7],a[0:3]
EX v104,v104
MM a[8:11],a[132:135],v[12:15],a[8:11]
CV v69,v98,v99
CV v70,v100,v101
MM a[8:11],a[128:131],v[8:11],a[8:11]
EX v105,v105
MM a[12:15],a[136:139],v[8:11],a[12:15]
EX v106,v106
MM a[12:15],a[140:143],v[12:15],a[12:15]
EX v107,v107
MM a[4:7],a[140:143],v[4:7],a[4:7]
EX v108,v108
MM a[4:7],a[136:139],v[0:3],a[4:7]
RD a[192:195],v224 offset:0
CV v71,v102,v103
MM a[32:35],a[144:147],v[0:3],a[32:35]
EX v109,v109
MM a[32:35],a[148:151],v[4:7],a[32:35]
buffer_load_dwordx4 v228, s[8:11], s64 offen offset:0 lds
MM a[40:43],a[148:151],v[12:15],a[40:43]
RD a[196:199],v225 offset:0
EX v110,v110
MM a[40:43],a[144:147],v[8:11],a[40:43]
EX v111,v111
CV v76,v104,v105
MM a[44:47],a[152:155],v[8:11],a[44:47]
RD a[200:203],v226 offset:0
EX v112,v112
MM a[44:47],a[156:159],v[12:15],a[44:47]
buffer_load_dwordx4 v229, s[8:11], s64 offen offset:1024 lds
MM a[36:39],a[156:159],v[4:7],a[36:39]
RD a[204:207],v227 offset:0
EX v113,v113
CV v77,v106,v107
MM a[36:39],a[152:155],v[0:3],a[36:39]
EX v114,v114
MM a[64:67],a[160:163],v[0:3],a[64:67]
RD a[208:211],v224 offset:4096
EX v115,v115
CV v78,v108,v109
MM a[64:67],a[164:167],v[4:7],a[64:67]
buffer_load_dwordx4 v230, s[8:11], s64 offen offset:2048 lds
MM a[72:75],a[164:167],v[12:15],a[72:75]
RD a[212:215],v225 offset:4096
EX v116,v116
MM a[72:75],a[160:163],v[8:11],a[72:75]
EX v117,v117
CV v79,v110,v111
MM a[76:79],a[168:171],v[8:11],a[76:79]
RD a[216:219],v226 offset:4096
CV v84,v112,v113
EX v118,v118
MM a[76:79],a[172:175],v[12:15],a[76:79]
buffer_load_dwordx4 v231, s[8:11], s64 offen offset:3072 lds
MM a[68:71],a[172:175],v[4:7],a[68:71]
s_add_u32 m0, s65, 98304
RD a[220:223],v227 offset:4096
EX v119,v119
MM a[68:71],a[168:171],v[0:3],a[68:71]
EX v120,v120
MM a[96:99],a[176:179],v[0:3],a[96:99]
RD a[224:227],v224 offset:8192
CV v85,v114,v115
EX v121,v121
MM a[96:99],a[180:183],v[4:7],a[96:99]
RD a[228:231],v225 offset:8192
CV v86,v116,v117
MM a[104:107],a[180:183],v[12:15],a[104:107]
buffer_load_dwordx4 v235, s[12:15], s81 offen offset:0 lds
EX v122,v122
MM a[104:107],a[176:179],v[8:11],a[104:107]
EX v123,v123
MM a[108:111],a[184:187],v[8:11],a[108:111]
RD a[232:235],v226 offset:8192
CV v87,v118,v119
MM a[108:111],a[188:191],v[12:15],a[108:111]
EX v124,v124
MM a[100:103],a[188:191],v[4:7],a[100:103]
RD a[236:239],v227 offset:8192
buffer_load_dwordx4 v235, s[12:15], s64 offen offset:1024 lds
EX v125,v125
MM a[100:103],a[184:187],v[0:3],a[100:103]
EX v126,v126
v_smfmac_f32_16x16x64_bf16 v[236:239], v[144:147], v[64:71], v234
RD a[240:243],v224 offset:12288
RD a[244:247],v225 offset:12288
RD a[248:251],v226 offset:12288
RD a[252:255],v227 offset:12288
EX v127,v127
v_smfmac_f32_16x16x64_bf16 v[236:239], v[148:151], v[72:79], v234
CV v92,v120,v121
v_smfmac_f32_16x16x64_bf16 v[236:239], v[152:155], v[80:87], v234
buffer_load_dwordx4 v235, s[12:15], s81 offen offset:2048 lds
CV v93,v122,v123
CV v94,v124,v125
CV v95,v126,v127
s_nop 1
v_smfmac_f32_16x16x64_bf16 v[236:239], v[156:159], v[88:95], v234
s_waitcnt lgkmcnt(0)
buffer_load_dwordx4 v235, s[12:15], s64 offen offset:3072 lds
MM v[0:3],a[192:195],v[160:163],v[128:131]
s_add_u32 s64, s64, 16384
TR a[128:129],v232 offset:49152
s_add_u32 s81, s64, 896
MM v[0:3],a[196:199],v[176:179],v[0:3]
TR a[130:131],v232 offset:53248
TR a[132:133],v232 offset:57344
MM v[0:3],a[200:203],v[192:195],v[0:3]
TR a[134:135],v232 offset:61440
TR a[136:137],v233 offset:49152
MM v[0:3],a[204:207],v[208:211],v[0:3]
TR a[138:139],v233 offset:53248
v_add_f32 v247, v236, v237
MM v[4:7],a[220:223],v[208:211],v[128:131]
TR a[140:141],v233 offset:57344
v_cmp_nge_f32 vcc, s71, v247
MM v[4:7],a[216:219],v[192:195],v[4:7]
s_cbranch_vccnz LSITE3_%=
LBACK3_%=:
TR a[142:143],v233 offset:61440
TR a[144:145],v232 offset:49664
MM v[4:7],a[212:215],v[176:179],v[4:7]
s_cmp_lg_u32 %[sb], 0
s_cbranch_scc1 LEP_%=
v_mov_b32 v250, s69
s_mov_b32 s76, 32768
s_mov_b64 exec, 1
buffer_store_dword v250, off, s[20:23], s76 sc1
s_mov_b64 exec, -1
s_nop 1
LEP_%=:
MM v[4:7],a[208:211],v[160:163],v[4:7]
s_add_u32 s79, %[slds], 154112
TR a[146:147],v232 offset:53760
TR a[148:149],v232 offset:57856
MM v[12:15],a[208:211],v[164:167],v[132:135]
s_lshl_b32 s80, %[swave], 2
EX v0,v0
MM v[12:15],a[212:215],v[180:183],v[12:15]
s_mov_b32 s92, 0
v_lshrrev_b32 v250, 2, v248
v_add_u32 v250, s79, v250
MM v[12:15],a[216:219],v[196:199],v[12:15]
TR a[150:151],v232 offset:61952
v_mov_b32 v251, 0
MM v[12:15],a[220:223],v[212:215],v[12:15]
ds_write_b32 v250, v251 offset:0
v_lshrrev_b32 v252, 6, v248
MM v[8:11],a[204:207],v[212:215],v[132:135]
v_mul_u32_u24 v252, 3456, v252
v_and_b32 v251, 48, v248
MM v[8:11],a[200:203],v[196:199],v[8:11]
TR a[152:153],v233 offset:49664
v_add_u32 v252, v252, v251
MM v[8:11],a[196:199],v[180:183],v[8:11]
s_and_b32 s90, %[swave], 1
EX v1,v1
MM v[8:11],a[192:195],v[164:167],v[8:11]
s_mul_i32 s90, s90, 276480
EX v2,v2
MM v[16:19],a[192:195],v[168:171],v[136:139]
s_bfe_u32 s91, s48, 131077
EX v3,v3
MM v[16:19],a[196:199],v[184:187],v[16:19]
s_mul_i32 s91, s91, 552960
EX v4,v4
MM v[16:19],a[200:203],v[200:203],v[16:19]
s_add_u32 s90, s90, s91
CV v0,v0,v1
CV v1,v2,v3
MM v[16:19],a[204:207],v[216:219],v[16:19]
s_and_b32 s91, s48, 0xffffff80
EX v5,v5
MM v[20:23],a[220:223],v[216:219],v[136:139]
s_add_u32 s90, s90, s91
EX v6,v6
MM v[20:23],a[216:219],v[200:203],v[20:23]
s_and_b32 s91, s48, 16
TR a[154:155],v233 offset:53760
CV v2,v4,v5
MM v[20:23],a[212:215],v[184:187],v[20:23]
s_lshl_b32 s91, s91, 2
EX v7,v7
MM v[20:23],a[208:211],v[168:171],v[20:23]
s_add_u32 s66, s90, s91
TR a[156:157],v233 offset:57856
CV v3,v6,v7
MM v[28:31],a[208:211],v[172:175],v[140:143]
s_mul_i32 s91, %[swave], 5120
EX v8,v8
MM v[28:31],a[212:215],v[188:191],v[28:31]
s_add_u32 s91, s91, %[slds]
EX v9,v9
MM v[28:31],a[216:219],v[204:207],v[28:31]
s_add_u32 s67, s91, 131072
TR a[158:159],v233 offset:61952
CV v8,v8,v9
MM v[28:31],a[220:223],v[220:223],v[28:31]
s_add_u32 s85, s66, 221184
EX v10,v10
MM v[24:27],a[204:207],v[220:223],v[140:143]
)ASM"
R"ASM(
s_add_u32 s86, s67, 4096
TR a[160:161],v232 offset:50176
TR a[162:163],v232 offset:54272
MM v[24:27],a[200:203],v[204:207],v[24:27]
EX v11,v11
LPG6_%=:
s_cbranch_execz LPG10_%=
MM v[24:27],a[196:199],v[188:191],v[24:27]
TR a[164:165],v232 offset:58368
CV v9,v10,v11
MM v[24:27],a[192:195],v[172:175],v[24:27]
EX v12,v12
MM v[32:35],a[224:227],v[160:163],v[128:131]
EX v13,v13
MM v[32:35],a[228:231],v[176:179],v[32:35]
TR a[166:167],v232 offset:62464
CV v10,v12,v13
MM v[32:35],a[232:235],v[192:195],v[32:35]
EX v14,v14
MM v[32:35],a[236:239],v[208:211],v[32:35]
TR a[168:169],v233 offset:50176
TR a[170:171],v233 offset:54272
MM v[36:39],a[252:255],v[208:211],v[128:131]
EX v15,v15
MM v[36:39],a[248:251],v[192:195],v[36:39]
TR a[172:173],v233 offset:58368
CV v11,v14,v15
MM v[36:39],a[244:247],v[176:179],v[36:39]
EX v16,v16
MM v[36:39],a[240:243],v[160:163],v[36:39]
EX v17,v17
MM v[44:47],a[240:243],v[164:167],v[132:135]
TR a[174:175],v233 offset:62464
CV v16,v16,v17
MM v[44:47],a[244:247],v[180:183],v[44:47]
EX v18,v18
MM v[44:47],a[248:251],v[196:199],v[44:47]
TR a[176:177],v232 offset:50688
TR a[178:179],v232 offset:54784
MM v[44:47],a[252:255],v[212:215],v[44:47]
EX v19,v19
MM v[40:43],a[236:239],v[212:215],v[132:135]
TR a[180:181],v232 offset:58880
CV v17,v18,v19
MM v[40:43],a[232:235],v[196:199],v[40:43]
EX v20,v20
MM v[40:43],a[228:231],v[180:183],v[40:43]
TR a[182:183],v232 offset:62976
TR a[184:185],v233 offset:50688
MM v[40:43],a[224:227],v[164:167],v[40:43]
EX v21,v21
MM v[48:51],a[224:227],v[168:171],v[136:139]
TR a[186:187],v233 offset:54784
CV v18,v20,v21
MM v[48:51],a[228:231],v[184:187],v[48:51]
EX v22,v22
MM v[48:51],a[232:235],v[200:203],v[48:51]
EX v23,v23
MM v[48:51],a[236:239],v[216:219],v[48:51]
TR a[188:189],v233 offset:58880
CV v19,v22,v23
MM v[52:55],a[252:255],v[216:219],v[136:139]
EX v24,v24
MM v[52:55],a[248:251],v[200:203],v[52:55]
EX v25,v25
MM v[52:55],a[244:247],v[184:187],v[52:55]
TR a[190:191],v233 offset:62976
CV v24,v24,v25
MM v[52:55],a[240:243],v[168:171],v[52:55]
EX v26,v26
MM v[60:63],a[240:243],v[172:175],v[140:143]
EX v27,v27
MM v[60:63],a[244:247],v[188:191],v[60:63]
EX v28,v28
MM v[60:63],a[248:251],v[204:207],v[60:63]
EX v29,v29
MM v[60:63],a[252:255],v[220:223],v[60:63]
EX v30,v30
MM v[56:59],a[236:239],v[220:223],v[140:143]
EX v31,v31
MM v[56:59],a[232:235],v[204:207],v[56:59]
EX v32,v32
MM v[56:59],a[228:231],v[188:191],v[56:59]
EX v33,v33
MM v[56:59],a[224:227],v[172:175],v[56:59]
s_waitcnt vmcnt(8) lgkmcnt(0)
s_barrier
MM a[0:3],a[128:131],v[64:67],a[0:3]
s_add_u32 m0, s65, 49152
CV v25,v26,v27
CV v26,v28,v29
MM a[0:3],a[132:135],v[68:71],a[0:3]
RD a[192:195],v224 offset:16384
CV v27,v30,v31
MM a[8:11],a[132:135],v[76:79],a[8:11]
buffer_load_dwordx4 v228, s[8:11], s64 offen offset:0 lds
MM a[8:11],a[128:131],v[72:75],a[8:11]
RD a[196:199],v225 offset:16384
CV v4,v32,v33
MM a[16:19],a[128:131],v[80:83],0
RD a[200:203],v226 offset:16384
RD a[204:207],v227 offset:16384
MM a[16:19],a[132:135],v[84:87],a[16:19]
buffer_load_dwordx4 v229, s[8:11], s64 offen offset:1024 lds
MM a[24:27],a[132:135],v[92:95],0
EX v34,v34
MM a[24:27],a[128:131],v[88:91],a[24:27]
RD a[208:211],v224 offset:20480
RD a[212:215],v225 offset:20480
MM a[28:31],a[136:139],v[88:91],0
EX v35,v35
MM a[28:31],a[140:143],v[92:95],a[28:31]
RD a[216:219],v226 offset:20480
CV v5,v34,v35
MM a[20:23],a[140:143],v[84:87],0
EX v36,v36
MM a[20:23],a[136:139],v[80:83],a[20:23]
EX v37,v37
MM a[12:15],a[136:139],v[72:75],a[12:15]
RD a[220:223],v227 offset:20480
CV v6,v36,v37
MM a[12:15],a[140:143],v[76:79],a[12:15]
buffer_load_dwordx4 v230, s[8:11], s64 offen offset:2048 lds
MM a[4:7],a[140:143],v[68:71],a[4:7]
EX v38,v38
MM a[4:7],a[136:139],v[64:67],a[4:7]
EX v39,v39
MM a[32:35],a[144:147],v[64:67],a[32:35]
RD a[224:227],v224 offset:24576
CV v7,v38,v39
MM a[32:35],a[148:151],v[68:71],a[32:35]
EX v40,v40
MM a[40:43],a[148:151],v[76:79],a[40:43]
EX v41,v41
MM a[40:43],a[144:147],v[72:75],a[40:43]
RD a[228:231],v225 offset:24576
CV v12,v40,v41
MM a[48:51],a[144:147],v[80:83],0
buffer_load_dwordx4 v231, s[8:11], s64 offen offset:3072 lds
MM a[48:51],a[148:151],v[84:87],a[48:51]
s_add_u32 m0, s65, 114688
EX v42,v42
MM a[56:59],a[148:151],v[92:95],0
EX v43,v43
MM a[56:59],a[144:147],v[88:91],a[56:59]
RD a[232:235],v226 offset:24576
CV v13,v42,v43
MM a[60:63],a[152:155],v[88:91],0
EX v44,v44
MM a[60:63],a[156:159],v[92:95],a[60:63]
buffer_load_dwordx4 v235, s[12:15], s81 offen offset:0 lds
MM a[52:55],a[156:159],v[84:87],0
EX v45,v45
MM a[52:55],a[152:155],v[80:83],a[52:55]
RD a[236:239],v227 offset:24576
CV v14,v44,v45
MM a[44:47],a[152:155],v[72:75],a[44:47]
EX v46,v46
MM a[44:47],a[156:159],v[76:79],a[44:47]
EX v47,v47
MM a[36:39],a[156:159],v[68:71],a[36:39]
RD a[240:243],v224 offset:28672
CV v15,v46,v47
MM a[36:39],a[152:155],v[64:67],a[36:39]
EX v48,v48
MM a[64:67],a[160:163],v[64:67],a[64:67]
buffer_load_dwordx4 v235, s[12:15], s64 offen offset:1024 lds
MM a[64:67],a[164:167],v[68:71],a[64:67]
EX v49,v49
MM a[72:75],a[164:167],v[76:79],a[72:75]
RD a[244:247],v225 offset:28672
CV v20,v48,v49
MM a[72:75],a[160:163],v[72:75],a[72:75]
EX v50,v50
MM a[80:83],a[160:163],v[80:83],0
EX v51,v51
MM a[80:83],a[164:167],v[84:87],a[80:83]
RD a[248:251],v226 offset:28672
CV v21,v50,v51
MM a[88:91],a[164:167],v[92:95],0
EX v52,v52
MM a[88:91],a[160:163],v[88:91],a[88:91]
buffer_load_dwordx4 v235, s[12:15], s81 offen offset:2048 lds
MM a[92:95],a[168:171],v[88:91],0
EX v53,v53
MM a[92:95],a[172:175],v[92:95],a[92:95]
RD a[252:255],v227 offset:28672
CV v22,v52,v53
MM a[84:87],a[172:175],v[84:87],0
EX v54,v54
MM a[84:87],a[168:171],v[80:83],a[84:87]
EX v55,v55
MM a[76:79],a[168:171],v[72:75],a[76:79]
CV v23,v54,v55
MM a[76:79],a[172:175],v[76:79],a[76:79]
EX v56,v56
MM a[68:71],a[172:175],v[68:71],a[68:71]
buffer_load_dwordx4 v235, s[12:15], s64 offen offset:3072 lds
MM a[68:71],a[168:171],v[64:67],a[68:71]
EX v57,v57
MM a[96:99],a[176:179],v[64:67],a[96:99]
EX v58,v58
MM a[96:99],a[180:183],v[68:71],a[96:99]
EX v59,v59
MM a[104:107],a[180:183],v[76:79],a[104:107]
EX v60,v60
MM a[104:107],a[176:179],v[72:75],a[104:107]
EX v61,v61
MM a[112:115],a[176:179],v[80:83],0
EX v62,v62
MM a[112:115],a[180:183],v[84:87],a[112:115]
EX v63,v63
MM a[120:123],a[180:183],v[92:95],0
CV v28,v56,v57
CV v29,v58,v59
MM a[120:123],a[176:179],v[88:91],a[120:123]
CV v30,v60,v61
CV v31,v62,v63
MM a[124:127],a[184:187],v[88:91],0
MM a[124:127],a[188:191],v[92:95],a[124:127]
MM a[116:119],a[188:191],v[84:87],0
MM a[116:119],a[184:187],v[80:83],a[116:119]
MM a[108:111],a[184:187],v[72:75],a[108:111]
MM a[108:111],a[188:191],v[76:79],a[108:111]
MM a[100:103],a[188:191],v[68:71],a[100:103]
MM a[100:103],a[184:187],v[64:67],a[100:103]
v_smfmac_f32_16x16x64_bf16 v[236:239], v[144:147], v[0:7], v234
v_smfmac_f32_16x16x64_bf16 v[236:239], v[148:151], v[8:15], v234
v_smfmac_f32_16x16x64_bf16 v[236:239], v[152:155], v[16:23], v234
v_smfmac_f32_16x16x64_bf16 v[236:239], v[156:159], v[24:31], v234
s_waitcnt lgkmcnt(0)
s_mov_b32 s70, 2
LLA_%=:
MM v[64:67],a[192:195],v[160:163],v[128:131]
s_add_u32 s64, s64, 16384
TR a[128:129],v232 offset:0
s_add_u32 s81, s64, 896
MM v[64:67],a[196:199],v[176:179],v[64:67]
TR a[130:131],v232 offset:4096
TR a[132:133],v232 offset:8192
MM v[64:67],a[200:203],v[192:195],v[64:67]
TR a[134:135],v232 offset:12288
TR a[136:137],v233 offset:0
MM v[64:67],a[204:207],v[208:211],v[64:67]
TR a[138:139],v233 offset:4096
v_add_f32 v246, v236, v237
MM v[68:71],a[220:223],v[208:211],v[128:131]
TR a[140:141],v233 offset:8192
v_cmp_nge_f32 vcc, s71, v246
MM v[68:71],a[216:219],v[192:195],v[68:71]
s_cbranch_vccnz LSITE4_%=
LBACK4_%=:
TR a[142:143],v233 offset:12288
TR a[144:145],v232 offset:512
MM v[68:71],a[212:215],v[176:179],v[68:71]
s_mov_b32 m0, s67
TR a[146:147],v232 offset:4608
TR a[148:149],v232 offset:8704
MM v[68:71],a[208:211],v[160:163],v[68:71]
buffer_load_dwordx4 v252, s[36:39], s66 offen offset:0 lds
MM v[76:79],a[208:211],v[164:167],v[132:135]
s_add_u32 s66, s66, 55296
EX v64,v64
MM v[76:79],a[212:215],v[180:183],v[76:79]
s_min_u32 s66, s66, s85
EX v65,v65
MM v[76:79],a[216:219],v[196:199],v[76:79]
s_add_u32 s67, s67, 1024
TR a[150:151],v232 offset:12800
CV v64,v64,v65
MM v[76:79],a[220:223],v[212:215],v[76:79]
s_min_u32 s67, s67, s86
EX v66,v66
MM v[72:75],a[204:207],v[212:215],v[132:135]
EX v67,v67
MM v[72:75],a[200:203],v[196:199],v[72:75]
TR a[152:153],v233 offset:512
CV v65,v66,v67
MM v[72:75],a[196:199],v[180:183],v[72:75]
EX v68,v68
MM v[72:75],a[192:195],v[164:167],v[72:75]
EX v69,v69
MM v[80:83],a[192:195],v[168:171],v[136:139]
TR a[154:155],v233 offset:4608
CV v66,v68,v69
MM v[80:83],a[196:199],v[184:187],v[80:83]
EX v70,v70
MM v[80:83],a[200:203],v[200:203],v[80:83]
EX v71,v71
MM v[80:83],a[204:207],v[216:219],v[80:83]
TR a[156:157],v233 offset:8704
CV v67,v70,v71
MM v[84:87],a[220:223],v[216:219],v[136:139]
EX v72,v72
MM v[84:87],a[216:219],v[200:203],v[84:87]
TR a[158:159],v233 offset:12800
TR a[160:161],v232 offset:1024
MM v[84:87],a[212:215],v[184:187],v[84:87]
EX v73,v73
MM v[84:87],a[208:211],v[168:171],v[84:87]
TR a[162:163],v232 offset:5120
CV v72,v72,v73
MM v[92:95],a[208:211],v[172:175],v[140:143]
EX v74,v74
MM v[92:95],a[212:215],v[188:191],v[92:95]
TR a[164:165],v232 offset:9216
TR a[166:167],v232 offset:13312
MM v[92:95],a[216:219],v[204:207],v[92:95]
EX v75,v75
MM v[92:95],a[220:223],v[220:223],v[92:95]
TR a[168:169],v233 offset:1024
CV v73,v74,v75
MM v[88:91],a[204:207],v[220:223],v[140:143]
EX v76,v76
MM v[88:91],a[200:203],v[204:207],v[88:91]
EX v77,v77
MM v[88:91],a[196:199],v[188:191],v[88:91]
TR a[170:171],v233 offset:5120
CV v74,v76,v77
MM v[88:91],a[192:195],v[172:175],v[88:91]
EX v78,v78
MM v[96:99],a[224:227],v[160:163],v[128:131]
TR a[172:173],v233 offset:9216
TR a[174:175],v233 offset:13312
MM v[96:99],a[228:231],v[176:179],v[96:99]
EX v79,v79
MM v[96:99],a[232:235],v[192:195],v[96:99]
TR a[176:177],v232 offset:1536
CV v75,v78,v79
MM v[96:99],a[236:239],v[208:211],v[96:99]
EX v80,v80
MM v[100:103],a[252:255],v[208:211],v[128:131]
EX v81,v81
MM v[100:103],a[248:251],v[192:195],v[100:103]
TR a[178:179],v232 offset:5632
CV v80,v80,v81
MM v[100:103],a[244:247],v[176:179],v[100:103]
EX v82,v82
MM v[100:103],a[240:243],v[160:163],v[100:103]
TR a[180:181],v232 offset:9728
TR a[182:183],v232 offset:13824
MM v[108:111],a[240:243],v[164:167],v[132:135]
v_add_u32 v44, s78, v248
v_mov_b32 v45, s79
MM v[108:111],a[244:247],v[180:183],v[108:111]
EX v83,v83
MM v[108:111],a[248:251],v[196:199],v[108:111]
TR a[184:185],v233 offset:1536
CV v81,v82,v83
MM v[108:111],a[252:255],v[212:215],v[108:111]
EX v84,v84
MM v[104:107],a[236:239],v[212:215],v[132:135]
EX v85,v85
MM v[104:107],a[232:235],v[196:199],v[104:107]
TR a[186:187],v233 offset:5632
CV v82,v84,v85
MM v[104:107],a[228:231],v[180:183],v[104:107]
EX v86,v86
MM v[104:107],a[224:227],v[164:167],v[104:107]
TR a[188:189],v233 offset:9728
TR a[190:191],v233 offset:13824
MM v[112:115],a[224:227],v[168:171],v[136:139]
EX v87,v87
MM v[112:115],a[228:231],v[184:187],v[112:115]
CV v83,v86,v87
MM v[112:115],a[232:235],v[200:203],v[112:115]
EX v88,v88
MM v[112:115],a[236:239],v[216:219],v[112:115]
EX v89,v89
MM v[116:119],a[252:255],v[216:219],v[136:139]
EX v90,v90
MM v[116:119],a[248:251],v[200:203],v[116:119]
EX v91,v91
MM v[116:119],a[244:247],v[184:187],v[116:119]
EX v92,v92
MM v[116:119],a[240:243],v[168:171],v[116:119]
EX v93,v93
MM v[124:127],a[240:243],v[172:175],v[140:143]
EX v94,v94
MM v[124:127],a[244:247],v[188:191],v[124:127]
EX v95,v95
MM v[124:127],a[248:251],v[204:207],v[124:127]
EX v96,v96
MM v[124:127],a[252:255],v[220:223],v[124:127]
EX v97,v97
MM v[120:123],a[236:239],v[220:223],v[140:143]
EX v98,v98
MM v[120:123],a[232:235],v[204:207],v[120:123]
EX v99,v99
MM v[120:123],a[228:231],v[188:191],v[120:123]
EX v100,v100
MM v[120:123],a[224:227],v[172:175],v[120:123]
s_waitcnt vmcnt(13) lgkmcnt(0)
s_barrier
MM a[0:3],a[128:131],v[0:3],a[0:3]
s_add_u32 m0, s65, 0
RD v[40:43],v45 offset:0
RD v[32:35],v44 offset:0
MM a[0:3],a[132:135],v[4:7],a[0:3]
)ASM"
R"ASM(
RD v[36:39],v44 offset:1024
CV v88,v88,v89
MM a[8:11],a[132:135],v[12:15],a[8:11]
CV v89,v90,v91
CV v90,v92,v93
MM a[8:11],a[128:131],v[8:11],a[8:11]
RD a[192:195],v224 offset:32768
CV v91,v94,v95
MM a[16:19],a[128:131],v[16:19],a[16:19]
buffer_load_dwordx4 v228, s[8:11], s64 offen offset:0 lds
MM a[16:19],a[132:135],v[20:23],a[16:19]
RD a[196:199],v225 offset:32768
CV v68,v96,v97
MM a[24:27],a[132:135],v[28:31],a[24:27]
s_waitcnt lgkmcnt(2)
v_min3_u32 v40, v40, v41, v42
v_min_u32 v40, v40, v43
MM a[24:27],a[128:131],v[24:27],a[24:27]
v_readfirstlane_b32 s76, v40
v_add_u32 v45, s80, v45
MM a[28:31],a[136:139],v[24:27],a[28:31]
s_sub_u32 s77, s92, s76
RD a[200:203],v226 offset:32768
CV v69,v98,v99
MM a[28:31],a[140:143],v[28:31],a[28:31]
s_sub_u32 s92, s77, 1
v_sub_u32 v32, s92, v32
v_sub_u32 v33, s92, v33
MM a[20:23],a[140:143],v[20:23],a[20:23]
s_and_b32 s77, s77, 65535
v_sub_u32 v34, s92, v34
v_sub_u32 v35, s92, v35
MM a[20:23],a[136:139],v[16:19],a[20:23]
s_mov_b32 s55, s59
v_sub_u32 v36, s92, v36
v_sub_u32 v37, s92, v37
MM a[12:15],a[136:139],v[8:11],a[12:15]
s_sub_u32 s59, 65535, s77
v_sub_u32 v38, s92, v38
v_sub_u32 v39, s92, v39
MM a[12:15],a[140:143],v[12:15],a[12:15]
v_min3_u32 v32, v32, v33, v34
v_min3_u32 v35, v35, v36, v37
MM a[4:7],a[140:143],v[4:7],a[4:7]
v_min3_u32 v32, v32, v38, v39
v_min_u32 v32, v32, v35
MM a[4:7],a[136:139],v[0:3],a[4:7]
RD a[204:207],v227 offset:32768
v_min_u32_dpp v32, v32, v32 row_shr:1 row_mask:0xf bank_mask:0xf
MM a[32:35],a[144:147],v[0:3],a[32:35]
RD a[208:211],v224 offset:36864
v_min_u32_dpp v32, v32, v32 row_shr:2 row_mask:0xf bank_mask:0xf
MM a[32:35],a[148:151],v[4:7],a[32:35]
RD a[212:215],v225 offset:36864
v_min_u32_dpp v32, v32, v32 row_shr:4 row_mask:0xf bank_mask:0xf
EX v101,v101
MM a[40:43],a[148:151],v[12:15],a[40:43]
v_min_u32_dpp v32, v32, v32 row_shr:8 row_mask:0xf bank_mask:0xf
CV v70,v100,v101
MM a[40:43],a[144:147],v[8:11],a[40:43]
RD a[216:219],v226 offset:36864
v_min_u32_dpp v32, v32, v32 row_bcast:15 row_mask:0xa bank_mask:0xf
EX v102,v102
MM a[48:51],a[144:147],v[16:19],a[48:51]
RD a[220:223],v227 offset:36864
v_min_u32_dpp v32, v32, v32 row_bcast:31 row_mask:0xc bank_mask:0xf
MM a[48:51],a[148:151],v[20:23],a[48:51]
s_mov_b32 exec_lo, 0
s_mov_b32 exec_hi, 0x80000000
ds_write_b32 v45, v32 offset:16
s_mov_b64 exec, -1
EX v103,v103
MM a[56:59],a[148:151],v[28:31],a[56:59]
EX v104,v104
MM a[56:59],a[144:147],v[24:27],a[56:59]
buffer_load_dwordx4 v229, s[8:11], s64 offen offset:1024 lds
MM a[60:63],a[152:155],v[24:27],a[60:63]
EX v105,v105
MM a[60:63],a[156:159],v[28:31],a[60:63]
EX v106,v106
MM a[52:55],a[156:159],v[20:23],a[52:55]
CV v71,v102,v103
CV v76,v104,v105
MM a[52:55],a[152:155],v[16:19],a[52:55]
EX v107,v107
MM a[44:47],a[152:155],v[8:11],a[44:47]
buffer_load_dwordx4 v230, s[8:11], s64 offen offset:2048 lds
MM a[44:47],a[156:159],v[12:15],a[44:47]
EX v108,v108
MM a[36:39],a[156:159],v[4:7],a[36:39]
EX v109,v109
MM a[36:39],a[152:155],v[0:3],a[36:39]
CV v77,v106,v107
CV v78,v108,v109
MM a[64:67],a[160:163],v[0:3],a[64:67]
EX v110,v110
MM a[64:67],a[164:167],v[4:7],a[64:67]
EX v111,v111
MM a[72:75],a[164:167],v[12:15],a[72:75]
buffer_load_dwordx4 v231, s[8:11], s64 offen offset:3072 lds
MM a[72:75],a[160:163],v[8:11],a[72:75]
s_add_u32 m0, s65, 65536
EX v112,v112
MM a[80:83],a[160:163],v[16:19],a[80:83]
RD a[224:227],v224 offset:40960
CV v79,v110,v111
MM a[80:83],a[164:167],v[20:23],a[80:83]
EX v113,v113
MM a[88:91],a[164:167],v[28:31],a[88:91]
EX v114,v114
MM a[88:91],a[160:163],v[24:27],a[88:91]
buffer_load_dwordx4 v235, s[12:15], s81 offen offset:0 lds
MM a[92:95],a[168:171],v[24:27],a[92:95]
EX v115,v115
MM a[92:95],a[172:175],v[28:31],a[92:95]
RD a[228:231],v225 offset:40960
CV v84,v112,v113
MM a[84:87],a[172:175],v[20:23],a[84:87]
EX v116,v116
MM a[84:87],a[168:171],v[16:19],a[84:87]
RD a[232:235],v226 offset:40960
CV v85,v114,v115
MM a[76:79],a[168:171],v[8:11],a[76:79]
EX v117,v117
MM a[76:79],a[172:175],v[12:15],a[76:79]
RD a[236:239],v227 offset:40960
CV v86,v116,v117
MM a[68:71],a[172:175],v[4:7],a[68:71]
EX v118,v118
MM a[68:71],a[168:171],v[0:3],a[68:71]
buffer_load_dwordx4 v235, s[12:15], s64 offen offset:1024 lds
MM a[96:99],a[176:179],v[0:3],a[96:99]
EX v119,v119
MM a[96:99],a[180:183],v[4:7],a[96:99]
EX v120,v120
MM a[104:107],a[180:183],v[12:15],a[104:107]
RD a[240:243],v224 offset:45056
CV v87,v118,v119
MM a[104:107],a[176:179],v[8:11],a[104:107]
EX v121,v121
MM a[112:115],a[176:179],v[16:19],a[112:115]
RD a[244:247],v225 offset:45056
CV v92,v120,v121
MM a[112:115],a[180:183],v[20:23],a[112:115]
EX v122,v122
MM a[120:123],a[180:183],v[28:31],a[120:123]
buffer_load_dwordx4 v235, s[12:15], s81 offen offset:2048 lds
MM a[120:123],a[176:179],v[24:27],a[120:123]
EX v123,v123
MM a[124:127],a[184:187],v[24:27],a[124:127]
RD a[248:251],v226 offset:45056
CV v93,v122,v123
MM a[124:127],a[188:191],v[28:31],a[124:127]
EX v124,v124
MM a[116:119],a[188:191],v[20:23],a[116:119]
EX v125,v125
MM a[116:119],a[184:187],v[16:19],a[116:119]
RD a[252:255],v227 offset:45056
CV v94,v124,v125
MM a[108:111],a[184:187],v[8:11],a[108:111]
EX v126,v126
MM a[108:111],a[188:191],v[12:15],a[108:111]
buffer_load_dwordx4 v235, s[12:15], s64 offen offset:3072 lds
MM a[100:103],a[188:191],v[4:7],a[100:103]
EX v127,v127
MM a[100:103],a[184:187],v[0:3],a[100:103]
CV v95,v126,v127
v_smfmac_f32_16x16x64_bf16 v[236:239], v[144:147], v[64:71], v234
v_smfmac_f32_16x16x64_bf16 v[236:239], v[148:151], v[72:79], v234
v_smfmac_f32_16x16x64_bf16 v[236:239], v[152:155], v[80:87], v234
v_smfmac_f32_16x16x64_bf16 v[236:239], v[156:159], v[88:95], v234
s_waitcnt lgkmcnt(0)
MM v[0:3],a[192:195],v[160:163],v[128:131]
s_add_u32 s64, s64, 16384
TR a[128:129],v232 offset:16384
s_add_u32 s81, s64, 896
MM v[0:3],a[196:199],v[176:179],v[0:3]
TR a[130:131],v232 offset:20480
TR a[132:133],v232 offset:24576
MM v[0:3],a[200:203],v[192:195],v[0:3]
TR a[134:135],v232 offset:28672
TR a[136:137],v233 offset:16384
MM v[0:3],a[204:207],v[208:211],v[0:3]
TR a[138:139],v233 offset:20480
v_add_f32 v247, v236, v237
MM v[4:7],a[220:223],v[208:211],v[128:131]
TR a[140:141],v233 offset:24576
v_cmp_nge_f32 vcc, s71, v247
MM v[4:7],a[216:219],v[192:195],v[4:7]
s_cbranch_vccnz LSITE5_%=
LBACK5_%=:
TR a[142:143],v233 offset:28672
TR a[144:145],v232 offset:16896
MM v[4:7],a[212:215],v[176:179],v[4:7]
s_mov_b32 m0, s67
TR a[146:147],v232 offset:20992
TR a[148:149],v232 offset:25088
MM v[4:7],a[208:211],v[160:163],v[4:7]
buffer_load_dwordx4 v252, s[36:39], s66 offen offset:0 lds
s_nop 0
LPG7_%=:
s_cbranch_execz LPG11_%=
MM v[12:15],a[208:211],v[164:167],v[132:135]
s_add_u32 s66, s66, 55296
EX v0,v0
MM v[12:15],a[212:215],v[180:183],v[12:15]
s_min_u32 s66, s66, s85
EX v1,v1
MM v[12:15],a[216:219],v[196:199],v[12:15]
s_add_u32 s67, s67, 1024
TR a[150:151],v232 offset:29184
CV v0,v0,v1
MM v[12:15],a[220:223],v[212:215],v[12:15]
s_min_u32 s67, s67, s86
EX v2,v2
MM v[8:11],a[204:207],v[212:215],v[132:135]
EX v3,v3
MM v[8:11],a[200:203],v[196:199],v[8:11]
TR a[152:153],v233 offset:16896
CV v1,v2,v3
MM v[8:11],a[196:199],v[180:183],v[8:11]
EX v4,v4
MM v[8:11],a[192:195],v[164:167],v[8:11]
EX v5,v5
MM v[16:19],a[192:195],v[168:171],v[136:139]
TR a[154:155],v233 offset:20992
CV v2,v4,v5
MM v[16:19],a[196:199],v[184:187],v[16:19]
EX v6,v6
MM v[16:19],a[200:203],v[200:203],v[16:19]
EX v7,v7
MM v[16:19],a[204:207],v[216:219],v[16:19]
TR a[156:157],v233 offset:25088
CV v3,v6,v7
MM v[20:23],a[220:223],v[216:219],v[136:139]
EX v8,v8
MM v[20:23],a[216:219],v[200:203],v[20:23]
TR a[158:159],v233 offset:29184
TR a[160:161],v232 offset:17408
MM v[20:23],a[212:215],v[184:187],v[20:23]
EX v9,v9
MM v[20:23],a[208:211],v[168:171],v[20:23]
TR a[162:163],v232 offset:21504
CV v8,v8,v9
MM v[28:31],a[208:211],v[172:175],v[140:143]
EX v10,v10
MM v[28:31],a[212:215],v[188:191],v[28:31]
TR a[164:165],v232 offset:25600
TR a[166:167],v232 offset:29696
MM v[28:31],a[216:219],v[204:207],v[28:31]
EX v11,v11
MM v[28:31],a[220:223],v[220:223],v[28:31]
TR a[168:169],v233 offset:17408
CV v9,v10,v11
MM v[24:27],a[204:207],v[220:223],v[140:143]
EX v12,v12
MM v[24:27],a[200:203],v[204:207],v[24:27]
EX v13,v13
MM v[24:27],a[196:199],v[188:191],v[24:27]
TR a[170:171],v233 offset:21504
CV v10,v12,v13
MM v[24:27],a[192:195],v[172:175],v[24:27]
EX v14,v14
MM v[32:35],a[224:227],v[160:163],v[128:131]
TR a[172:173],v233 offset:25600
TR a[174:175],v233 offset:29696
MM v[32:35],a[228:231],v[176:179],v[32:35]
EX v15,v15
MM v[32:35],a[232:235],v[192:195],v[32:35]
TR a[176:177],v232 offset:17920
CV v11,v14,v15
MM v[32:35],a[236:239],v[208:211],v[32:35]
EX v16,v16
MM v[36:39],a[252:255],v[208:211],v[128:131]
EX v17,v17
MM v[36:39],a[248:251],v[192:195],v[36:39]
TR a[178:179],v232 offset:22016
CV v16,v16,v17
MM v[36:39],a[244:247],v[176:179],v[36:39]
EX v18,v18
MM v[36:39],a[240:243],v[160:163],v[36:39]
TR a[180:181],v232 offset:26112
TR a[182:183],v232 offset:30208
MM v[44:47],a[240:243],v[164:167],v[132:135]
v_add_u32 v108, s78, v248
v_mov_b32 v109, s79
MM v[44:47],a[244:247],v[180:183],v[44:47]
EX v19,v19
MM v[44:47],a[248:251],v[196:199],v[44:47]
TR a[184:185],v233 offset:17920
CV v17,v18,v19
MM v[44:47],a[252:255],v[212:215],v[44:47]
EX v20,v20
MM v[40:43],a[236:239],v[212:215],v[132:135]
EX v21,v21
MM v[40:43],a[232:235],v[196:199],v[40:43]
TR a[186:187],v233 offset:22016
CV v18,v20,v21
MM v[40:43],a[228:231],v[180:183],v[40:43]
EX v22,v22
MM v[40:43],a[224:227],v[164:167],v[40:43]
TR a[188:189],v233 offset:26112
TR a[190:191],v233 offset:30208
MM v[48:51],a[224:227],v[168:171],v[136:139]
EX v23,v23
MM v[48:51],a[228:231],v[184:187],v[48:51]
CV v19,v22,v23
MM v[48:51],a[232:235],v[200:203],v[48:51]
EX v24,v24
MM v[48:51],a[236:239],v[216:219],v[48:51]
EX v25,v25
MM v[52:55],a[252:255],v[216:219],v[136:139]
EX v26,v26
MM v[52:55],a[248:251],v[200:203],v[52:55]
EX v27,v27
MM v[52:55],a[244:247],v[184:187],v[52:55]
EX v28,v28
MM v[52:55],a[240:243],v[168:171],v[52:55]
EX v29,v29
MM v[60:63],a[240:243],v[172:175],v[140:143]
EX v30,v30
MM v[60:63],a[244:247],v[188:191],v[60:63]
EX v31,v31
MM v[60:63],a[248:251],v[204:207],v[60:63]
EX v32,v32
MM v[60:63],a[252:255],v[220:223],v[60:63]
EX v33,v33
MM v[56:59],a[236:239],v[220:223],v[140:143]
EX v34,v34
MM v[56:59],a[232:235],v[204:207],v[56:59]
EX v35,v35
MM v[56:59],a[228:231],v[188:191],v[56:59]
EX v36,v36
MM v[56:59],a[224:227],v[172:175],v[56:59]
s_waitcnt vmcnt(13) lgkmcnt(0)
s_barrier
MM a[0:3],a[128:131],v[64:67],a[0:3]
s_add_u32 m0, s65, 16384
RD v[104:107],v109 offset:16
RD v[96:99],v108 offset:0
MM a[0:3],a[132:135],v[68:71],a[0:3]
RD v[100:103],v108 offset:1024
CV v24,v24,v25
MM a[8:11],a[132:135],v[76:79],a[8:11]
CV v25,v26,v27
CV v26,v28,v29
MM a[8:11],a[128:131],v[72:75],a[8:11]
RD a[192:195],v224 offset:49152
CV v27,v30,v31
MM a[16:19],a[128:131],v[80:83],a[16:19]
buffer_load_dwordx4 v228, s[8:11], s64 offen offset:0 lds
MM a[16:19],a[132:135],v[84:87],a[16:19]
RD a[196:199],v225 offset:49152
CV v4,v32,v33
MM a[24:27],a[132:135],v[92:95],a[24:27]
s_waitcnt lgkmcnt(2)
v_min3_u32 v104, v104, v105, v106
v_min_u32 v104, v104, v107
MM a[24:27],a[128:131],v[88:91],a[24:27]
v_readfirstlane_b32 s76, v104
v_add_u32 v109, s80, v109
MM a[28:31],a[136:139],v[88:91],a[28:31]
s_sub_u32 s77, s92, s76
RD a[200:203],v226 offset:49152
CV v5,v34,v35
MM a[28:31],a[140:143],v[92:95],a[28:31]
s_sub_u32 s92, s77, 1
v_sub_u32 v96, s92, v96
v_sub_u32 v97, s92, v97
MM a[20:23],a[140:143],v[84:87],a[20:23]
s_and_b32 s77, s77, 65535
v_sub_u32 v98, s92, v98
v_sub_u32 v99, s92, v99
MM a[20:23],a[136:139],v[80:83],a[20:23]
s_mov_b32 s52, s56
v_sub_u32 v100, s92, v100
v_sub_u32 v101, s92, v101
MM a[12:15],a[136:139],v[72:75],a[12:15]
s_sub_u32 s56, 65535, s77
v_sub_u32 v102, s92, v102
v_sub_u32 v103, s92, v103
MM a[12:15],a[140:143],v[76:79],a[12:15]
v_min3_u32 v96, v96, v97, v98
v_min3_u32 v99, v99, v100, v101
MM a[4:7],a[140:143],v[68:71],a[4:7]
v_min3_u32 v96, v96, v102, v103
v_min_u32 v96, v96, v99
MM a[4:7],a[136:139],v[64:67],a[4:7]
RD a[204:207],v227 offset:49152
v_min_u32_dpp v96, v96, v96 row_shr:1 row_mask:0xf bank_mask:0xf
)ASM"
R"ASM(
MM a[32:35],a[144:147],v[64:67],a[32:35]
RD a[208:211],v224 offset:53248
v_min_u32_dpp v96, v96, v96 row_shr:2 row_mask:0xf bank_mask:0xf
MM a[32:35],a[148:151],v[68:71],a[32:35]
RD a[212:215],v225 offset:53248
v_min_u32_dpp v96, v96, v96 row_shr:4 row_mask:0xf bank_mask:0xf
EX v37,v37
MM a[40:43],a[148:151],v[76:79],a[40:43]
v_min_u32_dpp v96, v96, v96 row_shr:8 row_mask:0xf bank_mask:0xf
CV v6,v36,v37
MM a[40:43],a[144:147],v[72:75],a[40:43]
RD a[216:219],v226 offset:53248
v_min_u32_dpp v96, v96, v96 row_bcast:15 row_mask:0xa bank_mask:0xf
EX v38,v38
MM a[48:51],a[144:147],v[80:83],a[48:51]
RD a[220:223],v227 offset:53248
v_min_u32_dpp v96, v96, v96 row_bcast:31 row_mask:0xc bank_mask:0xf
MM a[48:51],a[148:151],v[84:87],a[48:51]
s_mov_b32 exec_lo, 0
s_mov_b32 exec_hi, 0x80000000
ds_write_b32 v109, v96 offset:0
s_mov_b64 exec, -1
EX v39,v39
MM a[56:59],a[148:151],v[92:95],a[56:59]
EX v40,v40
MM a[56:59],a[144:147],v[88:91],a[56:59]
buffer_load_dwordx4 v229, s[8:11], s64 offen offset:1024 lds
MM a[60:63],a[152:155],v[88:91],a[60:63]
EX v41,v41
MM a[60:63],a[156:159],v[92:95],a[60:63]
EX v42,v42
MM a[52:55],a[156:159],v[84:87],a[52:55]
CV v7,v38,v39
CV v12,v40,v41
MM a[52:55],a[152:155],v[80:83],a[52:55]
EX v43,v43
MM a[44:47],a[152:155],v[72:75],a[44:47]
buffer_load_dwordx4 v230, s[8:11], s64 offen offset:2048 lds
MM a[44:47],a[156:159],v[76:79],a[44:47]
EX v44,v44
MM a[36:39],a[156:159],v[68:71],a[36:39]
EX v45,v45
MM a[36:39],a[152:155],v[64:67],a[36:39]
CV v13,v42,v43
CV v14,v44,v45
MM a[64:67],a[160:163],v[64:67],a[64:67]
EX v46,v46
MM a[64:67],a[164:167],v[68:71],a[64:67]
EX v47,v47
MM a[72:75],a[164:167],v[76:79],a[72:75]
buffer_load_dwordx4 v231, s[8:11], s64 offen offset:3072 lds
MM a[72:75],a[160:163],v[72:75],a[72:75]
s_add_u32 m0, s65, 81920
EX v48,v48
MM a[80:83],a[160:163],v[80:83],a[80:83]
RD a[224:227],v224 offset:57344
CV v15,v46,v47
MM a[80:83],a[164:167],v[84:87],a[80:83]
EX v49,v49
MM a[88:91],a[164:167],v[92:95],a[88:91]
EX v50,v50
MM a[88:91],a[160:163],v[88:91],a[88:91]
buffer_load_dwordx4 v235, s[12:15], s81 offen offset:0 lds
MM a[92:95],a[168:171],v[88:91],a[92:95]
EX v51,v51
MM a[92:95],a[172:175],v[92:95],a[92:95]
RD a[228:231],v225 offset:57344
CV v20,v48,v49
MM a[84:87],a[172:175],v[84:87],a[84:87]
EX v52,v52
MM a[84:87],a[168:171],v[80:83],a[84:87]
RD a[232:235],v226 offset:57344
CV v21,v50,v51
MM a[76:79],a[168:171],v[72:75],a[76:79]
EX v53,v53
MM a[76:79],a[172:175],v[76:79],a[76:79]
RD a[236:239],v227 offset:57344
CV v22,v52,v53
MM a[68:71],a[172:175],v[68:71],a[68:71]
EX v54,v54
MM a[68:71],a[168:171],v[64:67],a[68:71]
buffer_load_dwordx4 v235, s[12:15], s64 offen offset:1024 lds
MM a[96:99],a[176:179],v[64:67],a[96:99]
EX v55,v55
MM a[96:99],a[180:183],v[68:71],a[96:99]
EX v56,v56
MM a[104:107],a[180:183],v[76:79],a[104:107]
RD a[240:243],v224 offset:61440
CV v23,v54,v55
MM a[104:107],a[176:179],v[72:75],a[104:107]
EX v57,v57
MM a[112:115],a[176:179],v[80:83],a[112:115]
RD a[244:247],v225 offset:61440
CV v28,v56,v57
MM a[112:115],a[180:183],v[84:87],a[112:115]
EX v58,v58
MM a[120:123],a[180:183],v[92:95],a[120:123]
buffer_load_dwordx4 v235, s[12:15], s81 offen offset:2048 lds
MM a[120:123],a[176:179],v[88:91],a[120:123]
EX v59,v59
MM a[124:127],a[184:187],v[88:91],a[124:127]
RD a[248:251],v226 offset:61440
CV v29,v58,v59
MM a[124:127],a[188:191],v[92:95],a[124:127]
EX v60,v60
MM a[116:119],a[188:191],v[84:87],a[116:119]
EX v61,v61
MM a[116:119],a[184:187],v[80:83],a[116:119]
RD a[252:255],v227 offset:61440
CV v30,v60,v61
MM a[108:111],a[184:187],v[72:75],a[108:111]
EX v62,v62
MM a[108:111],a[188:191],v[76:79],a[108:111]
buffer_load_dwordx4 v235, s[12:15], s64 offen offset:3072 lds
MM a[100:103],a[188:191],v[68:71],a[100:103]
EX v63,v63
MM a[100:103],a[184:187],v[64:67],a[100:103]
CV v31,v62,v63
v_smfmac_f32_16x16x64_bf16 v[236:239], v[144:147], v[0:7], v234
v_smfmac_f32_16x16x64_bf16 v[236:239], v[148:151], v[8:15], v234
v_smfmac_f32_16x16x64_bf16 v[236:239], v[152:155], v[16:23], v234
v_smfmac_f32_16x16x64_bf16 v[236:239], v[156:159], v[24:31], v234
s_waitcnt lgkmcnt(0)
MM v[64:67],a[192:195],v[160:163],v[128:131]
s_add_u32 s64, s64, 16384
TR a[128:129],v232 offset:32768
s_add_u32 s81, s64, 896
MM v[64:67],a[196:199],v[176:179],v[64:67]
TR a[130:131],v232 offset:36864
TR a[132:133],v232 offset:40960
MM v[64:67],a[200:203],v[192:195],v[64:67]
TR a[134:135],v232 offset:45056
TR a[136:137],v233 offset:32768
MM v[64:67],a[204:207],v[208:211],v[64:67]
TR a[138:139],v233 offset:36864
v_add_f32 v246, v236, v237
MM v[68:71],a[220:223],v[208:211],v[128:131]
TR a[140:141],v233 offset:40960
v_cmp_nge_f32 vcc, s71, v246
MM v[68:71],a[216:219],v[192:195],v[68:71]
s_cbranch_vccnz LSITE6_%=
LBACK6_%=:
TR a[142:143],v233 offset:45056
TR a[144:145],v232 offset:33280
MM v[68:71],a[212:215],v[176:179],v[68:71]
s_mov_b32 m0, s67
TR a[146:147],v232 offset:37376
TR a[148:149],v232 offset:41472
MM v[68:71],a[208:211],v[160:163],v[68:71]
buffer_load_dwordx4 v252, s[36:39], s66 offen offset:0 lds
MM v[76:79],a[208:211],v[164:167],v[132:135]
s_add_u32 s66, s66, 55296
EX v64,v64
MM v[76:79],a[212:215],v[180:183],v[76:79]
s_min_u32 s66, s66, s85
EX v65,v65
MM v[76:79],a[216:219],v[196:199],v[76:79]
s_add_u32 s67, s67, 1024
TR a[150:151],v232 offset:45568
CV v64,v64,v65
MM v[76:79],a[220:223],v[212:215],v[76:79]
s_min_u32 s67, s67, s86
EX v66,v66
MM v[72:75],a[204:207],v[212:215],v[132:135]
EX v67,v67
MM v[72:75],a[200:203],v[196:199],v[72:75]
TR a[152:153],v233 offset:33280
CV v65,v66,v67
MM v[72:75],a[196:199],v[180:183],v[72:75]
EX v68,v68
MM v[72:75],a[192:195],v[164:167],v[72:75]
EX v69,v69
MM v[80:83],a[192:195],v[168:171],v[136:139]
TR a[154:155],v233 offset:37376
CV v66,v68,v69
MM v[80:83],a[196:199],v[184:187],v[80:83]
EX v70,v70
MM v[80:83],a[200:203],v[200:203],v[80:83]
EX v71,v71
MM v[80:83],a[204:207],v[216:219],v[80:83]
TR a[156:157],v233 offset:41472
CV v67,v70,v71
MM v[84:87],a[220:223],v[216:219],v[136:139]
EX v72,v72
MM v[84:87],a[216:219],v[200:203],v[84:87]
TR a[158:159],v233 offset:45568
TR a[160:161],v232 offset:33792
MM v[84:87],a[212:215],v[184:187],v[84:87]
EX v73,v73
MM v[84:87],a[208:211],v[168:171],v[84:87]
TR a[162:163],v232 offset:37888
CV v72,v72,v73
MM v[92:95],a[208:211],v[172:175],v[140:143]
EX v74,v74
MM v[92:95],a[212:215],v[188:191],v[92:95]
TR a[164:165],v232 offset:41984
TR a[166:167],v232 offset:46080
MM v[92:95],a[216:219],v[204:207],v[92:95]
EX v75,v75
MM v[92:95],a[220:223],v[220:223],v[92:95]
TR a[168:169],v233 offset:33792
CV v73,v74,v75
MM v[88:91],a[204:207],v[220:223],v[140:143]
EX v76,v76
MM v[88:91],a[200:203],v[204:207],v[88:91]
EX v77,v77
MM v[88:91],a[196:199],v[188:191],v[88:91]
TR a[170:171],v233 offset:37888
CV v74,v76,v77
MM v[88:91],a[192:195],v[172:175],v[88:91]
EX v78,v78
MM v[96:99],a[224:227],v[160:163],v[128:131]
TR a[172:173],v233 offset:41984
TR a[174:175],v233 offset:46080
MM v[96:99],a[228:231],v[176:179],v[96:99]
EX v79,v79
MM v[96:99],a[232:235],v[192:195],v[96:99]
TR a[176:177],v232 offset:34304
CV v75,v78,v79
MM v[96:99],a[236:239],v[208:211],v[96:99]
EX v80,v80
MM v[100:103],a[252:255],v[208:211],v[128:131]
EX v81,v81
MM v[100:103],a[248:251],v[192:195],v[100:103]
TR a[178:179],v232 offset:38400
CV v80,v80,v81
MM v[100:103],a[244:247],v[176:179],v[100:103]
EX v82,v82
MM v[100:103],a[240:243],v[160:163],v[100:103]
TR a[180:181],v232 offset:42496
TR a[182:183],v232 offset:46592
MM v[108:111],a[240:243],v[164:167],v[132:135]
v_add_u32 v44, s78, v248
v_mov_b32 v45, s79
MM v[108:111],a[244:247],v[180:183],v[108:111]
EX v83,v83
MM v[108:111],a[248:251],v[196:199],v[108:111]
TR a[184:185],v233 offset:34304
CV v81,v82,v83
MM v[108:111],a[252:255],v[212:215],v[108:111]
EX v84,v84
MM v[104:107],a[236:239],v[212:215],v[132:135]
EX v85,v85
MM v[104:107],a[232:235],v[196:199],v[104:107]
TR a[186:187],v233 offset:38400
CV v82,v84,v85
MM v[104:107],a[228:231],v[180:183],v[104:107]
EX v86,v86
MM v[104:107],a[224:227],v[164:167],v[104:107]
TR a[188:189],v233 offset:42496
TR a[190:191],v233 offset:46592
MM v[112:115],a[224:227],v[168:171],v[136:139]
EX v87,v87
MM v[112:115],a[228:231],v[184:187],v[112:115]
CV v83,v86,v87
MM v[112:115],a[232:235],v[200:203],v[112:115]
EX v88,v88
MM v[112:115],a[236:239],v[216:219],v[112:115]
EX v89,v89
MM v[116:119],a[252:255],v[216:219],v[136:139]
EX v90,v90
MM v[116:119],a[248:251],v[200:203],v[116:119]
EX v91,v91
MM v[116:119],a[244:247],v[184:187],v[116:119]
EX v92,v92
MM v[116:119],a[240:243],v[168:171],v[116:119]
EX v93,v93
MM v[124:127],a[240:243],v[172:175],v[140:143]
EX v94,v94
MM v[124:127],a[244:247],v[188:191],v[124:127]
EX v95,v95
MM v[124:127],a[248:251],v[204:207],v[124:127]
EX v96,v96
MM v[124:127],a[252:255],v[220:223],v[124:127]
EX v97,v97
MM v[120:123],a[236:239],v[220:223],v[140:143]
EX v98,v98
MM v[120:123],a[232:235],v[204:207],v[120:123]
EX v99,v99
MM v[120:123],a[228:231],v[188:191],v[120:123]
EX v100,v100
MM v[120:123],a[224:227],v[172:175],v[120:123]
s_waitcnt vmcnt(13) lgkmcnt(0)
s_barrier
MM a[0:3],a[128:131],v[0:3],a[0:3]
s_add_u32 m0, s65, 32768
RD v[40:43],v45 offset:0
RD v[32:35],v44 offset:0
MM a[0:3],a[132:135],v[4:7],a[0:3]
RD v[36:39],v44 offset:1024
CV v88,v88,v89
MM a[8:11],a[132:135],v[12:15],a[8:11]
CV v89,v90,v91
CV v90,v92,v93
MM a[8:11],a[128:131],v[8:11],a[8:11]
RD a[192:195],v224 offset:0
CV v91,v94,v95
MM a[16:19],a[128:131],v[16:19],a[16:19]
buffer_load_dwordx4 v228, s[8:11], s64 offen offset:0 lds
MM a[16:19],a[132:135],v[20:23],a[16:19]
RD a[196:199],v225 offset:0
CV v68,v96,v97
MM a[24:27],a[132:135],v[28:31],a[24:27]
s_waitcnt lgkmcnt(2)
v_min3_u32 v40, v40, v41, v42
v_min_u32 v40, v40, v43
MM a[24:27],a[128:131],v[24:27],a[24:27]
v_readfirstlane_b32 s76, v40
v_add_u32 v45, s80, v45
MM a[28:31],a[136:139],v[24:27],a[28:31]
s_sub_u32 s77, s92, s76
RD a[200:203],v226 offset:0
CV v69,v98,v99
MM a[28:31],a[140:143],v[28:31],a[28:31]
s_sub_u32 s92, s77, 1
v_sub_u32 v32, s92, v32
v_sub_u32 v33, s92, v33
MM a[20:23],a[140:143],v[20:23],a[20:23]
s_and_b32 s77, s77, 65535
v_sub_u32 v34, s92, v34
v_sub_u32 v35, s92, v35
MM a[20:23],a[136:139],v[16:19],a[20:23]
s_mov_b32 s53, s57
v_sub_u32 v36, s92, v36
v_sub_u32 v37, s92, v37
MM a[12:15],a[136:139],v[8:11],a[12:15]
s_sub_u32 s57, 65535, s77
v_sub_u32 v38, s92, v38
v_sub_u32 v39, s92, v39
MM a[12:15],a[140:143],v[12:15],a[12:15]
v_min3_u32 v32, v32, v33, v34
v_min3_u32 v35, v35, v36, v37
MM a[4:7],a[140:143],v[4:7],a[4:7]
v_min3_u32 v32, v32, v38, v39
v_min_u32 v32, v32, v35
MM a[4:7],a[136:139],v[0:3],a[4:7]
RD a[204:207],v227 offset:0
v_min_u32_dpp v32, v32, v32 row_shr:1 row_mask:0xf bank_mask:0xf
MM a[32:35],a[144:147],v[0:3],a[32:35]
RD a[208:211],v224 offset:4096
v_min_u32_dpp v32, v32, v32 row_shr:2 row_mask:0xf bank_mask:0xf
MM a[32:35],a[148:151],v[4:7],a[32:35]
RD a[212:215],v225 offset:4096
v_min_u32_dpp v32, v32, v32 row_shr:4 row_mask:0xf bank_mask:0xf
EX v101,v101
MM a[40:43],a[148:151],v[12:15],a[40:43]
v_min_u32_dpp v32, v32, v32 row_shr:8 row_mask:0xf bank_mask:0xf
CV v70,v100,v101
MM a[40:43],a[144:147],v[8:11],a[40:43]
RD a[216:219],v226 offset:4096
v_min_u32_dpp v32, v32, v32 row_bcast:15 row_mask:0xa bank_mask:0xf
EX v102,v102
MM a[48:51],a[144:147],v[16:19],a[48:51]
RD a[220:223],v227 offset:4096
v_min_u32_dpp v32, v32, v32 row_bcast:31 row_mask:0xc bank_mask:0xf
MM a[48:51],a[148:151],v[20:23],a[48:51]
s_mov_b32 exec_lo, 0
s_mov_b32 exec_hi, 0x80000000
ds_write_b32 v45, v32 offset:16
s_mov_b64 exec, -1
EX v103,v103
MM a[56:59],a[148:151],v[28:31],a[56:59]
EX v104,v104
MM a[56:59],a[144:147],v[24:27],a[56:59]
buffer_load_dwordx4 v229, s[8:11], s64 offen offset:1024 lds
MM a[60:63],a[152:155],v[24:27],a[60:63]
EX v105,v105
MM a[60:63],a[156:159],v[28:31],a[60:63]
EX v106,v106
MM a[52:55],a[156:159],v[20:23],a[52:55]
CV v71,v102,v103
CV v76,v104,v105
MM a[52:55],a[152:155],v[16:19],a[52:55]
EX v107,v107
MM a[44:47],a[152:155],v[8:11],a[44:47]
buffer_load_dwordx4 v230, s[8:11], s64 offen offset:2048 lds
MM a[44:47],a[156:159],v[12:15],a[44:47]
EX v108,v108
MM a[36:39],a[156:159],v[4:7],a[36:39]
)ASM"
R"ASM(
EX v109,v109
MM a[36:39],a[152:155],v[0:3],a[36:39]
CV v77,v106,v107
CV v78,v108,v109
MM a[64:67],a[160:163],v[0:3],a[64:67]
EX v110,v110
MM a[64:67],a[164:167],v[4:7],a[64:67]
EX v111,v111
MM a[72:75],a[164:167],v[12:15],a[72:75]
buffer_load_dwordx4 v231, s[8:11], s64 offen offset:3072 lds
MM a[72:75],a[160:163],v[8:11],a[72:75]
s_add_u32 m0, s65, 98304
EX v112,v112
MM a[80:83],a[160:163],v[16:19],a[80:83]
RD a[224:227],v224 offset:8192
CV v79,v110,v111
MM a[80:83],a[164:167],v[20:23],a[80:83]
EX v113,v113
MM a[88:91],a[164:167],v[28:31],a[88:91]
EX v114,v114
MM a[88:91],a[160:163],v[24:27],a[88:91]
buffer_load_dwordx4 v235, s[12:15], s81 offen offset:0 lds
MM a[92:95],a[168:171],v[24:27],a[92:95]
EX v115,v115
MM a[92:95],a[172:175],v[28:31],a[92:95]
RD a[228:231],v225 offset:8192
CV v84,v112,v113
MM a[84:87],a[172:175],v[20:23],a[84:87]
EX v116,v116
MM a[84:87],a[168:171],v[16:19],a[84:87]
s_nop 0
LPG8_%=:
s_cbranch_execz LPG12_%=
RD a[232:235],v226 offset:8192
CV v85,v114,v115
MM a[76:79],a[168:171],v[8:11],a[76:79]
EX v117,v117
MM a[76:79],a[172:175],v[12:15],a[76:79]
RD a[236:239],v227 offset:8192
CV v86,v116,v117
MM a[68:71],a[172:175],v[4:7],a[68:71]
EX v118,v118
MM a[68:71],a[168:171],v[0:3],a[68:71]
buffer_load_dwordx4 v235, s[12:15], s64 offen offset:1024 lds
MM a[96:99],a[176:179],v[0:3],a[96:99]
EX v119,v119
MM a[96:99],a[180:183],v[4:7],a[96:99]
EX v120,v120
MM a[104:107],a[180:183],v[12:15],a[104:107]
RD a[240:243],v224 offset:12288
CV v87,v118,v119
MM a[104:107],a[176:179],v[8:11],a[104:107]
EX v121,v121
MM a[112:115],a[176:179],v[16:19],a[112:115]
RD a[244:247],v225 offset:12288
CV v92,v120,v121
MM a[112:115],a[180:183],v[20:23],a[112:115]
EX v122,v122
MM a[120:123],a[180:183],v[28:31],a[120:123]
buffer_load_dwordx4 v235, s[12:15], s81 offen offset:2048 lds
MM a[120:123],a[176:179],v[24:27],a[120:123]
EX v123,v123
MM a[124:127],a[184:187],v[24:27],a[124:127]
RD a[248:251],v226 offset:12288
CV v93,v122,v123
MM a[124:127],a[188:191],v[28:31],a[124:127]
EX v124,v124
MM a[116:119],a[188:191],v[20:23],a[116:119]
EX v125,v125
MM a[116:119],a[184:187],v[16:19],a[116:119]
RD a[252:255],v227 offset:12288
CV v94,v124,v125
MM a[108:111],a[184:187],v[8:11],a[108:111]
EX v126,v126
MM a[108:111],a[188:191],v[12:15],a[108:111]
buffer_load_dwordx4 v235, s[12:15], s64 offen offset:3072 lds
MM a[100:103],a[188:191],v[4:7],a[100:103]
EX v127,v127
MM a[100:103],a[184:187],v[0:3],a[100:103]
CV v95,v126,v127
v_smfmac_f32_16x16x64_bf16 v[236:239], v[144:147], v[64:71], v234
v_smfmac_f32_16x16x64_bf16 v[236:239], v[148:151], v[72:79], v234
v_smfmac_f32_16x16x64_bf16 v[236:239], v[152:155], v[80:87], v234
v_smfmac_f32_16x16x64_bf16 v[236:239], v[156:159], v[88:95], v234
s_waitcnt lgkmcnt(0)
MM v[0:3],a[192:195],v[160:163],v[128:131]
s_add_u32 s64, s64, 16384
TR a[128:129],v232 offset:49152
s_add_u32 s81, s64, 896
MM v[0:3],a[196:199],v[176:179],v[0:3]
TR a[130:131],v232 offset:53248
TR a[132:133],v232 offset:57344
MM v[0:3],a[200:203],v[192:195],v[0:3]
TR a[134:135],v232 offset:61440
TR a[136:137],v233 offset:49152
MM v[0:3],a[204:207],v[208:211],v[0:3]
TR a[138:139],v233 offset:53248
v_add_f32 v247, v236, v237
MM v[4:7],a[220:223],v[208:211],v[128:131]
TR a[140:141],v233 offset:57344
v_cmp_nge_f32 vcc, s71, v247
MM v[4:7],a[216:219],v[192:195],v[4:7]
s_cbranch_vccnz LSITE7_%=
LBACK7_%=:
TR a[142:143],v233 offset:61440
TR a[144:145],v232 offset:49664
MM v[4:7],a[212:215],v[176:179],v[4:7]
s_mov_b32 m0, s67
TR a[146:147],v232 offset:53760
TR a[148:149],v232 offset:57856
MM v[4:7],a[208:211],v[160:163],v[4:7]
buffer_load_dwordx4 v252, s[36:39], s66 offen offset:0 lds
MM v[12:15],a[208:211],v[164:167],v[132:135]
s_add_u32 s66, s66, 55296
EX v0,v0
MM v[12:15],a[212:215],v[180:183],v[12:15]
s_min_u32 s66, s66, s85
EX v1,v1
MM v[12:15],a[216:219],v[196:199],v[12:15]
s_add_u32 s67, s67, 1024
TR a[150:151],v232 offset:61952
CV v0,v0,v1
MM v[12:15],a[220:223],v[212:215],v[12:15]
s_min_u32 s67, s67, s86
EX v2,v2
MM v[8:11],a[204:207],v[212:215],v[132:135]
EX v3,v3
MM v[8:11],a[200:203],v[196:199],v[8:11]
TR a[152:153],v233 offset:49664
CV v1,v2,v3
MM v[8:11],a[196:199],v[180:183],v[8:11]
EX v4,v4
MM v[8:11],a[192:195],v[164:167],v[8:11]
EX v5,v5
MM v[16:19],a[192:195],v[168:171],v[136:139]
TR a[154:155],v233 offset:53760
CV v2,v4,v5
MM v[16:19],a[196:199],v[184:187],v[16:19]
EX v6,v6
MM v[16:19],a[200:203],v[200:203],v[16:19]
EX v7,v7
MM v[16:19],a[204:207],v[216:219],v[16:19]
TR a[156:157],v233 offset:57856
CV v3,v6,v7
MM v[20:23],a[220:223],v[216:219],v[136:139]
EX v8,v8
MM v[20:23],a[216:219],v[200:203],v[20:23]
TR a[158:159],v233 offset:61952
TR a[160:161],v232 offset:50176
MM v[20:23],a[212:215],v[184:187],v[20:23]
EX v9,v9
MM v[20:23],a[208:211],v[168:171],v[20:23]
TR a[162:163],v232 offset:54272
CV v8,v8,v9
MM v[28:31],a[208:211],v[172:175],v[140:143]
EX v10,v10
MM v[28:31],a[212:215],v[188:191],v[28:31]
TR a[164:165],v232 offset:58368
TR a[166:167],v232 offset:62464
MM v[28:31],a[216:219],v[204:207],v[28:31]
EX v11,v11
MM v[28:31],a[220:223],v[220:223],v[28:31]
TR a[168:169],v233 offset:50176
CV v9,v10,v11
MM v[24:27],a[204:207],v[220:223],v[140:143]
EX v12,v12
MM v[24:27],a[200:203],v[204:207],v[24:27]
EX v13,v13
MM v[24:27],a[196:199],v[188:191],v[24:27]
TR a[170:171],v233 offset:54272
CV v10,v12,v13
MM v[24:27],a[192:195],v[172:175],v[24:27]
EX v14,v14
MM v[32:35],a[224:227],v[160:163],v[128:131]
TR a[172:173],v233 offset:58368
TR a[174:175],v233 offset:62464
MM v[32:35],a[228:231],v[176:179],v[32:35]
EX v15,v15
MM v[32:35],a[232:235],v[192:195],v[32:35]
TR a[176:177],v232 offset:50688
CV v11,v14,v15
MM v[32:35],a[236:239],v[208:211],v[32:35]
EX v16,v16
MM v[36:39],a[252:255],v[208:211],v[128:131]
EX v17,v17
MM v[36:39],a[248:251],v[192:195],v[36:39]
TR a[178:179],v232 offset:54784
CV v16,v16,v17
MM v[36:39],a[244:247],v[176:179],v[36:39]
EX v18,v18
MM v[36:39],a[240:243],v[160:163],v[36:39]
TR a[180:181],v232 offset:58880
TR a[182:183],v232 offset:62976
MM v[44:47],a[240:243],v[164:167],v[132:135]
v_add_u32 v108, s78, v248
v_mov_b32 v109, s79
MM v[44:47],a[244:247],v[180:183],v[44:47]
EX v19,v19
MM v[44:47],a[248:251],v[196:199],v[44:47]
TR a[184:185],v233 offset:50688
CV v17,v18,v19
MM v[44:47],a[252:255],v[212:215],v[44:47]
EX v20,v20
MM v[40:43],a[236:239],v[212:215],v[132:135]
EX v21,v21
MM v[40:43],a[232:235],v[196:199],v[40:43]
TR a[186:187],v233 offset:54784
CV v18,v20,v21
MM v[40:43],a[228:231],v[180:183],v[40:43]
EX v22,v22
MM v[40:43],a[224:227],v[164:167],v[40:43]
TR a[188:189],v233 offset:58880
TR a[190:191],v233 offset:62976
MM v[48:51],a[224:227],v[168:171],v[136:139]
EX v23,v23
MM v[48:51],a[228:231],v[184:187],v[48:51]
CV v19,v22,v23
MM v[48:51],a[232:235],v[200:203],v[48:51]
EX v24,v24
MM v[48:51],a[236:239],v[216:219],v[48:51]
EX v25,v25
MM v[52:55],a[252:255],v[216:219],v[136:139]
EX v26,v26
MM v[52:55],a[248:251],v[200:203],v[52:55]
EX v27,v27
MM v[52:55],a[244:247],v[184:187],v[52:55]
EX v28,v28
MM v[52:55],a[240:243],v[168:171],v[52:55]
EX v29,v29
MM v[60:63],a[240:243],v[172:175],v[140:143]
EX v30,v30
MM v[60:63],a[244:247],v[188:191],v[60:63]
EX v31,v31
MM v[60:63],a[248:251],v[204:207],v[60:63]
EX v32,v32
MM v[60:63],a[252:255],v[220:223],v[60:63]
EX v33,v33
MM v[56:59],a[236:239],v[220:223],v[140:143]
EX v34,v34
MM v[56:59],a[232:235],v[204:207],v[56:59]
EX v35,v35
MM v[56:59],a[228:231],v[188:191],v[56:59]
EX v36,v36
MM v[56:59],a[224:227],v[172:175],v[56:59]
s_waitcnt vmcnt(13) lgkmcnt(0)
s_barrier
MM a[0:3],a[128:131],v[64:67],a[0:3]
s_add_u32 m0, s65, 49152
RD v[104:107],v109 offset:16
RD v[96:99],v108 offset:0
MM a[0:3],a[132:135],v[68:71],a[0:3]
RD v[100:103],v108 offset:1024
CV v24,v24,v25
MM a[8:11],a[132:135],v[76:79],a[8:11]
CV v25,v26,v27
CV v26,v28,v29
MM a[8:11],a[128:131],v[72:75],a[8:11]
RD a[192:195],v224 offset:16384
CV v27,v30,v31
MM a[16:19],a[128:131],v[80:83],a[16:19]
buffer_load_dwordx4 v228, s[8:11], s64 offen offset:0 lds
MM a[16:19],a[132:135],v[84:87],a[16:19]
RD a[196:199],v225 offset:16384
CV v4,v32,v33
MM a[24:27],a[132:135],v[92:95],a[24:27]
s_waitcnt lgkmcnt(2)
v_min3_u32 v104, v104, v105, v106
v_min_u32 v104, v104, v107
MM a[24:27],a[128:131],v[88:91],a[24:27]
v_readfirstlane_b32 s76, v104
v_add_u32 v109, s80, v109
MM a[28:31],a[136:139],v[88:91],a[28:31]
s_sub_u32 s77, s92, s76
RD a[200:203],v226 offset:16384
CV v5,v34,v35
MM a[28:31],a[140:143],v[92:95],a[28:31]
s_sub_u32 s92, s77, 1
v_sub_u32 v96, s92, v96
v_sub_u32 v97, s92, v97
MM a[20:23],a[140:143],v[84:87],a[20:23]
s_and_b32 s77, s77, 65535
v_sub_u32 v98, s92, v98
v_sub_u32 v99, s92, v99
MM a[20:23],a[136:139],v[80:83],a[20:23]
s_mov_b32 s54, s58
v_sub_u32 v100, s92, v100
v_sub_u32 v101, s92, v101
MM a[12:15],a[136:139],v[72:75],a[12:15]
s_sub_u32 s58, 65535, s77
v_sub_u32 v102, s92, v102
v_sub_u32 v103, s92, v103
MM a[12:15],a[140:143],v[76:79],a[12:15]
v_min3_u32 v96, v96, v97, v98
v_min3_u32 v99, v99, v100, v101
MM a[4:7],a[140:143],v[68:71],a[4:7]
v_min3_u32 v96, v96, v102, v103
v_min_u32 v96, v96, v99
MM a[4:7],a[136:139],v[64:67],a[4:7]
RD a[204:207],v227 offset:16384
v_min_u32_dpp v96, v96, v96 row_shr:1 row_mask:0xf bank_mask:0xf
MM a[32:35],a[144:147],v[64:67],a[32:35]
RD a[208:211],v224 offset:20480
v_min_u32_dpp v96, v96, v96 row_shr:2 row_mask:0xf bank_mask:0xf
MM a[32:35],a[148:151],v[68:71],a[32:35]
RD a[212:215],v225 offset:20480
v_min_u32_dpp v96, v96, v96 row_shr:4 row_mask:0xf bank_mask:0xf
EX v37,v37
MM a[40:43],a[148:151],v[76:79],a[40:43]
v_min_u32_dpp v96, v96, v96 row_shr:8 row_mask:0xf bank_mask:0xf
CV v6,v36,v37
MM a[40:43],a[144:147],v[72:75],a[40:43]
RD a[216:219],v226 offset:20480
v_min_u32_dpp v96, v96, v96 row_bcast:15 row_mask:0xa bank_mask:0xf
EX v38,v38
MM a[48:51],a[144:147],v[80:83],a[48:51]
RD a[220:223],v227 offset:20480
v_min_u32_dpp v96, v96, v96 row_bcast:31 row_mask:0xc bank_mask:0xf
MM a[48:51],a[148:151],v[84:87],a[48:51]
s_mov_b32 exec_lo, 0
s_mov_b32 exec_hi, 0x80000000
ds_write_b32 v109, v96 offset:0
s_mov_b64 exec, -1
EX v39,v39
MM a[56:59],a[148:151],v[92:95],a[56:59]
EX v40,v40
MM a[56:59],a[144:147],v[88:91],a[56:59]
buffer_load_dwordx4 v229, s[8:11], s64 offen offset:1024 lds
MM a[60:63],a[152:155],v[88:91],a[60:63]
EX v41,v41
MM a[60:63],a[156:159],v[92:95],a[60:63]
EX v42,v42
MM a[52:55],a[156:159],v[84:87],a[52:55]
CV v7,v38,v39
CV v12,v40,v41
MM a[52:55],a[152:155],v[80:83],a[52:55]
EX v43,v43
MM a[44:47],a[152:155],v[72:75],a[44:47]
buffer_load_dwordx4 v230, s[8:11], s64 offen offset:2048 lds
MM a[44:47],a[156:159],v[76:79],a[44:47]
EX v44,v44
MM a[36:39],a[156:159],v[68:71],a[36:39]
EX v45,v45
MM a[36:39],a[152:155],v[64:67],a[36:39]
CV v13,v42,v43
CV v14,v44,v45
MM a[64:67],a[160:163],v[64:67],a[64:67]
EX v46,v46
MM a[64:67],a[164:167],v[68:71],a[64:67]
EX v47,v47
MM a[72:75],a[164:167],v[76:79],a[72:75]
buffer_load_dwordx4 v231, s[8:11], s64 offen offset:3072 lds
MM a[72:75],a[160:163],v[72:75],a[72:75]
s_add_u32 m0, s65, 114688
EX v48,v48
MM a[80:83],a[160:163],v[80:83],a[80:83]
RD a[224:227],v224 offset:24576
CV v15,v46,v47
MM a[80:83],a[164:167],v[84:87],a[80:83]
EX v49,v49
MM a[88:91],a[164:167],v[92:95],a[88:91]
EX v50,v50
MM a[88:91],a[160:163],v[88:91],a[88:91]
buffer_load_dwordx4 v235, s[12:15], s81 offen offset:0 lds
MM a[92:95],a[168:171],v[88:91],a[92:95]
EX v51,v51
MM a[92:95],a[172:175],v[92:95],a[92:95]
RD a[228:231],v225 offset:24576
CV v20,v48,v49
MM a[84:87],a[172:175],v[84:87],a[84:87]
EX v52,v52
MM a[84:87],a[168:171],v[80:83],a[84:87]
RD a[232:235],v226 offset:24576
CV v21,v50,v51
MM a[76:79],a[168:171],v[72:75],a[76:79]
EX v53,v53
MM a[76:79],a[172:175],v[76:79],a[76:79]
RD a[236:239],v227 offset:24576
CV v22,v52,v53
MM a[68:71],a[172:175],v[68:71],a[68:71]
EX v54,v54
MM a[68:71],a[168:171],v[64:67],a[68:71]
buffer_load_dwordx4 v235, s[12:15], s64 offen offset:1024 lds
MM a[96:99],a[176:179],v[64:67],a[96:99]
EX v55,v55
MM a[96:99],a[180:183],v[68:71],a[96:99]
EX v56,v56
MM a[104:107],a[180:183],v[76:79],a[104:107]
RD a[240:243],v224 offset:28672
CV v23,v54,v55
MM a[104:107],a[176:179],v[72:75],a[104:107]
)ASM"
R"ASM(
EX v57,v57
MM a[112:115],a[176:179],v[80:83],a[112:115]
RD a[244:247],v225 offset:28672
CV v28,v56,v57
MM a[112:115],a[180:183],v[84:87],a[112:115]
EX v58,v58
MM a[120:123],a[180:183],v[92:95],a[120:123]
buffer_load_dwordx4 v235, s[12:15], s81 offen offset:2048 lds
MM a[120:123],a[176:179],v[88:91],a[120:123]
EX v59,v59
MM a[124:127],a[184:187],v[88:91],a[124:127]
RD a[248:251],v226 offset:28672
CV v29,v58,v59
MM a[124:127],a[188:191],v[92:95],a[124:127]
EX v60,v60
MM a[116:119],a[188:191],v[84:87],a[116:119]
EX v61,v61
MM a[116:119],a[184:187],v[80:83],a[116:119]
RD a[252:255],v227 offset:28672
CV v30,v60,v61
MM a[108:111],a[184:187],v[72:75],a[108:111]
EX v62,v62
MM a[108:111],a[188:191],v[76:79],a[108:111]
buffer_load_dwordx4 v235, s[12:15], s64 offen offset:3072 lds
MM a[100:103],a[188:191],v[68:71],a[100:103]
EX v63,v63
MM a[100:103],a[184:187],v[64:67],a[100:103]
CV v31,v62,v63
v_smfmac_f32_16x16x64_bf16 v[236:239], v[144:147], v[0:7], v234
v_smfmac_f32_16x16x64_bf16 v[236:239], v[148:151], v[8:15], v234
v_smfmac_f32_16x16x64_bf16 v[236:239], v[152:155], v[16:23], v234
v_smfmac_f32_16x16x64_bf16 v[236:239], v[156:159], v[24:31], v234
s_waitcnt lgkmcnt(0)
s_sub_u32 s70, s70, 1
s_cmp_lg_u32 s70, 0
s_cbranch_scc1 LLA_%=
MM v[64:67],a[192:195],v[160:163],v[128:131]
s_add_u32 s64, s64, 16384
TR a[128:129],v232 offset:0
s_add_u32 s81, s64, 896
MM v[64:67],a[196:199],v[176:179],v[64:67]
TR a[130:131],v232 offset:4096
TR a[132:133],v232 offset:8192
MM v[64:67],a[200:203],v[192:195],v[64:67]
TR a[134:135],v232 offset:12288
TR a[136:137],v233 offset:0
MM v[64:67],a[204:207],v[208:211],v[64:67]
TR a[138:139],v233 offset:4096
v_add_f32 v246, v236, v237
MM v[68:71],a[220:223],v[208:211],v[128:131]
TR a[140:141],v233 offset:8192
v_cmp_nge_f32 vcc, s71, v246
MM v[68:71],a[216:219],v[192:195],v[68:71]
s_cbranch_vccnz LSITE8_%=
LBACK8_%=:
TR a[142:143],v233 offset:12288
TR a[144:145],v232 offset:512
MM v[68:71],a[212:215],v[176:179],v[68:71]
TR a[146:147],v232 offset:4608
TR a[148:149],v232 offset:8704
MM v[68:71],a[208:211],v[160:163],v[68:71]
TR a[150:151],v232 offset:12800
TR a[152:153],v233 offset:512
MM v[76:79],a[208:211],v[164:167],v[132:135]
EX v64,v64
MM v[76:79],a[212:215],v[180:183],v[76:79]
EX v65,v65
MM v[76:79],a[216:219],v[196:199],v[76:79]
TR a[154:155],v233 offset:4608
CV v64,v64,v65
MM v[76:79],a[220:223],v[212:215],v[76:79]
EX v66,v66
MM v[72:75],a[204:207],v[212:215],v[132:135]
EX v67,v67
MM v[72:75],a[200:203],v[196:199],v[72:75]
TR a[156:157],v233 offset:8704
CV v65,v66,v67
MM v[72:75],a[196:199],v[180:183],v[72:75]
EX v68,v68
MM v[72:75],a[192:195],v[164:167],v[72:75]
EX v69,v69
MM v[80:83],a[192:195],v[168:171],v[136:139]
TR a[158:159],v233 offset:12800
CV v66,v68,v69
MM v[80:83],a[196:199],v[184:187],v[80:83]
EX v70,v70
MM v[80:83],a[200:203],v[200:203],v[80:83]
EX v71,v71
MM v[80:83],a[204:207],v[216:219],v[80:83]
TR a[160:161],v232 offset:1024
CV v67,v70,v71
MM v[84:87],a[220:223],v[216:219],v[136:139]
EX v72,v72
MM v[84:87],a[216:219],v[200:203],v[84:87]
EX v73,v73
MM v[84:87],a[212:215],v[184:187],v[84:87]
TR a[162:163],v232 offset:5120
CV v72,v72,v73
MM v[84:87],a[208:211],v[168:171],v[84:87]
EX v74,v74
MM v[92:95],a[208:211],v[172:175],v[140:143]
TR a[164:165],v232 offset:9216
TR a[166:167],v232 offset:13312
MM v[92:95],a[212:215],v[188:191],v[92:95]
EX v75,v75
MM v[92:95],a[216:219],v[204:207],v[92:95]
TR a[168:169],v233 offset:1024
CV v73,v74,v75
MM v[92:95],a[220:223],v[220:223],v[92:95]
EX v76,v76
MM v[88:91],a[204:207],v[220:223],v[140:143]
EX v77,v77
MM v[88:91],a[200:203],v[204:207],v[88:91]
TR a[170:171],v233 offset:5120
CV v74,v76,v77
MM v[88:91],a[196:199],v[188:191],v[88:91]
EX v78,v78
MM v[88:91],a[192:195],v[172:175],v[88:91]
TR a[172:173],v233 offset:9216
TR a[174:175],v233 offset:13312
MM v[96:99],a[224:227],v[160:163],v[128:131]
EX v79,v79
MM v[96:99],a[228:231],v[176:179],v[96:99]
TR a[176:177],v232 offset:1536
CV v75,v78,v79
MM v[96:99],a[232:235],v[192:195],v[96:99]
EX v80,v80
MM v[96:99],a[236:239],v[208:211],v[96:99]
TR a[178:179],v232 offset:5632
TR a[180:181],v232 offset:9728
MM v[100:103],a[252:255],v[208:211],v[128:131]
EX v81,v81
MM v[100:103],a[248:251],v[192:195],v[100:103]
TR a[182:183],v232 offset:13824
CV v80,v80,v81
MM v[100:103],a[244:247],v[176:179],v[100:103]
EX v82,v82
MM v[100:103],a[240:243],v[160:163],v[100:103]
EX v83,v83
MM v[108:111],a[240:243],v[164:167],v[132:135]
TR a[184:185],v233 offset:1536
CV v81,v82,v83
MM v[108:111],a[244:247],v[180:183],v[108:111]
EX v84,v84
MM v[108:111],a[248:251],v[196:199],v[108:111]
TR a[186:187],v233 offset:5632
TR a[188:189],v233 offset:9728
MM v[108:111],a[252:255],v[212:215],v[108:111]
EX v85,v85
MM v[104:107],a[236:239],v[212:215],v[132:135]
TR a[190:191],v233 offset:13824
CV v82,v84,v85
MM v[104:107],a[232:235],v[196:199],v[104:107]
EX v86,v86
MM v[104:107],a[228:231],v[180:183],v[104:107]
EX v87,v87
MM v[104:107],a[224:227],v[164:167],v[104:107]
CV v83,v86,v87
MM v[112:115],a[224:227],v[168:171],v[136:139]
EX v88,v88
MM v[112:115],a[228:231],v[184:187],v[112:115]
EX v89,v89
MM v[112:115],a[232:235],v[200:203],v[112:115]
EX v90,v90
MM v[112:115],a[236:239],v[216:219],v[112:115]
EX v91,v91
MM v[116:119],a[252:255],v[216:219],v[136:139]
EX v92,v92
MM v[116:119],a[248:251],v[200:203],v[116:119]
EX v93,v93
MM v[116:119],a[244:247],v[184:187],v[116:119]
EX v94,v94
MM v[116:119],a[240:243],v[168:171],v[116:119]
EX v95,v95
MM v[124:127],a[240:243],v[172:175],v[140:143]
EX v96,v96
MM v[124:127],a[244:247],v[188:191],v[124:127]
EX v97,v97
MM v[124:127],a[248:251],v[204:207],v[124:127]
EX v98,v98
MM v[124:127],a[252:255],v[220:223],v[124:127]
EX v99,v99
MM v[120:123],a[236:239],v[220:223],v[140:143]
EX v100,v100
MM v[120:123],a[232:235],v[204:207],v[120:123]
EX v101,v101
MM v[120:123],a[228:231],v[188:191],v[120:123]
EX v102,v102
MM v[120:123],a[224:227],v[172:175],v[120:123]
s_waitcnt vmcnt(13) lgkmcnt(0)
s_barrier
MM a[0:3],a[128:131],v[0:3],a[0:3]
s_add_u32 m0, s65, 0
v_mov_b32 v45, s79
CV v88,v88,v89
MM a[0:3],a[132:135],v[4:7],a[0:3]
RD v[40:43],v45 offset:0
CV v89,v90,v91
MM a[8:11],a[132:135],v[12:15],a[8:11]
RD a[192:195],v224 offset:32768
CV v90,v92,v93
MM a[8:11],a[128:131],v[8:11],a[8:11]
RD a[196:199],v225 offset:32768
CV v91,v94,v95
MM a[16:19],a[128:131],v[16:19],a[16:19]
buffer_load_dwordx4 v228, s[8:11], s64 offen offset:0 lds
MM a[16:19],a[132:135],v[20:23],a[16:19]
RD a[200:203],v226 offset:32768
CV v68,v96,v97
MM a[24:27],a[132:135],v[28:31],a[24:27]
s_waitcnt lgkmcnt(3)
v_min3_u32 v40, v40, v41, v42
v_min_u32 v40, v40, v43
MM a[24:27],a[128:131],v[24:27],a[24:27]
v_readfirstlane_b32 s76, v40
v_add_u32 v45, s80, v45
MM a[28:31],a[136:139],v[24:27],a[28:31]
s_sub_u32 s77, s92, s76
RD a[204:207],v227 offset:32768
CV v69,v98,v99
MM a[28:31],a[140:143],v[28:31],a[28:31]
s_sub_u32 s92, s77, 1
buffer_load_dwordx4 v229, s[8:11], s64 offen offset:1024 lds
MM a[20:23],a[140:143],v[20:23],a[20:23]
s_and_b32 s77, s77, 65535
RD a[208:211],v224 offset:36864
CV v70,v100,v101
MM a[20:23],a[136:139],v[16:19],a[20:23]
s_mov_b32 s55, s59
RD a[212:215],v225 offset:36864
MM a[12:15],a[136:139],v[8:11],a[12:15]
s_sub_u32 s59, 65535, s77
buffer_load_dwordx4 v230, s[8:11], s64 offen offset:2048 lds
MM a[12:15],a[140:143],v[12:15],a[12:15]
s_mul_i32 s91, %[swave], 3072
RD a[216:219],v226 offset:36864
MM a[4:7],a[140:143],v[4:7],a[4:7]
LPG9_%=:
s_cbranch_execz LTR0_%=
s_add_u32 s91, s91, %[slds]
EX v103,v103
MM a[4:7],a[136:139],v[0:3],a[4:7]
s_bfe_u32 s90, s48, 131077
RD a[220:223],v227 offset:36864
CV v71,v102,v103
MM a[32:35],a[144:147],v[0:3],a[32:35]
s_mul_i32 s90, s90, 320
EX v104,v104
MM a[32:35],a[148:151],v[4:7],a[32:35]
s_add_u32 s90, s90, 131072
buffer_load_dwordx4 v231, s[8:11], s64 offen offset:3072 lds
MM a[40:43],a[148:151],v[12:15],a[40:43]
s_mul_i32 s66, s52, 1280
EX v105,v105
MM a[40:43],a[144:147],v[8:11],a[40:43]
s_add_u32 s66, s66, s90
s_add_u32 m0, s91, 151552
s_mov_b64 exec, 0xfffff
buffer_load_dwordx4 v248, s[20:23], s66 offen offset:0 sc1 lds
s_mov_b64 exec, -1
MM a[48:51],a[144:147],v[16:19],a[48:51]
s_mul_i32 s66, s53, 1280
RD a[224:227],v224 offset:40960
CV v76,v104,v105
MM a[48:51],a[148:151],v[20:23],a[48:51]
s_add_u32 s66, s66, s90
s_add_u32 m0, s91, 151872
s_mov_b64 exec, 0xfffff
buffer_load_dwordx4 v248, s[20:23], s66 offen offset:0 sc1 lds
s_mov_b64 exec, -1
MM a[56:59],a[148:151],v[28:31],a[56:59]
s_mul_i32 s66, s54, 1280
EX v106,v106
MM a[56:59],a[144:147],v[24:27],a[56:59]
s_add_u32 s66, s66, s90
EX v107,v107
MM a[60:63],a[152:155],v[24:27],a[60:63]
s_add_u32 m0, s65, 65536
s_add_u32 m0, s91, 152192
s_mov_b64 exec, 0xfffff
buffer_load_dwordx4 v248, s[20:23], s66 offen offset:0 sc1 lds
s_mov_b64 exec, -1
MM a[60:63],a[156:159],v[28:31],a[60:63]
s_mul_i32 s66, s55, 1280
RD a[228:231],v225 offset:40960
CV v77,v106,v107
MM a[52:55],a[156:159],v[20:23],a[52:55]
s_add_u32 s66, s66, s90
EX v108,v108
MM a[52:55],a[152:155],v[16:19],a[52:55]
s_add_u32 m0, s91, 152512
s_mov_b64 exec, 0xfffff
buffer_load_dwordx4 v248, s[20:23], s66 offen offset:0 sc1 lds
s_mov_b64 exec, -1
MM a[44:47],a[152:155],v[8:11],a[44:47]
s_mul_i32 s66, s56, 1280
EX v109,v109
MM a[44:47],a[156:159],v[12:15],a[44:47]
s_add_u32 s66, s66, s90
RD a[232:235],v226 offset:40960
CV v78,v108,v109
MM a[36:39],a[156:159],v[4:7],a[36:39]
s_add_u32 m0, s91, 152832
s_mov_b64 exec, 0xfffff
buffer_load_dwordx4 v248, s[20:23], s66 offen offset:0 sc1 lds
s_mov_b64 exec, -1
MM a[36:39],a[152:155],v[0:3],a[36:39]
s_mul_i32 s66, s57, 1280
EX v110,v110
MM a[64:67],a[160:163],v[0:3],a[64:67]
s_add_u32 s66, s66, s90
EX v111,v111
MM a[64:67],a[164:167],v[4:7],a[64:67]
s_add_u32 m0, s91, 153152
s_mov_b64 exec, 0xfffff
buffer_load_dwordx4 v248, s[20:23], s66 offen offset:0 sc1 lds
s_mov_b64 exec, -1
MM a[72:75],a[164:167],v[12:15],a[72:75]
s_mul_i32 s66, s58, 1280
EX v112,v112
MM a[72:75],a[160:163],v[8:11],a[72:75]
s_add_u32 s66, s66, s90
RD a[236:239],v227 offset:40960
CV v79,v110,v111
MM a[80:83],a[160:163],v[16:19],a[80:83]
s_add_u32 m0, s91, 153472
s_mov_b64 exec, 0xfffff
buffer_load_dwordx4 v248, s[20:23], s66 offen offset:0 sc1 lds
s_mov_b64 exec, -1
MM a[80:83],a[164:167],v[20:23],a[80:83]
s_mul_i32 s66, s59, 1280
EX v113,v113
MM a[88:91],a[164:167],v[28:31],a[88:91]
s_add_u32 s66, s66, s90
EX v114,v114
MM a[88:91],a[160:163],v[24:27],a[88:91]
s_add_u32 m0, s91, 153792
s_mov_b64 exec, 0xfffff
buffer_load_dwordx4 v248, s[20:23], s66 offen offset:0 sc1 lds
s_mov_b64 exec, -1
MM a[92:95],a[168:171],v[24:27],a[92:95]
EX v115,v115
MM a[92:95],a[172:175],v[28:31],a[92:95]
CV v84,v112,v113
CV v85,v114,v115
MM a[84:87],a[172:175],v[20:23],a[84:87]
s_add_u32 m0, s65, 65536
s_nop 0
buffer_load_dwordx4 v235, s[12:15], s81 offen offset:0 lds
MM a[84:87],a[168:171],v[16:19],a[84:87]
EX v116,v116
MM a[76:79],a[168:171],v[8:11],a[76:79]
EX v117,v117
MM a[76:79],a[172:175],v[12:15],a[76:79]
RD a[240:243],v224 offset:45056
CV v86,v116,v117
MM a[68:71],a[172:175],v[4:7],a[68:71]
EX v118,v118
MM a[68:71],a[168:171],v[0:3],a[68:71]
buffer_load_dwordx4 v235, s[12:15], s64 offen offset:1024 lds
MM a[96:99],a[176:179],v[0:3],a[96:99]
EX v119,v119
MM a[96:99],a[180:183],v[4:7],a[96:99]
EX v120,v120
MM a[104:107],a[180:183],v[12:15],a[104:107]
RD a[244:247],v225 offset:45056
CV v87,v118,v119
MM a[104:107],a[176:179],v[8:11],a[104:107]
EX v121,v121
MM a[112:115],a[176:179],v[16:19],a[112:115]
RD a[248:251],v226 offset:45056
CV v92,v120,v121
MM a[112:115],a[180:183],v[20:23],a[112:115]
EX v122,v122
MM a[120:123],a[180:183],v[28:31],a[120:123]
buffer_load_dwordx4 v235, s[12:15], s81 offen offset:2048 lds
MM a[120:123],a[176:179],v[24:27],a[120:123]
EX v123,v123
MM a[124:127],a[184:187],v[24:27],a[124:127]
RD a[252:255],v227 offset:45056
CV v93,v122,v123
MM a[124:127],a[188:191],v[28:31],a[124:127]
EX v124,v124
MM a[116:119],a[188:191],v[20:23],a[116:119]
EX v125,v125
MM a[116:119],a[184:187],v[16:19],a[116:119]
buffer_load_dwordx4 v235, s[12:15], s64 offen offset:3072 lds
MM a[108:111],a[184:187],v[8:11],a[108:111]
EX v126,v126
MM a[108:111],a[188:191],v[12:15],a[108:111]
)ASM"
R"ASM(
EX v127,v127
MM a[100:103],a[188:191],v[4:7],a[100:103]
CV v94,v124,v125
CV v95,v126,v127
MM a[100:103],a[184:187],v[0:3],a[100:103]
v_smfmac_f32_16x16x64_bf16 v[236:239], v[144:147], v[64:71], v234
v_smfmac_f32_16x16x64_bf16 v[236:239], v[148:151], v[72:79], v234
v_smfmac_f32_16x16x64_bf16 v[236:239], v[152:155], v[80:87], v234
v_smfmac_f32_16x16x64_bf16 v[236:239], v[156:159], v[88:95], v234
s_waitcnt lgkmcnt(0)
MM v[0:3],a[192:195],v[160:163],v[128:131]
s_add_u32 s64, s64, 16384
TR a[128:129],v232 offset:16384
s_add_u32 s81, s64, 896
MM v[0:3],a[196:199],v[176:179],v[0:3]
TR a[130:131],v232 offset:20480
TR a[132:133],v232 offset:24576
MM v[0:3],a[200:203],v[192:195],v[0:3]
TR a[134:135],v232 offset:28672
TR a[136:137],v233 offset:16384
MM v[0:3],a[204:207],v[208:211],v[0:3]
TR a[138:139],v233 offset:20480
v_add_f32 v247, v236, v237
MM v[4:7],a[220:223],v[208:211],v[128:131]
TR a[140:141],v233 offset:24576
v_cmp_nge_f32 vcc, s71, v247
MM v[4:7],a[216:219],v[192:195],v[4:7]
s_cbranch_vccnz LSITE9_%=
LBACK9_%=:
TR a[142:143],v233 offset:28672
TR a[144:145],v232 offset:16896
MM v[4:7],a[212:215],v[176:179],v[4:7]
v_lshrrev_b32 v127, 8, v248
v_lshlrev_b32 v124, 9, v127
MM v[4:7],a[208:211],v[160:163],v[4:7]
v_and_b32 v126, 192, v248
v_add_u32 v124, v124, v126
MM v[12:15],a[208:211],v[164:167],v[132:135]
v_bfe_u32 v126, v248, 4, 2
v_lshl_add_u32 v124, v126, 3, v124
MM v[12:15],a[212:215],v[180:183],v[12:15]
s_lshr_b32 s76, %[swave], 1
EX v0,v0
MM v[12:15],a[216:219],v[196:199],v[12:15]
s_mul_i32 s76, s76, 10240
EX v1,v1
MM v[12:15],a[220:223],v[212:215],v[12:15]
s_and_b32 s77, %[swave], 1
TR a[146:147],v232 offset:20992
CV v0,v0,v1
MM v[8:11],a[204:207],v[212:215],v[132:135]
s_lshl_b32 s77, s77, 5
EX v2,v2
MM v[8:11],a[200:203],v[196:199],v[8:11]
s_add_u32 s76, s76, s77
EX v3,v3
MM v[8:11],a[196:199],v[180:183],v[8:11]
s_add_u32 s76, s76, %[slds]
TR a[148:149],v232 offset:25088
CV v1,v2,v3
MM v[8:11],a[192:195],v[164:167],v[8:11]
s_add_u32 s76, s76, 131072
v_add_u32 v124, s76, v124
v_bfe_u32 v126, v248, 4, 3
MM v[16:19],a[192:195],v[168:171],v[136:139]
v_mul_u32_u24 v125, 320, v126
v_lshl_add_u32 v125, v127, 4, v125
MM v[16:19],a[196:199],v[184:187],v[16:19]
s_mul_i32 s76, %[swave], 3072
EX v4,v4
MM v[16:19],a[200:203],v[200:203],v[16:19]
s_add_u32 s76, s76, %[slds]
EX v5,v5
MM v[16:19],a[204:207],v[216:219],v[16:19]
s_add_u32 s76, s76, 151552
v_add_u32 v125, s76, v125
v_lshl_add_u32 v126, v127, 3, v126
MM v[20:23],a[220:223],v[216:219],v[136:139]
v_lshlrev_b32 v126, 4, v126
CV v2,v4,v5
MM v[20:23],a[216:219],v[200:203],v[20:23]
s_lshr_b32 s76, s48, 7
EX v6,v6
MM v[20:23],a[212:215],v[184:187],v[20:23]
s_lshl_b32 s76, s76, 5
TR a[150:151],v232 offset:29184
TR a[152:153],v233 offset:16896
MM v[20:23],a[208:211],v[168:171],v[20:23]
s_and_b32 s77, s48, 16
EX v7,v7
MM v[28:31],a[208:211],v[172:175],v[140:143]
s_add_u32 s76, s76, s77
EX v8,v8
MM v[28:31],a[212:215],v[188:191],v[28:31]
s_bfe_u32 s77, s48, 131077
TR a[154:155],v233 offset:20992
CV v3,v6,v7
MM v[28:31],a[216:219],v[204:207],v[28:31]
s_lshl_b32 s77, s77, 2
EX v9,v9
MM v[28:31],a[220:223],v[220:223],v[28:31]
s_add_u32 s76, s76, s77
TR a[156:157],v233 offset:25088
TR a[158:159],v233 offset:29184
MM v[24:27],a[204:207],v[220:223],v[140:143]
s_add_u32 s76, s76, %[swave]
EX v10,v10
MM v[24:27],a[200:203],v[204:207],v[24:27]
s_lshl_b32 s76, s76, 9
TR a[160:161],v232 offset:17408
CV v8,v8,v9
MM v[24:27],a[196:199],v[188:191],v[24:27]
s_add_u32 s76, s76, 22183936
EX v11,v11
MM v[24:27],a[192:195],v[172:175],v[24:27]
EX v12,v12
MM v[32:35],a[224:227],v[160:163],v[128:131]
TR a[162:163],v232 offset:21504
CV v9,v10,v11
MM v[32:35],a[228:231],v[176:179],v[32:35]
EX v13,v13
MM v[32:35],a[232:235],v[192:195],v[32:35]
TR a[164:165],v232 offset:25600
CV v10,v12,v13
MM v[32:35],a[236:239],v[208:211],v[32:35]
EX v14,v14
MM v[36:39],a[252:255],v[208:211],v[128:131]
TR a[166:167],v232 offset:29696
TR a[168:169],v233 offset:17408
MM v[36:39],a[248:251],v[192:195],v[36:39]
EX v15,v15
MM v[36:39],a[244:247],v[176:179],v[36:39]
TR a[170:171],v233 offset:21504
CV v11,v14,v15
MM v[36:39],a[240:243],v[160:163],v[36:39]
EX v16,v16
MM v[44:47],a[240:243],v[164:167],v[132:135]
TR a[172:173],v233 offset:25600
TR a[174:175],v233 offset:29696
MM v[44:47],a[244:247],v[180:183],v[44:47]
EX v17,v17
MM v[44:47],a[248:251],v[196:199],v[44:47]
EX v18,v18
MM v[44:47],a[252:255],v[212:215],v[44:47]
TR a[176:177],v232 offset:17920
CV v16,v16,v17
MM v[40:43],a[236:239],v[212:215],v[132:135]
EX v19,v19
MM v[40:43],a[232:235],v[196:199],v[40:43]
TR a[178:179],v232 offset:22016
CV v17,v18,v19
MM v[40:43],a[228:231],v[180:183],v[40:43]
EX v20,v20
MM v[40:43],a[224:227],v[164:167],v[40:43]
TR a[180:181],v232 offset:26112
TR a[182:183],v232 offset:30208
MM v[48:51],a[224:227],v[168:171],v[136:139]
EX v21,v21
MM v[48:51],a[228:231],v[184:187],v[48:51]
TR a[184:185],v233 offset:17920
CV v18,v20,v21
MM v[48:51],a[232:235],v[200:203],v[48:51]
EX v22,v22
MM v[48:51],a[236:239],v[216:219],v[48:51]
EX v23,v23
MM v[52:55],a[252:255],v[216:219],v[136:139]
TR a[186:187],v233 offset:22016
CV v19,v22,v23
MM v[52:55],a[248:251],v[200:203],v[52:55]
EX v24,v24
MM v[52:55],a[244:247],v[184:187],v[52:55]
TR a[188:189],v233 offset:26112
TR a[190:191],v233 offset:30208
MM v[52:55],a[240:243],v[168:171],v[52:55]
EX v25,v25
MM v[60:63],a[240:243],v[172:175],v[140:143]
EX v26,v26
MM v[60:63],a[244:247],v[188:191],v[60:63]
EX v27,v27
MM v[60:63],a[248:251],v[204:207],v[60:63]
EX v28,v28
MM v[60:63],a[252:255],v[220:223],v[60:63]
EX v29,v29
MM v[56:59],a[236:239],v[220:223],v[140:143]
EX v30,v30
MM v[56:59],a[232:235],v[204:207],v[56:59]
EX v31,v31
MM v[56:59],a[228:231],v[188:191],v[56:59]
EX v32,v32
MM v[56:59],a[224:227],v[172:175],v[56:59]
s_waitcnt vmcnt(20) lgkmcnt(0)
s_barrier
MM a[0:3],a[128:131],v[64:67],a[0:3]
s_add_u32 m0, s65, 16384
CV v24,v24,v25
CV v25,v26,v27
MM a[0:3],a[132:135],v[68:71],a[0:3]
RD a[192:195],v224 offset:49152
CV v26,v28,v29
MM a[8:11],a[132:135],v[76:79],a[8:11]
RD a[196:199],v225 offset:49152
CV v27,v30,v31
MM a[8:11],a[128:131],v[72:75],a[8:11]
buffer_load_dwordx4 v228, s[8:11], s64 offen offset:0 lds
MM a[16:19],a[128:131],v[80:83],a[16:19]
RD a[200:203],v226 offset:49152
RD a[204:207],v227 offset:49152
MM a[16:19],a[132:135],v[84:87],a[16:19]
EX v33,v33
MM a[24:27],a[132:135],v[92:95],a[24:27]
RD a[208:211],v224 offset:53248
CV v4,v32,v33
MM a[24:27],a[128:131],v[88:91],a[24:27]
EX v34,v34
MM a[28:31],a[136:139],v[88:91],a[28:31]
buffer_load_dwordx4 v229, s[8:11], s64 offen offset:1024 lds
MM a[28:31],a[140:143],v[92:95],a[28:31]
EX v35,v35
MM a[20:23],a[140:143],v[84:87],a[20:23]
RD a[212:215],v225 offset:53248
CV v5,v34,v35
MM a[20:23],a[136:139],v[80:83],a[20:23]
EX v36,v36
MM a[12:15],a[136:139],v[72:75],a[12:15]
s_waitcnt vmcnt(6)
TR v[96:97],v124 offset:0
TR v[98:99],v124 offset:256
MM a[12:15],a[140:143],v[76:79],a[12:15]
RD v[108:111],v125 offset:0
TR v[100:101],v124 offset:2048
MM a[4:7],a[140:143],v[68:71],a[4:7]
TR v[102:103],v124 offset:2304
RD v[112:115],v125 offset:64
MM a[4:7],a[136:139],v[64:67],a[4:7]
TR v[104:105],v124 offset:4096
TR v[106:107],v124 offset:4352
MM a[32:35],a[144:147],v[64:67],a[32:35]
RD v[116:119],v125 offset:128
RD a[216:219],v226 offset:53248
MM a[32:35],a[148:151],v[68:71],a[32:35]
EX v37,v37
MM a[40:43],a[148:151],v[76:79],a[40:43]
EX v38,v38
MM a[40:43],a[144:147],v[72:75],a[40:43]
EX v39,v39
MM a[48:51],a[144:147],v[80:83],a[48:51]
s_waitcnt lgkmcnt(1)
CV v6,v36,v37
CV v7,v38,v39
MM a[48:51],a[148:151],v[84:87],a[48:51]
EX v40,v40
MM v[120:123],v[96:99],v[108:111],0
MM v[120:123],v[100:103],v[112:115],v[120:123]
MM v[120:123],v[104:107],v[116:119],v[120:123]
MM a[56:59],a[148:151],v[92:95],a[56:59]
TR v[96:97],v124 offset:6144
TR v[98:99],v124 offset:6400
MM a[56:59],a[144:147],v[88:91],a[56:59]
RD v[108:111],v125 offset:192
TR v[100:101],v124 offset:8192
MM a[60:63],a[152:155],v[88:91],a[60:63]
TR v[102:103],v124 offset:8448
RD v[112:115],v125 offset:256
EX v41,v41
MM a[60:63],a[156:159],v[92:95],a[60:63]
EX v42,v42
MM a[52:55],a[156:159],v[84:87],a[52:55]
EX v43,v43
MM a[52:55],a[152:155],v[80:83],a[52:55]
buffer_load_dwordx4 v230, s[8:11], s64 offen offset:2048 lds
MM a[44:47],a[152:155],v[72:75],a[44:47]
s_waitcnt lgkmcnt(0)
EX v44,v44
MM a[44:47],a[156:159],v[76:79],a[44:47]
CV v12,v40,v41
CV v13,v42,v43
MM v[120:123],v[96:99],v[108:111],v[120:123]
MM v[120:123],v[100:103],v[112:115],v[120:123]
MM a[36:39],a[156:159],v[68:71],a[36:39]
EX v45,v45
MM a[36:39],a[152:155],v[64:67],a[36:39]
EX v46,v46
MM a[64:67],a[160:163],v[64:67],a[64:67]
buffer_load_dwordx4 v231, s[8:11], s64 offen offset:3072 lds
MM a[64:67],a[164:167],v[68:71],a[64:67]
s_add_u32 m0, s65, 81920
EX v47,v47
MM a[72:75],a[164:167],v[76:79],a[72:75]
RD a[220:223],v227 offset:53248
CV v14,v44,v45
MM a[72:75],a[160:163],v[72:75],a[72:75]
EX v48,v48
MM a[80:83],a[160:163],v[80:83],a[80:83]
s_mov_b32 exec_lo, 0x00ff00ff
s_mov_b32 exec_hi, 0x00ff00ff
buffer_store_dwordx4 v[120:123], v126, s[20:23], s76 offen offset:0 sc1
s_mov_b64 exec, -1
s_nop 0
MM a[80:83],a[164:167],v[84:87],a[80:83]
s_lshl_b32 s76, s50, 2
EX v49,v49
MM a[88:91],a[164:167],v[92:95],a[88:91]
s_add_u32 s76, s76, s84
RD a[224:227],v224 offset:57344
CV v15,v46,v47
MM a[88:91],a[160:163],v[88:91],a[88:91]
s_lshl_b32 s76, s76, 3
EX v50,v50
MM a[92:95],a[168:171],v[88:91],a[92:95]
s_lshr_b32 s77, s49, 2
EX v51,v51
MM a[92:95],a[172:175],v[92:95],a[92:95]
s_add_u32 s76, s76, s77
buffer_load_dwordx4 v235, s[12:15], s81 offen offset:0 lds
MM a[84:87],a[172:175],v[84:87],a[84:87]
s_mul_i32 s95, s76, 82944
RD a[228:231],v225 offset:57344
CV v20,v48,v49
MM a[84:87],a[168:171],v[80:83],a[84:87]
s_mul_i32 s76, %[swave], 20480
EX v52,v52
MM a[76:79],a[168:171],v[72:75],a[76:79]
s_add_u32 s95, s95, s76
EX v53,v53
MM a[76:79],a[172:175],v[76:79],a[76:79]
s_add_u32 s95, s95, 22708224
v_mov_b32 v240, -1
v_mov_b32 v241, -1
MM a[68:71],a[172:175],v[68:71],a[68:71]
v_mov_b32 v242, -1
v_mov_b32 v243, -1
MM a[68:71],a[168:171],v[64:67],a[68:71]
EX v54,v54
MM a[96:99],a[176:179],v[64:67],a[96:99]
RD a[232:235],v226 offset:57344
CV v21,v50,v51
MM a[96:99],a[180:183],v[68:71],a[96:99]
EX v55,v55
MM a[104:107],a[180:183],v[76:79],a[104:107]
RD a[236:239],v227 offset:57344
CV v22,v52,v53
MM a[104:107],a[176:179],v[72:75],a[104:107]
EX v56,v56
MM a[112:115],a[176:179],v[80:83],a[112:115]
buffer_load_dwordx4 v235, s[12:15], s64 offen offset:1024 lds
MM a[112:115],a[180:183],v[84:87],a[112:115]
EX v57,v57
MM a[120:123],a[180:183],v[92:95],a[120:123]
EX v58,v58
MM a[120:123],a[176:179],v[88:91],a[120:123]
RD a[240:243],v224 offset:61440
CV v23,v54,v55
MM a[124:127],a[184:187],v[88:91],a[124:127]
EX v59,v59
MM a[124:127],a[188:191],v[92:95],a[124:127]
RD a[244:247],v225 offset:61440
CV v28,v56,v57
MM a[116:119],a[188:191],v[84:87],a[116:119]
EX v60,v60
MM a[116:119],a[184:187],v[80:83],a[116:119]
buffer_load_dwordx4 v235, s[12:15], s81 offen offset:2048 lds
MM a[108:111],a[184:187],v[72:75],a[108:111]
EX v61,v61
MM a[108:111],a[188:191],v[76:79],a[108:111]
RD a[248:251],v226 offset:61440
CV v29,v58,v59
MM a[100:103],a[188:191],v[68:71],a[100:103]
EX v62,v62
MM a[100:103],a[184:187],v[64:67],a[100:103]
EX v63,v63
v_smfmac_f32_16x16x64_bf16 v[236:239], v[144:147], v[0:7], v234
RD a[252:255],v227 offset:61440
v_smfmac_f32_16x16x64_bf16 v[236:239], v[148:151], v[8:15], v234
CV v30,v60,v61
v_smfmac_f32_16x16x64_bf16 v[236:239], v[152:155], v[16:23], v234
CV v31,v62,v63
s_nop 1
v_smfmac_f32_16x16x64_bf16 v[236:239], v[156:159], v[24:31], v234
s_waitcnt lgkmcnt(0)
buffer_load_dwordx4 v235, s[12:15], s64 offen offset:3072 lds
MM v[64:67],a[192:195],v[160:163],v[128:131]
s_add_u32 s64, s64, 16384
TR a[128:129],v232 offset:32768
s_add_u32 s81, s64, 896
MM v[64:67],a[196:199],v[176:179],v[64:67]
TR a[130:131],v232 offset:36864
TR a[132:133],v232 offset:40960
MM v[64:67],a[200:203],v[192:195],v[64:67]
TR a[134:135],v232 offset:45056
TR a[136:137],v233 offset:32768
MM v[64:67],a[204:207],v[208:211],v[64:67]
TR a[138:139],v233 offset:36864
)ASM"
R"ASM(
v_add_f32 v246, v236, v237
MM v[68:71],a[220:223],v[208:211],v[128:131]
TR a[140:141],v233 offset:40960
v_cmp_nge_f32 vcc, s71, v246
MM v[68:71],a[216:219],v[192:195],v[68:71]
s_cbranch_vccnz LSITE10_%=
LBACK10_%=:
TR a[142:143],v233 offset:45056
TR a[144:145],v232 offset:33280
MM v[68:71],a[212:215],v[176:179],v[68:71]
s_lshr_b32 s77, %[sb], 3
TR a[146:147],v232 offset:37376
TR a[148:149],v232 offset:41472
MM v[68:71],a[208:211],v[160:163],v[68:71]
s_lshl_b32 s77, s77, 2
TR a[150:151],v232 offset:45568
TR a[152:153],v233 offset:33280
MM v[76:79],a[208:211],v[164:167],v[132:135]
s_add_u32 s77, s77, %[swave]
EX v64,v64
MM v[76:79],a[212:215],v[180:183],v[76:79]
s_lshl_b32 s77, s77, 9
EX v65,v65
MM v[76:79],a[216:219],v[196:199],v[76:79]
s_add_u32 s77, s77, 0
v_lshrrev_b32 v253, 1, v248
v_add_u32 v253, s77, v253
MM v[76:79],a[220:223],v[212:215],v[76:79]
s_mov_b32 s77, LKEND_ka-LPD0_%=-16
v_min_u32 v253, s77, v253
CV v64,v64,v65
MM v[72:75],a[204:207],v[212:215],v[132:135]
s_getpc_b64 s[98:99]
LPD0_%=:
global_load_dword v253, v253, s[98:99]
MM v[72:75],a[200:203],v[196:199],v[72:75]
EX v66,v66
MM v[72:75],a[196:199],v[180:183],v[72:75]
EX v67,v67
MM v[72:75],a[192:195],v[164:167],v[72:75]
TR a[154:155],v233 offset:37376
CV v65,v66,v67
MM v[80:83],a[192:195],v[168:171],v[136:139]
EX v68,v68
MM v[80:83],a[196:199],v[184:187],v[80:83]
EX v69,v69
MM v[80:83],a[200:203],v[200:203],v[80:83]
TR a[156:157],v233 offset:41472
CV v66,v68,v69
MM v[80:83],a[204:207],v[216:219],v[80:83]
EX v70,v70
MM v[84:87],a[220:223],v[216:219],v[136:139]
EX v71,v71
MM v[84:87],a[216:219],v[200:203],v[84:87]
TR a[158:159],v233 offset:45568
CV v67,v70,v71
s_nop 0
LPG10_%=:
s_cbranch_execz LTR0_%=
MM v[84:87],a[212:215],v[184:187],v[84:87]
EX v72,v72
MM v[84:87],a[208:211],v[168:171],v[84:87]
EX v73,v73
MM v[92:95],a[208:211],v[172:175],v[140:143]
TR a[160:161],v232 offset:33792
CV v72,v72,v73
MM v[92:95],a[212:215],v[188:191],v[92:95]
EX v74,v74
MM v[92:95],a[216:219],v[204:207],v[92:95]
TR a[162:163],v232 offset:37888
TR a[164:165],v232 offset:41984
MM v[92:95],a[220:223],v[220:223],v[92:95]
EX v75,v75
MM v[88:91],a[204:207],v[220:223],v[140:143]
TR a[166:167],v232 offset:46080
CV v73,v74,v75
MM v[88:91],a[200:203],v[204:207],v[88:91]
EX v76,v76
MM v[88:91],a[196:199],v[188:191],v[88:91]
EX v77,v77
MM v[88:91],a[192:195],v[172:175],v[88:91]
TR a[168:169],v233 offset:33792
CV v74,v76,v77
MM v[96:99],a[224:227],v[160:163],v[128:131]
EX v78,v78
MM v[96:99],a[228:231],v[176:179],v[96:99]
TR a[170:171],v233 offset:37888
TR a[172:173],v233 offset:41984
MM v[96:99],a[232:235],v[192:195],v[96:99]
EX v79,v79
MM v[96:99],a[236:239],v[208:211],v[96:99]
TR a[174:175],v233 offset:46080
CV v75,v78,v79
MM v[100:103],a[252:255],v[208:211],v[128:131]
EX v80,v80
MM v[100:103],a[248:251],v[192:195],v[100:103]
EX v81,v81
MM v[100:103],a[244:247],v[176:179],v[100:103]
TR a[176:177],v232 offset:34304
CV v80,v80,v81
MM v[100:103],a[240:243],v[160:163],v[100:103]
EX v82,v82
MM v[108:111],a[240:243],v[164:167],v[132:135]
s_add_u32 s67, s95, 10240
buffer_store_dwordx4 v[240:243], v248, s[20:23], s67 offen offset:0 sc1
MM v[108:111],a[244:247],v[180:183],v[108:111]
TR a[178:179],v232 offset:38400
TR a[180:181],v232 offset:42496
MM v[108:111],a[248:251],v[196:199],v[108:111]
EX v83,v83
MM v[108:111],a[252:255],v[212:215],v[108:111]
TR a[182:183],v232 offset:46592
CV v81,v82,v83
MM v[104:107],a[236:239],v[212:215],v[132:135]
EX v84,v84
MM v[104:107],a[232:235],v[196:199],v[104:107]
EX v85,v85
MM v[104:107],a[228:231],v[180:183],v[104:107]
TR a[184:185],v233 offset:34304
CV v82,v84,v85
MM v[104:107],a[224:227],v[164:167],v[104:107]
EX v86,v86
MM v[112:115],a[224:227],v[168:171],v[136:139]
TR a[186:187],v233 offset:38400
TR a[188:189],v233 offset:42496
MM v[112:115],a[228:231],v[184:187],v[112:115]
EX v87,v87
MM v[112:115],a[232:235],v[200:203],v[112:115]
TR a[190:191],v233 offset:46592
CV v83,v86,v87
MM v[112:115],a[236:239],v[216:219],v[112:115]
EX v88,v88
MM v[116:119],a[252:255],v[216:219],v[136:139]
EX v89,v89
MM v[116:119],a[248:251],v[200:203],v[116:119]
EX v90,v90
MM v[116:119],a[244:247],v[184:187],v[116:119]
EX v91,v91
MM v[116:119],a[240:243],v[168:171],v[116:119]
EX v92,v92
MM v[124:127],a[240:243],v[172:175],v[140:143]
EX v93,v93
MM v[124:127],a[244:247],v[188:191],v[124:127]
EX v94,v94
MM v[124:127],a[248:251],v[204:207],v[124:127]
EX v95,v95
MM v[124:127],a[252:255],v[220:223],v[124:127]
EX v96,v96
MM v[120:123],a[236:239],v[220:223],v[140:143]
EX v97,v97
MM v[120:123],a[232:235],v[204:207],v[120:123]
EX v98,v98
MM v[120:123],a[228:231],v[188:191],v[120:123]
EX v99,v99
MM v[120:123],a[224:227],v[172:175],v[120:123]
s_waitcnt lgkmcnt(0)
s_barrier
MM a[0:3],a[128:131],v[0:3],a[0:3]
s_add_u32 m0, s65, 32768
CV v88,v88,v89
CV v89,v90,v91
MM a[0:3],a[132:135],v[4:7],a[0:3]
RD a[192:195],v224 offset:0
CV v90,v92,v93
MM a[8:11],a[132:135],v[12:15],a[8:11]
RD a[196:199],v225 offset:0
CV v91,v94,v95
MM a[8:11],a[128:131],v[8:11],a[8:11]
buffer_load_dwordx4 v228, s[8:11], s64 offen offset:0 lds
MM a[16:19],a[128:131],v[16:19],a[16:19]
RD a[200:203],v226 offset:0
CV v68,v96,v97
MM a[16:19],a[132:135],v[20:23],a[16:19]
RD a[204:207],v227 offset:0
CV v69,v98,v99
MM a[24:27],a[132:135],v[28:31],a[24:27]
buffer_load_dwordx4 v229, s[8:11], s64 offen offset:1024 lds
MM a[24:27],a[128:131],v[24:27],a[24:27]
RD a[208:211],v224 offset:4096
RD a[212:215],v225 offset:4096
MM a[28:31],a[136:139],v[24:27],a[28:31]
buffer_load_dwordx4 v230, s[8:11], s64 offen offset:2048 lds
MM a[28:31],a[140:143],v[28:31],a[28:31]
EX v100,v100
MM a[20:23],a[140:143],v[20:23],a[20:23]
EX v101,v101
MM a[20:23],a[136:139],v[16:19],a[20:23]
RD a[216:219],v226 offset:4096
CV v70,v100,v101
MM a[12:15],a[136:139],v[8:11],a[12:15]
EX v102,v102
MM a[12:15],a[140:143],v[12:15],a[12:15]
RD a[220:223],v227 offset:4096
RD a[224:227],v224 offset:8192
MM a[4:7],a[140:143],v[4:7],a[4:7]
EX v103,v103
MM a[4:7],a[136:139],v[0:3],a[4:7]
RD a[228:231],v225 offset:8192
CV v71,v102,v103
MM a[32:35],a[144:147],v[0:3],a[32:35]
EX v104,v104
MM a[32:35],a[148:151],v[4:7],a[32:35]
buffer_load_dwordx4 v231, s[8:11], s64 offen offset:3072 lds
MM a[40:43],a[148:151],v[12:15],a[40:43]
s_add_u32 m0, s65, 98304
EX v105,v105
MM a[40:43],a[144:147],v[8:11],a[40:43]
RD a[232:235],v226 offset:8192
CV v76,v104,v105
MM a[48:51],a[144:147],v[16:19],a[48:51]
EX v106,v106
MM a[48:51],a[148:151],v[20:23],a[48:51]
EX v107,v107
MM a[56:59],a[148:151],v[28:31],a[56:59]
RD a[236:239],v227 offset:8192
CV v77,v106,v107
MM a[56:59],a[144:147],v[24:27],a[56:59]
EX v108,v108
MM a[60:63],a[152:155],v[24:27],a[60:63]
buffer_load_dwordx4 v235, s[12:15], s81 offen offset:0 lds
MM a[60:63],a[156:159],v[28:31],a[60:63]
EX v109,v109
MM a[52:55],a[156:159],v[20:23],a[52:55]
RD a[240:243],v224 offset:12288
CV v78,v108,v109
MM a[52:55],a[152:155],v[16:19],a[52:55]
EX v110,v110
MM a[44:47],a[152:155],v[8:11],a[44:47]
EX v111,v111
MM a[44:47],a[156:159],v[12:15],a[44:47]
RD a[244:247],v225 offset:12288
CV v79,v110,v111
MM a[36:39],a[156:159],v[4:7],a[36:39]
buffer_load_dwordx4 v235, s[12:15], s64 offen offset:1024 lds
MM a[36:39],a[152:155],v[0:3],a[36:39]
EX v112,v112
MM a[64:67],a[160:163],v[0:3],a[64:67]
EX v113,v113
MM a[64:67],a[164:167],v[4:7],a[64:67]
RD a[248:251],v226 offset:12288
CV v84,v112,v113
MM a[72:75],a[164:167],v[12:15],a[72:75]
EX v114,v114
MM a[72:75],a[160:163],v[8:11],a[72:75]
EX v115,v115
MM a[80:83],a[160:163],v[16:19],a[80:83]
RD a[252:255],v227 offset:12288
CV v85,v114,v115
MM a[80:83],a[164:167],v[20:23],a[80:83]
buffer_load_dwordx4 v235, s[12:15], s81 offen offset:2048 lds
MM a[88:91],a[164:167],v[28:31],a[88:91]
EX v116,v116
MM a[88:91],a[160:163],v[24:27],a[88:91]
EX v117,v117
MM a[92:95],a[168:171],v[24:27],a[92:95]
EX v118,v118
MM a[92:95],a[172:175],v[28:31],a[92:95]
buffer_load_dwordx4 v235, s[12:15], s64 offen offset:3072 lds
MM a[84:87],a[172:175],v[20:23],a[84:87]
EX v119,v119
MM a[84:87],a[168:171],v[16:19],a[84:87]
EX v120,v120
MM a[76:79],a[168:171],v[8:11],a[76:79]
EX v121,v121
MM a[76:79],a[172:175],v[12:15],a[76:79]
EX v122,v122
MM a[68:71],a[172:175],v[4:7],a[68:71]
EX v123,v123
MM a[68:71],a[168:171],v[0:3],a[68:71]
EX v124,v124
MM a[96:99],a[176:179],v[0:3],a[96:99]
EX v125,v125
MM a[96:99],a[180:183],v[4:7],a[96:99]
EX v126,v126
MM a[104:107],a[180:183],v[12:15],a[104:107]
EX v127,v127
MM a[104:107],a[176:179],v[8:11],a[104:107]
CV v86,v116,v117
CV v87,v118,v119
MM a[112:115],a[176:179],v[16:19],a[112:115]
CV v92,v120,v121
CV v93,v122,v123
MM a[112:115],a[180:183],v[20:23],a[112:115]
CV v94,v124,v125
CV v95,v126,v127
MM a[120:123],a[180:183],v[28:31],a[120:123]
MM a[120:123],a[176:179],v[24:27],a[120:123]
MM a[124:127],a[184:187],v[24:27],a[124:127]
MM a[124:127],a[188:191],v[28:31],a[124:127]
MM a[116:119],a[188:191],v[20:23],a[116:119]
MM a[116:119],a[184:187],v[16:19],a[116:119]
MM a[108:111],a[184:187],v[8:11],a[108:111]
MM a[108:111],a[188:191],v[12:15],a[108:111]
MM a[100:103],a[188:191],v[4:7],a[100:103]
MM a[100:103],a[184:187],v[0:3],a[100:103]
v_smfmac_f32_16x16x64_bf16 v[236:239], v[144:147], v[64:71], v234
v_smfmac_f32_16x16x64_bf16 v[236:239], v[148:151], v[72:79], v234
v_smfmac_f32_16x16x64_bf16 v[236:239], v[152:155], v[80:87], v234
v_smfmac_f32_16x16x64_bf16 v[236:239], v[156:159], v[88:95], v234
s_waitcnt lgkmcnt(0)
MM v[0:3],a[192:195],v[160:163],v[128:131]
s_add_u32 s64, s64, 16384
TR a[128:129],v232 offset:49152
s_add_u32 s81, s64, 896
MM v[0:3],a[196:199],v[176:179],v[0:3]
TR a[130:131],v232 offset:53248
TR a[132:133],v232 offset:57344
MM v[0:3],a[200:203],v[192:195],v[0:3]
TR a[134:135],v232 offset:61440
TR a[136:137],v233 offset:49152
MM v[0:3],a[204:207],v[208:211],v[0:3]
TR a[138:139],v233 offset:53248
v_add_f32 v247, v236, v237
MM v[4:7],a[220:223],v[208:211],v[128:131]
TR a[140:141],v233 offset:57344
v_cmp_nge_f32 vcc, s71, v247
MM v[4:7],a[216:219],v[192:195],v[4:7]
s_cbranch_vccnz LSITE11_%=
LBACK11_%=:
TR a[142:143],v233 offset:61440
TR a[144:145],v232 offset:49664
MM v[4:7],a[212:215],v[176:179],v[4:7]
s_add_u32 s67, s95, 11264
buffer_store_dwordx4 v[240:243], v248, s[20:23], s67 offen offset:0 sc1
MM v[4:7],a[208:211],v[160:163],v[4:7]
TR a[146:147],v232 offset:53760
TR a[148:149],v232 offset:57856
MM v[12:15],a[208:211],v[164:167],v[132:135]
EX v0,v0
MM v[12:15],a[212:215],v[180:183],v[12:15]
EX v1,v1
MM v[12:15],a[216:219],v[196:199],v[12:15]
TR a[150:151],v232 offset:61952
CV v0,v0,v1
MM v[12:15],a[220:223],v[212:215],v[12:15]
EX v2,v2
MM v[8:11],a[204:207],v[212:215],v[132:135]
EX v3,v3
MM v[8:11],a[200:203],v[196:199],v[8:11]
TR a[152:153],v233 offset:49664
CV v1,v2,v3
MM v[8:11],a[196:199],v[180:183],v[8:11]
EX v4,v4
MM v[8:11],a[192:195],v[164:167],v[8:11]
EX v5,v5
MM v[16:19],a[192:195],v[168:171],v[136:139]
TR a[154:155],v233 offset:53760
CV v2,v4,v5
MM v[16:19],a[196:199],v[184:187],v[16:19]
EX v6,v6
MM v[16:19],a[200:203],v[200:203],v[16:19]
EX v7,v7
MM v[16:19],a[204:207],v[216:219],v[16:19]
TR a[156:157],v233 offset:57856
CV v3,v6,v7
MM v[20:23],a[220:223],v[216:219],v[136:139]
EX v8,v8
MM v[20:23],a[216:219],v[200:203],v[20:23]
TR a[158:159],v233 offset:61952
TR a[160:161],v232 offset:50176
MM v[20:23],a[212:215],v[184:187],v[20:23]
EX v9,v9
MM v[20:23],a[208:211],v[168:171],v[20:23]
TR a[162:163],v232 offset:54272
CV v8,v8,v9
MM v[28:31],a[208:211],v[172:175],v[140:143]
EX v10,v10
MM v[28:31],a[212:215],v[188:191],v[28:31]
TR a[164:165],v232 offset:58368
TR a[166:167],v232 offset:62464
MM v[28:31],a[216:219],v[204:207],v[28:31]
EX v11,v11
MM v[28:31],a[220:223],v[220:223],v[28:31]
TR a[168:169],v233 offset:50176
CV v9,v10,v11
MM v[24:27],a[204:207],v[220:223],v[140:143]
EX v12,v12
MM v[24:27],a[200:203],v[204:207],v[24:27]
EX v13,v13
MM v[24:27],a[196:199],v[188:191],v[24:27]
TR a[170:171],v233 offset:54272
CV v10,v12,v13
MM v[24:27],a[192:195],v[172:175],v[24:27]
EX v14,v14
MM v[32:35],a[224:227],v[160:163],v[128:131]
TR a[172:173],v233 offset:58368
TR a[174:175],v233 offset:62464
MM v[32:35],a[228:231],v[176:179],v[32:35]
EX v15,v15
)ASM"
R"ASM(
MM v[32:35],a[232:235],v[192:195],v[32:35]
TR a[176:177],v232 offset:50688
CV v11,v14,v15
MM v[32:35],a[236:239],v[208:211],v[32:35]
EX v16,v16
MM v[36:39],a[252:255],v[208:211],v[128:131]
EX v17,v17
MM v[36:39],a[248:251],v[192:195],v[36:39]
TR a[178:179],v232 offset:54784
CV v16,v16,v17
MM v[36:39],a[244:247],v[176:179],v[36:39]
EX v18,v18
MM v[36:39],a[240:243],v[160:163],v[36:39]
TR a[180:181],v232 offset:58880
TR a[182:183],v232 offset:62976
MM v[44:47],a[240:243],v[164:167],v[132:135]
s_add_u32 s67, s95, 12288
buffer_store_dwordx4 v[240:243], v248, s[20:23], s67 offen offset:0 sc1
MM v[44:47],a[244:247],v[180:183],v[44:47]
EX v19,v19
MM v[44:47],a[248:251],v[196:199],v[44:47]
TR a[184:185],v233 offset:50688
CV v17,v18,v19
MM v[44:47],a[252:255],v[212:215],v[44:47]
EX v20,v20
MM v[40:43],a[236:239],v[212:215],v[132:135]
EX v21,v21
MM v[40:43],a[232:235],v[196:199],v[40:43]
TR a[186:187],v233 offset:54784
CV v18,v20,v21
MM v[40:43],a[228:231],v[180:183],v[40:43]
EX v22,v22
MM v[40:43],a[224:227],v[164:167],v[40:43]
TR a[188:189],v233 offset:58880
TR a[190:191],v233 offset:62976
MM v[48:51],a[224:227],v[168:171],v[136:139]
EX v23,v23
MM v[48:51],a[228:231],v[184:187],v[48:51]
CV v19,v22,v23
MM v[48:51],a[232:235],v[200:203],v[48:51]
EX v24,v24
MM v[48:51],a[236:239],v[216:219],v[48:51]
EX v25,v25
MM v[52:55],a[252:255],v[216:219],v[136:139]
EX v26,v26
MM v[52:55],a[248:251],v[200:203],v[52:55]
EX v27,v27
MM v[52:55],a[244:247],v[184:187],v[52:55]
EX v28,v28
MM v[52:55],a[240:243],v[168:171],v[52:55]
EX v29,v29
MM v[60:63],a[240:243],v[172:175],v[140:143]
EX v30,v30
MM v[60:63],a[244:247],v[188:191],v[60:63]
EX v31,v31
MM v[60:63],a[248:251],v[204:207],v[60:63]
EX v32,v32
MM v[60:63],a[252:255],v[220:223],v[60:63]
EX v33,v33
MM v[56:59],a[236:239],v[220:223],v[140:143]
EX v34,v34
MM v[56:59],a[232:235],v[204:207],v[56:59]
EX v35,v35
MM v[56:59],a[228:231],v[188:191],v[56:59]
s_getpc_b64 s[98:99]
LTA1_%=:
s_lshl_b32 s73, %[swave], 12
s_add_u32 s73, s73, LPG14_%=-LTA1_%=
s_add_u32 s98, s98, s73
s_addc_u32 s99, s99, 0
s_mov_b64 exec, 0
s_setpc_b64 s[98:99]
LTR1_%=:
s_mov_b64 exec, -1
s_nop 0
CV v24,v24,v25
MM v[56:59],a[224:227],v[172:175],v[56:59]
s_waitcnt vmcnt(17) lgkmcnt(0)
s_barrier
MM a[0:3],a[128:131],v[64:67],a[0:3]
s_lshr_b32 s91, s48, 7
CV v25,v26,v27
CV v26,v28,v29
MM a[0:3],a[132:135],v[68:71],a[0:3]
s_lshl_b32 s91, s91, 5
RD a[192:195],v224 offset:16384
CV v27,v30,v31
MM a[8:11],a[132:135],v[76:79],a[8:11]
s_and_b32 s77, s48, 16
s_add_u32 m0, s65, 49152
RD a[196:199],v225 offset:16384
MM a[8:11],a[128:131],v[72:75],a[8:11]
s_add_u32 s91, s91, s77
buffer_load_dwordx4 v228, s[8:11], s64 offen offset:0 lds
MM a[16:19],a[128:131],v[80:83],a[16:19]
s_bfe_u32 s77, s48, 131077
RD a[200:203],v226 offset:16384
CV v4,v32,v33
MM a[16:19],a[132:135],v[84:87],a[16:19]
s_lshl_b32 s77, s77, 2
CV v5,v34,v35
MM a[24:27],a[132:135],v[92:95],a[24:27]
s_add_u32 s91, s91, s77
buffer_load_dwordx4 v229, s[8:11], s64 offen offset:1024 lds
MM a[24:27],a[128:131],v[88:91],a[24:27]
s_add_u32 s91, s91, %[swave]
RD a[204:207],v227 offset:16384
RD a[208:211],v224 offset:20480
MM a[28:31],a[136:139],v[88:91],a[28:31]
s_lshl_b32 s91, s91, 2
RD a[212:215],v225 offset:20480
MM a[28:31],a[140:143],v[92:95],a[28:31]
s_add_u32 s91, s91, 77824
EX v36,v36
MM a[20:23],a[140:143],v[84:87],a[20:23]
s_waitcnt vmcnt(18)
v_mov_b32 v250, s69
s_mov_b64 exec, 1
buffer_store_dword v250, off, s[20:23], s91 offset:0 sc1
s_mov_b64 exec, -1
MM a[20:23],a[136:139],v[80:83],a[20:23]
EX v37,v37
MM a[12:15],a[136:139],v[72:75],a[12:15]
RD a[216:219],v226 offset:20480
CV v6,v36,v37
MM a[12:15],a[140:143],v[76:79],a[12:15]
buffer_load_dwordx4 v230, s[8:11], s64 offen offset:2048 lds
MM a[4:7],a[140:143],v[68:71],a[4:7]
EX v38,v38
MM a[4:7],a[136:139],v[64:67],a[4:7]
EX v39,v39
MM a[32:35],a[144:147],v[64:67],a[32:35]
RD a[220:223],v227 offset:20480
CV v7,v38,v39
MM a[32:35],a[148:151],v[68:71],a[32:35]
EX v40,v40
MM a[40:43],a[148:151],v[76:79],a[40:43]
EX v41,v41
MM a[40:43],a[144:147],v[72:75],a[40:43]
RD a[224:227],v224 offset:24576
CV v12,v40,v41
MM a[48:51],a[144:147],v[80:83],a[48:51]
buffer_load_dwordx4 v231, s[8:11], s64 offen offset:3072 lds
MM a[48:51],a[148:151],v[84:87],a[48:51]
s_add_u32 m0, s65, 114688
EX v42,v42
MM a[56:59],a[148:151],v[92:95],a[56:59]
EX v43,v43
MM a[56:59],a[144:147],v[88:91],a[56:59]
RD a[228:231],v225 offset:24576
CV v13,v42,v43
MM a[60:63],a[152:155],v[88:91],a[60:63]
EX v44,v44
MM a[60:63],a[156:159],v[92:95],a[60:63]
buffer_load_dwordx4 v235, s[12:15], s81 offen offset:0 lds
MM a[52:55],a[156:159],v[84:87],a[52:55]
EX v45,v45
MM a[52:55],a[152:155],v[80:83],a[52:55]
RD a[232:235],v226 offset:24576
CV v14,v44,v45
MM a[44:47],a[152:155],v[72:75],a[44:47]
EX v46,v46
MM a[44:47],a[156:159],v[76:79],a[44:47]
EX v47,v47
MM a[36:39],a[156:159],v[68:71],a[36:39]
RD a[236:239],v227 offset:24576
CV v15,v46,v47
MM a[36:39],a[152:155],v[64:67],a[36:39]
EX v48,v48
MM a[64:67],a[160:163],v[64:67],a[64:67]
buffer_load_dwordx4 v235, s[12:15], s64 offen offset:1024 lds
MM a[64:67],a[164:167],v[68:71],a[64:67]
EX v49,v49
MM a[72:75],a[164:167],v[76:79],a[72:75]
RD a[240:243],v224 offset:28672
CV v20,v48,v49
MM a[72:75],a[160:163],v[72:75],a[72:75]
EX v50,v50
MM a[80:83],a[160:163],v[80:83],a[80:83]
EX v51,v51
MM a[80:83],a[164:167],v[84:87],a[80:83]
RD a[244:247],v225 offset:28672
CV v21,v50,v51
MM a[88:91],a[164:167],v[92:95],a[88:91]
EX v52,v52
MM a[88:91],a[160:163],v[88:91],a[88:91]
buffer_load_dwordx4 v235, s[12:15], s81 offen offset:2048 lds
MM a[92:95],a[168:171],v[88:91],a[92:95]
EX v53,v53
MM a[92:95],a[172:175],v[92:95],a[92:95]
RD a[248:251],v226 offset:28672
CV v22,v52,v53
MM a[84:87],a[172:175],v[84:87],a[84:87]
EX v54,v54
MM a[84:87],a[168:171],v[80:83],a[84:87]
EX v55,v55
MM a[76:79],a[168:171],v[72:75],a[76:79]
RD a[252:255],v227 offset:28672
CV v23,v54,v55
MM a[76:79],a[172:175],v[76:79],a[76:79]
EX v56,v56
MM a[68:71],a[172:175],v[68:71],a[68:71]
buffer_load_dwordx4 v235, s[12:15], s64 offen offset:3072 lds
MM a[68:71],a[168:171],v[64:67],a[68:71]
EX v57,v57
MM a[96:99],a[176:179],v[64:67],a[96:99]
EX v58,v58
MM a[96:99],a[180:183],v[68:71],a[96:99]
EX v59,v59
MM a[104:107],a[180:183],v[76:79],a[104:107]
EX v60,v60
MM a[104:107],a[176:179],v[72:75],a[104:107]
EX v61,v61
MM a[112:115],a[176:179],v[80:83],a[112:115]
EX v62,v62
MM a[112:115],a[180:183],v[84:87],a[112:115]
EX v63,v63
MM a[120:123],a[180:183],v[92:95],a[120:123]
CV v28,v56,v57
CV v29,v58,v59
MM a[120:123],a[176:179],v[88:91],a[120:123]
CV v30,v60,v61
CV v31,v62,v63
MM a[124:127],a[184:187],v[88:91],a[124:127]
MM a[124:127],a[188:191],v[92:95],a[124:127]
MM a[116:119],a[188:191],v[84:87],a[116:119]
MM a[116:119],a[184:187],v[80:83],a[116:119]
MM a[108:111],a[184:187],v[72:75],a[108:111]
MM a[108:111],a[188:191],v[76:79],a[108:111]
MM a[100:103],a[188:191],v[68:71],a[100:103]
MM a[100:103],a[184:187],v[64:67],a[100:103]
v_smfmac_f32_16x16x64_bf16 v[236:239], v[144:147], v[0:7], v234
v_smfmac_f32_16x16x64_bf16 v[236:239], v[148:151], v[8:15], v234
v_smfmac_f32_16x16x64_bf16 v[236:239], v[152:155], v[16:23], v234
v_smfmac_f32_16x16x64_bf16 v[236:239], v[156:159], v[24:31], v234
s_waitcnt lgkmcnt(0)
MM v[64:67],a[192:195],v[160:163],v[128:131]
s_add_u32 s64, s64, 16384
TR a[128:129],v232 offset:0
s_add_u32 s81, s64, 896
MM v[64:67],a[196:199],v[176:179],v[64:67]
TR a[130:131],v232 offset:4096
TR a[132:133],v232 offset:8192
MM v[64:67],a[200:203],v[192:195],v[64:67]
TR a[134:135],v232 offset:12288
TR a[136:137],v233 offset:0
MM v[64:67],a[204:207],v[208:211],v[64:67]
TR a[138:139],v233 offset:4096
v_add_f32 v246, v236, v237
MM v[68:71],a[220:223],v[208:211],v[128:131]
TR a[140:141],v233 offset:8192
v_cmp_nge_f32 vcc, s71, v246
MM v[68:71],a[216:219],v[192:195],v[68:71]
s_cbranch_vccnz LSITE12_%=
LBACK12_%=:
TR a[142:143],v233 offset:12288
TR a[144:145],v232 offset:512
MM v[68:71],a[212:215],v[176:179],v[68:71]
s_add_u32 s67, s95, 13312
buffer_store_dwordx4 v[240:243], v248, s[20:23], s67 offen offset:0 sc1
MM v[68:71],a[208:211],v[160:163],v[68:71]
s_nop 0
LPG11_%=:
s_cbranch_execz LTR0_%=
TR a[146:147],v232 offset:4608
TR a[148:149],v232 offset:8704
MM v[76:79],a[208:211],v[164:167],v[132:135]
EX v64,v64
MM v[76:79],a[212:215],v[180:183],v[76:79]
EX v65,v65
MM v[76:79],a[216:219],v[196:199],v[76:79]
TR a[150:151],v232 offset:12800
CV v64,v64,v65
MM v[76:79],a[220:223],v[212:215],v[76:79]
EX v66,v66
MM v[72:75],a[204:207],v[212:215],v[132:135]
EX v67,v67
MM v[72:75],a[200:203],v[196:199],v[72:75]
TR a[152:153],v233 offset:512
CV v65,v66,v67
MM v[72:75],a[196:199],v[180:183],v[72:75]
EX v68,v68
MM v[72:75],a[192:195],v[164:167],v[72:75]
EX v69,v69
MM v[80:83],a[192:195],v[168:171],v[136:139]
TR a[154:155],v233 offset:4608
CV v66,v68,v69
MM v[80:83],a[196:199],v[184:187],v[80:83]
EX v70,v70
MM v[80:83],a[200:203],v[200:203],v[80:83]
EX v71,v71
MM v[80:83],a[204:207],v[216:219],v[80:83]
TR a[156:157],v233 offset:8704
CV v67,v70,v71
MM v[84:87],a[220:223],v[216:219],v[136:139]
EX v72,v72
MM v[84:87],a[216:219],v[200:203],v[84:87]
TR a[158:159],v233 offset:12800
TR a[160:161],v232 offset:1024
MM v[84:87],a[212:215],v[184:187],v[84:87]
EX v73,v73
MM v[84:87],a[208:211],v[168:171],v[84:87]
TR a[162:163],v232 offset:5120
CV v72,v72,v73
MM v[92:95],a[208:211],v[172:175],v[140:143]
s_and_b32 s44, s50, 7
EX v74,v74
MM v[92:95],a[212:215],v[188:191],v[92:95]
s_lshl_b32 s44, s44, 2
TR a[164:165],v232 offset:9216
TR a[166:167],v232 offset:13312
MM v[92:95],a[216:219],v[204:207],v[92:95]
s_add_u32 s44, s44, s84
EX v75,v75
MM v[92:95],a[220:223],v[220:223],v[92:95]
s_lshl_b32 s44, s44, 2
TR a[168:169],v233 offset:1024
CV v73,v74,v75
MM v[88:91],a[204:207],v[220:223],v[140:143]
s_add_u32 s44, s44, %[swave]
EX v76,v76
MM v[88:91],a[200:203],v[204:207],v[88:91]
s_mul_i32 s44, s44, 5120
EX v77,v77
MM v[88:91],a[196:199],v[188:191],v[88:91]
s_mul_i32 s45, s84, 320
TR a[170:171],v233 offset:5120
CV v74,v76,v77
MM v[88:91],a[192:195],v[172:175],v[88:91]
s_sub_u32 s44, s44, s45
EX v78,v78
MM v[96:99],a[224:227],v[160:163],v[128:131]
s_lshl_b32 s45, %[swave], 10
TR a[172:173],v233 offset:9216
TR a[174:175],v233 offset:13312
MM v[96:99],a[228:231],v[176:179],v[96:99]
s_add_u32 s45, s45, %[slds]
s_add_u32 m0, s45, 147456
s_add_u32 s44, s44, 0
buffer_load_dwordx4 v248, s[24:27], s44 offen offset:0 lds
MM v[96:99],a[232:235],v[192:195],v[96:99]
EX v79,v79
MM v[96:99],a[236:239],v[208:211],v[96:99]
TR a[176:177],v232 offset:1536
CV v75,v78,v79
MM v[100:103],a[252:255],v[208:211],v[128:131]
EX v80,v80
MM v[100:103],a[248:251],v[192:195],v[100:103]
EX v81,v81
MM v[100:103],a[244:247],v[176:179],v[100:103]
TR a[178:179],v232 offset:5632
CV v80,v80,v81
MM v[100:103],a[240:243],v[160:163],v[100:103]
EX v82,v82
MM v[108:111],a[240:243],v[164:167],v[132:135]
s_add_u32 s67, s95, 14336
buffer_store_dwordx4 v[240:243], v248, s[20:23], s67 offen offset:0 sc1
MM v[108:111],a[244:247],v[180:183],v[108:111]
EX v83,v83
MM v[108:111],a[248:251],v[196:199],v[108:111]
TR a[180:181],v232 offset:9728
CV v81,v82,v83
MM v[108:111],a[252:255],v[212:215],v[108:111]
EX v84,v84
MM v[104:107],a[236:239],v[212:215],v[132:135]
TR a[182:183],v232 offset:13824
TR a[184:185],v233 offset:1536
MM v[104:107],a[232:235],v[196:199],v[104:107]
EX v85,v85
MM v[104:107],a[228:231],v[180:183],v[104:107]
TR a[186:187],v233 offset:5632
CV v82,v84,v85
MM v[104:107],a[224:227],v[164:167],v[104:107]
EX v86,v86
MM v[112:115],a[224:227],v[168:171],v[136:139]
TR a[188:189],v233 offset:9728
TR a[190:191],v233 offset:13824
MM v[112:115],a[228:231],v[184:187],v[112:115]
EX v87,v87
MM v[112:115],a[232:235],v[200:203],v[112:115]
CV v83,v86,v87
MM v[112:115],a[236:239],v[216:219],v[112:115]
EX v88,v88
MM v[116:119],a[252:255],v[216:219],v[136:139]
EX v89,v89
MM v[116:119],a[248:251],v[200:203],v[116:119]
EX v90,v90
MM v[116:119],a[244:247],v[184:187],v[116:119]
EX v91,v91
MM v[116:119],a[240:243],v[168:171],v[116:119]
EX v92,v92
MM v[124:127],a[240:243],v[172:175],v[140:143]
EX v93,v93
MM v[124:127],a[244:247],v[188:191],v[124:127]
)ASM"
R"ASM(
EX v94,v94
MM v[124:127],a[248:251],v[204:207],v[124:127]
EX v95,v95
MM v[124:127],a[252:255],v[220:223],v[124:127]
EX v96,v96
MM v[120:123],a[236:239],v[220:223],v[140:143]
EX v97,v97
MM v[120:123],a[232:235],v[204:207],v[120:123]
EX v98,v98
MM v[120:123],a[228:231],v[188:191],v[120:123]
EX v99,v99
MM v[120:123],a[224:227],v[172:175],v[120:123]
s_waitcnt vmcnt(18) lgkmcnt(0)
s_barrier
MM a[0:3],a[128:131],v[0:3],a[0:3]
s_add_u32 m0, s65, 0
CV v88,v88,v89
CV v89,v90,v91
MM a[0:3],a[132:135],v[4:7],a[0:3]
RD a[192:195],v224 offset:32768
CV v90,v92,v93
MM a[8:11],a[132:135],v[12:15],a[8:11]
RD a[196:199],v225 offset:32768
CV v91,v94,v95
MM a[8:11],a[128:131],v[8:11],a[8:11]
buffer_load_dwordx4 v228, s[8:11], s64 offen offset:0 lds
MM a[16:19],a[128:131],v[16:19],a[16:19]
RD a[200:203],v226 offset:32768
CV v68,v96,v97
MM a[16:19],a[132:135],v[20:23],a[16:19]
RD a[204:207],v227 offset:32768
CV v69,v98,v99
MM a[24:27],a[132:135],v[28:31],a[24:27]
buffer_load_dwordx4 v229, s[8:11], s64 offen offset:1024 lds
MM a[24:27],a[128:131],v[24:27],a[24:27]
RD a[208:211],v224 offset:36864
RD a[212:215],v225 offset:36864
MM a[28:31],a[136:139],v[24:27],a[28:31]
buffer_load_dwordx4 v230, s[8:11], s64 offen offset:2048 lds
MM a[28:31],a[140:143],v[28:31],a[28:31]
EX v100,v100
MM a[20:23],a[140:143],v[20:23],a[20:23]
EX v101,v101
MM a[20:23],a[136:139],v[16:19],a[20:23]
RD a[216:219],v226 offset:36864
CV v70,v100,v101
MM a[12:15],a[136:139],v[8:11],a[12:15]
EX v102,v102
MM a[12:15],a[140:143],v[12:15],a[12:15]
RD a[220:223],v227 offset:36864
RD a[224:227],v224 offset:40960
MM a[4:7],a[140:143],v[4:7],a[4:7]
EX v103,v103
MM a[4:7],a[136:139],v[0:3],a[4:7]
RD a[228:231],v225 offset:40960
CV v71,v102,v103
MM a[32:35],a[144:147],v[0:3],a[32:35]
EX v104,v104
MM a[32:35],a[148:151],v[4:7],a[32:35]
buffer_load_dwordx4 v231, s[8:11], s64 offen offset:3072 lds
MM a[40:43],a[148:151],v[12:15],a[40:43]
s_add_u32 m0, s65, 65536
EX v105,v105
MM a[40:43],a[144:147],v[8:11],a[40:43]
RD a[232:235],v226 offset:40960
CV v76,v104,v105
MM a[48:51],a[144:147],v[16:19],a[48:51]
EX v106,v106
MM a[48:51],a[148:151],v[20:23],a[48:51]
EX v107,v107
MM a[56:59],a[148:151],v[28:31],a[56:59]
RD a[236:239],v227 offset:40960
CV v77,v106,v107
MM a[56:59],a[144:147],v[24:27],a[56:59]
EX v108,v108
MM a[60:63],a[152:155],v[24:27],a[60:63]
buffer_load_dwordx4 v235, s[12:15], s81 offen offset:0 lds
MM a[60:63],a[156:159],v[28:31],a[60:63]
EX v109,v109
MM a[52:55],a[156:159],v[20:23],a[52:55]
RD a[240:243],v224 offset:45056
CV v78,v108,v109
MM a[52:55],a[152:155],v[16:19],a[52:55]
EX v110,v110
MM a[44:47],a[152:155],v[8:11],a[44:47]
EX v111,v111
MM a[44:47],a[156:159],v[12:15],a[44:47]
RD a[244:247],v225 offset:45056
CV v79,v110,v111
MM a[36:39],a[156:159],v[4:7],a[36:39]
buffer_load_dwordx4 v235, s[12:15], s64 offen offset:1024 lds
MM a[36:39],a[152:155],v[0:3],a[36:39]
EX v112,v112
MM a[64:67],a[160:163],v[0:3],a[64:67]
EX v113,v113
MM a[64:67],a[164:167],v[4:7],a[64:67]
RD a[248:251],v226 offset:45056
CV v84,v112,v113
MM a[72:75],a[164:167],v[12:15],a[72:75]
EX v114,v114
MM a[72:75],a[160:163],v[8:11],a[72:75]
EX v115,v115
MM a[80:83],a[160:163],v[16:19],a[80:83]
RD a[252:255],v227 offset:45056
CV v85,v114,v115
MM a[80:83],a[164:167],v[20:23],a[80:83]
buffer_load_dwordx4 v235, s[12:15], s81 offen offset:2048 lds
MM a[88:91],a[164:167],v[28:31],a[88:91]
EX v116,v116
MM a[88:91],a[160:163],v[24:27],a[88:91]
EX v117,v117
MM a[92:95],a[168:171],v[24:27],a[92:95]
EX v118,v118
MM a[92:95],a[172:175],v[28:31],a[92:95]
buffer_load_dwordx4 v235, s[12:15], s64 offen offset:3072 lds
MM a[84:87],a[172:175],v[20:23],a[84:87]
EX v119,v119
MM a[84:87],a[168:171],v[16:19],a[84:87]
EX v120,v120
MM a[76:79],a[168:171],v[8:11],a[76:79]
EX v121,v121
MM a[76:79],a[172:175],v[12:15],a[76:79]
EX v122,v122
MM a[68:71],a[172:175],v[4:7],a[68:71]
EX v123,v123
MM a[68:71],a[168:171],v[0:3],a[68:71]
EX v124,v124
MM a[96:99],a[176:179],v[0:3],a[96:99]
EX v125,v125
MM a[96:99],a[180:183],v[4:7],a[96:99]
EX v126,v126
MM a[104:107],a[180:183],v[12:15],a[104:107]
EX v127,v127
MM a[104:107],a[176:179],v[8:11],a[104:107]
CV v86,v116,v117
CV v87,v118,v119
MM a[112:115],a[176:179],v[16:19],a[112:115]
CV v92,v120,v121
CV v93,v122,v123
MM a[112:115],a[180:183],v[20:23],a[112:115]
CV v94,v124,v125
CV v95,v126,v127
MM a[120:123],a[180:183],v[28:31],a[120:123]
MM a[120:123],a[176:179],v[24:27],a[120:123]
MM a[124:127],a[184:187],v[24:27],a[124:127]
MM a[124:127],a[188:191],v[28:31],a[124:127]
MM a[116:119],a[188:191],v[20:23],a[116:119]
MM a[116:119],a[184:187],v[16:19],a[116:119]
MM a[108:111],a[184:187],v[8:11],a[108:111]
MM a[108:111],a[188:191],v[12:15],a[108:111]
MM a[100:103],a[188:191],v[4:7],a[100:103]
MM a[100:103],a[184:187],v[0:3],a[100:103]
v_smfmac_f32_16x16x64_bf16 v[236:239], v[144:147], v[64:71], v234
v_smfmac_f32_16x16x64_bf16 v[236:239], v[148:151], v[72:79], v234
v_smfmac_f32_16x16x64_bf16 v[236:239], v[152:155], v[80:87], v234
v_smfmac_f32_16x16x64_bf16 v[236:239], v[156:159], v[88:95], v234
s_waitcnt lgkmcnt(0)
MM v[0:3],a[192:195],v[160:163],v[128:131]
s_add_u32 s64, s64, 16384
TR a[128:129],v232 offset:16384
s_add_u32 s81, s64, 896
MM v[0:3],a[196:199],v[176:179],v[0:3]
TR a[130:131],v232 offset:20480
TR a[132:133],v232 offset:24576
MM v[0:3],a[200:203],v[192:195],v[0:3]
TR a[134:135],v232 offset:28672
TR a[136:137],v233 offset:16384
MM v[0:3],a[204:207],v[208:211],v[0:3]
TR a[138:139],v233 offset:20480
v_add_f32 v247, v236, v237
MM v[4:7],a[220:223],v[208:211],v[128:131]
TR a[140:141],v233 offset:24576
v_cmp_nge_f32 vcc, s71, v247
MM v[4:7],a[216:219],v[192:195],v[4:7]
s_cbranch_vccnz LSITE13_%=
LBACK13_%=:
TR a[142:143],v233 offset:28672
TR a[144:145],v232 offset:16896
MM v[4:7],a[212:215],v[176:179],v[4:7]
TR a[146:147],v232 offset:20992
TR a[148:149],v232 offset:25088
MM v[4:7],a[208:211],v[160:163],v[4:7]
TR a[150:151],v232 offset:29184
TR a[152:153],v233 offset:16896
MM v[12:15],a[208:211],v[164:167],v[132:135]
EX v0,v0
MM v[12:15],a[212:215],v[180:183],v[12:15]
EX v1,v1
MM v[12:15],a[216:219],v[196:199],v[12:15]
TR a[154:155],v233 offset:20992
CV v0,v0,v1
MM v[12:15],a[220:223],v[212:215],v[12:15]
EX v2,v2
MM v[8:11],a[204:207],v[212:215],v[132:135]
EX v3,v3
MM v[8:11],a[200:203],v[196:199],v[8:11]
TR a[156:157],v233 offset:25088
CV v1,v2,v3
MM v[8:11],a[196:199],v[180:183],v[8:11]
EX v4,v4
MM v[8:11],a[192:195],v[164:167],v[8:11]
EX v5,v5
MM v[16:19],a[192:195],v[168:171],v[136:139]
TR a[158:159],v233 offset:29184
CV v2,v4,v5
MM v[16:19],a[196:199],v[184:187],v[16:19]
EX v6,v6
MM v[16:19],a[200:203],v[200:203],v[16:19]
EX v7,v7
MM v[16:19],a[204:207],v[216:219],v[16:19]
TR a[160:161],v232 offset:17408
CV v3,v6,v7
MM v[20:23],a[220:223],v[216:219],v[136:139]
EX v8,v8
MM v[20:23],a[216:219],v[200:203],v[20:23]
EX v9,v9
MM v[20:23],a[212:215],v[184:187],v[20:23]
TR a[162:163],v232 offset:21504
CV v8,v8,v9
MM v[20:23],a[208:211],v[168:171],v[20:23]
EX v10,v10
MM v[28:31],a[208:211],v[172:175],v[140:143]
s_add_u32 m0, s45, 147456
s_add_u32 s44, s44, 1024
buffer_load_dwordx4 v248, s[24:27], s44 offen offset:0 lds
MM v[28:31],a[212:215],v[188:191],v[28:31]
TR a[164:165],v232 offset:25600
TR a[166:167],v232 offset:29696
MM v[28:31],a[216:219],v[204:207],v[28:31]
EX v11,v11
MM v[28:31],a[220:223],v[220:223],v[28:31]
TR a[168:169],v233 offset:17408
CV v9,v10,v11
MM v[24:27],a[204:207],v[220:223],v[140:143]
EX v12,v12
MM v[24:27],a[200:203],v[204:207],v[24:27]
EX v13,v13
MM v[24:27],a[196:199],v[188:191],v[24:27]
TR a[170:171],v233 offset:21504
CV v10,v12,v13
MM v[24:27],a[192:195],v[172:175],v[24:27]
EX v14,v14
MM v[32:35],a[224:227],v[160:163],v[128:131]
TR a[172:173],v233 offset:25600
TR a[174:175],v233 offset:29696
MM v[32:35],a[228:231],v[176:179],v[32:35]
EX v15,v15
MM v[32:35],a[232:235],v[192:195],v[32:35]
TR a[176:177],v232 offset:17920
CV v11,v14,v15
MM v[32:35],a[236:239],v[208:211],v[32:35]
EX v16,v16
MM v[36:39],a[252:255],v[208:211],v[128:131]
EX v17,v17
MM v[36:39],a[248:251],v[192:195],v[36:39]
TR a[178:179],v232 offset:22016
CV v16,v16,v17
MM v[36:39],a[244:247],v[176:179],v[36:39]
EX v18,v18
MM v[36:39],a[240:243],v[160:163],v[36:39]
TR a[180:181],v232 offset:26112
TR a[182:183],v232 offset:30208
MM v[44:47],a[240:243],v[164:167],v[132:135]
v_bfe_u32 v253, v248, 4, 4
v_lshlrev_b32 v253, 2, v253
MM v[44:47],a[244:247],v[180:183],v[44:47]
s_and_b32 s76, s48, 0xffffffc0
EX v19,v19
MM v[44:47],a[248:251],v[196:199],v[44:47]
s_add_u32 s76, s76, 77824
buffer_load_dword v252, v253, s[20:23], s76 offen offset:0 sc1
MM v[44:47],a[252:255],v[212:215],v[44:47]
TR a[184:185],v233 offset:17920
CV v17,v18,v19
MM v[40:43],a[236:239],v[212:215],v[132:135]
EX v20,v20
MM v[40:43],a[232:235],v[196:199],v[40:43]
EX v21,v21
MM v[40:43],a[228:231],v[180:183],v[40:43]
TR a[186:187],v233 offset:22016
CV v18,v20,v21
MM v[40:43],a[224:227],v[164:167],v[40:43]
EX v22,v22
MM v[48:51],a[224:227],v[168:171],v[136:139]
TR a[188:189],v233 offset:26112
TR a[190:191],v233 offset:30208
MM v[48:51],a[228:231],v[184:187],v[48:51]
EX v23,v23
MM v[48:51],a[232:235],v[200:203],v[48:51]
CV v19,v22,v23
MM v[48:51],a[236:239],v[216:219],v[48:51]
EX v24,v24
MM v[52:55],a[252:255],v[216:219],v[136:139]
EX v25,v25
MM v[52:55],a[248:251],v[200:203],v[52:55]
EX v26,v26
MM v[52:55],a[244:247],v[184:187],v[52:55]
EX v27,v27
MM v[52:55],a[240:243],v[168:171],v[52:55]
EX v28,v28
MM v[60:63],a[240:243],v[172:175],v[140:143]
EX v29,v29
MM v[60:63],a[244:247],v[188:191],v[60:63]
EX v30,v30
MM v[60:63],a[248:251],v[204:207],v[60:63]
EX v31,v31
MM v[60:63],a[252:255],v[220:223],v[60:63]
EX v32,v32
MM v[56:59],a[236:239],v[220:223],v[140:143]
EX v33,v33
MM v[56:59],a[232:235],v[204:207],v[56:59]
EX v34,v34
MM v[56:59],a[228:231],v[188:191],v[56:59]
EX v35,v35
MM v[56:59],a[224:227],v[172:175],v[56:59]
s_waitcnt vmcnt(17) lgkmcnt(0)
s_barrier
MM a[0:3],a[128:131],v[64:67],a[0:3]
s_lshl_b32 s60, s50, 2
CV v24,v24,v25
CV v25,v26,v27
MM a[0:3],a[132:135],v[68:71],a[0:3]
s_add_u32 s60, s60, %[swave]
RD a[192:195],v224 offset:49152
CV v26,v28,v29
MM a[8:11],a[132:135],v[76:79],a[8:11]
s_lshl_b32 s60, s60, 2
s_add_u32 m0, s65, 16384
CV v27,v30,v31
MM a[8:11],a[128:131],v[72:75],a[8:11]
s_bfe_u32 s62, s49, 65538
buffer_load_dwordx4 v228, s[8:11], s64 offen offset:0 lds
MM a[16:19],a[128:131],v[80:83],a[16:19]
s_add_u32 s93, s60, s62
RD a[196:199],v225 offset:49152
CV v4,v32,v33
MM a[16:19],a[132:135],v[84:87],a[16:19]
s_lshl_b32 s93, s93, 2
RD a[200:203],v226 offset:49152
CV v5,v34,v35
MM a[24:27],a[132:135],v[92:95],a[24:27]
s_lshr_b32 s61, s49, 3
RD a[204:207],v227 offset:49152
MM a[24:27],a[128:131],v[88:91],a[24:27]
s_add_u32 s93, s93, s61
buffer_load_dwordx4 v229, s[8:11], s64 offen offset:1024 lds
MM a[28:31],a[136:139],v[88:91],a[28:31]
s_mul_i32 s18, s93, 5120
RD a[208:211],v224 offset:53248
MM a[28:31],a[140:143],v[92:95],a[28:31]
s_mul_i32 s60, s84, 320
EX v36,v36
MM a[20:23],a[140:143],v[84:87],a[20:23]
s_add_u32 s18, s18, s60
buffer_load_dwordx4 v230, s[8:11], s64 offen offset:2048 lds
MM a[20:23],a[136:139],v[80:83],a[20:23]
s_and_b32 s60, s84, 1
EX v37,v37
MM a[12:15],a[136:139],v[72:75],a[12:15]
s_xor_b32 s60, s60, 1
RD a[212:215],v225 offset:53248
CV v6,v36,v37
MM a[12:15],a[140:143],v[76:79],a[12:15]
s_lshl_b32 s60, s60, 8
EX v38,v38
MM a[4:7],a[140:143],v[68:71],a[4:7]
s_add_u32 s63, s18, s60
RD a[216:219],v226 offset:53248
MM a[4:7],a[136:139],v[64:67],a[4:7]
s_and_b32 s60, s84, 1
EX v39,v39
MM a[32:35],a[144:147],v[64:67],a[32:35]
s_lshl_b32 s60, s60, 6
RD a[220:223],v227 offset:53248
CV v7,v38,v39
MM a[32:35],a[148:151],v[68:71],a[32:35]
s_add_u32 s18, s18, s60
EX v40,v40
MM a[40:43],a[148:151],v[76:79],a[40:43]
s_lshl_b32 s60, s50, 2
EX v41,v41
MM a[40:43],a[144:147],v[72:75],a[40:43]
s_add_u32 s60, s60, s84
RD a[224:227],v224 offset:57344
CV v12,v40,v41
MM a[48:51],a[144:147],v[80:83],a[48:51]
s_mul_i32 s16, s60, 663552
buffer_load_dwordx4 v231, s[8:11], s64 offen offset:3072 lds
MM a[48:51],a[148:151],v[84:87],a[48:51]
s_lshl_b32 s62, s62, 2
EX v42,v42
MM a[56:59],a[148:151],v[92:95],a[56:59]
)ASM"
R"ASM(
s_add_u32 s62, s62, s61
EX v43,v43
MM a[56:59],a[144:147],v[88:91],a[56:59]
s_lshl_b32 s62, s62, 8
s_add_u32 m0, s65, 81920
CV v13,v42,v43
MM a[60:63],a[152:155],v[88:91],a[60:63]
s_add_u32 s16, s16, s62
EX v44,v44
MM a[60:63],a[156:159],v[92:95],a[60:63]
s_mul_i32 s60, %[swave], 20480
RD a[228:231],v225 offset:57344
RD a[232:235],v226 offset:57344
MM a[52:55],a[156:159],v[84:87],a[52:55]
s_add_u32 s16, s16, s60
EX v45,v45
MM a[52:55],a[152:155],v[80:83],a[52:55]
s_lshr_b32 s61, s49, 4
RD a[236:239],v227 offset:57344
CV v14,v44,v45
MM a[44:47],a[152:155],v[72:75],a[44:47]
s_mul_i32 s61, s61, 331776
EX v46,v46
MM a[44:47],a[156:159],v[76:79],a[44:47]
s_add_u32 s16, s16, s61
buffer_load_dwordx4 v235, s[12:15], s81 offen offset:0 lds
MM a[36:39],a[156:159],v[68:71],a[36:39]
s_add_u32 s16, s16, 22708224
EX v47,v47
MM a[36:39],a[152:155],v[64:67],a[36:39]
s_mov_b32 s17, 31
CV v15,v46,v47
MM a[64:67],a[160:163],v[64:67],a[64:67]
s_mov_b32 s72, 0
EX v48,v48
MM a[64:67],a[164:167],v[68:71],a[64:67]
s_lshl_b32 s60, s50, 2
EX v49,v49
MM a[72:75],a[164:167],v[76:79],a[72:75]
s_add_u32 s60, s60, s84
RD a[240:243],v224 offset:61440
CV v20,v48,v49
MM a[72:75],a[160:163],v[72:75],a[72:75]
s_lshl_b32 s60, s60, 2
buffer_load_dwordx4 v235, s[12:15], s64 offen offset:1024 lds
MM a[80:83],a[160:163],v[80:83],a[80:83]
s_add_u32 s60, s60, %[swave]
EX v50,v50
MM a[80:83],a[164:167],v[84:87],a[80:83]
s_lshl_b32 s60, s60, 5
EX v51,v51
MM a[88:91],a[164:167],v[92:95],a[88:91]
s_and_b32 s61, s49, 28
CV v21,v50,v51
MM a[88:91],a[160:163],v[88:91],a[88:91]
s_add_u32 s19, s60, s61
EX v52,v52
MM a[92:95],a[168:171],v[88:91],a[92:95]
s_lshl_b32 s19, s19, 2
EX v53,v53
MM a[92:95],a[172:175],v[92:95],a[92:95]
s_add_u32 s19, s19, 16384
RD a[244:247],v225 offset:61440
CV v22,v52,v53
MM a[84:87],a[172:175],v[84:87],a[84:87]
s_and_b32 s68, s19, 0xffffffc0
buffer_load_dwordx4 v235, s[12:15], s81 offen offset:2048 lds
MM a[84:87],a[168:171],v[80:83],a[84:87]
s_mov_b32 s82, 0
EX v54,v54
MM a[76:79],a[168:171],v[72:75],a[76:79]
s_lshr_b32 s61, s52, 2
EX v55,v55
MM a[76:79],a[172:175],v[76:79],a[76:79]
s_sub_u32 s61, s61, s93
s_cmp_eq_u32 s61, 0
s_cselect_b32 s60, 1, 0
MM a[68:71],a[172:175],v[68:71],a[68:71]
s_or_b32 s82, s82, s60
s_cmp_eq_u32 s61, 8
s_nop 0
LPG12_%=:
s_cbranch_execz LTR0_%=
s_cselect_b32 s60, 256, 0
MM a[68:71],a[168:171],v[64:67],a[68:71]
s_or_b32 s82, s82, s60
RD a[248:251],v226 offset:61440
CV v23,v54,v55
MM a[96:99],a[176:179],v[64:67],a[96:99]
s_lshr_b32 s61, s53, 2
EX v56,v56
MM a[96:99],a[180:183],v[68:71],a[96:99]
s_sub_u32 s61, s61, s93
s_cmp_eq_u32 s61, 0
s_cselect_b32 s60, 2, 0
MM a[104:107],a[180:183],v[76:79],a[104:107]
s_or_b32 s82, s82, s60
s_cmp_eq_u32 s61, 8
s_cselect_b32 s60, 512, 0
MM a[104:107],a[176:179],v[72:75],a[104:107]
s_or_b32 s82, s82, s60
EX v57,v57
MM a[112:115],a[176:179],v[80:83],a[112:115]
s_lshr_b32 s61, s54, 2
CV v28,v56,v57
MM a[112:115],a[180:183],v[84:87],a[112:115]
s_sub_u32 s61, s61, s93
s_cmp_eq_u32 s61, 0
s_cselect_b32 s60, 4, 0
MM a[120:123],a[180:183],v[92:95],a[120:123]
s_or_b32 s82, s82, s60
s_cmp_eq_u32 s61, 8
s_cselect_b32 s60, 1024, 0
MM a[120:123],a[176:179],v[88:91],a[120:123]
s_or_b32 s82, s82, s60
EX v58,v58
MM a[124:127],a[184:187],v[88:91],a[124:127]
s_lshr_b32 s61, s55, 2
EX v59,v59
MM a[124:127],a[188:191],v[92:95],a[124:127]
s_sub_u32 s61, s61, s93
s_cmp_eq_u32 s61, 0
s_cselect_b32 s60, 8, 0
MM a[116:119],a[188:191],v[84:87],a[116:119]
s_or_b32 s82, s82, s60
s_cmp_eq_u32 s61, 8
s_cselect_b32 s60, 2048, 0
MM a[116:119],a[184:187],v[80:83],a[116:119]
s_or_b32 s82, s82, s60
EX v60,v60
MM a[108:111],a[184:187],v[72:75],a[108:111]
s_lshr_b32 s61, s56, 2
EX v61,v61
MM a[108:111],a[188:191],v[76:79],a[108:111]
s_sub_u32 s61, s61, s93
s_cmp_eq_u32 s61, 0
s_cselect_b32 s60, 16, 0
MM a[100:103],a[188:191],v[68:71],a[100:103]
s_or_b32 s82, s82, s60
s_cmp_eq_u32 s61, 8
s_cselect_b32 s60, 4096, 0
MM a[100:103],a[184:187],v[64:67],a[100:103]
s_or_b32 s82, s82, s60
EX v62,v62
v_smfmac_f32_16x16x64_bf16 v[236:239], v[144:147], v[0:7], v234
s_lshr_b32 s61, s57, 2
RD a[252:255],v227 offset:61440
EX v63,v63
v_smfmac_f32_16x16x64_bf16 v[236:239], v[148:151], v[8:15], v234
s_sub_u32 s61, s61, s93
CV v29,v58,v59
v_smfmac_f32_16x16x64_bf16 v[236:239], v[152:155], v[16:23], v234
CV v30,v60,v61
CV v31,v62,v63
s_nop 1
v_smfmac_f32_16x16x64_bf16 v[236:239], v[156:159], v[24:31], v234
s_waitcnt lgkmcnt(0)
buffer_load_dwordx4 v235, s[12:15], s64 offen offset:3072 lds
s_cmp_eq_u32 s61, 0
s_cselect_b32 s60, 32, 0
MM v[64:67],a[192:195],v[160:163],v[128:131]
s_or_b32 s82, s82, s60
s_cmp_eq_u32 s61, 8
s_cselect_b32 s60, 8192, 0
MM v[64:67],a[196:199],v[176:179],v[64:67]
s_or_b32 s82, s82, s60
TR a[128:129],v232 offset:32768
TR a[130:131],v232 offset:36864
MM v[64:67],a[200:203],v[192:195],v[64:67]
s_lshr_b32 s61, s58, 2
s_add_u32 s64, s64, 16384
s_add_u32 s81, s64, 896
MM v[64:67],a[204:207],v[208:211],v[64:67]
s_sub_u32 s61, s61, s93
TR a[132:133],v232 offset:40960
v_add_f32 v246, v236, v237
MM v[68:71],a[220:223],v[208:211],v[128:131]
s_cmp_eq_u32 s61, 0
s_cselect_b32 s60, 64, 0
v_cmp_nge_f32 vcc, s71, v246
MM v[68:71],a[216:219],v[192:195],v[68:71]
s_cbranch_vccnz LSITE14_%=
LBACK14_%=:
TR a[134:135],v232 offset:45056
TR a[136:137],v233 offset:32768
MM v[68:71],a[212:215],v[176:179],v[68:71]
s_or_b32 s82, s82, s60
s_cmp_eq_u32 s61, 8
s_cselect_b32 s60, 16384, 0
MM v[68:71],a[208:211],v[160:163],v[68:71]
s_or_b32 s82, s82, s60
TR a[138:139],v233 offset:36864
TR a[140:141],v233 offset:40960
MM v[76:79],a[208:211],v[164:167],v[132:135]
s_lshr_b32 s61, s59, 2
EX v64,v64
MM v[76:79],a[212:215],v[180:183],v[76:79]
s_sub_u32 s61, s61, s93
s_cmp_eq_u32 s61, 0
s_cselect_b32 s60, 128, 0
MM v[76:79],a[216:219],v[196:199],v[76:79]
s_or_b32 s82, s82, s60
s_cmp_eq_u32 s61, 8
s_cselect_b32 s60, 32768, 0
MM v[76:79],a[220:223],v[212:215],v[76:79]
s_or_b32 s82, s82, s60
s_and_b32 s60, s82, 255
s_cselect_b32 s51, 0, 31
MM v[72:75],a[204:207],v[212:215],v[132:135]
EX v65,v65
MM v[72:75],a[200:203],v[196:199],v[72:75]
EX v66,v66
MM v[72:75],a[196:199],v[180:183],v[72:75]
TR a[142:143],v233 offset:45056
CV v64,v64,v65
MM v[72:75],a[192:195],v[164:167],v[72:75]
EX v67,v67
MM v[80:83],a[192:195],v[168:171],v[136:139]
TR a[144:145],v232 offset:33280
CV v65,v66,v67
MM v[80:83],a[196:199],v[184:187],v[80:83]
EX v68,v68
MM v[80:83],a[200:203],v[200:203],v[80:83]
TR a[146:147],v232 offset:37376
TR a[148:149],v232 offset:41472
MM v[80:83],a[204:207],v[216:219],v[80:83]
EX v69,v69
MM v[84:87],a[220:223],v[216:219],v[136:139]
EX v70,v70
MM v[84:87],a[216:219],v[200:203],v[84:87]
TR a[150:151],v232 offset:45568
CV v66,v68,v69
MM v[84:87],a[212:215],v[184:187],v[84:87]
EX v71,v71
MM v[84:87],a[208:211],v[168:171],v[84:87]
TR a[152:153],v233 offset:33280
CV v67,v70,v71
MM v[92:95],a[208:211],v[172:175],v[140:143]
EX v72,v72
MM v[92:95],a[212:215],v[188:191],v[92:95]
TR a[154:155],v233 offset:37376
TR a[156:157],v233 offset:41472
MM v[92:95],a[216:219],v[204:207],v[92:95]
EX v73,v73
MM v[92:95],a[220:223],v[220:223],v[92:95]
EX v74,v74
MM v[88:91],a[204:207],v[220:223],v[140:143]
TR a[158:159],v233 offset:45568
CV v72,v72,v73
MM v[88:91],a[200:203],v[204:207],v[88:91]
EX v75,v75
MM v[88:91],a[196:199],v[188:191],v[88:91]
TR a[160:161],v232 offset:33792
CV v73,v74,v75
MM v[88:91],a[192:195],v[172:175],v[88:91]
EX v76,v76
MM v[96:99],a[224:227],v[160:163],v[128:131]
TR a[162:163],v232 offset:37888
TR a[164:165],v232 offset:41984
MM v[96:99],a[228:231],v[176:179],v[96:99]
EX v77,v77
MM v[96:99],a[232:235],v[192:195],v[96:99]
TR a[166:167],v232 offset:46080
CV v74,v76,v77
MM v[96:99],a[236:239],v[208:211],v[96:99]
EX v78,v78
MM v[100:103],a[252:255],v[208:211],v[128:131]
TR a[168:169],v233 offset:33792
TR a[170:171],v233 offset:37888
MM v[100:103],a[248:251],v[192:195],v[100:103]
EX v79,v79
MM v[100:103],a[244:247],v[176:179],v[100:103]
TR a[172:173],v233 offset:41984
CV v75,v78,v79
MM v[100:103],a[240:243],v[160:163],v[100:103]
EX v80,v80
MM v[108:111],a[240:243],v[164:167],v[132:135]
EX v81,v81
MM v[108:111],a[244:247],v[180:183],v[108:111]
TR a[174:175],v233 offset:46080
CV v80,v80,v81
MM v[108:111],a[248:251],v[196:199],v[108:111]
EX v82,v82
MM v[108:111],a[252:255],v[212:215],v[108:111]
TR a[176:177],v232 offset:34304
TR a[178:179],v232 offset:38400
MM v[104:107],a[236:239],v[212:215],v[132:135]
EX v83,v83
MM v[104:107],a[232:235],v[196:199],v[104:107]
TR a[180:181],v232 offset:42496
CV v81,v82,v83
MM v[104:107],a[228:231],v[180:183],v[104:107]
EX v84,v84
MM v[104:107],a[224:227],v[164:167],v[104:107]
EX v85,v85
MM v[112:115],a[224:227],v[168:171],v[136:139]
TR a[182:183],v232 offset:46592
CV v82,v84,v85
MM v[112:115],a[228:231],v[184:187],v[112:115]
EX v86,v86
MM v[112:115],a[232:235],v[200:203],v[112:115]
TR a[184:185],v233 offset:34304
TR a[186:187],v233 offset:38400
MM v[112:115],a[236:239],v[216:219],v[112:115]
EX v87,v87
MM v[116:119],a[252:255],v[216:219],v[136:139]
TR a[188:189],v233 offset:42496
CV v83,v86,v87
MM v[116:119],a[248:251],v[200:203],v[116:119]
EX v88,v88
MM v[116:119],a[244:247],v[184:187],v[116:119]
EX v89,v89
MM v[116:119],a[240:243],v[168:171],v[116:119]
TR a[190:191],v233 offset:46592
CV v88,v88,v89
MM v[124:127],a[240:243],v[172:175],v[140:143]
EX v90,v90
MM v[124:127],a[244:247],v[188:191],v[124:127]
EX v91,v91
MM v[124:127],a[248:251],v[204:207],v[124:127]
EX v92,v92
MM v[124:127],a[252:255],v[220:223],v[124:127]
EX v93,v93
MM v[120:123],a[236:239],v[220:223],v[140:143]
EX v94,v94
MM v[120:123],a[232:235],v[204:207],v[120:123]
EX v95,v95
MM v[120:123],a[228:231],v[188:191],v[120:123]
EX v96,v96
MM v[120:123],a[224:227],v[172:175],v[120:123]
s_waitcnt vmcnt(14) lgkmcnt(0)
s_barrier
MM a[0:3],a[128:131],v[0:3],a[0:3]
s_waitcnt vmcnt(8)
v_cmp_ne_u32 vcc, s69, v252
s_cbranch_vccnz LRFG_%=
LRFGb_%=:
MM a[0:3],a[132:135],v[4:7],a[0:3]
s_add_u32 m0, s65, 32768
v_lshrrev_b32 v253, 8, v248
v_lshlrev_b32 v253, 10, v253
MM a[8:11],a[132:135],v[12:15],a[8:11]
v_and_b32 v252, 240, v248
v_add_u32 v253, v253, v252
MM a[8:11],a[128:131],v[8:11],a[8:11]
s_lshr_b32 s76, s48, 6
CV v89,v90,v91
CV v90,v92,v93
MM a[16:19],a[128:131],v[16:19],a[16:19]
s_lshl_b32 s76, s76, 13
RD a[192:195],v224 offset:0
CV v91,v94,v95
MM a[16:19],a[132:135],v[20:23],a[16:19]
s_bfe_u32 s90, s48, 131076
buffer_load_dwordx4 v228, s[8:11], s64 offen offset:0 lds
MM a[24:27],a[132:135],v[28:31],a[24:27]
s_lshl_b32 s90, s90, 8
EX v97,v97
MM a[24:27],a[128:131],v[24:27],a[24:27]
s_add_u32 s76, s76, s90
RD a[196:199],v225 offset:0
CV v68,v96,v97
MM a[28:31],a[136:139],v[24:27],a[28:31]
s_add_u32 s76, s76, 22183936
EX v98,v98
MM a[28:31],a[140:143],v[28:31],a[28:31]
s_lshl_b32 s90, %[swave], 11
RD a[200:203],v226 offset:0
MM a[20:23],a[140:143],v[20:23],a[20:23]
s_add_u32 s90, s90, %[slds]
s_add_u32 m0, s90, 131072
s_nop 0
buffer_load_dwordx4 v253, s[20:23], s76 offen offset:0 sc1 lds
MM a[20:23],a[136:139],v[16:19],a[20:23]
s_add_u32 m0, s90, 132096
s_add_u32 s76, s76, 4096
buffer_load_dwordx4 v253, s[20:23], s76 offen offset:0 sc1 lds
MM a[12:15],a[136:139],v[8:11],a[12:15]
EX v99,v99
MM a[12:15],a[140:143],v[12:15],a[12:15]
EX v100,v100
MM a[4:7],a[140:143],v[4:7],a[4:7]
EX v101,v101
MM a[4:7],a[136:139],v[0:3],a[4:7]
RD a[204:207],v227 offset:0
CV v69,v98,v99
MM a[32:35],a[144:147],v[0:3],a[32:35]
EX v102,v102
MM a[32:35],a[148:151],v[4:7],a[32:35]
s_add_u32 m0, s65, 32768
s_nop 0
buffer_load_dwordx4 v229, s[8:11], s64 offen offset:1024 lds
MM a[40:43],a[148:151],v[12:15],a[40:43]
EX v103,v103
MM a[40:43],a[144:147],v[8:11],a[40:43]
RD a[208:211],v224 offset:4096
CV v70,v100,v101
MM a[48:51],a[144:147],v[16:19],a[48:51]
EX v104,v104
MM a[48:51],a[148:151],v[20:23],a[48:51]
RD a[212:215],v225 offset:4096
CV v71,v102,v103
MM a[56:59],a[148:151],v[28:31],a[56:59]
EX v105,v105
MM a[56:59],a[144:147],v[24:27],a[56:59]
buffer_load_dwordx4 v230, s[8:11], s64 offen offset:2048 lds
MM a[60:63],a[152:155],v[24:27],a[60:63]
EX v106,v106
MM a[60:63],a[156:159],v[28:31],a[60:63]
EX v107,v107
MM a[52:55],a[156:159],v[20:23],a[52:55]
)ASM"
R"ASM(
RD a[216:219],v226 offset:4096
CV v76,v104,v105
MM a[52:55],a[152:155],v[16:19],a[52:55]
EX v108,v108
MM a[44:47],a[152:155],v[8:11],a[44:47]
RD a[220:223],v227 offset:4096
CV v77,v106,v107
MM a[44:47],a[156:159],v[12:15],a[44:47]
EX v109,v109
MM a[36:39],a[156:159],v[4:7],a[36:39]
buffer_load_dwordx4 v231, s[8:11], s64 offen offset:3072 lds
MM a[36:39],a[152:155],v[0:3],a[36:39]
s_add_u32 m0, s65, 98304
EX v110,v110
MM a[64:67],a[160:163],v[0:3],a[64:67]
RD a[224:227],v224 offset:8192
CV v78,v108,v109
MM a[64:67],a[164:167],v[4:7],a[64:67]
EX v111,v111
MM a[72:75],a[164:167],v[12:15],a[72:75]
EX v112,v112
MM a[72:75],a[160:163],v[8:11],a[72:75]
RD a[228:231],v225 offset:8192
CV v79,v110,v111
MM a[80:83],a[160:163],v[16:19],a[80:83]
EX v113,v113
MM a[80:83],a[164:167],v[20:23],a[80:83]
buffer_load_dwordx4 v235, s[12:15], s81 offen offset:0 lds
MM a[88:91],a[164:167],v[28:31],a[88:91]
EX v114,v114
MM a[88:91],a[160:163],v[24:27],a[88:91]
RD a[232:235],v226 offset:8192
CV v84,v112,v113
MM a[92:95],a[168:171],v[24:27],a[92:95]
EX v115,v115
MM a[92:95],a[172:175],v[28:31],a[92:95]
RD a[236:239],v227 offset:8192
CV v85,v114,v115
MM a[84:87],a[172:175],v[20:23],a[84:87]
EX v116,v116
MM a[84:87],a[168:171],v[16:19],a[84:87]
EX v117,v117
MM a[76:79],a[168:171],v[8:11],a[76:79]
RD a[240:243],v224 offset:12288
CV v86,v116,v117
MM a[76:79],a[172:175],v[12:15],a[76:79]
EX v118,v118
MM a[68:71],a[172:175],v[4:7],a[68:71]
buffer_load_dwordx4 v235, s[12:15], s64 offen offset:1024 lds
MM a[68:71],a[168:171],v[0:3],a[68:71]
EX v119,v119
MM a[96:99],a[176:179],v[0:3],a[96:99]
RD a[244:247],v225 offset:12288
CV v87,v118,v119
MM a[96:99],a[180:183],v[4:7],a[96:99]
EX v120,v120
MM a[104:107],a[180:183],v[12:15],a[104:107]
EX v121,v121
MM a[104:107],a[176:179],v[8:11],a[104:107]
RD a[248:251],v226 offset:12288
CV v92,v120,v121
MM a[112:115],a[176:179],v[16:19],a[112:115]
buffer_load_dwordx4 v235, s[12:15], s81 offen offset:2048 lds
MM a[112:115],a[180:183],v[20:23],a[112:115]
EX v122,v122
MM a[120:123],a[180:183],v[28:31],a[120:123]
EX v123,v123
MM a[120:123],a[176:179],v[24:27],a[120:123]
RD a[252:255],v227 offset:12288
CV v93,v122,v123
MM a[124:127],a[184:187],v[24:27],a[124:127]
EX v124,v124
MM a[124:127],a[188:191],v[28:31],a[124:127]
EX v125,v125
MM a[116:119],a[188:191],v[20:23],a[116:119]
buffer_load_dwordx4 v235, s[12:15], s64 offen offset:3072 lds
MM a[116:119],a[184:187],v[16:19],a[116:119]
EX v126,v126
MM a[108:111],a[184:187],v[8:11],a[108:111]
EX v127,v127
MM a[108:111],a[188:191],v[12:15],a[108:111]
CV v94,v124,v125
CV v95,v126,v127
MM a[100:103],a[188:191],v[4:7],a[100:103]
MM a[100:103],a[184:187],v[0:3],a[100:103]
v_smfmac_f32_16x16x64_bf16 v[236:239], v[144:147], v[64:71], v234
v_smfmac_f32_16x16x64_bf16 v[236:239], v[148:151], v[72:79], v234
v_smfmac_f32_16x16x64_bf16 v[236:239], v[152:155], v[80:87], v234
v_smfmac_f32_16x16x64_bf16 v[236:239], v[156:159], v[88:95], v234
s_waitcnt lgkmcnt(0)
MM v[0:3],a[192:195],v[160:163],v[128:131]
s_add_u32 s64, s64, 16384
TR a[128:129],v232 offset:49152
s_add_u32 s81, s64, 896
MM v[0:3],a[196:199],v[176:179],v[0:3]
TR a[130:131],v232 offset:53248
TR a[132:133],v232 offset:57344
MM v[0:3],a[200:203],v[192:195],v[0:3]
TR a[134:135],v232 offset:61440
TR a[136:137],v233 offset:49152
MM v[0:3],a[204:207],v[208:211],v[0:3]
TR a[138:139],v233 offset:53248
v_add_f32 v247, v236, v237
MM v[4:7],a[220:223],v[208:211],v[128:131]
TR a[140:141],v233 offset:57344
v_cmp_nge_f32 vcc, s71, v247
MM v[4:7],a[216:219],v[192:195],v[4:7]
s_cbranch_vccnz LSITE15_%=
LBACK15_%=:
TR a[142:143],v233 offset:61440
TR a[144:145],v232 offset:49664
MM v[4:7],a[212:215],v[176:179],v[4:7]
v_lshrrev_b32 v253, 7, v248
v_mul_u32_u24 v253, 1280, v253
MM v[4:7],a[208:211],v[160:163],v[4:7]
v_and_b32 v252, 112, v248
v_add_u32 v253, v253, v252
MM v[12:15],a[208:211],v[164:167],v[132:135]
s_mul_i32 s90, %[swave], 30720
EX v0,v0
MM v[12:15],a[212:215],v[180:183],v[12:15]
s_mul_i32 s91, %[swave], 3072
EX v1,v1
MM v[12:15],a[216:219],v[196:199],v[12:15]
s_add_u32 s91, s91, %[slds]
s_add_u32 m0, s91, 151552
s_add_u32 s77, s90, 0
buffer_load_dwordx4 v253, s[28:31], s77 offen offset:0 lds
MM v[12:15],a[220:223],v[212:215],v[12:15]
TR a[146:147],v232 offset:53760
CV v0,v0,v1
MM v[8:11],a[204:207],v[212:215],v[132:135]
EX v2,v2
MM v[8:11],a[200:203],v[196:199],v[8:11]
s_add_u32 m0, s91, 152576
s_add_u32 s77, s90, 10240
buffer_load_dwordx4 v253, s[28:31], s77 offen offset:0 lds
MM v[8:11],a[196:199],v[180:183],v[8:11]
EX v3,v3
MM v[8:11],a[192:195],v[164:167],v[8:11]
TR a[148:149],v232 offset:57856
CV v1,v2,v3
MM v[16:19],a[192:195],v[168:171],v[136:139]
s_add_u32 m0, s91, 153600
s_add_u32 s77, s90, 20480
buffer_load_dwordx4 v253, s[28:31], s77 offen offset:0 lds
MM v[16:19],a[196:199],v[184:187],v[16:19]
EX v4,v4
MM v[16:19],a[200:203],v[200:203],v[16:19]
EX v5,v5
MM v[16:19],a[204:207],v[216:219],v[16:19]
TR a[150:151],v232 offset:61952
CV v2,v4,v5
MM v[20:23],a[220:223],v[216:219],v[136:139]
EX v6,v6
MM v[20:23],a[216:219],v[200:203],v[20:23]
TR a[152:153],v233 offset:49664
TR a[154:155],v233 offset:53760
MM v[20:23],a[212:215],v[184:187],v[20:23]
EX v7,v7
MM v[20:23],a[208:211],v[168:171],v[20:23]
TR a[156:157],v233 offset:57856
CV v3,v6,v7
MM v[28:31],a[208:211],v[172:175],v[140:143]
EX v8,v8
MM v[28:31],a[212:215],v[188:191],v[28:31]
EX v9,v9
MM v[28:31],a[216:219],v[204:207],v[28:31]
TR a[158:159],v233 offset:61952
CV v8,v8,v9
MM v[28:31],a[220:223],v[220:223],v[28:31]
EX v10,v10
MM v[24:27],a[204:207],v[220:223],v[140:143]
TR a[160:161],v232 offset:50176
TR a[162:163],v232 offset:54272
MM v[24:27],a[200:203],v[204:207],v[24:27]
EX v11,v11
MM v[24:27],a[196:199],v[188:191],v[24:27]
TR a[164:165],v232 offset:58368
CV v9,v10,v11
MM v[24:27],a[192:195],v[172:175],v[24:27]
EX v12,v12
MM v[32:35],a[224:227],v[160:163],v[128:131]
EX v13,v13
MM v[32:35],a[228:231],v[176:179],v[32:35]
TR a[166:167],v232 offset:62464
CV v10,v12,v13
MM v[32:35],a[232:235],v[192:195],v[32:35]
EX v14,v14
MM v[32:35],a[236:239],v[208:211],v[32:35]
TR a[168:169],v233 offset:50176
TR a[170:171],v233 offset:54272
MM v[36:39],a[252:255],v[208:211],v[128:131]
EX v15,v15
MM v[36:39],a[248:251],v[192:195],v[36:39]
TR a[172:173],v233 offset:58368
CV v11,v14,v15
MM v[36:39],a[244:247],v[176:179],v[36:39]
EX v16,v16
MM v[36:39],a[240:243],v[160:163],v[36:39]
EX v17,v17
MM v[44:47],a[240:243],v[164:167],v[132:135]
TR a[174:175],v233 offset:62464
CV v16,v16,v17
MM v[44:47],a[244:247],v[180:183],v[44:47]
EX v18,v18
MM v[44:47],a[248:251],v[196:199],v[44:47]
TR a[176:177],v232 offset:50688
TR a[178:179],v232 offset:54784
MM v[44:47],a[252:255],v[212:215],v[44:47]
EX v19,v19
MM v[40:43],a[236:239],v[212:215],v[132:135]
TR a[180:181],v232 offset:58880
CV v17,v18,v19
MM v[40:43],a[232:235],v[196:199],v[40:43]
EX v20,v20
MM v[40:43],a[228:231],v[180:183],v[40:43]
TR a[182:183],v232 offset:62976
TR a[184:185],v233 offset:50688
MM v[40:43],a[224:227],v[164:167],v[40:43]
EX v21,v21
MM v[48:51],a[224:227],v[168:171],v[136:139]
TR a[186:187],v233 offset:54784
CV v18,v20,v21
MM v[48:51],a[228:231],v[184:187],v[48:51]
EX v22,v22
MM v[48:51],a[232:235],v[200:203],v[48:51]
EX v23,v23
MM v[48:51],a[236:239],v[216:219],v[48:51]
TR a[188:189],v233 offset:58880
CV v19,v22,v23
MM v[52:55],a[252:255],v[216:219],v[136:139]
EX v24,v24
MM v[52:55],a[248:251],v[200:203],v[52:55]
EX v25,v25
MM v[52:55],a[244:247],v[184:187],v[52:55]
TR a[190:191],v233 offset:62976
CV v24,v24,v25
MM v[52:55],a[240:243],v[168:171],v[52:55]
EX v26,v26
MM v[60:63],a[240:243],v[172:175],v[140:143]
EX v27,v27
MM v[60:63],a[244:247],v[188:191],v[60:63]
EX v28,v28
MM v[60:63],a[248:251],v[204:207],v[60:63]
EX v29,v29
MM v[60:63],a[252:255],v[220:223],v[60:63]
EX v30,v30
MM v[56:59],a[236:239],v[220:223],v[140:143]
EX v31,v31
MM v[56:59],a[232:235],v[204:207],v[56:59]
EX v32,v32
MM v[56:59],a[228:231],v[188:191],v[56:59]
EX v33,v33
MM v[56:59],a[224:227],v[172:175],v[56:59]
s_waitcnt vmcnt(17) lgkmcnt(0)
s_barrier
MM a[0:3],a[128:131],v[64:67],a[0:3]
s_add_u32 m0, s65, 49152
CV v25,v26,v27
CV v26,v28,v29
MM a[0:3],a[132:135],v[68:71],a[0:3]
RD a[192:195],v224 offset:16384
CV v27,v30,v31
MM a[8:11],a[132:135],v[76:79],a[8:11]
buffer_load_dwordx4 v228, s[8:11], s64 offen offset:0 lds
MM a[8:11],a[128:131],v[72:75],a[8:11]
RD a[196:199],v225 offset:16384
CV v4,v32,v33
MM a[16:19],a[128:131],v[80:83],a[16:19]
RD a[200:203],v226 offset:16384
RD a[204:207],v227 offset:16384
MM a[16:19],a[132:135],v[84:87],a[16:19]
buffer_load_dwordx4 v229, s[8:11], s64 offen offset:1024 lds
MM a[24:27],a[132:135],v[92:95],a[24:27]
EX v34,v34
MM a[24:27],a[128:131],v[88:91],a[24:27]
RD a[208:211],v224 offset:20480
RD a[212:215],v225 offset:20480
MM a[28:31],a[136:139],v[88:91],a[28:31]
EX v35,v35
LPG13_%=:
s_nop 0
MM a[28:31],a[140:143],v[92:95],a[28:31]
RD a[216:219],v226 offset:20480
CV v5,v34,v35
MM a[20:23],a[140:143],v[84:87],a[20:23]
EX v36,v36
MM a[20:23],a[136:139],v[80:83],a[20:23]
EX v37,v37
MM a[12:15],a[136:139],v[72:75],a[12:15]
RD a[220:223],v227 offset:20480
CV v6,v36,v37
MM a[12:15],a[140:143],v[76:79],a[12:15]
buffer_load_dwordx4 v230, s[8:11], s64 offen offset:2048 lds
MM a[4:7],a[140:143],v[68:71],a[4:7]
EX v38,v38
MM a[4:7],a[136:139],v[64:67],a[4:7]
EX v39,v39
MM a[32:35],a[144:147],v[64:67],a[32:35]
RD a[224:227],v224 offset:24576
CV v7,v38,v39
MM a[32:35],a[148:151],v[68:71],a[32:35]
EX v40,v40
MM a[40:43],a[148:151],v[76:79],a[40:43]
EX v41,v41
MM a[40:43],a[144:147],v[72:75],a[40:43]
RD a[228:231],v225 offset:24576
CV v12,v40,v41
MM a[48:51],a[144:147],v[80:83],a[48:51]
buffer_load_dwordx4 v231, s[8:11], s64 offen offset:3072 lds
MM a[48:51],a[148:151],v[84:87],a[48:51]
s_add_u32 m0, s65, 114688
EX v42,v42
MM a[56:59],a[148:151],v[92:95],a[56:59]
EX v43,v43
MM a[56:59],a[144:147],v[88:91],a[56:59]
RD a[232:235],v226 offset:24576
CV v13,v42,v43
MM a[60:63],a[152:155],v[88:91],a[60:63]
EX v44,v44
MM a[60:63],a[156:159],v[92:95],a[60:63]
buffer_load_dwordx4 v235, s[12:15], s81 offen offset:0 lds
MM a[52:55],a[156:159],v[84:87],a[52:55]
EX v45,v45
MM a[52:55],a[152:155],v[80:83],a[52:55]
RD a[236:239],v227 offset:24576
CV v14,v44,v45
MM a[44:47],a[152:155],v[72:75],a[44:47]
EX v46,v46
MM a[44:47],a[156:159],v[76:79],a[44:47]
EX v47,v47
MM a[36:39],a[156:159],v[68:71],a[36:39]
RD a[240:243],v224 offset:28672
CV v15,v46,v47
MM a[36:39],a[152:155],v[64:67],a[36:39]
EX v48,v48
MM a[64:67],a[160:163],v[64:67],a[64:67]
buffer_load_dwordx4 v235, s[12:15], s64 offen offset:1024 lds
MM a[64:67],a[164:167],v[68:71],a[64:67]
EX v49,v49
MM a[72:75],a[164:167],v[76:79],a[72:75]
RD a[244:247],v225 offset:28672
CV v20,v48,v49
MM a[72:75],a[160:163],v[72:75],a[72:75]
EX v50,v50
MM a[80:83],a[160:163],v[80:83],a[80:83]
EX v51,v51
MM a[80:83],a[164:167],v[84:87],a[80:83]
RD a[248:251],v226 offset:28672
CV v21,v50,v51
MM a[88:91],a[164:167],v[92:95],a[88:91]
EX v52,v52
MM a[88:91],a[160:163],v[88:91],a[88:91]
buffer_load_dwordx4 v235, s[12:15], s81 offen offset:2048 lds
MM a[92:95],a[168:171],v[88:91],a[92:95]
EX v53,v53
MM a[92:95],a[172:175],v[92:95],a[92:95]
RD a[252:255],v227 offset:28672
CV v22,v52,v53
MM a[84:87],a[172:175],v[84:87],a[84:87]
EX v54,v54
MM a[84:87],a[168:171],v[80:83],a[84:87]
EX v55,v55
MM a[76:79],a[168:171],v[72:75],a[76:79]
CV v23,v54,v55
MM a[76:79],a[172:175],v[76:79],a[76:79]
EX v56,v56
MM a[68:71],a[172:175],v[68:71],a[68:71]
buffer_load_dwordx4 v235, s[12:15], s64 offen offset:3072 lds
MM a[68:71],a[168:171],v[64:67],a[68:71]
EX v57,v57
MM a[96:99],a[176:179],v[64:67],a[96:99]
EX v58,v58
MM a[96:99],a[180:183],v[68:71],a[96:99]
EX v59,v59
MM a[104:107],a[180:183],v[76:79],a[104:107]
EX v60,v60
MM a[104:107],a[176:179],v[72:75],a[104:107]
EX v61,v61
MM a[112:115],a[176:179],v[80:83],a[112:115]
EX v62,v62
MM a[112:115],a[180:183],v[84:87],a[112:115]
EX v63,v63
MM a[120:123],a[180:183],v[92:95],a[120:123]
CV v28,v56,v57
CV v29,v58,v59
MM a[120:123],a[176:179],v[88:91],a[120:123]
CV v30,v60,v61
CV v31,v62,v63
MM a[124:127],a[184:187],v[88:91],a[124:127]
MM a[124:127],a[188:191],v[92:95],a[124:127]
)ASM"
R"ASM(
MM a[116:119],a[188:191],v[84:87],a[116:119]
MM a[116:119],a[184:187],v[80:83],a[116:119]
MM a[108:111],a[184:187],v[72:75],a[108:111]
MM a[108:111],a[188:191],v[76:79],a[108:111]
MM a[100:103],a[188:191],v[68:71],a[100:103]
MM a[100:103],a[184:187],v[64:67],a[100:103]
v_smfmac_f32_16x16x64_bf16 v[236:239], v[144:147], v[0:7], v234
v_smfmac_f32_16x16x64_bf16 v[236:239], v[148:151], v[8:15], v234
v_smfmac_f32_16x16x64_bf16 v[236:239], v[152:155], v[16:23], v234
v_smfmac_f32_16x16x64_bf16 v[236:239], v[156:159], v[24:31], v234
s_waitcnt lgkmcnt(0)
MM v[64:67],a[192:195],v[160:163],v[128:131]
s_add_u32 s64, s64, 16384
TR a[128:129],v232 offset:0
s_add_u32 s81, s64, 896
MM v[64:67],a[196:199],v[176:179],v[64:67]
TR a[130:131],v232 offset:4096
TR a[132:133],v232 offset:8192
MM v[64:67],a[200:203],v[192:195],v[64:67]
TR a[134:135],v232 offset:12288
TR a[136:137],v233 offset:0
MM v[64:67],a[204:207],v[208:211],v[64:67]
TR a[138:139],v233 offset:4096
v_add_f32 v246, v236, v237
MM v[68:71],a[220:223],v[208:211],v[128:131]
TR a[140:141],v233 offset:8192
v_cmp_nge_f32 vcc, s71, v246
MM v[68:71],a[216:219],v[192:195],v[68:71]
s_cbranch_vccnz LSITE16_%=
LBACK16_%=:
TR a[142:143],v233 offset:12288
TR a[144:145],v232 offset:512
MM v[68:71],a[212:215],v[176:179],v[68:71]
v_lshrrev_b32 v43, 7, v248
v_bfe_u32 v42, v248, 6, 1
MM v[68:71],a[208:211],v[160:163],v[68:71]
v_lshl_add_u32 v40, v42, 3, v43
v_lshlrev_b32 v40, 4, v40
MM v[76:79],a[208:211],v[164:167],v[132:135]
v_bfe_u32 v42, v248, 4, 2
v_lshl_add_u32 v40, v42, 2, v40
MM v[76:79],a[212:215],v[180:183],v[76:79]
s_lshl_b32 s76, %[swave], 11
EX v64,v64
MM v[76:79],a[216:219],v[196:199],v[76:79]
s_add_u32 s76, s76, %[slds]
EX v65,v65
MM v[76:79],a[220:223],v[212:215],v[76:79]
s_add_u32 s76, s76, 131072
v_add_u32 v40, s76, v40
CV v64,v64,v65
MM v[72:75],a[204:207],v[212:215],v[132:135]
s_waitcnt vmcnt(18)
ds_read_b32 v32, v40 offset:0
ds_read_b32 v36, v40 offset:256
MM v[72:75],a[200:203],v[196:199],v[72:75]
ds_read_b32 v33, v40 offset:512
ds_read_b32 v37, v40 offset:768
MM v[72:75],a[196:199],v[180:183],v[72:75]
ds_read_b32 v34, v40 offset:1024
ds_read_b32 v38, v40 offset:1280
MM v[72:75],a[192:195],v[164:167],v[72:75]
ds_read_b32 v35, v40 offset:1536
ds_read_b32 v39, v40 offset:1792
MM v[80:83],a[192:195],v[168:171],v[136:139]
v_mul_u32_u24 v41, 3456, v43
v_bfe_u32 v42, v248, 4, 3
MM v[80:83],a[196:199],v[184:187],v[80:83]
TR a[146:147],v232 offset:4608
v_lshl_add_u32 v41, v42, 1, v41
MM v[80:83],a[200:203],v[200:203],v[80:83]
s_add_u32 s76, s48, 21626880
EX v66,v66
MM v[80:83],a[204:207],v[216:219],v[80:83]
s_lshl_b32 s77, %[swave], 4
EX v67,v67
MM v[84:87],a[220:223],v[216:219],v[136:139]
EX v68,v68
MM v[84:87],a[216:219],v[200:203],v[84:87]
s_waitcnt lgkmcnt(1)
v_add_f32 v32, v32, v33
v_add_f32 v36, v36, v37
MM v[84:87],a[212:215],v[184:187],v[84:87]
v_add_f32 v34, v34, v35
v_add_f32 v38, v38, v39
EX v69,v69
MM v[84:87],a[208:211],v[168:171],v[84:87]
v_add_f32 v32, v32, v34
v_add_f32 v36, v36, v38
EX v70,v70
MM v[92:95],a[208:211],v[172:175],v[140:143]
CV v42,v32,v36
v_lshlrev_b32 v32, 16, v42
MM v[92:95],a[212:215],v[188:191],v[92:95]
TR a[148:149],v232 offset:8704
v_and_b32 v36, 0xffff0000, v42
v_mul_f32 v42, 0xbfb8aa3b, v32
EX v71,v71
MM v[92:95],a[216:219],v[204:207],v[92:95]
EX v42,v42
MM v[92:95],a[220:223],v[220:223],v[92:95]
TR a[150:151],v232 offset:12800
v_add_f32 v42, 1.0, v42
EX v72,v72
MM v[88:91],a[204:207],v[220:223],v[140:143]
v_rcp_f32 v42, v42
MM v[88:91],a[200:203],v[204:207],v[88:91]
TR a[152:153],v233 offset:512
v_mul_f32 v42, v32, v42
CV v42,v42,v42
EX v73,v73
CV v65,v66,v67
MM v[88:91],a[196:199],v[188:191],v[88:91]
TR a[154:155],v233 offset:4608
v_lshlrev_b32 v42, 16, v42
v_mul_f32 v32, v42, v36
MM v[88:91],a[192:195],v[172:175],v[88:91]
CV v43,v32,v32
CV v66,v68,v69
EX v74,v74
MM v[96:99],a[224:227],v[160:163],v[128:131]
s_lshl_b64 exec, 0xffff, s77
buffer_store_short v43, v41, s[20:23], s76 offen offset:0 sc1
s_mov_b64 exec, -1
s_nop 0
TR a[156:157],v233 offset:8704
EX v75,v75
MM v[96:99],a[228:231],v[176:179],v[96:99]
TR a[158:159],v233 offset:12800
CV v67,v70,v71
MM v[96:99],a[232:235],v[192:195],v[96:99]
EX v76,v76
MM v[96:99],a[236:239],v[208:211],v[96:99]
TR a[160:161],v232 offset:1024
TR a[162:163],v232 offset:5120
MM v[100:103],a[252:255],v[208:211],v[128:131]
EX v77,v77
MM v[100:103],a[248:251],v[192:195],v[100:103]
EX v78,v78
MM v[100:103],a[244:247],v[176:179],v[100:103]
TR a[164:165],v232 offset:9216
CV v72,v72,v73
MM v[100:103],a[240:243],v[160:163],v[100:103]
EX v79,v79
MM v[108:111],a[240:243],v[164:167],v[132:135]
TR a[166:167],v232 offset:13312
CV v73,v74,v75
MM v[108:111],a[244:247],v[180:183],v[108:111]
EX v80,v80
MM v[108:111],a[248:251],v[196:199],v[108:111]
TR a[168:169],v233 offset:1024
TR a[170:171],v233 offset:5120
MM v[108:111],a[252:255],v[212:215],v[108:111]
EX v81,v81
MM v[104:107],a[236:239],v[212:215],v[132:135]
TR a[172:173],v233 offset:9216
CV v74,v76,v77
MM v[104:107],a[232:235],v[196:199],v[104:107]
EX v82,v82
MM v[104:107],a[228:231],v[180:183],v[104:107]
EX v83,v83
MM v[104:107],a[224:227],v[164:167],v[104:107]
TR a[174:175],v233 offset:13312
CV v75,v78,v79
MM v[112:115],a[224:227],v[168:171],v[136:139]
EX v84,v84
MM v[112:115],a[228:231],v[184:187],v[112:115]
TR a[176:177],v232 offset:1536
TR a[178:179],v232 offset:5632
MM v[112:115],a[232:235],v[200:203],v[112:115]
EX v85,v85
MM v[112:115],a[236:239],v[216:219],v[112:115]
TR a[180:181],v232 offset:9728
CV v80,v80,v81
MM v[116:119],a[252:255],v[216:219],v[136:139]
EX v86,v86
MM v[116:119],a[248:251],v[200:203],v[116:119]
TR a[182:183],v232 offset:13824
CV v81,v82,v83
MM v[116:119],a[244:247],v[184:187],v[116:119]
EX v87,v87
MM v[116:119],a[240:243],v[168:171],v[116:119]
TR a[184:185],v233 offset:1536
CV v82,v84,v85
MM v[124:127],a[240:243],v[172:175],v[140:143]
EX v88,v88
MM v[124:127],a[244:247],v[188:191],v[124:127]
TR a[186:187],v233 offset:5632
TR a[188:189],v233 offset:9728
MM v[124:127],a[248:251],v[204:207],v[124:127]
TR a[190:191],v233 offset:13824
CV v83,v86,v87
MM v[124:127],a[252:255],v[220:223],v[124:127]
EX v89,v89
MM v[120:123],a[236:239],v[220:223],v[140:143]
EX v90,v90
MM v[120:123],a[232:235],v[204:207],v[120:123]
EX v91,v91
MM v[120:123],a[228:231],v[188:191],v[120:123]
EX v92,v92
MM v[120:123],a[224:227],v[172:175],v[120:123]
s_waitcnt vmcnt(16) lgkmcnt(0)
s_barrier
MM a[0:3],a[128:131],v[0:3],a[0:3]
s_add_u32 m0, s65, 0
CV v88,v88,v89
CV v89,v90,v91
MM a[0:3],a[132:135],v[4:7],a[0:3]
EX v93,v93
MM a[8:11],a[132:135],v[12:15],a[8:11]
EX v94,v94
MM a[8:11],a[128:131],v[8:11],a[8:11]
EX v95,v95
MM a[16:19],a[128:131],v[16:19],a[16:19]
RD a[192:195],v224 offset:32768
CV v90,v92,v93
MM a[16:19],a[132:135],v[20:23],a[16:19]
EX v96,v96
MM a[24:27],a[132:135],v[28:31],a[24:27]
RD a[196:199],v225 offset:32768
CV v91,v94,v95
MM a[24:27],a[128:131],v[24:27],a[24:27]
EX v97,v97
MM a[28:31],a[136:139],v[24:27],a[28:31]
buffer_load_dwordx4 v228, s[8:11], s64 offen offset:0 lds
MM a[28:31],a[140:143],v[28:31],a[28:31]
EX v98,v98
MM a[20:23],a[140:143],v[20:23],a[20:23]
EX v99,v99
MM a[20:23],a[136:139],v[16:19],a[20:23]
RD a[200:203],v226 offset:32768
CV v68,v96,v97
MM a[12:15],a[136:139],v[8:11],a[12:15]
EX v100,v100
MM a[12:15],a[140:143],v[12:15],a[12:15]
RD a[204:207],v227 offset:32768
CV v69,v98,v99
MM a[4:7],a[140:143],v[4:7],a[4:7]
EX v101,v101
MM a[4:7],a[136:139],v[0:3],a[4:7]
buffer_load_dwordx4 v229, s[8:11], s64 offen offset:1024 lds
MM a[32:35],a[144:147],v[0:3],a[32:35]
EX v102,v102
MM a[32:35],a[148:151],v[4:7],a[32:35]
RD a[208:211],v224 offset:36864
CV v70,v100,v101
MM a[40:43],a[148:151],v[12:15],a[40:43]
EX v103,v103
MM a[40:43],a[144:147],v[8:11],a[40:43]
RD a[212:215],v225 offset:36864
CV v71,v102,v103
MM a[48:51],a[144:147],v[16:19],a[48:51]
EX v104,v104
MM a[48:51],a[148:151],v[20:23],a[48:51]
EX v105,v105
MM a[56:59],a[148:151],v[28:31],a[56:59]
buffer_load_dwordx4 v230, s[8:11], s64 offen offset:2048 lds
MM a[56:59],a[144:147],v[24:27],a[56:59]
EX v106,v106
MM a[60:63],a[152:155],v[24:27],a[60:63]
RD a[216:219],v226 offset:36864
CV v76,v104,v105
MM a[60:63],a[156:159],v[28:31],a[60:63]
EX v107,v107
MM a[52:55],a[156:159],v[20:23],a[52:55]
RD a[220:223],v227 offset:36864
CV v77,v106,v107
MM a[52:55],a[152:155],v[16:19],a[52:55]
EX v108,v108
MM a[44:47],a[152:155],v[8:11],a[44:47]
EX v109,v109
MM a[44:47],a[156:159],v[12:15],a[44:47]
buffer_load_dwordx4 v231, s[8:11], s64 offen offset:3072 lds
MM a[36:39],a[156:159],v[4:7],a[36:39]
s_add_u32 m0, s65, 65536
RD a[224:227],v224 offset:40960
CV v78,v108,v109
MM a[36:39],a[152:155],v[0:3],a[36:39]
EX v110,v110
MM a[64:67],a[160:163],v[0:3],a[64:67]
EX v111,v111
MM a[64:67],a[164:167],v[4:7],a[64:67]
RD a[228:231],v225 offset:40960
CV v79,v110,v111
MM a[72:75],a[164:167],v[12:15],a[72:75]
EX v112,v112
MM a[72:75],a[160:163],v[8:11],a[72:75]
buffer_load_dwordx4 v235, s[12:15], s81 offen offset:0 lds
MM a[80:83],a[160:163],v[16:19],a[80:83]
EX v113,v113
MM a[80:83],a[164:167],v[20:23],a[80:83]
RD a[232:235],v226 offset:40960
CV v84,v112,v113
MM a[88:91],a[164:167],v[28:31],a[88:91]
EX v114,v114
MM a[88:91],a[160:163],v[24:27],a[88:91]
EX v115,v115
MM a[92:95],a[168:171],v[24:27],a[92:95]
RD a[236:239],v227 offset:40960
CV v85,v114,v115
MM a[92:95],a[172:175],v[28:31],a[92:95]
EX v116,v116
MM a[84:87],a[172:175],v[20:23],a[84:87]
buffer_load_dwordx4 v235, s[12:15], s64 offen offset:1024 lds
MM a[84:87],a[168:171],v[16:19],a[84:87]
EX v117,v117
MM a[76:79],a[168:171],v[8:11],a[76:79]
RD a[240:243],v224 offset:45056
CV v86,v116,v117
MM a[76:79],a[172:175],v[12:15],a[76:79]
EX v118,v118
MM a[68:71],a[172:175],v[4:7],a[68:71]
EX v119,v119
MM a[68:71],a[168:171],v[0:3],a[68:71]
RD a[244:247],v225 offset:45056
CV v87,v118,v119
MM a[96:99],a[176:179],v[0:3],a[96:99]
EX v120,v120
MM a[96:99],a[180:183],v[4:7],a[96:99]
buffer_load_dwordx4 v235, s[12:15], s81 offen offset:2048 lds
MM a[104:107],a[180:183],v[12:15],a[104:107]
EX v121,v121
MM a[104:107],a[176:179],v[8:11],a[104:107]
RD a[248:251],v226 offset:45056
CV v92,v120,v121
MM a[112:115],a[176:179],v[16:19],a[112:115]
EX v122,v122
MM a[112:115],a[180:183],v[20:23],a[112:115]
EX v123,v123
MM a[120:123],a[180:183],v[28:31],a[120:123]
RD a[252:255],v227 offset:45056
CV v93,v122,v123
MM a[120:123],a[176:179],v[24:27],a[120:123]
EX v124,v124
MM a[124:127],a[184:187],v[24:27],a[124:127]
buffer_load_dwordx4 v235, s[12:15], s64 offen offset:3072 lds
MM a[124:127],a[188:191],v[28:31],a[124:127]
EX v125,v125
MM a[116:119],a[188:191],v[20:23],a[116:119]
EX v126,v126
MM a[116:119],a[184:187],v[16:19],a[116:119]
EX v127,v127
MM a[108:111],a[184:187],v[8:11],a[108:111]
CV v94,v124,v125
CV v95,v126,v127
MM a[108:111],a[188:191],v[12:15],a[108:111]
MM a[100:103],a[188:191],v[4:7],a[100:103]
MM a[100:103],a[184:187],v[0:3],a[100:103]
v_smfmac_f32_16x16x64_bf16 v[236:239], v[144:147], v[64:71], v234
v_smfmac_f32_16x16x64_bf16 v[236:239], v[148:151], v[72:79], v234
v_smfmac_f32_16x16x64_bf16 v[236:239], v[152:155], v[80:87], v234
v_smfmac_f32_16x16x64_bf16 v[236:239], v[156:159], v[88:95], v234
s_waitcnt lgkmcnt(0)
MM v[0:3],a[192:195],v[160:163],v[128:131]
s_add_u32 s64, s64, 16384
TR a[128:129],v232 offset:16384
s_add_u32 s81, s64, 896
MM v[0:3],a[196:199],v[176:179],v[0:3]
TR a[130:131],v232 offset:20480
TR a[132:133],v232 offset:24576
MM v[0:3],a[200:203],v[192:195],v[0:3]
TR a[134:135],v232 offset:28672
TR a[136:137],v233 offset:16384
MM v[0:3],a[204:207],v[208:211],v[0:3]
TR a[138:139],v233 offset:20480
v_add_f32 v247, v236, v237
MM v[4:7],a[220:223],v[208:211],v[128:131]
TR a[140:141],v233 offset:24576
v_cmp_nge_f32 vcc, s71, v247
MM v[4:7],a[216:219],v[192:195],v[4:7]
s_cbranch_vccnz LSITE17_%=
LBACK17_%=:
TR a[142:143],v233 offset:28672
TR a[144:145],v232 offset:16896
MM v[4:7],a[212:215],v[176:179],v[4:7]
s_lshr_b32 s77, %[sb], 3
TR a[146:147],v232 offset:20992
TR a[148:149],v232 offset:25088
MM v[4:7],a[208:211],v[160:163],v[4:7]
s_lshl_b32 s77, s77, 2
TR a[150:151],v232 offset:29184
TR a[152:153],v233 offset:16896
)ASM"
R"ASM(
MM v[12:15],a[208:211],v[164:167],v[132:135]
s_add_u32 s77, s77, %[swave]
EX v0,v0
MM v[12:15],a[212:215],v[180:183],v[12:15]
s_lshl_b32 s77, s77, 9
EX v1,v1
MM v[12:15],a[216:219],v[196:199],v[12:15]
s_add_u32 s77, s77, 0
v_lshrrev_b32 v253, 1, v248
v_add_u32 v253, s77, v253
MM v[12:15],a[220:223],v[212:215],v[12:15]
s_mov_b32 s77, LKEND_ka-LPD1_%=-16
v_min_u32 v253, s77, v253
CV v0,v0,v1
MM v[8:11],a[204:207],v[212:215],v[132:135]
s_getpc_b64 s[98:99]
LPD1_%=:
global_load_dword v253, v253, s[98:99]
MM v[8:11],a[200:203],v[196:199],v[8:11]
EX v2,v2
MM v[8:11],a[196:199],v[180:183],v[8:11]
EX v3,v3
MM v[8:11],a[192:195],v[164:167],v[8:11]
TR a[154:155],v233 offset:20992
CV v1,v2,v3
MM v[16:19],a[192:195],v[168:171],v[136:139]
EX v4,v4
MM v[16:19],a[196:199],v[184:187],v[16:19]
EX v5,v5
MM v[16:19],a[200:203],v[200:203],v[16:19]
TR a[156:157],v233 offset:25088
CV v2,v4,v5
MM v[16:19],a[204:207],v[216:219],v[16:19]
EX v6,v6
MM v[20:23],a[220:223],v[216:219],v[136:139]
EX v7,v7
MM v[20:23],a[216:219],v[200:203],v[20:23]
TR a[158:159],v233 offset:29184
CV v3,v6,v7
MM v[20:23],a[212:215],v[184:187],v[20:23]
EX v8,v8
MM v[20:23],a[208:211],v[168:171],v[20:23]
EX v9,v9
MM v[28:31],a[208:211],v[172:175],v[140:143]
s_add_u32 m0, s45, 147456
s_add_u32 s44, s44, 1024
buffer_load_dwordx4 v248, s[24:27], s44 offen offset:0 lds
MM v[28:31],a[212:215],v[188:191],v[28:31]
TR a[160:161],v232 offset:17408
CV v8,v8,v9
MM v[28:31],a[216:219],v[204:207],v[28:31]
EX v10,v10
MM v[28:31],a[220:223],v[220:223],v[28:31]
EX v11,v11
MM v[24:27],a[204:207],v[220:223],v[140:143]
TR a[162:163],v232 offset:21504
CV v9,v10,v11
MM v[24:27],a[200:203],v[204:207],v[24:27]
TR a[164:165],v232 offset:25600
TR a[166:167],v232 offset:29696
MM v[24:27],a[196:199],v[188:191],v[24:27]
EX v12,v12
MM v[24:27],a[192:195],v[172:175],v[24:27]
EX v13,v13
MM v[32:35],a[224:227],v[160:163],v[128:131]
TR a[168:169],v233 offset:17408
CV v10,v12,v13
MM v[32:35],a[228:231],v[176:179],v[32:35]
EX v14,v14
MM v[32:35],a[232:235],v[192:195],v[32:35]
TR a[170:171],v233 offset:21504
TR a[172:173],v233 offset:25600
MM v[32:35],a[236:239],v[208:211],v[32:35]
EX v15,v15
MM v[36:39],a[252:255],v[208:211],v[128:131]
TR a[174:175],v233 offset:29696
CV v11,v14,v15
MM v[36:39],a[248:251],v[192:195],v[36:39]
EX v16,v16
MM v[36:39],a[244:247],v[176:179],v[36:39]
EX v17,v17
MM v[36:39],a[240:243],v[160:163],v[36:39]
TR a[176:177],v232 offset:17920
CV v16,v16,v17
MM v[44:47],a[240:243],v[164:167],v[132:135]
s_add_u32 s67, s95, 15360
buffer_store_dwordx4 v[240:243], v248, s[20:23], s67 offen offset:0 sc1
MM v[44:47],a[244:247],v[180:183],v[44:47]
EX v18,v18
MM v[44:47],a[248:251],v[196:199],v[44:47]
TR a[178:179],v232 offset:22016
TR a[180:181],v232 offset:26112
MM v[44:47],a[252:255],v[212:215],v[44:47]
EX v19,v19
MM v[40:43],a[236:239],v[212:215],v[132:135]
TR a[182:183],v232 offset:30208
CV v17,v18,v19
MM v[40:43],a[232:235],v[196:199],v[40:43]
EX v20,v20
MM v[40:43],a[228:231],v[180:183],v[40:43]
EX v21,v21
MM v[40:43],a[224:227],v[164:167],v[40:43]
LPG14_%=:
s_cbranch_execz LTR1_%=
TR a[184:185],v233 offset:17920
CV v18,v20,v21
MM v[48:51],a[224:227],v[168:171],v[136:139]
EX v22,v22
MM v[48:51],a[228:231],v[184:187],v[48:51]
TR a[186:187],v233 offset:22016
TR a[188:189],v233 offset:26112
MM v[48:51],a[232:235],v[200:203],v[48:51]
EX v23,v23
MM v[48:51],a[236:239],v[216:219],v[48:51]
TR a[190:191],v233 offset:30208
CV v19,v22,v23
MM v[52:55],a[252:255],v[216:219],v[136:139]
EX v24,v24
MM v[52:55],a[248:251],v[200:203],v[52:55]
EX v25,v25
MM v[52:55],a[244:247],v[184:187],v[52:55]
EX v26,v26
MM v[52:55],a[240:243],v[168:171],v[52:55]
EX v27,v27
MM v[60:63],a[240:243],v[172:175],v[140:143]
EX v28,v28
MM v[60:63],a[244:247],v[188:191],v[60:63]
EX v29,v29
MM v[60:63],a[248:251],v[204:207],v[60:63]
EX v30,v30
MM v[60:63],a[252:255],v[220:223],v[60:63]
EX v31,v31
MM v[56:59],a[236:239],v[220:223],v[140:143]
EX v32,v32
MM v[56:59],a[232:235],v[204:207],v[56:59]
EX v33,v33
MM v[56:59],a[228:231],v[188:191],v[56:59]
EX v34,v34
MM v[56:59],a[224:227],v[172:175],v[56:59]
s_waitcnt vmcnt(16) lgkmcnt(0)
s_barrier
MM a[0:3],a[128:131],v[64:67],a[0:3]
s_add_u32 m0, s65, 16384
CV v24,v24,v25
CV v25,v26,v27
MM a[0:3],a[132:135],v[68:71],a[0:3]
RD a[192:195],v224 offset:49152
CV v26,v28,v29
MM a[8:11],a[132:135],v[76:79],a[8:11]
RD a[196:199],v225 offset:49152
CV v27,v30,v31
MM a[8:11],a[128:131],v[72:75],a[8:11]
buffer_load_dwordx4 v228, s[8:11], s64 offen offset:0 lds
MM a[16:19],a[128:131],v[80:83],a[16:19]
RD a[200:203],v226 offset:49152
CV v4,v32,v33
MM a[16:19],a[132:135],v[84:87],a[16:19]
buffer_load_dwordx4 v229, s[8:11], s64 offen offset:1024 lds
MM a[24:27],a[132:135],v[92:95],a[24:27]
RD a[204:207],v227 offset:49152
RD a[208:211],v224 offset:53248
MM a[24:27],a[128:131],v[88:91],a[24:27]
EX v35,v35
MM a[28:31],a[136:139],v[88:91],a[28:31]
RD a[212:215],v225 offset:53248
CV v5,v34,v35
MM a[28:31],a[140:143],v[92:95],a[28:31]
EX v36,v36
MM a[20:23],a[140:143],v[84:87],a[20:23]
buffer_load_dwordx4 v230, s[8:11], s64 offen offset:2048 lds
MM a[20:23],a[136:139],v[80:83],a[20:23]
EX v37,v37
MM a[12:15],a[136:139],v[72:75],a[12:15]
RD a[216:219],v226 offset:53248
CV v6,v36,v37
MM a[12:15],a[140:143],v[76:79],a[12:15]
EX v38,v38
MM a[4:7],a[140:143],v[68:71],a[4:7]
EX v39,v39
MM a[4:7],a[136:139],v[64:67],a[4:7]
RD a[220:223],v227 offset:53248
CV v7,v38,v39
MM a[32:35],a[144:147],v[64:67],a[32:35]
EX v40,v40
MM a[32:35],a[148:151],v[68:71],a[32:35]
buffer_load_dwordx4 v231, s[8:11], s64 offen offset:3072 lds
MM a[40:43],a[148:151],v[76:79],a[40:43]
s_add_u32 m0, s65, 81920
EX v41,v41
MM a[40:43],a[144:147],v[72:75],a[40:43]
RD a[224:227],v224 offset:57344
CV v12,v40,v41
MM a[48:51],a[144:147],v[80:83],a[48:51]
EX v42,v42
MM a[48:51],a[148:151],v[84:87],a[48:51]
EX v43,v43
MM a[56:59],a[148:151],v[92:95],a[56:59]
RD a[228:231],v225 offset:57344
CV v13,v42,v43
MM a[56:59],a[144:147],v[88:91],a[56:59]
EX v44,v44
MM a[60:63],a[152:155],v[88:91],a[60:63]
buffer_load_dwordx4 v235, s[12:15], s81 offen offset:0 lds
MM a[60:63],a[156:159],v[92:95],a[60:63]
EX v45,v45
MM a[52:55],a[156:159],v[84:87],a[52:55]
RD a[232:235],v226 offset:57344
CV v14,v44,v45
MM a[52:55],a[152:155],v[80:83],a[52:55]
EX v46,v46
MM a[44:47],a[152:155],v[72:75],a[44:47]
EX v47,v47
MM a[44:47],a[156:159],v[76:79],a[44:47]
RD a[236:239],v227 offset:57344
CV v15,v46,v47
MM a[36:39],a[156:159],v[68:71],a[36:39]
buffer_load_dwordx4 v235, s[12:15], s64 offen offset:1024 lds
MM a[36:39],a[152:155],v[64:67],a[36:39]
EX v48,v48
MM a[64:67],a[160:163],v[64:67],a[64:67]
EX v49,v49
MM a[64:67],a[164:167],v[68:71],a[64:67]
RD a[240:243],v224 offset:61440
CV v20,v48,v49
MM a[72:75],a[164:167],v[76:79],a[72:75]
EX v50,v50
MM a[72:75],a[160:163],v[72:75],a[72:75]
RD a[244:247],v225 offset:61440
RD a[248:251],v226 offset:61440
MM a[80:83],a[160:163],v[80:83],a[80:83]
EX v51,v51
MM a[80:83],a[164:167],v[84:87],a[80:83]
RD a[252:255],v227 offset:61440
CV v21,v50,v51
MM a[88:91],a[164:167],v[92:95],a[88:91]
EX v52,v52
MM a[88:91],a[160:163],v[88:91],a[88:91]
buffer_load_dwordx4 v235, s[12:15], s81 offen offset:2048 lds
MM a[92:95],a[168:171],v[88:91],a[92:95]
EX v53,v53
MM a[92:95],a[172:175],v[92:95],a[92:95]
EX v54,v54
MM a[84:87],a[172:175],v[84:87],a[84:87]
buffer_load_dwordx4 v235, s[12:15], s64 offen offset:3072 lds
MM a[84:87],a[168:171],v[80:83],a[84:87]
EX v55,v55
MM a[76:79],a[168:171],v[72:75],a[76:79]
EX v56,v56
MM a[76:79],a[172:175],v[76:79],a[76:79]
EX v57,v57
MM a[68:71],a[172:175],v[68:71],a[68:71]
EX v58,v58
MM a[68:71],a[168:171],v[64:67],a[68:71]
EX v59,v59
MM a[96:99],a[176:179],v[64:67],a[96:99]
EX v60,v60
MM a[96:99],a[180:183],v[68:71],a[96:99]
EX v61,v61
MM a[104:107],a[180:183],v[76:79],a[104:107]
EX v62,v62
MM a[104:107],a[176:179],v[72:75],a[104:107]
EX v63,v63
MM a[112:115],a[176:179],v[80:83],a[112:115]
CV v22,v52,v53
CV v23,v54,v55
MM a[112:115],a[180:183],v[84:87],a[112:115]
CV v28,v56,v57
CV v29,v58,v59
MM a[120:123],a[180:183],v[92:95],a[120:123]
CV v30,v60,v61
CV v31,v62,v63
MM a[120:123],a[176:179],v[88:91],a[120:123]
MM a[124:127],a[184:187],v[88:91],a[124:127]
MM a[124:127],a[188:191],v[92:95],a[124:127]
MM a[116:119],a[188:191],v[84:87],a[116:119]
MM a[116:119],a[184:187],v[80:83],a[116:119]
MM a[108:111],a[184:187],v[72:75],a[108:111]
MM a[108:111],a[188:191],v[76:79],a[108:111]
MM a[100:103],a[188:191],v[68:71],a[100:103]
MM a[100:103],a[184:187],v[64:67],a[100:103]
v_smfmac_f32_16x16x64_bf16 v[236:239], v[144:147], v[0:7], v234
v_smfmac_f32_16x16x64_bf16 v[236:239], v[148:151], v[8:15], v234
v_smfmac_f32_16x16x64_bf16 v[236:239], v[152:155], v[16:23], v234
v_smfmac_f32_16x16x64_bf16 v[236:239], v[156:159], v[24:31], v234
s_waitcnt lgkmcnt(0)
MM v[64:67],a[192:195],v[160:163],v[128:131]
s_add_u32 s64, s64, 16384
TR a[128:129],v232 offset:32768
s_add_u32 s81, s64, 896
MM v[64:67],a[196:199],v[176:179],v[64:67]
TR a[130:131],v232 offset:36864
TR a[132:133],v232 offset:40960
MM v[64:67],a[200:203],v[192:195],v[64:67]
TR a[134:135],v232 offset:45056
TR a[136:137],v233 offset:32768
MM v[64:67],a[204:207],v[208:211],v[64:67]
TR a[138:139],v233 offset:36864
v_add_f32 v246, v236, v237
MM v[68:71],a[220:223],v[208:211],v[128:131]
TR a[140:141],v233 offset:40960
v_cmp_nge_f32 vcc, s71, v246
MM v[68:71],a[216:219],v[192:195],v[68:71]
s_cbranch_vccnz LSITE18_%=
LBACK18_%=:
TR a[142:143],v233 offset:45056
TR a[144:145],v232 offset:33280
MM v[68:71],a[212:215],v[176:179],v[68:71]
s_add_u32 s67, s95, 16384
buffer_store_dwordx4 v[240:243], v248, s[20:23], s67 offen offset:0 sc1
MM v[68:71],a[208:211],v[160:163],v[68:71]
TR a[146:147],v232 offset:37376
TR a[148:149],v232 offset:41472
MM v[76:79],a[208:211],v[164:167],v[132:135]
EX v64,v64
MM v[76:79],a[212:215],v[180:183],v[76:79]
EX v65,v65
MM v[76:79],a[216:219],v[196:199],v[76:79]
TR a[150:151],v232 offset:45568
CV v64,v64,v65
MM v[76:79],a[220:223],v[212:215],v[76:79]
EX v66,v66
MM v[72:75],a[204:207],v[212:215],v[132:135]
EX v67,v67
MM v[72:75],a[200:203],v[196:199],v[72:75]
TR a[152:153],v233 offset:33280
CV v65,v66,v67
MM v[72:75],a[196:199],v[180:183],v[72:75]
EX v68,v68
MM v[72:75],a[192:195],v[164:167],v[72:75]
EX v69,v69
MM v[80:83],a[192:195],v[168:171],v[136:139]
TR a[154:155],v233 offset:37376
CV v66,v68,v69
MM v[80:83],a[196:199],v[184:187],v[80:83]
EX v70,v70
MM v[80:83],a[200:203],v[200:203],v[80:83]
EX v71,v71
MM v[80:83],a[204:207],v[216:219],v[80:83]
TR a[156:157],v233 offset:41472
CV v67,v70,v71
MM v[84:87],a[220:223],v[216:219],v[136:139]
EX v72,v72
MM v[84:87],a[216:219],v[200:203],v[84:87]
TR a[158:159],v233 offset:45568
TR a[160:161],v232 offset:33792
MM v[84:87],a[212:215],v[184:187],v[84:87]
EX v73,v73
MM v[84:87],a[208:211],v[168:171],v[84:87]
TR a[162:163],v232 offset:37888
CV v72,v72,v73
MM v[92:95],a[208:211],v[172:175],v[140:143]
EX v74,v74
MM v[92:95],a[212:215],v[188:191],v[92:95]
TR a[164:165],v232 offset:41984
TR a[166:167],v232 offset:46080
MM v[92:95],a[216:219],v[204:207],v[92:95]
EX v75,v75
MM v[92:95],a[220:223],v[220:223],v[92:95]
TR a[168:169],v233 offset:33792
CV v73,v74,v75
MM v[88:91],a[204:207],v[220:223],v[140:143]
EX v76,v76
MM v[88:91],a[200:203],v[204:207],v[88:91]
EX v77,v77
MM v[88:91],a[196:199],v[188:191],v[88:91]
TR a[170:171],v233 offset:37888
CV v74,v76,v77
MM v[88:91],a[192:195],v[172:175],v[88:91]
EX v78,v78
MM v[96:99],a[224:227],v[160:163],v[128:131]
TR a[172:173],v233 offset:41984
TR a[174:175],v233 offset:46080
MM v[96:99],a[228:231],v[176:179],v[96:99]
EX v79,v79
MM v[96:99],a[232:235],v[192:195],v[96:99]
TR a[176:177],v232 offset:34304
CV v75,v78,v79
MM v[96:99],a[236:239],v[208:211],v[96:99]
EX v80,v80
MM v[100:103],a[252:255],v[208:211],v[128:131]
EX v81,v81
MM v[100:103],a[248:251],v[192:195],v[100:103]
TR a[178:179],v232 offset:38400
CV v80,v80,v81
MM v[100:103],a[244:247],v[176:179],v[100:103]
EX v82,v82
MM v[100:103],a[240:243],v[160:163],v[100:103]
TR a[180:181],v232 offset:42496
TR a[182:183],v232 offset:46592
MM v[108:111],a[240:243],v[164:167],v[132:135]
s_add_u32 s67, s95, 17408
)ASM"
R"ASM(
buffer_store_dwordx4 v[240:243], v248, s[20:23], s67 offen offset:0 sc1
MM v[108:111],a[244:247],v[180:183],v[108:111]
EX v83,v83
MM v[108:111],a[248:251],v[196:199],v[108:111]
TR a[184:185],v233 offset:34304
CV v81,v82,v83
MM v[108:111],a[252:255],v[212:215],v[108:111]
EX v84,v84
MM v[104:107],a[236:239],v[212:215],v[132:135]
EX v85,v85
MM v[104:107],a[232:235],v[196:199],v[104:107]
TR a[186:187],v233 offset:38400
CV v82,v84,v85
MM v[104:107],a[228:231],v[180:183],v[104:107]
EX v86,v86
MM v[104:107],a[224:227],v[164:167],v[104:107]
TR a[188:189],v233 offset:42496
TR a[190:191],v233 offset:46592
MM v[112:115],a[224:227],v[168:171],v[136:139]
EX v87,v87
MM v[112:115],a[228:231],v[184:187],v[112:115]
CV v83,v86,v87
MM v[112:115],a[232:235],v[200:203],v[112:115]
EX v88,v88
MM v[112:115],a[236:239],v[216:219],v[112:115]
EX v89,v89
MM v[116:119],a[252:255],v[216:219],v[136:139]
EX v90,v90
MM v[116:119],a[248:251],v[200:203],v[116:119]
EX v91,v91
MM v[116:119],a[244:247],v[184:187],v[116:119]
EX v92,v92
MM v[116:119],a[240:243],v[168:171],v[116:119]
EX v93,v93
MM v[124:127],a[240:243],v[172:175],v[140:143]
EX v94,v94
MM v[124:127],a[244:247],v[188:191],v[124:127]
EX v95,v95
MM v[124:127],a[248:251],v[204:207],v[124:127]
EX v96,v96
MM v[124:127],a[252:255],v[220:223],v[124:127]
EX v97,v97
MM v[120:123],a[236:239],v[220:223],v[140:143]
EX v98,v98
MM v[120:123],a[232:235],v[204:207],v[120:123]
EX v99,v99
MM v[120:123],a[228:231],v[188:191],v[120:123]
s_getpc_b64 s[98:99]
LTA2_%=:
s_lshl_b32 s73, %[swave], 12
s_add_u32 s73, s73, LPG18_%=-LTA2_%=
s_add_u32 s98, s98, s73
s_addc_u32 s99, s99, 0
s_mov_b64 exec, 0
s_setpc_b64 s[98:99]
LTR2_%=:
s_mov_b64 exec, -1
s_nop 0
CV v88,v88,v89
MM v[120:123],a[224:227],v[172:175],v[120:123]
s_waitcnt vmcnt(17) lgkmcnt(0)
s_barrier
MM a[0:3],a[128:131],v[0:3],a[0:3]
s_lshl_b32 s91, %[swave], 2
CV v89,v90,v91
CV v90,v92,v93
MM a[0:3],a[132:135],v[4:7],a[0:3]
s_add_u32 s91, s91, s48
RD a[192:195],v224 offset:0
CV v91,v94,v95
MM a[8:11],a[132:135],v[12:15],a[8:11]
s_add_u32 s91, s91, 69632
s_add_u32 m0, s65, 32768
RD a[196:199],v225 offset:0
MM a[8:11],a[128:131],v[8:11],a[8:11]
v_mov_b32 v250, s69
s_mov_b64 exec, 1
buffer_store_dword v250, off, s[20:23], s91 offset:0 sc1
s_mov_b64 exec, -1
MM a[16:19],a[128:131],v[16:19],a[16:19]
buffer_load_dwordx4 v228, s[8:11], s64 offen offset:0 lds
MM a[16:19],a[132:135],v[20:23],a[16:19]
RD a[200:203],v226 offset:0
CV v68,v96,v97
MM a[24:27],a[132:135],v[28:31],a[24:27]
RD a[204:207],v227 offset:0
CV v69,v98,v99
MM a[24:27],a[128:131],v[24:27],a[24:27]
buffer_load_dwordx4 v229, s[8:11], s64 offen offset:1024 lds
MM a[28:31],a[136:139],v[24:27],a[28:31]
RD a[208:211],v224 offset:4096
RD a[212:215],v225 offset:4096
MM a[28:31],a[140:143],v[28:31],a[28:31]
EX v100,v100
MM a[20:23],a[140:143],v[20:23],a[20:23]
buffer_load_dwordx4 v230, s[8:11], s64 offen offset:2048 lds
MM a[20:23],a[136:139],v[16:19],a[20:23]
EX v101,v101
MM a[12:15],a[136:139],v[8:11],a[12:15]
RD a[216:219],v226 offset:4096
CV v70,v100,v101
MM a[12:15],a[140:143],v[12:15],a[12:15]
EX v102,v102
MM a[4:7],a[140:143],v[4:7],a[4:7]
EX v103,v103
MM a[4:7],a[136:139],v[0:3],a[4:7]
RD a[220:223],v227 offset:4096
CV v71,v102,v103
MM a[32:35],a[144:147],v[0:3],a[32:35]
EX v104,v104
MM a[32:35],a[148:151],v[4:7],a[32:35]
buffer_load_dwordx4 v231, s[8:11], s64 offen offset:3072 lds
MM a[40:43],a[148:151],v[12:15],a[40:43]
s_add_u32 m0, s65, 98304
EX v105,v105
MM a[40:43],a[144:147],v[8:11],a[40:43]
RD a[224:227],v224 offset:8192
CV v76,v104,v105
MM a[48:51],a[144:147],v[16:19],a[48:51]
EX v106,v106
MM a[48:51],a[148:151],v[20:23],a[48:51]
EX v107,v107
MM a[56:59],a[148:151],v[28:31],a[56:59]
RD a[228:231],v225 offset:8192
CV v77,v106,v107
MM a[56:59],a[144:147],v[24:27],a[56:59]
EX v108,v108
MM a[60:63],a[152:155],v[24:27],a[60:63]
buffer_load_dwordx4 v235, s[12:15], s81 offen offset:0 lds
MM a[60:63],a[156:159],v[28:31],a[60:63]
EX v109,v109
MM a[52:55],a[156:159],v[20:23],a[52:55]
RD a[232:235],v226 offset:8192
CV v78,v108,v109
MM a[52:55],a[152:155],v[16:19],a[52:55]
EX v110,v110
MM a[44:47],a[152:155],v[8:11],a[44:47]
EX v111,v111
MM a[44:47],a[156:159],v[12:15],a[44:47]
RD a[236:239],v227 offset:8192
CV v79,v110,v111
MM a[36:39],a[156:159],v[4:7],a[36:39]
buffer_load_dwordx4 v235, s[12:15], s64 offen offset:1024 lds
MM a[36:39],a[152:155],v[0:3],a[36:39]
EX v112,v112
MM a[64:67],a[160:163],v[0:3],a[64:67]
EX v113,v113
MM a[64:67],a[164:167],v[4:7],a[64:67]
RD a[240:243],v224 offset:12288
CV v84,v112,v113
MM a[72:75],a[164:167],v[12:15],a[72:75]
EX v114,v114
MM a[72:75],a[160:163],v[8:11],a[72:75]
RD a[244:247],v225 offset:12288
RD a[248:251],v226 offset:12288
MM a[80:83],a[160:163],v[16:19],a[80:83]
EX v115,v115
MM a[80:83],a[164:167],v[20:23],a[80:83]
RD a[252:255],v227 offset:12288
CV v85,v114,v115
MM a[88:91],a[164:167],v[28:31],a[88:91]
EX v116,v116
MM a[88:91],a[160:163],v[24:27],a[88:91]
buffer_load_dwordx4 v235, s[12:15], s81 offen offset:2048 lds
MM a[92:95],a[168:171],v[24:27],a[92:95]
EX v117,v117
MM a[92:95],a[172:175],v[28:31],a[92:95]
EX v118,v118
MM a[84:87],a[172:175],v[20:23],a[84:87]
buffer_load_dwordx4 v235, s[12:15], s64 offen offset:3072 lds
MM a[84:87],a[168:171],v[16:19],a[84:87]
EX v119,v119
MM a[76:79],a[168:171],v[8:11],a[76:79]
EX v120,v120
MM a[76:79],a[172:175],v[12:15],a[76:79]
EX v121,v121
MM a[68:71],a[172:175],v[4:7],a[68:71]
EX v122,v122
MM a[68:71],a[168:171],v[0:3],a[68:71]
EX v123,v123
MM a[96:99],a[176:179],v[0:3],a[96:99]
EX v124,v124
MM a[96:99],a[180:183],v[4:7],a[96:99]
EX v125,v125
MM a[104:107],a[180:183],v[12:15],a[104:107]
EX v126,v126
MM a[104:107],a[176:179],v[8:11],a[104:107]
EX v127,v127
MM a[112:115],a[176:179],v[16:19],a[112:115]
CV v86,v116,v117
CV v87,v118,v119
MM a[112:115],a[180:183],v[20:23],a[112:115]
CV v92,v120,v121
CV v93,v122,v123
MM a[120:123],a[180:183],v[28:31],a[120:123]
CV v94,v124,v125
CV v95,v126,v127
MM a[120:123],a[176:179],v[24:27],a[120:123]
MM a[124:127],a[184:187],v[24:27],a[124:127]
MM a[124:127],a[188:191],v[28:31],a[124:127]
MM a[116:119],a[188:191],v[20:23],a[116:119]
MM a[116:119],a[184:187],v[16:19],a[116:119]
MM a[108:111],a[184:187],v[8:11],a[108:111]
MM a[108:111],a[188:191],v[12:15],a[108:111]
MM a[100:103],a[188:191],v[4:7],a[100:103]
MM a[100:103],a[184:187],v[0:3],a[100:103]
v_smfmac_f32_16x16x64_bf16 v[236:239], v[144:147], v[64:71], v234
v_smfmac_f32_16x16x64_bf16 v[236:239], v[148:151], v[72:79], v234
v_smfmac_f32_16x16x64_bf16 v[236:239], v[152:155], v[80:87], v234
v_smfmac_f32_16x16x64_bf16 v[236:239], v[156:159], v[88:95], v234
s_waitcnt lgkmcnt(0)
MM v[0:3],a[192:195],v[160:163],v[128:131]
s_add_u32 s64, s64, 16384
TR a[128:129],v232 offset:49152
s_add_u32 s81, s64, 896
MM v[0:3],a[196:199],v[176:179],v[0:3]
TR a[130:131],v232 offset:53248
TR a[132:133],v232 offset:57344
MM v[0:3],a[200:203],v[192:195],v[0:3]
TR a[134:135],v232 offset:61440
TR a[136:137],v233 offset:49152
MM v[0:3],a[204:207],v[208:211],v[0:3]
TR a[138:139],v233 offset:53248
v_add_f32 v247, v236, v237
MM v[4:7],a[220:223],v[208:211],v[128:131]
TR a[140:141],v233 offset:57344
v_cmp_nge_f32 vcc, s71, v247
MM v[4:7],a[216:219],v[192:195],v[4:7]
s_cbranch_vccnz LSITE19_%=
LBACK19_%=:
TR a[142:143],v233 offset:61440
TR a[144:145],v232 offset:49664
MM v[4:7],a[212:215],v[176:179],v[4:7]
s_add_u32 s67, s95, 18432
buffer_store_dwordx4 v[240:243], v248, s[20:23], s67 offen offset:0 sc1
MM v[4:7],a[208:211],v[160:163],v[4:7]
TR a[146:147],v232 offset:53760
TR a[148:149],v232 offset:57856
MM v[12:15],a[208:211],v[164:167],v[132:135]
EX v0,v0
MM v[12:15],a[212:215],v[180:183],v[12:15]
EX v1,v1
MM v[12:15],a[216:219],v[196:199],v[12:15]
TR a[150:151],v232 offset:61952
CV v0,v0,v1
MM v[12:15],a[220:223],v[212:215],v[12:15]
EX v2,v2
MM v[8:11],a[204:207],v[212:215],v[132:135]
EX v3,v3
MM v[8:11],a[200:203],v[196:199],v[8:11]
TR a[152:153],v233 offset:49664
CV v1,v2,v3
MM v[8:11],a[196:199],v[180:183],v[8:11]
EX v4,v4
MM v[8:11],a[192:195],v[164:167],v[8:11]
EX v5,v5
MM v[16:19],a[192:195],v[168:171],v[136:139]
TR a[154:155],v233 offset:53760
CV v2,v4,v5
MM v[16:19],a[196:199],v[184:187],v[16:19]
EX v6,v6
MM v[16:19],a[200:203],v[200:203],v[16:19]
EX v7,v7
MM v[16:19],a[204:207],v[216:219],v[16:19]
TR a[156:157],v233 offset:57856
CV v3,v6,v7
MM v[20:23],a[220:223],v[216:219],v[136:139]
EX v8,v8
MM v[20:23],a[216:219],v[200:203],v[20:23]
TR a[158:159],v233 offset:61952
TR a[160:161],v232 offset:50176
MM v[20:23],a[212:215],v[184:187],v[20:23]
EX v9,v9
MM v[20:23],a[208:211],v[168:171],v[20:23]
TR a[162:163],v232 offset:54272
CV v8,v8,v9
MM v[28:31],a[208:211],v[172:175],v[140:143]
s_add_u32 m0, s45, 147456
s_add_u32 s44, s44, 1024
buffer_load_dwordx4 v248, s[24:27], s44 offen offset:0 lds
MM v[28:31],a[212:215],v[188:191],v[28:31]
EX v10,v10
MM v[28:31],a[216:219],v[204:207],v[28:31]
EX v11,v11
MM v[28:31],a[220:223],v[220:223],v[28:31]
TR a[164:165],v232 offset:58368
CV v9,v10,v11
MM v[24:27],a[204:207],v[220:223],v[140:143]
EX v12,v12
MM v[24:27],a[200:203],v[204:207],v[24:27]
TR a[166:167],v232 offset:62464
TR a[168:169],v233 offset:50176
MM v[24:27],a[196:199],v[188:191],v[24:27]
EX v13,v13
MM v[24:27],a[192:195],v[172:175],v[24:27]
TR a[170:171],v233 offset:54272
CV v10,v12,v13
MM v[32:35],a[224:227],v[160:163],v[128:131]
EX v14,v14
MM v[32:35],a[228:231],v[176:179],v[32:35]
EX v15,v15
MM v[32:35],a[232:235],v[192:195],v[32:35]
LPG15_%=:
s_cbranch_execz LTR1_%=
TR a[172:173],v233 offset:58368
CV v11,v14,v15
MM v[32:35],a[236:239],v[208:211],v[32:35]
TR a[174:175],v233 offset:62464
TR a[176:177],v232 offset:50688
MM v[36:39],a[252:255],v[208:211],v[128:131]
EX v16,v16
MM v[36:39],a[248:251],v[192:195],v[36:39]
EX v17,v17
MM v[36:39],a[244:247],v[176:179],v[36:39]
TR a[178:179],v232 offset:54784
CV v16,v16,v17
MM v[36:39],a[240:243],v[160:163],v[36:39]
EX v18,v18
MM v[44:47],a[240:243],v[164:167],v[132:135]
s_add_u32 s67, s95, 19456
buffer_store_dwordx4 v[240:243], v248, s[20:23], s67 offen offset:0 sc1
MM v[44:47],a[244:247],v[180:183],v[44:47]
EX v19,v19
MM v[44:47],a[248:251],v[196:199],v[44:47]
TR a[180:181],v232 offset:58880
CV v17,v18,v19
MM v[44:47],a[252:255],v[212:215],v[44:47]
EX v20,v20
MM v[40:43],a[236:239],v[212:215],v[132:135]
TR a[182:183],v232 offset:62976
TR a[184:185],v233 offset:50688
MM v[40:43],a[232:235],v[196:199],v[40:43]
EX v21,v21
MM v[40:43],a[228:231],v[180:183],v[40:43]
TR a[186:187],v233 offset:54784
CV v18,v20,v21
MM v[40:43],a[224:227],v[164:167],v[40:43]
EX v22,v22
MM v[48:51],a[224:227],v[168:171],v[136:139]
TR a[188:189],v233 offset:58880
TR a[190:191],v233 offset:62976
MM v[48:51],a[228:231],v[184:187],v[48:51]
EX v23,v23
MM v[48:51],a[232:235],v[200:203],v[48:51]
CV v19,v22,v23
MM v[48:51],a[236:239],v[216:219],v[48:51]
EX v24,v24
MM v[52:55],a[252:255],v[216:219],v[136:139]
EX v25,v25
MM v[52:55],a[248:251],v[200:203],v[52:55]
EX v26,v26
MM v[52:55],a[244:247],v[184:187],v[52:55]
EX v27,v27
MM v[52:55],a[240:243],v[168:171],v[52:55]
EX v28,v28
MM v[60:63],a[240:243],v[172:175],v[140:143]
EX v29,v29
MM v[60:63],a[244:247],v[188:191],v[60:63]
EX v30,v30
MM v[60:63],a[248:251],v[204:207],v[60:63]
EX v31,v31
MM v[60:63],a[252:255],v[220:223],v[60:63]
EX v32,v32
MM v[56:59],a[236:239],v[220:223],v[140:143]
EX v33,v33
MM v[56:59],a[232:235],v[204:207],v[56:59]
EX v34,v34
MM v[56:59],a[228:231],v[188:191],v[56:59]
EX v35,v35
MM v[56:59],a[224:227],v[172:175],v[56:59]
s_waitcnt vmcnt(18) lgkmcnt(0)
s_barrier
MM a[0:3],a[128:131],v[64:67],a[0:3]
s_add_u32 m0, s65, 49152
CV v24,v24,v25
CV v25,v26,v27
MM a[0:3],a[132:135],v[68:71],a[0:3]
RD a[192:195],v224 offset:16384
CV v26,v28,v29
MM a[8:11],a[132:135],v[76:79],a[8:11]
RD a[196:199],v225 offset:16384
CV v27,v30,v31
MM a[8:11],a[128:131],v[72:75],a[8:11]
buffer_load_dwordx4 v228, s[8:11], s64 offen offset:0 lds
MM a[16:19],a[128:131],v[80:83],a[16:19]
RD a[200:203],v226 offset:16384
CV v4,v32,v33
MM a[16:19],a[132:135],v[84:87],a[16:19]
RD a[204:207],v227 offset:16384
CV v5,v34,v35
MM a[24:27],a[132:135],v[92:95],a[24:27]
buffer_load_dwordx4 v229, s[8:11], s64 offen offset:1024 lds
)ASM"
R"ASM(
MM a[24:27],a[128:131],v[88:91],a[24:27]
RD a[208:211],v224 offset:20480
RD a[212:215],v225 offset:20480
MM a[28:31],a[136:139],v[88:91],a[28:31]
buffer_load_dwordx4 v230, s[8:11], s64 offen offset:2048 lds
MM a[28:31],a[140:143],v[92:95],a[28:31]
EX v36,v36
MM a[20:23],a[140:143],v[84:87],a[20:23]
EX v37,v37
MM a[20:23],a[136:139],v[80:83],a[20:23]
RD a[216:219],v226 offset:20480
CV v6,v36,v37
MM a[12:15],a[136:139],v[72:75],a[12:15]
EX v38,v38
MM a[12:15],a[140:143],v[76:79],a[12:15]
RD a[220:223],v227 offset:20480
RD a[224:227],v224 offset:24576
MM a[4:7],a[140:143],v[68:71],a[4:7]
EX v39,v39
MM a[4:7],a[136:139],v[64:67],a[4:7]
RD a[228:231],v225 offset:24576
CV v7,v38,v39
MM a[32:35],a[144:147],v[64:67],a[32:35]
EX v40,v40
MM a[32:35],a[148:151],v[68:71],a[32:35]
buffer_load_dwordx4 v231, s[8:11], s64 offen offset:3072 lds
MM a[40:43],a[148:151],v[76:79],a[40:43]
s_add_u32 m0, s65, 114688
EX v41,v41
MM a[40:43],a[144:147],v[72:75],a[40:43]
RD a[232:235],v226 offset:24576
CV v12,v40,v41
MM a[48:51],a[144:147],v[80:83],a[48:51]
EX v42,v42
MM a[48:51],a[148:151],v[84:87],a[48:51]
EX v43,v43
MM a[56:59],a[148:151],v[92:95],a[56:59]
RD a[236:239],v227 offset:24576
CV v13,v42,v43
MM a[56:59],a[144:147],v[88:91],a[56:59]
EX v44,v44
MM a[60:63],a[152:155],v[88:91],a[60:63]
buffer_load_dwordx4 v235, s[12:15], s81 offen offset:0 lds
MM a[60:63],a[156:159],v[92:95],a[60:63]
EX v45,v45
MM a[52:55],a[156:159],v[84:87],a[52:55]
RD a[240:243],v224 offset:28672
CV v14,v44,v45
MM a[52:55],a[152:155],v[80:83],a[52:55]
EX v46,v46
MM a[44:47],a[152:155],v[72:75],a[44:47]
EX v47,v47
MM a[44:47],a[156:159],v[76:79],a[44:47]
RD a[244:247],v225 offset:28672
CV v15,v46,v47
MM a[36:39],a[156:159],v[68:71],a[36:39]
buffer_load_dwordx4 v235, s[12:15], s64 offen offset:1024 lds
MM a[36:39],a[152:155],v[64:67],a[36:39]
EX v48,v48
MM a[64:67],a[160:163],v[64:67],a[64:67]
EX v49,v49
MM a[64:67],a[164:167],v[68:71],a[64:67]
RD a[248:251],v226 offset:28672
CV v20,v48,v49
MM a[72:75],a[164:167],v[76:79],a[72:75]
EX v50,v50
MM a[72:75],a[160:163],v[72:75],a[72:75]
EX v51,v51
MM a[80:83],a[160:163],v[80:83],a[80:83]
RD a[252:255],v227 offset:28672
CV v21,v50,v51
MM a[80:83],a[164:167],v[84:87],a[80:83]
buffer_load_dwordx4 v235, s[12:15], s81 offen offset:2048 lds
MM a[88:91],a[164:167],v[92:95],a[88:91]
EX v52,v52
MM a[88:91],a[160:163],v[88:91],a[88:91]
EX v53,v53
MM a[92:95],a[168:171],v[88:91],a[92:95]
EX v54,v54
MM a[92:95],a[172:175],v[92:95],a[92:95]
buffer_load_dwordx4 v235, s[12:15], s64 offen offset:3072 lds
MM a[84:87],a[172:175],v[84:87],a[84:87]
EX v55,v55
MM a[84:87],a[168:171],v[80:83],a[84:87]
EX v56,v56
MM a[76:79],a[168:171],v[72:75],a[76:79]
EX v57,v57
MM a[76:79],a[172:175],v[76:79],a[76:79]
EX v58,v58
MM a[68:71],a[172:175],v[68:71],a[68:71]
EX v59,v59
MM a[68:71],a[168:171],v[64:67],a[68:71]
EX v60,v60
MM a[96:99],a[176:179],v[64:67],a[96:99]
EX v61,v61
MM a[96:99],a[180:183],v[68:71],a[96:99]
EX v62,v62
MM a[104:107],a[180:183],v[76:79],a[104:107]
EX v63,v63
MM a[104:107],a[176:179],v[72:75],a[104:107]
CV v22,v52,v53
CV v23,v54,v55
MM a[112:115],a[176:179],v[80:83],a[112:115]
CV v28,v56,v57
CV v29,v58,v59
MM a[112:115],a[180:183],v[84:87],a[112:115]
CV v30,v60,v61
CV v31,v62,v63
MM a[120:123],a[180:183],v[92:95],a[120:123]
MM a[120:123],a[176:179],v[88:91],a[120:123]
MM a[124:127],a[184:187],v[88:91],a[124:127]
MM a[124:127],a[188:191],v[92:95],a[124:127]
MM a[116:119],a[188:191],v[84:87],a[116:119]
MM a[116:119],a[184:187],v[80:83],a[116:119]
MM a[108:111],a[184:187],v[72:75],a[108:111]
MM a[108:111],a[188:191],v[76:79],a[108:111]
MM a[100:103],a[188:191],v[68:71],a[100:103]
MM a[100:103],a[184:187],v[64:67],a[100:103]
v_smfmac_f32_16x16x64_bf16 v[236:239], v[144:147], v[0:7], v234
v_smfmac_f32_16x16x64_bf16 v[236:239], v[148:151], v[8:15], v234
v_smfmac_f32_16x16x64_bf16 v[236:239], v[152:155], v[16:23], v234
v_smfmac_f32_16x16x64_bf16 v[236:239], v[156:159], v[24:31], v234
s_waitcnt lgkmcnt(0)
MM v[64:67],a[192:195],v[160:163],v[128:131]
s_add_u32 s64, s64, 16384
TR a[128:129],v232 offset:0
s_add_u32 s81, s64, 896
MM v[64:67],a[196:199],v[176:179],v[64:67]
TR a[130:131],v232 offset:4096
TR a[132:133],v232 offset:8192
MM v[64:67],a[200:203],v[192:195],v[64:67]
TR a[134:135],v232 offset:12288
TR a[136:137],v233 offset:0
MM v[64:67],a[204:207],v[208:211],v[64:67]
TR a[138:139],v233 offset:4096
v_add_f32 v246, v236, v237
MM v[68:71],a[220:223],v[208:211],v[128:131]
TR a[140:141],v233 offset:8192
v_cmp_nge_f32 vcc, s71, v246
MM v[68:71],a[216:219],v[192:195],v[68:71]
s_cbranch_vccnz LSITE20_%=
LBACK20_%=:
TR a[142:143],v233 offset:12288
TR a[144:145],v232 offset:512
MM v[68:71],a[212:215],v[176:179],v[68:71]
v_bfe_u32 v242, v248, 6, 3
v_mul_u32_u24 v242, 1280, v242
MM v[68:71],a[208:211],v[160:163],v[68:71]
v_and_b32 v240, 48, v248
v_bfe_u32 v243, v248, 8, 1
MM v[76:79],a[208:211],v[164:167],v[132:135]
v_lshlrev_b32 v243, 5, v243
v_xor_b32 v240, v240, v243
MM v[76:79],a[212:215],v[180:183],v[76:79]
v_add_u32 v240, v240, v242
v_lshrrev_b32 v243, 9, v248
MM v[76:79],a[216:219],v[196:199],v[76:79]
v_mul_u32_u24 v242, 10240, v243
v_add_u32 v241, v240, v242
MM v[76:79],a[220:223],v[212:215],v[76:79]
TR a[146:147],v232 offset:4608
v_lshl_add_u32 v240, v243, 6, v240
MM v[72:75],a[204:207],v[212:215],v[132:135]
s_and_b32 s66, s84, 1
EX v64,v64
MM v[72:75],a[200:203],v[196:199],v[72:75]
s_lshl_b32 s94, s66, 6
TR a[148:149],v232 offset:8704
v_add_u32 v240, s94, v240
MM v[72:75],a[196:199],v[180:183],v[72:75]
s_xor_b32 s66, s66, 1
EX v65,v65
MM v[72:75],a[192:195],v[164:167],v[72:75]
s_lshl_b32 s66, s66, 8
v_add_u32 v241, s66, v241
CV v64,v64,v65
MM v[80:83],a[192:195],v[168:171],v[136:139]
s_mul_i32 s89, %[swave], 20480
EX v66,v66
MM v[80:83],a[196:199],v[184:187],v[80:83]
s_lshl_b32 s94, %[swave], 10
EX v67,v67
MM v[80:83],a[200:203],v[200:203],v[80:83]
s_add_u32 s94, s94, %[slds]
EX v68,v68
MM v[80:83],a[204:207],v[216:219],v[80:83]
s_xor_b32 s66, s84, 0
EX v69,v69
MM v[84:87],a[220:223],v[216:219],v[136:139]
s_mul_i32 s66, s66, 163840
CV v65,v66,v67
CV v66,v68,v69
MM v[84:87],a[216:219],v[200:203],v[84:87]
s_add_u32 s85, s66, s89
EX v70,v70
MM v[84:87],a[212:215],v[184:187],v[84:87]
s_xor_b32 s66, s84, 1
TR a[150:151],v232 offset:12800
TR a[152:153],v233 offset:512
MM v[84:87],a[208:211],v[168:171],v[84:87]
s_mul_i32 s66, s66, 163840
EX v71,v71
MM v[92:95],a[208:211],v[172:175],v[140:143]
s_add_u32 s86, s66, s89
s_add_u32 m0, s45, 147456
s_add_u32 s44, s44, 1024
buffer_load_dwordx4 v248, s[24:27], s44 offen offset:0 lds
MM v[92:95],a[212:215],v[188:191],v[92:95]
s_xor_b32 s66, s84, 2
EX v72,v72
MM v[92:95],a[216:219],v[204:207],v[92:95]
s_mul_i32 s66, s66, 163840
TR a[154:155],v233 offset:4608
CV v67,v70,v71
MM v[92:95],a[220:223],v[220:223],v[92:95]
s_add_u32 s87, s66, s89
EX v73,v73
MM v[88:91],a[204:207],v[220:223],v[140:143]
s_xor_b32 s66, s84, 3
TR a[156:157],v233 offset:8704
TR a[158:159],v233 offset:12800
MM v[88:91],a[200:203],v[204:207],v[88:91]
s_mul_i32 s66, s66, 163840
EX v74,v74
MM v[88:91],a[196:199],v[188:191],v[88:91]
s_add_u32 s88, s66, s89
EX v75,v75
MM v[88:91],a[192:195],v[172:175],v[88:91]
s_lshl_b32 s89, %[swave], 11
TR a[160:161],v232 offset:1024
CV v72,v72,v73
MM v[96:99],a[224:227],v[160:163],v[128:131]
s_add_u32 s89, s89, %[slds]
EX v76,v76
MM v[96:99],a[228:231],v[176:179],v[96:99]
s_lshr_b32 s96, s49, 2
TR a[162:163],v232 offset:5120
CV v73,v74,v75
MM v[96:99],a[232:235],v[192:195],v[96:99]
s_lshl_b32 s97, s50, 3
EX v77,v77
MM v[96:99],a[236:239],v[208:211],v[96:99]
s_add_u32 s96, s96, s97
TR a[164:165],v232 offset:9216
TR a[166:167],v232 offset:13312
MM v[100:103],a[252:255],v[208:211],v[128:131]
s_lshl_b32 s96, s96, 2
EX v78,v78
MM v[100:103],a[248:251],v[192:195],v[100:103]
s_add_u32 s96, s96, %[swave]
EX v79,v79
MM v[100:103],a[244:247],v[176:179],v[100:103]
s_lshl_b32 s96, s96, 6
TR a[168:169],v233 offset:1024
CV v74,v76,v77
MM v[100:103],a[240:243],v[160:163],v[100:103]
s_add_u32 s96, s96, 0
buffer_load_dword v242, off, s[20:23], s96 sc1
MM v[108:111],a[240:243],v[164:167],v[132:135]
buffer_load_dword v243, off, s[20:23], s19 sc1
MM v[108:111],a[244:247],v[180:183],v[108:111]
EX v80,v80
MM v[108:111],a[248:251],v[196:199],v[108:111]
EX v81,v81
MM v[108:111],a[252:255],v[212:215],v[108:111]
TR a[170:171],v233 offset:5120
TR a[172:173],v233 offset:9216
MM v[104:107],a[236:239],v[212:215],v[132:135]
EX v82,v82
MM v[104:107],a[232:235],v[196:199],v[104:107]
TR a[174:175],v233 offset:13312
CV v75,v78,v79
MM v[104:107],a[228:231],v[180:183],v[104:107]
EX v83,v83
MM v[104:107],a[224:227],v[164:167],v[104:107]
TR a[176:177],v232 offset:1536
CV v80,v80,v81
MM v[112:115],a[224:227],v[168:171],v[136:139]
EX v84,v84
MM v[112:115],a[228:231],v[184:187],v[112:115]
TR a[178:179],v232 offset:5632
TR a[180:181],v232 offset:9728
MM v[112:115],a[232:235],v[200:203],v[112:115]
EX v85,v85
MM v[112:115],a[236:239],v[216:219],v[112:115]
EX v86,v86
MM v[116:119],a[252:255],v[216:219],v[136:139]
TR a[182:183],v232 offset:13824
CV v81,v82,v83
MM v[116:119],a[248:251],v[200:203],v[116:119]
EX v87,v87
MM v[116:119],a[244:247],v[184:187],v[116:119]
TR a[184:185],v233 offset:1536
CV v82,v84,v85
MM v[116:119],a[240:243],v[168:171],v[116:119]
EX v88,v88
MM v[124:127],a[240:243],v[172:175],v[140:143]
TR a[186:187],v233 offset:5632
CV v83,v86,v87
MM v[124:127],a[244:247],v[188:191],v[124:127]
EX v89,v89
MM v[124:127],a[248:251],v[204:207],v[124:127]
TR a[188:189],v233 offset:9728
TR a[190:191],v233 offset:13824
MM v[124:127],a[252:255],v[220:223],v[124:127]
EX v90,v90
MM v[120:123],a[236:239],v[220:223],v[140:143]
EX v91,v91
MM v[120:123],a[232:235],v[204:207],v[120:123]
EX v92,v92
MM v[120:123],a[228:231],v[188:191],v[120:123]
EX v93,v93
MM v[120:123],a[224:227],v[172:175],v[120:123]
s_waitcnt vmcnt(18) lgkmcnt(0)
s_barrier
MM a[0:3],a[128:131],v[0:3],a[0:3]
CV v88,v88,v89
CV v89,v90,v91
MM a[0:3],a[132:135],v[4:7],a[0:3]
s_mul_i32 s76, s46, 192
EX v94,v94
MM a[8:11],a[132:135],v[12:15],a[8:11]
s_add_u32 s76, s76, 69632
v_lshrrev_b32 v253, 2, v248
v_min_u32 v253, 188, v253
MM a[8:11],a[128:131],v[8:11],a[8:11]
s_add_u32 m0, s65, 0
buffer_load_dword v252, v253, s[20:23], s76 offen offset:0 sc1
MM a[16:19],a[128:131],v[16:19],a[16:19]
EX v95,v95
MM a[16:19],a[132:135],v[20:23],a[16:19]
RD a[192:195],v224 offset:32768
CV v90,v92,v93
MM a[24:27],a[132:135],v[28:31],a[24:27]
EX v96,v96
MM a[24:27],a[128:131],v[24:27],a[24:27]
EX v97,v97
MM a[28:31],a[136:139],v[24:27],a[28:31]
RD a[196:199],v225 offset:32768
CV v91,v94,v95
MM a[28:31],a[140:143],v[28:31],a[28:31]
EX v98,v98
MM a[20:23],a[140:143],v[20:23],a[20:23]
buffer_load_dwordx4 v228, s[8:11], s64 offen offset:0 lds
MM a[20:23],a[136:139],v[16:19],a[20:23]
EX v99,v99
MM a[12:15],a[136:139],v[8:11],a[12:15]
RD a[200:203],v226 offset:32768
CV v68,v96,v97
MM a[12:15],a[140:143],v[12:15],a[12:15]
EX v100,v100
MM a[4:7],a[140:143],v[4:7],a[4:7]
EX v101,v101
MM a[4:7],a[136:139],v[0:3],a[4:7]
RD a[204:207],v227 offset:32768
CV v69,v98,v99
MM a[32:35],a[144:147],v[0:3],a[32:35]
EX v102,v102
MM a[32:35],a[148:151],v[4:7],a[32:35]
buffer_load_dwordx4 v229, s[8:11], s64 offen offset:1024 lds
MM a[40:43],a[148:151],v[12:15],a[40:43]
EX v103,v103
MM a[40:43],a[144:147],v[8:11],a[40:43]
RD a[208:211],v224 offset:36864
CV v70,v100,v101
MM a[48:51],a[144:147],v[16:19],a[48:51]
EX v104,v104
MM a[48:51],a[148:151],v[20:23],a[48:51]
RD a[212:215],v225 offset:36864
CV v71,v102,v103
MM a[56:59],a[148:151],v[28:31],a[56:59]
EX v105,v105
MM a[56:59],a[144:147],v[24:27],a[56:59]
buffer_load_dwordx4 v230, s[8:11], s64 offen offset:2048 lds
MM a[60:63],a[152:155],v[24:27],a[60:63]
EX v106,v106
MM a[60:63],a[156:159],v[28:31],a[60:63]
EX v107,v107
MM a[52:55],a[156:159],v[20:23],a[52:55]
RD a[216:219],v226 offset:36864
CV v76,v104,v105
MM a[52:55],a[152:155],v[16:19],a[52:55]
EX v108,v108
MM a[44:47],a[152:155],v[8:11],a[44:47]
RD a[220:223],v227 offset:36864
CV v77,v106,v107
MM a[44:47],a[156:159],v[12:15],a[44:47]
EX v109,v109
MM a[36:39],a[156:159],v[4:7],a[36:39]
)ASM"
R"ASM(
buffer_load_dwordx4 v231, s[8:11], s64 offen offset:3072 lds
MM a[36:39],a[152:155],v[0:3],a[36:39]
s_add_u32 m0, s65, 65536
EX v110,v110
MM a[64:67],a[160:163],v[0:3],a[64:67]
RD a[224:227],v224 offset:40960
CV v78,v108,v109
MM a[64:67],a[164:167],v[4:7],a[64:67]
EX v111,v111
MM a[72:75],a[164:167],v[12:15],a[72:75]
EX v112,v112
MM a[72:75],a[160:163],v[8:11],a[72:75]
RD a[228:231],v225 offset:40960
CV v79,v110,v111
MM a[80:83],a[160:163],v[16:19],a[80:83]
EX v113,v113
MM a[80:83],a[164:167],v[20:23],a[80:83]
buffer_load_dwordx4 v235, s[12:15], s81 offen offset:0 lds
MM a[88:91],a[164:167],v[28:31],a[88:91]
EX v114,v114
MM a[88:91],a[160:163],v[24:27],a[88:91]
RD a[232:235],v226 offset:40960
CV v84,v112,v113
MM a[92:95],a[168:171],v[24:27],a[92:95]
EX v115,v115
MM a[92:95],a[172:175],v[28:31],a[92:95]
RD a[236:239],v227 offset:40960
CV v85,v114,v115
MM a[84:87],a[172:175],v[20:23],a[84:87]
EX v116,v116
MM a[84:87],a[168:171],v[16:19],a[84:87]
EX v117,v117
MM a[76:79],a[168:171],v[8:11],a[76:79]
RD a[240:243],v224 offset:45056
CV v86,v116,v117
MM a[76:79],a[172:175],v[12:15],a[76:79]
EX v118,v118
MM a[68:71],a[172:175],v[4:7],a[68:71]
buffer_load_dwordx4 v235, s[12:15], s64 offen offset:1024 lds
MM a[68:71],a[168:171],v[0:3],a[68:71]
EX v119,v119
MM a[96:99],a[176:179],v[0:3],a[96:99]
RD a[244:247],v225 offset:45056
CV v87,v118,v119
MM a[96:99],a[180:183],v[4:7],a[96:99]
EX v120,v120
MM a[104:107],a[180:183],v[12:15],a[104:107]
EX v121,v121
MM a[104:107],a[176:179],v[8:11],a[104:107]
RD a[248:251],v226 offset:45056
CV v92,v120,v121
MM a[112:115],a[176:179],v[16:19],a[112:115]
buffer_load_dwordx4 v235, s[12:15], s81 offen offset:2048 lds
MM a[112:115],a[180:183],v[20:23],a[112:115]
EX v122,v122
MM a[120:123],a[180:183],v[28:31],a[120:123]
EX v123,v123
MM a[120:123],a[176:179],v[24:27],a[120:123]
RD a[252:255],v227 offset:45056
CV v93,v122,v123
MM a[124:127],a[184:187],v[24:27],a[124:127]
EX v124,v124
MM a[124:127],a[188:191],v[28:31],a[124:127]
EX v125,v125
MM a[116:119],a[188:191],v[20:23],a[116:119]
buffer_load_dwordx4 v235, s[12:15], s64 offen offset:3072 lds
MM a[116:119],a[184:187],v[16:19],a[116:119]
EX v126,v126
MM a[108:111],a[184:187],v[8:11],a[108:111]
EX v127,v127
MM a[108:111],a[188:191],v[12:15],a[108:111]
CV v94,v124,v125
CV v95,v126,v127
MM a[100:103],a[188:191],v[4:7],a[100:103]
MM a[100:103],a[184:187],v[0:3],a[100:103]
v_smfmac_f32_16x16x64_bf16 v[236:239], v[144:147], v[64:71], v234
v_smfmac_f32_16x16x64_bf16 v[236:239], v[148:151], v[72:79], v234
v_smfmac_f32_16x16x64_bf16 v[236:239], v[152:155], v[80:87], v234
v_smfmac_f32_16x16x64_bf16 v[236:239], v[156:159], v[88:95], v234
s_waitcnt lgkmcnt(0)
MM v[0:3],a[192:195],v[160:163],v[128:131]
s_add_u32 s64, s64, 16384
TR a[128:129],v232 offset:16384
s_add_u32 s81, s64, 896
MM v[0:3],a[196:199],v[176:179],v[0:3]
TR a[130:131],v232 offset:20480
TR a[132:133],v232 offset:24576
MM v[0:3],a[200:203],v[192:195],v[0:3]
TR a[134:135],v232 offset:28672
TR a[136:137],v233 offset:16384
MM v[0:3],a[204:207],v[208:211],v[0:3]
TR a[138:139],v233 offset:20480
v_add_f32 v247, v236, v237
MM v[4:7],a[220:223],v[208:211],v[128:131]
TR a[140:141],v233 offset:24576
v_cmp_nge_f32 vcc, s71, v247
MM v[4:7],a[216:219],v[192:195],v[4:7]
s_cbranch_vccnz LSITE21_%=
LBACK21_%=:
TR a[142:143],v233 offset:28672
TR a[144:145],v232 offset:16896
MM v[4:7],a[212:215],v[176:179],v[4:7]
TR a[146:147],v232 offset:20992
TR a[148:149],v232 offset:25088
MM v[4:7],a[208:211],v[160:163],v[4:7]
TR a[150:151],v232 offset:29184
TR a[152:153],v233 offset:16896
MM v[12:15],a[208:211],v[164:167],v[132:135]
EX v0,v0
MM v[12:15],a[212:215],v[180:183],v[12:15]
EX v1,v1
MM v[12:15],a[216:219],v[196:199],v[12:15]
TR a[154:155],v233 offset:20992
CV v0,v0,v1
MM v[12:15],a[220:223],v[212:215],v[12:15]
EX v2,v2
s_nop 0
LPG16_%=:
s_cbranch_execz LTR1_%=
MM v[8:11],a[204:207],v[212:215],v[132:135]
EX v3,v3
MM v[8:11],a[200:203],v[196:199],v[8:11]
TR a[156:157],v233 offset:25088
CV v1,v2,v3
MM v[8:11],a[196:199],v[180:183],v[8:11]
EX v4,v4
MM v[8:11],a[192:195],v[164:167],v[8:11]
EX v5,v5
MM v[16:19],a[192:195],v[168:171],v[136:139]
TR a[158:159],v233 offset:29184
CV v2,v4,v5
MM v[16:19],a[196:199],v[184:187],v[16:19]
EX v6,v6
MM v[16:19],a[200:203],v[200:203],v[16:19]
EX v7,v7
MM v[16:19],a[204:207],v[216:219],v[16:19]
TR a[160:161],v232 offset:17408
CV v3,v6,v7
MM v[20:23],a[220:223],v[216:219],v[136:139]
EX v8,v8
MM v[20:23],a[216:219],v[200:203],v[20:23]
EX v9,v9
MM v[20:23],a[212:215],v[184:187],v[20:23]
TR a[162:163],v232 offset:21504
CV v8,v8,v9
MM v[20:23],a[208:211],v[168:171],v[20:23]
EX v10,v10
MM v[28:31],a[208:211],v[172:175],v[140:143]
TR a[164:165],v232 offset:25600
TR a[166:167],v232 offset:29696
MM v[28:31],a[212:215],v[188:191],v[28:31]
EX v11,v11
MM v[28:31],a[216:219],v[204:207],v[28:31]
TR a[168:169],v233 offset:17408
CV v9,v10,v11
MM v[28:31],a[220:223],v[220:223],v[28:31]
EX v12,v12
MM v[24:27],a[204:207],v[220:223],v[140:143]
EX v13,v13
MM v[24:27],a[200:203],v[204:207],v[24:27]
TR a[170:171],v233 offset:21504
CV v10,v12,v13
MM v[24:27],a[196:199],v[188:191],v[24:27]
EX v14,v14
MM v[24:27],a[192:195],v[172:175],v[24:27]
TR a[172:173],v233 offset:25600
TR a[174:175],v233 offset:29696
MM v[32:35],a[224:227],v[160:163],v[128:131]
EX v15,v15
MM v[32:35],a[228:231],v[176:179],v[32:35]
TR a[176:177],v232 offset:17920
CV v11,v14,v15
MM v[32:35],a[232:235],v[192:195],v[32:35]
EX v16,v16
MM v[32:35],a[236:239],v[208:211],v[32:35]
TR a[178:179],v232 offset:22016
TR a[180:181],v232 offset:26112
MM v[36:39],a[252:255],v[208:211],v[128:131]
EX v17,v17
MM v[36:39],a[248:251],v[192:195],v[36:39]
TR a[182:183],v232 offset:30208
CV v16,v16,v17
MM v[36:39],a[244:247],v[176:179],v[36:39]
EX v18,v18
MM v[36:39],a[240:243],v[160:163],v[36:39]
EX v19,v19
MM v[44:47],a[240:243],v[164:167],v[132:135]
TR a[184:185],v233 offset:17920
CV v17,v18,v19
MM v[44:47],a[244:247],v[180:183],v[44:47]
EX v20,v20
MM v[44:47],a[248:251],v[196:199],v[44:47]
TR a[186:187],v233 offset:22016
TR a[188:189],v233 offset:26112
MM v[44:47],a[252:255],v[212:215],v[44:47]
EX v21,v21
MM v[40:43],a[236:239],v[212:215],v[132:135]
TR a[190:191],v233 offset:30208
CV v18,v20,v21
MM v[40:43],a[232:235],v[196:199],v[40:43]
EX v22,v22
MM v[40:43],a[228:231],v[180:183],v[40:43]
EX v23,v23
MM v[40:43],a[224:227],v[164:167],v[40:43]
CV v19,v22,v23
MM v[48:51],a[224:227],v[168:171],v[136:139]
EX v24,v24
MM v[48:51],a[228:231],v[184:187],v[48:51]
EX v25,v25
MM v[48:51],a[232:235],v[200:203],v[48:51]
EX v26,v26
MM v[48:51],a[236:239],v[216:219],v[48:51]
EX v27,v27
MM v[52:55],a[252:255],v[216:219],v[136:139]
EX v28,v28
MM v[52:55],a[248:251],v[200:203],v[52:55]
EX v29,v29
MM v[52:55],a[244:247],v[184:187],v[52:55]
EX v30,v30
MM v[52:55],a[240:243],v[168:171],v[52:55]
EX v31,v31
MM v[60:63],a[240:243],v[172:175],v[140:143]
EX v32,v32
MM v[60:63],a[244:247],v[188:191],v[60:63]
EX v33,v33
MM v[60:63],a[248:251],v[204:207],v[60:63]
EX v34,v34
MM v[60:63],a[252:255],v[220:223],v[60:63]
EX v35,v35
MM v[56:59],a[236:239],v[220:223],v[140:143]
EX v36,v36
MM v[56:59],a[232:235],v[204:207],v[56:59]
EX v37,v37
MM v[56:59],a[228:231],v[188:191],v[56:59]
EX v38,v38
MM v[56:59],a[224:227],v[172:175],v[56:59]
s_waitcnt vmcnt(16) lgkmcnt(0)
s_barrier
MM a[0:3],a[128:131],v[64:67],a[0:3]
s_waitcnt vmcnt(8)
v_cmp_ne_u32 vcc, s69, v252
s_cbranch_vccnz LRFH_%=
LRFHb_%=:
MM a[0:3],a[132:135],v[68:71],a[0:3]
s_add_u32 m0, s65, 16384
v_lshrrev_b32 v253, 7, v248
v_mul_u32_u24 v253, 3328, v253
MM a[8:11],a[132:135],v[76:79],a[8:11]
v_add_u32 v253, v253, v248
v_lshrrev_b32 v252, 6, v248
MM a[8:11],a[128:131],v[72:75],a[8:11]
v_mul_u32_u24 v252, 3392, v252
v_add_u32 v252, v252, v248
MM a[16:19],a[128:131],v[80:83],a[16:19]
s_add_u32 s76, s76, 21557248
CV v24,v24,v25
CV v25,v26,v27
MM a[16:19],a[132:135],v[84:87],a[16:19]
s_lshl_b32 s90, %[swave], 11
RD a[192:195],v224 offset:49152
CV v26,v28,v29
MM a[24:27],a[132:135],v[92:95],a[24:27]
s_add_u32 s90, s90, %[slds]
s_add_u32 m0, s90, 139264
s_nop 0
buffer_load_dwordx4 v253, s[20:23], s76 offen offset:0 sc1 lds
MM a[24:27],a[128:131],v[88:91],a[24:27]
RD a[196:199],v225 offset:49152
CV v27,v30,v31
MM a[28:31],a[136:139],v[88:91],a[28:31]
s_add_u32 s76, s76, 128
s_add_u32 m0, s90, 140288
s_mov_b32 exec_hi, 0
buffer_load_dwordx4 v252, s[20:23], s76 offen offset:0 sc1 lds
s_mov_b64 exec, -1
MM a[28:31],a[140:143],v[92:95],a[28:31]
RD a[200:203],v226 offset:49152
CV v4,v32,v33
MM a[20:23],a[140:143],v[84:87],a[20:23]
s_add_u32 m0, s65, 16384
s_nop 0
buffer_load_dwordx4 v228, s[8:11], s64 offen offset:0 lds
MM a[20:23],a[136:139],v[80:83],a[20:23]
RD a[204:207],v227 offset:49152
CV v5,v34,v35
MM a[12:15],a[136:139],v[72:75],a[12:15]
RD a[208:211],v224 offset:53248
CV v6,v36,v37
MM a[12:15],a[140:143],v[76:79],a[12:15]
buffer_load_dwordx4 v229, s[8:11], s64 offen offset:1024 lds
MM a[4:7],a[140:143],v[68:71],a[4:7]
RD a[212:215],v225 offset:53248
RD a[216:219],v226 offset:53248
MM a[4:7],a[136:139],v[64:67],a[4:7]
EX v39,v39
MM a[32:35],a[144:147],v[64:67],a[32:35]
RD a[220:223],v227 offset:53248
CV v7,v38,v39
MM a[32:35],a[148:151],v[68:71],a[32:35]
buffer_load_dwordx4 v230, s[8:11], s64 offen offset:2048 lds
MM a[40:43],a[148:151],v[76:79],a[40:43]
EX v40,v40
MM a[40:43],a[144:147],v[72:75],a[40:43]
EX v41,v41
MM a[48:51],a[144:147],v[80:83],a[48:51]
RD a[224:227],v224 offset:57344
CV v12,v40,v41
MM a[48:51],a[148:151],v[84:87],a[48:51]
EX v42,v42
MM a[56:59],a[148:151],v[92:95],a[56:59]
buffer_load_dwordx4 v231, s[8:11], s64 offen offset:3072 lds
MM a[56:59],a[144:147],v[88:91],a[56:59]
s_add_u32 m0, s65, 81920
EX v43,v43
MM a[60:63],a[152:155],v[88:91],a[60:63]
RD a[228:231],v225 offset:57344
CV v13,v42,v43
MM a[60:63],a[156:159],v[92:95],a[60:63]
EX v44,v44
MM a[52:55],a[156:159],v[84:87],a[52:55]
EX v45,v45
MM a[52:55],a[152:155],v[80:83],a[52:55]
RD a[232:235],v226 offset:57344
CV v14,v44,v45
MM a[44:47],a[152:155],v[72:75],a[44:47]
EX v46,v46
MM a[44:47],a[156:159],v[76:79],a[44:47]
buffer_load_dwordx4 v235, s[12:15], s81 offen offset:0 lds
MM a[36:39],a[156:159],v[68:71],a[36:39]
EX v47,v47
MM a[36:39],a[152:155],v[64:67],a[36:39]
RD a[236:239],v227 offset:57344
CV v15,v46,v47
MM a[64:67],a[160:163],v[64:67],a[64:67]
EX v48,v48
MM a[64:67],a[164:167],v[68:71],a[64:67]
EX v49,v49
MM a[72:75],a[164:167],v[76:79],a[72:75]
RD a[240:243],v224 offset:61440
CV v20,v48,v49
MM a[72:75],a[160:163],v[72:75],a[72:75]
buffer_load_dwordx4 v235, s[12:15], s64 offen offset:1024 lds
MM a[80:83],a[160:163],v[80:83],a[80:83]
EX v50,v50
MM a[80:83],a[164:167],v[84:87],a[80:83]
EX v51,v51
MM a[88:91],a[164:167],v[92:95],a[88:91]
RD a[244:247],v225 offset:61440
CV v21,v50,v51
MM a[88:91],a[160:163],v[88:91],a[88:91]
EX v52,v52
MM a[92:95],a[168:171],v[88:91],a[92:95]
EX v53,v53
MM a[92:95],a[172:175],v[92:95],a[92:95]
RD a[248:251],v226 offset:61440
CV v22,v52,v53
MM a[84:87],a[172:175],v[84:87],a[84:87]
buffer_load_dwordx4 v235, s[12:15], s81 offen offset:2048 lds
MM a[84:87],a[168:171],v[80:83],a[84:87]
EX v54,v54
MM a[76:79],a[168:171],v[72:75],a[76:79]
EX v55,v55
MM a[76:79],a[172:175],v[76:79],a[76:79]
RD a[252:255],v227 offset:61440
CV v23,v54,v55
MM a[68:71],a[172:175],v[68:71],a[68:71]
EX v56,v56
MM a[68:71],a[168:171],v[64:67],a[68:71]
EX v57,v57
MM a[96:99],a[176:179],v[64:67],a[96:99]
buffer_load_dwordx4 v235, s[12:15], s64 offen offset:3072 lds
MM a[96:99],a[180:183],v[68:71],a[96:99]
EX v58,v58
MM a[104:107],a[180:183],v[76:79],a[104:107]
EX v59,v59
MM a[104:107],a[176:179],v[72:75],a[104:107]
EX v60,v60
MM a[112:115],a[176:179],v[80:83],a[112:115]
EX v61,v61
MM a[112:115],a[180:183],v[84:87],a[112:115]
EX v62,v62
MM a[120:123],a[180:183],v[92:95],a[120:123]
EX v63,v63
MM a[120:123],a[176:179],v[88:91],a[120:123]
CV v28,v56,v57
CV v29,v58,v59
MM a[124:127],a[184:187],v[88:91],a[124:127]
CV v30,v60,v61
CV v31,v62,v63
MM a[124:127],a[188:191],v[92:95],a[124:127]
MM a[116:119],a[188:191],v[84:87],a[116:119]
MM a[116:119],a[184:187],v[80:83],a[116:119]
MM a[108:111],a[184:187],v[72:75],a[108:111]
)ASM"
R"ASM(
MM a[108:111],a[188:191],v[76:79],a[108:111]
MM a[100:103],a[188:191],v[68:71],a[100:103]
MM a[100:103],a[184:187],v[64:67],a[100:103]
v_smfmac_f32_16x16x64_bf16 v[236:239], v[144:147], v[0:7], v234
v_smfmac_f32_16x16x64_bf16 v[236:239], v[148:151], v[8:15], v234
v_smfmac_f32_16x16x64_bf16 v[236:239], v[152:155], v[16:23], v234
v_smfmac_f32_16x16x64_bf16 v[236:239], v[156:159], v[24:31], v234
s_waitcnt lgkmcnt(0)
MM v[64:67],a[192:195],v[160:163],v[128:131]
s_add_u32 s64, s64, 16384
TR a[128:129],v232 offset:32768
s_add_u32 s81, s64, 896
MM v[64:67],a[196:199],v[176:179],v[64:67]
TR a[130:131],v232 offset:36864
TR a[132:133],v232 offset:40960
MM v[64:67],a[200:203],v[192:195],v[64:67]
TR a[134:135],v232 offset:45056
TR a[136:137],v233 offset:32768
MM v[64:67],a[204:207],v[208:211],v[64:67]
TR a[138:139],v233 offset:36864
v_add_f32 v246, v236, v237
MM v[68:71],a[220:223],v[208:211],v[128:131]
TR a[140:141],v233 offset:40960
v_cmp_nge_f32 vcc, s71, v246
MM v[68:71],a[216:219],v[192:195],v[68:71]
s_cbranch_vccnz LSITE22_%=
LBACK22_%=:
TR a[142:143],v233 offset:45056
TR a[144:145],v232 offset:33280
MM v[68:71],a[212:215],v[176:179],v[68:71]
s_add_u32 m0, s94, 131072
s_add_u32 s66, s85, 0
buffer_load_dwordx4 v241, s[24:27], s66 offen offset:0 lds
MM v[68:71],a[208:211],v[160:163],v[68:71]
TR a[146:147],v232 offset:37376
TR a[148:149],v232 offset:41472
MM v[76:79],a[208:211],v[164:167],v[132:135]
EX v64,v64
MM v[76:79],a[212:215],v[180:183],v[76:79]
EX v65,v65
MM v[76:79],a[216:219],v[196:199],v[76:79]
TR a[150:151],v232 offset:45568
CV v64,v64,v65
MM v[76:79],a[220:223],v[212:215],v[76:79]
EX v66,v66
MM v[72:75],a[204:207],v[212:215],v[132:135]
EX v67,v67
MM v[72:75],a[200:203],v[196:199],v[72:75]
TR a[152:153],v233 offset:33280
CV v65,v66,v67
MM v[72:75],a[196:199],v[180:183],v[72:75]
s_add_u32 m0, s94, 135168
s_add_u32 s66, s85, 81920
buffer_load_dwordx4 v241, s[24:27], s66 offen offset:0 lds
MM v[72:75],a[192:195],v[164:167],v[72:75]
EX v68,v68
MM v[80:83],a[192:195],v[168:171],v[136:139]
EX v69,v69
MM v[80:83],a[196:199],v[184:187],v[80:83]
TR a[154:155],v233 offset:37376
CV v66,v68,v69
MM v[80:83],a[200:203],v[200:203],v[80:83]
EX v70,v70
MM v[80:83],a[204:207],v[216:219],v[80:83]
EX v71,v71
MM v[84:87],a[220:223],v[216:219],v[136:139]
TR a[156:157],v233 offset:41472
CV v67,v70,v71
MM v[84:87],a[216:219],v[200:203],v[84:87]
EX v72,v72
MM v[84:87],a[212:215],v[184:187],v[84:87]
s_add_u32 m0, s94, 147456
s_add_u32 s66, s87, 0
buffer_load_dwordx4 v241, s[24:27], s66 offen offset:0 lds
MM v[84:87],a[208:211],v[168:171],v[84:87]
TR a[158:159],v233 offset:45568
TR a[160:161],v232 offset:33792
MM v[92:95],a[208:211],v[172:175],v[140:143]
EX v73,v73
MM v[92:95],a[212:215],v[188:191],v[92:95]
TR a[162:163],v232 offset:37888
CV v72,v72,v73
MM v[92:95],a[216:219],v[204:207],v[92:95]
EX v74,v74
MM v[92:95],a[220:223],v[220:223],v[92:95]
EX v75,v75
MM v[88:91],a[204:207],v[220:223],v[140:143]
TR a[164:165],v232 offset:41984
CV v73,v74,v75
MM v[88:91],a[200:203],v[204:207],v[88:91]
EX v76,v76
MM v[88:91],a[196:199],v[188:191],v[88:91]
TR a[166:167],v232 offset:46080
TR a[168:169],v233 offset:33792
MM v[88:91],a[192:195],v[172:175],v[88:91]
EX v77,v77
MM v[96:99],a[224:227],v[160:163],v[128:131]
TR a[170:171],v233 offset:37888
CV v74,v76,v77
MM v[96:99],a[228:231],v[176:179],v[96:99]
EX v78,v78
MM v[96:99],a[232:235],v[192:195],v[96:99]
EX v79,v79
MM v[96:99],a[236:239],v[208:211],v[96:99]
TR a[172:173],v233 offset:41984
CV v75,v78,v79
MM v[100:103],a[252:255],v[208:211],v[128:131]
EX v80,v80
MM v[100:103],a[248:251],v[192:195],v[100:103]
TR a[174:175],v233 offset:46080
TR a[176:177],v232 offset:34304
MM v[100:103],a[244:247],v[176:179],v[100:103]
EX v81,v81
MM v[100:103],a[240:243],v[160:163],v[100:103]
TR a[178:179],v232 offset:38400
CV v80,v80,v81
MM v[108:111],a[240:243],v[164:167],v[132:135]
EX v82,v82
MM v[108:111],a[244:247],v[180:183],v[108:111]
EX v83,v83
MM v[108:111],a[248:251],v[196:199],v[108:111]
TR a[180:181],v232 offset:42496
CV v81,v82,v83
MM v[108:111],a[252:255],v[212:215],v[108:111]
EX v84,v84
MM v[104:107],a[236:239],v[212:215],v[132:135]
TR a[182:183],v232 offset:46592
TR a[184:185],v233 offset:34304
MM v[104:107],a[232:235],v[196:199],v[104:107]
EX v85,v85
MM v[104:107],a[228:231],v[180:183],v[104:107]
TR a[186:187],v233 offset:38400
CV v82,v84,v85
MM v[104:107],a[224:227],v[164:167],v[104:107]
EX v86,v86
MM v[112:115],a[224:227],v[168:171],v[136:139]
TR a[188:189],v233 offset:42496
TR a[190:191],v233 offset:46592
MM v[112:115],a[228:231],v[184:187],v[112:115]
EX v87,v87
MM v[112:115],a[232:235],v[200:203],v[112:115]
CV v83,v86,v87
MM v[112:115],a[236:239],v[216:219],v[112:115]
EX v88,v88
MM v[116:119],a[252:255],v[216:219],v[136:139]
EX v89,v89
MM v[116:119],a[248:251],v[200:203],v[116:119]
EX v90,v90
MM v[116:119],a[244:247],v[184:187],v[116:119]
EX v91,v91
MM v[116:119],a[240:243],v[168:171],v[116:119]
EX v92,v92
MM v[124:127],a[240:243],v[172:175],v[140:143]
EX v93,v93
MM v[124:127],a[244:247],v[188:191],v[124:127]
EX v94,v94
MM v[124:127],a[248:251],v[204:207],v[124:127]
EX v95,v95
MM v[124:127],a[252:255],v[220:223],v[124:127]
EX v96,v96
MM v[120:123],a[236:239],v[220:223],v[140:143]
EX v97,v97
MM v[120:123],a[232:235],v[204:207],v[120:123]
EX v98,v98
MM v[120:123],a[228:231],v[188:191],v[120:123]
EX v99,v99
MM v[120:123],a[224:227],v[172:175],v[120:123]
s_waitcnt vmcnt(17) lgkmcnt(0)
s_barrier
MM a[0:3],a[128:131],v[0:3],a[0:3]
s_add_u32 m0, s65, 32768
CV v88,v88,v89
CV v89,v90,v91
MM a[0:3],a[132:135],v[4:7],a[0:3]
RD a[192:195],v224 offset:0
CV v90,v92,v93
MM a[8:11],a[132:135],v[12:15],a[8:11]
RD a[196:199],v225 offset:0
CV v91,v94,v95
MM a[8:11],a[128:131],v[8:11],a[8:11]
buffer_load_dwordx4 v228, s[8:11], s64 offen offset:0 lds
MM a[16:19],a[128:131],v[16:19],a[16:19]
RD a[200:203],v226 offset:0
CV v68,v96,v97
MM a[16:19],a[132:135],v[20:23],a[16:19]
RD a[204:207],v227 offset:0
CV v69,v98,v99
MM a[24:27],a[132:135],v[28:31],a[24:27]
buffer_load_dwordx4 v229, s[8:11], s64 offen offset:1024 lds
MM a[24:27],a[128:131],v[24:27],a[24:27]
RD a[208:211],v224 offset:4096
RD a[212:215],v225 offset:4096
MM a[28:31],a[136:139],v[24:27],a[28:31]
buffer_load_dwordx4 v230, s[8:11], s64 offen offset:2048 lds
MM a[28:31],a[140:143],v[28:31],a[28:31]
EX v100,v100
MM a[20:23],a[140:143],v[20:23],a[20:23]
EX v101,v101
MM a[20:23],a[136:139],v[16:19],a[20:23]
RD a[216:219],v226 offset:4096
CV v70,v100,v101
MM a[12:15],a[136:139],v[8:11],a[12:15]
EX v102,v102
MM a[12:15],a[140:143],v[12:15],a[12:15]
RD a[220:223],v227 offset:4096
RD a[224:227],v224 offset:8192
MM a[4:7],a[140:143],v[4:7],a[4:7]
EX v103,v103
MM a[4:7],a[136:139],v[0:3],a[4:7]
RD a[228:231],v225 offset:8192
CV v71,v102,v103
MM a[32:35],a[144:147],v[0:3],a[32:35]
EX v104,v104
MM a[32:35],a[148:151],v[4:7],a[32:35]
buffer_load_dwordx4 v231, s[8:11], s64 offen offset:3072 lds
MM a[40:43],a[148:151],v[12:15],a[40:43]
s_add_u32 m0, s65, 98304
EX v105,v105
MM a[40:43],a[144:147],v[8:11],a[40:43]
RD a[232:235],v226 offset:8192
CV v76,v104,v105
MM a[48:51],a[144:147],v[16:19],a[48:51]
EX v106,v106
MM a[48:51],a[148:151],v[20:23],a[48:51]
EX v107,v107
MM a[56:59],a[148:151],v[28:31],a[56:59]
RD a[236:239],v227 offset:8192
CV v77,v106,v107
MM a[56:59],a[144:147],v[24:27],a[56:59]
EX v108,v108
MM a[60:63],a[152:155],v[24:27],a[60:63]
buffer_load_dwordx4 v235, s[12:15], s81 offen offset:0 lds
MM a[60:63],a[156:159],v[28:31],a[60:63]
EX v109,v109
MM a[52:55],a[156:159],v[20:23],a[52:55]
RD a[240:243],v224 offset:12288
CV v78,v108,v109
MM a[52:55],a[152:155],v[16:19],a[52:55]
EX v110,v110
MM a[44:47],a[152:155],v[8:11],a[44:47]
EX v111,v111
MM a[44:47],a[156:159],v[12:15],a[44:47]
RD a[244:247],v225 offset:12288
CV v79,v110,v111
MM a[36:39],a[156:159],v[4:7],a[36:39]
buffer_load_dwordx4 v235, s[12:15], s64 offen offset:1024 lds
MM a[36:39],a[152:155],v[0:3],a[36:39]
EX v112,v112
MM a[64:67],a[160:163],v[0:3],a[64:67]
EX v113,v113
MM a[64:67],a[164:167],v[4:7],a[64:67]
RD a[248:251],v226 offset:12288
CV v84,v112,v113
MM a[72:75],a[164:167],v[12:15],a[72:75]
EX v114,v114
MM a[72:75],a[160:163],v[8:11],a[72:75]
EX v115,v115
MM a[80:83],a[160:163],v[16:19],a[80:83]
RD a[252:255],v227 offset:12288
CV v85,v114,v115
MM a[80:83],a[164:167],v[20:23],a[80:83]
buffer_load_dwordx4 v235, s[12:15], s81 offen offset:2048 lds
MM a[88:91],a[164:167],v[28:31],a[88:91]
EX v116,v116
MM a[88:91],a[160:163],v[24:27],a[88:91]
EX v117,v117
MM a[92:95],a[168:171],v[24:27],a[92:95]
EX v118,v118
MM a[92:95],a[172:175],v[28:31],a[92:95]
buffer_load_dwordx4 v235, s[12:15], s64 offen offset:3072 lds
MM a[84:87],a[172:175],v[20:23],a[84:87]
EX v119,v119
MM a[84:87],a[168:171],v[16:19],a[84:87]
EX v120,v120
MM a[76:79],a[168:171],v[8:11],a[76:79]
EX v121,v121
MM a[76:79],a[172:175],v[12:15],a[76:79]
EX v122,v122
MM a[68:71],a[172:175],v[4:7],a[68:71]
EX v123,v123
MM a[68:71],a[168:171],v[0:3],a[68:71]
EX v124,v124
MM a[96:99],a[176:179],v[0:3],a[96:99]
EX v125,v125
MM a[96:99],a[180:183],v[4:7],a[96:99]
EX v126,v126
MM a[104:107],a[180:183],v[12:15],a[104:107]
EX v127,v127
MM a[104:107],a[176:179],v[8:11],a[104:107]
CV v86,v116,v117
CV v87,v118,v119
MM a[112:115],a[176:179],v[16:19],a[112:115]
CV v92,v120,v121
CV v93,v122,v123
MM a[112:115],a[180:183],v[20:23],a[112:115]
CV v94,v124,v125
CV v95,v126,v127
MM a[120:123],a[180:183],v[28:31],a[120:123]
MM a[120:123],a[176:179],v[24:27],a[120:123]
MM a[124:127],a[184:187],v[24:27],a[124:127]
MM a[124:127],a[188:191],v[28:31],a[124:127]
MM a[116:119],a[188:191],v[20:23],a[116:119]
MM a[116:119],a[184:187],v[16:19],a[116:119]
MM a[108:111],a[184:187],v[8:11],a[108:111]
MM a[108:111],a[188:191],v[12:15],a[108:111]
MM a[100:103],a[188:191],v[4:7],a[100:103]
MM a[100:103],a[184:187],v[0:3],a[100:103]
v_smfmac_f32_16x16x64_bf16 v[236:239], v[144:147], v[64:71], v234
v_smfmac_f32_16x16x64_bf16 v[236:239], v[148:151], v[72:79], v234
v_smfmac_f32_16x16x64_bf16 v[236:239], v[152:155], v[80:87], v234
v_smfmac_f32_16x16x64_bf16 v[236:239], v[156:159], v[88:95], v234
s_waitcnt lgkmcnt(0)
MM v[0:3],a[192:195],v[160:163],v[128:131]
s_add_u32 s64, s64, 16384
LPG17_%=:
s_cbranch_execz LTR1_%=
TR a[128:129],v232 offset:49152
s_add_u32 s81, s64, 896
MM v[0:3],a[196:199],v[176:179],v[0:3]
TR a[130:131],v232 offset:53248
TR a[132:133],v232 offset:57344
MM v[0:3],a[200:203],v[192:195],v[0:3]
TR a[134:135],v232 offset:61440
TR a[136:137],v233 offset:49152
MM v[0:3],a[204:207],v[208:211],v[0:3]
TR a[138:139],v233 offset:53248
v_add_f32 v247, v236, v237
MM v[4:7],a[220:223],v[208:211],v[128:131]
TR a[140:141],v233 offset:57344
v_cmp_nge_f32 vcc, s71, v247
MM v[4:7],a[216:219],v[192:195],v[4:7]
s_cbranch_vccnz LSITE23_%=
LBACK23_%=:
TR a[142:143],v233 offset:61440
TR a[144:145],v232 offset:49664
MM v[4:7],a[212:215],v[176:179],v[4:7]
v_lshrrev_b32 v125, 8, v248
v_lshlrev_b32 v124, 10, v125
MM v[4:7],a[208:211],v[160:163],v[4:7]
v_bfe_u32 v126, v248, 6, 2
v_lshl_add_u32 v124, v126, 7, v124
MM v[12:15],a[208:211],v[164:167],v[132:135]
v_bfe_u32 v126, v248, 4, 2
v_lshl_add_u32 v124, v126, 3, v124
MM v[12:15],a[212:215],v[180:183],v[12:15]
s_lshl_b32 s91, %[swave], 5
EX v0,v0
MM v[12:15],a[216:219],v[196:199],v[12:15]
s_add_u32 s91, s91, %[slds]
EX v1,v1
MM v[12:15],a[220:223],v[212:215],v[12:15]
s_add_u32 s91, s91, 151552
v_add_u32 v124, s91, v124
v_and_b32 v126, 112, v248
MM v[8:11],a[204:207],v[212:215],v[132:135]
s_lshl_b32 s91, %[swave], 11
EX v2,v2
MM v[8:11],a[200:203],v[196:199],v[8:11]
s_add_u32 s91, s91, %[slds]
TR a[146:147],v232 offset:53760
CV v0,v0,v1
MM v[8:11],a[196:199],v[180:183],v[8:11]
s_add_u32 s91, s91, 139264
v_lshl_add_u32 v127, v125, 4, s91
v_lshl_add_u32 v127, v126, 2, v127
MM v[8:11],a[192:195],v[164:167],v[8:11]
TR a[148:149],v232 offset:57856
v_lshl_add_u32 v126, v126, 2, v127
MM v[16:19],a[192:195],v[168:171],v[136:139]
s_waitcnt vmcnt(19)
TR v[96:97],v124 offset:0
TR v[98:99],v124 offset:512
MM v[16:19],a[196:199],v[184:187],v[16:19]
RD v[108:111],v126 offset:0
TR v[100:101],v124 offset:4096
MM v[16:19],a[200:203],v[200:203],v[16:19]
)ASM"
R"ASM(
TR v[102:103],v124 offset:4608
RD v[112:115],v126 offset:64
MM v[16:19],a[204:207],v[216:219],v[16:19]
TR v[104:105],v124 offset:8192
TR v[106:107],v124 offset:8704
MM v[20:23],a[220:223],v[216:219],v[136:139]
RD v[116:119],v127 offset:1024
v_bfe_u32 v127, v248, 4, 3
EX v3,v3
MM v[20:23],a[216:219],v[200:203],v[20:23]
v_mul_u32_u24 v127, 2560, v127
v_lshl_add_u32 v127, v125, 4, v127
EX v4,v4
MM v[20:23],a[212:215],v[184:187],v[20:23]
s_lshl_b32 s90, %[swave], 6
EX v5,v5
MM v[20:23],a[208:211],v[168:171],v[20:23]
s_lshl_b32 s91, s47, 8
EX v6,v6
MM v[28:31],a[208:211],v[172:175],v[140:143]
s_add_u32 s90, s90, s91
EX v7,v7
MM v[28:31],a[212:215],v[188:191],v[28:31]
s_mul_i32 s91, s46, 17920
TR a[150:151],v232 offset:61952
CV v1,v2,v3
MM v[28:31],a[216:219],v[204:207],v[28:31]
s_add_u32 s90, s90, s91
EX v8,v8
MM v[28:31],a[220:223],v[220:223],v[28:31]
s_add_u32 s90, s90, 21659648
TR a[152:153],v233 offset:49664
CV v2,v4,v5
MM v[24:27],a[204:207],v[220:223],v[140:143]
EX v9,v9
MM v[24:27],a[200:203],v[204:207],v[24:27]
s_waitcnt lgkmcnt(2)
TR a[154:155],v233 offset:53760
TR a[156:157],v233 offset:57856
MM v[24:27],a[196:199],v[188:191],v[24:27]
EX v10,v10
MM v[120:123],v[96:99],v[108:111],0
MM v[120:123],v[100:103],v[112:115],v[120:123]
MM v[120:123],v[104:107],v[116:119],v[120:123]
MM v[24:27],a[192:195],v[172:175],v[24:27]
EX v11,v11
MM v[32:35],a[224:227],v[160:163],v[128:131]
TR a[158:159],v233 offset:61952
CV v3,v6,v7
MM v[32:35],a[228:231],v[176:179],v[32:35]
EX v12,v12
MM v[32:35],a[232:235],v[192:195],v[32:35]
TR a[160:161],v232 offset:50176
CV v8,v8,v9
MM v[32:35],a[236:239],v[208:211],v[32:35]
EX v13,v13
MM v[36:39],a[252:255],v[208:211],v[128:131]
TR a[162:163],v232 offset:54272
TR a[164:165],v232 offset:58368
MM v[36:39],a[248:251],v[192:195],v[36:39]
s_mov_b32 exec_lo, 0x00ff00ff
s_mov_b32 exec_hi, 0x00ff00ff
buffer_store_dwordx4 v[120:123], v127, s[20:23], s90 offen offset:0 sc1
s_mov_b64 exec, -1
s_nop 0
MM v[36:39],a[244:247],v[176:179],v[36:39]
EX v14,v14
MM v[36:39],a[240:243],v[160:163],v[36:39]
EX v15,v15
MM v[44:47],a[240:243],v[164:167],v[132:135]
TR a[166:167],v232 offset:62464
CV v9,v10,v11
MM v[44:47],a[244:247],v[180:183],v[44:47]
EX v16,v16
MM v[44:47],a[248:251],v[196:199],v[44:47]
TR a[168:169],v233 offset:50176
TR a[170:171],v233 offset:54272
MM v[44:47],a[252:255],v[212:215],v[44:47]
EX v17,v17
MM v[40:43],a[236:239],v[212:215],v[132:135]
TR a[172:173],v233 offset:58368
CV v10,v12,v13
MM v[40:43],a[232:235],v[196:199],v[40:43]
EX v18,v18
MM v[40:43],a[228:231],v[180:183],v[40:43]
EX v19,v19
MM v[40:43],a[224:227],v[164:167],v[40:43]
TR a[174:175],v233 offset:62464
CV v11,v14,v15
MM v[48:51],a[224:227],v[168:171],v[136:139]
EX v20,v20
MM v[48:51],a[228:231],v[184:187],v[48:51]
TR a[176:177],v232 offset:50688
TR a[178:179],v232 offset:54784
MM v[48:51],a[232:235],v[200:203],v[48:51]
EX v21,v21
MM v[48:51],a[236:239],v[216:219],v[48:51]
TR a[180:181],v232 offset:58880
CV v16,v16,v17
MM v[52:55],a[252:255],v[216:219],v[136:139]
EX v22,v22
MM v[52:55],a[248:251],v[200:203],v[52:55]
TR a[182:183],v232 offset:62976
CV v17,v18,v19
MM v[52:55],a[244:247],v[184:187],v[52:55]
EX v23,v23
MM v[52:55],a[240:243],v[168:171],v[52:55]
TR a[184:185],v233 offset:50688
CV v18,v20,v21
MM v[60:63],a[240:243],v[172:175],v[140:143]
EX v24,v24
MM v[60:63],a[244:247],v[188:191],v[60:63]
TR a[186:187],v233 offset:54784
TR a[188:189],v233 offset:58880
MM v[60:63],a[248:251],v[204:207],v[60:63]
TR a[190:191],v233 offset:62976
CV v19,v22,v23
MM v[60:63],a[252:255],v[220:223],v[60:63]
EX v25,v25
MM v[56:59],a[236:239],v[220:223],v[140:143]
EX v26,v26
MM v[56:59],a[232:235],v[204:207],v[56:59]
EX v27,v27
MM v[56:59],a[228:231],v[188:191],v[56:59]
EX v28,v28
MM v[56:59],a[224:227],v[172:175],v[56:59]
s_waitcnt vmcnt(16) lgkmcnt(0)
s_barrier
MM a[0:3],a[128:131],v[64:67],a[0:3]
s_add_u32 m0, s65, 49152
CV v24,v24,v25
CV v25,v26,v27
MM a[0:3],a[132:135],v[68:71],a[0:3]
EX v29,v29
MM a[8:11],a[132:135],v[76:79],a[8:11]
EX v30,v30
MM a[8:11],a[128:131],v[72:75],a[8:11]
EX v31,v31
MM a[16:19],a[128:131],v[80:83],a[16:19]
RD a[192:195],v224 offset:16384
CV v26,v28,v29
MM a[16:19],a[132:135],v[84:87],a[16:19]
EX v32,v32
MM a[24:27],a[132:135],v[92:95],a[24:27]
RD a[196:199],v225 offset:16384
CV v27,v30,v31
MM a[24:27],a[128:131],v[88:91],a[24:27]
EX v33,v33
MM a[28:31],a[136:139],v[88:91],a[28:31]
buffer_load_dwordx4 v228, s[8:11], s64 offen offset:0 lds
MM a[28:31],a[140:143],v[92:95],a[28:31]
EX v34,v34
MM a[20:23],a[140:143],v[84:87],a[20:23]
EX v35,v35
MM a[20:23],a[136:139],v[80:83],a[20:23]
RD a[200:203],v226 offset:16384
CV v4,v32,v33
MM a[12:15],a[136:139],v[72:75],a[12:15]
EX v36,v36
MM a[12:15],a[140:143],v[76:79],a[12:15]
RD a[204:207],v227 offset:16384
CV v5,v34,v35
MM a[4:7],a[140:143],v[68:71],a[4:7]
EX v37,v37
MM a[4:7],a[136:139],v[64:67],a[4:7]
buffer_load_dwordx4 v229, s[8:11], s64 offen offset:1024 lds
MM a[32:35],a[144:147],v[64:67],a[32:35]
EX v38,v38
MM a[32:35],a[148:151],v[68:71],a[32:35]
RD a[208:211],v224 offset:20480
CV v6,v36,v37
MM a[40:43],a[148:151],v[76:79],a[40:43]
EX v39,v39
MM a[40:43],a[144:147],v[72:75],a[40:43]
RD a[212:215],v225 offset:20480
CV v7,v38,v39
MM a[48:51],a[144:147],v[80:83],a[48:51]
EX v40,v40
MM a[48:51],a[148:151],v[84:87],a[48:51]
EX v41,v41
MM a[56:59],a[148:151],v[92:95],a[56:59]
buffer_load_dwordx4 v230, s[8:11], s64 offen offset:2048 lds
MM a[56:59],a[144:147],v[88:91],a[56:59]
EX v42,v42
MM a[60:63],a[152:155],v[88:91],a[60:63]
RD a[216:219],v226 offset:20480
CV v12,v40,v41
MM a[60:63],a[156:159],v[92:95],a[60:63]
EX v43,v43
MM a[52:55],a[156:159],v[84:87],a[52:55]
RD a[220:223],v227 offset:20480
CV v13,v42,v43
MM a[52:55],a[152:155],v[80:83],a[52:55]
EX v44,v44
MM a[44:47],a[152:155],v[72:75],a[44:47]
EX v45,v45
MM a[44:47],a[156:159],v[76:79],a[44:47]
buffer_load_dwordx4 v231, s[8:11], s64 offen offset:3072 lds
MM a[36:39],a[156:159],v[68:71],a[36:39]
s_add_u32 m0, s65, 114688
RD a[224:227],v224 offset:24576
CV v14,v44,v45
MM a[36:39],a[152:155],v[64:67],a[36:39]
EX v46,v46
MM a[64:67],a[160:163],v[64:67],a[64:67]
EX v47,v47
MM a[64:67],a[164:167],v[68:71],a[64:67]
RD a[228:231],v225 offset:24576
CV v15,v46,v47
MM a[72:75],a[164:167],v[76:79],a[72:75]
EX v48,v48
MM a[72:75],a[160:163],v[72:75],a[72:75]
buffer_load_dwordx4 v235, s[12:15], s81 offen offset:0 lds
MM a[80:83],a[160:163],v[80:83],a[80:83]
EX v49,v49
MM a[80:83],a[164:167],v[84:87],a[80:83]
RD a[232:235],v226 offset:24576
CV v20,v48,v49
MM a[88:91],a[164:167],v[92:95],a[88:91]
EX v50,v50
MM a[88:91],a[160:163],v[88:91],a[88:91]
EX v51,v51
MM a[92:95],a[168:171],v[88:91],a[92:95]
RD a[236:239],v227 offset:24576
CV v21,v50,v51
MM a[92:95],a[172:175],v[92:95],a[92:95]
EX v52,v52
MM a[84:87],a[172:175],v[84:87],a[84:87]
buffer_load_dwordx4 v235, s[12:15], s64 offen offset:1024 lds
MM a[84:87],a[168:171],v[80:83],a[84:87]
EX v53,v53
MM a[76:79],a[168:171],v[72:75],a[76:79]
RD a[240:243],v224 offset:28672
CV v22,v52,v53
MM a[76:79],a[172:175],v[76:79],a[76:79]
EX v54,v54
MM a[68:71],a[172:175],v[68:71],a[68:71]
EX v55,v55
MM a[68:71],a[168:171],v[64:67],a[68:71]
RD a[244:247],v225 offset:28672
CV v23,v54,v55
MM a[96:99],a[176:179],v[64:67],a[96:99]
EX v56,v56
MM a[96:99],a[180:183],v[68:71],a[96:99]
buffer_load_dwordx4 v235, s[12:15], s81 offen offset:2048 lds
MM a[104:107],a[180:183],v[76:79],a[104:107]
EX v57,v57
MM a[104:107],a[176:179],v[72:75],a[104:107]
RD a[248:251],v226 offset:28672
CV v28,v56,v57
MM a[112:115],a[176:179],v[80:83],a[112:115]
EX v58,v58
MM a[112:115],a[180:183],v[84:87],a[112:115]
EX v59,v59
MM a[120:123],a[180:183],v[92:95],a[120:123]
RD a[252:255],v227 offset:28672
CV v29,v58,v59
MM a[120:123],a[176:179],v[88:91],a[120:123]
EX v60,v60
MM a[124:127],a[184:187],v[88:91],a[124:127]
buffer_load_dwordx4 v235, s[12:15], s64 offen offset:3072 lds
MM a[124:127],a[188:191],v[92:95],a[124:127]
EX v61,v61
MM a[116:119],a[188:191],v[84:87],a[116:119]
EX v62,v62
MM a[116:119],a[184:187],v[80:83],a[116:119]
EX v63,v63
MM a[108:111],a[184:187],v[72:75],a[108:111]
CV v30,v60,v61
CV v31,v62,v63
MM a[108:111],a[188:191],v[76:79],a[108:111]
MM a[100:103],a[188:191],v[68:71],a[100:103]
MM a[100:103],a[184:187],v[64:67],a[100:103]
v_smfmac_f32_16x16x64_bf16 v[236:239], v[144:147], v[0:7], v234
v_smfmac_f32_16x16x64_bf16 v[236:239], v[148:151], v[8:15], v234
v_smfmac_f32_16x16x64_bf16 v[236:239], v[152:155], v[16:23], v234
v_smfmac_f32_16x16x64_bf16 v[236:239], v[156:159], v[24:31], v234
s_waitcnt lgkmcnt(0)
MM v[64:67],a[192:195],v[160:163],v[128:131]
s_sub_u32 s64, s64, 507904
TR a[128:129],v232 offset:0
s_add_u32 s81, s64, 896
MM v[64:67],a[196:199],v[176:179],v[64:67]
TR a[130:131],v232 offset:4096
TR a[132:133],v232 offset:8192
MM v[64:67],a[200:203],v[192:195],v[64:67]
TR a[134:135],v232 offset:12288
TR a[136:137],v233 offset:0
MM v[64:67],a[204:207],v[208:211],v[64:67]
TR a[138:139],v233 offset:4096
v_add_f32 v246, v236, v237
MM v[68:71],a[220:223],v[208:211],v[128:131]
TR a[140:141],v233 offset:8192
v_cmp_nge_f32 vcc, s71, v246
MM v[68:71],a[216:219],v[192:195],v[68:71]
s_cbranch_vccnz LSITE24_%=
LBACK24_%=:
TR a[142:143],v233 offset:12288
TR a[144:145],v232 offset:512
MM v[68:71],a[212:215],v[176:179],v[68:71]
TR a[146:147],v232 offset:4608
TR a[148:149],v232 offset:8704
MM v[68:71],a[208:211],v[160:163],v[68:71]
TR a[150:151],v232 offset:12800
TR a[152:153],v233 offset:512
MM v[76:79],a[208:211],v[164:167],v[132:135]
EX v64,v64
MM v[76:79],a[212:215],v[180:183],v[76:79]
EX v65,v65
MM v[76:79],a[216:219],v[196:199],v[76:79]
TR a[154:155],v233 offset:4608
CV v64,v64,v65
MM v[76:79],a[220:223],v[212:215],v[76:79]
EX v66,v66
MM v[72:75],a[204:207],v[212:215],v[132:135]
EX v67,v67
MM v[72:75],a[200:203],v[196:199],v[72:75]
TR a[156:157],v233 offset:8704
CV v65,v66,v67
MM v[72:75],a[196:199],v[180:183],v[72:75]
EX v68,v68
MM v[72:75],a[192:195],v[164:167],v[72:75]
EX v69,v69
MM v[80:83],a[192:195],v[168:171],v[136:139]
TR a[158:159],v233 offset:12800
CV v66,v68,v69
MM v[80:83],a[196:199],v[184:187],v[80:83]
EX v70,v70
MM v[80:83],a[200:203],v[200:203],v[80:83]
EX v71,v71
MM v[80:83],a[204:207],v[216:219],v[80:83]
TR a[160:161],v232 offset:1024
CV v67,v70,v71
MM v[84:87],a[220:223],v[216:219],v[136:139]
EX v72,v72
MM v[84:87],a[216:219],v[200:203],v[84:87]
EX v73,v73
MM v[84:87],a[212:215],v[184:187],v[84:87]
TR a[162:163],v232 offset:5120
CV v72,v72,v73
MM v[84:87],a[208:211],v[168:171],v[84:87]
EX v74,v74
MM v[92:95],a[208:211],v[172:175],v[140:143]
TR a[164:165],v232 offset:9216
TR a[166:167],v232 offset:13312
MM v[92:95],a[212:215],v[188:191],v[92:95]
EX v75,v75
MM v[92:95],a[216:219],v[204:207],v[92:95]
TR a[168:169],v233 offset:1024
CV v73,v74,v75
MM v[92:95],a[220:223],v[220:223],v[92:95]
EX v76,v76
MM v[88:91],a[204:207],v[220:223],v[140:143]
EX v77,v77
MM v[88:91],a[200:203],v[204:207],v[88:91]
TR a[170:171],v233 offset:5120
CV v74,v76,v77
MM v[88:91],a[196:199],v[188:191],v[88:91]
EX v78,v78
MM v[88:91],a[192:195],v[172:175],v[88:91]
TR a[172:173],v233 offset:9216
TR a[174:175],v233 offset:13312
MM v[96:99],a[224:227],v[160:163],v[128:131]
EX v79,v79
MM v[96:99],a[228:231],v[176:179],v[96:99]
TR a[176:177],v232 offset:1536
CV v75,v78,v79
MM v[96:99],a[232:235],v[192:195],v[96:99]
EX v80,v80
MM v[96:99],a[236:239],v[208:211],v[96:99]
TR a[178:179],v232 offset:5632
TR a[180:181],v232 offset:9728
MM v[100:103],a[252:255],v[208:211],v[128:131]
EX v81,v81
MM v[100:103],a[248:251],v[192:195],v[100:103]
TR a[182:183],v232 offset:13824
CV v80,v80,v81
MM v[100:103],a[244:247],v[176:179],v[100:103]
EX v82,v82
MM v[100:103],a[240:243],v[160:163],v[100:103]
EX v83,v83
MM v[108:111],a[240:243],v[164:167],v[132:135]
TR a[184:185],v233 offset:1536
CV v81,v82,v83
MM v[108:111],a[244:247],v[180:183],v[108:111]
EX v84,v84
MM v[108:111],a[248:251],v[196:199],v[108:111]
TR a[186:187],v233 offset:5632
TR a[188:189],v233 offset:9728
MM v[108:111],a[252:255],v[212:215],v[108:111]
EX v85,v85
MM v[104:107],a[236:239],v[212:215],v[132:135]
TR a[190:191],v233 offset:13824
CV v82,v84,v85
)ASM"
R"ASM(
MM v[104:107],a[232:235],v[196:199],v[104:107]
EX v86,v86
MM v[104:107],a[228:231],v[180:183],v[104:107]
EX v87,v87
MM v[104:107],a[224:227],v[164:167],v[104:107]
CV v83,v86,v87
MM v[112:115],a[224:227],v[168:171],v[136:139]
EX v88,v88
MM v[112:115],a[228:231],v[184:187],v[112:115]
EX v89,v89
MM v[112:115],a[232:235],v[200:203],v[112:115]
EX v90,v90
MM v[112:115],a[236:239],v[216:219],v[112:115]
EX v91,v91
MM v[116:119],a[252:255],v[216:219],v[136:139]
EX v92,v92
MM v[116:119],a[248:251],v[200:203],v[116:119]
EX v93,v93
MM v[116:119],a[244:247],v[184:187],v[116:119]
EX v94,v94
MM v[116:119],a[240:243],v[168:171],v[116:119]
EX v95,v95
MM v[124:127],a[240:243],v[172:175],v[140:143]
EX v96,v96
MM v[124:127],a[244:247],v[188:191],v[124:127]
EX v97,v97
MM v[124:127],a[248:251],v[204:207],v[124:127]
EX v98,v98
MM v[124:127],a[252:255],v[220:223],v[124:127]
EX v99,v99
MM v[120:123],a[236:239],v[220:223],v[140:143]
EX v100,v100
MM v[120:123],a[232:235],v[204:207],v[120:123]
EX v101,v101
MM v[120:123],a[228:231],v[188:191],v[120:123]
EX v102,v102
MM v[120:123],a[224:227],v[172:175],v[120:123]
s_waitcnt vmcnt(13) lgkmcnt(0)
s_barrier
MM a[0:3],a[128:131],v[0:3],a[0:3]
s_add_u32 m0, s65, 0
s_add_u32 m0, s94, 139264
s_add_u32 s66, s86, 0
buffer_load_dwordx4 v241, s[24:27], s66 offen offset:0 lds
MM a[0:3],a[132:135],v[4:7],a[0:3]
CV v88,v88,v89
CV v89,v90,v91
MM a[8:11],a[132:135],v[12:15],a[8:11]
RD a[192:195],v224 offset:32768
CV v90,v92,v93
MM a[8:11],a[128:131],v[8:11],a[8:11]
RD a[196:199],v225 offset:32768
CV v91,v94,v95
MM a[16:19],a[128:131],v[16:19],a[16:19]
s_add_u32 m0, s65, 0
s_nop 0
buffer_load_dwordx4 v228, s[8:11], s64 offen offset:0 lds
MM a[16:19],a[132:135],v[20:23],a[16:19]
RD a[200:203],v226 offset:32768
CV v68,v96,v97
MM a[24:27],a[132:135],v[28:31],a[24:27]
RD a[204:207],v227 offset:32768
CV v69,v98,v99
MM a[24:27],a[128:131],v[24:27],a[24:27]
buffer_load_dwordx4 v229, s[8:11], s64 offen offset:1024 lds
MM a[28:31],a[136:139],v[24:27],a[28:31]
s_add_u32 m0, s94, 143360
s_add_u32 s66, s86, 81920
buffer_load_dwordx4 v241, s[24:27], s66 offen offset:0 lds
MM a[28:31],a[140:143],v[28:31],a[28:31]
RD a[208:211],v224 offset:36864
CV v70,v100,v101
MM a[20:23],a[140:143],v[20:23],a[20:23]
RD a[212:215],v225 offset:36864
RD a[216:219],v226 offset:36864
MM a[20:23],a[136:139],v[16:19],a[20:23]
s_add_u32 m0, s65, 0
s_nop 0
buffer_load_dwordx4 v230, s[8:11], s64 offen offset:2048 lds
MM a[12:15],a[136:139],v[8:11],a[12:15]
RD a[220:223],v227 offset:36864
RD a[224:227],v224 offset:40960
MM a[12:15],a[140:143],v[12:15],a[12:15]
EX v103,v103
MM a[4:7],a[140:143],v[4:7],a[4:7]
RD a[228:231],v225 offset:40960
CV v71,v102,v103
MM a[4:7],a[136:139],v[0:3],a[4:7]
buffer_load_dwordx4 v231, s[8:11], s64 offen offset:3072 lds
MM a[32:35],a[144:147],v[0:3],a[32:35]
s_add_u32 m0, s65, 65536
EX v104,v104
MM a[32:35],a[148:151],v[4:7],a[32:35]
EX v105,v105
MM a[40:43],a[148:151],v[12:15],a[40:43]
RD a[232:235],v226 offset:40960
CV v76,v104,v105
MM a[40:43],a[144:147],v[8:11],a[40:43]
EX v106,v106
MM a[48:51],a[144:147],v[16:19],a[48:51]
buffer_load_dwordx4 v235, s[12:15], s81 offen offset:0 lds
MM a[48:51],a[148:151],v[20:23],a[48:51]
EX v107,v107
MM a[56:59],a[148:151],v[28:31],a[56:59]
RD a[236:239],v227 offset:40960
CV v77,v106,v107
MM a[56:59],a[144:147],v[24:27],a[56:59]
EX v108,v108
MM a[60:63],a[152:155],v[24:27],a[60:63]
EX v109,v109
MM a[60:63],a[156:159],v[28:31],a[60:63]
RD a[240:243],v224 offset:45056
CV v78,v108,v109
MM a[52:55],a[156:159],v[20:23],a[52:55]
EX v110,v110
MM a[52:55],a[152:155],v[16:19],a[52:55]
buffer_load_dwordx4 v235, s[12:15], s64 offen offset:1024 lds
MM a[44:47],a[152:155],v[8:11],a[44:47]
EX v111,v111
MM a[44:47],a[156:159],v[12:15],a[44:47]
RD a[244:247],v225 offset:45056
CV v79,v110,v111
MM a[36:39],a[156:159],v[4:7],a[36:39]
EX v112,v112
MM a[36:39],a[152:155],v[0:3],a[36:39]
EX v113,v113
MM a[64:67],a[160:163],v[0:3],a[64:67]
RD a[248:251],v226 offset:45056
CV v84,v112,v113
MM a[64:67],a[164:167],v[4:7],a[64:67]
EX v114,v114
MM a[72:75],a[164:167],v[12:15],a[72:75]
buffer_load_dwordx4 v235, s[12:15], s81 offen offset:2048 lds
LPG18_%=:
s_cbranch_execz LPG22_%=
MM a[72:75],a[160:163],v[8:11],a[72:75]
EX v115,v115
MM a[80:83],a[160:163],v[16:19],a[80:83]
RD a[252:255],v227 offset:45056
CV v85,v114,v115
MM a[80:83],a[164:167],v[20:23],a[80:83]
EX v116,v116
MM a[88:91],a[164:167],v[28:31],a[88:91]
EX v117,v117
MM a[88:91],a[160:163],v[24:27],a[88:91]
buffer_load_dwordx4 v235, s[12:15], s64 offen offset:3072 lds
MM a[92:95],a[168:171],v[24:27],a[92:95]
EX v118,v118
MM a[92:95],a[172:175],v[28:31],a[92:95]
EX v119,v119
MM a[84:87],a[172:175],v[20:23],a[84:87]
EX v120,v120
MM a[84:87],a[168:171],v[16:19],a[84:87]
EX v121,v121
MM a[76:79],a[168:171],v[8:11],a[76:79]
EX v122,v122
MM a[76:79],a[172:175],v[12:15],a[76:79]
EX v123,v123
MM a[68:71],a[172:175],v[4:7],a[68:71]
EX v124,v124
MM a[68:71],a[168:171],v[0:3],a[68:71]
EX v125,v125
MM a[96:99],a[176:179],v[0:3],a[96:99]
EX v126,v126
MM a[96:99],a[180:183],v[4:7],a[96:99]
EX v127,v127
MM a[104:107],a[180:183],v[12:15],a[104:107]
CV v86,v116,v117
CV v87,v118,v119
MM a[104:107],a[176:179],v[8:11],a[104:107]
CV v92,v120,v121
CV v93,v122,v123
MM a[112:115],a[176:179],v[16:19],a[112:115]
CV v94,v124,v125
CV v95,v126,v127
MM a[112:115],a[180:183],v[20:23],a[112:115]
MM a[120:123],a[180:183],v[28:31],a[120:123]
MM a[120:123],a[176:179],v[24:27],a[120:123]
MM a[124:127],a[184:187],v[24:27],a[124:127]
MM a[124:127],a[188:191],v[28:31],a[124:127]
MM a[116:119],a[188:191],v[20:23],a[116:119]
MM a[116:119],a[184:187],v[16:19],a[116:119]
MM a[108:111],a[184:187],v[8:11],a[108:111]
MM a[108:111],a[188:191],v[12:15],a[108:111]
MM a[100:103],a[188:191],v[4:7],a[100:103]
MM a[100:103],a[184:187],v[0:3],a[100:103]
v_smfmac_f32_16x16x64_bf16 v[236:239], v[144:147], v[64:71], v234
v_smfmac_f32_16x16x64_bf16 v[236:239], v[148:151], v[72:79], v234
v_smfmac_f32_16x16x64_bf16 v[236:239], v[152:155], v[80:87], v234
v_smfmac_f32_16x16x64_bf16 v[236:239], v[156:159], v[88:95], v234
s_waitcnt lgkmcnt(0)
MM v[0:3],a[192:195],v[160:163],v[128:131]
s_add_u32 s64, s64, 16384
TR a[128:129],v232 offset:16384
s_add_u32 s81, s64, 896
MM v[0:3],a[196:199],v[176:179],v[0:3]
TR a[130:131],v232 offset:20480
TR a[132:133],v232 offset:24576
MM v[0:3],a[200:203],v[192:195],v[0:3]
TR a[134:135],v232 offset:28672
TR a[136:137],v233 offset:16384
MM v[0:3],a[204:207],v[208:211],v[0:3]
TR a[138:139],v233 offset:20480
v_add_f32 v247, v236, v237
MM v[4:7],a[220:223],v[208:211],v[128:131]
TR a[140:141],v233 offset:24576
v_cmp_nge_f32 vcc, s71, v247
MM v[4:7],a[216:219],v[192:195],v[4:7]
s_cbranch_vccnz LSITE25_%=
LBACK25_%=:
TR a[142:143],v233 offset:28672
TR a[144:145],v232 offset:16896
MM v[4:7],a[212:215],v[176:179],v[4:7]
s_add_u32 m0, s94, 151552
s_add_u32 s66, s87, 81920
buffer_load_dwordx4 v241, s[24:27], s66 offen offset:0 lds
MM v[4:7],a[208:211],v[160:163],v[4:7]
v_bfe_u32 v96, v248, 6, 2
v_mul_u32_u24 v96, 1280, v96
MM v[12:15],a[208:211],v[164:167],v[132:135]
v_bfe_u32 v97, v248, 8, 1
v_mul_u32_u24 v97, 40960, v97
MM v[12:15],a[212:215],v[180:183],v[12:15]
v_add_u32 v96, v96, v97
v_and_b32 v97, 48, v248
MM v[12:15],a[216:219],v[196:199],v[12:15]
v_mul_u32_u24 v97, 6, v97
v_add_u32 v96, v96, v97
MM v[12:15],a[220:223],v[212:215],v[12:15]
s_min_u32 s60, s18, s63
EX v0,v0
MM v[8:11],a[204:207],v[212:215],v[132:135]
s_add_u32 s61, s60, 131072
s_mov_b32 exec_hi, 0
buffer_load_dword v100, v96, s[32:35], s60 offen offset:0
s_mov_b64 exec, -1
MM v[8:11],a[200:203],v[196:199],v[8:11]
EX v1,v1
MM v[8:11],a[196:199],v[180:183],v[8:11]
s_add_u32 m0, s94, 155648
s_add_u32 s66, s88, 0
buffer_load_dwordx4 v241, s[24:27], s66 offen offset:0 lds
MM v[8:11],a[192:195],v[164:167],v[8:11]
s_mov_b32 exec_hi, 0
buffer_load_dword v101, v96, s[20:23], s61 offen offset:0 sc1
s_mov_b64 exec, -1
MM v[16:19],a[192:195],v[168:171],v[136:139]
EX v2,v2
MM v[16:19],a[196:199],v[184:187],v[16:19]
EX v3,v3
MM v[16:19],a[200:203],v[200:203],v[16:19]
EX v4,v4
MM v[16:19],a[204:207],v[216:219],v[16:19]
TR a[146:147],v232 offset:20992
CV v0,v0,v1
MM v[20:23],a[220:223],v[216:219],v[136:139]
EX v5,v5
MM v[20:23],a[216:219],v[200:203],v[20:23]
TR a[148:149],v232 offset:25088
CV v1,v2,v3
MM v[20:23],a[212:215],v[184:187],v[20:23]
s_add_u32 m0, s94, 159744
s_add_u32 s66, s88, 81920
buffer_load_dwordx4 v241, s[24:27], s66 offen offset:0 lds
MM v[20:23],a[208:211],v[168:171],v[20:23]
EX v6,v6
MM v[28:31],a[208:211],v[172:175],v[140:143]
EX v7,v7
MM v[28:31],a[212:215],v[188:191],v[28:31]
TR a[150:151],v232 offset:29184
TR a[152:153],v233 offset:16896
MM v[28:31],a[216:219],v[204:207],v[28:31]
EX v8,v8
MM v[28:31],a[220:223],v[220:223],v[28:31]
EX v9,v9
MM v[24:27],a[204:207],v[220:223],v[140:143]
TR a[154:155],v233 offset:20992
CV v2,v4,v5
MM v[24:27],a[200:203],v[204:207],v[24:27]
EX v10,v10
MM v[24:27],a[196:199],v[188:191],v[24:27]
TR a[156:157],v233 offset:25088
CV v3,v6,v7
MM v[24:27],a[192:195],v[172:175],v[24:27]
EX v11,v11
MM v[32:35],a[224:227],v[160:163],v[128:131]
TR a[158:159],v233 offset:29184
TR a[160:161],v232 offset:17408
MM v[32:35],a[228:231],v[176:179],v[32:35]
EX v12,v12
MM v[32:35],a[232:235],v[192:195],v[32:35]
TR a[162:163],v232 offset:21504
CV v8,v8,v9
MM v[32:35],a[236:239],v[208:211],v[32:35]
EX v13,v13
MM v[36:39],a[252:255],v[208:211],v[128:131]
TR a[164:165],v232 offset:25600
CV v9,v10,v11
MM v[36:39],a[248:251],v[192:195],v[36:39]
EX v14,v14
MM v[36:39],a[244:247],v[176:179],v[36:39]
EX v15,v15
MM v[36:39],a[240:243],v[160:163],v[36:39]
TR a[166:167],v232 offset:29696
TR a[168:169],v233 offset:17408
MM v[44:47],a[240:243],v[164:167],v[132:135]
EX v16,v16
MM v[44:47],a[244:247],v[180:183],v[44:47]
TR a[170:171],v233 offset:21504
CV v10,v12,v13
MM v[44:47],a[248:251],v[196:199],v[44:47]
EX v17,v17
MM v[44:47],a[252:255],v[212:215],v[44:47]
TR a[172:173],v233 offset:25600
CV v11,v14,v15
MM v[40:43],a[236:239],v[212:215],v[132:135]
EX v18,v18
MM v[40:43],a[232:235],v[196:199],v[40:43]
TR a[174:175],v233 offset:29696
TR a[176:177],v232 offset:17920
MM v[40:43],a[228:231],v[180:183],v[40:43]
EX v19,v19
MM v[40:43],a[224:227],v[164:167],v[40:43]
EX v20,v20
MM v[48:51],a[224:227],v[168:171],v[136:139]
TR a[178:179],v232 offset:22016
CV v16,v16,v17
MM v[48:51],a[228:231],v[184:187],v[48:51]
EX v21,v21
MM v[48:51],a[232:235],v[200:203],v[48:51]
TR a[180:181],v232 offset:26112
CV v17,v18,v19
MM v[48:51],a[236:239],v[216:219],v[48:51]
EX v22,v22
MM v[52:55],a[252:255],v[216:219],v[136:139]
TR a[182:183],v232 offset:30208
CV v18,v20,v21
MM v[52:55],a[248:251],v[200:203],v[52:55]
EX v23,v23
MM v[52:55],a[244:247],v[184:187],v[52:55]
TR a[184:185],v233 offset:17920
TR a[186:187],v233 offset:22016
MM v[52:55],a[240:243],v[168:171],v[52:55]
EX v24,v24
MM v[60:63],a[240:243],v[172:175],v[140:143]
EX v25,v25
MM v[60:63],a[244:247],v[188:191],v[60:63]
TR a[188:189],v233 offset:26112
CV v19,v22,v23
MM v[60:63],a[248:251],v[204:207],v[60:63]
TR a[190:191],v233 offset:30208
CV v24,v24,v25
MM v[60:63],a[252:255],v[220:223],v[60:63]
EX v26,v26
MM v[56:59],a[236:239],v[220:223],v[140:143]
EX v27,v27
MM v[56:59],a[232:235],v[204:207],v[56:59]
EX v28,v28
MM v[56:59],a[228:231],v[188:191],v[56:59]
EX v29,v29
MM v[56:59],a[224:227],v[172:175],v[56:59]
s_waitcnt vmcnt(19) lgkmcnt(0)
s_barrier
MM a[0:3],a[128:131],v[64:67],a[0:3]
CV v25,v26,v27
CV v26,v28,v29
MM a[0:3],a[132:135],v[68:71],a[0:3]
s_lshl_b32 s91, s47, 2
EX v30,v30
MM a[8:11],a[132:135],v[76:79],a[8:11]
s_add_u32 s91, s91, %[swave]
EX v31,v31
MM a[8:11],a[128:131],v[72:75],a[8:11]
s_lshl_b32 s91, s91, 2
RD a[192:195],v224 offset:49152
CV v27,v30,v31
MM a[16:19],a[128:131],v[80:83],a[16:19]
s_add_u32 s91, s91, 73728
s_add_u32 m0, s65, 16384
RD a[196:199],v225 offset:49152
MM a[16:19],a[132:135],v[84:87],a[16:19]
v_mov_b32 v250, s69
s_mov_b64 exec, 1
buffer_store_dword v250, off, s[20:23], s91 offset:0 sc1
s_mov_b64 exec, -1
MM a[24:27],a[132:135],v[92:95],a[24:27]
s_lshr_b32 s77, %[sb], 3
EX v32,v32
MM a[24:27],a[128:131],v[88:91],a[24:27]
)ASM"
R"ASM(
s_lshl_b32 s77, s77, 2
EX v33,v33
MM a[28:31],a[136:139],v[88:91],a[28:31]
s_add_u32 s77, s77, %[swave]
buffer_load_dwordx4 v228, s[8:11], s64 offen offset:0 lds
MM a[28:31],a[140:143],v[92:95],a[28:31]
s_lshl_b32 s77, s77, 9
EX v34,v34
MM a[20:23],a[140:143],v[84:87],a[20:23]
s_add_u32 s77, s77, 0
v_lshrrev_b32 v253, 1, v248
v_add_u32 v253, s77, v253
MM a[20:23],a[136:139],v[80:83],a[20:23]
s_mov_b32 s77, LKEND_ka-LPD2_%=-16
v_min_u32 v253, s77, v253
CV v4,v32,v33
MM a[12:15],a[136:139],v[72:75],a[12:15]
s_getpc_b64 s[98:99]
LPD2_%=:
global_load_dword v253, v253, s[98:99]
MM a[12:15],a[140:143],v[76:79],a[12:15]
EX v35,v35
MM a[4:7],a[140:143],v[68:71],a[4:7]
EX v36,v36
MM a[4:7],a[136:139],v[64:67],a[4:7]
EX v37,v37
MM a[32:35],a[144:147],v[64:67],a[32:35]
RD a[200:203],v226 offset:49152
CV v5,v34,v35
MM a[32:35],a[148:151],v[68:71],a[32:35]
EX v38,v38
MM a[40:43],a[148:151],v[76:79],a[40:43]
buffer_load_dwordx4 v229, s[8:11], s64 offen offset:1024 lds
MM a[40:43],a[144:147],v[72:75],a[40:43]
EX v39,v39
MM a[48:51],a[144:147],v[80:83],a[48:51]
RD a[204:207],v227 offset:49152
CV v6,v36,v37
MM a[48:51],a[148:151],v[84:87],a[48:51]
EX v40,v40
MM a[56:59],a[148:151],v[92:95],a[56:59]
EX v41,v41
MM a[56:59],a[144:147],v[88:91],a[56:59]
RD a[208:211],v224 offset:53248
CV v7,v38,v39
MM a[60:63],a[152:155],v[88:91],a[60:63]
EX v42,v42
MM a[60:63],a[156:159],v[92:95],a[60:63]
buffer_load_dwordx4 v230, s[8:11], s64 offen offset:2048 lds
MM a[52:55],a[156:159],v[84:87],a[52:55]
EX v43,v43
MM a[52:55],a[152:155],v[80:83],a[52:55]
RD a[212:215],v225 offset:53248
CV v12,v40,v41
MM a[44:47],a[152:155],v[72:75],a[44:47]
EX v44,v44
MM a[44:47],a[156:159],v[76:79],a[44:47]
EX v45,v45
MM a[36:39],a[156:159],v[68:71],a[36:39]
RD a[216:219],v226 offset:53248
CV v13,v42,v43
MM a[36:39],a[152:155],v[64:67],a[36:39]
EX v46,v46
MM a[64:67],a[160:163],v[64:67],a[64:67]
buffer_load_dwordx4 v231, s[8:11], s64 offen offset:3072 lds
MM a[64:67],a[164:167],v[68:71],a[64:67]
s_add_u32 m0, s65, 81920
EX v47,v47
MM a[72:75],a[164:167],v[76:79],a[72:75]
RD a[220:223],v227 offset:53248
CV v14,v44,v45
MM a[72:75],a[160:163],v[72:75],a[72:75]
EX v48,v48
MM a[80:83],a[160:163],v[80:83],a[80:83]
RD a[224:227],v224 offset:57344
CV v15,v46,v47
MM a[80:83],a[164:167],v[84:87],a[80:83]
EX v49,v49
MM a[88:91],a[164:167],v[92:95],a[88:91]
EX v50,v50
MM a[88:91],a[160:163],v[88:91],a[88:91]
buffer_load_dwordx4 v235, s[12:15], s81 offen offset:0 lds
MM a[92:95],a[168:171],v[88:91],a[92:95]
EX v51,v51
MM a[92:95],a[172:175],v[92:95],a[92:95]
RD a[228:231],v225 offset:57344
CV v20,v48,v49
MM a[84:87],a[172:175],v[84:87],a[84:87]
EX v52,v52
MM a[84:87],a[168:171],v[80:83],a[84:87]
RD a[232:235],v226 offset:57344
CV v21,v50,v51
MM a[76:79],a[168:171],v[72:75],a[76:79]
EX v53,v53
MM a[76:79],a[172:175],v[76:79],a[76:79]
RD a[236:239],v227 offset:57344
CV v22,v52,v53
MM a[68:71],a[172:175],v[68:71],a[68:71]
EX v54,v54
MM a[68:71],a[168:171],v[64:67],a[68:71]
buffer_load_dwordx4 v235, s[12:15], s64 offen offset:1024 lds
MM a[96:99],a[176:179],v[64:67],a[96:99]
EX v55,v55
MM a[96:99],a[180:183],v[68:71],a[96:99]
EX v56,v56
MM a[104:107],a[180:183],v[76:79],a[104:107]
RD a[240:243],v224 offset:61440
CV v23,v54,v55
MM a[104:107],a[176:179],v[72:75],a[104:107]
EX v57,v57
MM a[112:115],a[176:179],v[80:83],a[112:115]
RD a[244:247],v225 offset:61440
CV v28,v56,v57
MM a[112:115],a[180:183],v[84:87],a[112:115]
EX v58,v58
MM a[120:123],a[180:183],v[92:95],a[120:123]
s_waitcnt vmcnt(9)
buffer_load_dwordx4 v235, s[12:15], s81 offen offset:2048 lds
MM a[120:123],a[176:179],v[88:91],a[120:123]
EX v59,v59
MM a[124:127],a[184:187],v[88:91],a[124:127]
RD a[248:251],v226 offset:61440
CV v29,v58,v59
MM a[124:127],a[188:191],v[92:95],a[124:127]
EX v60,v60
MM a[116:119],a[188:191],v[84:87],a[116:119]
EX v61,v61
MM a[116:119],a[184:187],v[80:83],a[116:119]
RD a[252:255],v227 offset:61440
CV v30,v60,v61
MM a[108:111],a[184:187],v[72:75],a[108:111]
EX v62,v62
MM a[108:111],a[188:191],v[76:79],a[108:111]
buffer_load_dwordx4 v235, s[12:15], s64 offen offset:3072 lds
MM a[100:103],a[188:191],v[68:71],a[100:103]
EX v63,v63
MM a[100:103],a[184:187],v[64:67],a[100:103]
CV v31,v62,v63
v_smfmac_f32_16x16x64_bf16 v[236:239], v[144:147], v[0:7], v234
v_smfmac_f32_16x16x64_bf16 v[236:239], v[148:151], v[8:15], v234
v_smfmac_f32_16x16x64_bf16 v[236:239], v[152:155], v[16:23], v234
v_smfmac_f32_16x16x64_bf16 v[236:239], v[156:159], v[24:31], v234
s_waitcnt lgkmcnt(0)
MM v[64:67],a[192:195],v[160:163],v[128:131]
s_add_u32 s64, s64, 16384
TR a[128:129],v232 offset:32768
s_add_u32 s81, s64, 896
MM v[64:67],a[196:199],v[176:179],v[64:67]
TR a[130:131],v232 offset:36864
TR a[132:133],v232 offset:40960
MM v[64:67],a[200:203],v[192:195],v[64:67]
TR a[134:135],v232 offset:45056
TR a[136:137],v233 offset:32768
MM v[64:67],a[204:207],v[208:211],v[64:67]
TR a[138:139],v233 offset:36864
v_add_f32 v246, v236, v237
MM v[68:71],a[220:223],v[208:211],v[128:131]
TR a[140:141],v233 offset:40960
v_cmp_nge_f32 vcc, s71, v246
MM v[68:71],a[216:219],v[192:195],v[68:71]
s_cbranch_vccnz LSITE26_%=
LBACK26_%=:
TR a[142:143],v233 offset:45056
TR a[144:145],v232 offset:33280
MM v[68:71],a[212:215],v[176:179],v[68:71]
TR a[146:147],v232 offset:37376
TR a[148:149],v232 offset:41472
MM v[68:71],a[208:211],v[160:163],v[68:71]
TR a[150:151],v232 offset:45568
TR a[152:153],v233 offset:33280
MM v[76:79],a[208:211],v[164:167],v[132:135]
EX v64,v64
MM v[76:79],a[212:215],v[180:183],v[76:79]
EX v65,v65
MM v[76:79],a[216:219],v[196:199],v[76:79]
TR a[154:155],v233 offset:37376
CV v64,v64,v65
MM v[76:79],a[220:223],v[212:215],v[76:79]
EX v66,v66
MM v[72:75],a[204:207],v[212:215],v[132:135]
EX v67,v67
MM v[72:75],a[200:203],v[196:199],v[72:75]
TR a[156:157],v233 offset:41472
CV v65,v66,v67
MM v[72:75],a[196:199],v[180:183],v[72:75]
EX v68,v68
MM v[72:75],a[192:195],v[164:167],v[72:75]
EX v69,v69
MM v[80:83],a[192:195],v[168:171],v[136:139]
TR a[158:159],v233 offset:45568
CV v66,v68,v69
MM v[80:83],a[196:199],v[184:187],v[80:83]
EX v70,v70
MM v[80:83],a[200:203],v[200:203],v[80:83]
EX v71,v71
MM v[80:83],a[204:207],v[216:219],v[80:83]
TR a[160:161],v232 offset:33792
CV v67,v70,v71
MM v[84:87],a[220:223],v[216:219],v[136:139]
EX v72,v72
MM v[84:87],a[216:219],v[200:203],v[84:87]
EX v73,v73
MM v[84:87],a[212:215],v[184:187],v[84:87]
TR a[162:163],v232 offset:37888
CV v72,v72,v73
MM v[84:87],a[208:211],v[168:171],v[84:87]
EX v74,v74
MM v[92:95],a[208:211],v[172:175],v[140:143]
TR a[164:165],v232 offset:41984
TR a[166:167],v232 offset:46080
MM v[92:95],a[212:215],v[188:191],v[92:95]
EX v75,v75
MM v[92:95],a[216:219],v[204:207],v[92:95]
TR a[168:169],v233 offset:33792
CV v73,v74,v75
MM v[92:95],a[220:223],v[220:223],v[92:95]
EX v76,v76
MM v[88:91],a[204:207],v[220:223],v[140:143]
EX v77,v77
MM v[88:91],a[200:203],v[204:207],v[88:91]
TR a[170:171],v233 offset:37888
CV v74,v76,v77
MM v[88:91],a[196:199],v[188:191],v[88:91]
EX v78,v78
MM v[88:91],a[192:195],v[172:175],v[88:91]
TR a[172:173],v233 offset:41984
TR a[174:175],v233 offset:46080
MM v[96:99],a[224:227],v[160:163],v[128:131]
EX v79,v79
MM v[96:99],a[228:231],v[176:179],v[96:99]
TR a[176:177],v232 offset:34304
CV v75,v78,v79
MM v[96:99],a[232:235],v[192:195],v[96:99]
EX v80,v80
MM v[96:99],a[236:239],v[208:211],v[96:99]
TR a[178:179],v232 offset:38400
TR a[180:181],v232 offset:42496
MM v[100:103],a[252:255],v[208:211],v[128:131]
EX v81,v81
MM v[100:103],a[248:251],v[192:195],v[100:103]
TR a[182:183],v232 offset:46592
CV v80,v80,v81
MM v[100:103],a[244:247],v[176:179],v[100:103]
EX v82,v82
MM v[100:103],a[240:243],v[160:163],v[100:103]
EX v83,v83
MM v[108:111],a[240:243],v[164:167],v[132:135]
TR a[184:185],v233 offset:34304
CV v81,v82,v83
MM v[108:111],a[244:247],v[180:183],v[108:111]
EX v84,v84
MM v[108:111],a[248:251],v[196:199],v[108:111]
TR a[186:187],v233 offset:38400
TR a[188:189],v233 offset:42496
MM v[108:111],a[252:255],v[212:215],v[108:111]
EX v85,v85
MM v[104:107],a[236:239],v[212:215],v[132:135]
TR a[190:191],v233 offset:46592
CV v82,v84,v85
MM v[104:107],a[232:235],v[196:199],v[104:107]
EX v86,v86
MM v[104:107],a[228:231],v[180:183],v[104:107]
EX v87,v87
MM v[104:107],a[224:227],v[164:167],v[104:107]
CV v83,v86,v87
MM v[112:115],a[224:227],v[168:171],v[136:139]
EX v88,v88
MM v[112:115],a[228:231],v[184:187],v[112:115]
EX v89,v89
MM v[112:115],a[232:235],v[200:203],v[112:115]
EX v90,v90
MM v[112:115],a[236:239],v[216:219],v[112:115]
EX v91,v91
MM v[116:119],a[252:255],v[216:219],v[136:139]
EX v92,v92
MM v[116:119],a[248:251],v[200:203],v[116:119]
EX v93,v93
MM v[116:119],a[244:247],v[184:187],v[116:119]
EX v94,v94
MM v[116:119],a[240:243],v[168:171],v[116:119]
EX v95,v95
MM v[124:127],a[240:243],v[172:175],v[140:143]
EX v96,v96
MM v[124:127],a[244:247],v[188:191],v[124:127]
EX v97,v97
MM v[124:127],a[248:251],v[204:207],v[124:127]
EX v98,v98
MM v[124:127],a[252:255],v[220:223],v[124:127]
EX v99,v99
MM v[120:123],a[236:239],v[220:223],v[140:143]
EX v100,v100
MM v[120:123],a[232:235],v[204:207],v[120:123]
EX v101,v101
MM v[120:123],a[228:231],v[188:191],v[120:123]
EX v102,v102
MM v[120:123],a[224:227],v[172:175],v[120:123]
s_waitcnt lgkmcnt(0)
s_barrier
MM a[0:3],a[128:131],v[0:3],a[0:3]
s_add_u32 m0, s65, 32768
CV v88,v88,v89
CV v89,v90,v91
MM a[0:3],a[132:135],v[4:7],a[0:3]
RD a[192:195],v224 offset:0
CV v90,v92,v93
MM a[8:11],a[132:135],v[12:15],a[8:11]
RD a[196:199],v225 offset:0
CV v91,v94,v95
MM a[8:11],a[128:131],v[8:11],a[8:11]
buffer_load_dwordx4 v228, s[8:11], s64 offen offset:0 lds
MM a[16:19],a[128:131],v[16:19],a[16:19]
RD a[200:203],v226 offset:0
CV v68,v96,v97
MM a[16:19],a[132:135],v[20:23],a[16:19]
RD a[204:207],v227 offset:0
CV v69,v98,v99
MM a[24:27],a[132:135],v[28:31],a[24:27]
buffer_load_dwordx4 v229, s[8:11], s64 offen offset:1024 lds
MM a[24:27],a[128:131],v[24:27],a[24:27]
RD a[208:211],v224 offset:4096
CV v70,v100,v101
MM a[28:31],a[136:139],v[24:27],a[28:31]
RD a[212:215],v225 offset:4096
RD a[216:219],v226 offset:4096
MM a[28:31],a[140:143],v[28:31],a[28:31]
buffer_load_dwordx4 v230, s[8:11], s64 offen offset:2048 lds
MM a[20:23],a[140:143],v[20:23],a[20:23]
RD a[220:223],v227 offset:4096
RD a[224:227],v224 offset:8192
MM a[20:23],a[136:139],v[16:19],a[20:23]
buffer_load_dwordx4 v231, s[8:11], s64 offen offset:3072 lds
MM a[12:15],a[136:139],v[8:11],a[12:15]
s_add_u32 m0, s65, 98304
EX v103,v103
MM a[12:15],a[140:143],v[12:15],a[12:15]
RD a[228:231],v225 offset:8192
CV v71,v102,v103
MM a[4:7],a[140:143],v[4:7],a[4:7]
LPG19_%=:
s_cbranch_execz LPG23_%=
EX v104,v104
MM a[4:7],a[136:139],v[0:3],a[4:7]
buffer_load_dwordx4 v235, s[12:15], s81 offen offset:0 lds
MM a[32:35],a[144:147],v[0:3],a[32:35]
EX v105,v105
MM a[32:35],a[148:151],v[4:7],a[32:35]
RD a[232:235],v226 offset:8192
CV v76,v104,v105
MM a[40:43],a[148:151],v[12:15],a[40:43]
EX v106,v106
MM a[40:43],a[144:147],v[8:11],a[40:43]
EX v107,v107
MM a[48:51],a[144:147],v[16:19],a[48:51]
RD a[236:239],v227 offset:8192
CV v77,v106,v107
MM a[48:51],a[148:151],v[20:23],a[48:51]
EX v108,v108
MM a[56:59],a[148:151],v[28:31],a[56:59]
buffer_load_dwordx4 v235, s[12:15], s64 offen offset:1024 lds
MM a[56:59],a[144:147],v[24:27],a[56:59]
EX v109,v109
MM a[60:63],a[152:155],v[24:27],a[60:63]
RD a[240:243],v224 offset:12288
CV v78,v108,v109
MM a[60:63],a[156:159],v[28:31],a[60:63]
EX v110,v110
MM a[52:55],a[156:159],v[20:23],a[52:55]
EX v111,v111
MM a[52:55],a[152:155],v[16:19],a[52:55]
RD a[244:247],v225 offset:12288
CV v79,v110,v111
MM a[44:47],a[152:155],v[8:11],a[44:47]
EX v112,v112
MM a[44:47],a[156:159],v[12:15],a[44:47]
buffer_load_dwordx4 v235, s[12:15], s81 offen offset:2048 lds
MM a[36:39],a[156:159],v[4:7],a[36:39]
EX v113,v113
MM a[36:39],a[152:155],v[0:3],a[36:39]
RD a[248:251],v226 offset:12288
CV v84,v112,v113
MM a[64:67],a[160:163],v[0:3],a[64:67]
EX v114,v114
MM a[64:67],a[164:167],v[4:7],a[64:67]
EX v115,v115
MM a[72:75],a[164:167],v[12:15],a[72:75]
RD a[252:255],v227 offset:12288
CV v85,v114,v115
MM a[72:75],a[160:163],v[8:11],a[72:75]
EX v116,v116
MM a[80:83],a[160:163],v[16:19],a[80:83]
)ASM"
R"ASM(
buffer_load_dwordx4 v235, s[12:15], s64 offen offset:3072 lds
MM a[80:83],a[164:167],v[20:23],a[80:83]
EX v117,v117
MM a[88:91],a[164:167],v[28:31],a[88:91]
EX v118,v118
MM a[88:91],a[160:163],v[24:27],a[88:91]
EX v119,v119
MM a[92:95],a[168:171],v[24:27],a[92:95]
EX v120,v120
MM a[92:95],a[172:175],v[28:31],a[92:95]
EX v121,v121
MM a[84:87],a[172:175],v[20:23],a[84:87]
EX v122,v122
MM a[84:87],a[168:171],v[16:19],a[84:87]
EX v123,v123
MM a[76:79],a[168:171],v[8:11],a[76:79]
EX v124,v124
MM a[76:79],a[172:175],v[12:15],a[76:79]
EX v125,v125
MM a[68:71],a[172:175],v[4:7],a[68:71]
EX v126,v126
MM a[68:71],a[168:171],v[0:3],a[68:71]
EX v127,v127
MM a[96:99],a[176:179],v[0:3],a[96:99]
CV v86,v116,v117
CV v87,v118,v119
MM a[96:99],a[180:183],v[4:7],a[96:99]
CV v92,v120,v121
CV v93,v122,v123
MM a[104:107],a[180:183],v[12:15],a[104:107]
CV v94,v124,v125
CV v95,v126,v127
MM a[104:107],a[176:179],v[8:11],a[104:107]
MM a[112:115],a[176:179],v[16:19],a[112:115]
MM a[112:115],a[180:183],v[20:23],a[112:115]
MM a[120:123],a[180:183],v[28:31],a[120:123]
MM a[120:123],a[176:179],v[24:27],a[120:123]
MM a[124:127],a[184:187],v[24:27],a[124:127]
MM a[124:127],a[188:191],v[28:31],a[124:127]
MM a[116:119],a[188:191],v[20:23],a[116:119]
MM a[116:119],a[184:187],v[16:19],a[116:119]
MM a[108:111],a[184:187],v[8:11],a[108:111]
MM a[108:111],a[188:191],v[12:15],a[108:111]
MM a[100:103],a[188:191],v[4:7],a[100:103]
MM a[100:103],a[184:187],v[0:3],a[100:103]
v_smfmac_f32_16x16x64_bf16 v[236:239], v[144:147], v[64:71], v234
v_smfmac_f32_16x16x64_bf16 v[236:239], v[148:151], v[72:79], v234
v_smfmac_f32_16x16x64_bf16 v[236:239], v[152:155], v[80:87], v234
v_smfmac_f32_16x16x64_bf16 v[236:239], v[156:159], v[88:95], v234
s_waitcnt lgkmcnt(0)
MM v[16:19],a[192:195],v[168:171],v[136:139]
TR a[128:129],v232 offset:49152
TR a[130:131],v232 offset:53248
MM v[16:19],a[196:199],v[184:187],v[16:19]
TR a[132:133],v232 offset:57344
TR a[134:135],v232 offset:61440
MM v[16:19],a[200:203],v[200:203],v[16:19]
TR a[136:137],v233 offset:49152
TR a[138:139],v233 offset:53248
MM v[16:19],a[204:207],v[216:219],v[16:19]
TR a[140:141],v233 offset:57344
v_add_f32 v247, v236, v237
MM v[20:23],a[220:223],v[216:219],v[136:139]
TR a[142:143],v233 offset:61440
v_cmp_nge_f32 vcc, s71, v247
MM v[20:23],a[216:219],v[200:203],v[20:23]
s_cbranch_vccnz LSITE27_%=
LBACK27_%=:
TR a[144:145],v232 offset:49664
TR a[146:147],v232 offset:53760
MM v[20:23],a[212:215],v[184:187],v[20:23]
v_rcp_f32 v145, v247
MM v[20:23],a[208:211],v[168:171],v[20:23]
TR a[148:149],v232 offset:57856
TR a[150:151],v232 offset:61952
MM v[28:31],a[208:211],v[172:175],v[140:143]
EX v16,v16
MM v[28:31],a[212:215],v[188:191],v[28:31]
TR a[152:153],v233 offset:49664
TR a[154:155],v233 offset:53760
MM v[28:31],a[216:219],v[204:207],v[28:31]
EX v17,v17
MM v[28:31],a[220:223],v[220:223],v[28:31]
TR a[156:157],v233 offset:57856
CV v16,v16,v17
MM v[24:27],a[204:207],v[220:223],v[140:143]
TR a[158:159],v233 offset:61952
TR a[160:161],v232 offset:50176
MM v[24:27],a[200:203],v[204:207],v[24:27]
TR a[162:163],v232 offset:54272
TR a[164:165],v232 offset:58368
MM v[24:27],a[196:199],v[188:191],v[24:27]
EX v18,v18
MM v[24:27],a[192:195],v[172:175],v[24:27]
TR a[166:167],v232 offset:62464
TR a[168:169],v233 offset:50176
MM v[48:51],a[224:227],v[168:171],v[136:139]
EX v19,v19
MM v[48:51],a[228:231],v[184:187],v[48:51]
TR a[170:171],v233 offset:54272
CV v17,v18,v19
MM v[48:51],a[232:235],v[200:203],v[48:51]
TR a[172:173],v233 offset:58368
TR a[174:175],v233 offset:62464
MM v[48:51],a[236:239],v[216:219],v[48:51]
TR a[176:177],v232 offset:50688
TR a[178:179],v232 offset:54784
MM v[52:55],a[252:255],v[216:219],v[136:139]
EX v20,v20
MM v[52:55],a[248:251],v[200:203],v[52:55]
TR a[180:181],v232 offset:58880
TR a[182:183],v232 offset:62976
MM v[52:55],a[244:247],v[184:187],v[52:55]
EX v21,v21
MM v[52:55],a[240:243],v[168:171],v[52:55]
TR a[184:185],v233 offset:50688
TR a[186:187],v233 offset:54784
MM v[60:63],a[240:243],v[172:175],v[140:143]
TR a[188:189],v233 offset:58880
CV v18,v20,v21
MM v[60:63],a[244:247],v[188:191],v[60:63]
TR a[190:191],v233 offset:62976
v_mov_b32 v144, v145
MM v[60:63],a[248:251],v[204:207],v[60:63]
EX v22,v22
MM v[60:63],a[252:255],v[220:223],v[60:63]
EX v23,v23
MM v[56:59],a[236:239],v[220:223],v[140:143]
CV v19,v22,v23
v_permlane32_swap_b32 v145, v144
MM v[56:59],a[232:235],v[204:207],v[56:59]
EX v24,v24
MM v[56:59],a[228:231],v[188:191],v[56:59]
EX v25,v25
MM v[56:59],a[224:227],v[172:175],v[56:59]
s_waitcnt vmcnt(12) lgkmcnt(0)
s_barrier
CV v24,v24,v25
MM a[0:3],a[128:131],v[64:67],a[0:3]
s_add_u32 m0, s89, 49152
RD a[192:195],v224 offset:16384
RD a[196:199],v225 offset:16384
MM a[0:3],a[132:135],v[68:71],a[0:3]
s_add_u32 s66, s85, 0
buffer_load_dwordx4 v240, s[24:27], s66 offen offset:0 lds
MM a[8:11],a[132:135],v[76:79],a[8:11]
s_add_u32 m0, s89, 50176
EX v26,v26
MM a[8:11],a[128:131],v[72:75],a[8:11]
s_add_u32 s66, s85, 10240
RD a[200:203],v226 offset:16384
RD a[204:207],v227 offset:16384
MM a[12:15],a[136:139],v[72:75],a[12:15]
EX v27,v27
MM a[12:15],a[140:143],v[76:79],a[12:15]
RD a[208:211],v224 offset:20480
CV v25,v26,v27
MM a[4:7],a[140:143],v[68:71],a[4:7]
buffer_load_dwordx4 v240, s[24:27], s66 offen offset:0 lds
MM a[4:7],a[136:139],v[64:67],a[4:7]
s_add_u32 m0, s89, 57344
EX v28,v28
MM a[32:35],a[144:147],v[64:67],a[32:35]
s_add_u32 s66, s85, 81920
EX v29,v29
MM a[32:35],a[148:151],v[68:71],a[32:35]
CV v26,v28,v29
v_mov_b32 v149, v145
MM a[40:43],a[148:151],v[76:79],a[40:43]
RD a[212:215],v225 offset:20480
v_permlane16_swap_b32 v145, v149
MM a[40:43],a[144:147],v[72:75],a[40:43]
s_mul_i32 s78, s49, 17408
s_add_u32 s78, s83, s78
s_add_u32 s78, s78, 0
MM a[44:47],a[152:155],v[72:75],a[44:47]
s_add_u32 s79, s78, 4096
AR v0,a0
AR v1,a1
MM a[44:47],a[156:159],v[76:79],a[44:47]
AR v2,a2
AR v3,a3
MM a[36:39],a[156:159],v[68:71],a[36:39]
AR v4,a4
AR v5,a5
MM a[36:39],a[152:155],v[64:67],a[36:39]
AR v6,a6
AR v7,a7
MM a[64:67],a[160:163],v[64:67],a[64:67]
EX v30,v30
MM a[64:67],a[164:167],v[68:71],a[64:67]
v_mul_f32 v0, v0, v145
v_mul_f32 v1, v1, v145
MM a[72:75],a[164:167],v[76:79],a[72:75]
v_mul_f32 v2, v2, v145
v_mul_f32 v3, v3, v145
MM a[72:75],a[160:163],v[72:75],a[72:75]
v_mul_f32 v4, v4, v145
v_mul_f32 v5, v5, v145
MM a[76:79],a[168:171],v[72:75],a[76:79]
v_mul_f32 v6, v6, v145
v_mul_f32 v7, v7, v145
MM a[76:79],a[172:175],v[76:79],a[76:79]
EX v31,v31
MM a[68:71],a[172:175],v[68:71],a[68:71]
CV v160,v0,v1
CV v161,v2,v3
MM a[68:71],a[168:171],v[64:67],a[68:71]
CV v162,v4,v5
CV v163,v6,v7
MM a[96:99],a[176:179],v[64:67],a[96:99]
buffer_store_dwordx4 v[160:163], v248, s[20:23], s78 offen offset:0 sc1
EX v48,v48
MM a[96:99],a[180:183],v[68:71],a[96:99]
AR v8,a8
AR v9,a9
MM a[104:107],a[180:183],v[76:79],a[104:107]
AR v10,a10
AR v11,a11
MM a[104:107],a[176:179],v[72:75],a[104:107]
AR v12,a12
AR v13,a13
EX v49,v49
MM a[108:111],a[184:187],v[72:75],a[108:111]
AR v14,a14
AR v15,a15
MM a[108:111],a[188:191],v[76:79],a[108:111]
v_mul_f32 v8, v8, v149
v_mul_f32 v9, v9, v149
EX v50,v50
MM a[100:103],a[188:191],v[68:71],a[100:103]
v_mul_f32 v10, v10, v149
v_mul_f32 v11, v11, v149
MM a[100:103],a[184:187],v[64:67],a[100:103]
buffer_load_dwordx4 v240, s[24:27], s66 offen offset:0 lds
v_mul_f32 v12, v12, v149
v_mul_f32 v13, v13, v149
MM a[16:19],a[128:131],v[80:83],a[16:19]
s_add_u32 m0, s89, 58368
v_mul_f32 v14, v14, v149
v_mul_f32 v15, v15, v149
EX v51,v51
MM a[16:19],a[132:135],v[84:87],a[16:19]
s_add_u32 s66, s85, 92160
CV v164,v8,v9
CV v27,v30,v31
MM a[24:27],a[132:135],v[92:95],a[24:27]
CV v165,v10,v11
CV v166,v12,v13
MM a[24:27],a[128:131],v[88:91],a[24:27]
RD a[216:219],v226 offset:20480
CV v167,v14,v15
EX v52,v52
MM a[28:31],a[136:139],v[88:91],a[28:31]
buffer_store_dwordx4 v[164:167], v248, s[20:23], s79 offen offset:0 sc1
AR v32,a32
MM a[28:31],a[140:143],v[92:95],a[28:31]
AR v33,a33
AR v34,a34
EX v53,v53
MM a[20:23],a[140:143],v[84:87],a[20:23]
AR v35,a35
AR v36,a36
MM a[20:23],a[136:139],v[80:83],a[20:23]
buffer_load_dwordx4 v240, s[24:27], s66 offen offset:0 lds
AR v37,a37
AR v38,a38
MM a[48:51],a[144:147],v[80:83],a[48:51]
s_add_u32 m0, s89, 114688
AR v39,a39
v_mul_f32 v32, v32, v145
EX v54,v54
MM a[48:51],a[148:151],v[84:87],a[48:51]
s_add_u32 s66, s86, 0
v_mul_f32 v33, v33, v145
CV v20,v48,v49
MM a[56:59],a[148:151],v[92:95],a[56:59]
RD a[220:223],v227 offset:20480
v_mul_f32 v34, v34, v145
v_mul_f32 v35, v35, v145
EX v55,v55
MM a[56:59],a[144:147],v[88:91],a[56:59]
v_mul_f32 v36, v36, v145
v_mul_f32 v37, v37, v145
MM a[60:63],a[152:155],v[88:91],a[60:63]
v_mul_f32 v38, v38, v145
v_mul_f32 v39, v39, v145
MM a[60:63],a[156:159],v[92:95],a[60:63]
CV v176,v32,v33
CV v177,v34,v35
EX v56,v56
MM a[52:55],a[156:159],v[84:87],a[52:55]
RD a[224:227],v224 offset:24576
buffer_load_dwordx4 v240, s[24:27], s66 offen offset:0 lds
CV v178,v36,v37
CV v179,v38,v39
CV v21,v50,v51
MM a[52:55],a[152:155],v[80:83],a[52:55]
s_add_u32 m0, s89, 115712
buffer_store_dwordx4 v[176:179], v248, s[20:23], s78 offen offset:1024 sc1
MM a[80:83],a[160:163],v[80:83],a[80:83]
s_add_u32 s66, s86, 10240
AR v40,a40
AR v41,a41
EX v57,v57
MM a[80:83],a[164:167],v[84:87],a[80:83]
AR v42,a42
AR v43,a43
MM a[88:91],a[164:167],v[92:95],a[88:91]
RD a[228:231],v225 offset:24576
AR v44,a44
AR v45,a45
MM a[88:91],a[160:163],v[88:91],a[88:91]
AR v46,a46
CV v22,v52,v53
EX v58,v58
MM a[92:95],a[168:171],v[88:91],a[92:95]
AR v47,a47
v_mul_f32 v40, v40, v149
MM a[92:95],a[172:175],v[92:95],a[92:95]
v_mul_f32 v41, v41, v149
v_mul_f32 v42, v42, v149
EX v59,v59
MM a[84:87],a[172:175],v[84:87],a[84:87]
RD a[232:235],v226 offset:24576
buffer_load_dwordx4 v240, s[24:27], s66 offen offset:0 lds
v_mul_f32 v43, v43, v149
v_mul_f32 v44, v44, v149
MM a[84:87],a[168:171],v[80:83],a[84:87]
s_add_u32 m0, s89, 122880
v_mul_f32 v45, v45, v149
v_mul_f32 v46, v46, v149
MM a[112:115],a[176:179],v[80:83],a[112:115]
s_add_u32 s66, s86, 81920
v_mul_f32 v47, v47, v149
CV v180,v40,v41
EX v60,v60
CV v23,v54,v55
MM a[112:115],a[180:183],v[84:87],a[112:115]
CV v181,v42,v43
CV v182,v44,v45
MM a[120:123],a[180:183],v[92:95],a[120:123]
RD a[236:239],v227 offset:24576
CV v183,v46,v47
EX v61,v61
MM a[120:123],a[176:179],v[88:91],a[120:123]
buffer_store_dwordx4 v[180:183], v248, s[20:23], s79 offen offset:1024 sc1
AR v0,a64
MM a[124:127],a[184:187],v[88:91],a[124:127]
AR v1,a65
AR v2,a66
MM a[124:127],a[188:191],v[92:95],a[124:127]
AR v3,a67
AR v4,a68
EX v62,v62
MM a[116:119],a[188:191],v[84:87],a[116:119]
RD a[240:243],v224 offset:28672
RD a[244:247],v225 offset:28672
RD a[248:251],v226 offset:28672
RD a[252:255],v227 offset:28672
buffer_load_dwordx4 v240, s[24:27], s66 offen offset:0 lds
EX v63,v63
AR v5,a69
CV v28,v56,v57
MM a[116:119],a[184:187],v[80:83],a[116:119]
s_add_u32 m0, s89, 123904
AR v6,a70
AR v7,a71
v_smfmac_f32_16x16x64_bf16 v[236:239], v[152:155], v[16:23], v234
s_add_u32 s66, s86, 92160
buffer_load_dwordx4 v240, s[24:27], s66 offen offset:0 lds
CV v29,v58,v59
CV v30,v60,v61
CV v31,v62,v63
v_mul_f32 v0, v0, v145
v_mul_f32 v1, v1, v145
v_smfmac_f32_16x16x64_bf16 v[236:239], v[156:159], v[24:31], v234
s_waitcnt lgkmcnt(0)
v_mul_f32 v2, v2, v145
v_mul_f32 v3, v3, v145
MM v[80:83],a[192:195],v[168:171],v[136:139]
v_mul_f32 v4, v4, v145
v_mul_f32 v5, v5, v145
MM v[80:83],a[196:199],v[184:187],v[80:83]
v_mul_f32 v6, v6, v145
v_mul_f32 v7, v7, v145
MM v[80:83],a[200:203],v[200:203],v[80:83]
CV v192,v0,v1
CV v193,v2,v3
MM v[80:83],a[204:207],v[216:219],v[80:83]
TR a[128:129],v232 offset:0
v_add_f32 v246, v236, v237
CV v194,v4,v5
CV v195,v6,v7
MM v[84:87],a[220:223],v[216:219],v[136:139]
TR a[130:131],v232 offset:4096
buffer_store_dwordx4 v[192:195], v248, s[20:23], s78 offen offset:2048 sc1
TR a[132:133],v232 offset:8192
v_cmp_nge_f32 vcc, s71, v246
MM v[84:87],a[216:219],v[200:203],v[84:87]
s_cbranch_vccnz LSITE28_%=
LBACK28_%=:
TR a[134:135],v232 offset:12288
AR v8,a72
AR v9,a73
MM v[84:87],a[212:215],v[184:187],v[84:87]
TR a[136:137],v233 offset:0
AR v10,a74
AR v11,a75
MM v[84:87],a[208:211],v[168:171],v[84:87]
TR a[138:139],v233 offset:4096
)ASM"
R"ASM(
AR v12,a76
MM v[92:95],a[208:211],v[172:175],v[140:143]
TR a[140:141],v233 offset:8192
TR a[142:143],v233 offset:12288
AR v13,a77
AR v14,a78
MM v[92:95],a[212:215],v[188:191],v[92:95]
TR a[144:145],v232 offset:512
AR v15,a79
v_mul_f32 v8, v8, v149
MM v[92:95],a[216:219],v[204:207],v[92:95]
TR a[146:147],v232 offset:4608
v_mul_f32 v9, v9, v149
v_mul_f32 v10, v10, v149
MM v[92:95],a[220:223],v[220:223],v[92:95]
TR a[148:149],v232 offset:8704
v_mul_f32 v11, v11, v149
v_mul_f32 v12, v12, v149
MM v[88:91],a[204:207],v[220:223],v[140:143]
TR a[150:151],v232 offset:12800
v_mul_f32 v13, v13, v149
EX v80,v80
v_mul_f32 v14, v14, v149
MM v[88:91],a[200:203],v[204:207],v[88:91]
TR a[152:153],v233 offset:512
TR a[154:155],v233 offset:4608
v_mul_f32 v15, v15, v149
CV v196,v8,v9
EX v81,v81
MM v[88:91],a[196:199],v[188:191],v[88:91]
TR a[156:157],v233 offset:8704
CV v197,v10,v11
CV v198,v12,v13
MM v[88:91],a[192:195],v[172:175],v[88:91]
TR a[158:159],v233 offset:12800
CV v199,v14,v15
EX v82,v82
MM v[112:115],a[224:227],v[168:171],v[136:139]
buffer_store_dwordx4 v[196:199], v248, s[20:23], s79 offen offset:2048 sc1
TR a[160:161],v232 offset:1024
AR v32,a96
EX v83,v83
MM v[112:115],a[228:231],v[184:187],v[112:115]
TR a[162:163],v232 offset:5120
TR a[164:165],v232 offset:9216
AR v33,a97
AR v34,a98
MM v[112:115],a[232:235],v[200:203],v[112:115]
TR a[166:167],v232 offset:13312
AR v35,a99
EX v84,v84
MM v[112:115],a[236:239],v[216:219],v[112:115]
TR a[168:169],v233 offset:1024
AR v36,a100
AR v37,a101
MM v[116:119],a[252:255],v[216:219],v[136:139]
TR a[170:171],v233 offset:5120
AR v38,a102
EX v85,v85
AR v39,a103
MM v[116:119],a[248:251],v[200:203],v[116:119]
TR a[172:173],v233 offset:9216
TR a[174:175],v233 offset:13312
v_mul_f32 v32, v32, v145
v_mul_f32 v33, v33, v145
MM v[116:119],a[244:247],v[184:187],v[116:119]
TR a[176:177],v232 offset:1536
v_mul_f32 v34, v34, v145
v_mul_f32 v35, v35, v145
EX v86,v86
MM v[116:119],a[240:243],v[168:171],v[116:119]
TR a[178:179],v232 offset:5632
v_mul_f32 v36, v36, v145
v_mul_f32 v37, v37, v145
EX v87,v87
MM v[124:127],a[240:243],v[172:175],v[140:143]
TR a[180:181],v232 offset:9728
v_mul_f32 v38, v38, v145
v_mul_f32 v39, v39, v145
CV v80,v80,v81
MM v[124:127],a[244:247],v[188:191],v[124:127]
TR a[182:183],v232 offset:13824
CV v208,v32,v33
EX v88,v88
CV v209,v34,v35
MM v[124:127],a[248:251],v[204:207],v[124:127]
TR a[184:185],v233 offset:1536
TR a[186:187],v233 offset:5632
TR a[188:189],v233 offset:9728
TR a[190:191],v233 offset:13824
CV v210,v36,v37
CV v211,v38,v39
EX v89,v89
MM v[124:127],a[252:255],v[220:223],v[124:127]
buffer_store_dwordx4 v[208:211], v248, s[20:23], s78 offen offset:3072 sc1
CV v81,v82,v83
MM v[120:123],a[236:239],v[220:223],v[140:143]
AR v40,a104
AR v41,a105
EX v90,v90
MM v[120:123],a[232:235],v[204:207],v[120:123]
AR v42,a106
AR v43,a107
MM v[120:123],a[228:231],v[188:191],v[120:123]
EX v91,v91
AR v44,a108
CV v82,v84,v85
MM v[120:123],a[224:227],v[172:175],v[120:123]
s_waitcnt vmcnt(19) lgkmcnt(0)
s_barrier
AR v45,a109
AR v46,a110
EX v92,v92
MM a[16:19],a[128:131],v[16:19],a[16:19]
s_add_u32 m0, s89, 0
AR v47,a111
v_mul_f32 v40, v40, v149
MM a[16:19],a[132:135],v[20:23],a[16:19]
s_add_u32 s66, s87, 0
v_mul_f32 v41, v41, v149
v_mul_f32 v42, v42, v149
EX v93,v93
CV v83,v86,v87
MM a[24:27],a[132:135],v[28:31],a[24:27]
v_mul_f32 v43, v43, v149
v_mul_f32 v44, v44, v149
MM a[24:27],a[128:131],v[24:27],a[24:27]
v_mul_f32 v45, v45, v149
v_mul_f32 v46, v46, v149
EX v94,v94
MM a[28:31],a[136:139],v[24:27],a[28:31]
LPG20_%=:
s_cbranch_execz LPG24_%=
v_mul_f32 v47, v47, v149
CV v212,v40,v41
EX v95,v95
MM a[28:31],a[140:143],v[28:31],a[28:31]
CV v213,v42,v43
CV v88,v88,v89
CV v214,v44,v45
MM a[20:23],a[140:143],v[20:23],a[20:23]
CV v215,v46,v47
CV v89,v90,v91
EX v112,v112
MM a[20:23],a[136:139],v[16:19],a[20:23]
buffer_store_dwordx4 v[212:215], v248, s[20:23], s79 offen offset:3072 sc1
MM a[48:51],a[144:147],v[16:19],a[48:51]
buffer_load_dwordx4 v240, s[24:27], s66 offen offset:0 lds
RD a[192:195],v224 offset:32768
EX v113,v113
MM a[48:51],a[148:151],v[20:23],a[48:51]
s_add_u32 m0, s89, 1024
EX v114,v114
MM a[56:59],a[148:151],v[28:31],a[56:59]
s_add_u32 s66, s87, 10240
RD a[196:199],v225 offset:32768
CV v90,v92,v93
MM a[56:59],a[144:147],v[24:27],a[56:59]
buffer_load_dwordx4 v240, s[24:27], s66 offen offset:0 lds
RD a[200:203],v226 offset:32768
EX v115,v115
MM a[60:63],a[152:155],v[24:27],a[60:63]
s_add_u32 m0, s89, 8192
EX v116,v116
MM a[60:63],a[156:159],v[28:31],a[60:63]
s_add_u32 s66, s87, 81920
RD a[204:207],v227 offset:32768
CV v91,v94,v95
MM a[52:55],a[156:159],v[20:23],a[52:55]
EX v117,v117
MM a[52:55],a[152:155],v[16:19],a[52:55]
RD a[208:211],v224 offset:36864
RD a[212:215],v225 offset:36864
buffer_load_dwordx4 v240, s[24:27], s66 offen offset:0 lds
MM a[80:83],a[160:163],v[16:19],a[80:83]
s_add_u32 m0, s89, 9216
EX v118,v118
MM a[80:83],a[164:167],v[20:23],a[80:83]
s_add_u32 s66, s87, 92160
RD a[216:219],v226 offset:36864
CV v84,v112,v113
MM a[88:91],a[164:167],v[28:31],a[88:91]
EX v119,v119
MM a[88:91],a[160:163],v[24:27],a[88:91]
buffer_load_dwordx4 v240, s[24:27], s66 offen offset:0 lds
EX v120,v120
MM a[92:95],a[168:171],v[24:27],a[92:95]
s_add_u32 m0, s89, 65536
RD a[220:223],v227 offset:36864
CV v85,v114,v115
MM a[92:95],a[172:175],v[28:31],a[92:95]
s_add_u32 s66, s88, 0
EX v121,v121
MM a[84:87],a[172:175],v[20:23],a[84:87]
RD a[224:227],v224 offset:40960
CV v86,v116,v117
MM a[84:87],a[168:171],v[16:19],a[84:87]
buffer_load_dwordx4 v240, s[24:27], s66 offen offset:0 lds
EX v122,v122
MM a[112:115],a[176:179],v[16:19],a[112:115]
s_add_u32 m0, s89, 66560
RD a[228:231],v225 offset:40960
EX v123,v123
MM a[112:115],a[180:183],v[20:23],a[112:115]
s_add_u32 s66, s88, 10240
RD a[232:235],v226 offset:40960
CV v87,v118,v119
MM a[120:123],a[180:183],v[28:31],a[120:123]
EX v124,v124
MM a[120:123],a[176:179],v[24:27],a[120:123]
buffer_load_dwordx4 v240, s[24:27], s66 offen offset:0 lds
MM a[124:127],a[184:187],v[24:27],a[124:127]
s_add_u32 m0, s89, 73728
RD a[236:239],v227 offset:40960
EX v125,v125
MM a[124:127],a[188:191],v[28:31],a[124:127]
s_add_u32 s66, s88, 81920
EX v126,v126
MM a[116:119],a[188:191],v[20:23],a[116:119]
RD a[240:243],v224 offset:45056
RD a[244:247],v225 offset:45056
RD a[248:251],v226 offset:45056
RD a[252:255],v227 offset:45056
EX v127,v127
MM a[116:119],a[184:187],v[16:19],a[116:119]
buffer_load_dwordx4 v240, s[24:27], s66 offen offset:0 lds
CV v92,v120,v121
v_smfmac_f32_16x16x64_bf16 v[236:239], v[152:155], v[80:87], v234
s_add_u32 m0, s89, 74752
CV v93,v122,v123
CV v94,v124,v125
CV v95,v126,v127
s_nop 1
v_smfmac_f32_16x16x64_bf16 v[236:239], v[156:159], v[88:95], v234
s_waitcnt lgkmcnt(0)
s_add_u32 s66, s88, 92160
buffer_load_dwordx4 v240, s[24:27], s66 offen offset:0 lds
v_lshrrev_b32 v230, 9, v248
v_lshlrev_b32 v230, 9, v230
v_sub_u32 v244, v232, v230
v_sub_u32 v235, v233, v230
v_sub_u32 v244, v244, v230
v_sub_u32 v235, v235, v230
v_sub_u32 v228, v244, v230
v_sub_u32 v229, v235, v230
v_add_u32 v228, 65536, v228
v_add_u32 v229, 65536, v229
v_subrev_u32 v230, 65536, v244
v_subrev_u32 v231, 65536, v235
MM v[16:19],a[192:195],v[168:171],v[136:139]
TR a[128:129],v232 offset:16384
TR a[130:131],v232 offset:20480
MM v[16:19],a[196:199],v[184:187],v[16:19]
TR a[132:133],v232 offset:24576
TR a[134:135],v232 offset:28672
MM v[16:19],a[200:203],v[200:203],v[16:19]
TR a[136:137],v233 offset:16384
TR a[138:139],v233 offset:20480
MM v[16:19],a[204:207],v[216:219],v[16:19]
TR a[140:141],v233 offset:24576
v_add_f32 v247, v236, v237
MM v[20:23],a[220:223],v[216:219],v[136:139]
TR a[142:143],v233 offset:28672
v_cmp_nge_f32 vcc, s71, v247
MM v[20:23],a[216:219],v[200:203],v[20:23]
s_cbranch_vccnz LSITE29_%=
LBACK29_%=:
TR a[144:145],v232 offset:16896
TR a[146:147],v232 offset:20992
MM v[20:23],a[212:215],v[184:187],v[20:23]
TR a[148:149],v232 offset:25088
TR a[150:151],v232 offset:29184
MM v[20:23],a[208:211],v[168:171],v[20:23]
TR a[152:153],v233 offset:16896
TR a[154:155],v233 offset:20992
MM v[28:31],a[208:211],v[172:175],v[140:143]
EX v16,v16
MM v[28:31],a[212:215],v[188:191],v[28:31]
EX v17,v17
MM v[28:31],a[216:219],v[204:207],v[28:31]
s_lshl_b32 s97, s84, 2
s_add_u32 s97, s97, s96
s_add_u32 s97, s97, 0
MM v[28:31],a[220:223],v[220:223],v[28:31]
s_waitcnt vmcnt(8)
v_mov_b32 v242, s69
s_mov_b64 exec, 1
buffer_store_dword v242, off, s[20:23], s97 offset:0 sc1
s_mov_b64 exec, -1
MM v[24:27],a[204:207],v[220:223],v[140:143]
TR a[156:157],v233 offset:25088
TR a[158:159],v233 offset:29184
MM v[24:27],a[200:203],v[204:207],v[24:27]
EX v18,v18
MM v[24:27],a[196:199],v[188:191],v[24:27]
TR a[160:161],v232 offset:17408
TR a[162:163],v232 offset:21504
MM v[24:27],a[192:195],v[172:175],v[24:27]
EX v19,v19
MM v[48:51],a[224:227],v[168:171],v[136:139]
TR a[164:165],v232 offset:25600
TR a[166:167],v232 offset:29696
MM v[48:51],a[228:231],v[184:187],v[48:51]
EX v20,v20
MM v[48:51],a[232:235],v[200:203],v[48:51]
TR a[168:169],v233 offset:17408
TR a[170:171],v233 offset:21504
MM v[48:51],a[236:239],v[216:219],v[48:51]
EX v21,v21
MM v[52:55],a[252:255],v[216:219],v[136:139]
TR a[172:173],v233 offset:25600
CV v16,v16,v17
MM v[52:55],a[248:251],v[200:203],v[52:55]
TR a[174:175],v233 offset:29696
TR a[176:177],v232 offset:17920
MM v[52:55],a[244:247],v[184:187],v[52:55]
EX v22,v22
MM v[52:55],a[240:243],v[168:171],v[52:55]
TR a[178:179],v232 offset:22016
TR a[180:181],v232 offset:26112
EX v23,v23
MM v[60:63],a[240:243],v[172:175],v[140:143]
TR a[182:183],v232 offset:30208
CV v17,v18,v19
MM v[60:63],a[244:247],v[188:191],v[60:63]
EX v24,v24
MM v[60:63],a[248:251],v[204:207],v[60:63]
TR a[184:185],v233 offset:17920
TR a[186:187],v233 offset:22016
TR a[188:189],v233 offset:26112
TR a[190:191],v233 offset:30208
EX v25,v25
MM v[60:63],a[252:255],v[220:223],v[60:63]
EX v26,v26
MM v[56:59],a[236:239],v[220:223],v[140:143]
EX v27,v27
MM v[56:59],a[232:235],v[204:207],v[56:59]
EX v28,v28
MM v[56:59],a[228:231],v[188:191],v[56:59]
EX v29,v29
CV v18,v20,v21
MM v[56:59],a[224:227],v[172:175],v[56:59]
s_waitcnt lgkmcnt(0)
s_barrier
CV v19,v22,v23
MM a[16:19],a[128:131],v[80:83],a[16:19]
s_add_u32 m0, s89, 16384
EX v30,v30
MM a[16:19],a[132:135],v[84:87],a[16:19]
s_add_u32 s66, s85, 128
EX v31,v31
MM a[24:27],a[132:135],v[92:95],a[24:27]
buffer_load_dwordx4 v240, s[24:27], s66 offen offset:0 lds
MM a[24:27],a[128:131],v[88:91],a[24:27]
s_add_u32 m0, s89, 17408
EX v48,v48
MM a[28:31],a[136:139],v[88:91],a[28:31]
s_add_u32 s66, s85, 10368
CV v24,v24,v25
CV v25,v26,v27
MM a[28:31],a[140:143],v[92:95],a[28:31]
CV v26,v28,v29
CV v27,v30,v31
MM a[20:23],a[140:143],v[84:87],a[20:23]
EX v49,v49
MM a[20:23],a[136:139],v[80:83],a[20:23]
EX v50,v50
MM a[48:51],a[144:147],v[80:83],a[48:51]
buffer_load_dwordx4 v240, s[24:27], s66 offen offset:0 lds
MM a[48:51],a[148:151],v[84:87],a[48:51]
s_add_u32 m0, s89, 24576
EX v51,v51
MM a[56:59],a[148:151],v[92:95],a[56:59]
s_add_u32 s66, s85, 82048
TR a[128:129],v232 offset:32768
CV v20,v48,v49
MM a[56:59],a[144:147],v[88:91],a[56:59]
EX v52,v52
MM a[60:63],a[152:155],v[88:91],a[60:63]
TR a[130:131],v232 offset:36864
TR a[132:133],v232 offset:40960
MM a[60:63],a[156:159],v[92:95],a[60:63]
EX v53,v53
MM a[52:55],a[156:159],v[84:87],a[52:55]
TR a[134:135],v232 offset:45056
TR a[136:137],v233 offset:32768
MM a[52:55],a[152:155],v[80:83],a[52:55]
buffer_load_dwordx4 v240, s[24:27], s66 offen offset:0 lds
MM a[80:83],a[160:163],v[80:83],a[80:83]
s_add_u32 m0, s89, 25600
TR a[138:139],v233 offset:36864
TR a[140:141],v233 offset:40960
EX v54,v54
MM a[80:83],a[164:167],v[84:87],a[80:83]
s_add_u32 s66, s85, 92288
TR a[142:143],v233 offset:45056
TR a[144:145],v232 offset:33280
MM a[88:91],a[164:167],v[92:95],a[88:91]
EX v55,v55
MM a[88:91],a[160:163],v[88:91],a[88:91]
TR a[146:147],v232 offset:37376
TR a[148:149],v232 offset:41472
buffer_load_dwordx4 v240, s[24:27], s66 offen offset:0 lds
EX v56,v56
MM a[92:95],a[168:171],v[88:91],a[92:95]
s_add_u32 m0, s89, 81920
TR a[150:151],v232 offset:45568
CV v21,v50,v51
MM a[92:95],a[172:175],v[92:95],a[92:95]
)ASM"
R"ASM(
s_add_u32 s66, s86, 128
TR a[152:153],v233 offset:33280
EX v57,v57
MM a[84:87],a[172:175],v[84:87],a[84:87]
TR a[154:155],v233 offset:37376
TR a[156:157],v233 offset:41472
MM a[84:87],a[168:171],v[80:83],a[84:87]
TR a[158:159],v233 offset:45568
buffer_load_dwordx4 v240, s[24:27], s66 offen offset:0 lds
EX v58,v58
MM a[112:115],a[176:179],v[80:83],a[112:115]
s_add_u32 m0, s89, 82944
TR a[160:161],v232 offset:33792
CV v22,v52,v53
EX v59,v59
MM a[112:115],a[180:183],v[84:87],a[112:115]
s_add_u32 s66, s86, 10368
TR a[162:163],v232 offset:37888
TR a[164:165],v232 offset:41984
MM a[120:123],a[180:183],v[92:95],a[120:123]
TR a[166:167],v232 offset:46080
CV v23,v54,v55
EX v60,v60
MM a[120:123],a[176:179],v[88:91],a[120:123]
TR a[168:169],v233 offset:33792
TR a[170:171],v233 offset:37888
buffer_load_dwordx4 v240, s[24:27], s66 offen offset:0 lds
MM a[124:127],a[184:187],v[88:91],a[124:127]
s_add_u32 m0, s89, 90112
TR a[172:173],v233 offset:41984
v_lshrrev_b32 v243, 2, v248
v_min_u32 v243, 12, v243
EX v61,v61
MM a[124:127],a[188:191],v[92:95],a[124:127]
s_add_u32 s66, s86, 82048
buffer_load_dword v242, v243, s[20:23], s96 offen offset:0 sc1
TR a[174:175],v233 offset:46080
EX v62,v62
MM a[116:119],a[188:191],v[84:87],a[116:119]
TR a[176:177],v232 offset:34304
TR a[178:179],v232 offset:38400
EX v63,v63
MM a[116:119],a[184:187],v[80:83],a[116:119]
TR a[180:181],v232 offset:42496
buffer_load_dwordx4 v240, s[24:27], s66 offen offset:0 lds
CV v28,v56,v57
v_smfmac_f32_16x16x64_bf16 v[236:239], v[152:155], v[16:23], v234
s_add_u32 m0, s89, 91136
TR a[182:183],v232 offset:46592
CV v29,v58,v59
CV v30,v60,v61
CV v31,v62,v63
s_nop 1
v_smfmac_f32_16x16x64_bf16 v[236:239], v[156:159], v[24:31], v234
TR a[184:185],v233 offset:34304
TR a[186:187],v233 offset:38400
TR a[188:189],v233 offset:42496
TR a[190:191],v233 offset:46592
s_waitcnt lgkmcnt(0)
s_add_u32 s66, s86, 92288
buffer_load_dwordx4 v240, s[24:27], s66 offen offset:0 lds
s_nop 6
v_add_f32 v246, v236, v237
v_cmp_nge_f32 vcc, s71, v246
s_cbranch_vccnz LSITE30_%=
LBACK30_%=:
s_waitcnt lgkmcnt(0)
MM a[16:19],a[128:131],v[16:19],a[16:19]
s_waitcnt vmcnt(10)
s_barrier
MM a[16:19],a[132:135],v[20:23],a[16:19]
v_rcp_f32 v152, v246
MM a[24:27],a[132:135],v[28:31],a[24:27]
v_mov_b32 v153, v152
MM a[24:27],a[128:131],v[24:27],a[24:27]
s_nop 0
v_permlane32_swap_b32 v152, v153
MM a[28:31],a[136:139],v[24:27],a[28:31]
v_mov_b32 v157, v153
MM a[28:31],a[140:143],v[28:31],a[28:31]
s_nop 0
v_permlane16_swap_b32 v153, v157
MM a[20:23],a[140:143],v[20:23],a[20:23]
s_mul_i32 s78, s49, 17408
s_add_u32 s78, s83, s78
s_add_u32 s78, s78, 8192
MM a[20:23],a[136:139],v[16:19],a[20:23]
s_add_u32 s79, s78, 4096
MM a[48:51],a[144:147],v[16:19],a[48:51]
TR a[128:129],v230 offset:49152
TR a[130:131],v230 offset:51200
MM a[48:51],a[148:151],v[20:23],a[48:51]
MM a[56:59],a[148:151],v[28:31],a[56:59]
TR a[132:133],v230 offset:53248
TR a[134:135],v230 offset:55296
MM a[56:59],a[144:147],v[24:27],a[56:59]
AR v80,a16
AR v81,a17
MM a[60:63],a[152:155],v[24:27],a[60:63]
TR a[136:137],v230 offset:57344
TR a[138:139],v230 offset:59392
MM a[60:63],a[156:159],v[28:31],a[60:63]
AR v82,a18
AR v83,a19
MM a[52:55],a[156:159],v[20:23],a[52:55]
TR a[140:141],v230 offset:61440
TR a[142:143],v230 offset:63488
MM a[52:55],a[152:155],v[16:19],a[52:55]
AR v84,a20
AR v85,a21
MM a[80:83],a[160:163],v[16:19],a[80:83]
TR a[144:145],v231 offset:49152
TR a[146:147],v231 offset:51200
MM a[80:83],a[164:167],v[20:23],a[80:83]
AR v86,a22
AR v87,a23
MM a[88:91],a[164:167],v[28:31],a[88:91]
TR a[148:149],v231 offset:53248
TR a[150:151],v231 offset:55296
MM a[88:91],a[160:163],v[24:27],a[88:91]
s_waitcnt vmcnt(2)
v_cmp_ne_u32 vcc, s69, v242
s_cbranch_vccnz LRPO0_%=
LRPO0b_%=:
v_mul_f32 v80, v80, v153
MM a[92:95],a[168:171],v[24:27],a[92:95]
s_xor_b32 s92, s49, 1
TR a[152:153],v231 offset:57344
TR a[154:155],v231 offset:59392
v_mul_f32 v81, v81, v153
v_mul_f32 v82, v82, v153
MM a[92:95],a[172:175],v[28:31],a[92:95]
s_mul_i32 s92, s92, 17408
s_add_u32 s92, s83, s92
v_mul_f32 v83, v83, v153
MM a[84:87],a[172:175],v[20:23],a[84:87]
TR a[156:157],v231 offset:61440
TR a[158:159],v231 offset:63488
v_mul_f32 v84, v84, v153
v_mul_f32 v85, v85, v153
MM a[84:87],a[168:171],v[16:19],a[84:87]
buffer_load_dwordx4 a[0:3], v248, s[20:23], s92 offen offset:0 sc1
v_mul_f32 v86, v86, v153
v_mul_f32 v87, v87, v153
MM a[112:115],a[176:179],v[16:19],a[112:115]
TR a[160:161],v230 offset:49664
TR a[162:163],v230 offset:51712
CV v168,v80,v81
MM a[112:115],a[180:183],v[20:23],a[112:115]
buffer_load_dwordx4 a[32:35], v248, s[20:23], s92 offen offset:1024 sc1
CV v169,v82,v83
CV v170,v84,v85
MM a[120:123],a[180:183],v[28:31],a[120:123]
TR a[164:165],v230 offset:53760
TR a[166:167],v230 offset:55808
CV v171,v86,v87
MM a[120:123],a[176:179],v[24:27],a[120:123]
buffer_load_dwordx4 a[64:67], v248, s[20:23], s92 offen offset:2048 sc1
MM a[124:127],a[184:187],v[24:27],a[124:127]
TR a[168:169],v230 offset:57856
TR a[170:171],v230 offset:59904
MM a[124:127],a[188:191],v[28:31],a[124:127]
MM a[116:119],a[188:191],v[20:23],a[116:119]
buffer_store_dwordx4 v[168:171], v248, s[20:23], s78 offen offset:0 sc1
TR a[172:173],v230 offset:61952
TR a[174:175],v230 offset:64000
AR v88,a24
AR v89,a25
AR v90,a26
AR v91,a27
AR v92,a28
MM a[116:119],a[184:187],v[16:19],a[116:119]
AR v93,a29
AR v94,a30
s_waitcnt lgkmcnt(15)
MM v[0:3],a[128:131],v[160:163],0
TR a[176:177],v231 offset:49664
TR a[178:179],v231 offset:51712
AR v95,a31
MM v[0:3],a[132:135],v[176:179],v[0:3]
v_bfe_u32 v249, v248, 6, 2
v_lshlrev_b32 v249, 8, v249
v_mul_f32 v88, v88, v157
v_mul_f32 v89, v89, v157
MM v[0:3],a[136:139],v[192:195],v[0:3]
TR a[180:181],v231 offset:53760
TR a[182:183],v231 offset:55808
v_mul_f32 v90, v90, v157
v_mul_f32 v91, v91, v157
MM v[0:3],a[140:143],v[208:211],v[0:3]
buffer_load_dwordx4 a[96:99], v248, s[20:23], s92 offen offset:3072 sc1
v_lshrrev_b32 v250, 8, v248
v_lshl_add_u32 v249, v250, 6, v249
v_mul_f32 v92, v92, v157
MM v[8:11],a[140:143],v[212:215],0
s_add_u32 s92, s92, 4096
TR a[184:185],v231 offset:57856
TR a[186:187],v231 offset:59904
v_mul_f32 v93, v93, v157
v_mul_f32 v94, v94, v157
MM v[8:11],a[136:139],v[196:199],v[8:11]
v_and_b32 v251, 48, v248
v_add_u32 v249, v249, v251
v_mul_f32 v95, v95, v157
CV v172,v88,v89
MM v[8:11],a[132:135],v[180:183],v[8:11]
s_lshr_b32 s45, s49, 3
buffer_load_dwordx4 a[4:7], v248, s[20:23], s92 offen offset:0 sc1
TR a[188:189],v231 offset:61952
TR a[190:191],v231 offset:64000
CV v173,v90,v91
MM v[8:11],a[128:131],v[164:167],v[8:11]
s_waitcnt lgkmcnt(15)
v_bfe_u32 v245, v248, 6, 2
v_xor_b32 v251, s45, v245
CV v174,v92,v93
CV v175,v94,v95
MM v[4:7],a[144:147],v[160:163],0
buffer_store_dwordx4 v[172:175], v248, s[20:23], s79 offen offset:0 sc1
TR a[128:129],v228 offset:0
TR a[130:131],v228 offset:1024
MM v[4:7],a[148:151],v[176:179],v[4:7]
v_add_u32 v251, -1, v251
v_and_b32 v251, 0x40000000, v251
AR v112,a48
MM v[4:7],a[152:155],v[192:195],v[4:7]
TR a[132:133],v228 offset:2048
TR a[134:135],v228 offset:3072
AR v113,a49
AR v114,a50
MM v[4:7],a[156:159],v[208:211],v[4:7]
v_lshlrev_b32 v245, 6, v245
v_lshl_add_u32 v245, s45, 4, v245
AR v115,a51
MM v[12:15],a[156:159],v[212:215],0
TR a[136:137],v228 offset:4096
TR a[138:139],v228 offset:5120
AR v116,a52
AR v117,a53
MM v[12:15],a[152:155],v[196:199],v[12:15]
v_mov_b32 v234, v249
v_add_u32 v249, v249, v251
AR v118,a54
AR v119,a55
MM v[12:15],a[148:151],v[180:183],v[12:15]
buffer_load_dwordx4 a[36:39], v248, s[20:23], s92 offen offset:1024 sc1
TR a[140:141],v228 offset:6144
TR a[142:143],v228 offset:7168
v_mul_f32 v112, v112, v153
MM v[12:15],a[144:147],v[164:167],v[12:15]
s_waitcnt lgkmcnt(15)
v_bfe_u32 v251, v248, 4, 2
v_lshl_add_u32 v245, v251, 2, v245
v_mul_f32 v113, v113, v153
v_mul_f32 v114, v114, v153
MM v[32:35],a[160:163],v[160:163],0
s_lshl_b32 s47, %[sb], 10
TR a[144:145],v229 offset:0
TR a[146:147],v229 offset:1024
v_mul_f32 v115, v115, v153
v_mul_f32 v116, v116, v153
MM v[32:35],a[164:167],v[176:179],v[32:35]
s_and_b32 s47, s47, 1024
s_add_u32 s46, s95, s47
s_sub_u32 s47, s95, s47
v_mul_f32 v117, v117, v153
MM v[32:35],a[168:171],v[192:195],v[32:35]
TR a[148:149],v229 offset:2048
TR a[150:151],v229 offset:3072
v_mul_f32 v118, v118, v153
v_mul_f32 v119, v119, v153
MM v[32:35],a[172:175],v[208:211],v[32:35]
s_bitcmp1_b32 s49, 4
s_cselect_b32 s44, -1, 1
s_mul_i32 s44, s44, 331776
buffer_load_dwordx4 a[68:71], v248, s[20:23], s92 offen offset:2048 sc1
CV v184,v112,v113
MM v[40:43],a[172:175],v[212:215],0
s_add_u32 s44, s44, s16
TR a[152:153],v229 offset:4096
TR a[154:155],v229 offset:5120
CV v185,v114,v115
CV v186,v116,v117
MM v[40:43],a[168:171],v[196:199],v[40:43]
s_xor_b32 s45, s68, 64
buffer_load_dwordx4 a[100:103], v248, s[20:23], s92 offen offset:3072 sc1
CV v187,v118,v119
MM v[40:43],a[164:167],v[180:183],v[40:43]
s_bfe_u32 s80, s49, 131074
TR a[156:157],v229 offset:6144
TR a[158:159],v229 offset:7168
v_xor_b32 v250, s80, v250
v_mul_u32_u24 v250, 82944, v250
v_and_b32 v252, 240, v248
v_add_u32 v250, v250, v252
v_lshrrev_b32 v251, 8, v248
v_mul_u32_u24 v251, 1280, v251
v_bfe_u32 v252, v248, 4, 3
v_lshl_add_u32 v251, v252, 3, v251
v_bfe_u32 v252, v248, 7, 1
v_mul_u32_u24 v254, 40960, v252
v_add_u32 v254, v254, v251
v_lshl_add_u32 v251, v252, 6, v251
v_lshrrev_b32 v253, 8, v248
MM v[40:43],a[160:163],v[164:167],v[40:43]
s_waitcnt lgkmcnt(15)
v_and_b32 v252, 240, v248
v_lshl_add_u32 v232, v253, 2, v252
s_waitcnt vmcnt(10)
s_barrier
MM v[36:39],a[176:179],v[160:163],0
TR a[160:161],v230 offset:16384
TR a[162:163],v230 offset:18432
s_nop 0
LPG21_%=:
s_cbranch_execz LPG25_%=
MM v[36:39],a[180:183],v[176:179],v[36:39]
buffer_store_dwordx4 v[184:187], v248, s[20:23], s78 offen offset:1024 sc1
AR v120,a56
AR v121,a57
AR v122,a58
AR v123,a59
AR v124,a60
v_xor_b32 v253, s80, v253
MM v[36:39],a[184:187],v[192:195],v[36:39]
TR a[164:165],v230 offset:20480
TR a[166:167],v230 offset:22528
AR v125,a61
AR v126,a62
MM v[36:39],a[188:191],v[208:211],v[36:39]
AR v127,a63
v_lshlrev_b32 v253, 4, v253
v_mul_f32 v120, v120, v157
MM v[44:47],a[188:191],v[212:215],0
TR a[168:169],v230 offset:24576
TR a[170:171],v230 offset:26624
v_mul_f32 v121, v121, v157
v_mov_b32 v233, s69
MM v[44:47],a[184:187],v[196:199],v[44:47]
s_add_u32 s80, s18, 131072
v_mul_f32 v122, v122, v157
v_mul_f32 v123, v123, v157
MM v[44:47],a[180:183],v[180:183],v[44:47]
buffer_load_dwordx2 v[224:225], v251, s[20:23], s80 offen offset:0 sc1
TR a[172:173],v230 offset:28672
TR a[174:175],v230 offset:30720
v_mul_f32 v124, v124, v157
MM v[44:47],a[176:179],v[164:167],v[44:47]
s_waitcnt lgkmcnt(15)
v_mul_f32 v125, v125, v157
v_mul_f32 v126, v126, v157
MM v[128:131],a[128:131],v[160:163],0
s_add_u32 s80, s18, 131200
TR a[176:177],v231 offset:16384
TR a[178:179],v231 offset:18432
v_mul_f32 v127, v127, v157
CV v188,v120,v121
MM v[128:131],a[132:135],v[176:179],v[128:131]
buffer_load_dwordx2 v[226:227], v251, s[20:23], s80 offen offset:0 sc1
CV v189,v122,v123
CV v190,v124,v125
MM v[128:131],a[136:139],v[192:195],v[128:131]
TR a[180:181],v231 offset:20480
TR a[182:183],v231 offset:22528
CV v191,v126,v127
MM v[128:131],a[140:143],v[208:211],v[128:131]
s_add_u32 s80, s18, 172032
buffer_load_dwordx2 v[236:237], v251, s[20:23], s80 offen offset:0 sc1
MM v[136:139],a[140:143],v[212:215],0
TR a[184:185],v231 offset:24576
TR a[186:187],v231 offset:26624
MM v[136:139],a[136:139],v[196:199],v[136:139]
s_add_u32 s80, s18, 172160
buffer_load_dwordx2 v[238:239], v251, s[20:23], s80 offen offset:0 sc1
MM v[136:139],a[132:135],v[180:183],v[136:139]
buffer_store_dwordx4 v[188:191], v248, s[20:23], s79 offen offset:1024 sc1
TR a[188:189],v231 offset:28672
TR a[190:191],v231 offset:30720
AR v80,a80
AR v81,a81
AR v82,a82
AR v83,a83
AR v84,a84
MM v[136:139],a[128:131],v[164:167],v[136:139]
s_waitcnt lgkmcnt(15)
AR v85,a85
AR v86,a86
MM v[132:135],a[144:147],v[160:163],0
s_add_u32 s80, s63, 131072
buffer_load_dwordx2 v[246:247], v254, s[20:23], s80 offen offset:0 sc1
TR a[128:129],v230 offset:16896
TR a[130:131],v230 offset:18944
AR v87,a87
MM v[132:135],a[148:151],v[176:179],v[132:135]
)ASM"
R"ASM(
v_mul_f32 v80, v80, v153
v_mul_f32 v81, v81, v153
MM v[132:135],a[152:155],v[192:195],v[132:135]
TR a[132:133],v230 offset:20992
TR a[134:135],v230 offset:23040
v_mul_f32 v82, v82, v153
MM v[132:135],a[156:159],v[208:211],v[132:135]
v_mul_f32 v83, v83, v153
v_mul_f32 v84, v84, v153
MM v[140:143],a[156:159],v[212:215],0
TR a[136:137],v230 offset:25088
TR a[138:139],v230 offset:27136
v_mul_f32 v85, v85, v153
MM v[140:143],a[152:155],v[196:199],v[140:143]
v_mul_f32 v86, v86, v153
v_mul_f32 v87, v87, v153
MM v[140:143],a[148:151],v[180:183],v[140:143]
TR a[140:141],v230 offset:29184
TR a[142:143],v230 offset:31232
CV v200,v80,v81
CV v201,v82,v83
MM v[140:143],a[144:147],v[164:167],v[140:143]
s_waitcnt lgkmcnt(15)
CV v202,v84,v85
CV v203,v86,v87
MM v[64:67],a[160:163],v[160:163],0
TR a[144:145],v231 offset:16896
TR a[146:147],v231 offset:18944
MM v[64:67],a[164:167],v[176:179],v[64:67]
MM v[64:67],a[168:171],v[192:195],v[64:67]
TR a[148:149],v231 offset:20992
TR a[150:151],v231 offset:23040
MM v[64:67],a[172:175],v[208:211],v[64:67]
buffer_store_dwordx4 v[200:203], v248, s[20:23], s78 offen offset:2048 sc1
AR v88,a88
AR v89,a89
AR v90,a90
AR v91,a91
MM v[72:75],a[172:175],v[212:215],0
TR a[152:153],v231 offset:25088
TR a[154:155],v231 offset:27136
AR v92,a92
MM v[72:75],a[168:171],v[196:199],v[72:75]
AR v93,a93
AR v94,a94
MM v[72:75],a[164:167],v[180:183],v[72:75]
TR a[156:157],v231 offset:29184
TR a[158:159],v231 offset:31232
AR v95,a95
v_mul_f32 v88, v88, v157
MM v[72:75],a[160:163],v[164:167],v[72:75]
s_waitcnt lgkmcnt(15)
v_mul_f32 v89, v89, v157
v_mul_f32 v90, v90, v157
MM v[68:71],a[176:179],v[160:163],0
TR a[160:161],v244 offset:49152
TR a[162:163],v244 offset:51200
v_mul_f32 v91, v91, v157
MM v[68:71],a[180:183],v[176:179],v[68:71]
v_mul_f32 v92, v92, v157
v_mul_f32 v93, v93, v157
MM v[68:71],a[184:187],v[192:195],v[68:71]
TR a[164:165],v244 offset:53248
TR a[166:167],v244 offset:55296
v_mul_f32 v94, v94, v157
MM v[68:71],a[188:191],v[208:211],v[68:71]
v_mul_f32 v95, v95, v157
CV v204,v88,v89
MM v[76:79],a[188:191],v[212:215],0
TR a[168:169],v244 offset:57344
TR a[170:171],v244 offset:59392
CV v205,v90,v91
CV v206,v92,v93
MM v[76:79],a[184:187],v[196:199],v[76:79]
CV v207,v94,v95
MM v[76:79],a[180:183],v[180:183],v[76:79]
TR a[172:173],v244 offset:61440
TR a[174:175],v244 offset:63488
MM v[76:79],a[176:179],v[164:167],v[76:79]
s_waitcnt lgkmcnt(15)
MM v[96:99],a[128:131],v[160:163],0
s_xor_b32 s92, s49, 2
buffer_store_dwordx4 v[204:207], v248, s[20:23], s79 offen offset:2048 sc1
TR a[176:177],v235 offset:49152
TR a[178:179],v235 offset:51200
AR v112,a112
AR v113,a113
AR v114,a114
MM v[96:99],a[132:135],v[176:179],v[96:99]
s_mul_i32 s92, s92, 17408
s_add_u32 s92, s83, s92
AR v115,a115
AR v116,a116
MM v[96:99],a[136:139],v[192:195],v[96:99]
TR a[180:181],v235 offset:53248
TR a[182:183],v235 offset:55296
AR v117,a117
MM v[96:99],a[140:143],v[208:211],v[96:99]
buffer_load_dwordx4 a[8:11], v248, s[20:23], s92 offen offset:0 sc1
AR v118,a118
AR v119,a119
MM v[104:107],a[140:143],v[212:215],0
TR a[184:185],v235 offset:57344
TR a[186:187],v235 offset:59392
v_mul_f32 v112, v112, v153
v_mul_f32 v113, v113, v153
MM v[104:107],a[136:139],v[196:199],v[104:107]
buffer_load_dwordx4 a[40:43], v248, s[20:23], s92 offen offset:1024 sc1
v_mul_f32 v114, v114, v153
MM v[104:107],a[132:135],v[180:183],v[104:107]
TR a[188:189],v235 offset:61440
TR a[190:191],v235 offset:63488
v_mul_f32 v115, v115, v153
v_mul_f32 v116, v116, v153
MM v[104:107],a[128:131],v[164:167],v[104:107]
s_waitcnt lgkmcnt(15)
buffer_load_dwordx4 a[72:75], v248, s[20:23], s92 offen offset:2048 sc1
v_mul_f32 v117, v117, v153
v_mul_f32 v118, v118, v153
MM v[100:103],a[144:147],v[160:163],0
TR a[128:129],v244 offset:49664
TR a[130:131],v244 offset:51712
v_mul_f32 v119, v119, v153
MM v[100:103],a[148:151],v[176:179],v[100:103]
CV v216,v112,v113
CV v217,v114,v115
MM v[100:103],a[152:155],v[192:195],v[100:103]
TR a[132:133],v244 offset:53760
TR a[134:135],v244 offset:55808
CV v218,v116,v117
MM v[100:103],a[156:159],v[208:211],v[100:103]
buffer_load_dwordx4 a[104:107], v248, s[20:23], s92 offen offset:3072 sc1
CV v219,v118,v119
MM v[108:111],a[156:159],v[212:215],0
s_add_u32 s92, s92, 4096
TR a[136:137],v244 offset:57856
TR a[138:139],v244 offset:59904
MM v[108:111],a[152:155],v[196:199],v[108:111]
buffer_load_dwordx4 a[12:15], v248, s[20:23], s92 offen offset:0 sc1
MM v[108:111],a[148:151],v[180:183],v[108:111]
TR a[140:141],v244 offset:61952
TR a[142:143],v244 offset:64000
MM v[108:111],a[144:147],v[164:167],v[108:111]
s_waitcnt lgkmcnt(15)
s_waitcnt vmcnt(19)
MM v[0:3],a[160:163],a[0:3],v[0:3]
buffer_store_dwordx4 v[216:219], v248, s[20:23], s78 offen offset:3072 sc1
TR a[144:145],v235 offset:49664
TR a[146:147],v235 offset:51712
AR v120,a120
AR v121,a121
AR v122,a122
AR v123,a123
AR v124,a124
AR v125,a125
AR v126,a126
MM v[0:3],a[164:167],a[32:35],v[0:3]
AR v127,a127
v_mul_f32 v120, v120, v157
MM v[0:3],a[168:171],a[64:67],v[0:3]
TR a[148:149],v235 offset:53760
TR a[150:151],v235 offset:55808
v_mul_f32 v121, v121, v157
v_mul_f32 v122, v122, v157
MM v[0:3],a[172:175],a[96:99],v[0:3]
s_waitcnt vmcnt(15)
buffer_load_dwordx4 a[44:47], v248, s[20:23], s92 offen offset:1024 sc1
v_mul_f32 v123, v123, v157
MM v[8:11],a[172:175],a[100:103],v[8:11]
TR a[152:153],v235 offset:57856
TR a[154:155],v235 offset:59904
v_mul_f32 v124, v124, v157
v_mul_f32 v125, v125, v157
MM v[8:11],a[168:171],a[68:71],v[8:11]
v_mul_f32 v126, v126, v157
v_mul_f32 v127, v127, v157
MM v[8:11],a[164:167],a[36:39],v[8:11]
TR a[156:157],v235 offset:61952
TR a[158:159],v235 offset:64000
CV v220,v120,v121
MM v[8:11],a[160:163],a[4:7],v[8:11]
s_waitcnt lgkmcnt(15)
buffer_load_dwordx4 a[76:79], v248, s[20:23], s92 offen offset:2048 sc1
CV v221,v122,v123
CV v222,v124,v125
MM v[4:7],a[176:179],a[0:3],v[4:7]
TR a[160:161],v228 offset:8192
TR a[162:163],v228 offset:9216
CV v223,v126,v127
MM v[4:7],a[180:183],a[32:35],v[4:7]
buffer_load_dwordx4 a[108:111], v248, s[20:23], s92 offen offset:3072 sc1
MM v[4:7],a[184:187],a[64:67],v[4:7]
TR a[164:165],v228 offset:10240
TR a[166:167],v228 offset:11264
MM v[4:7],a[188:191],a[96:99],v[4:7]
MM v[12:15],a[188:191],a[100:103],v[12:15]
buffer_store_dwordx4 v[220:223], v248, s[20:23], s79 offen offset:3072 sc1
TR a[168:169],v228 offset:12288
TR a[170:171],v228 offset:13312
MM v[12:15],a[184:187],a[68:71],v[12:15]
MM v[12:15],a[180:183],a[36:39],v[12:15]
TR a[172:173],v228 offset:14336
TR a[174:175],v228 offset:15360
MM v[12:15],a[176:179],a[4:7],v[12:15]
s_waitcnt lgkmcnt(15)
MM v[32:35],a[128:131],a[0:3],v[32:35]
TR a[176:177],v229 offset:8192
TR a[178:179],v229 offset:9216
MM v[32:35],a[132:135],a[32:35],v[32:35]
MM v[32:35],a[136:139],a[64:67],v[32:35]
TR a[180:181],v229 offset:10240
TR a[182:183],v229 offset:11264
MM v[32:35],a[140:143],a[96:99],v[32:35]
MM v[40:43],a[140:143],a[100:103],v[40:43]
TR a[184:185],v229 offset:12288
TR a[186:187],v229 offset:13312
MM v[40:43],a[136:139],a[68:71],v[40:43]
MM v[40:43],a[132:135],a[36:39],v[40:43]
TR a[188:189],v229 offset:14336
TR a[190:191],v229 offset:15360
MM v[40:43],a[128:131],a[4:7],v[40:43]
s_waitcnt lgkmcnt(15)
MM v[36:39],a[144:147],a[0:3],v[36:39]
s_waitcnt vmcnt(12)
TR a[128:129],v230 offset:0
TR a[130:131],v230 offset:2048
MM v[36:39],a[148:151],a[32:35],v[36:39]
v_lshlrev_b32 v112, 16, v224
v_and_b32 v113, 0xffff0000, v224
MM v[36:39],a[152:155],a[64:67],v[36:39]
TR a[132:133],v230 offset:4096
TR a[134:135],v230 offset:6144
MM v[36:39],a[156:159],a[96:99],v[36:39]
v_lshlrev_b32 v114, 16, v225
v_and_b32 v115, 0xffff0000, v225
MM v[44:47],a[156:159],a[100:103],v[44:47]
TR a[136:137],v230 offset:8192
TR a[138:139],v230 offset:10240
MM v[44:47],a[152:155],a[68:71],v[44:47]
v_mul_f32 v116, 0xbfb8aa3b, v112
v_mul_f32 v117, 0xbfb8aa3b, v113
MM v[44:47],a[148:151],a[36:39],v[44:47]
TR a[140:141],v230 offset:12288
TR a[142:143],v230 offset:14336
MM v[44:47],a[144:147],a[4:7],v[44:47]
s_waitcnt lgkmcnt(15)
v_mul_f32 v118, 0xbfb8aa3b, v114
v_mul_f32 v119, 0xbfb8aa3b, v115
MM v[128:131],a[160:163],a[0:3],v[128:131]
TR a[144:145],v231 offset:0
TR a[146:147],v231 offset:2048
MM v[128:131],a[164:167],a[32:35],v[128:131]
EX v116,v116
MM v[128:131],a[168:171],a[64:67],v[128:131]
TR a[148:149],v231 offset:4096
TR a[150:151],v231 offset:6144
MM v[128:131],a[172:175],a[96:99],v[128:131]
EX v117,v117
MM v[136:139],a[172:175],a[100:103],v[136:139]
TR a[152:153],v231 offset:8192
TR a[154:155],v231 offset:10240
MM v[136:139],a[168:171],a[68:71],v[136:139]
EX v118,v118
MM v[136:139],a[164:167],a[36:39],v[136:139]
TR a[156:157],v231 offset:12288
TR a[158:159],v231 offset:14336
MM v[136:139],a[160:163],a[4:7],v[136:139]
s_waitcnt lgkmcnt(15)
EX v119,v119
MM v[132:135],a[176:179],a[0:3],v[132:135]
s_xor_b32 s92, s49, 3
TR a[160:161],v230 offset:512
TR a[162:163],v230 offset:2560
MM v[132:135],a[180:183],a[32:35],v[132:135]
s_mul_i32 s92, s92, 17408
s_add_u32 s92, s83, s92
v_add_f32 v116, 1.0, v116
MM v[132:135],a[184:187],a[64:67],v[132:135]
TR a[164:165],v230 offset:4608
TR a[166:167],v230 offset:6656
MM v[132:135],a[188:191],a[96:99],v[132:135]
buffer_load_dwordx4 a[192:195], v248, s[20:23], s92 offen offset:0 sc1
MM v[140:143],a[188:191],a[100:103],v[140:143]
TR a[168:169],v230 offset:8704
TR a[170:171],v230 offset:10752
MM v[140:143],a[184:187],a[68:71],v[140:143]
buffer_load_dwordx4 a[208:211], v248, s[20:23], s92 offen offset:1024 sc1
MM v[140:143],a[180:183],a[36:39],v[140:143]
TR a[172:173],v230 offset:12800
TR a[174:175],v230 offset:14848
MM v[140:143],a[176:179],a[4:7],v[140:143]
s_waitcnt lgkmcnt(15)
buffer_load_dwordx4 a[224:227], v248, s[20:23], s92 offen offset:2048 sc1
s_waitcnt vmcnt(9)
MM v[0:3],a[128:131],a[8:11],v[0:3]
TR a[176:177],v231 offset:512
TR a[178:179],v231 offset:2560
MM v[0:3],a[132:135],a[40:43],v[0:3]
v_add_f32 v117, 1.0, v117
v_add_f32 v118, 1.0, v118
MM v[0:3],a[136:139],a[72:75],v[0:3]
TR a[180:181],v231 offset:4608
TR a[182:183],v231 offset:6656
MM v[0:3],a[140:143],a[104:107],v[0:3]
s_waitcnt vmcnt(4)
buffer_load_dwordx4 a[240:243], v248, s[20:23], s92 offen offset:3072 sc1
MM v[8:11],a[140:143],a[108:111],v[8:11]
s_add_u32 s92, s92, 4096
TR a[184:185],v231 offset:8704
TR a[186:187],v231 offset:10752
MM v[8:11],a[136:139],a[76:79],v[8:11]
buffer_load_dwordx4 a[196:199], v248, s[20:23], s92 offen offset:0 sc1
MM v[8:11],a[132:135],a[44:47],v[8:11]
TR a[188:189],v231 offset:12800
TR a[190:191],v231 offset:14848
MM v[8:11],a[128:131],a[12:15],v[8:11]
s_waitcnt lgkmcnt(15)
v_add_f32 v119, 1.0, v119
MM v[4:7],a[144:147],a[8:11],v[4:7]
TR a[128:129],v228 offset:16384
TR a[130:131],v228 offset:17408
MM v[4:7],a[148:151],a[40:43],v[4:7]
buffer_load_dwordx4 a[212:215], v248, s[20:23], s92 offen offset:1024 sc1
MM v[4:7],a[152:155],a[72:75],v[4:7]
TR a[132:133],v228 offset:18432
TR a[134:135],v228 offset:19456
MM v[4:7],a[156:159],a[104:107],v[4:7]
buffer_load_dwordx4 a[228:231], v248, s[20:23], s92 offen offset:2048 sc1
MM v[12:15],a[156:159],a[108:111],v[12:15]
TR a[136:137],v228 offset:20480
TR a[138:139],v228 offset:21504
MM v[12:15],a[152:155],a[76:79],v[12:15]
v_rcp_f32 v116, v116
MM v[12:15],a[148:151],a[44:47],v[12:15]
TR a[140:141],v228 offset:22528
TR a[142:143],v228 offset:23552
MM v[12:15],a[144:147],a[12:15],v[12:15]
s_waitcnt lgkmcnt(15)
buffer_load_dwordx4 a[244:247], v248, s[20:23], s92 offen offset:3072 sc1
MM v[32:35],a[160:163],a[8:11],v[32:35]
TR a[144:145],v229 offset:16384
TR a[146:147],v229 offset:17408
MM v[32:35],a[164:167],a[40:43],v[32:35]
s_add_u32 m0, s89, 32768
v_rcp_f32 v117, v117
MM v[32:35],a[168:171],a[72:75],v[32:35]
s_add_u32 s66, s87, 128
TR a[148:149],v229 offset:18432
TR a[150:151],v229 offset:19456
MM v[32:35],a[172:175],a[104:107],v[32:35]
buffer_load_dwordx4 v240, s[24:27], s66 offen offset:0 lds
MM v[40:43],a[172:175],a[108:111],v[40:43]
s_add_u32 m0, s89, 33792
TR a[152:153],v229 offset:20480
TR a[154:155],v229 offset:21504
MM v[40:43],a[168:171],a[76:79],v[40:43]
s_add_u32 s66, s87, 10368
buffer_load_dwordx4 v240, s[24:27], s66 offen offset:0 lds
)ASM"
R"ASM(
MM v[40:43],a[164:167],a[44:47],v[40:43]
s_add_u32 m0, s89, 40960
TR a[156:157],v229 offset:22528
TR a[158:159],v229 offset:23552
MM v[40:43],a[160:163],a[12:15],v[40:43]
s_waitcnt lgkmcnt(15)
v_rcp_f32 v118, v118
MM v[36:39],a[176:179],a[8:11],v[36:39]
s_add_u32 s66, s87, 82048
TR a[160:161],v244 offset:0
TR a[162:163],v244 offset:2048
MM v[36:39],a[180:183],a[40:43],v[36:39]
buffer_load_dwordx4 v240, s[24:27], s66 offen offset:0 lds
MM v[36:39],a[184:187],a[72:75],v[36:39]
s_add_u32 m0, s89, 41984
TR a[164:165],v244 offset:4096
TR a[166:167],v244 offset:6144
MM v[36:39],a[188:191],a[104:107],v[36:39]
s_add_u32 s66, s87, 92288
buffer_load_dwordx4 v240, s[24:27], s66 offen offset:0 lds
MM v[44:47],a[188:191],a[108:111],v[44:47]
s_add_u32 m0, s89, 98304
TR a[168:169],v244 offset:8192
TR a[170:171],v244 offset:10240
MM v[44:47],a[184:187],a[76:79],v[44:47]
s_add_u32 s66, s88, 128
buffer_load_dwordx4 v240, s[24:27], s66 offen offset:0 lds
MM v[44:47],a[180:183],a[44:47],v[44:47]
s_add_u32 m0, s89, 99328
TR a[172:173],v244 offset:12288
TR a[174:175],v244 offset:14336
MM v[44:47],a[176:179],a[12:15],v[44:47]
s_waitcnt lgkmcnt(15)
v_rcp_f32 v119, v119
MM v[128:131],a[128:131],a[8:11],v[128:131]
s_add_u32 s66, s88, 10368
TR a[176:177],v235 offset:0
TR a[178:179],v235 offset:2048
MM v[128:131],a[132:135],a[40:43],v[128:131]
buffer_load_dwordx4 v240, s[24:27], s66 offen offset:0 lds
MM v[128:131],a[136:139],a[72:75],v[128:131]
s_add_u32 m0, s89, 106496
TR a[180:181],v235 offset:4096
TR a[182:183],v235 offset:6144
MM v[128:131],a[140:143],a[104:107],v[128:131]
s_add_u32 s66, s88, 82048
buffer_load_dwordx4 v240, s[24:27], s66 offen offset:0 lds
MM v[136:139],a[140:143],a[108:111],v[136:139]
s_add_u32 m0, s89, 107520
TR a[184:185],v235 offset:8192
TR a[186:187],v235 offset:10240
MM v[136:139],a[136:139],a[76:79],v[136:139]
s_add_u32 s66, s88, 92288
v_mul_f32 v116, v112, v116
v_mul_f32 v117, v113, v117
MM v[136:139],a[132:135],a[44:47],v[136:139]
TR a[188:189],v235 offset:12288
TR a[190:191],v235 offset:14336
MM v[136:139],a[128:131],a[12:15],v[136:139]
s_waitcnt lgkmcnt(15)
buffer_load_dwordx4 v240, s[24:27], s66 offen offset:0 lds
MM v[132:135],a[144:147],a[8:11],v[132:135]
s_lshl_b32 s97, s84, 2
TR a[128:129],v244 offset:512
TR a[130:131],v244 offset:2560
MM v[132:135],a[148:151],a[40:43],v[132:135]
s_add_u32 s97, s97, s96
s_add_u32 s97, s97, 16
s_waitcnt vmcnt(16)
MM v[132:135],a[152:155],a[72:75],v[132:135]
TR a[132:133],v244 offset:4608
TR a[134:135],v244 offset:6656
MM v[132:135],a[156:159],a[104:107],v[132:135]
v_mov_b32 v242, s69
s_mov_b64 exec, 1
buffer_store_dword v242, off, s[20:23], s97 offset:0 sc1
s_mov_b64 exec, -1
MM v[140:143],a[156:159],a[108:111],v[140:143]
TR a[136:137],v244 offset:8704
TR a[138:139],v244 offset:10752
MM v[140:143],a[152:155],a[76:79],v[140:143]
v_mul_f32 v118, v114, v118
v_mul_f32 v119, v115, v119
MM v[140:143],a[148:151],a[44:47],v[140:143]
TR a[140:141],v244 offset:12800
TR a[142:143],v244 offset:14848
MM v[140:143],a[144:147],a[12:15],v[140:143]
s_waitcnt lgkmcnt(15)
s_waitcnt vmcnt(13)
MM v[0:3],a[160:163],a[192:195],v[0:3]
TR a[144:145],v235 offset:512
TR a[146:147],v235 offset:2560
MM v[0:3],a[164:167],a[208:211],v[0:3]
MM v[0:3],a[168:171],a[224:227],v[0:3]
TR a[148:149],v235 offset:4608
TR a[150:151],v235 offset:6656
MM v[0:3],a[172:175],a[240:243],v[0:3]
s_waitcnt vmcnt(9)
MM v[8:11],a[172:175],a[244:247],v[8:11]
TR a[152:153],v235 offset:8704
TR a[154:155],v235 offset:10752
MM v[8:11],a[168:171],a[228:231],v[8:11]
MM v[8:11],a[164:167],a[212:215],v[8:11]
TR a[156:157],v235 offset:12800
TR a[158:159],v235 offset:14848
MM v[8:11],a[160:163],a[196:199],v[8:11]
s_waitcnt lgkmcnt(15)
MM v[4:7],a[176:179],a[192:195],v[4:7]
TR a[160:161],v228 offset:24576
TR a[162:163],v228 offset:25600
MM v[4:7],a[180:183],a[208:211],v[4:7]
MM v[4:7],a[184:187],a[224:227],v[4:7]
TR a[164:165],v228 offset:26624
TR a[166:167],v228 offset:27648
MM v[4:7],a[188:191],a[240:243],v[4:7]
MM v[12:15],a[188:191],a[244:247],v[12:15]
TR a[168:169],v228 offset:28672
TR a[170:171],v228 offset:29696
MM v[12:15],a[184:187],a[228:231],v[12:15]
MM v[12:15],a[180:183],a[212:215],v[12:15]
TR a[172:173],v228 offset:30720
TR a[174:175],v228 offset:31744
MM v[12:15],a[176:179],a[196:199],v[12:15]
s_waitcnt lgkmcnt(15)
MM v[32:35],a[128:131],a[192:195],v[32:35]
s_nop 0
LPG22_%=:
s_cbranch_execz LTR2_%=
TR a[176:177],v229 offset:24576
TR a[178:179],v229 offset:25600
MM v[32:35],a[132:135],a[208:211],v[32:35]
MM v[32:35],a[136:139],a[224:227],v[32:35]
TR a[180:181],v229 offset:26624
TR a[182:183],v229 offset:27648
MM v[32:35],a[140:143],a[240:243],v[32:35]
s_add_u32 s67, s46, 0
CV v0,v0,v1
CV v1,v2,v3
MM v[40:43],a[140:143],a[244:247],v[40:43]
TR a[184:185],v229 offset:28672
TR a[186:187],v229 offset:29696
MM v[40:43],a[136:139],a[228:231],v[40:43]
CV v2,v4,v5
CV v3,v6,v7
MM v[40:43],a[132:135],a[212:215],v[40:43]
TR a[188:189],v229 offset:30720
TR a[190:191],v229 offset:31744
MM v[40:43],a[128:131],a[196:199],v[40:43]
s_waitcnt lgkmcnt(15)
buffer_store_dwordx4 v[0:3], v249, s[20:23], s67 offen offset:0 sc1
MM v[36:39],a[144:147],a[192:195],v[36:39]
s_add_u32 s67, s47, 0
TR a[128:129],v244 offset:16384
TR a[130:131],v244 offset:18432
MM v[36:39],a[148:151],a[208:211],v[36:39]
CV v8,v8,v9
CV v9,v10,v11
MM v[36:39],a[152:155],a[224:227],v[36:39]
TR a[132:133],v244 offset:20480
TR a[134:135],v244 offset:22528
MM v[36:39],a[156:159],a[240:243],v[36:39]
CV v10,v12,v13
CV v11,v14,v15
MM v[44:47],a[156:159],a[244:247],v[44:47]
TR a[136:137],v244 offset:24576
TR a[138:139],v244 offset:26624
MM v[44:47],a[152:155],a[228:231],v[44:47]
buffer_store_dwordx4 v[8:11], v234, s[20:23], s67 offen offset:1024 sc1
MM v[44:47],a[148:151],a[212:215],v[44:47]
TR a[140:141],v244 offset:28672
TR a[142:143],v244 offset:30720
MM v[44:47],a[144:147],a[196:199],v[44:47]
s_waitcnt lgkmcnt(15)
ds_bpermute_b32 v160, v245, v0
ds_bpermute_b32 v161, v245, v1
MM v[128:131],a[160:163],a[192:195],v[128:131]
TR a[144:145],v235 offset:16384
TR a[146:147],v235 offset:18432
MM v[128:131],a[164:167],a[208:211],v[128:131]
ds_bpermute_b32 v162, v245, v2
ds_bpermute_b32 v163, v245, v3
MM v[128:131],a[168:171],a[224:227],v[128:131]
TR a[148:149],v235 offset:20480
TR a[150:151],v235 offset:22528
MM v[128:131],a[172:175],a[240:243],v[128:131]
CV v0,v116,v117
CV v1,v118,v119
MM v[136:139],a[172:175],a[244:247],v[136:139]
s_add_u32 s67, s46, 0
TR a[152:153],v235 offset:24576
TR a[154:155],v235 offset:26624
MM v[136:139],a[168:171],a[228:231],v[136:139]
CV v32,v32,v33
CV v33,v34,v35
MM v[136:139],a[164:167],a[212:215],v[136:139]
TR a[156:157],v235 offset:28672
TR a[158:159],v235 offset:30720
MM v[136:139],a[160:163],a[196:199],v[136:139]
s_waitcnt lgkmcnt(15)
CV v34,v36,v37
CV v35,v38,v39
s_waitcnt vmcnt(3)
s_barrier
MM v[132:135],a[176:179],a[192:195],v[132:135]
TR a[160:161],v230 offset:32768
TR a[162:163],v230 offset:34816
MM v[132:135],a[180:183],a[208:211],v[132:135]
buffer_store_dwordx4 v[32:35], v249, s[20:23], s67 offen offset:2048 sc1
MM v[132:135],a[184:187],a[224:227],v[132:135]
s_add_u32 s67, s47, 0
TR a[164:165],v230 offset:36864
TR a[166:167],v230 offset:38912
v_lshrrev_b32 v243, 2, v248
MM v[132:135],a[188:191],a[240:243],v[132:135]
CV v40,v40,v41
CV v41,v42,v43
MM v[140:143],a[188:191],a[244:247],v[140:143]
TR a[168:169],v230 offset:40960
TR a[170:171],v230 offset:43008
MM v[140:143],a[184:187],a[228:231],v[140:143]
CV v42,v44,v45
CV v43,v46,v47
v_min_u32 v243, 12, v243
MM v[140:143],a[180:183],a[212:215],v[140:143]
TR a[172:173],v230 offset:45056
TR a[174:175],v230 offset:47104
MM v[140:143],a[176:179],a[196:199],v[140:143]
s_waitcnt lgkmcnt(15)
buffer_store_dwordx4 v[40:43], v234, s[20:23], s67 offen offset:3072 sc1
MM v[64:67],a[128:131],a[0:3],v[64:67]
buffer_load_dword v242, v243, s[20:23], s96 offen offset:16 sc1
TR a[176:177],v231 offset:32768
TR a[178:179],v231 offset:34816
MM v[64:67],a[132:135],a[32:35],v[64:67]
ds_bpermute_b32 v176, v245, v32
ds_bpermute_b32 v177, v245, v33
MM v[64:67],a[136:139],a[64:67],v[64:67]
TR a[180:181],v231 offset:36864
TR a[182:183],v231 offset:38912
MM v[64:67],a[140:143],a[96:99],v[64:67]
ds_bpermute_b32 v178, v245, v34
ds_bpermute_b32 v179, v245, v35
MM v[72:75],a[140:143],a[100:103],v[72:75]
TR a[184:185],v231 offset:40960
TR a[186:187],v231 offset:43008
MM v[72:75],a[136:139],a[68:71],v[72:75]
v_lshlrev_b32 v112, 16, v226
v_and_b32 v113, 0xffff0000, v226
MM v[72:75],a[132:135],a[36:39],v[72:75]
s_add_u32 s67, s46, 8192
TR a[188:189],v231 offset:45056
TR a[190:191],v231 offset:47104
MM v[72:75],a[128:131],a[4:7],v[72:75]
s_waitcnt lgkmcnt(15)
CV v128,v128,v129
CV v129,v130,v131
MM v[68:71],a[144:147],a[0:3],v[68:71]
TR a[128:129],v244 offset:32768
TR a[130:131],v244 offset:34816
MM v[68:71],a[148:151],a[32:35],v[68:71]
CV v130,v132,v133
CV v131,v134,v135
MM v[68:71],a[152:155],a[64:67],v[68:71]
TR a[132:133],v244 offset:36864
TR a[134:135],v244 offset:38912
MM v[68:71],a[156:159],a[96:99],v[68:71]
buffer_store_dwordx4 v[128:131], v249, s[20:23], s67 offen offset:0 sc1
MM v[76:79],a[156:159],a[100:103],v[76:79]
s_add_u32 s67, s47, 8192
TR a[136:137],v244 offset:40960
TR a[138:139],v244 offset:43008
MM v[76:79],a[152:155],a[68:71],v[76:79]
CV v136,v136,v137
CV v137,v138,v139
MM v[76:79],a[148:151],a[36:39],v[76:79]
TR a[140:141],v244 offset:45056
TR a[142:143],v244 offset:47104
MM v[76:79],a[144:147],a[4:7],v[76:79]
s_waitcnt lgkmcnt(15)
CV v138,v140,v141
CV v139,v142,v143
MM v[64:67],a[160:163],a[8:11],v[64:67]
TR a[144:145],v235 offset:32768
TR a[146:147],v235 offset:34816
MM v[64:67],a[164:167],a[40:43],v[64:67]
buffer_store_dwordx4 v[136:139], v234, s[20:23], s67 offen offset:1024 sc1
MM v[64:67],a[168:171],a[72:75],v[64:67]
TR a[148:149],v235 offset:36864
TR a[150:151],v235 offset:38912
MM v[64:67],a[172:175],a[104:107],v[64:67]
ds_bpermute_b32 v164, v245, v128
ds_bpermute_b32 v165, v245, v129
MM v[72:75],a[172:175],a[108:111],v[72:75]
TR a[152:153],v235 offset:40960
TR a[154:155],v235 offset:43008
MM v[72:75],a[168:171],a[76:79],v[72:75]
ds_bpermute_b32 v166, v245, v130
ds_bpermute_b32 v167, v245, v131
MM v[72:75],a[164:167],a[44:47],v[72:75]
TR a[156:157],v235 offset:45056
TR a[158:159],v235 offset:47104
MM v[72:75],a[160:163],a[12:15],v[72:75]
s_waitcnt lgkmcnt(15)
v_lshlrev_b32 v114, 16, v227
v_and_b32 v115, 0xffff0000, v227
MM v[68:71],a[176:179],a[8:11],v[68:71]
TR a[160:161],v244 offset:16896
TR a[162:163],v244 offset:18944
MM v[68:71],a[180:183],a[40:43],v[68:71]
v_mul_f32 v116, 0xbfb8aa3b, v112
v_mul_f32 v117, 0xbfb8aa3b, v113
MM v[68:71],a[184:187],a[72:75],v[68:71]
TR a[164:165],v244 offset:20992
TR a[166:167],v244 offset:23040
MM v[68:71],a[188:191],a[104:107],v[68:71]
v_mul_f32 v118, 0xbfb8aa3b, v114
v_mul_f32 v119, 0xbfb8aa3b, v115
MM v[76:79],a[188:191],a[108:111],v[76:79]
TR a[168:169],v244 offset:25088
TR a[170:171],v244 offset:27136
MM v[76:79],a[184:187],a[76:79],v[76:79]
EX v116,v116
MM v[76:79],a[180:183],a[44:47],v[76:79]
TR a[172:173],v244 offset:29184
TR a[174:175],v244 offset:31232
MM v[76:79],a[176:179],a[12:15],v[76:79]
s_waitcnt lgkmcnt(15)
EX v117,v117
MM v[64:67],a[128:131],a[192:195],v[64:67]
TR a[176:177],v235 offset:16896
TR a[178:179],v235 offset:18944
MM v[64:67],a[132:135],a[208:211],v[64:67]
EX v118,v118
MM v[64:67],a[136:139],a[224:227],v[64:67]
TR a[180:181],v235 offset:20992
TR a[182:183],v235 offset:23040
MM v[64:67],a[140:143],a[240:243],v[64:67]
EX v119,v119
MM v[72:75],a[140:143],a[244:247],v[72:75]
TR a[184:185],v235 offset:25088
TR a[186:187],v235 offset:27136
MM v[72:75],a[136:139],a[228:231],v[72:75]
v_add_f32 v116, 1.0, v116
v_add_f32 v117, 1.0, v117
MM v[72:75],a[132:135],a[212:215],v[72:75]
TR a[188:189],v235 offset:29184
TR a[190:191],v235 offset:31232
MM v[72:75],a[128:131],a[196:199],v[72:75]
s_waitcnt lgkmcnt(15)
v_add_f32 v118, 1.0, v118
v_add_f32 v119, 1.0, v119
MM v[68:71],a[144:147],a[192:195],v[68:71]
TR a[128:129],v230 offset:33280
TR a[130:131],v230 offset:35328
MM v[68:71],a[148:151],a[208:211],v[68:71]
v_rcp_f32 v116, v116
)ASM"
R"ASM(
MM v[68:71],a[152:155],a[224:227],v[68:71]
TR a[132:133],v230 offset:37376
TR a[134:135],v230 offset:39424
MM v[68:71],a[156:159],a[240:243],v[68:71]
v_rcp_f32 v117, v117
MM v[76:79],a[156:159],a[244:247],v[76:79]
TR a[136:137],v230 offset:41472
TR a[138:139],v230 offset:43520
MM v[76:79],a[152:155],a[228:231],v[76:79]
v_rcp_f32 v118, v118
MM v[76:79],a[148:151],a[212:215],v[76:79]
TR a[140:141],v230 offset:45568
TR a[142:143],v230 offset:47616
MM v[76:79],a[144:147],a[196:199],v[76:79]
s_waitcnt lgkmcnt(15)
v_rcp_f32 v119, v119
MM v[96:99],a[160:163],a[0:3],v[96:99]
s_waitcnt vmcnt(2)
TR a[144:145],v231 offset:33280
TR a[146:147],v231 offset:35328
MM v[96:99],a[164:167],a[32:35],v[96:99]
v_cmp_ne_u32 vcc, s69, v242
s_cbranch_vccnz LRPO1_%=
LRPO1b_%=:
MM v[96:99],a[168:171],a[64:67],v[96:99]
s_xor_b32 s92, s49, 1
TR a[148:149],v231 offset:37376
TR a[150:151],v231 offset:39424
MM v[96:99],a[172:175],a[96:99],v[96:99]
s_add_u32 s67, s46, 4096
CV v64,v64,v65
CV v65,v66,v67
MM v[104:107],a[172:175],a[100:103],v[104:107]
s_mul_i32 s92, s92, 17408
TR a[152:153],v231 offset:41472
TR a[154:155],v231 offset:43520
MM v[104:107],a[168:171],a[68:71],v[104:107]
s_add_u32 s92, s83, s92
CV v66,v68,v69
CV v67,v70,v71
MM v[104:107],a[164:167],a[36:39],v[104:107]
s_add_u32 s92, s92, 8192
TR a[156:157],v231 offset:45568
TR a[158:159],v231 offset:47616
MM v[104:107],a[160:163],a[4:7],v[104:107]
s_waitcnt lgkmcnt(15)
buffer_store_dwordx4 v[64:67], v249, s[20:23], s67 offen offset:0 sc1
MM v[100:103],a[176:179],a[0:3],v[100:103]
s_add_u32 s67, s47, 4096
TR a[160:161],v244 offset:33280
TR a[162:163],v244 offset:35328
MM v[100:103],a[180:183],a[32:35],v[100:103]
s_cmp_eq_u32 s82, 0
s_cbranch_scc1 LY0_%=
v_lshrrev_b32 v135, 4, v248
v_min_u32 v135, 59, v135
v_mul_u32_u24 v132, 205, v135
v_lshrrev_b32 v132, 11, v132
v_mul_u32_u24 v132, 30, v132
v_add_lshl_u32 v135, v135, v132, 2
s_mul_i32 s78, s84, 40
s_add_u32 s78, s78, 73728
buffer_load_dword v132, v135, s[20:23], s78 offen offset:0 sc1
buffer_load_dword v133, v135, s[20:23], s78 offen offset:960 sc1
buffer_load_dword v134, v135, s[20:23], s78 offen offset:1920 sc1
LY0_%=:
buffer_load_dwordx4 a[16:19], v248, s[20:23], s92 offen offset:0 sc1
CV v72,v72,v73
CV v73,v74,v75
MM v[100:103],a[184:187],a[64:67],v[100:103]
TR a[164:165],v244 offset:37376
TR a[166:167],v244 offset:39424
MM v[100:103],a[188:191],a[96:99],v[100:103]
CV v74,v76,v77
CV v75,v78,v79
MM v[108:111],a[188:191],a[100:103],v[108:111]
TR a[168:169],v244 offset:41472
TR a[170:171],v244 offset:43520
MM v[108:111],a[184:187],a[68:71],v[108:111]
buffer_store_dwordx4 v[72:75], v234, s[20:23], s67 offen offset:1024 sc1
MM v[108:111],a[180:183],a[36:39],v[108:111]
buffer_load_dwordx4 a[48:51], v248, s[20:23], s92 offen offset:1024 sc1
TR a[172:173],v244 offset:45568
TR a[174:175],v244 offset:47616
MM v[108:111],a[176:179],a[4:7],v[108:111]
s_waitcnt lgkmcnt(15)
ds_bpermute_b32 v192, v245, v64
ds_bpermute_b32 v193, v245, v65
MM v[96:99],a[128:131],a[8:11],v[96:99]
TR a[176:177],v235 offset:33280
TR a[178:179],v235 offset:35328
MM v[96:99],a[132:135],a[40:43],v[96:99]
ds_bpermute_b32 v194, v245, v66
ds_bpermute_b32 v195, v245, v67
MM v[96:99],a[136:139],a[72:75],v[96:99]
TR a[180:181],v235 offset:37376
TR a[182:183],v235 offset:39424
MM v[96:99],a[140:143],a[104:107],v[96:99]
buffer_load_dwordx4 a[80:83], v248, s[20:23], s92 offen offset:2048 sc1
MM v[104:107],a[140:143],a[108:111],v[104:107]
TR a[184:185],v235 offset:41472
TR a[186:187],v235 offset:43520
MM v[104:107],a[136:139],a[76:79],v[104:107]
buffer_load_dwordx4 a[112:115], v248, s[20:23], s92 offen offset:3072 sc1
MM v[104:107],a[132:135],a[44:47],v[104:107]
s_add_u32 s92, s92, 4096
TR a[188:189],v235 offset:45568
TR a[190:191],v235 offset:47616
MM v[104:107],a[128:131],a[12:15],v[104:107]
s_waitcnt lgkmcnt(15)
buffer_load_dwordx4 a[20:23], v248, s[20:23], s92 offen offset:0 sc1
MM v[100:103],a[144:147],a[8:11],v[100:103]
TR a[128:129],v230 offset:49152
TR a[130:131],v230 offset:51200
MM v[100:103],a[148:151],a[40:43],v[100:103]
v_mov_b32 v252, s69
s_mov_b64 exec, 1
buffer_store_dword v252, off, s[20:23], s19 offset:0 sc1
s_mov_b64 exec, -1
MM v[100:103],a[152:155],a[72:75],v[100:103]
TR a[132:133],v230 offset:53248
TR a[134:135],v230 offset:55296
MM v[100:103],a[156:159],a[104:107],v[100:103]
buffer_load_dwordx4 a[52:55], v248, s[20:23], s92 offen offset:1024 sc1
MM v[108:111],a[156:159],a[108:111],v[108:111]
TR a[136:137],v230 offset:57344
TR a[138:139],v230 offset:59392
MM v[108:111],a[152:155],a[76:79],v[108:111]
buffer_load_dwordx4 a[84:87], v248, s[20:23], s92 offen offset:2048 sc1
MM v[108:111],a[148:151],a[44:47],v[108:111]
TR a[140:141],v230 offset:61440
TR a[142:143],v230 offset:63488
MM v[108:111],a[144:147],a[12:15],v[108:111]
s_waitcnt lgkmcnt(15)
v_mul_f32 v116, v112, v116
v_mul_f32 v117, v113, v117
MM v[96:99],a[160:163],a[192:195],v[96:99]
TR a[144:145],v231 offset:49152
TR a[146:147],v231 offset:51200
MM v[96:99],a[164:167],a[208:211],v[96:99]
buffer_load_dwordx4 a[116:119], v248, s[20:23], s92 offen offset:3072 sc1
MM v[96:99],a[168:171],a[224:227],v[96:99]
TR a[148:149],v231 offset:53248
TR a[150:151],v231 offset:55296
MM v[96:99],a[172:175],a[240:243],v[96:99]
v_mul_f32 v118, v114, v118
v_mul_f32 v119, v115, v119
MM v[104:107],a[172:175],a[244:247],v[104:107]
TR a[152:153],v231 offset:57344
TR a[154:155],v231 offset:59392
MM v[104:107],a[168:171],a[228:231],v[104:107]
CV v2,v116,v117
CV v3,v118,v119
MM v[104:107],a[164:167],a[212:215],v[104:107]
TR a[156:157],v231 offset:61440
TR a[158:159],v231 offset:63488
MM v[104:107],a[160:163],a[196:199],v[104:107]
s_waitcnt lgkmcnt(15)
v_lshlrev_b32 v10, 16, v236
v_and_b32 v11, 0xffff0000, v236
MM v[100:103],a[176:179],a[192:195],v[100:103]
TR a[160:161],v230 offset:49664
TR a[162:163],v230 offset:51712
MM v[100:103],a[180:183],a[208:211],v[100:103]
v_lshlrev_b32 v12, 16, v237
v_and_b32 v13, 0xffff0000, v237
MM v[100:103],a[184:187],a[224:227],v[100:103]
TR a[164:165],v230 offset:53760
TR a[166:167],v230 offset:55808
MM v[100:103],a[188:191],a[240:243],v[100:103]
v_mul_f32 v14, 0xbfb8aa3b, v10
v_mul_f32 v15, 0xbfb8aa3b, v11
MM v[108:111],a[188:191],a[244:247],v[108:111]
TR a[168:169],v230 offset:57856
TR a[170:171],v230 offset:59904
MM v[108:111],a[184:187],a[228:231],v[108:111]
v_mul_f32 v42, 0xbfb8aa3b, v12
v_mul_f32 v43, 0xbfb8aa3b, v13
MM v[108:111],a[180:183],a[212:215],v[108:111]
TR a[172:173],v230 offset:61952
TR a[174:175],v230 offset:64000
MM v[108:111],a[176:179],a[196:199],v[108:111]
s_waitcnt lgkmcnt(15)
EX v14,v14
MM v[16:19],a[128:131],v[168:171],0
TR a[176:177],v231 offset:49664
TR a[178:179],v231 offset:51712
MM v[16:19],a[132:135],v[184:187],v[16:19]
AW a3,-1
AW a7,-1
MM v[16:19],a[136:139],v[200:203],v[16:19]
TR a[180:181],v231 offset:53760
TR a[182:183],v231 offset:55808
MM v[16:19],a[140:143],v[216:219],v[16:19]
s_add_u32 s67, s46, 4096
CV v96,v96,v97
CV v97,v98,v99
AW a11,-1
MM v[24:27],a[140:143],v[220:223],0
TR a[184:185],v231 offset:57856
TR a[186:187],v231 offset:59904
MM v[24:27],a[136:139],v[204:207],v[24:27]
CV v98,v100,v101
CV v99,v102,v103
MM v[24:27],a[132:135],v[188:191],v[24:27]
TR a[188:189],v231 offset:61952
TR a[190:191],v231 offset:64000
AW a15,-1
MM v[24:27],a[128:131],v[172:175],v[24:27]
s_waitcnt lgkmcnt(15)
buffer_store_dwordx4 v[96:99], v249, s[20:23], s67 offen offset:2048 sc1
MM v[20:23],a[144:147],v[168:171],0
s_add_u32 s67, s47, 4096
TR a[128:129],v230 offset:16384
TR a[130:131],v230 offset:18432
MM v[20:23],a[148:151],v[184:187],v[20:23]
CV v104,v104,v105
CV v105,v106,v107
AW a35,-1
MM v[20:23],a[152:155],v[200:203],v[20:23]
TR a[132:133],v230 offset:20480
TR a[134:135],v230 offset:22528
MM v[20:23],a[156:159],v[216:219],v[20:23]
CV v106,v108,v109
CV v107,v110,v111
s_mov_b64 exec, 0xffff
AW a0,v160
AW a1,v161
AW a2,v162
AW a3,v163
s_mov_b64 exec, -1
MM v[28:31],a[156:159],v[220:223],0
TR a[136:137],v230 offset:24576
TR a[138:139],v230 offset:26624
AW a39,-1
MM v[28:31],a[152:155],v[204:207],v[28:31]
buffer_store_dwordx4 v[104:107], v234, s[20:23], s67 offen offset:3072 sc1
MM v[28:31],a[148:151],v[188:191],v[28:31]
TR a[140:141],v230 offset:28672
TR a[142:143],v230 offset:30720
s_mov_b64 exec, 0xffff
AW a8,v176
AW a9,v177
AW a10,v178
AW a11,v179
s_mov_b64 exec, -1
MM v[28:31],a[144:147],v[172:175],v[28:31]
s_waitcnt lgkmcnt(15)
ds_bpermute_b32 v208, v245, v96
ds_bpermute_b32 v209, v245, v97
AW a43,-1
MM v[48:51],a[160:163],v[168:171],0
TR a[144:145],v231 offset:16384
TR a[146:147],v231 offset:18432
MM v[48:51],a[164:167],v[184:187],v[48:51]
ds_bpermute_b32 v210, v245, v98
ds_bpermute_b32 v211, v245, v99
MM v[48:51],a[168:171],v[200:203],v[48:51]
TR a[148:149],v231 offset:20480
TR a[150:151],v231 offset:22528
AW a47,-1
MM v[48:51],a[172:175],v[216:219],v[48:51]
buffer_load_dword v240, v253, s[20:23], s68 offen offset:0 sc1
buffer_load_dword v140, v253, s[20:23], s45 offen offset:0 sc1
MM v[56:59],a[172:175],v[220:223],0
TR a[152:153],v231 offset:24576
TR a[154:155],v231 offset:26624
MM v[56:59],a[168:171],v[204:207],v[56:59]
AW a71,-1
AW a79,-1
MM v[56:59],a[164:167],v[188:191],v[56:59]
TR a[156:157],v231 offset:28672
TR a[158:159],v231 offset:30720
MM v[56:59],a[160:163],v[172:175],v[56:59]
s_waitcnt lgkmcnt(15)
AW a103,-1
AW a111,-1
MM v[52:55],a[176:179],v[168:171],0
TR a[160:161],v230 offset:16896
TR a[162:163],v230 offset:18944
MM v[52:55],a[180:183],v[184:187],v[52:55]
AW a195,-1
AW a199,-1
MM v[52:55],a[184:187],v[200:203],v[52:55]
TR a[164:165],v230 offset:20992
TR a[166:167],v230 offset:23040
MM v[52:55],a[188:191],v[216:219],v[52:55]
AW a215,-1
MM v[60:63],a[188:191],v[220:223],0
TR a[168:169],v230 offset:25088
TR a[170:171],v230 offset:27136
MM v[60:63],a[184:187],v[204:207],v[60:63]
s_mov_b64 exec, 0xffff
AW a32,v192
AW a33,v193
AW a34,v194
AW a35,v195
s_mov_b64 exec, -1
MM v[60:63],a[180:183],v[188:191],v[60:63]
s_waitcnt lgkmcnt(12)
TR a[172:173],v230 offset:29184
TR a[174:175],v230 offset:31232
MM v[60:63],a[176:179],v[172:175],v[60:63]
s_mov_b64 exec, 0xffff
AW a40,v208
AW a41,v209
AW a42,v210
AW a43,v211
s_mov_b64 exec, -1
MM v[80:83],a[128:131],v[168:171],0
TR a[176:177],v231 offset:16896
TR a[178:179],v231 offset:18944
MM v[80:83],a[132:135],v[184:187],v[80:83]
s_mov_b64 exec, 0xffff
AW a192,v164
AW a193,v165
AW a194,v166
AW a195,v167
s_mov_b64 exec, -1
MM v[80:83],a[136:139],v[200:203],v[80:83]
LPG23_%=:
s_cbranch_execz LTR2_%=
TR a[180:181],v231 offset:20992
TR a[182:183],v231 offset:23040
MM v[80:83],a[140:143],v[216:219],v[80:83]
v_lshrrev_b32 v104, 8, v248
v_min_u32 v104, 1, v104
MM v[88:91],a[140:143],v[220:223],0
TR a[184:185],v231 offset:25088
TR a[186:187],v231 offset:27136
MM v[88:91],a[136:139],v[204:207],v[88:91]
EX v15,v15
v_sub_u32 v104, 0, v104
MM v[88:91],a[132:135],v[188:191],v[88:91]
TR a[188:189],v231 offset:29184
TR a[190:191],v231 offset:31232
MM v[88:91],a[128:131],v[172:175],v[88:91]
s_waitcnt lgkmcnt(15)
EX v42,v42
MM v[84:87],a[144:147],v[168:171],0
TR a[128:129],v228 offset:0
TR a[130:131],v228 offset:1024
MM v[84:87],a[148:151],v[184:187],v[84:87]
EX v43,v43
MM v[84:87],a[152:155],v[200:203],v[84:87]
TR a[132:133],v228 offset:2048
TR a[134:135],v228 offset:3072
MM v[84:87],a[156:159],v[216:219],v[84:87]
v_add_f32 v14, 1.0, v14
v_add_f32 v15, 1.0, v15
MM v[92:95],a[156:159],v[220:223],0
TR a[136:137],v228 offset:4096
TR a[138:139],v228 offset:5120
MM v[92:95],a[152:155],v[204:207],v[92:95]
v_add_f32 v42, 1.0, v42
v_add_f32 v43, 1.0, v43
MM v[92:95],a[148:151],v[188:191],v[92:95]
s_xor_b32 s92, s49, 2
TR a[140:141],v228 offset:6144
TR a[142:143],v228 offset:7168
MM v[92:95],a[144:147],v[172:175],v[92:95]
s_waitcnt lgkmcnt(15)
s_mul_i32 s92, s92, 17408
s_add_u32 s92, s83, s92
MM v[112:115],a[160:163],v[168:171],0
s_add_u32 s92, s92, 8192
TR a[144:145],v229 offset:0
TR a[146:147],v229 offset:1024
MM v[112:115],a[164:167],v[184:187],v[112:115]
buffer_load_dwordx4 a[24:27], v248, s[20:23], s92 offen offset:0 sc1
MM v[112:115],a[168:171],v[200:203],v[112:115]
TR a[148:149],v229 offset:2048
TR a[150:151],v229 offset:3072
)ASM"
R"ASM(
MM v[112:115],a[172:175],v[216:219],v[112:115]
buffer_load_dwordx4 a[56:59], v248, s[20:23], s92 offen offset:1024 sc1
MM v[120:123],a[172:175],v[220:223],0
TR a[152:153],v229 offset:4096
TR a[154:155],v229 offset:5120
MM v[120:123],a[168:171],v[204:207],v[120:123]
buffer_load_dwordx4 a[88:91], v248, s[20:23], s92 offen offset:2048 sc1
MM v[120:123],a[164:167],v[188:191],v[120:123]
TR a[156:157],v229 offset:6144
TR a[158:159],v229 offset:7168
MM v[120:123],a[160:163],v[172:175],v[120:123]
s_waitcnt lgkmcnt(15)
v_rcp_f32 v14, v14
MM v[116:119],a[176:179],v[168:171],0
TR a[160:161],v244 offset:49152
TR a[162:163],v244 offset:51200
MM v[116:119],a[180:183],v[184:187],v[116:119]
buffer_load_dwordx4 a[120:123], v248, s[20:23], s92 offen offset:3072 sc1
MM v[116:119],a[184:187],v[200:203],v[116:119]
s_add_u32 s92, s92, 4096
TR a[164:165],v244 offset:53248
TR a[166:167],v244 offset:55296
MM v[116:119],a[188:191],v[216:219],v[116:119]
buffer_load_dwordx4 a[28:31], v248, s[20:23], s92 offen offset:0 sc1
MM v[124:127],a[188:191],v[220:223],0
TR a[168:169],v244 offset:57344
TR a[170:171],v244 offset:59392
MM v[124:127],a[184:187],v[204:207],v[124:127]
v_rcp_f32 v15, v15
MM v[124:127],a[180:183],v[188:191],v[124:127]
TR a[172:173],v244 offset:61440
TR a[174:175],v244 offset:63488
MM v[124:127],a[176:179],v[172:175],v[124:127]
s_waitcnt lgkmcnt(15)
buffer_load_dwordx4 a[60:63], v248, s[20:23], s92 offen offset:1024 sc1
MM v[144:147],a[128:131],v[168:171],0
TR a[176:177],v235 offset:49152
TR a[178:179],v235 offset:51200
MM v[144:147],a[132:135],v[184:187],v[144:147]
buffer_load_dwordx4 a[92:95], v248, s[20:23], s92 offen offset:2048 sc1
MM v[144:147],a[136:139],v[200:203],v[144:147]
TR a[180:181],v235 offset:53248
TR a[182:183],v235 offset:55296
MM v[144:147],a[140:143],v[216:219],v[144:147]
v_rcp_f32 v42, v42
MM v[152:155],a[140:143],v[220:223],0
TR a[184:185],v235 offset:57344
TR a[186:187],v235 offset:59392
MM v[152:155],a[136:139],v[204:207],v[152:155]
buffer_load_dwordx4 a[124:127], v248, s[20:23], s92 offen offset:3072 sc1
MM v[152:155],a[132:135],v[188:191],v[152:155]
s_xor_b32 s92, s49, 3
TR a[188:189],v235 offset:61440
TR a[190:191],v235 offset:63488
MM v[152:155],a[128:131],v[172:175],v[152:155]
s_waitcnt lgkmcnt(15)
s_mul_i32 s92, s92, 17408
s_add_u32 s92, s83, s92
MM v[148:151],a[144:147],v[168:171],0
s_add_u32 s92, s92, 8192
TR a[128:129],v244 offset:49664
TR a[130:131],v244 offset:51712
MM v[148:151],a[148:151],v[184:187],v[148:151]
buffer_load_dwordx4 a[200:203], v248, s[20:23], s92 offen offset:0 sc1
MM v[148:151],a[152:155],v[200:203],v[148:151]
TR a[132:133],v244 offset:53760
TR a[134:135],v244 offset:55808
MM v[148:151],a[156:159],v[216:219],v[148:151]
buffer_load_dwordx4 a[216:219], v248, s[20:23], s92 offen offset:1024 sc1
MM v[156:159],a[156:159],v[220:223],0
TR a[136:137],v244 offset:57856
TR a[138:139],v244 offset:59904
MM v[156:159],a[152:155],v[204:207],v[156:159]
buffer_load_dwordx4 a[232:235], v248, s[20:23], s92 offen offset:2048 sc1
MM v[156:159],a[148:151],v[188:191],v[156:159]
TR a[140:141],v244 offset:61952
TR a[142:143],v244 offset:64000
MM v[156:159],a[144:147],v[172:175],v[156:159]
s_waitcnt lgkmcnt(15)
s_waitcnt vmcnt(11)
v_cmp_eq_u32 s[36:37], s69, v240
MM v[16:19],a[160:163],a[16:19],v[16:19]
TR a[144:145],v235 offset:49664
TR a[146:147],v235 offset:51712
MM v[16:19],a[164:167],a[48:51],v[16:19]
buffer_load_dwordx4 a[248:251], v248, s[20:23], s92 offen offset:3072 sc1
v_cmp_eq_u32 s[38:39], s69, v140
MM v[16:19],a[168:171],a[80:83],v[16:19]
s_add_u32 s92, s92, 4096
TR a[148:149],v235 offset:53760
TR a[150:151],v235 offset:55808
MM v[16:19],a[172:175],a[112:115],v[16:19]
s_nop 0
buffer_load_dwordx4 a[204:207], v248, s[20:23], s92 offen offset:0 sc1
MM v[24:27],a[172:175],a[116:119],v[24:27]
s_and_b32 s36, s36, 0xffff0000
TR a[152:153],v235 offset:57856
TR a[154:155],v235 offset:59904
MM v[24:27],a[168:171],a[84:87],v[24:27]
v_rcp_f32 v43, v43
MM v[24:27],a[164:167],a[52:55],v[24:27]
TR a[156:157],v235 offset:61952
TR a[158:159],v235 offset:64000
MM v[24:27],a[160:163],a[20:23],v[24:27]
s_waitcnt lgkmcnt(15)
buffer_load_dwordx4 a[220:223], v248, s[20:23], s92 offen offset:1024 sc1
MM v[20:23],a[176:179],a[16:19],v[20:23]
TR a[160:161],v244 offset:16384
TR a[162:163],v244 offset:18432
MM v[20:23],a[180:183],a[48:51],v[20:23]
buffer_load_dwordx4 a[236:239], v248, s[20:23], s92 offen offset:2048 sc1
MM v[20:23],a[184:187],a[80:83],v[20:23]
v_mov_b32 v252, s69
s_mov_b64 exec, 1
buffer_store_dword v252, off, s[20:23], s19 offset:4 sc1
s_mov_b64 exec, -1
TR a[164:165],v244 offset:20480
TR a[166:167],v244 offset:22528
MM v[20:23],a[188:191],a[112:115],v[20:23]
v_mul_f32 v14, v10, v14
v_mul_f32 v15, v11, v15
MM v[28:31],a[188:191],a[116:119],v[28:31]
TR a[168:169],v244 offset:24576
TR a[170:171],v244 offset:26624
MM v[28:31],a[184:187],a[84:87],v[28:31]
buffer_load_dwordx4 a[252:255], v248, s[20:23], s92 offen offset:3072 sc1
MM v[28:31],a[180:183],a[52:55],v[28:31]
TR a[172:173],v244 offset:28672
TR a[174:175],v244 offset:30720
MM v[28:31],a[176:179],a[20:23],v[28:31]
s_waitcnt lgkmcnt(15)
s_add_u32 s66, s16, 0
v_mul_f32 v42, v12, v42
MM v[48:51],a[128:131],a[16:19],v[48:51]
TR a[176:177],v235 offset:16384
TR a[178:179],v235 offset:18432
MM v[48:51],a[132:135],a[48:51],v[48:51]
s_mov_b64 exec, s[36:37]
buffer_load_dwordx4 a[0:3], v250, s[20:23], s66 offen offset:0 sc1
s_mov_b64 exec, -1
MM v[48:51],a[136:139],a[80:83],v[48:51]
s_add_u32 s66, s44, 0
TR a[180:181],v235 offset:20480
TR a[182:183],v235 offset:22528
MM v[48:51],a[140:143],a[112:115],v[48:51]
s_mov_b64 exec, s[38:39]
buffer_load_dwordx4 a[4:7], v250, s[20:23], s66 offen offset:0 sc1
s_mov_b64 exec, -1
MM v[56:59],a[140:143],a[116:119],v[56:59]
s_add_u32 s66, s16, 2048
TR a[184:185],v235 offset:24576
TR a[186:187],v235 offset:26624
MM v[56:59],a[136:139],a[84:87],v[56:59]
s_mov_b64 exec, s[36:37]
buffer_load_dwordx4 a[8:11], v250, s[20:23], s66 offen offset:0 sc1
s_mov_b64 exec, -1
MM v[56:59],a[132:135],a[52:55],v[56:59]
s_add_u32 s66, s44, 2048
TR a[188:189],v235 offset:28672
TR a[190:191],v235 offset:30720
MM v[56:59],a[128:131],a[20:23],v[56:59]
s_waitcnt lgkmcnt(15)
v_mul_f32 v43, v13, v43
CV v4,v14,v15
MM v[52:55],a[144:147],a[16:19],v[52:55]
TR a[128:129],v244 offset:16896
TR a[130:131],v244 offset:18944
MM v[52:55],a[148:151],a[48:51],v[52:55]
s_mov_b64 exec, s[38:39]
buffer_load_dwordx4 a[12:15], v250, s[20:23], s66 offen offset:0 sc1
s_mov_b64 exec, -1
MM v[52:55],a[152:155],a[80:83],v[52:55]
TR a[132:133],v244 offset:20992
TR a[134:135],v244 offset:23040
MM v[52:55],a[156:159],a[112:115],v[52:55]
CV v5,v42,v43
v_lshlrev_b32 v10, 16, v238
MM v[60:63],a[156:159],a[116:119],v[60:63]
TR a[136:137],v244 offset:25088
TR a[138:139],v244 offset:27136
MM v[60:63],a[152:155],a[84:87],v[60:63]
v_and_b32 v11, 0xffff0000, v238
v_lshlrev_b32 v12, 16, v239
MM v[60:63],a[148:151],a[52:55],v[60:63]
TR a[140:141],v244 offset:29184
TR a[142:143],v244 offset:31232
MM v[60:63],a[144:147],a[20:23],v[60:63]
s_waitcnt lgkmcnt(15)
v_and_b32 v13, 0xffff0000, v239
v_mul_f32 v14, 0xbfb8aa3b, v10
MM v[80:83],a[160:163],a[16:19],v[80:83]
TR a[144:145],v235 offset:16896
TR a[146:147],v235 offset:18944
MM v[80:83],a[164:167],a[48:51],v[80:83]
v_mul_f32 v15, 0xbfb8aa3b, v11
v_mul_f32 v42, 0xbfb8aa3b, v12
MM v[80:83],a[168:171],a[80:83],v[80:83]
TR a[148:149],v235 offset:20992
TR a[150:151],v235 offset:23040
MM v[80:83],a[172:175],a[112:115],v[80:83]
v_mul_f32 v43, 0xbfb8aa3b, v13
MM v[88:91],a[172:175],a[116:119],v[88:91]
TR a[152:153],v235 offset:25088
TR a[154:155],v235 offset:27136
MM v[88:91],a[168:171],a[84:87],v[88:91]
EX v14,v14
MM v[88:91],a[164:167],a[52:55],v[88:91]
TR a[156:157],v235 offset:29184
TR a[158:159],v235 offset:31232
MM v[88:91],a[160:163],a[20:23],v[88:91]
s_waitcnt lgkmcnt(15)
EX v15,v15
MM v[84:87],a[176:179],a[16:19],v[84:87]
TR a[160:161],v228 offset:8192
TR a[162:163],v228 offset:9216
MM v[84:87],a[180:183],a[48:51],v[84:87]
EX v42,v42
MM v[84:87],a[184:187],a[80:83],v[84:87]
TR a[164:165],v228 offset:10240
TR a[166:167],v228 offset:11264
MM v[84:87],a[188:191],a[112:115],v[84:87]
EX v43,v43
MM v[92:95],a[188:191],a[116:119],v[92:95]
TR a[168:169],v228 offset:12288
TR a[170:171],v228 offset:13312
MM v[92:95],a[184:187],a[84:87],v[92:95]
v_add_f32 v14, 1.0, v14
v_add_f32 v15, 1.0, v15
MM v[92:95],a[180:183],a[52:55],v[92:95]
TR a[172:173],v228 offset:14336
TR a[174:175],v228 offset:15360
MM v[92:95],a[176:179],a[20:23],v[92:95]
s_waitcnt lgkmcnt(15)
v_add_f32 v42, 1.0, v42
v_add_f32 v43, 1.0, v43
MM v[112:115],a[128:131],a[16:19],v[112:115]
TR a[176:177],v229 offset:8192
TR a[178:179],v229 offset:9216
MM v[112:115],a[132:135],a[48:51],v[112:115]
v_rcp_f32 v14, v14
MM v[112:115],a[136:139],a[80:83],v[112:115]
TR a[180:181],v229 offset:10240
TR a[182:183],v229 offset:11264
MM v[112:115],a[140:143],a[112:115],v[112:115]
buffer_load_dword v241, v253, s[20:23], s68 offen offset:4 sc1
buffer_load_dword v141, v253, s[20:23], s45 offen offset:4 sc1
MM v[120:123],a[140:143],a[116:119],v[120:123]
TR a[184:185],v229 offset:12288
TR a[186:187],v229 offset:13312
MM v[120:123],a[136:139],a[84:87],v[120:123]
v_rcp_f32 v15, v15
MM v[120:123],a[132:135],a[52:55],v[120:123]
TR a[188:189],v229 offset:14336
TR a[190:191],v229 offset:15360
MM v[120:123],a[128:131],a[20:23],v[120:123]
s_waitcnt lgkmcnt(15)
v_rcp_f32 v42, v42
MM v[116:119],a[144:147],a[16:19],v[116:119]
TR a[128:129],v230 offset:0
TR a[130:131],v230 offset:2048
MM v[116:119],a[148:151],a[48:51],v[116:119]
v_rcp_f32 v43, v43
MM v[116:119],a[152:155],a[80:83],v[116:119]
TR a[132:133],v230 offset:4096
TR a[134:135],v230 offset:6144
MM v[116:119],a[156:159],a[112:115],v[116:119]
v_mul_f32 v14, v10, v14
v_mul_f32 v15, v11, v15
MM v[124:127],a[156:159],a[116:119],v[124:127]
TR a[136:137],v230 offset:8192
TR a[138:139],v230 offset:10240
MM v[124:127],a[152:155],a[84:87],v[124:127]
v_mul_f32 v42, v12, v42
v_mul_f32 v43, v13, v43
MM v[124:127],a[148:151],a[52:55],v[124:127]
TR a[140:141],v230 offset:12288
TR a[142:143],v230 offset:14336
MM v[124:127],a[144:147],a[20:23],v[124:127]
s_waitcnt lgkmcnt(15)
CV v6,v14,v15
CV v7,v42,v43
MM v[144:147],a[160:163],a[16:19],v[144:147]
s_add_u32 s80, s18, 0
TR a[144:145],v244 offset:0
TR a[146:147],v244 offset:2048
MM v[144:147],a[164:167],a[48:51],v[144:147]
buffer_load_dwordx2 v[32:33], v251, s[32:35], s80 offen offset:0
MM v[144:147],a[168:171],a[80:83],v[144:147]
s_add_u32 s80, s18, 128
TR a[148:149],v244 offset:4096
TR a[150:151],v244 offset:6144
MM v[144:147],a[172:175],a[112:115],v[144:147]
buffer_load_dwordx2 v[34:35], v251, s[32:35], s80 offen offset:0
MM v[152:155],a[172:175],a[116:119],v[152:155]
s_add_u32 s80, s18, 40960
TR a[152:153],v244 offset:8192
TR a[154:155],v244 offset:10240
MM v[152:155],a[168:171],a[84:87],v[152:155]
buffer_load_dwordx2 v[36:37], v251, s[32:35], s80 offen offset:0
MM v[152:155],a[164:167],a[52:55],v[152:155]
s_add_u32 s80, s18, 41088
TR a[156:157],v244 offset:12288
TR a[158:159],v244 offset:14336
MM v[152:155],a[160:163],a[20:23],v[152:155]
s_waitcnt lgkmcnt(15)
buffer_load_dwordx2 v[38:39], v251, s[32:35], s80 offen offset:0
MM v[148:151],a[176:179],a[16:19],v[148:151]
s_add_u32 s80, s63, 0
TR a[160:161],v231 offset:0
TR a[162:163],v231 offset:2048
MM v[148:151],a[180:183],a[48:51],v[148:151]
buffer_load_dwordx2 v[40:41], v254, s[32:35], s80 offen offset:0
MM v[148:151],a[184:187],a[80:83],v[148:151]
TR a[164:165],v231 offset:4096
TR a[166:167],v231 offset:6144
MM v[148:151],a[188:191],a[112:115],v[148:151]
v_mov_b32 v77, 0x3f80
v_and_b32 v76, 16, v248
MM v[156:159],a[188:191],a[116:119],v[156:159]
TR a[168:169],v231 offset:8192
TR a[170:171],v231 offset:10240
MM v[156:159],a[184:187],a[84:87],v[156:159]
v_lshlrev_b32 v76, v76, v77
s_mov_b32 s28, 0x30003
MM v[156:159],a[180:183],a[52:55],v[156:159]
s_mov_b32 s29, s28
TR a[172:173],v231 offset:12288
TR a[174:175],v231 offset:14336
MM v[156:159],a[176:179],a[20:23],v[156:159]
s_waitcnt lgkmcnt(15)
v_cndmask_b32 v64, 0, v76, s[28:29]
)ASM"
R"ASM(
s_lshl_b64 s[28:29], s[28:29], 2
s_waitcnt vmcnt(24)
MM v[16:19],a[128:131],a[24:27],v[16:19]
TR a[176:177],v235 offset:0
TR a[178:179],v235 offset:2048
MM v[16:19],a[132:135],a[56:59],v[16:19]
v_cndmask_b32 v65, 0, v76, s[28:29]
s_lshl_b64 s[28:29], s[28:29], 2
MM v[16:19],a[136:139],a[88:91],v[16:19]
TR a[180:181],v235 offset:4096
TR a[182:183],v235 offset:6144
MM v[16:19],a[140:143],a[120:123],v[16:19]
s_waitcnt vmcnt(20)
v_cndmask_b32 v66, 0, v76, s[28:29]
s_lshl_b64 s[28:29], s[28:29], 2
MM v[24:27],a[140:143],a[124:127],v[24:27]
TR a[184:185],v235 offset:8192
TR a[186:187],v235 offset:10240
MM v[24:27],a[136:139],a[92:95],v[24:27]
v_cndmask_b32 v67, 0, v76, s[28:29]
s_lshl_b64 s[28:29], s[28:29], 2
MM v[24:27],a[132:135],a[60:63],v[24:27]
TR a[188:189],v235 offset:12288
TR a[190:191],v235 offset:14336
MM v[24:27],a[128:131],a[28:31],v[24:27]
s_waitcnt lgkmcnt(15)
v_cndmask_b32 v68, 0, v76, s[28:29]
s_lshl_b64 s[28:29], s[28:29], 2
s_waitcnt vmcnt(16)
MM v[16:19],a[144:147],a[200:203],v[16:19]
TR a[128:129],v230 offset:512
TR a[130:131],v230 offset:2560
MM v[16:19],a[148:151],a[216:219],v[16:19]
v_cndmask_b32 v69, 0, v76, s[28:29]
s_lshl_b64 s[28:29], s[28:29], 2
MM v[16:19],a[152:155],a[232:235],v[16:19]
TR a[132:133],v230 offset:4608
TR a[134:135],v230 offset:6656
MM v[16:19],a[156:159],a[248:251],v[16:19]
s_waitcnt vmcnt(11)
v_cndmask_b32 v70, 0, v76, s[28:29]
s_lshl_b64 s[28:29], s[28:29], 2
MM v[24:27],a[156:159],a[252:255],v[24:27]
TR a[136:137],v230 offset:8704
TR a[138:139],v230 offset:10752
MM v[24:27],a[152:155],a[236:239],v[24:27]
v_cndmask_b32 v71, 0, v76, s[28:29]
MM v[24:27],a[148:151],a[220:223],v[24:27]
TR a[140:141],v230 offset:12800
TR a[142:143],v230 offset:14848
MM v[24:27],a[144:147],a[204:207],v[24:27]
s_waitcnt lgkmcnt(15)
MM v[20:23],a[160:163],a[24:27],v[20:23]
TR a[144:145],v244 offset:512
TR a[146:147],v244 offset:2560
MM v[20:23],a[164:167],a[56:59],v[20:23]
MM v[20:23],a[168:171],a[88:91],v[20:23]
TR a[148:149],v244 offset:4608
TR a[150:151],v244 offset:6656
MM v[20:23],a[172:175],a[120:123],v[20:23]
MM v[28:31],a[172:175],a[124:127],v[28:31]
TR a[152:153],v244 offset:8704
TR a[154:155],v244 offset:10752
MM v[28:31],a[168:171],a[92:95],v[28:31]
MM v[28:31],a[164:167],a[60:63],v[28:31]
TR a[156:157],v244 offset:12800
TR a[158:159],v244 offset:14848
MM v[28:31],a[160:163],a[28:31],v[28:31]
s_waitcnt lgkmcnt(15)
MM v[28:31],a[188:191],a[252:255],v[28:31]
TR a[160:161],v231 offset:512
TR a[162:163],v231 offset:2560
MM v[28:31],a[184:187],a[236:239],v[28:31]
MM v[28:31],a[180:183],a[220:223],v[28:31]
TR a[164:165],v231 offset:4608
TR a[166:167],v231 offset:6656
MM v[28:31],a[176:179],a[204:207],v[28:31]
MM v[20:23],a[176:179],a[200:203],v[20:23]
TR a[168:169],v231 offset:8704
TR a[170:171],v231 offset:10752
MM v[20:23],a[180:183],a[216:219],v[20:23]
MM v[20:23],a[184:187],a[232:235],v[20:23]
TR a[172:173],v231 offset:12800
TR a[174:175],v231 offset:14848
MM v[20:23],a[188:191],a[248:251],v[20:23]
s_waitcnt lgkmcnt(15)
s_waitcnt vmcnt(5)
v_cmp_eq_u32 s[8:9], s69, v241
MM v[48:51],a[128:131],a[24:27],v[48:51]
TR a[176:177],v235 offset:512
TR a[178:179],v235 offset:2560
MM v[48:51],a[132:135],a[56:59],v[48:51]
v_cmp_eq_u32 s[10:11], s69, v141
MM v[48:51],a[136:139],a[88:91],v[48:51]
TR a[180:181],v235 offset:4608
TR a[182:183],v235 offset:6656
MM v[48:51],a[140:143],a[120:123],v[48:51]
s_add_u32 s67, s46, 8192
CV v16,v16,v17
CV v17,v18,v19
MM v[56:59],a[140:143],a[124:127],v[56:59]
s_nop 0
TR a[184:185],v235 offset:8704
TR a[186:187],v235 offset:10752
MM v[56:59],a[136:139],a[92:95],v[56:59]
s_and_b32 s8, s8, 0xffff0000
CV v18,v20,v21
CV v19,v22,v23
MM v[56:59],a[132:135],a[60:63],v[56:59]
s_add_u32 s66, s16, 4096
TR a[188:189],v235 offset:12800
TR a[190:191],v235 offset:14848
MM v[56:59],a[128:131],a[28:31],v[56:59]
s_waitcnt lgkmcnt(15)
buffer_store_dwordx4 v[16:19], v249, s[20:23], s67 offen offset:2048 sc1
MM v[56:59],a[156:159],a[252:255],v[56:59]
s_add_u32 s67, s47, 8192
TR a[128:129],v230 offset:32768
TR a[130:131],v230 offset:34816
MM v[56:59],a[152:155],a[236:239],v[56:59]
s_mov_b64 exec, s[8:9]
buffer_load_dwordx4 a[32:35], v250, s[20:23], s66 offen offset:0 sc1
s_mov_b64 exec, -1
CV v24,v24,v25
CV v25,v26,v27
MM v[56:59],a[148:151],a[220:223],v[56:59]
s_add_u32 s66, s44, 4096
TR a[132:133],v230 offset:36864
TR a[134:135],v230 offset:38912
MM v[56:59],a[144:147],a[204:207],v[56:59]
CV v26,v28,v29
CV v27,v30,v31
MM v[48:51],a[144:147],a[200:203],v[48:51]
TR a[136:137],v230 offset:40960
TR a[138:139],v230 offset:43008
MM v[48:51],a[148:151],a[216:219],v[48:51]
buffer_store_dwordx4 v[24:27], v234, s[20:23], s67 offen offset:3072 sc1
MM v[48:51],a[152:155],a[232:235],v[48:51]
TR a[140:141],v230 offset:45056
TR a[142:143],v230 offset:47104
MM v[48:51],a[156:159],a[248:251],v[48:51]
s_waitcnt lgkmcnt(15)
ds_bpermute_b32 v16, v245, v16
ds_bpermute_b32 v17, v245, v17
MM v[52:55],a[160:163],a[24:27],v[52:55]
TR a[144:145],v244 offset:32768
TR a[146:147],v244 offset:34816
MM v[52:55],a[164:167],a[56:59],v[52:55]
s_mov_b64 exec, s[10:11]
buffer_load_dwordx4 a[36:39], v250, s[20:23], s66 offen offset:0 sc1
s_mov_b64 exec, -1
ds_bpermute_b32 v18, v245, v18
MM v[52:55],a[168:171],a[88:91],v[52:55]
s_add_u32 s66, s16, 6144
TR a[148:149],v244 offset:36864
TR a[150:151],v244 offset:38912
MM v[52:55],a[172:175],a[120:123],v[52:55]
ds_bpermute_b32 v19, v245, v19
MM v[60:63],a[172:175],a[124:127],v[60:63]
TR a[152:153],v244 offset:40960
TR a[154:155],v244 offset:43008
MM v[60:63],a[168:171],a[92:95],v[60:63]
MM v[60:63],a[164:167],a[60:63],v[60:63]
TR a[156:157],v244 offset:45056
TR a[158:159],v244 offset:47104
MM v[60:63],a[160:163],a[28:31],v[60:63]
s_waitcnt lgkmcnt(15)
s_mov_b64 exec, s[8:9]
buffer_load_dwordx4 a[40:43], v250, s[20:23], s66 offen offset:0 sc1
s_mov_b64 exec, -1
MM v[60:63],a[188:191],a[252:255],v[60:63]
s_add_u32 s66, s44, 6144
TR a[160:161],v231 offset:32768
TR a[162:163],v231 offset:34816
MM v[60:63],a[184:187],a[236:239],v[60:63]
s_waitcnt lgkmcnt(6)
v_or_b32 v19, v104, v19
MM v[60:63],a[180:183],a[220:223],v[60:63]
TR a[164:165],v231 offset:36864
TR a[166:167],v231 offset:38912
MM v[60:63],a[176:179],a[204:207],v[60:63]
s_nop 0
LPG24_%=:
s_cbranch_execz LTR2_%=
MM v[52:55],a[176:179],a[200:203],v[52:55]
TR a[168:169],v231 offset:40960
TR a[170:171],v231 offset:43008
MM v[52:55],a[180:183],a[216:219],v[52:55]
s_mov_b64 exec, s[10:11]
buffer_load_dwordx4 a[44:47], v250, s[20:23], s66 offen offset:0 sc1
s_mov_b64 exec, -1
MM v[52:55],a[184:187],a[232:235],v[52:55]
s_add_u32 s66, s16, 8192
TR a[172:173],v231 offset:45056
TR a[174:175],v231 offset:47104
MM v[52:55],a[188:191],a[248:251],v[52:55]
MM v[80:83],a[128:131],a[24:27],v[80:83]
TR a[176:177],v235 offset:32768
TR a[178:179],v235 offset:34816
MM v[80:83],a[132:135],a[56:59],v[80:83]
v_mov_b32 v252, s69
s_mov_b64 exec, 1
buffer_store_dword v252, off, s[20:23], s19 offset:8 sc1
s_mov_b64 exec, -1
MM v[80:83],a[136:139],a[88:91],v[80:83]
TR a[180:181],v235 offset:36864
TR a[182:183],v235 offset:38912
MM v[80:83],a[140:143],a[120:123],v[80:83]
s_add_u32 s67, s46, 12288
CV v48,v48,v49
CV v49,v50,v51
MM v[88:91],a[140:143],a[124:127],v[88:91]
TR a[184:185],v235 offset:40960
TR a[186:187],v235 offset:43008
MM v[88:91],a[136:139],a[92:95],v[88:91]
s_mov_b64 exec, s[8:9]
buffer_load_dwordx4 a[192:195], v250, s[20:23], s66 offen offset:0 sc1
s_mov_b64 exec, -1
CV v50,v52,v53
CV v51,v54,v55
MM v[88:91],a[132:135],a[60:63],v[88:91]
s_add_u32 s66, s44, 8192
TR a[188:189],v235 offset:45056
TR a[190:191],v235 offset:47104
MM v[88:91],a[128:131],a[28:31],v[88:91]
s_waitcnt lgkmcnt(15)
buffer_store_dwordx4 v[48:51], v249, s[20:23], s67 offen offset:0 sc1
MM v[88:91],a[156:159],a[252:255],v[88:91]
s_add_u32 s67, s47, 12288
TR a[128:129],v230 offset:33280
TR a[130:131],v230 offset:35328
MM v[88:91],a[152:155],a[236:239],v[88:91]
s_waitcnt vmcnt(9)
CV v56,v56,v57
CV v57,v58,v59
MM v[88:91],a[148:151],a[220:223],v[88:91]
TR a[132:133],v230 offset:37376
TR a[134:135],v230 offset:39424
MM v[72:75],v[64:67],a[0:3],0
MM v[72:75],v[64:67],a[4:7],v[72:75]
MM v[72:75],v[68:71],a[8:11],v[72:75]
MM v[72:75],v[68:71],a[12:15],v[72:75]
MM v[88:91],a[144:147],a[204:207],v[88:91]
CV v58,v60,v61
CV v59,v62,v63
MM v[80:83],a[144:147],a[200:203],v[80:83]
TR a[136:137],v230 offset:41472
TR a[138:139],v230 offset:43520
MM v[80:83],a[148:151],a[216:219],v[80:83]
MM v[80:83],a[152:155],a[232:235],v[80:83]
TR a[140:141],v230 offset:45568
TR a[142:143],v230 offset:47616
MM v[80:83],a[156:159],a[248:251],v[80:83]
s_waitcnt lgkmcnt(15)
MM v[84:87],a[160:163],a[24:27],v[84:87]
TR a[144:145],v244 offset:33280
TR a[146:147],v244 offset:35328
MM v[84:87],a[164:167],a[56:59],v[84:87]
buffer_store_dwordx4 v[56:59], v234, s[20:23], s67 offen offset:1024 sc1
MM v[84:87],a[168:171],a[88:91],v[84:87]
TR a[148:149],v244 offset:37376
TR a[150:151],v244 offset:39424
MM v[84:87],a[172:175],a[120:123],v[84:87]
ds_bpermute_b32 v48, v245, v48
ds_bpermute_b32 v49, v245, v49
MM v[92:95],a[172:175],a[124:127],v[92:95]
TR a[152:153],v244 offset:41472
TR a[154:155],v244 offset:43520
MM v[92:95],a[168:171],a[92:95],v[92:95]
ds_bpermute_b32 v50, v245, v50
ds_bpermute_b32 v51, v245, v51
MM v[92:95],a[164:167],a[60:63],v[92:95]
TR a[156:157],v244 offset:45568
TR a[158:159],v244 offset:47616
MM v[92:95],a[160:163],a[28:31],v[92:95]
s_waitcnt lgkmcnt(15)
v_cmp_u_f32 s[30:31], v72, v72
CV v76,v72,v73
MM v[92:95],a[188:191],a[252:255],v[92:95]
TR a[160:161],v231 offset:33280
TR a[162:163],v231 offset:35328
MM v[92:95],a[184:187],a[236:239],v[92:95]
CV v77,v74,v75
MM v[92:95],a[180:183],a[220:223],v[92:95]
TR a[164:165],v231 offset:37376
TR a[166:167],v231 offset:39424
MM v[92:95],a[176:179],a[204:207],v[92:95]
s_waitcnt lgkmcnt(6)
ds_bpermute_b32 v76, v232, v76
v_or_b32 v51, v104, v51
MM v[84:87],a[176:179],a[200:203],v[84:87]
TR a[168:169],v231 offset:41472
TR a[170:171],v231 offset:43520
MM v[84:87],a[180:183],a[216:219],v[84:87]
ds_bpermute_b32 v77, v232, v77
v_lshlrev_b32 v10, 16, v0
MM v[84:87],a[184:187],a[232:235],v[84:87]
TR a[172:173],v231 offset:45568
TR a[174:175],v231 offset:47616
MM v[84:87],a[188:191],a[248:251],v[84:87]
v_and_b32 v11, 0xffff0000, v0
v_lshlrev_b32 v12, 16, v1
MM v[112:115],a[128:131],a[24:27],v[112:115]
s_cmp_eq_u32 s82, 0
s_cbranch_scc1 LY1_%=
v_xor_b32 v132, s69, v132
v_xor_b32 v133, s69, v133
v_xor_b32 v134, s69, v134
v_or3_b32 v139, v132, v133, v134
v_cmp_ne_u32 vcc, 0, v139
s_cbranch_vccnz LRYF_%=
LRYFb_%=:
s_ff1_i32_b32 s76, s82
s_and_b32 s76, s76, 7
s_mov_b32 s79, s52
s_cmp_eq_u32 s76, 1
s_cselect_b32 s79, s53, s79
s_cmp_eq_u32 s76, 2
s_cselect_b32 s79, s54, s79
s_cmp_eq_u32 s76, 3
s_cselect_b32 s79, s55, s79
s_cmp_eq_u32 s76, 4
s_cselect_b32 s79, s56, s79
s_cmp_eq_u32 s76, 5
s_cselect_b32 s79, s57, s79
s_cmp_eq_u32 s76, 6
s_cselect_b32 s79, s58, s79
s_cmp_eq_u32 s76, 7
s_cselect_b32 s79, s59, s79
s_mul_i32 s76, s76, 2560
s_mul_i32 s90, s84, 640
s_add_u32 s76, s76, s90
s_add_u32 s76, s76, 21659648
s_lshl_b32 s90, s79, 2
s_add_u32 s90, s90, 53248
buffer_load_dword v138, off, s[20:23], s90 sc1
s_mul_i32 s90, s79, 1280
s_mul_i32 s77, s84, 320
s_add_u32 s90, s90, s77
s_add_u32 s90, s90, 131072
v_lshrrev_b32 v139, 1, v248
s_mov_b32 exec_lo, -1
s_mov_b32 exec_hi, 255
buffer_load_dwordx2 v[136:137], v139, s[20:23], s90 offen offset:0 sc1
s_mov_b32 exec_lo, -1
s_mov_b32 exec_hi, 255
buffer_load_dwordx4 v[160:163], v248, s[20:23], s76 offen offset:0 sc1
s_add_u32 s76, s76, 20480
buffer_load_dwordx4 v[164:167], v248, s[20:23], s76 offen offset:0 sc1
s_add_u32 s76, s76, 20480
buffer_load_dwordx4 v[168:171], v248, s[20:23], s76 offen offset:0 sc1
s_add_u32 s76, s76, 20480
buffer_load_dwordx4 v[172:175], v248, s[20:23], s76 offen offset:0 sc1
s_add_u32 s76, s76, 20480
buffer_load_dwordx4 v[176:179], v248, s[20:23], s76 offen offset:0 sc1
s_add_u32 s76, s76, 20480
buffer_load_dwordx4 v[180:183], v248, s[20:23], s76 offen offset:0 sc1
s_add_u32 s76, s76, 20480
s_mov_b64 exec, -1
LY1_%=:
TR a[176:177],v235 offset:33280
TR a[178:179],v235 offset:35328
)ASM"
R"ASM(
MM v[112:115],a[132:135],a[56:59],v[112:115]
s_mov_b64 exec, s[10:11]
buffer_load_dwordx4 a[196:199], v250, s[20:23], s66 offen offset:0 sc1
s_mov_b64 exec, -1
MM v[112:115],a[136:139],a[88:91],v[112:115]
TR a[180:181],v235 offset:37376
TR a[182:183],v235 offset:39424
MM v[112:115],a[140:143],a[120:123],v[112:115]
s_add_u32 s67, s46, 12288
CV v80,v80,v81
CV v81,v82,v83
MM v[120:123],a[140:143],a[124:127],v[120:123]
TR a[184:185],v235 offset:41472
TR a[186:187],v235 offset:43520
MM v[120:123],a[136:139],a[92:95],v[120:123]
CV v82,v84,v85
CV v83,v86,v87
MM v[120:123],a[132:135],a[60:63],v[120:123]
TR a[188:189],v235 offset:45568
TR a[190:191],v235 offset:47616
MM v[120:123],a[128:131],a[28:31],v[120:123]
s_waitcnt lgkmcnt(15)
buffer_store_dwordx4 v[80:83], v249, s[20:23], s67 offen offset:2048 sc1
MM v[120:123],a[156:159],a[252:255],v[120:123]
s_add_u32 s67, s47, 12288
TR a[128:129],v228 offset:16384
TR a[130:131],v228 offset:17408
MM v[120:123],a[152:155],a[236:239],v[120:123]
CV v88,v88,v89
CV v89,v90,v91
MM v[120:123],a[148:151],a[220:223],v[120:123]
TR a[132:133],v228 offset:18432
TR a[134:135],v228 offset:19456
MM v[120:123],a[144:147],a[204:207],v[120:123]
CV v90,v92,v93
CV v91,v94,v95
MM v[112:115],a[144:147],a[200:203],v[112:115]
TR a[136:137],v228 offset:20480
TR a[138:139],v228 offset:21504
MM v[112:115],a[148:151],a[216:219],v[112:115]
v_and_b32 v13, 0xffff0000, v1
v_lshlrev_b32 v14, 16, v32
MM v[112:115],a[152:155],a[232:235],v[112:115]
TR a[140:141],v228 offset:22528
TR a[142:143],v228 offset:23552
MM v[112:115],a[156:159],a[248:251],v[112:115]
s_waitcnt lgkmcnt(15)
buffer_store_dwordx4 v[88:91], v234, s[20:23], s67 offen offset:3072 sc1
MM v[116:119],a[160:163],a[24:27],v[116:119]
TR a[144:145],v228 offset:24576
TR a[146:147],v228 offset:25600
MM v[116:119],a[164:167],a[56:59],v[116:119]
buffer_load_dword v242, v253, s[20:23], s68 offen offset:8 sc1
buffer_load_dword v142, v253, s[20:23], s45 offen offset:8 sc1
MM v[116:119],a[168:171],a[88:91],v[116:119]
TR a[148:149],v228 offset:26624
TR a[150:151],v228 offset:27648
MM v[116:119],a[172:175],a[120:123],v[116:119]
ds_bpermute_b32 v80, v245, v80
ds_bpermute_b32 v81, v245, v81
MM v[124:127],a[172:175],a[124:127],v[124:127]
TR a[152:153],v228 offset:28672
TR a[154:155],v228 offset:29696
MM v[124:127],a[168:171],a[92:95],v[124:127]
ds_bpermute_b32 v82, v245, v82
ds_bpermute_b32 v83, v245, v83
MM v[124:127],a[164:167],a[60:63],v[124:127]
TR a[156:157],v228 offset:30720
TR a[158:159],v228 offset:31744
MM v[124:127],a[160:163],a[28:31],v[124:127]
s_waitcnt lgkmcnt(15)
v_and_b32 v15, 0xffff0000, v32
v_lshlrev_b32 v42, 16, v33
MM v[124:127],a[188:191],a[252:255],v[124:127]
s_cmp_eq_u32 s82, 0
s_cbranch_scc1 LY2_%=
s_mov_b32 exec_lo, -1
s_mov_b32 exec_hi, 255
buffer_load_dwordx4 v[184:187], v248, s[20:23], s76 offen offset:0 sc1
s_add_u32 s76, s76, 20480
buffer_load_dwordx4 v[188:191], v248, s[20:23], s76 offen offset:0 sc1
s_add_u32 s76, s76, 20480
buffer_load_dwordx4 v[192:195], v248, s[20:23], s76 offen offset:0 sc1
s_add_u32 s76, s76, 20480
buffer_load_dwordx4 v[196:199], v248, s[20:23], s76 offen offset:0 sc1
s_add_u32 s76, s76, 20480
buffer_load_dwordx4 v[200:203], v248, s[20:23], s76 offen offset:0 sc1
s_add_u32 s76, s76, 20480
buffer_load_dwordx4 v[204:207], v248, s[20:23], s76 offen offset:0 sc1
s_add_u32 s76, s76, 20480
s_mov_b64 exec, -1
LY2_%=:
TR a[160:161],v229 offset:16384
TR a[162:163],v229 offset:17408
MM v[124:127],a[184:187],a[236:239],v[124:127]
v_and_b32 v43, 0xffff0000, v33
s_add_u32 s80, s18, 0
MM v[124:127],a[180:183],a[220:223],v[124:127]
TR a[164:165],v229 offset:18432
TR a[166:167],v229 offset:19456
MM v[124:127],a[176:179],a[204:207],v[124:127]
s_waitcnt lgkmcnt(6)
v_or_b32 v83, v104, v83
v_lshlrev_b32 v72, 16, v76
MM v[116:119],a[176:179],a[200:203],v[116:119]
TR a[168:169],v229 offset:20480
TR a[170:171],v229 offset:21504
MM v[116:119],a[180:183],a[216:219],v[116:119]
v_and_b32 v73, 0xffff0000, v76
v_lshlrev_b32 v74, 16, v77
MM v[116:119],a[184:187],a[232:235],v[116:119]
TR a[172:173],v229 offset:22528
TR a[174:175],v229 offset:23552
MM v[116:119],a[188:191],a[248:251],v[116:119]
v_and_b32 v75, 0xffff0000, v77
v_mul_f32 v72, v72, v10
MM v[144:147],a[128:131],a[24:27],v[144:147]
TR a[176:177],v229 offset:24576
TR a[178:179],v229 offset:25600
MM v[144:147],a[132:135],a[56:59],v[144:147]
v_mul_f32 v73, v73, v11
v_mul_f32 v74, v74, v12
MM v[144:147],a[136:139],a[88:91],v[144:147]
TR a[180:181],v229 offset:26624
TR a[182:183],v229 offset:27648
MM v[144:147],a[140:143],a[120:123],v[144:147]
s_add_u32 s67, s46, 16384
CV v112,v112,v113
CV v113,v114,v115
MM v[152:155],a[140:143],a[124:127],v[152:155]
TR a[184:185],v229 offset:28672
TR a[186:187],v229 offset:29696
MM v[152:155],a[136:139],a[92:95],v[152:155]
CV v114,v116,v117
CV v115,v118,v119
MM v[152:155],a[132:135],a[60:63],v[152:155]
TR a[188:189],v229 offset:30720
TR a[190:191],v229 offset:31744
MM v[152:155],a[128:131],a[28:31],v[152:155]
s_waitcnt lgkmcnt(15)
buffer_store_dwordx4 v[112:115], v249, s[20:23], s67 offen offset:0 sc1
MM v[152:155],a[156:159],a[252:255],v[152:155]
s_add_u32 s67, s47, 16384
CV v120,v120,v121
CV v121,v122,v123
MM v[152:155],a[152:155],a[236:239],v[152:155]
CV v122,v124,v125
CV v123,v126,v127
MM v[152:155],a[148:151],a[220:223],v[152:155]
buffer_store_dwordx4 v[120:123], v234, s[20:23], s67 offen offset:1024 sc1
MM v[152:155],a[144:147],a[204:207],v[152:155]
v_mul_f32 v75, v75, v13
CV v76,v72,v73
MM v[144:147],a[144:147],a[200:203],v[144:147]
ds_bpermute_b32 v112, v245, v112
ds_bpermute_b32 v113, v245, v113
MM v[144:147],a[148:151],a[216:219],v[144:147]
ds_bpermute_b32 v114, v245, v114
ds_bpermute_b32 v115, v245, v115
MM v[144:147],a[152:155],a[232:235],v[144:147]
CV v77,v74,v75
v_lshlrev_b32 v72, 16, v76
MM v[144:147],a[156:159],a[248:251],v[144:147]
s_waitcnt lgkmcnt(12)
v_and_b32 v73, 0xffff0000, v76
v_lshlrev_b32 v74, 16, v77
MM v[148:151],a[160:163],a[24:27],v[148:151]
s_cmp_eq_u32 s82, 0
s_cbranch_scc1 LY3_%=
s_mov_b32 exec_lo, -1
s_mov_b32 exec_hi, 255
buffer_load_dwordx4 v[208:211], v248, s[20:23], s76 offen offset:0 sc1
s_add_u32 s76, s76, 20480
buffer_load_dwordx4 v[212:215], v248, s[20:23], s76 offen offset:0 sc1
s_add_u32 s76, s76, 20480
buffer_load_dwordx4 v[216:219], v248, s[20:23], s76 offen offset:0 sc1
s_add_u32 s76, s76, 20480
buffer_load_dwordx4 v[220:223], v248, s[20:23], s76 offen offset:0 sc1
s_add_u32 s76, s76, 20480
buffer_load_dwordx4 v[96:99], v248, s[20:23], s76 offen offset:0 sc1
s_add_u32 s76, s76, 20480
buffer_load_dwordx4 v[100:103], v248, s[20:23], s76 offen offset:0 sc1
s_mov_b64 exec, -1
LY3_%=:
v_and_b32 v75, 0xffff0000, v77
v_add_f32 v72, v72, v14
MM v[148:151],a[164:167],a[56:59],v[148:151]
v_add_f32 v73, v73, v15
v_add_f32 v74, v74, v42
MM v[148:151],a[168:171],a[88:91],v[148:151]
v_add_f32 v75, v75, v43
CV v44,v72,v73
MM v[148:151],a[172:175],a[120:123],v[148:151]
s_waitcnt lgkmcnt(0)
v_or_b32 v115, v104, v115
CV v45,v74,v75
MM v[156:159],a[172:175],a[124:127],v[156:159]
s_cmp_eq_u64 s[30:31], 0
s_cselect_b32 s94, s51, 0
s_and_b32 s94, s94, 1
MM v[156:159],a[168:171],a[92:95],v[156:159]
s_xor_b32 s17, s17, s94
s_cmp_lg_u32 s94, 0
s_cselect_b64 exec, -1, 0
buffer_store_dwordx2 v[44:45], v251, s[40:43], s80 offen offset:0 nt
s_mov_b64 exec, -1
MM v[156:159],a[164:167],a[60:63],v[156:159]
MM v[156:159],a[160:163],a[28:31],v[156:159]
MM v[156:159],a[188:191],a[252:255],v[156:159]
MM v[156:159],a[184:187],a[236:239],v[156:159]
MM v[156:159],a[180:183],a[220:223],v[156:159]
MM v[156:159],a[176:179],a[204:207],v[156:159]
MM v[148:151],a[176:179],a[200:203],v[148:151]
MM v[148:151],a[180:183],a[216:219],v[148:151]
MM v[148:151],a[184:187],a[232:235],v[148:151]
MM v[148:151],a[188:191],a[248:251],v[148:151]
s_nop 5
s_add_u32 s67, s46, 16384
CV v144,v144,v145
CV v145,v146,v147
CV v146,v148,v149
CV v147,v150,v151
buffer_store_dwordx4 v[144:147], v249, s[20:23], s67 offen offset:2048 sc1
s_add_u32 s67, s47, 16384
CV v152,v152,v153
CV v153,v154,v155
CV v154,v156,v157
CV v155,v158,v159
buffer_store_dwordx4 v[152:155], v234, s[20:23], s67 offen offset:3072 sc1
ds_bpermute_b32 v144, v245, v144
ds_bpermute_b32 v145, v245, v145
ds_bpermute_b32 v146, v245, v146
ds_bpermute_b32 v147, v245, v147
s_waitcnt vmcnt(5)
v_cmp_eq_u32 s[12:13], s69, v242
v_cmp_eq_u32 s[14:15], s69, v142
s_nop 0
s_and_b32 s12, s12, 0xffff0000
s_nop 0
s_add_u32 s66, s16, 10240
s_mov_b64 exec, s[12:13]
buffer_load_dwordx4 v[16:19], v250, s[20:23], s66 offen offset:0 sc1
s_mov_b64 exec, -1
s_add_u32 s66, s44, 10240
s_mov_b64 exec, s[14:15]
buffer_load_dwordx4 a[68:71], v250, s[20:23], s66 offen offset:0 sc1
s_mov_b64 exec, -1
s_add_u32 s66, s16, 12288
s_mov_b64 exec, s[12:13]
buffer_load_dwordx4 v[48:51], v250, s[20:23], s66 offen offset:0 sc1
s_mov_b64 exec, -1
s_add_u32 s66, s44, 12288
s_mov_b64 exec, s[14:15]
buffer_load_dwordx4 a[76:79], v250, s[20:23], s66 offen offset:0 sc1
s_mov_b64 exec, -1
s_add_u32 s66, s16, 14336
s_mov_b64 exec, s[12:13]
buffer_load_dwordx4 v[80:83], v250, s[20:23], s66 offen offset:0 sc1
s_mov_b64 exec, -1
s_add_u32 s66, s44, 14336
s_mov_b64 exec, s[14:15]
buffer_load_dwordx4 a[100:103], v250, s[20:23], s66 offen offset:0 sc1
s_mov_b64 exec, -1
s_add_u32 s66, s16, 16384
s_mov_b64 exec, s[12:13]
buffer_load_dwordx4 v[112:115], v250, s[20:23], s66 offen offset:0 sc1
s_mov_b64 exec, -1
s_add_u32 s66, s44, 16384
s_mov_b64 exec, s[14:15]
buffer_load_dwordx4 a[108:111], v250, s[20:23], s66 offen offset:0 sc1
s_mov_b64 exec, -1
s_waitcnt lgkmcnt(0)
v_or_b32 v147, v104, v147
s_add_u32 s66, s16, 18432
s_mov_b64 exec, s[12:13]
buffer_load_dwordx4 v[144:147], v250, s[20:23], s66 offen offset:0 sc1
s_mov_b64 exec, -1
s_add_u32 s66, s44, 18432
s_mov_b64 exec, s[14:15]
buffer_load_dwordx4 a[212:215], v250, s[20:23], s66 offen offset:0 sc1
s_mov_b64 exec, -1
buffer_load_dwordx4 v[240:243], v253, s[20:23], s68 offen offset:0 sc1
buffer_load_dwordx4 v[140:143], v253, s[20:23], s45 offen offset:0 sc1
v_lshlrev_b32 v10, 16, v246
v_and_b32 v11, 0xffff0000, v246
v_lshlrev_b32 v12, 16, v247
v_and_b32 v13, 0xffff0000, v247
v_mul_f32 v14, 0xbfb8aa3b, v10
v_mul_f32 v15, 0xbfb8aa3b, v11
v_mul_f32 v42, 0xbfb8aa3b, v12
v_mul_f32 v43, 0xbfb8aa3b, v13
EX v14,v14
EX v15,v15
EX v42,v42
EX v43,v43
v_add_f32 v14, 1.0, v14
v_add_f32 v15, 1.0, v15
v_add_f32 v42, 1.0, v42
v_add_f32 v43, 1.0, v43
v_rcp_f32 v14, v14
v_rcp_f32 v15, v15
v_rcp_f32 v42, v42
v_rcp_f32 v43, v43
v_mul_f32 v14, v10, v14
v_mul_f32 v15, v11, v15
v_mul_f32 v42, v12, v42
v_mul_f32 v43, v13, v43
CV v8,v14,v15
CV v9,v42,v43
s_cmp_lg_u32 s82, 0
s_cbranch_scc1 LYS_%=
LYE_%=:
s_mov_b32 s73, 30001
s_mov_b32 s65, 1
s_bitcmp0_b32 s17, 0
s_cbranch_scc1 LSA0_%=
MM v[72:75],v[64:67],a[0:3],0
MM v[72:75],v[64:67],a[4:7],v[72:75]
MM v[72:75],v[68:71],a[8:11],v[72:75]
MM v[72:75],v[68:71],a[12:15],v[72:75]
s_nop 7
v_cmp_u_f32 s[30:31], v72, v72
s_nop 1
s_cmp_lg_u64 s[30:31], 0
s_cbranch_scc1 LSA0_%=
s_bitset0_b32 s17, 0
CV v76,v72,v73
CV v77,v74,v75
ds_bpermute_b32 v76, v232, v76
ds_bpermute_b32 v77, v232, v77
v_lshlrev_b32 v10, 16, v0
v_and_b32 v11, 0xffff0000, v0
v_lshlrev_b32 v12, 16, v1
v_and_b32 v13, 0xffff0000, v1
v_lshlrev_b32 v14, 16, v32
v_and_b32 v15, 0xffff0000, v32
v_lshlrev_b32 v42, 16, v33
v_and_b32 v43, 0xffff0000, v33
s_add_u32 s80, s18, 0
s_waitcnt lgkmcnt(0)
v_lshlrev_b32 v72, 16, v76
v_and_b32 v73, 0xffff0000, v76
v_lshlrev_b32 v74, 16, v77
v_and_b32 v75, 0xffff0000, v77
v_mul_f32 v72, v72, v10
v_mul_f32 v73, v73, v11
v_mul_f32 v74, v74, v12
v_mul_f32 v75, v75, v13
CV v76,v72,v73
CV v77,v74,v75
v_lshlrev_b32 v72, 16, v76
v_and_b32 v73, 0xffff0000, v76
v_lshlrev_b32 v74, 16, v77
v_and_b32 v75, 0xffff0000, v77
v_add_f32 v72, v72, v14
v_add_f32 v73, v73, v15
v_add_f32 v74, v74, v42
v_add_f32 v75, v75, v43
CV v44,v72,v73
CV v45,v74,v75
buffer_store_dwordx2 v[44:45], v251, s[40:43], s80 offen offset:0 nt
s_nop 1
LSA0_%=:
s_bitcmp0_b32 s17, 1
s_cbranch_scc1 LSA1_%=
MM v[72:75],v[64:67],a[32:35],0
MM v[72:75],v[64:67],a[36:39],v[72:75]
MM v[72:75],v[68:71],a[40:43],v[72:75]
)ASM"
R"ASM(
MM v[72:75],v[68:71],a[44:47],v[72:75]
s_nop 7
v_cmp_u_f32 s[30:31], v72, v72
s_nop 1
s_cmp_lg_u64 s[30:31], 0
s_cbranch_scc1 LSA1_%=
s_bitset0_b32 s17, 1
CV v76,v72,v73
CV v77,v74,v75
ds_bpermute_b32 v76, v232, v76
ds_bpermute_b32 v77, v232, v77
v_lshlrev_b32 v10, 16, v2
v_and_b32 v11, 0xffff0000, v2
v_lshlrev_b32 v12, 16, v3
v_and_b32 v13, 0xffff0000, v3
v_lshlrev_b32 v14, 16, v34
v_and_b32 v15, 0xffff0000, v34
v_lshlrev_b32 v42, 16, v35
v_and_b32 v43, 0xffff0000, v35
s_add_u32 s80, s18, 128
s_waitcnt lgkmcnt(0)
v_lshlrev_b32 v72, 16, v76
v_and_b32 v73, 0xffff0000, v76
v_lshlrev_b32 v74, 16, v77
v_and_b32 v75, 0xffff0000, v77
v_mul_f32 v72, v72, v10
v_mul_f32 v73, v73, v11
v_mul_f32 v74, v74, v12
v_mul_f32 v75, v75, v13
CV v76,v72,v73
CV v77,v74,v75
v_lshlrev_b32 v72, 16, v76
v_and_b32 v73, 0xffff0000, v76
v_lshlrev_b32 v74, 16, v77
v_and_b32 v75, 0xffff0000, v77
v_add_f32 v72, v72, v14
v_add_f32 v73, v73, v15
v_add_f32 v74, v74, v42
v_add_f32 v75, v75, v43
CV v46,v72,v73
CV v47,v74,v75
buffer_store_dwordx2 v[46:47], v251, s[40:43], s80 offen offset:0 nt
s_nop 1
LSA1_%=:
LSW_%=:
s_sleep 1
s_waitcnt vmcnt(0)
s_cmp_eq_u32 s65, 0
s_cbranch_scc1 LSX_%=
s_mov_b64 exec, 1
buffer_store_dword v233, off, s[20:23], s19 offset:12 sc1
s_mov_b64 exec, -1
s_mov_b32 s65, 0
LSX_%=:
v_cmp_ne_u32 vcc, s69, v243
v_cmp_ne_u32 s[36:37], s69, v143
s_nop 1
s_or_b64 vcc, vcc, s[36:37]
s_cselect_b32 s76, 0, 1
s_add_u32 s72, s72, s76
s_sub_u32 s73, s73, 1
s_cselect_b32 s72, 2, s72
s_bitcmp0_b32 s17, 4
s_cbranch_scc1 LSN4_%=
MM v[72:75],v[64:67],a[192:195],0
MM v[72:75],v[64:67],a[196:199],v[72:75]
MM v[72:75],v[68:71],v[144:147],v[72:75]
MM v[72:75],v[68:71],a[212:215],v[72:75]
AR v76,a195
AR v77,a199
v_mov_b32 v78, v147
AR v79,a215
s_nop 3
v_cmp_u_f32 s[30:31], v72, v72
s_nop 1
s_cmp_eq_u64 s[30:31], 0
s_cbranch_scc1 LSF4_%=
s_cmp_ge_u32 s72, 2
LPG25_%=:
s_cbranch_execz LTR2_%=
s_cbranch_scc1 LSF4_%=
v_cmp_eq_u32 vcc, s69, v242
v_cmp_eq_u32 s[36:37], -1, v78
s_add_u32 s66, s16, 18432
s_and_b64 vcc, vcc, s[36:37]
s_cmp_eq_u32 s72, 0
s_cselect_b64 exec, vcc, -1
s_and_b32 exec_lo, exec_lo, 0xffff0000
buffer_load_dwordx4 v[144:147], v250, s[20:23], s66 offen offset:0 sc1
s_mov_b64 exec, -1
v_cmp_eq_u32 vcc, s69, v142
v_cmp_eq_u32 s[36:37], -1, v79
s_add_u32 s66, s44, 18432
s_and_b64 vcc, vcc, s[36:37]
s_cmp_eq_u32 s72, 0
s_cselect_b64 exec, vcc, -1
buffer_load_dwordx4 a[212:215], v250, s[20:23], s66 offen offset:0 sc1
s_mov_b64 exec, -1
v_cmp_eq_u32 vcc, s69, v241
v_cmp_eq_u32 s[36:37], -1, v76
s_add_u32 s66, s16, 8192
s_and_b64 vcc, vcc, s[36:37]
s_cmp_eq_u32 s72, 0
s_cselect_b64 exec, vcc, -1
s_and_b32 exec_lo, exec_lo, 0xffff0000
buffer_load_dwordx4 a[192:195], v250, s[20:23], s66 offen offset:0 sc1
s_mov_b64 exec, -1
v_cmp_eq_u32 vcc, s69, v141
v_cmp_eq_u32 s[36:37], -1, v77
s_add_u32 s66, s44, 8192
s_and_b64 vcc, vcc, s[36:37]
s_cmp_eq_u32 s72, 0
s_cselect_b64 exec, vcc, -1
buffer_load_dwordx4 a[196:199], v250, s[20:23], s66 offen offset:0 sc1
s_mov_b64 exec, -1
s_branch LSN4_%=
LSF4_%=:
s_bitset0_b32 s17, 4
CV v76,v72,v73
CV v77,v74,v75
ds_bpermute_b32 v76, v232, v76
ds_bpermute_b32 v77, v232, v77
v_lshlrev_b32 v10, 16, v8
v_and_b32 v11, 0xffff0000, v8
v_lshlrev_b32 v12, 16, v9
v_and_b32 v13, 0xffff0000, v9
v_lshlrev_b32 v14, 16, v40
v_and_b32 v15, 0xffff0000, v40
v_lshlrev_b32 v42, 16, v41
v_and_b32 v43, 0xffff0000, v41
s_add_u32 s80, s63, 0
s_waitcnt lgkmcnt(0)
v_lshlrev_b32 v72, 16, v76
v_and_b32 v73, 0xffff0000, v76
v_lshlrev_b32 v74, 16, v77
v_and_b32 v75, 0xffff0000, v77
v_mul_f32 v72, v72, v10
v_mul_f32 v73, v73, v11
v_mul_f32 v74, v74, v12
v_mul_f32 v75, v75, v13
CV v76,v72,v73
CV v77,v74,v75
v_lshlrev_b32 v72, 16, v76
v_and_b32 v73, 0xffff0000, v76
v_lshlrev_b32 v74, 16, v77
v_and_b32 v75, 0xffff0000, v77
v_add_f32 v72, v72, v14
v_add_f32 v73, v73, v15
v_add_f32 v74, v74, v42
v_add_f32 v75, v75, v43
CV v44,v72,v73
CV v45,v74,v75
buffer_store_dwordx2 v[44:45], v254, s[40:43], s80 offen offset:0 nt
s_nop 1
LSN4_%=:
s_bitcmp0_b32 s17, 3
s_cbranch_scc1 LSN3_%=
MM v[72:75],v[64:67],v[80:83],0
MM v[72:75],v[64:67],a[100:103],v[72:75]
MM v[72:75],v[68:71],v[112:115],v[72:75]
MM v[72:75],v[68:71],a[108:111],v[72:75]
v_mov_b32 v76, v83
AR v77,a103
v_mov_b32 v78, v115
AR v79,a111
s_nop 3
v_cmp_u_f32 s[30:31], v72, v72
s_nop 1
s_cmp_eq_u64 s[30:31], 0
s_cbranch_scc1 LSF3_%=
s_cmp_ge_u32 s72, 2
s_cbranch_scc1 LSF3_%=
v_cmp_eq_u32 vcc, s69, v242
v_cmp_eq_u32 s[36:37], -1, v78
s_add_u32 s66, s16, 16384
s_and_b64 vcc, vcc, s[36:37]
s_cmp_eq_u32 s72, 0
s_cselect_b64 exec, vcc, -1
s_and_b32 exec_lo, exec_lo, 0xffff0000
buffer_load_dwordx4 v[112:115], v250, s[20:23], s66 offen offset:0 sc1
s_mov_b64 exec, -1
v_cmp_eq_u32 vcc, s69, v142
v_cmp_eq_u32 s[36:37], -1, v79
s_add_u32 s66, s44, 16384
s_and_b64 vcc, vcc, s[36:37]
s_cmp_eq_u32 s72, 0
s_cselect_b64 exec, vcc, -1
buffer_load_dwordx4 a[108:111], v250, s[20:23], s66 offen offset:0 sc1
s_mov_b64 exec, -1
v_cmp_eq_u32 vcc, s69, v242
v_cmp_eq_u32 s[36:37], -1, v76
s_add_u32 s66, s16, 14336
s_and_b64 vcc, vcc, s[36:37]
s_cmp_eq_u32 s72, 0
s_cselect_b64 exec, vcc, -1
s_and_b32 exec_lo, exec_lo, 0xffff0000
buffer_load_dwordx4 v[80:83], v250, s[20:23], s66 offen offset:0 sc1
s_mov_b64 exec, -1
v_cmp_eq_u32 vcc, s69, v142
v_cmp_eq_u32 s[36:37], -1, v77
s_add_u32 s66, s44, 14336
s_and_b64 vcc, vcc, s[36:37]
s_cmp_eq_u32 s72, 0
s_cselect_b64 exec, vcc, -1
buffer_load_dwordx4 a[100:103], v250, s[20:23], s66 offen offset:0 sc1
s_mov_b64 exec, -1
s_branch LSN3_%=
LSF3_%=:
s_bitset0_b32 s17, 3
CV v76,v72,v73
CV v77,v74,v75
ds_bpermute_b32 v76, v232, v76
ds_bpermute_b32 v77, v232, v77
v_lshlrev_b32 v10, 16, v6
v_and_b32 v11, 0xffff0000, v6
v_lshlrev_b32 v12, 16, v7
v_and_b32 v13, 0xffff0000, v7
v_lshlrev_b32 v14, 16, v38
v_and_b32 v15, 0xffff0000, v38
v_lshlrev_b32 v42, 16, v39
v_and_b32 v43, 0xffff0000, v39
s_add_u32 s80, s18, 41088
s_waitcnt lgkmcnt(0)
v_lshlrev_b32 v72, 16, v76
v_and_b32 v73, 0xffff0000, v76
v_lshlrev_b32 v74, 16, v77
v_and_b32 v75, 0xffff0000, v77
v_mul_f32 v72, v72, v10
v_mul_f32 v73, v73, v11
v_mul_f32 v74, v74, v12
v_mul_f32 v75, v75, v13
CV v76,v72,v73
CV v77,v74,v75
v_lshlrev_b32 v72, 16, v76
v_and_b32 v73, 0xffff0000, v76
v_lshlrev_b32 v74, 16, v77
v_and_b32 v75, 0xffff0000, v77
v_add_f32 v72, v72, v14
v_add_f32 v73, v73, v15
v_add_f32 v74, v74, v42
v_add_f32 v75, v75, v43
CV v46,v72,v73
CV v47,v74,v75
buffer_store_dwordx2 v[46:47], v251, s[40:43], s80 offen offset:0 nt
s_nop 1
LSN3_%=:
s_bitcmp0_b32 s17, 2
s_cbranch_scc1 LSN2_%=
MM v[72:75],v[64:67],v[16:19],0
MM v[72:75],v[64:67],a[68:71],v[72:75]
MM v[72:75],v[68:71],v[48:51],v[72:75]
MM v[72:75],v[68:71],a[76:79],v[72:75]
v_mov_b32 v76, v19
AR v77,a71
v_mov_b32 v78, v51
AR v79,a79
s_nop 3
v_cmp_u_f32 s[30:31], v72, v72
s_nop 1
s_cmp_eq_u64 s[30:31], 0
s_cbranch_scc1 LSF2_%=
s_cmp_ge_u32 s72, 2
s_cbranch_scc1 LSF2_%=
v_cmp_eq_u32 vcc, s69, v242
v_cmp_eq_u32 s[36:37], -1, v78
s_add_u32 s66, s16, 12288
s_and_b64 vcc, vcc, s[36:37]
s_cmp_eq_u32 s72, 0
s_cselect_b64 exec, vcc, -1
s_and_b32 exec_lo, exec_lo, 0xffff0000
buffer_load_dwordx4 v[48:51], v250, s[20:23], s66 offen offset:0 sc1
s_mov_b64 exec, -1
v_cmp_eq_u32 vcc, s69, v142
v_cmp_eq_u32 s[36:37], -1, v79
s_add_u32 s66, s44, 12288
s_and_b64 vcc, vcc, s[36:37]
s_cmp_eq_u32 s72, 0
s_cselect_b64 exec, vcc, -1
buffer_load_dwordx4 a[76:79], v250, s[20:23], s66 offen offset:0 sc1
s_mov_b64 exec, -1
v_cmp_eq_u32 vcc, s69, v242
v_cmp_eq_u32 s[36:37], -1, v76
s_add_u32 s66, s16, 10240
s_and_b64 vcc, vcc, s[36:37]
s_cmp_eq_u32 s72, 0
s_cselect_b64 exec, vcc, -1
s_and_b32 exec_lo, exec_lo, 0xffff0000
buffer_load_dwordx4 v[16:19], v250, s[20:23], s66 offen offset:0 sc1
s_mov_b64 exec, -1
v_cmp_eq_u32 vcc, s69, v142
v_cmp_eq_u32 s[36:37], -1, v77
s_add_u32 s66, s44, 10240
s_and_b64 vcc, vcc, s[36:37]
s_cmp_eq_u32 s72, 0
s_cselect_b64 exec, vcc, -1
buffer_load_dwordx4 a[68:71], v250, s[20:23], s66 offen offset:0 sc1
s_mov_b64 exec, -1
s_branch LSN2_%=
LSF2_%=:
s_bitset0_b32 s17, 2
CV v76,v72,v73
CV v77,v74,v75
ds_bpermute_b32 v76, v232, v76
ds_bpermute_b32 v77, v232, v77
v_lshlrev_b32 v10, 16, v4
v_and_b32 v11, 0xffff0000, v4
v_lshlrev_b32 v12, 16, v5
v_and_b32 v13, 0xffff0000, v5
v_lshlrev_b32 v14, 16, v36
v_and_b32 v15, 0xffff0000, v36
v_lshlrev_b32 v42, 16, v37
v_and_b32 v43, 0xffff0000, v37
s_add_u32 s80, s18, 40960
s_waitcnt lgkmcnt(0)
v_lshlrev_b32 v72, 16, v76
v_and_b32 v73, 0xffff0000, v76
v_lshlrev_b32 v74, 16, v77
v_and_b32 v75, 0xffff0000, v77
v_mul_f32 v72, v72, v10
v_mul_f32 v73, v73, v11
v_mul_f32 v74, v74, v12
v_mul_f32 v75, v75, v13
CV v76,v72,v73
CV v77,v74,v75
v_lshlrev_b32 v72, 16, v76
v_and_b32 v73, 0xffff0000, v76
v_lshlrev_b32 v74, 16, v77
v_and_b32 v75, 0xffff0000, v77
v_add_f32 v72, v72, v14
v_add_f32 v73, v73, v15
v_add_f32 v74, v74, v42
v_add_f32 v75, v75, v43
CV v44,v72,v73
CV v45,v74,v75
buffer_store_dwordx2 v[44:45], v251, s[40:43], s80 offen offset:0 nt
s_nop 1
LSN2_%=:
s_bitcmp0_b32 s17, 1
s_cbranch_scc1 LSN1_%=
MM v[72:75],v[64:67],a[32:35],0
MM v[72:75],v[64:67],a[36:39],v[72:75]
MM v[72:75],v[68:71],a[40:43],v[72:75]
MM v[72:75],v[68:71],a[44:47],v[72:75]
AR v76,a35
AR v77,a39
AR v78,a43
AR v79,a47
s_nop 3
v_cmp_u_f32 s[30:31], v72, v72
s_nop 1
s_cmp_eq_u64 s[30:31], 0
s_cbranch_scc1 LSF1_%=
s_cmp_ge_u32 s72, 2
s_cbranch_scc1 LSF1_%=
v_cmp_eq_u32 vcc, s69, v241
v_cmp_eq_u32 s[36:37], -1, v78
s_add_u32 s66, s16, 6144
s_and_b64 vcc, vcc, s[36:37]
s_cmp_eq_u32 s72, 0
s_cselect_b64 exec, vcc, -1
s_and_b32 exec_lo, exec_lo, 0xffff0000
buffer_load_dwordx4 a[40:43], v250, s[20:23], s66 offen offset:0 sc1
s_mov_b64 exec, -1
v_cmp_eq_u32 vcc, s69, v141
v_cmp_eq_u32 s[36:37], -1, v79
s_add_u32 s66, s44, 6144
s_and_b64 vcc, vcc, s[36:37]
s_cmp_eq_u32 s72, 0
s_cselect_b64 exec, vcc, -1
buffer_load_dwordx4 a[44:47], v250, s[20:23], s66 offen offset:0 sc1
s_mov_b64 exec, -1
v_cmp_eq_u32 vcc, s69, v241
v_cmp_eq_u32 s[36:37], -1, v76
s_add_u32 s66, s16, 4096
s_and_b64 vcc, vcc, s[36:37]
s_cmp_eq_u32 s72, 0
s_cselect_b64 exec, vcc, -1
s_and_b32 exec_lo, exec_lo, 0xffff0000
buffer_load_dwordx4 a[32:35], v250, s[20:23], s66 offen offset:0 sc1
s_mov_b64 exec, -1
v_cmp_eq_u32 vcc, s69, v141
v_cmp_eq_u32 s[36:37], -1, v77
s_add_u32 s66, s44, 4096
s_and_b64 vcc, vcc, s[36:37]
s_cmp_eq_u32 s72, 0
s_cselect_b64 exec, vcc, -1
buffer_load_dwordx4 a[36:39], v250, s[20:23], s66 offen offset:0 sc1
s_mov_b64 exec, -1
s_branch LSN1_%=
LSF1_%=:
s_bitset0_b32 s17, 1
CV v76,v72,v73
CV v77,v74,v75
ds_bpermute_b32 v76, v232, v76
ds_bpermute_b32 v77, v232, v77
v_lshlrev_b32 v10, 16, v2
v_and_b32 v11, 0xffff0000, v2
v_lshlrev_b32 v12, 16, v3
v_and_b32 v13, 0xffff0000, v3
v_lshlrev_b32 v14, 16, v34
v_and_b32 v15, 0xffff0000, v34
v_lshlrev_b32 v42, 16, v35
v_and_b32 v43, 0xffff0000, v35
s_add_u32 s80, s18, 128
s_waitcnt lgkmcnt(0)
v_lshlrev_b32 v72, 16, v76
v_and_b32 v73, 0xffff0000, v76
v_lshlrev_b32 v74, 16, v77
v_and_b32 v75, 0xffff0000, v77
v_mul_f32 v72, v72, v10
v_mul_f32 v73, v73, v11
v_mul_f32 v74, v74, v12
v_mul_f32 v75, v75, v13
CV v76,v72,v73
CV v77,v74,v75
v_lshlrev_b32 v72, 16, v76
v_and_b32 v73, 0xffff0000, v76
v_lshlrev_b32 v74, 16, v77
v_and_b32 v75, 0xffff0000, v77
v_add_f32 v72, v72, v14
v_add_f32 v73, v73, v15
v_add_f32 v74, v74, v42
v_add_f32 v75, v75, v43
CV v46,v72,v73
CV v47,v74,v75
buffer_store_dwordx2 v[46:47], v251, s[40:43], s80 offen offset:0 nt
s_nop 1
LSN1_%=:
s_bitcmp0_b32 s17, 0
s_cbranch_scc1 LSN0_%=
MM v[72:75],v[64:67],a[0:3],0
MM v[72:75],v[64:67],a[4:7],v[72:75]
MM v[72:75],v[68:71],a[8:11],v[72:75]
MM v[72:75],v[68:71],a[12:15],v[72:75]
AR v76,a3
AR v77,a7
AR v78,a11
AR v79,a15
s_nop 3
v_cmp_u_f32 s[30:31], v72, v72
s_nop 1
s_cmp_eq_u64 s[30:31], 0
s_cbranch_scc1 LSF0_%=
s_cmp_ge_u32 s72, 2
s_cbranch_scc1 LSF0_%=
v_cmp_eq_u32 vcc, s69, v240
v_cmp_eq_u32 s[36:37], -1, v78
s_add_u32 s66, s16, 2048
s_and_b64 vcc, vcc, s[36:37]
s_cmp_eq_u32 s72, 0
s_cselect_b64 exec, vcc, -1
s_and_b32 exec_lo, exec_lo, 0xffff0000
buffer_load_dwordx4 a[8:11], v250, s[20:23], s66 offen offset:0 sc1
s_mov_b64 exec, -1
v_cmp_eq_u32 vcc, s69, v140
)ASM"
R"ASM(
v_cmp_eq_u32 s[36:37], -1, v79
s_add_u32 s66, s44, 2048
s_and_b64 vcc, vcc, s[36:37]
s_cmp_eq_u32 s72, 0
s_cselect_b64 exec, vcc, -1
buffer_load_dwordx4 a[12:15], v250, s[20:23], s66 offen offset:0 sc1
s_mov_b64 exec, -1
v_cmp_eq_u32 vcc, s69, v240
v_cmp_eq_u32 s[36:37], -1, v76
s_add_u32 s66, s16, 0
s_and_b64 vcc, vcc, s[36:37]
s_cmp_eq_u32 s72, 0
s_cselect_b64 exec, vcc, -1
s_and_b32 exec_lo, exec_lo, 0xffff0000
buffer_load_dwordx4 a[0:3], v250, s[20:23], s66 offen offset:0 sc1
s_mov_b64 exec, -1
v_cmp_eq_u32 vcc, s69, v140
v_cmp_eq_u32 s[36:37], -1, v77
s_add_u32 s66, s44, 0
s_and_b64 vcc, vcc, s[36:37]
s_cmp_eq_u32 s72, 0
s_cselect_b64 exec, vcc, -1
buffer_load_dwordx4 a[4:7], v250, s[20:23], s66 offen offset:0 sc1
s_mov_b64 exec, -1
s_branch LSN0_%=
LSF0_%=:
s_bitset0_b32 s17, 0
CV v76,v72,v73
CV v77,v74,v75
ds_bpermute_b32 v76, v232, v76
ds_bpermute_b32 v77, v232, v77
v_lshlrev_b32 v10, 16, v0
v_and_b32 v11, 0xffff0000, v0
v_lshlrev_b32 v12, 16, v1
v_and_b32 v13, 0xffff0000, v1
v_lshlrev_b32 v14, 16, v32
v_and_b32 v15, 0xffff0000, v32
v_lshlrev_b32 v42, 16, v33
v_and_b32 v43, 0xffff0000, v33
s_add_u32 s80, s18, 0
s_waitcnt lgkmcnt(0)
v_lshlrev_b32 v72, 16, v76
v_and_b32 v73, 0xffff0000, v76
v_lshlrev_b32 v74, 16, v77
v_and_b32 v75, 0xffff0000, v77
v_mul_f32 v72, v72, v10
v_mul_f32 v73, v73, v11
v_mul_f32 v74, v74, v12
v_mul_f32 v75, v75, v13
CV v76,v72,v73
CV v77,v74,v75
v_lshlrev_b32 v72, 16, v76
v_and_b32 v73, 0xffff0000, v76
v_lshlrev_b32 v74, 16, v77
v_and_b32 v75, 0xffff0000, v77
v_add_f32 v72, v72, v14
v_add_f32 v73, v73, v15
v_add_f32 v74, v74, v42
v_add_f32 v75, v75, v43
CV v44,v72,v73
CV v45,v74,v75
buffer_store_dwordx2 v[44:45], v251, s[40:43], s80 offen offset:0 nt
s_nop 1
LSN0_%=:
v_cmp_ne_u32 vcc, s69, v242
v_cmp_ne_u32 s[36:37], s69, v142
s_nop 1
s_or_b64 vcc, vcc, s[36:37]
s_cselect_b32 s76, 0, 15
s_and_b32 s76, s76, s73
s_cbranch_scc1 LSU_%=
buffer_load_dwordx4 v[240:243], v253, s[20:23], s68 offen offset:0 sc1
buffer_load_dwordx4 v[140:143], v253, s[20:23], s45 offen offset:0 sc1
LSU_%=:
s_or_b32 s76, s17, s65
s_cbranch_scc1 LSW_%=
s_endpgm
LYS_%=:
s_waitcnt vmcnt(15)
LYT_%=:
v_ashrrev_i32 v139, 31, v138
v_not_b32 v139, v139
v_or_b32 v139, 0x80000000, v139
v_xor_b32 v138, v138, v139
v_and_b32 v138, 0xffff0000, v138
v_mul_f32 v138, 0xbfb8aa3b, v138
EX v138,v138
v_pk_add_f32 v[160:161], v[160:161], v[196:197]
v_pk_add_f32 v[162:163], v[162:163], v[198:199]
v_pk_add_f32 v[164:165], v[164:165], v[200:201]
v_pk_add_f32 v[166:167], v[166:167], v[202:203]
v_pk_add_f32 v[168:169], v[168:169], v[204:205]
v_pk_add_f32 v[170:171], v[170:171], v[206:207]
v_pk_add_f32 v[172:173], v[172:173], v[208:209]
v_pk_add_f32 v[174:175], v[174:175], v[210:211]
v_pk_add_f32 v[176:177], v[176:177], v[212:213]
v_pk_add_f32 v[178:179], v[178:179], v[214:215]
v_pk_add_f32 v[180:181], v[180:181], v[216:217]
v_pk_add_f32 v[182:183], v[182:183], v[218:219]
v_pk_add_f32 v[184:185], v[184:185], v[220:221]
v_pk_add_f32 v[186:187], v[186:187], v[222:223]
v_pk_add_f32 v[188:189], v[188:189], v[96:97]
v_pk_add_f32 v[190:191], v[190:191], v[98:99]
v_pk_add_f32 v[192:193], v[192:193], v[100:101]
v_pk_add_f32 v[194:195], v[194:195], v[102:103]
v_pk_add_f32 v[160:161], v[160:161], v[192:193]
v_pk_add_f32 v[162:163], v[162:163], v[194:195]
v_add_f32 v138, 1.0, v138
v_rcp_f32 v138, v138
v_pk_add_f32 v[160:161], v[160:161], v[176:177]
v_pk_add_f32 v[162:163], v[162:163], v[178:179]
v_pk_add_f32 v[164:165], v[164:165], v[180:181]
v_pk_add_f32 v[166:167], v[166:167], v[182:183]
v_pk_add_f32 v[168:169], v[168:169], v[184:185]
v_pk_add_f32 v[170:171], v[170:171], v[186:187]
v_pk_add_f32 v[172:173], v[172:173], v[188:189]
v_pk_add_f32 v[174:175], v[174:175], v[190:191]
CV v138,v138,v138
v_and_b32 v138, 0xffff0000, v138
v_pk_add_f32 v[160:161], v[160:161], v[168:169]
v_pk_add_f32 v[162:163], v[162:163], v[170:171]
v_pk_add_f32 v[164:165], v[164:165], v[172:173]
v_pk_add_f32 v[166:167], v[166:167], v[174:175]
v_pk_add_f32 v[160:161], v[160:161], v[164:165]
v_pk_add_f32 v[162:163], v[162:163], v[166:167]
CV v164,v160,v161
CV v165,v162,v163
v_lshlrev_b32 v160, 16, v164
v_and_b32 v161, 0xffff0000, v164
v_lshlrev_b32 v162, 16, v165
v_and_b32 v163, 0xffff0000, v165
v_mul_f32 v160, v160, v138
v_mul_f32 v161, v161, v138
v_mul_f32 v162, v162, v138
v_mul_f32 v163, v163, v138
CV v164,v160,v161
CV v165,v162,v163
v_lshlrev_b32 v160, 16, v164
v_and_b32 v161, 0xffff0000, v164
v_lshlrev_b32 v162, 16, v165
v_and_b32 v163, 0xffff0000, v165
v_lshlrev_b32 v168, 16, v136
v_and_b32 v169, 0xffff0000, v136
v_lshlrev_b32 v170, 16, v137
v_and_b32 v171, 0xffff0000, v137
v_add_f32 v160, v160, v168
v_add_f32 v161, v161, v169
v_add_f32 v162, v162, v170
v_add_f32 v163, v163, v171
CV v164,v160,v161
CV v165,v162,v163
v_lshlrev_b32 v160, 16, v164
v_and_b32 v161, 0xffff0000, v164
v_lshlrev_b32 v162, 16, v165
v_and_b32 v163, 0xffff0000, v165
v_mul_f32 v168, 0xbfb8aa3b, v160
v_mul_f32 v169, 0xbfb8aa3b, v161
v_mul_f32 v170, 0xbfb8aa3b, v162
v_mul_f32 v171, 0xbfb8aa3b, v163
EX v168,v168
EX v169,v169
EX v170,v170
EX v171,v171
v_bfe_u32 v139, v248, 2, 6
v_and_b32 v132, 28, v139
v_add_f32 v168, 1.0, v168
v_add_f32 v169, 1.0, v169
v_add_f32 v170, 1.0, v170
v_add_f32 v171, 1.0, v171
v_rcp_f32 v168, v168
v_rcp_f32 v169, v169
v_rcp_f32 v170, v170
v_rcp_f32 v171, v171
s_and_b32 s77, s84, 1
s_lshl_b32 s76, s77, 5
v_add_u32 v139, s76, v139
s_xor_b32 s77, s77, 1
s_lshl_b32 s77, s77, 7
v_add_u32 v132, s77, v132
v_mul_f32 v168, v160, v168
v_mul_f32 v169, v161, v169
v_mul_f32 v170, v162, v170
v_mul_f32 v171, v163, v171
CV v172,v168,v169
CV v173,v170,v171
ds_bpermute_b32 v176, v139, v172 offset:0
ds_bpermute_b32 v177, v139, v173 offset:0
ds_bpermute_b32 v180, v139, v172 offset:64
ds_bpermute_b32 v181, v139, v173 offset:64
ds_bpermute_b32 v184, v132, v172 offset:0
ds_bpermute_b32 v185, v132, v173 offset:0
s_ff1_i32_b32 s76, s82
s_bitset0_b32 s82, s76
s_and_b32 s77, s79, 3
s_lshl_b32 s77, s77, 4
s_and_b32 s76, s76, 8
s_waitcnt lgkmcnt(0)
s_lshl_b64 vcc, 0xffff, s77
s_cmp_eq_u32 s76, 0
s_cselect_b64 exec, vcc, 0
v_mov_b32 v0, v176
v_mov_b32 v1, v177
v_mov_b32 v2, v180
v_mov_b32 v3, v181
s_cselect_b64 exec, 0, vcc
v_mov_b32 v4, v176
v_mov_b32 v5, v177
v_mov_b32 v6, v180
v_mov_b32 v7, v181
s_add_u32 s77, s77, s76
s_lshl_b64 exec, 0xff, s77
v_mov_b32 v8, v184
v_mov_b32 v9, v185
s_mov_b64 exec, -1
s_cmp_eq_u32 s82, 0
s_cbranch_scc1 LYE_%=
s_ff1_i32_b32 s76, s82
s_and_b32 s76, s76, 7
s_mov_b32 s79, s52
s_cmp_eq_u32 s76, 1
s_cselect_b32 s79, s53, s79
s_cmp_eq_u32 s76, 2
s_cselect_b32 s79, s54, s79
s_cmp_eq_u32 s76, 3
s_cselect_b32 s79, s55, s79
s_cmp_eq_u32 s76, 4
s_cselect_b32 s79, s56, s79
s_cmp_eq_u32 s76, 5
s_cselect_b32 s79, s57, s79
s_cmp_eq_u32 s76, 6
s_cselect_b32 s79, s58, s79
s_cmp_eq_u32 s76, 7
s_cselect_b32 s79, s59, s79
s_mul_i32 s76, s76, 2560
s_mul_i32 s90, s84, 640
s_add_u32 s76, s76, s90
s_add_u32 s76, s76, 21659648
s_lshl_b32 s90, s79, 2
s_add_u32 s90, s90, 53248
buffer_load_dword v138, off, s[20:23], s90 sc1
s_mul_i32 s90, s79, 1280
s_mul_i32 s77, s84, 320
s_add_u32 s90, s90, s77
s_add_u32 s90, s90, 131072
v_lshrrev_b32 v139, 1, v248
s_mov_b32 exec_lo, -1
s_mov_b32 exec_hi, 255
buffer_load_dwordx2 v[136:137], v139, s[20:23], s90 offen offset:0 sc1
s_mov_b32 exec_lo, -1
s_mov_b32 exec_hi, 255
buffer_load_dwordx4 v[160:163], v248, s[20:23], s76 offen offset:0 sc1
s_add_u32 s76, s76, 20480
buffer_load_dwordx4 v[164:167], v248, s[20:23], s76 offen offset:0 sc1
s_add_u32 s76, s76, 20480
buffer_load_dwordx4 v[168:171], v248, s[20:23], s76 offen offset:0 sc1
s_add_u32 s76, s76, 20480
buffer_load_dwordx4 v[172:175], v248, s[20:23], s76 offen offset:0 sc1
s_add_u32 s76, s76, 20480
buffer_load_dwordx4 v[176:179], v248, s[20:23], s76 offen offset:0 sc1
s_add_u32 s76, s76, 20480
buffer_load_dwordx4 v[180:183], v248, s[20:23], s76 offen offset:0 sc1
s_add_u32 s76, s76, 20480
buffer_load_dwordx4 v[184:187], v248, s[20:23], s76 offen offset:0 sc1
s_add_u32 s76, s76, 20480
buffer_load_dwordx4 v[188:191], v248, s[20:23], s76 offen offset:0 sc1
s_add_u32 s76, s76, 20480
buffer_load_dwordx4 v[192:195], v248, s[20:23], s76 offen offset:0 sc1
s_add_u32 s76, s76, 20480
buffer_load_dwordx4 v[196:199], v248, s[20:23], s76 offen offset:0 sc1
s_add_u32 s76, s76, 20480
buffer_load_dwordx4 v[200:203], v248, s[20:23], s76 offen offset:0 sc1
s_add_u32 s76, s76, 20480
buffer_load_dwordx4 v[204:207], v248, s[20:23], s76 offen offset:0 sc1
s_add_u32 s76, s76, 20480
buffer_load_dwordx4 v[208:211], v248, s[20:23], s76 offen offset:0 sc1
s_add_u32 s76, s76, 20480
buffer_load_dwordx4 v[212:215], v248, s[20:23], s76 offen offset:0 sc1
s_add_u32 s76, s76, 20480
buffer_load_dwordx4 v[216:219], v248, s[20:23], s76 offen offset:0 sc1
s_add_u32 s76, s76, 20480
buffer_load_dwordx4 v[220:223], v248, s[20:23], s76 offen offset:0 sc1
s_add_u32 s76, s76, 20480
buffer_load_dwordx4 v[96:99], v248, s[20:23], s76 offen offset:0 sc1
s_add_u32 s76, s76, 20480
buffer_load_dwordx4 v[100:103], v248, s[20:23], s76 offen offset:0 sc1
s_mov_b64 exec, -1
s_waitcnt vmcnt(0)
s_branch LYT_%=
LRG1o_%=:
s_mov_b32 s70, 30001
LRG1ol_%=:
s_sleep 2
buffer_load_dwordx4 v[200:203], v208, s[20:23], s93 offen offset:0 sc1
buffer_load_dwordx4 v[204:207], v208, s[20:23], s93 offen offset:256 sc1
s_waitcnt vmcnt(0)
v_xor_b32 v201, s69, v201
v_xor_b32 v203, s69, v203
v_xor_b32 v205, s69, v205
v_xor_b32 v207, s69, v207
v_or3_b32 v209, v201, v203, v205
v_or_b32 v209, v209, v207
v_cmp_ne_u32 vcc, 0, v209
s_cbranch_vccz LRG1ob_%=
s_sub_u32 s70, s70, 1
s_cbranch_scc0 LRG1ol_%=
s_branch LRG1ob_%=
LRFR_%=:
s_mov_b32 s91, 30001
LRFRl_%=:
s_sleep 2
buffer_load_dwordx4 v[48:51], v248, s[20:23], s90 offen offset:0 sc1
s_waitcnt vmcnt(0)
v_xor_b32 v48, s69, v48
v_xor_b32 v49, s69, v49
v_xor_b32 v50, s69, v50
v_xor_b32 v51, s69, v51
v_or3_b32 v52, v48, v49, v50
v_or_b32 v52, v52, v51
v_cmp_ne_u32 vcc, 0, v52
s_cbranch_vccz LRFRb_%=
s_sub_u32 s91, s91, 1
s_cbranch_scc0 LRFRl_%=
s_branch LRFRb_%=
LRFG_%=:
s_mov_b32 s91, 30001
LRFGl_%=:
s_sleep 2
buffer_load_dword v252, v253, s[20:23], s76 offen offset:0 sc1
s_waitcnt vmcnt(0)
v_cmp_ne_u32 vcc, s69, v252
s_cbranch_vccz LRFGb_%=
s_sub_u32 s91, s91, 1
s_cbranch_scc0 LRFGl_%=
s_branch LRFGb_%=
LRFH_%=:
s_mov_b32 s91, 30001
LRFHl_%=:
s_sleep 2
buffer_load_dword v252, v253, s[20:23], s76 offen offset:0 sc1
s_waitcnt vmcnt(0)
v_cmp_ne_u32 vcc, s69, v252
s_cbranch_vccz LRFHb_%=
s_sub_u32 s91, s91, 1
s_cbranch_scc0 LRFHl_%=
s_branch LRFHb_%=
LRPO0_%=:
s_mov_b32 s97, 30001
LRPO0l_%=:
s_sleep 2
buffer_load_dword v242, v243, s[20:23], s96 offen offset:0 sc1
s_waitcnt vmcnt(0)
v_cmp_ne_u32 vcc, s69, v242
s_cbranch_vccz LRPO0b_%=
s_sub_u32 s97, s97, 1
s_cbranch_scc0 LRPO0l_%=
s_branch LRPO0b_%=
LRPO1_%=:
s_mov_b32 s97, 30001
LRPO1l_%=:
s_sleep 2
buffer_load_dword v242, v243, s[20:23], s96 offen offset:16 sc1
s_waitcnt vmcnt(0)
v_cmp_ne_u32 vcc, s69, v242
s_cbranch_vccz LRPO1b_%=
s_sub_u32 s97, s97, 1
s_cbranch_scc0 LRPO1l_%=
s_branch LRPO1b_%=
LRYF_%=:
s_mov_b32 s91, 30001
LRYFl_%=:
s_sleep 2
buffer_load_dword v132, v135, s[20:23], s78 offen offset:0 sc1
buffer_load_dword v133, v135, s[20:23], s78 offen offset:960 sc1
buffer_load_dword v134, v135, s[20:23], s78 offen offset:1920 sc1
s_waitcnt vmcnt(0)
v_xor_b32 v132, s69, v132
v_xor_b32 v133, s69, v133
v_xor_b32 v134, s69, v134
v_or3_b32 v139, v132, v133, v134
v_cmp_ne_u32 vcc, 0, v139
s_cbranch_vccz LRYFb_%=
s_sub_u32 s91, s91, 1
s_cbranch_scc0 LRYFl_%=
s_branch LRYFb_%=
LKEND_ka:
LSITE0_%=:
s_mov_b32 s73, 0
s_mov_b32 s74, 0
s_mov_b32 s75, 16384
s_branch LRARE002_%=
LSITE2_%=:
s_mov_b32 s73, 2
s_mov_b32 s74, 32768
s_mov_b32 s75, 49152
s_branch LRARE002_%=
LRARE002_%=:
s_nop 15
s_waitcnt vmcnt(0) lgkmcnt(0)
v_add_u32 v249, s74, v224
RD a[192:195],v249 offset:0
RD a[208:211],v249 offset:4096
RD a[224:227],v249 offset:8192
RD a[240:243],v249 offset:12288
v_add_u32 v250, s74, v225
RD a[196:199],v250 offset:0
RD a[212:215],v250 offset:4096
RD a[228:231],v250 offset:8192
RD a[244:247],v250 offset:12288
v_add_u32 v251, s74, v226
RD a[200:203],v251 offset:0
)ASM"
R"ASM(
RD a[216:219],v251 offset:4096
RD a[232:235],v251 offset:8192
RD a[248:251],v251 offset:12288
v_add_u32 v254, s74, v227
RD a[204:207],v254 offset:0
RD a[220:223],v254 offset:4096
RD a[236:239],v254 offset:8192
RD a[252:255],v254 offset:12288
s_waitcnt lgkmcnt(0)
MM v[0:3],a[192:195],v[160:163],v[128:131]
MM v[0:3],a[196:199],v[176:179],v[0:3]
MM v[0:3],a[200:203],v[192:195],v[0:3]
MM v[0:3],a[204:207],v[208:211],v[0:3]
MM v[4:7],a[220:223],v[208:211],v[128:131]
MM v[4:7],a[216:219],v[192:195],v[4:7]
MM v[4:7],a[212:215],v[176:179],v[4:7]
MM v[4:7],a[208:211],v[160:163],v[4:7]
MM v[12:15],a[208:211],v[164:167],v[132:135]
MM v[12:15],a[212:215],v[180:183],v[12:15]
MM v[12:15],a[216:219],v[196:199],v[12:15]
MM v[12:15],a[220:223],v[212:215],v[12:15]
MM v[8:11],a[204:207],v[212:215],v[132:135]
MM v[8:11],a[200:203],v[196:199],v[8:11]
MM v[8:11],a[196:199],v[180:183],v[8:11]
MM v[8:11],a[192:195],v[164:167],v[8:11]
MM v[32:35],a[224:227],v[160:163],v[128:131]
MM v[32:35],a[228:231],v[176:179],v[32:35]
MM v[32:35],a[232:235],v[192:195],v[32:35]
MM v[32:35],a[236:239],v[208:211],v[32:35]
MM v[36:39],a[252:255],v[208:211],v[128:131]
MM v[36:39],a[248:251],v[192:195],v[36:39]
MM v[36:39],a[244:247],v[176:179],v[36:39]
MM v[36:39],a[240:243],v[160:163],v[36:39]
MM v[44:47],a[240:243],v[164:167],v[132:135]
MM v[44:47],a[244:247],v[180:183],v[44:47]
MM v[44:47],a[248:251],v[196:199],v[44:47]
MM v[44:47],a[252:255],v[212:215],v[44:47]
MM v[40:43],a[236:239],v[212:215],v[132:135]
MM v[40:43],a[232:235],v[196:199],v[40:43]
MM v[40:43],a[228:231],v[180:183],v[40:43]
MM v[40:43],a[224:227],v[164:167],v[40:43]
s_nop 15
v_add_u32 v249, s75, v224
RD a[192:195],v249 offset:0
RD a[208:211],v249 offset:4096
RD a[224:227],v249 offset:8192
RD a[240:243],v249 offset:12288
v_add_u32 v250, s75, v225
RD a[196:199],v250 offset:0
RD a[212:215],v250 offset:4096
RD a[228:231],v250 offset:8192
RD a[244:247],v250 offset:12288
v_add_u32 v251, s75, v226
RD a[200:203],v251 offset:0
RD a[216:219],v251 offset:4096
RD a[232:235],v251 offset:8192
RD a[248:251],v251 offset:12288
v_add_u32 v254, s75, v227
RD a[204:207],v254 offset:0
RD a[220:223],v254 offset:4096
RD a[236:239],v254 offset:8192
RD a[252:255],v254 offset:12288
s_waitcnt lgkmcnt(0)
v_mov_b32 v236, v247
v_mov_b32 v237, 0
v_max3_f32 v249, v0, v1, v2
v_max3_f32 v251, v32, v33, v34
v_max3_f32 v249, v249, v3, v4
v_max3_f32 v251, v251, v35, v36
v_max3_f32 v249, v249, v5, v6
v_max3_f32 v251, v251, v37, v38
v_max3_f32 v249, v249, v7, v39
v_max_f32 v249, v249, v251
v_log_f32 v250, v247
s_lshl_b64 exec, 0xffff, 0
v_max_f32 v249, v249, v250
s_mov_b64 exec, -1
v_mov_b32 v251, v249
s_nop 1
v_permlane16_swap_b32 v249, v251
v_max_f32 v249, v249, v251
v_mov_b32 v251, v249
s_nop 1
v_permlane32_swap_b32 v249, v251
v_max_f32 v249, v249, v251
v_max_f32 v249, 0, v249
v_sub_f32 v250, 0, v249
EX v250,v250
v_sub_f32 v128, v128, v249
v_sub_f32 v129, v129, v249
v_sub_f32 v130, v130, v249
v_sub_f32 v131, v131, v249
v_sub_f32 v64, v64, v249
v_sub_f32 v0, v0, v249
v_sub_f32 v65, v65, v249
v_sub_f32 v1, v1, v249
v_sub_f32 v66, v66, v249
v_sub_f32 v2, v2, v249
v_sub_f32 v67, v67, v249
v_sub_f32 v3, v3, v249
v_sub_f32 v68, v68, v249
v_sub_f32 v4, v4, v249
v_sub_f32 v69, v69, v249
v_sub_f32 v5, v5, v249
v_sub_f32 v70, v70, v249
v_sub_f32 v6, v6, v249
v_sub_f32 v71, v71, v249
v_sub_f32 v7, v7, v249
v_sub_f32 v96, v96, v249
v_sub_f32 v32, v32, v249
v_sub_f32 v97, v97, v249
v_sub_f32 v33, v33, v249
v_sub_f32 v98, v98, v249
v_sub_f32 v34, v34, v249
v_sub_f32 v99, v99, v249
v_sub_f32 v35, v35, v249
v_sub_f32 v100, v100, v249
v_sub_f32 v36, v36, v249
v_sub_f32 v101, v101, v249
v_sub_f32 v37, v37, v249
v_sub_f32 v102, v102, v249
v_sub_f32 v38, v38, v249
v_sub_f32 v103, v103, v249
v_sub_f32 v39, v39, v249
s_lshl_b64 exec, 0xffff, 0
v_mul_f32 v236, v236, v250
s_mov_b64 exec, -1
AR v254,a0
v_mul_f32 v254, v254, v250
AW a0,v254
AR v251,a1
v_mul_f32 v251, v251, v250
AW a1,v251
AR v254,a2
v_mul_f32 v254, v254, v250
AW a2,v254
AR v251,a3
v_mul_f32 v251, v251, v250
AW a3,v251
AR v254,a4
v_mul_f32 v254, v254, v250
AW a4,v254
AR v251,a5
v_mul_f32 v251, v251, v250
AW a5,v251
AR v254,a6
v_mul_f32 v254, v254, v250
AW a6,v254
AR v251,a7
v_mul_f32 v251, v251, v250
AW a7,v251
AR v254,a32
v_mul_f32 v254, v254, v250
AW a32,v254
AR v251,a33
v_mul_f32 v251, v251, v250
AW a33,v251
AR v254,a34
v_mul_f32 v254, v254, v250
AW a34,v254
AR v251,a35
v_mul_f32 v251, v251, v250
AW a35,v251
AR v254,a36
v_mul_f32 v254, v254, v250
AW a36,v254
AR v251,a37
v_mul_f32 v251, v251, v250
AW a37,v251
AR v254,a38
v_mul_f32 v254, v254, v250
AW a38,v254
AR v251,a39
v_mul_f32 v251, v251, v250
AW a39,v251
AR v254,a64
v_mul_f32 v254, v254, v250
AW a64,v254
AR v251,a65
v_mul_f32 v251, v251, v250
AW a65,v251
AR v254,a66
v_mul_f32 v254, v254, v250
AW a66,v254
AR v251,a67
v_mul_f32 v251, v251, v250
AW a67,v251
AR v254,a68
v_mul_f32 v254, v254, v250
AW a68,v254
AR v251,a69
v_mul_f32 v251, v251, v250
AW a69,v251
AR v254,a70
v_mul_f32 v254, v254, v250
AW a70,v254
AR v251,a71
v_mul_f32 v251, v251, v250
AW a71,v251
AR v254,a96
v_mul_f32 v254, v254, v250
AW a96,v254
AR v251,a97
v_mul_f32 v251, v251, v250
AW a97,v251
AR v254,a98
v_mul_f32 v254, v254, v250
AW a98,v254
AR v251,a99
v_mul_f32 v251, v251, v250
AW a99,v251
AR v254,a100
v_mul_f32 v254, v254, v250
AW a100,v254
AR v251,a101
v_mul_f32 v251, v251, v250
AW a101,v251
AR v254,a102
v_mul_f32 v254, v254, v250
AW a102,v254
AR v251,a103
v_mul_f32 v251, v251, v250
AW a103,v251
v_max3_f32 v249, v8, v9, v10
v_max3_f32 v251, v40, v41, v42
v_max3_f32 v249, v249, v11, v12
v_max3_f32 v251, v251, v43, v44
v_max3_f32 v249, v249, v13, v14
v_max3_f32 v251, v251, v45, v46
v_max3_f32 v249, v249, v15, v47
v_max_f32 v249, v249, v251
v_log_f32 v250, v247
s_lshl_b64 exec, 0xffff, 16
v_max_f32 v249, v249, v250
s_mov_b64 exec, -1
v_mov_b32 v251, v249
s_nop 1
v_permlane16_swap_b32 v249, v251
v_max_f32 v249, v249, v251
v_mov_b32 v251, v249
s_nop 1
v_permlane32_swap_b32 v249, v251
v_max_f32 v249, v249, v251
v_max_f32 v249, 0, v249
v_sub_f32 v250, 0, v249
EX v250,v250
v_sub_f32 v132, v132, v249
v_sub_f32 v133, v133, v249
v_sub_f32 v134, v134, v249
v_sub_f32 v135, v135, v249
v_sub_f32 v72, v72, v249
v_sub_f32 v8, v8, v249
v_sub_f32 v73, v73, v249
v_sub_f32 v9, v9, v249
v_sub_f32 v74, v74, v249
v_sub_f32 v10, v10, v249
v_sub_f32 v75, v75, v249
v_sub_f32 v11, v11, v249
v_sub_f32 v76, v76, v249
v_sub_f32 v12, v12, v249
v_sub_f32 v77, v77, v249
v_sub_f32 v13, v13, v249
v_sub_f32 v78, v78, v249
v_sub_f32 v14, v14, v249
v_sub_f32 v79, v79, v249
v_sub_f32 v15, v15, v249
v_sub_f32 v104, v104, v249
v_sub_f32 v40, v40, v249
v_sub_f32 v105, v105, v249
v_sub_f32 v41, v41, v249
v_sub_f32 v106, v106, v249
v_sub_f32 v42, v42, v249
v_sub_f32 v107, v107, v249
v_sub_f32 v43, v43, v249
v_sub_f32 v108, v108, v249
v_sub_f32 v44, v44, v249
v_sub_f32 v109, v109, v249
v_sub_f32 v45, v45, v249
v_sub_f32 v110, v110, v249
v_sub_f32 v46, v46, v249
v_sub_f32 v111, v111, v249
v_sub_f32 v47, v47, v249
s_lshl_b64 exec, 0xffff, 16
v_mul_f32 v236, v236, v250
s_mov_b64 exec, -1
AR v254,a8
v_mul_f32 v254, v254, v250
AW a8,v254
AR v251,a9
v_mul_f32 v251, v251, v250
AW a9,v251
AR v254,a10
v_mul_f32 v254, v254, v250
AW a10,v254
AR v251,a11
v_mul_f32 v251, v251, v250
AW a11,v251
AR v254,a12
v_mul_f32 v254, v254, v250
AW a12,v254
AR v251,a13
v_mul_f32 v251, v251, v250
AW a13,v251
AR v254,a14
v_mul_f32 v254, v254, v250
AW a14,v254
AR v251,a15
v_mul_f32 v251, v251, v250
AW a15,v251
AR v254,a40
v_mul_f32 v254, v254, v250
AW a40,v254
AR v251,a41
v_mul_f32 v251, v251, v250
AW a41,v251
AR v254,a42
v_mul_f32 v254, v254, v250
AW a42,v254
AR v251,a43
v_mul_f32 v251, v251, v250
AW a43,v251
AR v254,a44
v_mul_f32 v254, v254, v250
AW a44,v254
AR v251,a45
v_mul_f32 v251, v251, v250
AW a45,v251
AR v254,a46
v_mul_f32 v254, v254, v250
AW a46,v254
AR v251,a47
v_mul_f32 v251, v251, v250
AW a47,v251
AR v254,a72
v_mul_f32 v254, v254, v250
AW a72,v254
AR v251,a73
v_mul_f32 v251, v251, v250
AW a73,v251
AR v254,a74
v_mul_f32 v254, v254, v250
AW a74,v254
AR v251,a75
v_mul_f32 v251, v251, v250
AW a75,v251
AR v254,a76
v_mul_f32 v254, v254, v250
AW a76,v254
AR v251,a77
v_mul_f32 v251, v251, v250
AW a77,v251
AR v254,a78
v_mul_f32 v254, v254, v250
AW a78,v254
AR v251,a79
v_mul_f32 v251, v251, v250
AW a79,v251
AR v254,a104
v_mul_f32 v254, v254, v250
AW a104,v254
AR v251,a105
v_mul_f32 v251, v251, v250
AW a105,v251
AR v254,a106
v_mul_f32 v254, v254, v250
AW a106,v254
AR v251,a107
v_mul_f32 v251, v251, v250
AW a107,v251
AR v254,a108
v_mul_f32 v254, v254, v250
AW a108,v254
AR v251,a109
v_mul_f32 v251, v251, v250
AW a109,v251
AR v254,a110
v_mul_f32 v254, v254, v250
AW a110,v254
AR v251,a111
v_mul_f32 v251, v251, v250
AW a111,v251
EX v0,v0
EX v1,v1
EX v2,v2
EX v3,v3
EX v4,v4
EX v5,v5
EX v6,v6
EX v7,v7
EX v8,v8
EX v9,v9
EX v10,v10
EX v11,v11
EX v12,v12
EX v13,v13
EX v14,v14
EX v15,v15
EX v32,v32
EX v33,v33
EX v34,v34
EX v35,v35
EX v36,v36
EX v37,v37
EX v38,v38
EX v39,v39
EX v40,v40
EX v41,v41
EX v42,v42
EX v43,v43
EX v44,v44
EX v45,v45
EX v46,v46
EX v47,v47
CV v0,v0,v1
CV v1,v2,v3
CV v2,v4,v5
CV v3,v6,v7
CV v8,v8,v9
CV v9,v10,v11
CV v10,v12,v13
CV v11,v14,v15
CV v4,v32,v33
CV v5,v34,v35
CV v6,v36,v37
CV v7,v38,v39
CV v12,v40,v41
CV v13,v42,v43
CV v14,v44,v45
CV v15,v46,v47
v_smfmac_f32_16x16x64_bf16 v[236:239], v[144:147], v[0:7], v234
s_nop 0
v_smfmac_f32_16x16x64_bf16 v[236:239], v[148:151], v[8:15], v234
s_nop 9
v_add_f32 v246, v236, v237
s_cmp_eq_u32 s73, 0
s_cbranch_scc1 LBACK0_%=
s_cmp_eq_u32 s73, 2
s_cbranch_scc1 LBACK2_%=
s_endpgm
LSITE1_%=:
s_mov_b32 s73, 1
s_mov_b32 s74, 16384
s_mov_b32 s75, 32768
s_branch LRARE102_%=
LRARE102_%=:
s_nop 15
s_waitcnt vmcnt(0) lgkmcnt(0)
v_add_u32 v249, s74, v224
RD a[192:195],v249 offset:0
RD a[208:211],v249 offset:4096
RD a[224:227],v249 offset:8192
RD a[240:243],v249 offset:12288
v_add_u32 v250, s74, v225
RD a[196:199],v250 offset:0
RD a[212:215],v250 offset:4096
RD a[228:231],v250 offset:8192
RD a[244:247],v250 offset:12288
v_add_u32 v251, s74, v226
RD a[200:203],v251 offset:0
RD a[216:219],v251 offset:4096
RD a[232:235],v251 offset:8192
RD a[248:251],v251 offset:12288
v_add_u32 v254, s74, v227
RD a[204:207],v254 offset:0
RD a[220:223],v254 offset:4096
RD a[236:239],v254 offset:8192
RD a[252:255],v254 offset:12288
s_waitcnt lgkmcnt(0)
MM v[64:67],a[192:195],v[160:163],v[128:131]
MM v[64:67],a[196:199],v[176:179],v[64:67]
MM v[64:67],a[200:203],v[192:195],v[64:67]
MM v[64:67],a[204:207],v[208:211],v[64:67]
MM v[68:71],a[220:223],v[208:211],v[128:131]
MM v[68:71],a[216:219],v[192:195],v[68:71]
MM v[68:71],a[212:215],v[176:179],v[68:71]
MM v[68:71],a[208:211],v[160:163],v[68:71]
MM v[76:79],a[208:211],v[164:167],v[132:135]
MM v[76:79],a[212:215],v[180:183],v[76:79]
MM v[76:79],a[216:219],v[196:199],v[76:79]
MM v[76:79],a[220:223],v[212:215],v[76:79]
MM v[72:75],a[204:207],v[212:215],v[132:135]
MM v[72:75],a[200:203],v[196:199],v[72:75]
MM v[72:75],a[196:199],v[180:183],v[72:75]
MM v[72:75],a[192:195],v[164:167],v[72:75]
MM v[96:99],a[224:227],v[160:163],v[128:131]
MM v[96:99],a[228:231],v[176:179],v[96:99]
MM v[96:99],a[232:235],v[192:195],v[96:99]
MM v[96:99],a[236:239],v[208:211],v[96:99]
MM v[100:103],a[252:255],v[208:211],v[128:131]
MM v[100:103],a[248:251],v[192:195],v[100:103]
MM v[100:103],a[244:247],v[176:179],v[100:103]
MM v[100:103],a[240:243],v[160:163],v[100:103]
MM v[108:111],a[240:243],v[164:167],v[132:135]
MM v[108:111],a[244:247],v[180:183],v[108:111]
MM v[108:111],a[248:251],v[196:199],v[108:111]
MM v[108:111],a[252:255],v[212:215],v[108:111]
MM v[104:107],a[236:239],v[212:215],v[132:135]
MM v[104:107],a[232:235],v[196:199],v[104:107]
MM v[104:107],a[228:231],v[180:183],v[104:107]
MM v[104:107],a[224:227],v[164:167],v[104:107]
s_nop 15
v_add_u32 v249, s75, v224
RD a[192:195],v249 offset:0
RD a[208:211],v249 offset:4096
RD a[224:227],v249 offset:8192
RD a[240:243],v249 offset:12288
v_add_u32 v250, s75, v225
RD a[196:199],v250 offset:0
RD a[212:215],v250 offset:4096
RD a[228:231],v250 offset:8192
RD a[244:247],v250 offset:12288
v_add_u32 v251, s75, v226
)ASM"
R"ASM(
RD a[200:203],v251 offset:0
RD a[216:219],v251 offset:4096
RD a[232:235],v251 offset:8192
RD a[248:251],v251 offset:12288
v_add_u32 v254, s75, v227
RD a[204:207],v254 offset:0
RD a[220:223],v254 offset:4096
RD a[236:239],v254 offset:8192
RD a[252:255],v254 offset:12288
s_waitcnt lgkmcnt(0)
v_mov_b32 v236, v246
v_mov_b32 v237, 0
v_max3_f32 v249, v64, v65, v66
v_max3_f32 v251, v96, v97, v98
v_max3_f32 v249, v249, v67, v68
v_max3_f32 v251, v251, v99, v100
v_max3_f32 v249, v249, v69, v70
v_max3_f32 v251, v251, v101, v102
v_max3_f32 v249, v249, v71, v103
v_max_f32 v249, v249, v251
v_log_f32 v250, v246
s_lshl_b64 exec, 0xffff, 0
v_max_f32 v249, v249, v250
s_mov_b64 exec, -1
v_mov_b32 v251, v249
s_nop 1
v_permlane16_swap_b32 v249, v251
v_max_f32 v249, v249, v251
v_mov_b32 v251, v249
s_nop 1
v_permlane32_swap_b32 v249, v251
v_max_f32 v249, v249, v251
v_max_f32 v249, 0, v249
v_sub_f32 v250, 0, v249
EX v250,v250
v_sub_f32 v128, v128, v249
v_sub_f32 v129, v129, v249
v_sub_f32 v130, v130, v249
v_sub_f32 v131, v131, v249
v_sub_f32 v0, v0, v249
v_sub_f32 v64, v64, v249
v_sub_f32 v1, v1, v249
v_sub_f32 v65, v65, v249
v_sub_f32 v2, v2, v249
v_sub_f32 v66, v66, v249
v_sub_f32 v3, v3, v249
v_sub_f32 v67, v67, v249
v_sub_f32 v4, v4, v249
v_sub_f32 v68, v68, v249
v_sub_f32 v5, v5, v249
v_sub_f32 v69, v69, v249
v_sub_f32 v6, v6, v249
v_sub_f32 v70, v70, v249
v_sub_f32 v7, v7, v249
v_sub_f32 v71, v71, v249
v_sub_f32 v32, v32, v249
v_sub_f32 v96, v96, v249
v_sub_f32 v33, v33, v249
v_sub_f32 v97, v97, v249
v_sub_f32 v34, v34, v249
v_sub_f32 v98, v98, v249
v_sub_f32 v35, v35, v249
v_sub_f32 v99, v99, v249
v_sub_f32 v36, v36, v249
v_sub_f32 v100, v100, v249
v_sub_f32 v37, v37, v249
v_sub_f32 v101, v101, v249
v_sub_f32 v38, v38, v249
v_sub_f32 v102, v102, v249
v_sub_f32 v39, v39, v249
v_sub_f32 v103, v103, v249
s_lshl_b64 exec, 0xffff, 0
v_mul_f32 v236, v236, v250
s_mov_b64 exec, -1
AR v254,a0
v_mul_f32 v254, v254, v250
AW a0,v254
AR v251,a1
v_mul_f32 v251, v251, v250
AW a1,v251
AR v254,a2
v_mul_f32 v254, v254, v250
AW a2,v254
AR v251,a3
v_mul_f32 v251, v251, v250
AW a3,v251
AR v254,a4
v_mul_f32 v254, v254, v250
AW a4,v254
AR v251,a5
v_mul_f32 v251, v251, v250
AW a5,v251
AR v254,a6
v_mul_f32 v254, v254, v250
AW a6,v254
AR v251,a7
v_mul_f32 v251, v251, v250
AW a7,v251
AR v254,a32
v_mul_f32 v254, v254, v250
AW a32,v254
AR v251,a33
v_mul_f32 v251, v251, v250
AW a33,v251
AR v254,a34
v_mul_f32 v254, v254, v250
AW a34,v254
AR v251,a35
v_mul_f32 v251, v251, v250
AW a35,v251
AR v254,a36
v_mul_f32 v254, v254, v250
AW a36,v254
AR v251,a37
v_mul_f32 v251, v251, v250
AW a37,v251
AR v254,a38
v_mul_f32 v254, v254, v250
AW a38,v254
AR v251,a39
v_mul_f32 v251, v251, v250
AW a39,v251
AR v254,a64
v_mul_f32 v254, v254, v250
AW a64,v254
AR v251,a65
v_mul_f32 v251, v251, v250
AW a65,v251
AR v254,a66
v_mul_f32 v254, v254, v250
AW a66,v254
AR v251,a67
v_mul_f32 v251, v251, v250
AW a67,v251
AR v254,a68
v_mul_f32 v254, v254, v250
AW a68,v254
AR v251,a69
v_mul_f32 v251, v251, v250
AW a69,v251
AR v254,a70
v_mul_f32 v254, v254, v250
AW a70,v254
AR v251,a71
v_mul_f32 v251, v251, v250
AW a71,v251
AR v254,a96
v_mul_f32 v254, v254, v250
AW a96,v254
AR v251,a97
v_mul_f32 v251, v251, v250
AW a97,v251
AR v254,a98
v_mul_f32 v254, v254, v250
AW a98,v254
AR v251,a99
v_mul_f32 v251, v251, v250
AW a99,v251
AR v254,a100
v_mul_f32 v254, v254, v250
AW a100,v254
AR v251,a101
v_mul_f32 v251, v251, v250
AW a101,v251
AR v254,a102
v_mul_f32 v254, v254, v250
AW a102,v254
AR v251,a103
v_mul_f32 v251, v251, v250
AW a103,v251
v_max3_f32 v249, v72, v73, v74
v_max3_f32 v251, v104, v105, v106
v_max3_f32 v249, v249, v75, v76
v_max3_f32 v251, v251, v107, v108
v_max3_f32 v249, v249, v77, v78
v_max3_f32 v251, v251, v109, v110
v_max3_f32 v249, v249, v79, v111
v_max_f32 v249, v249, v251
v_log_f32 v250, v246
s_lshl_b64 exec, 0xffff, 16
v_max_f32 v249, v249, v250
s_mov_b64 exec, -1
v_mov_b32 v251, v249
s_nop 1
v_permlane16_swap_b32 v249, v251
v_max_f32 v249, v249, v251
v_mov_b32 v251, v249
s_nop 1
v_permlane32_swap_b32 v249, v251
v_max_f32 v249, v249, v251
v_max_f32 v249, 0, v249
v_sub_f32 v250, 0, v249
EX v250,v250
v_sub_f32 v132, v132, v249
v_sub_f32 v133, v133, v249
v_sub_f32 v134, v134, v249
v_sub_f32 v135, v135, v249
v_sub_f32 v8, v8, v249
v_sub_f32 v72, v72, v249
v_sub_f32 v9, v9, v249
v_sub_f32 v73, v73, v249
v_sub_f32 v10, v10, v249
v_sub_f32 v74, v74, v249
v_sub_f32 v11, v11, v249
v_sub_f32 v75, v75, v249
v_sub_f32 v12, v12, v249
v_sub_f32 v76, v76, v249
v_sub_f32 v13, v13, v249
v_sub_f32 v77, v77, v249
v_sub_f32 v14, v14, v249
v_sub_f32 v78, v78, v249
v_sub_f32 v15, v15, v249
v_sub_f32 v79, v79, v249
v_sub_f32 v40, v40, v249
v_sub_f32 v104, v104, v249
v_sub_f32 v41, v41, v249
v_sub_f32 v105, v105, v249
v_sub_f32 v42, v42, v249
v_sub_f32 v106, v106, v249
v_sub_f32 v43, v43, v249
v_sub_f32 v107, v107, v249
v_sub_f32 v44, v44, v249
v_sub_f32 v108, v108, v249
v_sub_f32 v45, v45, v249
v_sub_f32 v109, v109, v249
v_sub_f32 v46, v46, v249
v_sub_f32 v110, v110, v249
v_sub_f32 v47, v47, v249
v_sub_f32 v111, v111, v249
s_lshl_b64 exec, 0xffff, 16
v_mul_f32 v236, v236, v250
s_mov_b64 exec, -1
AR v254,a8
v_mul_f32 v254, v254, v250
AW a8,v254
AR v251,a9
v_mul_f32 v251, v251, v250
AW a9,v251
AR v254,a10
v_mul_f32 v254, v254, v250
AW a10,v254
AR v251,a11
v_mul_f32 v251, v251, v250
AW a11,v251
AR v254,a12
v_mul_f32 v254, v254, v250
AW a12,v254
AR v251,a13
v_mul_f32 v251, v251, v250
AW a13,v251
AR v254,a14
v_mul_f32 v254, v254, v250
AW a14,v254
AR v251,a15
v_mul_f32 v251, v251, v250
AW a15,v251
AR v254,a40
v_mul_f32 v254, v254, v250
AW a40,v254
AR v251,a41
v_mul_f32 v251, v251, v250
AW a41,v251
AR v254,a42
v_mul_f32 v254, v254, v250
AW a42,v254
AR v251,a43
v_mul_f32 v251, v251, v250
AW a43,v251
AR v254,a44
v_mul_f32 v254, v254, v250
AW a44,v254
AR v251,a45
v_mul_f32 v251, v251, v250
AW a45,v251
AR v254,a46
v_mul_f32 v254, v254, v250
AW a46,v254
AR v251,a47
v_mul_f32 v251, v251, v250
AW a47,v251
AR v254,a72
v_mul_f32 v254, v254, v250
AW a72,v254
AR v251,a73
v_mul_f32 v251, v251, v250
AW a73,v251
AR v254,a74
v_mul_f32 v254, v254, v250
AW a74,v254
AR v251,a75
v_mul_f32 v251, v251, v250
AW a75,v251
AR v254,a76
v_mul_f32 v254, v254, v250
AW a76,v254
AR v251,a77
v_mul_f32 v251, v251, v250
AW a77,v251
AR v254,a78
v_mul_f32 v254, v254, v250
AW a78,v254
AR v251,a79
v_mul_f32 v251, v251, v250
AW a79,v251
AR v254,a104
v_mul_f32 v254, v254, v250
AW a104,v254
AR v251,a105
v_mul_f32 v251, v251, v250
AW a105,v251
AR v254,a106
v_mul_f32 v254, v254, v250
AW a106,v254
AR v251,a107
v_mul_f32 v251, v251, v250
AW a107,v251
AR v254,a108
v_mul_f32 v254, v254, v250
AW a108,v254
AR v251,a109
v_mul_f32 v251, v251, v250
AW a109,v251
AR v254,a110
v_mul_f32 v254, v254, v250
AW a110,v254
AR v251,a111
v_mul_f32 v251, v251, v250
AW a111,v251
EX v64,v64
EX v65,v65
EX v66,v66
EX v67,v67
EX v68,v68
EX v69,v69
EX v70,v70
EX v71,v71
EX v72,v72
EX v73,v73
EX v74,v74
EX v75,v75
EX v76,v76
EX v77,v77
EX v78,v78
EX v79,v79
EX v96,v96
EX v97,v97
EX v98,v98
EX v99,v99
EX v100,v100
EX v101,v101
EX v102,v102
EX v103,v103
EX v104,v104
EX v105,v105
EX v106,v106
EX v107,v107
EX v108,v108
EX v109,v109
EX v110,v110
EX v111,v111
CV v64,v64,v65
CV v65,v66,v67
CV v66,v68,v69
CV v67,v70,v71
CV v72,v72,v73
CV v73,v74,v75
CV v74,v76,v77
CV v75,v78,v79
CV v68,v96,v97
CV v69,v98,v99
CV v70,v100,v101
CV v71,v102,v103
CV v76,v104,v105
CV v77,v106,v107
CV v78,v108,v109
CV v79,v110,v111
v_smfmac_f32_16x16x64_bf16 v[236:239], v[144:147], v[64:71], v234
s_nop 0
v_smfmac_f32_16x16x64_bf16 v[236:239], v[148:151], v[72:79], v234
s_nop 9
v_add_f32 v247, v236, v237
s_cmp_eq_u32 s73, 1
s_cbranch_scc1 LBACK1_%=
s_endpgm
LSITE3_%=:
s_mov_b32 s73, 3
s_mov_b32 s74, 49152
s_mov_b32 s75, 0
s_branch LRARE104_%=
LSITE5_%=:
s_mov_b32 s73, 5
s_mov_b32 s74, 16384
s_mov_b32 s75, 32768
s_branch LRARE104_%=
LSITE7_%=:
s_mov_b32 s73, 7
s_mov_b32 s74, 49152
s_mov_b32 s75, 0
s_branch LRARE104_%=
LSITE9_%=:
s_mov_b32 s73, 9
s_mov_b32 s74, 16384
s_mov_b32 s75, 32768
s_branch LRARE104_%=
LSITE11_%=:
s_mov_b32 s73, 11
s_mov_b32 s74, 49152
s_mov_b32 s75, 0
s_branch LRARE104_%=
LSITE13_%=:
s_mov_b32 s73, 13
s_mov_b32 s74, 16384
s_mov_b32 s75, 32768
s_branch LRARE104_%=
LSITE15_%=:
s_mov_b32 s73, 15
s_mov_b32 s74, 49152
s_mov_b32 s75, 0
s_branch LRARE104_%=
LSITE17_%=:
s_mov_b32 s73, 17
s_mov_b32 s74, 16384
s_mov_b32 s75, 32768
s_branch LRARE104_%=
LSITE19_%=:
s_mov_b32 s73, 19
s_mov_b32 s74, 49152
s_mov_b32 s75, 0
s_branch LRARE104_%=
LSITE21_%=:
s_mov_b32 s73, 21
s_mov_b32 s74, 16384
s_mov_b32 s75, 32768
s_branch LRARE104_%=
LSITE23_%=:
s_mov_b32 s73, 23
s_mov_b32 s74, 49152
s_mov_b32 s75, 0
s_branch LRARE104_%=
LSITE25_%=:
s_mov_b32 s73, 25
s_mov_b32 s74, 16384
s_mov_b32 s75, 32768
s_branch LRARE104_%=
LSITE27_%=:
s_mov_b32 s73, 27
s_mov_b32 s74, 49152
s_mov_b32 s75, 0
s_branch LRARE104_%=
LRARE104_%=:
s_nop 15
s_waitcnt vmcnt(0) lgkmcnt(0)
v_add_u32 v249, s74, v224
RD a[192:195],v249 offset:0
RD a[208:211],v249 offset:4096
RD a[224:227],v249 offset:8192
RD a[240:243],v249 offset:12288
v_add_u32 v250, s74, v225
RD a[196:199],v250 offset:0
RD a[212:215],v250 offset:4096
RD a[228:231],v250 offset:8192
RD a[244:247],v250 offset:12288
v_add_u32 v251, s74, v226
RD a[200:203],v251 offset:0
RD a[216:219],v251 offset:4096
RD a[232:235],v251 offset:8192
RD a[248:251],v251 offset:12288
v_add_u32 v254, s74, v227
RD a[204:207],v254 offset:0
RD a[220:223],v254 offset:4096
RD a[236:239],v254 offset:8192
RD a[252:255],v254 offset:12288
s_waitcnt lgkmcnt(0)
MM v[64:67],a[192:195],v[160:163],v[128:131]
MM v[64:67],a[196:199],v[176:179],v[64:67]
MM v[64:67],a[200:203],v[192:195],v[64:67]
MM v[64:67],a[204:207],v[208:211],v[64:67]
MM v[68:71],a[220:223],v[208:211],v[128:131]
MM v[68:71],a[216:219],v[192:195],v[68:71]
MM v[68:71],a[212:215],v[176:179],v[68:71]
MM v[68:71],a[208:211],v[160:163],v[68:71]
MM v[76:79],a[208:211],v[164:167],v[132:135]
MM v[76:79],a[212:215],v[180:183],v[76:79]
MM v[76:79],a[216:219],v[196:199],v[76:79]
MM v[76:79],a[220:223],v[212:215],v[76:79]
MM v[72:75],a[204:207],v[212:215],v[132:135]
MM v[72:75],a[200:203],v[196:199],v[72:75]
MM v[72:75],a[196:199],v[180:183],v[72:75]
MM v[72:75],a[192:195],v[164:167],v[72:75]
MM v[80:83],a[192:195],v[168:171],v[136:139]
MM v[80:83],a[196:199],v[184:187],v[80:83]
MM v[80:83],a[200:203],v[200:203],v[80:83]
MM v[80:83],a[204:207],v[216:219],v[80:83]
MM v[84:87],a[220:223],v[216:219],v[136:139]
MM v[84:87],a[216:219],v[200:203],v[84:87]
MM v[84:87],a[212:215],v[184:187],v[84:87]
MM v[84:87],a[208:211],v[168:171],v[84:87]
MM v[92:95],a[208:211],v[172:175],v[140:143]
MM v[92:95],a[212:215],v[188:191],v[92:95]
MM v[92:95],a[216:219],v[204:207],v[92:95]
MM v[92:95],a[220:223],v[220:223],v[92:95]
MM v[88:91],a[204:207],v[220:223],v[140:143]
MM v[88:91],a[200:203],v[204:207],v[88:91]
MM v[88:91],a[196:199],v[188:191],v[88:91]
MM v[88:91],a[192:195],v[172:175],v[88:91]
MM v[96:99],a[224:227],v[160:163],v[128:131]
MM v[96:99],a[228:231],v[176:179],v[96:99]
MM v[96:99],a[232:235],v[192:195],v[96:99]
MM v[96:99],a[236:239],v[208:211],v[96:99]
MM v[100:103],a[252:255],v[208:211],v[128:131]
MM v[100:103],a[248:251],v[192:195],v[100:103]
MM v[100:103],a[244:247],v[176:179],v[100:103]
MM v[100:103],a[240:243],v[160:163],v[100:103]
MM v[108:111],a[240:243],v[164:167],v[132:135]
MM v[108:111],a[244:247],v[180:183],v[108:111]
MM v[108:111],a[248:251],v[196:199],v[108:111]
MM v[108:111],a[252:255],v[212:215],v[108:111]
MM v[104:107],a[236:239],v[212:215],v[132:135]
MM v[104:107],a[232:235],v[196:199],v[104:107]
MM v[104:107],a[228:231],v[180:183],v[104:107]
MM v[104:107],a[224:227],v[164:167],v[104:107]
MM v[112:115],a[224:227],v[168:171],v[136:139]
MM v[112:115],a[228:231],v[184:187],v[112:115]
MM v[112:115],a[232:235],v[200:203],v[112:115]
MM v[112:115],a[236:239],v[216:219],v[112:115]
MM v[116:119],a[252:255],v[216:219],v[136:139]
MM v[116:119],a[248:251],v[200:203],v[116:119]
MM v[116:119],a[244:247],v[184:187],v[116:119]
MM v[116:119],a[240:243],v[168:171],v[116:119]
MM v[124:127],a[240:243],v[172:175],v[140:143]
MM v[124:127],a[244:247],v[188:191],v[124:127]
)ASM"
R"ASM(
MM v[124:127],a[248:251],v[204:207],v[124:127]
MM v[124:127],a[252:255],v[220:223],v[124:127]
MM v[120:123],a[236:239],v[220:223],v[140:143]
MM v[120:123],a[232:235],v[204:207],v[120:123]
MM v[120:123],a[228:231],v[188:191],v[120:123]
MM v[120:123],a[224:227],v[172:175],v[120:123]
s_nop 15
v_add_u32 v249, s75, v224
RD a[192:195],v249 offset:0
RD a[208:211],v249 offset:4096
RD a[224:227],v249 offset:8192
RD a[240:243],v249 offset:12288
v_add_u32 v250, s75, v225
RD a[196:199],v250 offset:0
RD a[212:215],v250 offset:4096
RD a[228:231],v250 offset:8192
RD a[244:247],v250 offset:12288
v_add_u32 v251, s75, v226
RD a[200:203],v251 offset:0
RD a[216:219],v251 offset:4096
RD a[232:235],v251 offset:8192
RD a[248:251],v251 offset:12288
v_add_u32 v254, s75, v227
RD a[204:207],v254 offset:0
RD a[220:223],v254 offset:4096
RD a[236:239],v254 offset:8192
RD a[252:255],v254 offset:12288
s_waitcnt lgkmcnt(0)
v_mov_b32 v236, v246
v_mov_b32 v237, 0
v_max3_f32 v249, v64, v65, v66
v_max3_f32 v251, v96, v97, v98
v_max3_f32 v249, v249, v67, v68
v_max3_f32 v251, v251, v99, v100
v_max3_f32 v249, v249, v69, v70
v_max3_f32 v251, v251, v101, v102
v_max3_f32 v249, v249, v71, v103
v_max_f32 v249, v249, v251
v_log_f32 v250, v246
s_lshl_b64 exec, 0xffff, 0
v_max_f32 v249, v249, v250
s_mov_b64 exec, -1
v_mov_b32 v251, v249
s_nop 1
v_permlane16_swap_b32 v249, v251
v_max_f32 v249, v249, v251
v_mov_b32 v251, v249
s_nop 1
v_permlane32_swap_b32 v249, v251
v_max_f32 v249, v249, v251
v_max_f32 v249, 0, v249
v_sub_f32 v250, 0, v249
EX v250,v250
v_sub_f32 v128, v128, v249
v_sub_f32 v129, v129, v249
v_sub_f32 v130, v130, v249
v_sub_f32 v131, v131, v249
v_sub_f32 v0, v0, v249
v_sub_f32 v64, v64, v249
v_sub_f32 v1, v1, v249
v_sub_f32 v65, v65, v249
v_sub_f32 v2, v2, v249
v_sub_f32 v66, v66, v249
v_sub_f32 v3, v3, v249
v_sub_f32 v67, v67, v249
v_sub_f32 v4, v4, v249
v_sub_f32 v68, v68, v249
v_sub_f32 v5, v5, v249
v_sub_f32 v69, v69, v249
v_sub_f32 v6, v6, v249
v_sub_f32 v70, v70, v249
v_sub_f32 v7, v7, v249
v_sub_f32 v71, v71, v249
v_sub_f32 v32, v32, v249
v_sub_f32 v96, v96, v249
v_sub_f32 v33, v33, v249
v_sub_f32 v97, v97, v249
v_sub_f32 v34, v34, v249
v_sub_f32 v98, v98, v249
v_sub_f32 v35, v35, v249
v_sub_f32 v99, v99, v249
v_sub_f32 v36, v36, v249
v_sub_f32 v100, v100, v249
v_sub_f32 v37, v37, v249
v_sub_f32 v101, v101, v249
v_sub_f32 v38, v38, v249
v_sub_f32 v102, v102, v249
v_sub_f32 v39, v39, v249
v_sub_f32 v103, v103, v249
s_lshl_b64 exec, 0xffff, 0
v_mul_f32 v236, v236, v250
s_mov_b64 exec, -1
AR v254,a0
v_mul_f32 v254, v254, v250
AW a0,v254
AR v251,a1
v_mul_f32 v251, v251, v250
AW a1,v251
AR v254,a2
v_mul_f32 v254, v254, v250
AW a2,v254
AR v251,a3
v_mul_f32 v251, v251, v250
AW a3,v251
AR v254,a4
v_mul_f32 v254, v254, v250
AW a4,v254
AR v251,a5
v_mul_f32 v251, v251, v250
AW a5,v251
AR v254,a6
v_mul_f32 v254, v254, v250
AW a6,v254
AR v251,a7
v_mul_f32 v251, v251, v250
AW a7,v251
AR v254,a32
v_mul_f32 v254, v254, v250
AW a32,v254
AR v251,a33
v_mul_f32 v251, v251, v250
AW a33,v251
AR v254,a34
v_mul_f32 v254, v254, v250
AW a34,v254
AR v251,a35
v_mul_f32 v251, v251, v250
AW a35,v251
AR v254,a36
v_mul_f32 v254, v254, v250
AW a36,v254
AR v251,a37
v_mul_f32 v251, v251, v250
AW a37,v251
AR v254,a38
v_mul_f32 v254, v254, v250
AW a38,v254
AR v251,a39
v_mul_f32 v251, v251, v250
AW a39,v251
AR v254,a64
v_mul_f32 v254, v254, v250
AW a64,v254
AR v251,a65
v_mul_f32 v251, v251, v250
AW a65,v251
AR v254,a66
v_mul_f32 v254, v254, v250
AW a66,v254
AR v251,a67
v_mul_f32 v251, v251, v250
AW a67,v251
AR v254,a68
v_mul_f32 v254, v254, v250
AW a68,v254
AR v251,a69
v_mul_f32 v251, v251, v250
AW a69,v251
AR v254,a70
v_mul_f32 v254, v254, v250
AW a70,v254
AR v251,a71
v_mul_f32 v251, v251, v250
AW a71,v251
AR v254,a96
v_mul_f32 v254, v254, v250
AW a96,v254
AR v251,a97
v_mul_f32 v251, v251, v250
AW a97,v251
AR v254,a98
v_mul_f32 v254, v254, v250
AW a98,v254
AR v251,a99
v_mul_f32 v251, v251, v250
AW a99,v251
AR v254,a100
v_mul_f32 v254, v254, v250
AW a100,v254
AR v251,a101
v_mul_f32 v251, v251, v250
AW a101,v251
AR v254,a102
v_mul_f32 v254, v254, v250
AW a102,v254
AR v251,a103
v_mul_f32 v251, v251, v250
AW a103,v251
v_max3_f32 v249, v72, v73, v74
v_max3_f32 v251, v104, v105, v106
v_max3_f32 v249, v249, v75, v76
v_max3_f32 v251, v251, v107, v108
v_max3_f32 v249, v249, v77, v78
v_max3_f32 v251, v251, v109, v110
v_max3_f32 v249, v249, v79, v111
v_max_f32 v249, v249, v251
v_log_f32 v250, v246
s_lshl_b64 exec, 0xffff, 16
v_max_f32 v249, v249, v250
s_mov_b64 exec, -1
v_mov_b32 v251, v249
s_nop 1
v_permlane16_swap_b32 v249, v251
v_max_f32 v249, v249, v251
v_mov_b32 v251, v249
s_nop 1
v_permlane32_swap_b32 v249, v251
v_max_f32 v249, v249, v251
v_max_f32 v249, 0, v249
v_sub_f32 v250, 0, v249
EX v250,v250
v_sub_f32 v132, v132, v249
v_sub_f32 v133, v133, v249
v_sub_f32 v134, v134, v249
v_sub_f32 v135, v135, v249
v_sub_f32 v8, v8, v249
v_sub_f32 v72, v72, v249
v_sub_f32 v9, v9, v249
v_sub_f32 v73, v73, v249
v_sub_f32 v10, v10, v249
v_sub_f32 v74, v74, v249
v_sub_f32 v11, v11, v249
v_sub_f32 v75, v75, v249
v_sub_f32 v12, v12, v249
v_sub_f32 v76, v76, v249
v_sub_f32 v13, v13, v249
v_sub_f32 v77, v77, v249
v_sub_f32 v14, v14, v249
v_sub_f32 v78, v78, v249
v_sub_f32 v15, v15, v249
v_sub_f32 v79, v79, v249
v_sub_f32 v40, v40, v249
v_sub_f32 v104, v104, v249
v_sub_f32 v41, v41, v249
v_sub_f32 v105, v105, v249
v_sub_f32 v42, v42, v249
v_sub_f32 v106, v106, v249
v_sub_f32 v43, v43, v249
v_sub_f32 v107, v107, v249
v_sub_f32 v44, v44, v249
v_sub_f32 v108, v108, v249
v_sub_f32 v45, v45, v249
v_sub_f32 v109, v109, v249
v_sub_f32 v46, v46, v249
v_sub_f32 v110, v110, v249
v_sub_f32 v47, v47, v249
v_sub_f32 v111, v111, v249
s_lshl_b64 exec, 0xffff, 16
v_mul_f32 v236, v236, v250
s_mov_b64 exec, -1
AR v254,a8
v_mul_f32 v254, v254, v250
AW a8,v254
AR v251,a9
v_mul_f32 v251, v251, v250
AW a9,v251
AR v254,a10
v_mul_f32 v254, v254, v250
AW a10,v254
AR v251,a11
v_mul_f32 v251, v251, v250
AW a11,v251
AR v254,a12
v_mul_f32 v254, v254, v250
AW a12,v254
AR v251,a13
v_mul_f32 v251, v251, v250
AW a13,v251
AR v254,a14
v_mul_f32 v254, v254, v250
AW a14,v254
AR v251,a15
v_mul_f32 v251, v251, v250
AW a15,v251
AR v254,a40
v_mul_f32 v254, v254, v250
AW a40,v254
AR v251,a41
v_mul_f32 v251, v251, v250
AW a41,v251
AR v254,a42
v_mul_f32 v254, v254, v250
AW a42,v254
AR v251,a43
v_mul_f32 v251, v251, v250
AW a43,v251
AR v254,a44
v_mul_f32 v254, v254, v250
AW a44,v254
AR v251,a45
v_mul_f32 v251, v251, v250
AW a45,v251
AR v254,a46
v_mul_f32 v254, v254, v250
AW a46,v254
AR v251,a47
v_mul_f32 v251, v251, v250
AW a47,v251
AR v254,a72
v_mul_f32 v254, v254, v250
AW a72,v254
AR v251,a73
v_mul_f32 v251, v251, v250
AW a73,v251
AR v254,a74
v_mul_f32 v254, v254, v250
AW a74,v254
AR v251,a75
v_mul_f32 v251, v251, v250
AW a75,v251
AR v254,a76
v_mul_f32 v254, v254, v250
AW a76,v254
AR v251,a77
v_mul_f32 v251, v251, v250
AW a77,v251
AR v254,a78
v_mul_f32 v254, v254, v250
AW a78,v254
AR v251,a79
v_mul_f32 v251, v251, v250
AW a79,v251
AR v254,a104
v_mul_f32 v254, v254, v250
AW a104,v254
AR v251,a105
v_mul_f32 v251, v251, v250
AW a105,v251
AR v254,a106
v_mul_f32 v254, v254, v250
AW a106,v254
AR v251,a107
v_mul_f32 v251, v251, v250
AW a107,v251
AR v254,a108
v_mul_f32 v254, v254, v250
AW a108,v254
AR v251,a109
v_mul_f32 v251, v251, v250
AW a109,v251
AR v254,a110
v_mul_f32 v254, v254, v250
AW a110,v254
AR v251,a111
v_mul_f32 v251, v251, v250
AW a111,v251
v_max3_f32 v249, v80, v81, v82
v_max3_f32 v251, v112, v113, v114
v_max3_f32 v249, v249, v83, v84
v_max3_f32 v251, v251, v115, v116
v_max3_f32 v249, v249, v85, v86
v_max3_f32 v251, v251, v117, v118
v_max3_f32 v249, v249, v87, v119
v_max_f32 v249, v249, v251
v_log_f32 v250, v246
s_lshl_b64 exec, 0xffff, 32
v_max_f32 v249, v249, v250
s_mov_b64 exec, -1
v_mov_b32 v251, v249
s_nop 1
v_permlane16_swap_b32 v249, v251
v_max_f32 v249, v249, v251
v_mov_b32 v251, v249
s_nop 1
v_permlane32_swap_b32 v249, v251
v_max_f32 v249, v249, v251
v_max_f32 v249, 0, v249
v_sub_f32 v250, 0, v249
EX v250,v250
v_sub_f32 v136, v136, v249
v_sub_f32 v137, v137, v249
v_sub_f32 v138, v138, v249
v_sub_f32 v139, v139, v249
v_sub_f32 v16, v16, v249
v_sub_f32 v80, v80, v249
v_sub_f32 v17, v17, v249
v_sub_f32 v81, v81, v249
v_sub_f32 v18, v18, v249
v_sub_f32 v82, v82, v249
v_sub_f32 v19, v19, v249
v_sub_f32 v83, v83, v249
v_sub_f32 v20, v20, v249
v_sub_f32 v84, v84, v249
v_sub_f32 v21, v21, v249
v_sub_f32 v85, v85, v249
v_sub_f32 v22, v22, v249
v_sub_f32 v86, v86, v249
v_sub_f32 v23, v23, v249
v_sub_f32 v87, v87, v249
v_sub_f32 v48, v48, v249
v_sub_f32 v112, v112, v249
v_sub_f32 v49, v49, v249
v_sub_f32 v113, v113, v249
v_sub_f32 v50, v50, v249
v_sub_f32 v114, v114, v249
v_sub_f32 v51, v51, v249
v_sub_f32 v115, v115, v249
v_sub_f32 v52, v52, v249
v_sub_f32 v116, v116, v249
v_sub_f32 v53, v53, v249
v_sub_f32 v117, v117, v249
v_sub_f32 v54, v54, v249
v_sub_f32 v118, v118, v249
v_sub_f32 v55, v55, v249
v_sub_f32 v119, v119, v249
s_lshl_b64 exec, 0xffff, 32
v_mul_f32 v236, v236, v250
s_mov_b64 exec, -1
AR v254,a16
v_mul_f32 v254, v254, v250
AW a16,v254
AR v251,a17
v_mul_f32 v251, v251, v250
AW a17,v251
AR v254,a18
v_mul_f32 v254, v254, v250
AW a18,v254
AR v251,a19
v_mul_f32 v251, v251, v250
AW a19,v251
AR v254,a20
v_mul_f32 v254, v254, v250
AW a20,v254
AR v251,a21
v_mul_f32 v251, v251, v250
AW a21,v251
AR v254,a22
v_mul_f32 v254, v254, v250
AW a22,v254
AR v251,a23
v_mul_f32 v251, v251, v250
AW a23,v251
AR v254,a48
v_mul_f32 v254, v254, v250
AW a48,v254
AR v251,a49
v_mul_f32 v251, v251, v250
AW a49,v251
AR v254,a50
v_mul_f32 v254, v254, v250
AW a50,v254
AR v251,a51
v_mul_f32 v251, v251, v250
AW a51,v251
AR v254,a52
v_mul_f32 v254, v254, v250
AW a52,v254
AR v251,a53
v_mul_f32 v251, v251, v250
AW a53,v251
AR v254,a54
v_mul_f32 v254, v254, v250
AW a54,v254
AR v251,a55
v_mul_f32 v251, v251, v250
AW a55,v251
AR v254,a80
v_mul_f32 v254, v254, v250
AW a80,v254
AR v251,a81
v_mul_f32 v251, v251, v250
AW a81,v251
AR v254,a82
v_mul_f32 v254, v254, v250
AW a82,v254
AR v251,a83
v_mul_f32 v251, v251, v250
AW a83,v251
AR v254,a84
v_mul_f32 v254, v254, v250
AW a84,v254
AR v251,a85
v_mul_f32 v251, v251, v250
AW a85,v251
AR v254,a86
v_mul_f32 v254, v254, v250
AW a86,v254
AR v251,a87
v_mul_f32 v251, v251, v250
AW a87,v251
AR v254,a112
v_mul_f32 v254, v254, v250
AW a112,v254
AR v251,a113
v_mul_f32 v251, v251, v250
AW a113,v251
AR v254,a114
v_mul_f32 v254, v254, v250
AW a114,v254
AR v251,a115
v_mul_f32 v251, v251, v250
AW a115,v251
AR v254,a116
v_mul_f32 v254, v254, v250
AW a116,v254
AR v251,a117
v_mul_f32 v251, v251, v250
AW a117,v251
AR v254,a118
v_mul_f32 v254, v254, v250
AW a118,v254
AR v251,a119
v_mul_f32 v251, v251, v250
AW a119,v251
v_max3_f32 v249, v88, v89, v90
v_max3_f32 v251, v120, v121, v122
v_max3_f32 v249, v249, v91, v92
v_max3_f32 v251, v251, v123, v124
v_max3_f32 v249, v249, v93, v94
v_max3_f32 v251, v251, v125, v126
v_max3_f32 v249, v249, v95, v127
v_max_f32 v249, v249, v251
v_log_f32 v250, v246
s_lshl_b64 exec, 0xffff, 48
v_max_f32 v249, v249, v250
s_mov_b64 exec, -1
v_mov_b32 v251, v249
s_nop 1
v_permlane16_swap_b32 v249, v251
v_max_f32 v249, v249, v251
v_mov_b32 v251, v249
s_nop 1
v_permlane32_swap_b32 v249, v251
v_max_f32 v249, v249, v251
v_max_f32 v249, 0, v249
v_sub_f32 v250, 0, v249
EX v250,v250
v_sub_f32 v140, v140, v249
v_sub_f32 v141, v141, v249
v_sub_f32 v142, v142, v249
v_sub_f32 v143, v143, v249
v_sub_f32 v24, v24, v249
v_sub_f32 v88, v88, v249
v_sub_f32 v25, v25, v249
v_sub_f32 v89, v89, v249
v_sub_f32 v26, v26, v249
v_sub_f32 v90, v90, v249
v_sub_f32 v27, v27, v249
v_sub_f32 v91, v91, v249
v_sub_f32 v28, v28, v249
v_sub_f32 v92, v92, v249
v_sub_f32 v29, v29, v249
v_sub_f32 v93, v93, v249
v_sub_f32 v30, v30, v249
v_sub_f32 v94, v94, v249
v_sub_f32 v31, v31, v249
v_sub_f32 v95, v95, v249
v_sub_f32 v56, v56, v249
v_sub_f32 v120, v120, v249
v_sub_f32 v57, v57, v249
v_sub_f32 v121, v121, v249
v_sub_f32 v58, v58, v249
v_sub_f32 v122, v122, v249
v_sub_f32 v59, v59, v249
v_sub_f32 v123, v123, v249
v_sub_f32 v60, v60, v249
v_sub_f32 v124, v124, v249
v_sub_f32 v61, v61, v249
v_sub_f32 v125, v125, v249
)ASM"
R"ASM(
v_sub_f32 v62, v62, v249
v_sub_f32 v126, v126, v249
v_sub_f32 v63, v63, v249
v_sub_f32 v127, v127, v249
s_lshl_b64 exec, 0xffff, 48
v_mul_f32 v236, v236, v250
s_mov_b64 exec, -1
AR v254,a24
v_mul_f32 v254, v254, v250
AW a24,v254
AR v251,a25
v_mul_f32 v251, v251, v250
AW a25,v251
AR v254,a26
v_mul_f32 v254, v254, v250
AW a26,v254
AR v251,a27
v_mul_f32 v251, v251, v250
AW a27,v251
AR v254,a28
v_mul_f32 v254, v254, v250
AW a28,v254
AR v251,a29
v_mul_f32 v251, v251, v250
AW a29,v251
AR v254,a30
v_mul_f32 v254, v254, v250
AW a30,v254
AR v251,a31
v_mul_f32 v251, v251, v250
AW a31,v251
AR v254,a56
v_mul_f32 v254, v254, v250
AW a56,v254
AR v251,a57
v_mul_f32 v251, v251, v250
AW a57,v251
AR v254,a58
v_mul_f32 v254, v254, v250
AW a58,v254
AR v251,a59
v_mul_f32 v251, v251, v250
AW a59,v251
AR v254,a60
v_mul_f32 v254, v254, v250
AW a60,v254
AR v251,a61
v_mul_f32 v251, v251, v250
AW a61,v251
AR v254,a62
v_mul_f32 v254, v254, v250
AW a62,v254
AR v251,a63
v_mul_f32 v251, v251, v250
AW a63,v251
AR v254,a88
v_mul_f32 v254, v254, v250
AW a88,v254
AR v251,a89
v_mul_f32 v251, v251, v250
AW a89,v251
AR v254,a90
v_mul_f32 v254, v254, v250
AW a90,v254
AR v251,a91
v_mul_f32 v251, v251, v250
AW a91,v251
AR v254,a92
v_mul_f32 v254, v254, v250
AW a92,v254
AR v251,a93
v_mul_f32 v251, v251, v250
AW a93,v251
AR v254,a94
v_mul_f32 v254, v254, v250
AW a94,v254
AR v251,a95
v_mul_f32 v251, v251, v250
AW a95,v251
AR v254,a120
v_mul_f32 v254, v254, v250
AW a120,v254
AR v251,a121
v_mul_f32 v251, v251, v250
AW a121,v251
AR v254,a122
v_mul_f32 v254, v254, v250
AW a122,v254
AR v251,a123
v_mul_f32 v251, v251, v250
AW a123,v251
AR v254,a124
v_mul_f32 v254, v254, v250
AW a124,v254
AR v251,a125
v_mul_f32 v251, v251, v250
AW a125,v251
AR v254,a126
v_mul_f32 v254, v254, v250
AW a126,v254
AR v251,a127
v_mul_f32 v251, v251, v250
AW a127,v251
EX v64,v64
EX v65,v65
EX v66,v66
EX v67,v67
EX v68,v68
EX v69,v69
EX v70,v70
EX v71,v71
EX v72,v72
EX v73,v73
EX v74,v74
EX v75,v75
EX v76,v76
EX v77,v77
EX v78,v78
EX v79,v79
EX v80,v80
EX v81,v81
EX v82,v82
EX v83,v83
EX v84,v84
EX v85,v85
EX v86,v86
EX v87,v87
EX v88,v88
EX v89,v89
EX v90,v90
EX v91,v91
EX v92,v92
EX v93,v93
EX v94,v94
EX v95,v95
EX v96,v96
EX v97,v97
EX v98,v98
EX v99,v99
EX v100,v100
EX v101,v101
EX v102,v102
EX v103,v103
EX v104,v104
EX v105,v105
EX v106,v106
EX v107,v107
EX v108,v108
EX v109,v109
EX v110,v110
EX v111,v111
EX v112,v112
EX v113,v113
EX v114,v114
EX v115,v115
EX v116,v116
EX v117,v117
EX v118,v118
EX v119,v119
EX v120,v120
EX v121,v121
EX v122,v122
EX v123,v123
EX v124,v124
EX v125,v125
EX v126,v126
EX v127,v127
CV v64,v64,v65
CV v65,v66,v67
CV v66,v68,v69
CV v67,v70,v71
CV v72,v72,v73
CV v73,v74,v75
CV v74,v76,v77
CV v75,v78,v79
CV v80,v80,v81
CV v81,v82,v83
CV v82,v84,v85
CV v83,v86,v87
CV v88,v88,v89
CV v89,v90,v91
CV v90,v92,v93
CV v91,v94,v95
CV v68,v96,v97
CV v69,v98,v99
CV v70,v100,v101
CV v71,v102,v103
CV v76,v104,v105
CV v77,v106,v107
CV v78,v108,v109
CV v79,v110,v111
CV v84,v112,v113
CV v85,v114,v115
CV v86,v116,v117
CV v87,v118,v119
CV v92,v120,v121
CV v93,v122,v123
CV v94,v124,v125
CV v95,v126,v127
v_smfmac_f32_16x16x64_bf16 v[236:239], v[144:147], v[64:71], v234
v_smfmac_f32_16x16x64_bf16 v[236:239], v[148:151], v[72:79], v234
v_smfmac_f32_16x16x64_bf16 v[236:239], v[152:155], v[80:87], v234
v_smfmac_f32_16x16x64_bf16 v[236:239], v[156:159], v[88:95], v234
s_nop 9
v_add_f32 v247, v236, v237
s_cmp_eq_u32 s73, 3
s_cbranch_scc1 LBACK3_%=
s_cmp_eq_u32 s73, 5
s_cbranch_scc1 LBACK5_%=
s_cmp_eq_u32 s73, 7
s_cbranch_scc1 LBACK7_%=
s_cmp_eq_u32 s73, 9
s_cbranch_scc1 LBACK9_%=
s_cmp_eq_u32 s73, 11
s_cbranch_scc1 LBACK11_%=
s_cmp_eq_u32 s73, 13
s_cbranch_scc1 LBACK13_%=
s_cmp_eq_u32 s73, 15
s_cbranch_scc1 LBACK15_%=
s_cmp_eq_u32 s73, 17
s_cbranch_scc1 LBACK17_%=
s_cmp_eq_u32 s73, 19
s_cbranch_scc1 LBACK19_%=
s_cmp_eq_u32 s73, 21
s_cbranch_scc1 LBACK21_%=
s_cmp_eq_u32 s73, 23
s_cbranch_scc1 LBACK23_%=
s_cmp_eq_u32 s73, 25
s_cbranch_scc1 LBACK25_%=
s_cmp_eq_u32 s73, 27
s_cbranch_scc1 LBACK27_%=
s_endpgm
LSITE4_%=:
s_mov_b32 s73, 4
s_mov_b32 s74, 0
s_mov_b32 s75, 16384
s_branch LRARE004_%=
LSITE6_%=:
s_mov_b32 s73, 6
s_mov_b32 s74, 32768
s_mov_b32 s75, 49152
s_branch LRARE004_%=
LSITE8_%=:
s_mov_b32 s73, 8
s_mov_b32 s74, 0
s_mov_b32 s75, 16384
s_branch LRARE004_%=
LSITE10_%=:
s_mov_b32 s73, 10
s_mov_b32 s74, 32768
s_mov_b32 s75, 49152
s_branch LRARE004_%=
LSITE12_%=:
s_mov_b32 s73, 12
s_mov_b32 s74, 0
s_mov_b32 s75, 16384
s_branch LRARE004_%=
LSITE14_%=:
s_mov_b32 s73, 14
s_mov_b32 s74, 32768
s_mov_b32 s75, 49152
s_branch LRARE004_%=
LSITE16_%=:
s_mov_b32 s73, 16
s_mov_b32 s74, 0
s_mov_b32 s75, 16384
s_branch LRARE004_%=
LSITE18_%=:
s_mov_b32 s73, 18
s_mov_b32 s74, 32768
s_mov_b32 s75, 49152
s_branch LRARE004_%=
LSITE20_%=:
s_mov_b32 s73, 20
s_mov_b32 s74, 0
s_mov_b32 s75, 16384
s_branch LRARE004_%=
LSITE22_%=:
s_mov_b32 s73, 22
s_mov_b32 s74, 32768
s_mov_b32 s75, 49152
s_branch LRARE004_%=
LSITE24_%=:
s_mov_b32 s73, 24
s_mov_b32 s74, 0
s_mov_b32 s75, 16384
s_branch LRARE004_%=
LSITE26_%=:
s_mov_b32 s73, 26
s_mov_b32 s74, 32768
s_mov_b32 s75, 49152
s_branch LRARE004_%=
LRARE004_%=:
s_nop 15
s_waitcnt vmcnt(0) lgkmcnt(0)
v_add_u32 v249, s74, v224
RD a[192:195],v249 offset:0
RD a[208:211],v249 offset:4096
RD a[224:227],v249 offset:8192
RD a[240:243],v249 offset:12288
v_add_u32 v250, s74, v225
RD a[196:199],v250 offset:0
RD a[212:215],v250 offset:4096
RD a[228:231],v250 offset:8192
RD a[244:247],v250 offset:12288
v_add_u32 v251, s74, v226
RD a[200:203],v251 offset:0
RD a[216:219],v251 offset:4096
RD a[232:235],v251 offset:8192
RD a[248:251],v251 offset:12288
v_add_u32 v254, s74, v227
RD a[204:207],v254 offset:0
RD a[220:223],v254 offset:4096
RD a[236:239],v254 offset:8192
RD a[252:255],v254 offset:12288
s_waitcnt lgkmcnt(0)
MM v[0:3],a[192:195],v[160:163],v[128:131]
MM v[0:3],a[196:199],v[176:179],v[0:3]
MM v[0:3],a[200:203],v[192:195],v[0:3]
MM v[0:3],a[204:207],v[208:211],v[0:3]
MM v[4:7],a[220:223],v[208:211],v[128:131]
MM v[4:7],a[216:219],v[192:195],v[4:7]
MM v[4:7],a[212:215],v[176:179],v[4:7]
MM v[4:7],a[208:211],v[160:163],v[4:7]
MM v[12:15],a[208:211],v[164:167],v[132:135]
MM v[12:15],a[212:215],v[180:183],v[12:15]
MM v[12:15],a[216:219],v[196:199],v[12:15]
MM v[12:15],a[220:223],v[212:215],v[12:15]
MM v[8:11],a[204:207],v[212:215],v[132:135]
MM v[8:11],a[200:203],v[196:199],v[8:11]
MM v[8:11],a[196:199],v[180:183],v[8:11]
MM v[8:11],a[192:195],v[164:167],v[8:11]
MM v[16:19],a[192:195],v[168:171],v[136:139]
MM v[16:19],a[196:199],v[184:187],v[16:19]
MM v[16:19],a[200:203],v[200:203],v[16:19]
MM v[16:19],a[204:207],v[216:219],v[16:19]
MM v[20:23],a[220:223],v[216:219],v[136:139]
MM v[20:23],a[216:219],v[200:203],v[20:23]
MM v[20:23],a[212:215],v[184:187],v[20:23]
MM v[20:23],a[208:211],v[168:171],v[20:23]
MM v[28:31],a[208:211],v[172:175],v[140:143]
MM v[28:31],a[212:215],v[188:191],v[28:31]
MM v[28:31],a[216:219],v[204:207],v[28:31]
MM v[28:31],a[220:223],v[220:223],v[28:31]
MM v[24:27],a[204:207],v[220:223],v[140:143]
MM v[24:27],a[200:203],v[204:207],v[24:27]
MM v[24:27],a[196:199],v[188:191],v[24:27]
MM v[24:27],a[192:195],v[172:175],v[24:27]
MM v[32:35],a[224:227],v[160:163],v[128:131]
MM v[32:35],a[228:231],v[176:179],v[32:35]
MM v[32:35],a[232:235],v[192:195],v[32:35]
MM v[32:35],a[236:239],v[208:211],v[32:35]
MM v[36:39],a[252:255],v[208:211],v[128:131]
MM v[36:39],a[248:251],v[192:195],v[36:39]
MM v[36:39],a[244:247],v[176:179],v[36:39]
MM v[36:39],a[240:243],v[160:163],v[36:39]
MM v[44:47],a[240:243],v[164:167],v[132:135]
MM v[44:47],a[244:247],v[180:183],v[44:47]
MM v[44:47],a[248:251],v[196:199],v[44:47]
MM v[44:47],a[252:255],v[212:215],v[44:47]
MM v[40:43],a[236:239],v[212:215],v[132:135]
MM v[40:43],a[232:235],v[196:199],v[40:43]
MM v[40:43],a[228:231],v[180:183],v[40:43]
MM v[40:43],a[224:227],v[164:167],v[40:43]
MM v[48:51],a[224:227],v[168:171],v[136:139]
MM v[48:51],a[228:231],v[184:187],v[48:51]
MM v[48:51],a[232:235],v[200:203],v[48:51]
MM v[48:51],a[236:239],v[216:219],v[48:51]
MM v[52:55],a[252:255],v[216:219],v[136:139]
MM v[52:55],a[248:251],v[200:203],v[52:55]
MM v[52:55],a[244:247],v[184:187],v[52:55]
MM v[52:55],a[240:243],v[168:171],v[52:55]
MM v[60:63],a[240:243],v[172:175],v[140:143]
MM v[60:63],a[244:247],v[188:191],v[60:63]
MM v[60:63],a[248:251],v[204:207],v[60:63]
MM v[60:63],a[252:255],v[220:223],v[60:63]
MM v[56:59],a[236:239],v[220:223],v[140:143]
MM v[56:59],a[232:235],v[204:207],v[56:59]
MM v[56:59],a[228:231],v[188:191],v[56:59]
MM v[56:59],a[224:227],v[172:175],v[56:59]
s_nop 15
v_add_u32 v249, s75, v224
RD a[192:195],v249 offset:0
RD a[208:211],v249 offset:4096
RD a[224:227],v249 offset:8192
RD a[240:243],v249 offset:12288
v_add_u32 v250, s75, v225
RD a[196:199],v250 offset:0
RD a[212:215],v250 offset:4096
RD a[228:231],v250 offset:8192
RD a[244:247],v250 offset:12288
v_add_u32 v251, s75, v226
RD a[200:203],v251 offset:0
RD a[216:219],v251 offset:4096
RD a[232:235],v251 offset:8192
RD a[248:251],v251 offset:12288
v_add_u32 v254, s75, v227
RD a[204:207],v254 offset:0
RD a[220:223],v254 offset:4096
RD a[236:239],v254 offset:8192
RD a[252:255],v254 offset:12288
s_waitcnt lgkmcnt(0)
v_mov_b32 v236, v247
v_mov_b32 v237, 0
v_max3_f32 v249, v0, v1, v2
v_max3_f32 v251, v32, v33, v34
v_max3_f32 v249, v249, v3, v4
v_max3_f32 v251, v251, v35, v36
v_max3_f32 v249, v249, v5, v6
v_max3_f32 v251, v251, v37, v38
v_max3_f32 v249, v249, v7, v39
v_max_f32 v249, v249, v251
v_log_f32 v250, v247
s_lshl_b64 exec, 0xffff, 0
v_max_f32 v249, v249, v250
s_mov_b64 exec, -1
v_mov_b32 v251, v249
s_nop 1
v_permlane16_swap_b32 v249, v251
v_max_f32 v249, v249, v251
v_mov_b32 v251, v249
s_nop 1
v_permlane32_swap_b32 v249, v251
v_max_f32 v249, v249, v251
v_max_f32 v249, 0, v249
v_sub_f32 v250, 0, v249
EX v250,v250
v_sub_f32 v128, v128, v249
v_sub_f32 v129, v129, v249
v_sub_f32 v130, v130, v249
v_sub_f32 v131, v131, v249
v_sub_f32 v64, v64, v249
v_sub_f32 v0, v0, v249
v_sub_f32 v65, v65, v249
v_sub_f32 v1, v1, v249
v_sub_f32 v66, v66, v249
v_sub_f32 v2, v2, v249
v_sub_f32 v67, v67, v249
v_sub_f32 v3, v3, v249
v_sub_f32 v68, v68, v249
v_sub_f32 v4, v4, v249
v_sub_f32 v69, v69, v249
v_sub_f32 v5, v5, v249
v_sub_f32 v70, v70, v249
v_sub_f32 v6, v6, v249
v_sub_f32 v71, v71, v249
v_sub_f32 v7, v7, v249
v_sub_f32 v96, v96, v249
v_sub_f32 v32, v32, v249
v_sub_f32 v97, v97, v249
v_sub_f32 v33, v33, v249
v_sub_f32 v98, v98, v249
v_sub_f32 v34, v34, v249
v_sub_f32 v99, v99, v249
v_sub_f32 v35, v35, v249
v_sub_f32 v100, v100, v249
v_sub_f32 v36, v36, v249
v_sub_f32 v101, v101, v249
v_sub_f32 v37, v37, v249
v_sub_f32 v102, v102, v249
v_sub_f32 v38, v38, v249
v_sub_f32 v103, v103, v249
v_sub_f32 v39, v39, v249
s_lshl_b64 exec, 0xffff, 0
v_mul_f32 v236, v236, v250
s_mov_b64 exec, -1
AR v254,a0
v_mul_f32 v254, v254, v250
AW a0,v254
AR v251,a1
v_mul_f32 v251, v251, v250
AW a1,v251
AR v254,a2
v_mul_f32 v254, v254, v250
AW a2,v254
AR v251,a3
v_mul_f32 v251, v251, v250
AW a3,v251
AR v254,a4
v_mul_f32 v254, v254, v250
AW a4,v254
AR v251,a5
v_mul_f32 v251, v251, v250
AW a5,v251
AR v254,a6
v_mul_f32 v254, v254, v250
AW a6,v254
AR v251,a7
v_mul_f32 v251, v251, v250
AW a7,v251
AR v254,a32
v_mul_f32 v254, v254, v250
AW a32,v254
AR v251,a33
v_mul_f32 v251, v251, v250
AW a33,v251
AR v254,a34
v_mul_f32 v254, v254, v250
AW a34,v254
AR v251,a35
v_mul_f32 v251, v251, v250
AW a35,v251
AR v254,a36
v_mul_f32 v254, v254, v250
AW a36,v254
AR v251,a37
v_mul_f32 v251, v251, v250
AW a37,v251
AR v254,a38
v_mul_f32 v254, v254, v250
AW a38,v254
AR v251,a39
v_mul_f32 v251, v251, v250
AW a39,v251
AR v254,a64
v_mul_f32 v254, v254, v250
AW a64,v254
AR v251,a65
v_mul_f32 v251, v251, v250
AW a65,v251
AR v254,a66
v_mul_f32 v254, v254, v250
AW a66,v254
AR v251,a67
v_mul_f32 v251, v251, v250
AW a67,v251
AR v254,a68
v_mul_f32 v254, v254, v250
AW a68,v254
AR v251,a69
v_mul_f32 v251, v251, v250
AW a69,v251
AR v254,a70
v_mul_f32 v254, v254, v250
AW a70,v254
AR v251,a71
v_mul_f32 v251, v251, v250
AW a71,v251
AR v254,a96
v_mul_f32 v254, v254, v250
AW a96,v254
AR v251,a97
v_mul_f32 v251, v251, v250
)ASM"
R"ASM(
AW a97,v251
AR v254,a98
v_mul_f32 v254, v254, v250
AW a98,v254
AR v251,a99
v_mul_f32 v251, v251, v250
AW a99,v251
AR v254,a100
v_mul_f32 v254, v254, v250
AW a100,v254
AR v251,a101
v_mul_f32 v251, v251, v250
AW a101,v251
AR v254,a102
v_mul_f32 v254, v254, v250
AW a102,v254
AR v251,a103
v_mul_f32 v251, v251, v250
AW a103,v251
v_max3_f32 v249, v8, v9, v10
v_max3_f32 v251, v40, v41, v42
v_max3_f32 v249, v249, v11, v12
v_max3_f32 v251, v251, v43, v44
v_max3_f32 v249, v249, v13, v14
v_max3_f32 v251, v251, v45, v46
v_max3_f32 v249, v249, v15, v47
v_max_f32 v249, v249, v251
v_log_f32 v250, v247
s_lshl_b64 exec, 0xffff, 16
v_max_f32 v249, v249, v250
s_mov_b64 exec, -1
v_mov_b32 v251, v249
s_nop 1
v_permlane16_swap_b32 v249, v251
v_max_f32 v249, v249, v251
v_mov_b32 v251, v249
s_nop 1
v_permlane32_swap_b32 v249, v251
v_max_f32 v249, v249, v251
v_max_f32 v249, 0, v249
v_sub_f32 v250, 0, v249
EX v250,v250
v_sub_f32 v132, v132, v249
v_sub_f32 v133, v133, v249
v_sub_f32 v134, v134, v249
v_sub_f32 v135, v135, v249
v_sub_f32 v72, v72, v249
v_sub_f32 v8, v8, v249
v_sub_f32 v73, v73, v249
v_sub_f32 v9, v9, v249
v_sub_f32 v74, v74, v249
v_sub_f32 v10, v10, v249
v_sub_f32 v75, v75, v249
v_sub_f32 v11, v11, v249
v_sub_f32 v76, v76, v249
v_sub_f32 v12, v12, v249
v_sub_f32 v77, v77, v249
v_sub_f32 v13, v13, v249
v_sub_f32 v78, v78, v249
v_sub_f32 v14, v14, v249
v_sub_f32 v79, v79, v249
v_sub_f32 v15, v15, v249
v_sub_f32 v104, v104, v249
v_sub_f32 v40, v40, v249
v_sub_f32 v105, v105, v249
v_sub_f32 v41, v41, v249
v_sub_f32 v106, v106, v249
v_sub_f32 v42, v42, v249
v_sub_f32 v107, v107, v249
v_sub_f32 v43, v43, v249
v_sub_f32 v108, v108, v249
v_sub_f32 v44, v44, v249
v_sub_f32 v109, v109, v249
v_sub_f32 v45, v45, v249
v_sub_f32 v110, v110, v249
v_sub_f32 v46, v46, v249
v_sub_f32 v111, v111, v249
v_sub_f32 v47, v47, v249
s_lshl_b64 exec, 0xffff, 16
v_mul_f32 v236, v236, v250
s_mov_b64 exec, -1
AR v254,a8
v_mul_f32 v254, v254, v250
AW a8,v254
AR v251,a9
v_mul_f32 v251, v251, v250
AW a9,v251
AR v254,a10
v_mul_f32 v254, v254, v250
AW a10,v254
AR v251,a11
v_mul_f32 v251, v251, v250
AW a11,v251
AR v254,a12
v_mul_f32 v254, v254, v250
AW a12,v254
AR v251,a13
v_mul_f32 v251, v251, v250
AW a13,v251
AR v254,a14
v_mul_f32 v254, v254, v250
AW a14,v254
AR v251,a15
v_mul_f32 v251, v251, v250
AW a15,v251
AR v254,a40
v_mul_f32 v254, v254, v250
AW a40,v254
AR v251,a41
v_mul_f32 v251, v251, v250
AW a41,v251
AR v254,a42
v_mul_f32 v254, v254, v250
AW a42,v254
AR v251,a43
v_mul_f32 v251, v251, v250
AW a43,v251
AR v254,a44
v_mul_f32 v254, v254, v250
AW a44,v254
AR v251,a45
v_mul_f32 v251, v251, v250
AW a45,v251
AR v254,a46
v_mul_f32 v254, v254, v250
AW a46,v254
AR v251,a47
v_mul_f32 v251, v251, v250
AW a47,v251
AR v254,a72
v_mul_f32 v254, v254, v250
AW a72,v254
AR v251,a73
v_mul_f32 v251, v251, v250
AW a73,v251
AR v254,a74
v_mul_f32 v254, v254, v250
AW a74,v254
AR v251,a75
v_mul_f32 v251, v251, v250
AW a75,v251
AR v254,a76
v_mul_f32 v254, v254, v250
AW a76,v254
AR v251,a77
v_mul_f32 v251, v251, v250
AW a77,v251
AR v254,a78
v_mul_f32 v254, v254, v250
AW a78,v254
AR v251,a79
v_mul_f32 v251, v251, v250
AW a79,v251
AR v254,a104
v_mul_f32 v254, v254, v250
AW a104,v254
AR v251,a105
v_mul_f32 v251, v251, v250
AW a105,v251
AR v254,a106
v_mul_f32 v254, v254, v250
AW a106,v254
AR v251,a107
v_mul_f32 v251, v251, v250
AW a107,v251
AR v254,a108
v_mul_f32 v254, v254, v250
AW a108,v254
AR v251,a109
v_mul_f32 v251, v251, v250
AW a109,v251
AR v254,a110
v_mul_f32 v254, v254, v250
AW a110,v254
AR v251,a111
v_mul_f32 v251, v251, v250
AW a111,v251
v_max3_f32 v249, v16, v17, v18
v_max3_f32 v251, v48, v49, v50
v_max3_f32 v249, v249, v19, v20
v_max3_f32 v251, v251, v51, v52
v_max3_f32 v249, v249, v21, v22
v_max3_f32 v251, v251, v53, v54
v_max3_f32 v249, v249, v23, v55
v_max_f32 v249, v249, v251
v_log_f32 v250, v247
s_lshl_b64 exec, 0xffff, 32
v_max_f32 v249, v249, v250
s_mov_b64 exec, -1
v_mov_b32 v251, v249
s_nop 1
v_permlane16_swap_b32 v249, v251
v_max_f32 v249, v249, v251
v_mov_b32 v251, v249
s_nop 1
v_permlane32_swap_b32 v249, v251
v_max_f32 v249, v249, v251
v_max_f32 v249, 0, v249
v_sub_f32 v250, 0, v249
EX v250,v250
v_sub_f32 v136, v136, v249
v_sub_f32 v137, v137, v249
v_sub_f32 v138, v138, v249
v_sub_f32 v139, v139, v249
v_sub_f32 v80, v80, v249
v_sub_f32 v16, v16, v249
v_sub_f32 v81, v81, v249
v_sub_f32 v17, v17, v249
v_sub_f32 v82, v82, v249
v_sub_f32 v18, v18, v249
v_sub_f32 v83, v83, v249
v_sub_f32 v19, v19, v249
v_sub_f32 v84, v84, v249
v_sub_f32 v20, v20, v249
v_sub_f32 v85, v85, v249
v_sub_f32 v21, v21, v249
v_sub_f32 v86, v86, v249
v_sub_f32 v22, v22, v249
v_sub_f32 v87, v87, v249
v_sub_f32 v23, v23, v249
v_sub_f32 v112, v112, v249
v_sub_f32 v48, v48, v249
v_sub_f32 v113, v113, v249
v_sub_f32 v49, v49, v249
v_sub_f32 v114, v114, v249
v_sub_f32 v50, v50, v249
v_sub_f32 v115, v115, v249
v_sub_f32 v51, v51, v249
v_sub_f32 v116, v116, v249
v_sub_f32 v52, v52, v249
v_sub_f32 v117, v117, v249
v_sub_f32 v53, v53, v249
v_sub_f32 v118, v118, v249
v_sub_f32 v54, v54, v249
v_sub_f32 v119, v119, v249
v_sub_f32 v55, v55, v249
s_lshl_b64 exec, 0xffff, 32
v_mul_f32 v236, v236, v250
s_mov_b64 exec, -1
AR v254,a16
v_mul_f32 v254, v254, v250
AW a16,v254
AR v251,a17
v_mul_f32 v251, v251, v250
AW a17,v251
AR v254,a18
v_mul_f32 v254, v254, v250
AW a18,v254
AR v251,a19
v_mul_f32 v251, v251, v250
AW a19,v251
AR v254,a20
v_mul_f32 v254, v254, v250
AW a20,v254
AR v251,a21
v_mul_f32 v251, v251, v250
AW a21,v251
AR v254,a22
v_mul_f32 v254, v254, v250
AW a22,v254
AR v251,a23
v_mul_f32 v251, v251, v250
AW a23,v251
AR v254,a48
v_mul_f32 v254, v254, v250
AW a48,v254
AR v251,a49
v_mul_f32 v251, v251, v250
AW a49,v251
AR v254,a50
v_mul_f32 v254, v254, v250
AW a50,v254
AR v251,a51
v_mul_f32 v251, v251, v250
AW a51,v251
AR v254,a52
v_mul_f32 v254, v254, v250
AW a52,v254
AR v251,a53
v_mul_f32 v251, v251, v250
AW a53,v251
AR v254,a54
v_mul_f32 v254, v254, v250
AW a54,v254
AR v251,a55
v_mul_f32 v251, v251, v250
AW a55,v251
AR v254,a80
v_mul_f32 v254, v254, v250
AW a80,v254
AR v251,a81
v_mul_f32 v251, v251, v250
AW a81,v251
AR v254,a82
v_mul_f32 v254, v254, v250
AW a82,v254
AR v251,a83
v_mul_f32 v251, v251, v250
AW a83,v251
AR v254,a84
v_mul_f32 v254, v254, v250
AW a84,v254
AR v251,a85
v_mul_f32 v251, v251, v250
AW a85,v251
AR v254,a86
v_mul_f32 v254, v254, v250
AW a86,v254
AR v251,a87
v_mul_f32 v251, v251, v250
AW a87,v251
AR v254,a112
v_mul_f32 v254, v254, v250
AW a112,v254
AR v251,a113
v_mul_f32 v251, v251, v250
AW a113,v251
AR v254,a114
v_mul_f32 v254, v254, v250
AW a114,v254
AR v251,a115
v_mul_f32 v251, v251, v250
AW a115,v251
AR v254,a116
v_mul_f32 v254, v254, v250
AW a116,v254
AR v251,a117
v_mul_f32 v251, v251, v250
AW a117,v251
AR v254,a118
v_mul_f32 v254, v254, v250
AW a118,v254
AR v251,a119
v_mul_f32 v251, v251, v250
AW a119,v251
v_max3_f32 v249, v24, v25, v26
v_max3_f32 v251, v56, v57, v58
v_max3_f32 v249, v249, v27, v28
v_max3_f32 v251, v251, v59, v60
v_max3_f32 v249, v249, v29, v30
v_max3_f32 v251, v251, v61, v62
v_max3_f32 v249, v249, v31, v63
v_max_f32 v249, v249, v251
v_log_f32 v250, v247
s_lshl_b64 exec, 0xffff, 48
v_max_f32 v249, v249, v250
s_mov_b64 exec, -1
v_mov_b32 v251, v249
s_nop 1
v_permlane16_swap_b32 v249, v251
v_max_f32 v249, v249, v251
v_mov_b32 v251, v249
s_nop 1
v_permlane32_swap_b32 v249, v251
v_max_f32 v249, v249, v251
v_max_f32 v249, 0, v249
v_sub_f32 v250, 0, v249
EX v250,v250
v_sub_f32 v140, v140, v249
v_sub_f32 v141, v141, v249
v_sub_f32 v142, v142, v249
v_sub_f32 v143, v143, v249
v_sub_f32 v88, v88, v249
v_sub_f32 v24, v24, v249
v_sub_f32 v89, v89, v249
v_sub_f32 v25, v25, v249
v_sub_f32 v90, v90, v249
v_sub_f32 v26, v26, v249
v_sub_f32 v91, v91, v249
v_sub_f32 v27, v27, v249
v_sub_f32 v92, v92, v249
v_sub_f32 v28, v28, v249
v_sub_f32 v93, v93, v249
v_sub_f32 v29, v29, v249
v_sub_f32 v94, v94, v249
v_sub_f32 v30, v30, v249
v_sub_f32 v95, v95, v249
v_sub_f32 v31, v31, v249
v_sub_f32 v120, v120, v249
v_sub_f32 v56, v56, v249
v_sub_f32 v121, v121, v249
v_sub_f32 v57, v57, v249
v_sub_f32 v122, v122, v249
v_sub_f32 v58, v58, v249
v_sub_f32 v123, v123, v249
v_sub_f32 v59, v59, v249
v_sub_f32 v124, v124, v249
v_sub_f32 v60, v60, v249
v_sub_f32 v125, v125, v249
v_sub_f32 v61, v61, v249
v_sub_f32 v126, v126, v249
v_sub_f32 v62, v62, v249
v_sub_f32 v127, v127, v249
v_sub_f32 v63, v63, v249
s_lshl_b64 exec, 0xffff, 48
v_mul_f32 v236, v236, v250
s_mov_b64 exec, -1
AR v254,a24
v_mul_f32 v254, v254, v250
AW a24,v254
AR v251,a25
v_mul_f32 v251, v251, v250
AW a25,v251
AR v254,a26
v_mul_f32 v254, v254, v250
AW a26,v254
AR v251,a27
v_mul_f32 v251, v251, v250
AW a27,v251
AR v254,a28
v_mul_f32 v254, v254, v250
AW a28,v254
AR v251,a29
v_mul_f32 v251, v251, v250
AW a29,v251
AR v254,a30
v_mul_f32 v254, v254, v250
AW a30,v254
AR v251,a31
v_mul_f32 v251, v251, v250
AW a31,v251
AR v254,a56
v_mul_f32 v254, v254, v250
AW a56,v254
AR v251,a57
v_mul_f32 v251, v251, v250
AW a57,v251
AR v254,a58
v_mul_f32 v254, v254, v250
AW a58,v254
AR v251,a59
v_mul_f32 v251, v251, v250
AW a59,v251
AR v254,a60
v_mul_f32 v254, v254, v250
AW a60,v254
AR v251,a61
v_mul_f32 v251, v251, v250
AW a61,v251
AR v254,a62
v_mul_f32 v254, v254, v250
AW a62,v254
AR v251,a63
v_mul_f32 v251, v251, v250
AW a63,v251
AR v254,a88
v_mul_f32 v254, v254, v250
AW a88,v254
AR v251,a89
v_mul_f32 v251, v251, v250
AW a89,v251
AR v254,a90
v_mul_f32 v254, v254, v250
AW a90,v254
AR v251,a91
v_mul_f32 v251, v251, v250
AW a91,v251
AR v254,a92
v_mul_f32 v254, v254, v250
AW a92,v254
AR v251,a93
v_mul_f32 v251, v251, v250
AW a93,v251
AR v254,a94
v_mul_f32 v254, v254, v250
AW a94,v254
AR v251,a95
v_mul_f32 v251, v251, v250
AW a95,v251
AR v254,a120
v_mul_f32 v254, v254, v250
AW a120,v254
AR v251,a121
v_mul_f32 v251, v251, v250
AW a121,v251
AR v254,a122
v_mul_f32 v254, v254, v250
AW a122,v254
AR v251,a123
v_mul_f32 v251, v251, v250
AW a123,v251
AR v254,a124
v_mul_f32 v254, v254, v250
AW a124,v254
AR v251,a125
v_mul_f32 v251, v251, v250
AW a125,v251
AR v254,a126
v_mul_f32 v254, v254, v250
AW a126,v254
AR v251,a127
v_mul_f32 v251, v251, v250
AW a127,v251
EX v0,v0
EX v1,v1
EX v2,v2
EX v3,v3
EX v4,v4
EX v5,v5
EX v6,v6
EX v7,v7
EX v8,v8
EX v9,v9
EX v10,v10
EX v11,v11
EX v12,v12
EX v13,v13
EX v14,v14
EX v15,v15
EX v16,v16
EX v17,v17
EX v18,v18
EX v19,v19
EX v20,v20
EX v21,v21
EX v22,v22
EX v23,v23
EX v24,v24
EX v25,v25
EX v26,v26
EX v27,v27
EX v28,v28
EX v29,v29
EX v30,v30
EX v31,v31
EX v32,v32
EX v33,v33
EX v34,v34
EX v35,v35
EX v36,v36
EX v37,v37
EX v38,v38
EX v39,v39
EX v40,v40
EX v41,v41
EX v42,v42
EX v43,v43
EX v44,v44
EX v45,v45
EX v46,v46
EX v47,v47
EX v48,v48
EX v49,v49
EX v50,v50
EX v51,v51
EX v52,v52
EX v53,v53
EX v54,v54
EX v55,v55
EX v56,v56
EX v57,v57
EX v58,v58
EX v59,v59
EX v60,v60
EX v61,v61
EX v62,v62
EX v63,v63
CV v0,v0,v1
CV v1,v2,v3
CV v2,v4,v5
CV v3,v6,v7
CV v8,v8,v9
CV v9,v10,v11
CV v10,v12,v13
CV v11,v14,v15
CV v16,v16,v17
CV v17,v18,v19
CV v18,v20,v21
CV v19,v22,v23
CV v24,v24,v25
CV v25,v26,v27
CV v26,v28,v29
CV v27,v30,v31
CV v4,v32,v33
CV v5,v34,v35
CV v6,v36,v37
CV v7,v38,v39
CV v12,v40,v41
CV v13,v42,v43
CV v14,v44,v45
CV v15,v46,v47
CV v20,v48,v49
CV v21,v50,v51
CV v22,v52,v53
CV v23,v54,v55
CV v28,v56,v57
CV v29,v58,v59
CV v30,v60,v61
CV v31,v62,v63
v_smfmac_f32_16x16x64_bf16 v[236:239], v[144:147], v[0:7], v234
v_smfmac_f32_16x16x64_bf16 v[236:239], v[148:151], v[8:15], v234
v_smfmac_f32_16x16x64_bf16 v[236:239], v[152:155], v[16:23], v234
v_smfmac_f32_16x16x64_bf16 v[236:239], v[156:159], v[24:31], v234
s_nop 9
v_add_f32 v246, v236, v237
s_cmp_eq_u32 s73, 4
s_cbranch_scc1 LBACK4_%=
s_cmp_eq_u32 s73, 6
s_cbranch_scc1 LBACK6_%=
s_cmp_eq_u32 s73, 8
s_cbranch_scc1 LBACK8_%=
s_cmp_eq_u32 s73, 10
s_cbranch_scc1 LBACK10_%=
s_cmp_eq_u32 s73, 12
s_cbranch_scc1 LBACK12_%=
s_cmp_eq_u32 s73, 14
s_cbranch_scc1 LBACK14_%=
s_cmp_eq_u32 s73, 16
s_cbranch_scc1 LBACK16_%=
s_cmp_eq_u32 s73, 18
s_cbranch_scc1 LBACK18_%=
s_cmp_eq_u32 s73, 20
s_cbranch_scc1 LBACK20_%=
s_cmp_eq_u32 s73, 22
s_cbranch_scc1 LBACK22_%=
s_cmp_eq_u32 s73, 24
s_cbranch_scc1 LBACK24_%=
s_cmp_eq_u32 s73, 26
s_cbranch_scc1 LBACK26_%=
)ASM"
R"ASM(
s_endpgm
LSITE28_%=:
s_mov_b32 s73, 28
s_mov_b32 s74, 0
s_mov_b32 s75, 16384
s_branch LRARE022_%=
LSITE30_%=:
s_mov_b32 s73, 30
s_mov_b32 s74, 32768
s_mov_b32 s75, 49152
s_branch LRARE022_%=
LRARE022_%=:
s_nop 15
s_waitcnt vmcnt(0) lgkmcnt(0)
v_add_u32 v249, s74, v224
RD a[192:195],v249 offset:0
RD a[208:211],v249 offset:4096
RD a[224:227],v249 offset:8192
RD a[240:243],v249 offset:12288
v_add_u32 v250, s74, v225
RD a[196:199],v250 offset:0
RD a[212:215],v250 offset:4096
RD a[228:231],v250 offset:8192
RD a[244:247],v250 offset:12288
v_add_u32 v251, s74, v226
RD a[200:203],v251 offset:0
RD a[216:219],v251 offset:4096
RD a[232:235],v251 offset:8192
RD a[248:251],v251 offset:12288
v_add_u32 v254, s74, v227
RD a[204:207],v254 offset:0
RD a[220:223],v254 offset:4096
RD a[236:239],v254 offset:8192
RD a[252:255],v254 offset:12288
s_waitcnt lgkmcnt(0)
MM v[16:19],a[192:195],v[168:171],v[136:139]
MM v[16:19],a[196:199],v[184:187],v[16:19]
MM v[16:19],a[200:203],v[200:203],v[16:19]
MM v[16:19],a[204:207],v[216:219],v[16:19]
MM v[20:23],a[220:223],v[216:219],v[136:139]
MM v[20:23],a[216:219],v[200:203],v[20:23]
MM v[20:23],a[212:215],v[184:187],v[20:23]
MM v[20:23],a[208:211],v[168:171],v[20:23]
MM v[28:31],a[208:211],v[172:175],v[140:143]
MM v[28:31],a[212:215],v[188:191],v[28:31]
MM v[28:31],a[216:219],v[204:207],v[28:31]
MM v[28:31],a[220:223],v[220:223],v[28:31]
MM v[24:27],a[204:207],v[220:223],v[140:143]
MM v[24:27],a[200:203],v[204:207],v[24:27]
MM v[24:27],a[196:199],v[188:191],v[24:27]
MM v[24:27],a[192:195],v[172:175],v[24:27]
MM v[48:51],a[224:227],v[168:171],v[136:139]
MM v[48:51],a[228:231],v[184:187],v[48:51]
MM v[48:51],a[232:235],v[200:203],v[48:51]
MM v[48:51],a[236:239],v[216:219],v[48:51]
MM v[52:55],a[252:255],v[216:219],v[136:139]
MM v[52:55],a[248:251],v[200:203],v[52:55]
MM v[52:55],a[244:247],v[184:187],v[52:55]
MM v[52:55],a[240:243],v[168:171],v[52:55]
MM v[60:63],a[240:243],v[172:175],v[140:143]
MM v[60:63],a[244:247],v[188:191],v[60:63]
MM v[60:63],a[248:251],v[204:207],v[60:63]
MM v[60:63],a[252:255],v[220:223],v[60:63]
MM v[56:59],a[236:239],v[220:223],v[140:143]
MM v[56:59],a[232:235],v[204:207],v[56:59]
MM v[56:59],a[228:231],v[188:191],v[56:59]
MM v[56:59],a[224:227],v[172:175],v[56:59]
s_nop 15
v_add_u32 v249, s75, v224
RD a[192:195],v249 offset:0
RD a[208:211],v249 offset:4096
RD a[224:227],v249 offset:8192
RD a[240:243],v249 offset:12288
v_add_u32 v250, s75, v225
RD a[196:199],v250 offset:0
RD a[212:215],v250 offset:4096
RD a[228:231],v250 offset:8192
RD a[244:247],v250 offset:12288
v_add_u32 v251, s75, v226
RD a[200:203],v251 offset:0
RD a[216:219],v251 offset:4096
RD a[232:235],v251 offset:8192
RD a[248:251],v251 offset:12288
v_add_u32 v254, s75, v227
RD a[204:207],v254 offset:0
RD a[220:223],v254 offset:4096
RD a[236:239],v254 offset:8192
RD a[252:255],v254 offset:12288
s_waitcnt lgkmcnt(0)
v_mov_b32 v236, v247
v_mov_b32 v237, 0
v_max3_f32 v249, v16, v17, v18
v_max3_f32 v251, v48, v49, v50
v_max3_f32 v249, v249, v19, v20
v_max3_f32 v251, v251, v51, v52
v_max3_f32 v249, v249, v21, v22
v_max3_f32 v251, v251, v53, v54
v_max3_f32 v249, v249, v23, v55
v_max_f32 v249, v249, v251
v_log_f32 v250, v247
s_lshl_b64 exec, 0xffff, 32
v_max_f32 v249, v249, v250
s_mov_b64 exec, -1
v_mov_b32 v251, v249
s_nop 1
v_permlane16_swap_b32 v249, v251
v_max_f32 v249, v249, v251
v_mov_b32 v251, v249
s_nop 1
v_permlane32_swap_b32 v249, v251
v_max_f32 v249, v249, v251
v_max_f32 v249, 0, v249
v_sub_f32 v250, 0, v249
EX v250,v250
v_sub_f32 v136, v136, v249
v_sub_f32 v137, v137, v249
v_sub_f32 v138, v138, v249
v_sub_f32 v139, v139, v249
v_sub_f32 v80, v80, v249
v_sub_f32 v16, v16, v249
v_sub_f32 v81, v81, v249
v_sub_f32 v17, v17, v249
v_sub_f32 v82, v82, v249
v_sub_f32 v18, v18, v249
v_sub_f32 v83, v83, v249
v_sub_f32 v19, v19, v249
v_sub_f32 v84, v84, v249
v_sub_f32 v20, v20, v249
v_sub_f32 v85, v85, v249
v_sub_f32 v21, v21, v249
v_sub_f32 v86, v86, v249
v_sub_f32 v22, v22, v249
v_sub_f32 v87, v87, v249
v_sub_f32 v23, v23, v249
v_sub_f32 v112, v112, v249
v_sub_f32 v48, v48, v249
v_sub_f32 v113, v113, v249
v_sub_f32 v49, v49, v249
v_sub_f32 v114, v114, v249
v_sub_f32 v50, v50, v249
v_sub_f32 v115, v115, v249
v_sub_f32 v51, v51, v249
v_sub_f32 v116, v116, v249
v_sub_f32 v52, v52, v249
v_sub_f32 v117, v117, v249
v_sub_f32 v53, v53, v249
v_sub_f32 v118, v118, v249
v_sub_f32 v54, v54, v249
v_sub_f32 v119, v119, v249
v_sub_f32 v55, v55, v249
s_lshl_b64 exec, 0xffff, 32
v_mul_f32 v236, v236, v250
s_mov_b64 exec, -1
AR v254,a16
v_mul_f32 v254, v254, v250
AW a16,v254
AR v251,a17
v_mul_f32 v251, v251, v250
AW a17,v251
AR v254,a18
v_mul_f32 v254, v254, v250
AW a18,v254
AR v251,a19
v_mul_f32 v251, v251, v250
AW a19,v251
AR v254,a20
v_mul_f32 v254, v254, v250
AW a20,v254
AR v251,a21
v_mul_f32 v251, v251, v250
AW a21,v251
AR v254,a22
v_mul_f32 v254, v254, v250
AW a22,v254
AR v251,a23
v_mul_f32 v251, v251, v250
AW a23,v251
AR v254,a48
v_mul_f32 v254, v254, v250
AW a48,v254
AR v251,a49
v_mul_f32 v251, v251, v250
AW a49,v251
AR v254,a50
v_mul_f32 v254, v254, v250
AW a50,v254
AR v251,a51
v_mul_f32 v251, v251, v250
AW a51,v251
AR v254,a52
v_mul_f32 v254, v254, v250
AW a52,v254
AR v251,a53
v_mul_f32 v251, v251, v250
AW a53,v251
AR v254,a54
v_mul_f32 v254, v254, v250
AW a54,v254
AR v251,a55
v_mul_f32 v251, v251, v250
AW a55,v251
AR v254,a80
v_mul_f32 v254, v254, v250
AW a80,v254
AR v251,a81
v_mul_f32 v251, v251, v250
AW a81,v251
AR v254,a82
v_mul_f32 v254, v254, v250
AW a82,v254
AR v251,a83
v_mul_f32 v251, v251, v250
AW a83,v251
AR v254,a84
v_mul_f32 v254, v254, v250
AW a84,v254
AR v251,a85
v_mul_f32 v251, v251, v250
AW a85,v251
AR v254,a86
v_mul_f32 v254, v254, v250
AW a86,v254
AR v251,a87
v_mul_f32 v251, v251, v250
AW a87,v251
AR v254,a112
v_mul_f32 v254, v254, v250
AW a112,v254
AR v251,a113
v_mul_f32 v251, v251, v250
AW a113,v251
AR v254,a114
v_mul_f32 v254, v254, v250
AW a114,v254
AR v251,a115
v_mul_f32 v251, v251, v250
AW a115,v251
AR v254,a116
v_mul_f32 v254, v254, v250
AW a116,v254
AR v251,a117
v_mul_f32 v251, v251, v250
AW a117,v251
AR v254,a118
v_mul_f32 v254, v254, v250
AW a118,v254
AR v251,a119
v_mul_f32 v251, v251, v250
AW a119,v251
v_max3_f32 v249, v24, v25, v26
v_max3_f32 v251, v56, v57, v58
v_max3_f32 v249, v249, v27, v28
v_max3_f32 v251, v251, v59, v60
v_max3_f32 v249, v249, v29, v30
v_max3_f32 v251, v251, v61, v62
v_max3_f32 v249, v249, v31, v63
v_max_f32 v249, v249, v251
v_log_f32 v250, v247
s_lshl_b64 exec, 0xffff, 48
v_max_f32 v249, v249, v250
s_mov_b64 exec, -1
v_mov_b32 v251, v249
s_nop 1
v_permlane16_swap_b32 v249, v251
v_max_f32 v249, v249, v251
v_mov_b32 v251, v249
s_nop 1
v_permlane32_swap_b32 v249, v251
v_max_f32 v249, v249, v251
v_max_f32 v249, 0, v249
v_sub_f32 v250, 0, v249
EX v250,v250
v_sub_f32 v140, v140, v249
v_sub_f32 v141, v141, v249
v_sub_f32 v142, v142, v249
v_sub_f32 v143, v143, v249
v_sub_f32 v88, v88, v249
v_sub_f32 v24, v24, v249
v_sub_f32 v89, v89, v249
v_sub_f32 v25, v25, v249
v_sub_f32 v90, v90, v249
v_sub_f32 v26, v26, v249
v_sub_f32 v91, v91, v249
v_sub_f32 v27, v27, v249
v_sub_f32 v92, v92, v249
v_sub_f32 v28, v28, v249
v_sub_f32 v93, v93, v249
v_sub_f32 v29, v29, v249
v_sub_f32 v94, v94, v249
v_sub_f32 v30, v30, v249
v_sub_f32 v95, v95, v249
v_sub_f32 v31, v31, v249
v_sub_f32 v120, v120, v249
v_sub_f32 v56, v56, v249
v_sub_f32 v121, v121, v249
v_sub_f32 v57, v57, v249
v_sub_f32 v122, v122, v249
v_sub_f32 v58, v58, v249
v_sub_f32 v123, v123, v249
v_sub_f32 v59, v59, v249
v_sub_f32 v124, v124, v249
v_sub_f32 v60, v60, v249
v_sub_f32 v125, v125, v249
v_sub_f32 v61, v61, v249
v_sub_f32 v126, v126, v249
v_sub_f32 v62, v62, v249
v_sub_f32 v127, v127, v249
v_sub_f32 v63, v63, v249
s_lshl_b64 exec, 0xffff, 48
v_mul_f32 v236, v236, v250
s_mov_b64 exec, -1
AR v254,a24
v_mul_f32 v254, v254, v250
AW a24,v254
AR v251,a25
v_mul_f32 v251, v251, v250
AW a25,v251
AR v254,a26
v_mul_f32 v254, v254, v250
AW a26,v254
AR v251,a27
v_mul_f32 v251, v251, v250
AW a27,v251
AR v254,a28
v_mul_f32 v254, v254, v250
AW a28,v254
AR v251,a29
v_mul_f32 v251, v251, v250
AW a29,v251
AR v254,a30
v_mul_f32 v254, v254, v250
AW a30,v254
AR v251,a31
v_mul_f32 v251, v251, v250
AW a31,v251
AR v254,a56
v_mul_f32 v254, v254, v250
AW a56,v254
AR v251,a57
v_mul_f32 v251, v251, v250
AW a57,v251
AR v254,a58
v_mul_f32 v254, v254, v250
AW a58,v254
AR v251,a59
v_mul_f32 v251, v251, v250
AW a59,v251
AR v254,a60
v_mul_f32 v254, v254, v250
AW a60,v254
AR v251,a61
v_mul_f32 v251, v251, v250
AW a61,v251
AR v254,a62
v_mul_f32 v254, v254, v250
AW a62,v254
AR v251,a63
v_mul_f32 v251, v251, v250
AW a63,v251
AR v254,a88
v_mul_f32 v254, v254, v250
AW a88,v254
AR v251,a89
v_mul_f32 v251, v251, v250
AW a89,v251
AR v254,a90
v_mul_f32 v254, v254, v250
AW a90,v254
AR v251,a91
v_mul_f32 v251, v251, v250
AW a91,v251
AR v254,a92
v_mul_f32 v254, v254, v250
AW a92,v254
AR v251,a93
v_mul_f32 v251, v251, v250
AW a93,v251
AR v254,a94
v_mul_f32 v254, v254, v250
AW a94,v254
AR v251,a95
v_mul_f32 v251, v251, v250
AW a95,v251
AR v254,a120
v_mul_f32 v254, v254, v250
AW a120,v254
AR v251,a121
v_mul_f32 v251, v251, v250
AW a121,v251
AR v254,a122
v_mul_f32 v254, v254, v250
AW a122,v254
AR v251,a123
v_mul_f32 v251, v251, v250
AW a123,v251
AR v254,a124
v_mul_f32 v254, v254, v250
AW a124,v254
AR v251,a125
v_mul_f32 v251, v251, v250
AW a125,v251
AR v254,a126
v_mul_f32 v254, v254, v250
AW a126,v254
AR v251,a127
v_mul_f32 v251, v251, v250
AW a127,v251
EX v16,v16
EX v17,v17
EX v18,v18
EX v19,v19
EX v20,v20
EX v21,v21
EX v22,v22
EX v23,v23
EX v24,v24
EX v25,v25
EX v26,v26
EX v27,v27
EX v28,v28
EX v29,v29
EX v30,v30
EX v31,v31
EX v48,v48
EX v49,v49
EX v50,v50
EX v51,v51
EX v52,v52
EX v53,v53
EX v54,v54
EX v55,v55
EX v56,v56
EX v57,v57
EX v58,v58
EX v59,v59
EX v60,v60
EX v61,v61
EX v62,v62
EX v63,v63
CV v16,v16,v17
CV v17,v18,v19
CV v18,v20,v21
CV v19,v22,v23
CV v24,v24,v25
CV v25,v26,v27
CV v26,v28,v29
CV v27,v30,v31
CV v20,v48,v49
CV v21,v50,v51
CV v22,v52,v53
CV v23,v54,v55
CV v28,v56,v57
CV v29,v58,v59
CV v30,v60,v61
CV v31,v62,v63
v_smfmac_f32_16x16x64_bf16 v[236:239], v[152:155], v[16:23], v234
s_nop 0
v_smfmac_f32_16x16x64_bf16 v[236:239], v[156:159], v[24:31], v234
s_nop 9
v_add_f32 v246, v236, v237
s_cmp_eq_u32 s73, 28
s_cbranch_scc1 LBACK28_%=
s_cmp_eq_u32 s73, 30
s_cbranch_scc1 LBACK30_%=
s_endpgm
LSITE29_%=:
s_mov_b32 s73, 29
s_mov_b32 s74, 16384
s_mov_b32 s75, 32768
s_branch LRARE122_%=
LRARE122_%=:
s_nop 15
s_waitcnt vmcnt(0) lgkmcnt(0)
v_add_u32 v249, s74, v224
RD a[192:195],v249 offset:0
RD a[208:211],v249 offset:4096
RD a[224:227],v249 offset:8192
RD a[240:243],v249 offset:12288
v_add_u32 v250, s74, v225
RD a[196:199],v250 offset:0
RD a[212:215],v250 offset:4096
RD a[228:231],v250 offset:8192
RD a[244:247],v250 offset:12288
v_add_u32 v251, s74, v226
RD a[200:203],v251 offset:0
RD a[216:219],v251 offset:4096
RD a[232:235],v251 offset:8192
RD a[248:251],v251 offset:12288
v_add_u32 v254, s74, v227
RD a[204:207],v254 offset:0
RD a[220:223],v254 offset:4096
RD a[236:239],v254 offset:8192
RD a[252:255],v254 offset:12288
s_waitcnt lgkmcnt(0)
MM v[80:83],a[192:195],v[168:171],v[136:139]
MM v[80:83],a[196:199],v[184:187],v[80:83]
MM v[80:83],a[200:203],v[200:203],v[80:83]
MM v[80:83],a[204:207],v[216:219],v[80:83]
MM v[84:87],a[220:223],v[216:219],v[136:139]
MM v[84:87],a[216:219],v[200:203],v[84:87]
MM v[84:87],a[212:215],v[184:187],v[84:87]
MM v[84:87],a[208:211],v[168:171],v[84:87]
MM v[92:95],a[208:211],v[172:175],v[140:143]
MM v[92:95],a[212:215],v[188:191],v[92:95]
MM v[92:95],a[216:219],v[204:207],v[92:95]
MM v[92:95],a[220:223],v[220:223],v[92:95]
MM v[88:91],a[204:207],v[220:223],v[140:143]
MM v[88:91],a[200:203],v[204:207],v[88:91]
MM v[88:91],a[196:199],v[188:191],v[88:91]
MM v[88:91],a[192:195],v[172:175],v[88:91]
MM v[112:115],a[224:227],v[168:171],v[136:139]
MM v[112:115],a[228:231],v[184:187],v[112:115]
MM v[112:115],a[232:235],v[200:203],v[112:115]
MM v[112:115],a[236:239],v[216:219],v[112:115]
MM v[116:119],a[252:255],v[216:219],v[136:139]
MM v[116:119],a[248:251],v[200:203],v[116:119]
MM v[116:119],a[244:247],v[184:187],v[116:119]
)ASM"
R"ASM(
MM v[116:119],a[240:243],v[168:171],v[116:119]
MM v[124:127],a[240:243],v[172:175],v[140:143]
MM v[124:127],a[244:247],v[188:191],v[124:127]
MM v[124:127],a[248:251],v[204:207],v[124:127]
MM v[124:127],a[252:255],v[220:223],v[124:127]
MM v[120:123],a[236:239],v[220:223],v[140:143]
MM v[120:123],a[232:235],v[204:207],v[120:123]
MM v[120:123],a[228:231],v[188:191],v[120:123]
MM v[120:123],a[224:227],v[172:175],v[120:123]
s_nop 15
v_add_u32 v249, s75, v224
RD a[192:195],v249 offset:0
RD a[208:211],v249 offset:4096
RD a[224:227],v249 offset:8192
RD a[240:243],v249 offset:12288
v_add_u32 v250, s75, v225
RD a[196:199],v250 offset:0
RD a[212:215],v250 offset:4096
RD a[228:231],v250 offset:8192
RD a[244:247],v250 offset:12288
v_add_u32 v251, s75, v226
RD a[200:203],v251 offset:0
RD a[216:219],v251 offset:4096
RD a[232:235],v251 offset:8192
RD a[248:251],v251 offset:12288
v_add_u32 v254, s75, v227
RD a[204:207],v254 offset:0
RD a[220:223],v254 offset:4096
RD a[236:239],v254 offset:8192
RD a[252:255],v254 offset:12288
s_waitcnt lgkmcnt(0)
v_mov_b32 v236, v246
v_mov_b32 v237, 0
v_max3_f32 v249, v80, v81, v82
v_max3_f32 v251, v112, v113, v114
v_max3_f32 v249, v249, v83, v84
v_max3_f32 v251, v251, v115, v116
v_max3_f32 v249, v249, v85, v86
v_max3_f32 v251, v251, v117, v118
v_max3_f32 v249, v249, v87, v119
v_max_f32 v249, v249, v251
v_log_f32 v250, v246
s_lshl_b64 exec, 0xffff, 32
v_max_f32 v249, v249, v250
s_mov_b64 exec, -1
v_mov_b32 v251, v249
s_nop 1
v_permlane16_swap_b32 v249, v251
v_max_f32 v249, v249, v251
v_mov_b32 v251, v249
s_nop 1
v_permlane32_swap_b32 v249, v251
v_max_f32 v249, v249, v251
v_max_f32 v249, 0, v249
v_sub_f32 v250, 0, v249
EX v250,v250
v_sub_f32 v136, v136, v249
v_sub_f32 v137, v137, v249
v_sub_f32 v138, v138, v249
v_sub_f32 v139, v139, v249
v_sub_f32 v16, v16, v249
v_sub_f32 v80, v80, v249
v_sub_f32 v17, v17, v249
v_sub_f32 v81, v81, v249
v_sub_f32 v18, v18, v249
v_sub_f32 v82, v82, v249
v_sub_f32 v19, v19, v249
v_sub_f32 v83, v83, v249
v_sub_f32 v20, v20, v249
v_sub_f32 v84, v84, v249
v_sub_f32 v21, v21, v249
v_sub_f32 v85, v85, v249
v_sub_f32 v22, v22, v249
v_sub_f32 v86, v86, v249
v_sub_f32 v23, v23, v249
v_sub_f32 v87, v87, v249
v_sub_f32 v48, v48, v249
v_sub_f32 v112, v112, v249
v_sub_f32 v49, v49, v249
v_sub_f32 v113, v113, v249
v_sub_f32 v50, v50, v249
v_sub_f32 v114, v114, v249
v_sub_f32 v51, v51, v249
v_sub_f32 v115, v115, v249
v_sub_f32 v52, v52, v249
v_sub_f32 v116, v116, v249
v_sub_f32 v53, v53, v249
v_sub_f32 v117, v117, v249
v_sub_f32 v54, v54, v249
v_sub_f32 v118, v118, v249
v_sub_f32 v55, v55, v249
v_sub_f32 v119, v119, v249
s_lshl_b64 exec, 0xffff, 32
v_mul_f32 v236, v236, v250
s_mov_b64 exec, -1
AR v254,a16
v_mul_f32 v254, v254, v250
AW a16,v254
AR v251,a17
v_mul_f32 v251, v251, v250
AW a17,v251
AR v254,a18
v_mul_f32 v254, v254, v250
AW a18,v254
AR v251,a19
v_mul_f32 v251, v251, v250
AW a19,v251
AR v254,a20
v_mul_f32 v254, v254, v250
AW a20,v254
AR v251,a21
v_mul_f32 v251, v251, v250
AW a21,v251
AR v254,a22
v_mul_f32 v254, v254, v250
AW a22,v254
AR v251,a23
v_mul_f32 v251, v251, v250
AW a23,v251
AR v254,a48
v_mul_f32 v254, v254, v250
AW a48,v254
AR v251,a49
v_mul_f32 v251, v251, v250
AW a49,v251
AR v254,a50
v_mul_f32 v254, v254, v250
AW a50,v254
AR v251,a51
v_mul_f32 v251, v251, v250
AW a51,v251
AR v254,a52
v_mul_f32 v254, v254, v250
AW a52,v254
AR v251,a53
v_mul_f32 v251, v251, v250
AW a53,v251
AR v254,a54
v_mul_f32 v254, v254, v250
AW a54,v254
AR v251,a55
v_mul_f32 v251, v251, v250
AW a55,v251
AR v254,a80
v_mul_f32 v254, v254, v250
AW a80,v254
AR v251,a81
v_mul_f32 v251, v251, v250
AW a81,v251
AR v254,a82
v_mul_f32 v254, v254, v250
AW a82,v254
AR v251,a83
v_mul_f32 v251, v251, v250
AW a83,v251
AR v254,a84
v_mul_f32 v254, v254, v250
AW a84,v254
AR v251,a85
v_mul_f32 v251, v251, v250
AW a85,v251
AR v254,a86
v_mul_f32 v254, v254, v250
AW a86,v254
AR v251,a87
v_mul_f32 v251, v251, v250
AW a87,v251
AR v254,a112
v_mul_f32 v254, v254, v250
AW a112,v254
AR v251,a113
v_mul_f32 v251, v251, v250
AW a113,v251
AR v254,a114
v_mul_f32 v254, v254, v250
AW a114,v254
AR v251,a115
v_mul_f32 v251, v251, v250
AW a115,v251
AR v254,a116
v_mul_f32 v254, v254, v250
AW a116,v254
AR v251,a117
v_mul_f32 v251, v251, v250
AW a117,v251
AR v254,a118
v_mul_f32 v254, v254, v250
AW a118,v254
AR v251,a119
v_mul_f32 v251, v251, v250
AW a119,v251
v_max3_f32 v249, v88, v89, v90
v_max3_f32 v251, v120, v121, v122
v_max3_f32 v249, v249, v91, v92
v_max3_f32 v251, v251, v123, v124
v_max3_f32 v249, v249, v93, v94
v_max3_f32 v251, v251, v125, v126
v_max3_f32 v249, v249, v95, v127
v_max_f32 v249, v249, v251
v_log_f32 v250, v246
s_lshl_b64 exec, 0xffff, 48
v_max_f32 v249, v249, v250
s_mov_b64 exec, -1
v_mov_b32 v251, v249
s_nop 1
v_permlane16_swap_b32 v249, v251
v_max_f32 v249, v249, v251
v_mov_b32 v251, v249
s_nop 1
v_permlane32_swap_b32 v249, v251
v_max_f32 v249, v249, v251
v_max_f32 v249, 0, v249
v_sub_f32 v250, 0, v249
EX v250,v250
v_sub_f32 v140, v140, v249
v_sub_f32 v141, v141, v249
v_sub_f32 v142, v142, v249
v_sub_f32 v143, v143, v249
v_sub_f32 v24, v24, v249
v_sub_f32 v88, v88, v249
v_sub_f32 v25, v25, v249
v_sub_f32 v89, v89, v249
v_sub_f32 v26, v26, v249
v_sub_f32 v90, v90, v249
v_sub_f32 v27, v27, v249
v_sub_f32 v91, v91, v249
v_sub_f32 v28, v28, v249
v_sub_f32 v92, v92, v249
v_sub_f32 v29, v29, v249
v_sub_f32 v93, v93, v249
v_sub_f32 v30, v30, v249
v_sub_f32 v94, v94, v249
v_sub_f32 v31, v31, v249
v_sub_f32 v95, v95, v249
v_sub_f32 v56, v56, v249
v_sub_f32 v120, v120, v249
v_sub_f32 v57, v57, v249
v_sub_f32 v121, v121, v249
v_sub_f32 v58, v58, v249
v_sub_f32 v122, v122, v249
v_sub_f32 v59, v59, v249
v_sub_f32 v123, v123, v249
v_sub_f32 v60, v60, v249
v_sub_f32 v124, v124, v249
v_sub_f32 v61, v61, v249
v_sub_f32 v125, v125, v249
v_sub_f32 v62, v62, v249
v_sub_f32 v126, v126, v249
v_sub_f32 v63, v63, v249
v_sub_f32 v127, v127, v249
s_lshl_b64 exec, 0xffff, 48
v_mul_f32 v236, v236, v250
s_mov_b64 exec, -1
AR v254,a24
v_mul_f32 v254, v254, v250
AW a24,v254
AR v251,a25
v_mul_f32 v251, v251, v250
AW a25,v251
AR v254,a26
v_mul_f32 v254, v254, v250
AW a26,v254
AR v251,a27
v_mul_f32 v251, v251, v250
AW a27,v251
AR v254,a28
v_mul_f32 v254, v254, v250
AW a28,v254
AR v251,a29
v_mul_f32 v251, v251, v250
AW a29,v251
AR v254,a30
v_mul_f32 v254, v254, v250
AW a30,v254
AR v251,a31
v_mul_f32 v251, v251, v250
AW a31,v251
AR v254,a56
v_mul_f32 v254, v254, v250
AW a56,v254
AR v251,a57
v_mul_f32 v251, v251, v250
AW a57,v251
AR v254,a58
v_mul_f32 v254, v254, v250
AW a58,v254
AR v251,a59
v_mul_f32 v251, v251, v250
AW a59,v251
AR v254,a60
v_mul_f32 v254, v254, v250
AW a60,v254
AR v251,a61
v_mul_f32 v251, v251, v250
AW a61,v251
AR v254,a62
v_mul_f32 v254, v254, v250
AW a62,v254
AR v251,a63
v_mul_f32 v251, v251, v250
AW a63,v251
AR v254,a88
v_mul_f32 v254, v254, v250
AW a88,v254
AR v251,a89
v_mul_f32 v251, v251, v250
AW a89,v251
AR v254,a90
v_mul_f32 v254, v254, v250
AW a90,v254
AR v251,a91
v_mul_f32 v251, v251, v250
AW a91,v251
AR v254,a92
v_mul_f32 v254, v254, v250
AW a92,v254
AR v251,a93
v_mul_f32 v251, v251, v250
AW a93,v251
AR v254,a94
v_mul_f32 v254, v254, v250
AW a94,v254
AR v251,a95
v_mul_f32 v251, v251, v250
AW a95,v251
AR v254,a120
v_mul_f32 v254, v254, v250
AW a120,v254
AR v251,a121
v_mul_f32 v251, v251, v250
AW a121,v251
AR v254,a122
v_mul_f32 v254, v254, v250
AW a122,v254
AR v251,a123
v_mul_f32 v251, v251, v250
AW a123,v251
AR v254,a124
v_mul_f32 v254, v254, v250
AW a124,v254
AR v251,a125
v_mul_f32 v251, v251, v250
AW a125,v251
AR v254,a126
v_mul_f32 v254, v254, v250
AW a126,v254
AR v251,a127
v_mul_f32 v251, v251, v250
AW a127,v251
EX v80,v80
EX v81,v81
EX v82,v82
EX v83,v83
EX v84,v84
EX v85,v85
EX v86,v86
EX v87,v87
EX v88,v88
EX v89,v89
EX v90,v90
EX v91,v91
EX v92,v92
EX v93,v93
EX v94,v94
EX v95,v95
EX v112,v112
EX v113,v113
EX v114,v114
EX v115,v115
EX v116,v116
EX v117,v117
EX v118,v118
EX v119,v119
EX v120,v120
EX v121,v121
EX v122,v122
EX v123,v123
EX v124,v124
EX v125,v125
EX v126,v126
EX v127,v127
CV v80,v80,v81
CV v81,v82,v83
CV v82,v84,v85
CV v83,v86,v87
CV v88,v88,v89
CV v89,v90,v91
CV v90,v92,v93
CV v91,v94,v95
CV v84,v112,v113
CV v85,v114,v115
CV v86,v116,v117
CV v87,v118,v119
CV v92,v120,v121
CV v93,v122,v123
CV v94,v124,v125
CV v95,v126,v127
v_smfmac_f32_16x16x64_bf16 v[236:239], v[152:155], v[80:87], v234
s_nop 0
v_smfmac_f32_16x16x64_bf16 v[236:239], v[156:159], v[88:95], v234
s_nop 9
v_add_f32 v247, v236, v237
s_cmp_eq_u32 s73, 29
s_cbranch_scc1 LBACK29_%=
s_endpgm
LEND_%=:
)ASM"

    :: [ka] "s"(__builtin_amdgcn_kernarg_segment_ptr()), [slds] "s"(slds), [swave] "s"(w), [sb] "s"(blockIdx.x)
    : "memory", "vcc", "scc", CLOB_V, CLOB_A, CLOB_S);
}

extern "C" void kernel_launch(void* const* d_in, const int* in_sizes, int n_in, void* d_out, int out_size, void* d_ws, size_t ws_size, hipStream_t stream) {
  hipFuncSetAttribute((const void*)ka_fused, hipFuncAttributeMaxDynamicSharedMemorySize, 163840);
  ka_fused<<<256, 256, 163840, stream>>>((const unsigned short*)d_in[0], (const unsigned short*)d_in[1], (const unsigned short*)d_in[2], (const unsigned short*)d_in[3],
                                         (const unsigned short*)d_in[4], (const unsigned short*)d_in[6], (const unsigned short*)d_in[7], (const unsigned short*)d_in[8],
                                         (const unsigned short*)d_in[9], (char*)d_ws, (unsigned short*)d_out
                                         );
}
```

```cpp
#include <hip/hip_runtime.h>
#include <stdint.h>

using f32x4  = __attribute__((ext_vector_type(4))) float;
using u32x4  = __attribute__((ext_vector_type(4))) unsigned;
using u32x2  = __attribute__((ext_vector_type(2))) unsigned;

constexpr int SEQ = 2048, DM = 640, NH = 32, DH = 128, HD = NH * DH, FF = 1728, TOPC = 8;

constexpr unsigned WS_FO  = 0;
constexpr unsigned WS_EP  = 32768;
constexpr unsigned WS_G1  = 36864;
constexpr unsigned WS_G2  = 53248;
constexpr unsigned WS_FH  = 69632;
constexpr unsigned WS_FY  = 73728;
constexpr unsigned WS_FG  = 77824;
constexpr unsigned WS_FR  = 81920;
constexpr unsigned WS_IX  = 86016;
constexpr unsigned WS_XN  = 131072;
constexpr unsigned WS_OF  = WS_XN + SEQ * DM * 2;
constexpr unsigned WS_H   = WS_OF + (18u << 20);
constexpr unsigned WS_Y   = WS_H + 32768;
constexpr unsigned WS_P   = WS_Y + 1048576;
constexpr unsigned WS_SPAN = 64u << 20;
constexpr int DN_FIRST = 76, YP = 18, SPIN = 30000;

#define CLOB_V "v0","v1","v2","v3","v4","v5","v6","v7","v8","v9","v10","v11","v12","v13","v14","v15","v16","v17","v18","v19","v20","v21","v22","v23","v24","v25","v26","v27","v28","v29","v30","v31","v32","v33","v34","v35","v36","v37","v38","v39","v40","v41","v42","v43","v44","v45","v46","v47","v48","v49","v50","v51","v52","v53","v54","v55","v56","v57","v58","v59","v60","v61","v62","v63","v64","v65","v66","v67","v68","v69","v70","v71","v72","v73","v74","v75","v76","v77","v78","v79","v80","v81","v82","v83","v84","v85","v86","v87","v88","v89","v90","v91","v92","v93","v94","v95","v96","v97","v98","v99","v100","v101","v102","v103","v104","v105","v106","v107","v108","v109","v110","v111","v112","v113","v114","v115","v116","v117","v118","v119","v120","v121","v122","v123","v124","v125","v126","v127","v128","v129","v130","v131","v132","v133","v134","v135","v136","v137","v138","v139","v140","v141","v142","v143","v144","v145","v146","v147","v148","v149","v150","v151","v152","v153","v154","v155","v156","v157","v158","v159","v160","v161","v162","v163","v164","v165","v166","v167","v168","v169","v170","v171","v172","v173","v174","v175","v176","v177","v178","v179","v180","v181","v182","v183","v184","v185","v186","v187","v188","v189","v190","v191","v192","v193","v194","v195","v196","v197","v198","v199","v200","v201","v202","v203","v204","v205","v206","v207","v208","v209","v210","v211","v212","v213","v214","v215","v216","v217","v218","v219","v220","v221","v222","v223","v224","v225","v226","v227","v228","v229","v230","v231","v232","v233","v234","v235","v236","v237","v238","v239","v240","v241","v242","v243","v244","v245","v246","v247","v248","v249","v250","v251","v252","v253","v254","v255"
#define CLOB_A "a0","a1","a2","a3","a4","a5","a6","a7","a8","a9","a10","a11","a12","a13","a14","a15","a16","a17","a18","a19","a20","a21","a22","a23","a24","a25","a26","a27","a28","a29","a30","a31","a32","a33","a34","a35","a36","a37","a38","a39","a40","a41","a42","a43","a44","a45","a46","a47","a48","a49","a50","a51","a52","a53","a54","a55","a56","a57","a58","a59","a60","a61","a62","a63","a64","a65","a66","a67","a68","a69","a70","a71","a72","a73","a74","a75","a76","a77","a78","a79","a80","a81","a82","a83","a84","a85","a86","a87","a88","a89","a90","a91","a92","a93","a94","a95","a96","a97","a98","a99","a100","a101","a102","a103","a104","a105","a106","a107","a108","a109","a110","a111","a112","a113","a114","a115","a116","a117","a118","a119","a120","a121","a122","a123","a124","a125","a126","a127","a128","a129","a130","a131","a132","a133","a134","a135","a136","a137","a138","a139","a140","a141","a142","a143","a144","a145","a146","a147","a148","a149","a150","a151","a152","a153","a154","a155","a156","a157","a158","a159","a160","a161","a162","a163","a164","a165","a166","a167","a168","a169","a170","a171","a172","a173","a174","a175","a176","a177","a178","a179","a180","a181","a182","a183","a184","a185","a186","a187","a188","a189","a190","a191","a192","a193","a194","a195","a196","a197","a198","a199","a200","a201","a202","a203","a204","a205","a206","a207","a208","a209","a210","a211","a212","a213","a214","a215","a216","a217","a218","a219","a220","a221","a222","a223","a224","a225","a226","a227","a228","a229","a230","a231","a232","a233","a234","a235","a236","a237","a238","a239","a240","a241","a242","a243","a244","a245","a246","a247","a248","a249","a250","a251","a252","a253","a254","a255"
#define CLOB_S "s8","s9","s10","s11","s12","s13","s14","s15","s16","s17","s18","s19","s20","s21","s22","s23","s24","s25","s26","s27","s28","s29","s30","s31","s32","s33","s34","s35","s36","s37","s38","s39","s40","s41","s42","s43","s44","s45","s46","s47","s48","s49","s50","s51","s52","s53","s54","s55","s56","s57","s58","s59","s60","s61","s62","s63","s64","s65","s66","s67","s68","s69","s70","s71","s72","s73","s74","s75","s76","s77","s78","s79","s80","s81","s82","s83","s84","s85","s86","s87","s88","s89","s90","s91","s92","s93","s94","s95","s96","s97","s98","s99"
__global__ __launch_bounds__(256, 1) __attribute__((aligned(4096))) void ka_fused(const unsigned short* __restrict__ x, const unsigned short* __restrict__ Kc, const unsigned short* __restrict__ Vc,
                                                   const unsigned short* __restrict__ wq, const unsigned short* __restrict__ wo,
                                                   const unsigned short* __restrict__ wr, const unsigned short* __restrict__ wg, const unsigned short* __restrict__ wu,
                                                   const unsigned short* __restrict__ wd,
                                                   char* __restrict__ ws, unsigned short* __restrict__ out
                                                   ) {
  extern __shared__ __attribute__((aligned(16))) char lds[];
  const unsigned w = __builtin_amdgcn_readfirstlane(threadIdx.x >> 6);
  const unsigned slds = __builtin_amdgcn_readfirstlane((unsigned)(size_t)lds);
  asm volatile(
R"ASM(
.macro MM a, b, c, d:vararg
v_mfma_f32_16x16x32_bf16 \a, \b, \c, \d
.endm
.macro TR a, b:vararg
ds_read_b64_tr_b16 \a, \b
.endm
.macro CV a, b, c:vararg
v_cvt_pk_bf16_f32 \a, \b, \c
.endm
.macro AR a, b:vararg
v_accvgpr_read_b32 \a, \b
.endm
.macro AW a, b:vararg
v_accvgpr_write_b32 \a, \b
.endm
.macro EX a, b:vararg
v_exp_f32 \a, \b
.endm
.macro RD a, b:vararg
ds_read_b128 \a, \b
.endm
s_branch LPG0_%=
.p2align 6
LPG0_%=:
s_load_dwordx2 s[16:17], %[ka], 24
s_load_dwordx2 s[32:33], %[ka], 0
s_load_dwordx2 s[20:21], %[ka], 72
s_load_dwordx2 s[8:9], %[ka], 8
s_load_dwordx2 s[12:13], %[ka], 16
s_load_dwordx2 s[24:25], %[ka], 32
s_load_dwordx2 s[44:45], %[ka], 40
s_load_dwordx2 s[28:29], %[ka], 64
s_load_dwordx2 s[40:41], %[ka], 80
s_lshl_b32 s77, %[swave], 2
s_and_b32 s77, s77, 8
s_add_u32 s77, s77, 48
s_load_dwordx2 s[36:37], %[ka], s77
v_mbcnt_lo_u32_b32 v127, -1, 0
v_mbcnt_hi_u32_b32 v127, -1, v127
v_lshlrev_b32 v248, 4, v127
s_lshr_b32 s77, %[sb], 3
s_lshl_b32 s77, s77, 2
s_add_u32 s77, s77, %[swave]
s_lshl_b32 s77, s77, 10
v_add_u32 v118, s77, v248
s_mov_b32 s77, LKEND_ka-LPC_%=-16
v_min_u32 v118, s77, v118
s_getpc_b64 s[98:99]
LPC_%=:
global_load_dwordx4 a[252:255], v118, s[98:99]
s_and_b32 s77, %[sb], 7
s_lshl_b32 s77, s77, 2
s_bfe_u32 s84, %[sb], 131075
s_add_u32 s49, s77, s84
s_lshr_b32 s50, %[sb], 5
s_lshr_b32 s76, %[sb], 8
s_lshl_b32 s76, s76, 7
s_and_b32 s77, %[sb], 127
s_or_b32 s76, s76, s77
s_lshl_b32 s76, s76, 2
s_add_u32 s76, s76, %[swave]
s_lshl_b32 s76, s76, 1
s_bitcmp1_b32 %[sb], 7
s_cselect_b64 s[52:53], -1, 0
s_and_b32 s54, s52, 1
s_mov_b32 s55, s54
s_and_b32 s54, s52, 0x10001
s_mov_b32 s55, s54
v_lshrrev_b32 v180, 5, v127
s_mul_i32 s82, s76, 2560
v_and_b32 v182, 240, v248
v_lshrrev_b32 v181, 4, v127
v_mul_u32_u24 v168, 1280, v181
v_add_u32 v168, v168, v182
s_lshl_b32 s77, %[swave], 12
s_add_u32 s65, s77, %[slds]
s_lshl_b32 s89, %[swave], 17
v_bfe_u32 v210, v127, 2, 3
v_and_b32 v212, 3, v127
v_lshlrev_b32 v213, 4, v212
v_lshl_add_u32 v213, v180, 6, v213
v_lshl_add_u32 v253, v210, 13, v213
v_add_u32 v254, 128, v253
s_mul_i32 s96, %[swave], 4112
s_add_u32 s96, s96, %[slds]
s_lshl_b32 s77, s50, 2
s_add_u32 s77, s77, %[swave]
s_mul_i32 s68, s77, 81920
s_mul_i32 s83, s77, 560128
s_add_u32 s83, s83, 2752512
s_lshl_b32 s93, s77, 9
s_lshl_b32 s94, %[swave], 13
s_add_u32 s94, s94, %[slds]
v_lshrrev_b32 v211, 3, v127
v_mul_u32_u24 v220, 1280, v211
v_lshrrev_b32 v212, 4, v127
v_and_b32 v211, 7, v127
v_xor_b32 v211, v211, v212
v_lshl_add_u32 v220, v211, 4, v220
v_xor_b32 v221, 64, v220
s_bitcmp1_b32 s84, 0
s_mov_b32 s77, 0x77552200
s_cselect_b32 s77, 0x77552200, s77
s_lshl_b32 s91, s50, 2
s_lshr_b32 s77, s77, s91
s_and_b32 s77, s77, 15
s_lshl_b32 s86, s77, 7
s_lshl_b32 s85, s77, 19
s_lshl_b32 s90, s49, 8
s_waitcnt lgkmcnt(0)
s_add_u32 s16, s16, s90
s_addc_u32 s17, s17, 0
s_sub_u32 s18, 5242880, s90
s_mov_b32 s19, 131072
s_mov_b32 s34, 2621440
s_mov_b32 s35, 131072
s_mov_b32 s22, 67108864
s_mov_b32 s23, 131072
s_bitcmp1_b32 %[sb], 7
s_cbranch_scc1 LCA_%=
s_add_u32 s87, s89, s85
s_add_u32 s85, s85, 524288
s_cmp_ge_u32 s85, 5242880
s_cselect_b32 s85, 0, s85
s_add_u32 m0, s96, 65536
s_nop 0
buffer_load_dwordx4 v253, s[16:19], s87 offen offset:0 lds
s_add_u32 m0, s96, 66560
s_nop 0
buffer_load_dwordx4 v254, s[16:19], s87 offen offset:0 lds
s_add_u32 s87, s87, 65536
s_add_u32 m0, s96, 67592
s_nop 0
buffer_load_dwordx4 v253, s[16:19], s87 offen offset:0 lds
s_add_u32 m0, s96, 68616
s_nop 0
buffer_load_dwordx4 v254, s[16:19], s87 offen offset:0 lds
s_add_u32 s88, s68, s86
s_add_u32 s86, s86, 128
s_cmp_ge_u32 s86, 1280
s_cselect_b32 s86, 0, s86
s_add_u32 m0, s94, 0
s_add_u32 s95, s88, 0
buffer_load_dwordx4 v220, s[32:35], s95 offen offset:0 lds
s_add_u32 s95, s88, 9216
buffer_load_dwordx4 v221, s[32:35], s95 offen offset:1024 lds
s_add_u32 s95, s88, 18432
buffer_load_dwordx4 v220, s[32:35], s95 offen offset:2048 lds
s_add_u32 s95, s88, 27648
buffer_load_dwordx4 v221, s[32:35], s95 offen offset:3072 lds
s_add_u32 m0, s94, 4096
s_add_u32 s95, s88, 40960
buffer_load_dwordx4 v220, s[32:35], s95 offen offset:0 lds
s_add_u32 s95, s88, 50176
buffer_load_dwordx4 v221, s[32:35], s95 offen offset:1024 lds
s_add_u32 s95, s88, 59392
buffer_load_dwordx4 v220, s[32:35], s95 offen offset:2048 lds
s_add_u32 s95, s88, 68608
buffer_load_dwordx4 v221, s[32:35], s95 offen offset:3072 lds
s_add_u32 s87, s89, s85
s_add_u32 s85, s85, 524288
s_cmp_ge_u32 s85, 5242880
s_cselect_b32 s85, 0, s85
s_add_u32 m0, s96, 81984
s_nop 0
buffer_load_dwordx4 v253, s[16:19], s87 offen offset:0 lds
s_add_u32 m0, s96, 83008
s_nop 0
buffer_load_dwordx4 v254, s[16:19], s87 offen offset:0 lds
s_add_u32 s87, s87, 65536
s_add_u32 m0, s96, 84040
s_nop 0
buffer_load_dwordx4 v253, s[16:19], s87 offen offset:0 lds
s_add_u32 m0, s96, 85064
s_nop 0
buffer_load_dwordx4 v254, s[16:19], s87 offen offset:0 lds
s_branch LCT_%=
LCA_%=:
buffer_load_dwordx4 v[128:131], v168, s[32:35], s82 offen offset:0
buffer_load_dwordx4 v[132:135], v168, s[32:35], s82 offen offset:256
buffer_load_dwordx4 v[136:139], v168, s[32:35], s82 offen offset:512
buffer_load_dwordx4 v[140:143], v168, s[32:35], s82 offen offset:768
buffer_load_dwordx4 v[144:147], v168, s[32:35], s82 offen offset:1024
global_load_dwordx4 v[100:103], v182, s[44:45] offset:0
global_load_dwordx4 v[104:107], v182, s[44:45] offset:256
global_load_dwordx4 v[108:111], v182, s[44:45] offset:512
global_load_dwordx4 v[112:115], v182, s[44:45] offset:768
global_load_dwordx4 v[116:119], v182, s[44:45] offset:1024
LCT_%=:
v_and_b32 v211, 15, v127
v_bfe_u32 v222, v127, 1, 3
v_xor_b32 v222, v222, v212
v_lshlrev_b32 v222, 4, v222
v_lshl_add_u32 v222, v211, 7, v222
s_lshl_b32 s77, %[sb], 11
s_and_b32 s77, s77, 2048
v_add_u32 v222, s77, v222
v_xor_b32 v223, 64, v222
v_xor_b32 v217, 2048, v222
v_add_u32 v217, s94, v217
v_xor_b32 v218, 2048, v223
v_add_u32 v218, s94, v218
v_add_u32 v222, s94, v222
v_add_u32 v223, s94, v223
v_and_b32 v214, 1, v212
v_lshlrev_b32 v215, 5, v214
v_xor_b32 v215, v215, v213
v_lshl_add_u32 v235, v210, 8, v215
v_add_u32 v235, 128, v235
v_and_b32 v234, 1, v127
v_mul_u32_u24 v234, 43690, v234
v_add_u32 v234, 17476, v234
v_bfe_u32 v215, v127, 2, 2
v_and_b32 v216, 3, v127
v_mul_u32_u24 v252, 2056, v212
v_lshl_add_u32 v252, v215, 6, v252
v_lshl_add_u32 v252, v216, 4, v252
s_xor_b32 s77, %[swave], 0
s_lshl_b32 s77, s77, 9
s_add_u32 s77, s77, %[slds]
s_add_u32 s77, s77, 65536
v_add_u32 v192, s77, v252
v_add_u32 v196, 49344, v192
s_xor_b32 s77, %[swave], 1
s_lshl_b32 s77, s77, 9
s_add_u32 s77, s77, %[slds]
s_add_u32 s77, s77, 65536
v_add_u32 v193, s77, v252
v_add_u32 v197, 49344, v193
s_xor_b32 s77, %[swave], 2
s_lshl_b32 s77, s77, 9
s_add_u32 s77, s77, %[slds]
s_add_u32 s77, s77, 65536
v_add_u32 v194, s77, v252
v_add_u32 v198, 49344, v194
s_xor_b32 s77, %[swave], 3
s_lshl_b32 s77, s77, 9
s_add_u32 s77, s77, %[slds]
s_add_u32 s77, s77, 65536
v_add_u32 v195, s77, v252
v_add_u32 v199, 49344, v195
v_mov_b32 v236, 0x3f803f80
v_mov_b32 v184, 0
v_mov_b32 v237, 0x3f803f80
v_mov_b32 v185, 0
v_mov_b32 v238, 0x3f803f80
v_mov_b32 v188, 0
v_mov_b32 v239, 0x3f803f80
v_mov_b32 v189, 0
s_getpc_b64 s[98:99]
LTA0_%=:
s_lshl_b32 s73, %[swave], 12
s_add_u32 s73, s73, LPG1_%=-LTA0_%=
s_add_u32 s98, s98, s73
s_addc_u32 s99, s99, 0
s_mov_b64 exec, 0
s_setpc_b64 s[98:99]
LTR0_%=:
s_mov_b64 exec, -1
s_nop 0
s_bitcmp1_b32 %[sb], 7
s_cbranch_scc1 LCB_%=
s_add_u32 s88, s68, s86
s_add_u32 s86, s86, 128
s_cmp_ge_u32 s86, 1280
s_cselect_b32 s86, 0, s86
s_add_u32 m0, s94, 32768
s_add_u32 s95, s88, 0
buffer_load_dwordx4 v220, s[32:35], s95 offen offset:0 lds
s_add_u32 s95, s88, 9216
buffer_load_dwordx4 v221, s[32:35], s95 offen offset:1024 lds
s_add_u32 s95, s88, 18432
buffer_load_dwordx4 v220, s[32:35], s95 offen offset:2048 lds
s_add_u32 s95, s88, 27648
buffer_load_dwordx4 v221, s[32:35], s95 offen offset:3072 lds
s_add_u32 m0, s94, 36864
s_add_u32 s95, s88, 40960
buffer_load_dwordx4 v220, s[32:35], s95 offen offset:0 lds
s_add_u32 s95, s88, 50176
buffer_load_dwordx4 v221, s[32:35], s95 offen offset:1024 lds
s_add_u32 s95, s88, 59392
buffer_load_dwordx4 v220, s[32:35], s95 offen offset:2048 lds
s_add_u32 s95, s88, 68608
buffer_load_dwordx4 v221, s[32:35], s95 offen offset:3072 lds
s_branch LCM_%=
LCB_%=:
s_waitcnt vmcnt(5)
s_mov_b32 s61, 0x3acccccd
s_add_u32 s87, s89, s85
s_add_u32 s85, s85, 524288
s_cmp_ge_u32 s85, 5242880
s_cselect_b32 s85, 0, s85
s_add_u32 m0, s96, 65536
s_nop 0
buffer_load_dwordx4 v253, s[16:19], s87 offen offset:0 lds
s_lshl_b32 s60, s76, 1
s_sub_u32 s60, 65535, s60
v_lshlrev_b32 v0, 16, v128
v_and_b32 v1, 0xffff0000, v128
v_lshlrev_b32 v2, 16, v129
v_and_b32 v3, 0xffff0000, v129
v_lshlrev_b32 v4, 16, v130
v_and_b32 v5, 0xffff0000, v130
s_add_u32 m0, s96, 66560
s_nop 0
buffer_load_dwordx4 v254, s[16:19], s87 offen offset:0 lds
v_lshlrev_b32 v6, 16, v131
v_and_b32 v7, 0xffff0000, v131
v_lshlrev_b32 v8, 16, v132
v_and_b32 v9, 0xffff0000, v132
v_lshlrev_b32 v10, 16, v133
v_and_b32 v11, 0xffff0000, v133
v_lshlrev_b32 v12, 16, v134
v_and_b32 v13, 0xffff0000, v134
s_add_u32 s87, s87, 65536
s_add_u32 m0, s96, 67592
s_nop 0
buffer_load_dwordx4 v253, s[16:19], s87 offen offset:0 lds
v_lshlrev_b32 v14, 16, v135
v_and_b32 v15, 0xffff0000, v135
v_lshlrev_b32 v16, 16, v136
v_and_b32 v17, 0xffff0000, v136
v_lshlrev_b32 v18, 16, v137
v_and_b32 v19, 0xffff0000, v137
v_lshlrev_b32 v20, 16, v138
v_and_b32 v21, 0xffff0000, v138
s_add_u32 m0, s96, 68616
s_nop 0
buffer_load_dwordx4 v254, s[16:19], s87 offen offset:0 lds
v_lshlrev_b32 v22, 16, v139
v_and_b32 v23, 0xffff0000, v139
v_lshlrev_b32 v24, 16, v140
v_and_b32 v25, 0xffff0000, v140
v_lshlrev_b32 v26, 16, v141
v_and_b32 v27, 0xffff0000, v141
v_lshlrev_b32 v28, 16, v142
v_and_b32 v29, 0xffff0000, v142
s_add_u32 s88, s68, s86
s_add_u32 s86, s86, 128
s_cmp_ge_u32 s86, 1280
s_cselect_b32 s86, 0, s86
s_add_u32 m0, s94, 0
s_add_u32 s95, s88, 0
buffer_load_dwordx4 v220, s[32:35], s95 offen offset:0 lds
v_lshlrev_b32 v30, 16, v143
v_and_b32 v31, 0xffff0000, v143
v_lshlrev_b32 v32, 16, v144
v_and_b32 v33, 0xffff0000, v144
v_lshlrev_b32 v34, 16, v145
v_and_b32 v35, 0xffff0000, v145
v_lshlrev_b32 v36, 16, v146
v_and_b32 v37, 0xffff0000, v146
s_add_u32 s95, s88, 9216
buffer_load_dwordx4 v221, s[32:35], s95 offen offset:1024 lds
v_lshlrev_b32 v38, 16, v147
v_and_b32 v39, 0xffff0000, v147
v_pk_add_f32 v[40:41], v[0:1], v[2:3]
v_pk_add_f32 v[42:43], v[4:5], v[6:7]
v_pk_add_f32 v[40:41], v[40:41], v[8:9]
v_pk_add_f32 v[42:43], v[42:43], v[10:11]
v_pk_add_f32 v[40:41], v[40:41], v[12:13]
v_pk_add_f32 v[42:43], v[42:43], v[14:15]
s_add_u32 s95, s88, 18432
buffer_load_dwordx4 v220, s[32:35], s95 offen offset:2048 lds
v_pk_add_f32 v[40:41], v[40:41], v[16:17]
v_pk_add_f32 v[42:43], v[42:43], v[18:19]
v_pk_add_f32 v[40:41], v[40:41], v[20:21]
v_pk_add_f32 v[42:43], v[42:43], v[22:23]
v_pk_add_f32 v[40:41], v[40:41], v[24:25]
v_pk_add_f32 v[42:43], v[42:43], v[26:27]
v_pk_add_f32 v[40:41], v[40:41], v[28:29]
v_pk_add_f32 v[42:43], v[42:43], v[30:31]
s_add_u32 s95, s88, 27648
buffer_load_dwordx4 v221, s[32:35], s95 offen offset:3072 lds
v_pk_add_f32 v[40:41], v[40:41], v[32:33]
v_pk_add_f32 v[42:43], v[42:43], v[34:35]
v_pk_add_f32 v[40:41], v[40:41], v[36:37]
v_pk_add_f32 v[42:43], v[42:43], v[38:39]
v_pk_add_f32 v[40:41], v[40:41], v[42:43]
v_add_f32 v48, v40, v41
s_nop 1
v_add_f32_dpp v48, v48, v48 row_ror:8 row_mask:0xf bank_mask:0xf
s_nop 1
v_add_f32_dpp v48, v48, v48 row_ror:4 row_mask:0xf bank_mask:0xf
s_add_u32 m0, s94, 4096
s_add_u32 s95, s88, 40960
buffer_load_dwordx4 v220, s[32:35], s95 offen offset:0 lds
v_add_f32_dpp v48, v48, v48 row_ror:2 row_mask:0xf bank_mask:0xf
s_nop 1
v_add_f32_dpp v48, v48, v48 row_ror:1 row_mask:0xf bank_mask:0xf
v_mul_f32 v48, 0x3acccccd, v48
CV v44,v48,v48
v_and_b32 v44, 0xffff0000, v44
v_sub_f32 v48, v48, v44
v_pk_add_f32 v[0:1], v[0:1], v[44:45] op_sel_hi:[1,0] neg_lo:[0,1] neg_hi:[0,1]
v_pk_add_f32 v[2:3], v[2:3], v[44:45] op_sel_hi:[1,0] neg_lo:[0,1] neg_hi:[0,1]
s_add_u32 s95, s88, 50176
buffer_load_dwordx4 v221, s[32:35], s95 offen offset:1024 lds
v_pk_add_f32 v[4:5], v[4:5], v[44:45] op_sel_hi:[1,0] neg_lo:[0,1] neg_hi:[0,1]
v_pk_add_f32 v[6:7], v[6:7], v[44:45] op_sel_hi:[1,0] neg_lo:[0,1] neg_hi:[0,1]
v_pk_add_f32 v[8:9], v[8:9], v[44:45] op_sel_hi:[1,0] neg_lo:[0,1] neg_hi:[0,1]
v_pk_add_f32 v[10:11], v[10:11], v[44:45] op_sel_hi:[1,0] neg_lo:[0,1] neg_hi:[0,1]
v_pk_add_f32 v[12:13], v[12:13], v[44:45] op_sel_hi:[1,0] neg_lo:[0,1] neg_hi:[0,1]
)ASM"
R"ASM(
v_pk_add_f32 v[14:15], v[14:15], v[44:45] op_sel_hi:[1,0] neg_lo:[0,1] neg_hi:[0,1]
v_pk_add_f32 v[16:17], v[16:17], v[44:45] op_sel_hi:[1,0] neg_lo:[0,1] neg_hi:[0,1]
v_pk_add_f32 v[18:19], v[18:19], v[44:45] op_sel_hi:[1,0] neg_lo:[0,1] neg_hi:[0,1]
s_add_u32 s95, s88, 59392
buffer_load_dwordx4 v220, s[32:35], s95 offen offset:2048 lds
v_pk_add_f32 v[20:21], v[20:21], v[44:45] op_sel_hi:[1,0] neg_lo:[0,1] neg_hi:[0,1]
v_pk_add_f32 v[22:23], v[22:23], v[44:45] op_sel_hi:[1,0] neg_lo:[0,1] neg_hi:[0,1]
v_pk_add_f32 v[24:25], v[24:25], v[44:45] op_sel_hi:[1,0] neg_lo:[0,1] neg_hi:[0,1]
v_pk_add_f32 v[26:27], v[26:27], v[44:45] op_sel_hi:[1,0] neg_lo:[0,1] neg_hi:[0,1]
v_pk_add_f32 v[28:29], v[28:29], v[44:45] op_sel_hi:[1,0] neg_lo:[0,1] neg_hi:[0,1]
v_pk_add_f32 v[30:31], v[30:31], v[44:45] op_sel_hi:[1,0] neg_lo:[0,1] neg_hi:[0,1]
v_pk_add_f32 v[32:33], v[32:33], v[44:45] op_sel_hi:[1,0] neg_lo:[0,1] neg_hi:[0,1]
v_pk_add_f32 v[34:35], v[34:35], v[44:45] op_sel_hi:[1,0] neg_lo:[0,1] neg_hi:[0,1]
s_add_u32 s95, s88, 68608
buffer_load_dwordx4 v221, s[32:35], s95 offen offset:3072 lds
v_pk_add_f32 v[36:37], v[36:37], v[44:45] op_sel_hi:[1,0] neg_lo:[0,1] neg_hi:[0,1]
v_pk_add_f32 v[38:39], v[38:39], v[44:45] op_sel_hi:[1,0] neg_lo:[0,1] neg_hi:[0,1]
v_pk_mul_f32 v[40:41], v[0:1], v[0:1]
v_pk_mul_f32 v[42:43], v[2:3], v[2:3]
v_pk_fma_f32 v[40:41], v[4:5], v[4:5], v[40:41]
v_pk_fma_f32 v[42:43], v[6:7], v[6:7], v[42:43]
v_pk_fma_f32 v[40:41], v[8:9], v[8:9], v[40:41]
v_pk_fma_f32 v[42:43], v[10:11], v[10:11], v[42:43]
s_add_u32 s87, s89, s85
s_add_u32 s85, s85, 524288
s_cmp_ge_u32 s85, 5242880
s_cselect_b32 s85, 0, s85
s_add_u32 m0, s96, 81984
s_nop 0
buffer_load_dwordx4 v253, s[16:19], s87 offen offset:0 lds
v_pk_fma_f32 v[40:41], v[12:13], v[12:13], v[40:41]
v_pk_fma_f32 v[42:43], v[14:15], v[14:15], v[42:43]
v_pk_fma_f32 v[40:41], v[16:17], v[16:17], v[40:41]
v_pk_fma_f32 v[42:43], v[18:19], v[18:19], v[42:43]
v_pk_fma_f32 v[40:41], v[20:21], v[20:21], v[40:41]
v_pk_fma_f32 v[42:43], v[22:23], v[22:23], v[42:43]
v_pk_fma_f32 v[40:41], v[24:25], v[24:25], v[40:41]
v_pk_fma_f32 v[42:43], v[26:27], v[26:27], v[42:43]
s_add_u32 m0, s96, 83008
s_nop 0
buffer_load_dwordx4 v254, s[16:19], s87 offen offset:0 lds
v_pk_fma_f32 v[40:41], v[28:29], v[28:29], v[40:41]
v_pk_fma_f32 v[42:43], v[30:31], v[30:31], v[42:43]
v_pk_fma_f32 v[40:41], v[32:33], v[32:33], v[40:41]
v_pk_fma_f32 v[42:43], v[34:35], v[34:35], v[42:43]
v_pk_fma_f32 v[40:41], v[36:37], v[36:37], v[40:41]
v_pk_fma_f32 v[42:43], v[38:39], v[38:39], v[42:43]
v_mul_f32 v48, v48, v48
v_pk_add_f32 v[40:41], v[40:41], v[42:43]
s_add_u32 s87, s87, 65536
s_add_u32 m0, s96, 84040
s_nop 0
buffer_load_dwordx4 v253, s[16:19], s87 offen offset:0 lds
v_add_f32 v49, v40, v41
CV v128,v0,v1
CV v129,v2,v3
v_add_f32_dpp v49, v49, v49 row_ror:8 row_mask:0xf bank_mask:0xf
CV v130,v4,v5
CV v131,v6,v7
v_add_f32_dpp v49, v49, v49 row_ror:4 row_mask:0xf bank_mask:0xf
CV v132,v8,v9
s_add_u32 m0, s96, 85064
s_nop 0
buffer_load_dwordx4 v254, s[16:19], s87 offen offset:0 lds
CV v133,v10,v11
v_add_f32_dpp v49, v49, v49 row_ror:2 row_mask:0xf bank_mask:0xf
CV v134,v12,v13
CV v135,v14,v15
v_add_f32_dpp v49, v49, v49 row_ror:1 row_mask:0xf bank_mask:0xf
v_fma_f32 v49, v49, s61, -v48
CV v136,v16,v17
v_max_f32 v49, 0, v49
s_add_u32 s88, s68, s86
s_add_u32 s86, s86, 128
s_cmp_ge_u32 s86, 1280
s_cselect_b32 s86, 0, s86
s_add_u32 m0, s94, 32768
s_add_u32 s95, s88, 0
buffer_load_dwordx4 v220, s[32:35], s95 offen offset:0 lds
CV v137,v18,v19
CV v49,v49,v49
CV v138,v20,v21
v_and_b32 v49, 0xffff0000, v49
CV v139,v22,v23
v_add_f32 v49, 0x3727c5ac, v49
CV v140,v24,v25
CV v49,v49,v49
s_add_u32 s95, s88, 9216
buffer_load_dwordx4 v221, s[32:35], s95 offen offset:1024 lds
CV v141,v26,v27
v_and_b32 v49, 0xffff0000, v49
CV v142,v28,v29
v_sqrt_f32 v49, v49
CV v143,v30,v31
CV v178,v44,v49
CV v144,v32,v33
v_and_b32 v49, 0xffff0000, v178
s_add_u32 s95, s88, 18432
buffer_load_dwordx4 v220, s[32:35], s95 offen offset:2048 lds
CV v145,v34,v35
v_rcp_f32 v46, v49
CV v146,v36,v37
CV v147,v38,v39
v_lshlrev_b32 v0, 16, v128
v_and_b32 v1, 0xffff0000, v128
v_lshlrev_b32 v2, 16, v129
v_and_b32 v3, 0xffff0000, v129
s_add_u32 s95, s88, 27648
buffer_load_dwordx4 v221, s[32:35], s95 offen offset:3072 lds
v_lshlrev_b32 v4, 16, v130
v_and_b32 v5, 0xffff0000, v130
v_lshlrev_b32 v6, 16, v131
v_and_b32 v7, 0xffff0000, v131
v_lshlrev_b32 v8, 16, v132
v_and_b32 v9, 0xffff0000, v132
v_lshlrev_b32 v10, 16, v133
v_and_b32 v11, 0xffff0000, v133
s_add_u32 m0, s94, 36864
s_add_u32 s95, s88, 40960
buffer_load_dwordx4 v220, s[32:35], s95 offen offset:0 lds
v_lshlrev_b32 v12, 16, v134
v_and_b32 v13, 0xffff0000, v134
v_lshlrev_b32 v14, 16, v135
v_and_b32 v15, 0xffff0000, v135
v_lshlrev_b32 v16, 16, v136
v_and_b32 v17, 0xffff0000, v136
v_lshlrev_b32 v18, 16, v137
v_and_b32 v19, 0xffff0000, v137
s_add_u32 s95, s88, 50176
buffer_load_dwordx4 v221, s[32:35], s95 offen offset:1024 lds
v_lshlrev_b32 v20, 16, v138
v_and_b32 v21, 0xffff0000, v138
v_lshlrev_b32 v22, 16, v139
v_and_b32 v23, 0xffff0000, v139
v_lshlrev_b32 v24, 16, v140
v_and_b32 v25, 0xffff0000, v140
v_lshlrev_b32 v26, 16, v141
v_and_b32 v27, 0xffff0000, v141
s_add_u32 s95, s88, 59392
buffer_load_dwordx4 v220, s[32:35], s95 offen offset:2048 lds
v_lshlrev_b32 v28, 16, v142
v_and_b32 v29, 0xffff0000, v142
v_lshlrev_b32 v30, 16, v143
v_and_b32 v31, 0xffff0000, v143
v_lshlrev_b32 v32, 16, v144
v_and_b32 v33, 0xffff0000, v144
v_lshlrev_b32 v34, 16, v145
v_and_b32 v35, 0xffff0000, v145
s_add_u32 s95, s88, 68608
buffer_load_dwordx4 v221, s[32:35], s95 offen offset:3072 lds
v_lshlrev_b32 v36, 16, v146
v_and_b32 v37, 0xffff0000, v146
v_lshlrev_b32 v38, 16, v147
v_and_b32 v39, 0xffff0000, v147
v_pk_mul_f32 v[0:1], v[0:1], v[46:47] op_sel_hi:[1,0]
v_pk_mul_f32 v[2:3], v[2:3], v[46:47] op_sel_hi:[1,0]
v_pk_mul_f32 v[4:5], v[4:5], v[46:47] op_sel_hi:[1,0]
v_pk_mul_f32 v[6:7], v[6:7], v[46:47] op_sel_hi:[1,0]
v_pk_mul_f32 v[8:9], v[8:9], v[46:47] op_sel_hi:[1,0]
v_pk_mul_f32 v[10:11], v[10:11], v[46:47] op_sel_hi:[1,0]
v_pk_mul_f32 v[12:13], v[12:13], v[46:47] op_sel_hi:[1,0]
v_pk_mul_f32 v[14:15], v[14:15], v[46:47] op_sel_hi:[1,0]
v_pk_mul_f32 v[16:17], v[16:17], v[46:47] op_sel_hi:[1,0]
v_pk_mul_f32 v[18:19], v[18:19], v[46:47] op_sel_hi:[1,0]
v_pk_mul_f32 v[20:21], v[20:21], v[46:47] op_sel_hi:[1,0]
v_pk_mul_f32 v[22:23], v[22:23], v[46:47] op_sel_hi:[1,0]
v_pk_mul_f32 v[24:25], v[24:25], v[46:47] op_sel_hi:[1,0]
v_pk_mul_f32 v[26:27], v[26:27], v[46:47] op_sel_hi:[1,0]
v_pk_mul_f32 v[28:29], v[28:29], v[46:47] op_sel_hi:[1,0]
v_pk_mul_f32 v[30:31], v[30:31], v[46:47] op_sel_hi:[1,0]
v_pk_mul_f32 v[32:33], v[32:33], v[46:47] op_sel_hi:[1,0]
v_pk_mul_f32 v[34:35], v[34:35], v[46:47] op_sel_hi:[1,0]
v_pk_mul_f32 v[36:37], v[36:37], v[46:47] op_sel_hi:[1,0]
v_pk_mul_f32 v[38:39], v[38:39], v[46:47] op_sel_hi:[1,0]
CV v128,v0,v1
CV v129,v2,v3
CV v130,v4,v5
CV v131,v6,v7
CV v132,v8,v9
CV v133,v10,v11
CV v134,v12,v13
CV v135,v14,v15
CV v136,v16,v17
CV v137,v18,v19
CV v138,v20,v21
CV v139,v22,v23
CV v140,v24,v25
CV v141,v26,v27
CV v142,v28,v29
CV v143,v30,v31
CV v144,v32,v33
CV v145,v34,v35
CV v146,v36,v37
CV v147,v38,v39
s_waitcnt vmcnt(24)
v_dot2_f32_bf16 v40, v128, v100, 0
v_dot2_f32_bf16 v41, v129, v101, 0
v_dot2_f32_bf16 v42, v130, v102, 0
v_dot2_f32_bf16 v43, v131, v103, 0
v_dot2_f32_bf16 v40, v132, v104, v40
v_dot2_f32_bf16 v41, v133, v105, v41
v_dot2_f32_bf16 v42, v134, v106, v42
v_dot2_f32_bf16 v43, v135, v107, v43
v_dot2_f32_bf16 v40, v136, v108, v40
v_dot2_f32_bf16 v41, v137, v109, v41
v_dot2_f32_bf16 v42, v138, v110, v42
v_dot2_f32_bf16 v43, v139, v111, v43
v_dot2_f32_bf16 v40, v140, v112, v40
v_dot2_f32_bf16 v41, v141, v113, v41
v_dot2_f32_bf16 v42, v142, v114, v42
v_dot2_f32_bf16 v43, v143, v115, v43
v_dot2_f32_bf16 v40, v144, v116, v40
v_dot2_f32_bf16 v41, v145, v117, v41
v_dot2_f32_bf16 v42, v146, v118, v42
v_dot2_f32_bf16 v43, v147, v119, v43
s_nop 2
v_pk_add_f32 v[40:41], v[40:41], v[42:43]
v_add_f32 v160, v40, v41
s_nop 1
v_add_f32_dpp v160, v160, v160 row_ror:8 row_mask:0xf bank_mask:0xf
s_nop 1
v_add_f32_dpp v160, v160, v160 row_ror:4 row_mask:0xf bank_mask:0xf
s_nop 1
v_add_f32_dpp v160, v160, v160 row_ror:2 row_mask:0xf bank_mask:0xf
s_nop 1
v_add_f32_dpp v160, v160, v160 row_ror:1 row_mask:0xf bank_mask:0xf
CV v160,v160,v160
v_ashrrev_i32 v50, 31, v160
v_or_b32 v50, 0x80000000, v50
v_xor_b32 v160, v160, v50
v_and_b32 v160, 0xffff0000, v160
v_sub_u32 v50, s60, v181
v_or_b32 v160, v160, v50
LCM_%=:
s_waitcnt vmcnt(12)
LPG1_%=:
s_cbranch_execz LPG5_%=
RD a[128:131],v222 offset:0
RD a[132:135],v223 offset:0
RD a[136:139],v217 offset:0
RD a[140:143],v218 offset:0
RD a[144:147],v222 offset:4096
RD a[148:151],v223 offset:4096
RD a[152:155],v217 offset:4096
RD a[156:159],v218 offset:4096
s_barrier
TR a[0:1],v192 offset:0
TR a[2:3],v192 offset:256
TR a[4:5],v192 offset:8224
TR a[6:7],v192 offset:8480
TR a[8:9],v192 offset:8
TR a[10:11],v192 offset:264
TR a[12:13],v192 offset:8232
TR a[14:15],v192 offset:8488
s_waitcnt lgkmcnt(8)
s_waitcnt lgkmcnt(4)
MM v[0:3],a[0:3],a[128:131],0
s_mov_b32 s66, 32768
TR a[16:17],v193 offset:0
TR a[18:19],v193 offset:256
MM v[0:3],a[4:7],a[132:135],v[0:3]
s_lshl_b32 s90, s76, 1
TR a[20:21],v193 offset:8224
TR a[22:23],v193 offset:8480
MM v[8:11],a[4:7],a[140:143],0
s_and_b32 s77, s90, 15
TR a[24:25],v193 offset:8
TR a[26:27],v193 offset:264
buffer_load_dword v169, off, s[20:23], s66 sc1
MM v[8:11],a[0:3],a[136:139],v[8:11]
s_add_u32 s82, s82, 131072
TR a[28:29],v193 offset:8232
TR a[30:31],v193 offset:8488
MM v[16:19],a[0:3],a[144:147],0
s_add_u32 s87, s89, s85
TR a[32:33],v194 offset:0
TR a[34:35],v194 offset:256
MM v[16:19],a[4:7],a[148:151],v[16:19]
s_add_u32 s85, s85, 524288
TR a[36:37],v194 offset:8224
TR a[38:39],v194 offset:8480
MM v[24:27],a[4:7],a[156:159],0
s_lshl_b32 s77, s77, 1
TR a[40:41],v194 offset:8
TR a[42:43],v194 offset:264
MM v[24:27],a[0:3],a[152:155],v[24:27]
s_waitcnt lgkmcnt(14)
TR a[44:45],v194 offset:8232
TR a[46:47],v194 offset:8488
s_cmp_ge_u32 s85, 5242880
s_cselect_b32 s85, 0, s85
MM v[4:7],a[8:11],a[128:131],0
s_add_u32 m0, s96, 98432
TR a[48:49],v195 offset:0
TR a[50:51],v195 offset:256
MM v[4:7],a[12:15],a[132:135],v[4:7]
s_bfe_u32 s91, s90, 65540
TR a[52:53],v195 offset:8224
TR a[54:55],v195 offset:8480
MM v[12:15],a[12:15],a[140:143],0
s_add_u32 s77, s77, s91
TR a[56:57],v195 offset:8
TR a[58:59],v195 offset:264
buffer_load_dwordx4 v253, s[16:19], s87 offen offset:0 lds
MM v[12:15],a[8:11],a[136:139],v[12:15]
s_add_u32 m0, s96, 99456
TR a[60:61],v195 offset:8232
TR a[62:63],v195 offset:8488
MM v[20:23],a[8:11],a[144:147],0
s_and_b32 s91, s90, 0xffffffe0
buffer_load_dwordx4 v254, s[16:19], s87 offen offset:0 lds
MM v[20:23],a[12:15],a[148:151],v[20:23]
s_add_u32 s87, s87, 65536
s_add_u32 m0, s96, 100488
s_add_u32 s77, s77, s91
MM v[28:31],a[12:15],a[156:159],0
s_lshl_b32 s77, s77, 3
s_add_u32 s97, s77, 36864
s_sub_u32 s79, 65535, s90
MM v[28:31],a[8:11],a[152:155],v[28:31]
s_lshl_b32 s78, s90, 2
s_add_u32 s78, s78, 53248
s_lshl_b32 s80, s76, 2
v_smfmac_f32_16x16x64_bf16 v[184:187], v[236:239], a[0:7], v234
s_add_u32 s80, s80, 81920
v_lshlrev_b32 v232, 11, v180
v_smfmac_f32_16x16x64_bf16 v[188:191], v[236:239], a[8:15], v234
s_waitcnt lgkmcnt(15)
v_lshl_add_u32 v232, v214, 8, v232
MM v[32:35],a[16:19],a[128:131],0
buffer_load_dwordx4 v253, s[16:19], s87 offen offset:0 lds
MM v[32:35],a[20:23],a[132:135],v[32:35]
s_add_u32 m0, s96, 101512
v_lshl_add_u32 v232, v215, 6, v232
v_lshl_add_u32 v232, v216, 3, v232
MM v[40:43],a[20:23],a[140:143],0
s_waitcnt vmcnt(12)
s_barrier
v_lshl_add_u32 v232, v214, 5, v232
MM v[40:43],a[16:19],a[136:139],v[40:43]
TR a[0:1],v192 offset:16448
TR a[2:3],v192 offset:16704
MM v[48:51],a[16:19],a[144:147],0
TR a[4:5],v192 offset:24672
TR a[6:7],v192 offset:24928
buffer_load_dwordx4 v254, s[16:19], s87 offen offset:0 lds
MM v[48:51],a[20:23],a[148:151],v[48:51]
TR a[8:9],v192 offset:16456
TR a[10:11],v192 offset:16712
MM v[56:59],a[20:23],a[156:159],0
s_add_u32 s88, s68, s86
)ASM"
R"ASM(
TR a[12:13],v192 offset:24680
TR a[14:15],v192 offset:24936
MM v[56:59],a[16:19],a[152:155],v[56:59]
s_add_u32 s86, s86, 128
s_cmp_ge_u32 s86, 1280
s_cselect_b32 s86, 0, s86
MM v[36:39],a[24:27],a[128:131],0
s_add_u32 m0, s94, 0
TR a[16:17],v193 offset:16448
TR a[18:19],v193 offset:16704
MM v[36:39],a[28:31],a[132:135],v[36:39]
s_add_u32 s95, s88, 0
TR a[20:21],v193 offset:24672
TR a[22:23],v193 offset:24928
MM v[44:47],a[28:31],a[140:143],0
buffer_load_dwordx4 v220, s[32:35], s95 offen offset:0 lds
MM v[44:47],a[24:27],a[136:139],v[44:47]
s_add_u32 s95, s88, 9216
v_xor_b32 v233, 32, v232
s_add_u32 s77, %[slds], 65536
MM v[52:55],a[24:27],a[144:147],0
buffer_load_dwordx4 v221, s[32:35], s95 offen offset:1024 lds
MM v[52:55],a[28:31],a[148:151],v[52:55]
s_add_u32 s95, s88, 18432
v_add_u32 v232, s77, v232
v_add_u32 v233, s77, v233
MM v[60:63],a[28:31],a[156:159],0
v_lshlrev_b32 v214, 8, v211
v_add_u32 v214, %[slds], v214
MM v[60:63],a[24:27],a[152:155],v[60:63]
s_waitcnt lgkmcnt(15)
v_xor_b32 v215, v211, v212
s_lshl_b32 s77, %[swave], 2
MM v[64:67],a[32:35],a[128:131],0
TR a[24:25],v193 offset:16456
TR a[26:27],v193 offset:16712
MM v[64:67],a[36:39],a[132:135],v[64:67]
TR a[28:29],v193 offset:24680
TR a[30:31],v193 offset:24936
MM v[72:75],a[36:39],a[140:143],0
buffer_load_dwordx4 v220, s[32:35], s95 offen offset:2048 lds
MM v[72:75],a[32:35],a[136:139],v[72:75]
s_add_u32 s95, s88, 27648
v_xor_b32 v213, s77, v215
v_xor_b32 v216, 0, v213
MM v[80:83],a[32:35],a[144:147],0
buffer_load_dwordx4 v221, s[32:35], s95 offen offset:3072 lds
MM v[80:83],a[36:39],a[148:151],v[80:83]
s_add_u32 m0, s94, 4096
s_add_u32 s95, s88, 40960
v_lshl_add_u32 v224, v216, 4, v214
MM v[88:91],a[36:39],a[156:159],0
v_xor_b32 v216, 4, v213
v_lshl_add_u32 v225, v216, 4, v214
MM v[88:91],a[32:35],a[152:155],v[88:91]
v_xor_b32 v216, 8, v213
v_lshl_add_u32 v226, v216, 4, v214
MM v[68:71],a[40:43],a[128:131],0
TR a[32:33],v194 offset:16448
TR a[34:35],v194 offset:16704
MM v[68:71],a[44:47],a[132:135],v[68:71]
s_waitcnt vmcnt(8)
TR a[36:37],v194 offset:24672
TR a[38:39],v194 offset:24928
MM v[76:79],a[44:47],a[140:143],0
buffer_load_dwordx4 v220, s[32:35], s95 offen offset:0 lds
MM v[76:79],a[40:43],a[136:139],v[76:79]
s_add_u32 s95, s88, 50176
v_readfirstlane_b32 s69, v169
v_lshlrev_b32 v173, 4, v181
MM v[84:87],a[40:43],a[144:147],0
s_add_u32 s69, s69, 1
buffer_load_dwordx4 v221, s[32:35], s95 offen offset:1024 lds
MM v[84:87],a[44:47],a[148:151],v[84:87]
s_add_u32 s95, s88, 59392
v_mov_b32 v179, s69
v_xor_b32 v216, 12, v213
MM v[92:95],a[44:47],a[156:159],0
s_mov_b64 exec, s[54:55]
buffer_store_dwordx2 v[178:179], v173, s[20:23], s97 offen offset:0 sc1
s_mov_b64 exec, -1
MM v[92:95],a[40:43],a[152:155],v[92:95]
v_lshl_add_u32 v227, v216, 4, v214
v_lshlrev_b32 v214, 8, v212
MM v[96:99],a[48:51],a[128:131],0
TR a[40:41],v194 offset:16456
TR a[42:43],v194 offset:16712
MM v[96:99],a[52:55],a[132:135],v[96:99]
TR a[44:45],v194 offset:24680
TR a[46:47],v194 offset:24936
MM v[104:107],a[52:55],a[140:143],0
buffer_load_dwordx4 v220, s[32:35], s95 offen offset:2048 lds
MM v[104:107],a[48:51],a[136:139],v[104:107]
s_add_u32 s95, s88, 68608
v_xor_b32 v216, 0, v215
v_lshl_add_u32 v228, v216, 4, v214
MM v[112:115],a[48:51],a[144:147],0
buffer_load_dwordx4 v221, s[32:35], s95 offen offset:3072 lds
MM v[112:115],a[52:55],a[148:151],v[112:115]
s_add_u32 s87, s89, s85
s_add_u32 s85, s85, 524288
v_xor_b32 v216, 4, v215
MM v[120:123],a[52:55],a[156:159],0
s_cmp_ge_u32 s85, 5242880
s_cselect_b32 s85, 0, s85
s_add_u32 m0, s96, 114880
MM v[120:123],a[48:51],a[152:155],v[120:123]
s_waitcnt lgkmcnt(15)
v_lshl_add_u32 v229, v216, 4, v214
v_xor_b32 v216, 8, v215
MM v[100:103],a[56:59],a[128:131],0
TR a[48:49],v195 offset:16448
TR a[50:51],v195 offset:16704
s_mov_b64 exec, s[52:53]
buffer_store_dwordx4 v[128:131], v168, s[20:23], s82 offen offset:0 sc1
s_mov_b64 exec, -1
MM v[100:103],a[60:63],a[132:135],v[100:103]
TR a[52:53],v195 offset:24672
TR a[54:55],v195 offset:24928
MM v[108:111],a[60:63],a[140:143],0
v_lshl_add_u32 v230, v216, 4, v214
v_xor_b32 v216, 12, v215
MM v[108:111],a[56:59],a[136:139],v[108:111]
v_lshl_add_u32 v231, v216, 4, v214
s_lshl_b32 s90, s49, 19
MM v[116:119],a[56:59],a[144:147],0
buffer_load_dwordx4 v253, s[16:19], s87 offen offset:0 lds
MM v[116:119],a[60:63],a[148:151],v[116:119]
s_add_u32 m0, s96, 115904
s_add_u32 s8, s8, s90
s_addc_u32 s9, s9, 0
MM v[124:127],a[60:63],a[156:159],0
s_mov_b32 s10, 524288
s_mov_b32 s11, 131072
MM v[124:127],a[56:59],a[152:155],v[124:127]
s_add_u32 s12, s12, s90
s_addc_u32 s13, s13, 0
s_mov_b32 s14, 525312
buffer_load_dwordx4 v254, s[16:19], s87 offen offset:0 lds
s_add_u32 s87, s87, 65536
s_add_u32 m0, s96, 116936
s_nop 0
buffer_load_dwordx4 v253, s[16:19], s87 offen offset:0 lds
s_add_u32 m0, s96, 117960
s_nop 0
buffer_load_dwordx4 v254, s[16:19], s87 offen offset:0 lds
RD a[160:163],v222 offset:32768
RD a[164:167],v223 offset:32768
RD a[168:171],v217 offset:32768
RD a[172:175],v218 offset:32768
RD a[176:179],v222 offset:36864
RD a[180:183],v223 offset:36864
RD a[184:187],v217 offset:36864
RD a[188:191],v218 offset:36864
s_waitcnt vmcnt(14) lgkmcnt(0)
s_barrier
MM v[0:3],a[0:3],a[160:163],v[0:3]
s_mov_b32 s15, 131072
TR a[56:57],v195 offset:16456
TR a[58:59],v195 offset:16712
MM v[0:3],a[4:7],a[164:167],v[0:3]
TR a[60:61],v195 offset:24680
TR a[62:63],v195 offset:24936
MM v[8:11],a[4:7],a[172:175],v[8:11]
s_mov_b64 exec, s[52:53]
buffer_store_dwordx4 v[132:135], v168, s[20:23], s82 offen offset:256 sc1
s_mov_b64 exec, -1
MM v[8:11],a[0:3],a[168:171],v[8:11]
s_add_u32 s88, s68, s86
s_add_u32 s86, s86, 128
MM v[16:19],a[0:3],a[176:179],v[16:19]
s_cmp_ge_u32 s86, 1280
s_cselect_b32 s86, 0, s86
s_add_u32 m0, s94, 32768
MM v[16:19],a[4:7],a[180:183],v[16:19]
s_add_u32 s95, s88, 0
buffer_load_dwordx4 v220, s[32:35], s95 offen offset:0 lds
MM v[24:27],a[4:7],a[188:191],v[24:27]
s_add_u32 s95, s88, 9216
s_sub_u32 s12, s12, 1024
s_subb_u32 s13, s13, 0
MM v[24:27],a[0:3],a[184:187],v[24:27]
s_lshr_b32 s90, s49, 2
s_mul_i32 s90, s90, 655360
s_mul_i32 s77, s84, 320
MM v[4:7],a[8:11],a[160:163],v[4:7]
s_add_u32 s90, s90, s77
buffer_load_dwordx4 v221, s[32:35], s95 offen offset:1024 lds
MM v[4:7],a[12:15],a[164:167],v[4:7]
s_add_u32 s95, s88, 18432
s_add_u32 s24, s24, s90
s_addc_u32 s25, s25, 0
MM v[12:15],a[12:15],a[172:175],v[12:15]
s_sub_u32 s26, 5242880, s90
s_mov_b32 s27, 131072
s_sub_u32 s77, %[sb], 76
MM v[12:15],a[8:11],a[168:171],v[12:15]
s_min_u32 s47, s77, %[sb]
s_mul_i32 s77, s47, 205
s_lshr_b32 s46, s77, 11
MM v[20:23],a[8:11],a[176:179],v[20:23]
s_mul_i32 s90, s46, 121600
buffer_load_dwordx4 v220, s[32:35], s95 offen offset:2048 lds
MM v[20:23],a[12:15],a[180:183],v[20:23]
s_add_u32 s95, s88, 27648
s_lshl_b32 s77, s47, 7
s_add_u32 s90, s90, s77
MM v[28:31],a[12:15],a[188:191],v[28:31]
s_add_u32 s28, s28, s90
s_addc_u32 s29, s29, 0
s_sub_u32 s30, 2211840, s90
MM v[28:31],a[8:11],a[184:187],v[28:31]
s_mov_b32 s31, 131072
s_mov_b32 s38, 2211840
s_mov_b32 s39, 131072
v_smfmac_f32_16x16x64_bf16 v[184:187], v[236:239], a[0:7], v234
s_mov_b32 s42, 2621440
s_mov_b32 s43, 131072
v_smfmac_f32_16x16x64_bf16 v[188:191], v[236:239], a[8:15], v234
s_lshr_b32 s77, %[sb], 6
s_lshl_b32 s77, s77, 3
MM v[32:35],a[16:19],a[160:163],v[32:35]
s_and_b32 s90, %[sb], 7
TR a[0:1],v192 offset:32896
TR a[2:3],v192 offset:33152
buffer_load_dwordx4 v221, s[32:35], s95 offen offset:3072 lds
MM v[32:35],a[20:23],a[164:167],v[32:35]
s_add_u32 m0, s94, 36864
TR a[4:5],v192 offset:41120
TR a[6:7],v192 offset:41376
MM v[40:43],a[20:23],a[172:175],v[40:43]
s_add_u32 s95, s88, 40960
TR a[8:9],v192 offset:32904
TR a[10:11],v192 offset:33160
s_mov_b64 exec, s[52:53]
buffer_store_dwordx4 v[136:139], v168, s[20:23], s82 offen offset:512 sc1
s_mov_b64 exec, -1
MM v[40:43],a[16:19],a[168:171],v[40:43]
s_add_u32 s77, s77, s90
TR a[12:13],v192 offset:41128
TR a[14:15],v192 offset:41384
MM v[48:51],a[16:19],a[176:179],v[48:51]
s_sub_u32 s90, s77, 8
s_cmp_ge_u32 s77, 27
s_cselect_b32 s77, s90, s77
MM v[48:51],a[20:23],a[180:183],v[48:51]
s_lshl_b32 s77, s77, 7
s_lshl_b32 s90, %[sb], 1
s_and_b32 s90, s90, 112
MM v[56:59],a[20:23],a[188:191],v[56:59]
s_add_u32 s48, s77, s90
buffer_load_dwordx4 v220, s[32:35], s95 offen offset:0 lds
MM v[56:59],a[16:19],a[184:187],v[56:59]
s_add_u32 s95, s88, 50176
s_mov_b32 s71, 0x4e800000
s_mov_b32 s72, 0x3e0293ee
MM v[36:39],a[24:27],a[160:163],v[36:39]
s_lshl_b32 s64, %[swave], 12
TR a[16:17],v193 offset:32896
TR a[18:19],v193 offset:33152
MM v[36:39],a[28:31],a[164:167],v[36:39]
TR a[20:21],v193 offset:41120
TR a[22:23],v193 offset:41376
MM v[44:47],a[28:31],a[172:175],v[44:47]
buffer_load_dwordx4 v221, s[32:35], s95 offen offset:1024 lds
MM v[44:47],a[24:27],a[168:171],v[44:47]
s_add_u32 s95, s88, 59392
MM v[52:55],a[24:27],a[176:179],v[52:55]
MM v[52:55],a[28:31],a[180:183],v[52:55]
MM v[60:63],a[28:31],a[188:191],v[60:63]
buffer_load_dwordx4 v220, s[32:35], s95 offen offset:2048 lds
MM v[60:63],a[24:27],a[184:187],v[60:63]
s_add_u32 s95, s88, 68608
MM v[64:67],a[32:35],a[160:163],v[64:67]
TR a[24:25],v193 offset:32904
TR a[26:27],v193 offset:33160
MM v[64:67],a[36:39],a[164:167],v[64:67]
TR a[28:29],v193 offset:41128
TR a[30:31],v193 offset:41384
MM v[72:75],a[36:39],a[172:175],v[72:75]
buffer_load_dwordx4 v221, s[32:35], s95 offen offset:3072 lds
MM v[72:75],a[32:35],a[168:171],v[72:75]
s_add_u32 s87, s89, s85
s_add_u32 s85, s85, 524288
MM v[80:83],a[32:35],a[176:179],v[80:83]
s_cmp_ge_u32 s85, 5242880
s_cselect_b32 s85, 0, s85
s_add_u32 m0, s96, 131328
MM v[80:83],a[36:39],a[180:183],v[80:83]
MM v[88:91],a[36:39],a[188:191],v[88:91]
buffer_load_dwordx4 v253, s[16:19], s87 offen offset:0 lds
MM v[88:91],a[32:35],a[184:187],v[88:91]
s_add_u32 m0, s96, 132352
MM v[68:71],a[40:43],a[160:163],v[68:71]
TR a[32:33],v194 offset:32896
TR a[34:35],v194 offset:33152
MM v[68:71],a[44:47],a[164:167],v[68:71]
TR a[36:37],v194 offset:41120
TR a[38:39],v194 offset:41376
MM v[76:79],a[44:47],a[172:175],v[76:79]
buffer_load_dwordx4 v254, s[16:19], s87 offen offset:0 lds
MM v[76:79],a[40:43],a[168:171],v[76:79]
s_add_u32 s87, s87, 65536
s_add_u32 m0, s96, 133384
MM v[84:87],a[40:43],a[176:179],v[84:87]
MM v[84:87],a[44:47],a[180:183],v[84:87]
MM v[92:95],a[44:47],a[188:191],v[92:95]
buffer_load_dwordx4 v253, s[16:19], s87 offen offset:0 lds
MM v[92:95],a[40:43],a[184:187],v[92:95]
s_add_u32 m0, s96, 134408
MM v[96:99],a[48:51],a[160:163],v[96:99]
TR a[40:41],v194 offset:32904
TR a[42:43],v194 offset:33160
MM v[96:99],a[52:55],a[164:167],v[96:99]
TR a[44:45],v194 offset:41128
TR a[46:47],v194 offset:41384
MM v[104:107],a[52:55],a[172:175],v[104:107]
buffer_load_dwordx4 v254, s[16:19], s87 offen offset:0 lds
MM v[104:107],a[48:51],a[168:171],v[104:107]
s_waitcnt vmcnt(19)
RD a[192:195],v222 offset:0
MM v[112:115],a[48:51],a[176:179],v[112:115]
RD a[196:199],v223 offset:0
MM v[112:115],a[52:55],a[180:183],v[112:115]
RD a[200:203],v217 offset:0
MM v[120:123],a[52:55],a[188:191],v[120:123]
s_mov_b64 exec, s[52:53]
buffer_store_dwordx4 v[140:143], v168, s[20:23], s82 offen offset:768 sc1
s_mov_b64 exec, -1
MM v[120:123],a[48:51],a[184:187],v[120:123]
s_waitcnt lgkmcnt(15)
RD a[204:207],v218 offset:0
MM v[100:103],a[56:59],a[160:163],v[100:103]
TR a[48:49],v195 offset:32896
TR a[50:51],v195 offset:33152
MM v[100:103],a[60:63],a[164:167],v[100:103]
TR a[52:53],v195 offset:41120
TR a[54:55],v195 offset:41376
MM v[108:111],a[60:63],a[172:175],v[108:111]
RD a[208:211],v222 offset:4096
MM v[108:111],a[56:59],a[168:171],v[108:111]
RD a[212:215],v223 offset:4096
MM v[116:119],a[56:59],a[176:179],v[116:119]
s_mov_b64 exec, s[52:53]
buffer_store_dwordx4 v[144:147], v168, s[20:23], s82 offen offset:1024 sc1
s_mov_b64 exec, -1
MM v[116:119],a[60:63],a[180:183],v[116:119]
RD a[216:219],v217 offset:4096
MM v[124:127],a[60:63],a[188:191],v[124:127]
RD a[220:223],v218 offset:4096
MM v[124:127],a[56:59],a[184:187],v[124:127]
s_waitcnt vmcnt(16) lgkmcnt(0)
s_barrier
MM v[0:3],a[0:3],a[192:195],v[0:3]
TR a[56:57],v195 offset:32904
)ASM"
R"ASM(
TR a[58:59],v195 offset:33160
MM v[0:3],a[4:7],a[196:199],v[0:3]
TR a[60:61],v195 offset:41128
TR a[62:63],v195 offset:41384
MM v[8:11],a[4:7],a[204:207],v[8:11]
s_add_u32 s88, s68, s86
s_add_u32 s86, s86, 128
MM v[8:11],a[0:3],a[200:203],v[8:11]
s_cmp_ge_u32 s86, 1280
s_cselect_b32 s86, 0, s86
s_add_u32 m0, s94, 0
MM v[16:19],a[0:3],a[208:211],v[16:19]
s_add_u32 s95, s88, 0
buffer_load_dwordx4 v220, s[32:35], s95 offen offset:0 lds
v_lshlrev_b32 v173, 2, v181
MM v[16:19],a[4:7],a[212:215],v[16:19]
s_add_u32 s95, s88, 9216
s_mov_b64 exec, s[54:55]
buffer_store_dword v160, v173, s[20:23], s78 offen offset:0 sc1
s_mov_b64 exec, -1
MM v[24:27],a[4:7],a[220:223],v[24:27]
buffer_load_dwordx4 v221, s[32:35], s95 offen offset:1024 lds
MM v[24:27],a[0:3],a[216:219],v[24:27]
s_add_u32 s95, s88, 18432
v_mov_b32 v170, s69
v_mov_b32 v171, s69
MM v[4:7],a[8:11],a[192:195],v[4:7]
MM v[4:7],a[12:15],a[196:199],v[4:7]
MM v[12:15],a[12:15],a[204:207],v[12:15]
buffer_load_dwordx4 v220, s[32:35], s95 offen offset:2048 lds
MM v[12:15],a[8:11],a[200:203],v[12:15]
s_add_u32 s95, s88, 27648
MM v[20:23],a[8:11],a[208:211],v[20:23]
MM v[20:23],a[12:15],a[212:215],v[20:23]
MM v[28:31],a[12:15],a[220:223],v[28:31]
buffer_load_dwordx4 v221, s[32:35], s95 offen offset:3072 lds
MM v[28:31],a[8:11],a[216:219],v[28:31]
s_add_u32 m0, s94, 4096
s_add_u32 s95, s88, 40960
v_smfmac_f32_16x16x64_bf16 v[184:187], v[236:239], a[0:7], v234
v_smfmac_f32_16x16x64_bf16 v[188:191], v[236:239], a[8:15], v234
MM v[32:35],a[16:19],a[192:195],v[32:35]
TR a[0:1],v196 offset:0
TR a[2:3],v196 offset:256
MM v[32:35],a[20:23],a[196:199],v[32:35]
TR a[4:5],v196 offset:8224
TR a[6:7],v196 offset:8480
MM v[40:43],a[20:23],a[204:207],v[40:43]
TR a[8:9],v196 offset:8
TR a[10:11],v196 offset:264
buffer_load_dwordx4 v220, s[32:35], s95 offen offset:0 lds
MM v[40:43],a[16:19],a[200:203],v[40:43]
s_add_u32 s95, s88, 50176
TR a[12:13],v196 offset:8232
TR a[14:15],v196 offset:8488
MM v[48:51],a[16:19],a[208:211],v[48:51]
buffer_load_dwordx4 v221, s[32:35], s95 offen offset:1024 lds
MM v[48:51],a[20:23],a[212:215],v[48:51]
s_add_u32 s95, s88, 59392
MM v[56:59],a[20:23],a[220:223],v[56:59]
MM v[56:59],a[16:19],a[216:219],v[56:59]
MM v[36:39],a[24:27],a[192:195],v[36:39]
TR a[16:17],v197 offset:0
TR a[18:19],v197 offset:256
MM v[36:39],a[28:31],a[196:199],v[36:39]
TR a[20:21],v197 offset:8224
TR a[22:23],v197 offset:8480
MM v[44:47],a[28:31],a[204:207],v[44:47]
buffer_load_dwordx4 v220, s[32:35], s95 offen offset:2048 lds
MM v[44:47],a[24:27],a[200:203],v[44:47]
s_add_u32 s95, s88, 68608
MM v[52:55],a[24:27],a[208:211],v[52:55]
buffer_load_dwordx4 v221, s[32:35], s95 offen offset:3072 lds
MM v[52:55],a[28:31],a[212:215],v[52:55]
s_add_u32 s87, s89, s85
s_add_u32 s85, s85, 524288
MM v[60:63],a[28:31],a[220:223],v[60:63]
s_cmp_ge_u32 s85, 5242880
s_cselect_b32 s85, 0, s85
s_add_u32 m0, s96, 65536
MM v[60:63],a[24:27],a[216:219],v[60:63]
MM v[64:67],a[32:35],a[192:195],v[64:67]
TR a[24:25],v197 offset:8
TR a[26:27],v197 offset:264
MM v[64:67],a[36:39],a[196:199],v[64:67]
TR a[28:29],v197 offset:8232
TR a[30:31],v197 offset:8488
MM v[72:75],a[36:39],a[204:207],v[72:75]
buffer_load_dwordx4 v253, s[16:19], s87 offen offset:0 lds
MM v[72:75],a[32:35],a[200:203],v[72:75]
s_add_u32 m0, s96, 66560
MM v[80:83],a[32:35],a[208:211],v[80:83]
buffer_load_dwordx4 v254, s[16:19], s87 offen offset:0 lds
MM v[80:83],a[36:39],a[212:215],v[80:83]
s_add_u32 s87, s87, 65536
s_add_u32 m0, s96, 67592
MM v[88:91],a[36:39],a[220:223],v[88:91]
MM v[88:91],a[32:35],a[216:219],v[88:91]
LPG2_%=:
s_cbranch_execz LPG6_%=
MM v[68:71],a[40:43],a[192:195],v[68:71]
TR a[32:33],v198 offset:0
TR a[34:35],v198 offset:256
MM v[68:71],a[44:47],a[196:199],v[68:71]
TR a[36:37],v198 offset:8224
TR a[38:39],v198 offset:8480
MM v[76:79],a[44:47],a[204:207],v[76:79]
buffer_load_dwordx4 v253, s[16:19], s87 offen offset:0 lds
MM v[76:79],a[40:43],a[200:203],v[76:79]
s_add_u32 m0, s96, 68616
MM v[84:87],a[40:43],a[208:211],v[84:87]
buffer_load_dwordx4 v254, s[16:19], s87 offen offset:0 lds
MM v[84:87],a[44:47],a[212:215],v[84:87]
s_waitcnt vmcnt(19)
RD a[224:227],v222 offset:32768
MM v[92:95],a[44:47],a[220:223],v[92:95]
RD a[228:231],v223 offset:32768
MM v[92:95],a[40:43],a[216:219],v[92:95]
RD a[232:235],v217 offset:32768
MM v[96:99],a[48:51],a[192:195],v[96:99]
TR a[40:41],v198 offset:8
TR a[42:43],v198 offset:264
MM v[96:99],a[52:55],a[196:199],v[96:99]
TR a[44:45],v198 offset:8232
TR a[46:47],v198 offset:8488
MM v[104:107],a[52:55],a[204:207],v[104:107]
RD a[236:239],v218 offset:32768
MM v[104:107],a[48:51],a[200:203],v[104:107]
RD a[240:243],v222 offset:36864
MM v[112:115],a[48:51],a[208:211],v[112:115]
RD a[244:247],v223 offset:36864
MM v[112:115],a[52:55],a[212:215],v[112:115]
RD a[248:251],v217 offset:36864
MM v[120:123],a[52:55],a[220:223],v[120:123]
RD a[252:255],v218 offset:36864
MM v[120:123],a[48:51],a[216:219],v[120:123]
s_waitcnt lgkmcnt(15)
MM v[100:103],a[56:59],a[192:195],v[100:103]
TR a[48:49],v199 offset:0
TR a[50:51],v199 offset:256
MM v[100:103],a[60:63],a[196:199],v[100:103]
TR a[52:53],v199 offset:8224
TR a[54:55],v199 offset:8480
MM v[108:111],a[60:63],a[204:207],v[108:111]
s_waitcnt lgkmcnt(4)
s_add_u32 s88, s68, s86
s_add_u32 s86, s86, 128
MM v[108:111],a[56:59],a[200:203],v[108:111]
s_cmp_ge_u32 s86, 1280
s_cselect_b32 s86, 0, s86
s_add_u32 m0, s94, 32768
MM v[116:119],a[56:59],a[208:211],v[116:119]
s_add_u32 s95, s88, 0
buffer_load_dwordx4 v220, s[32:35], s95 offen offset:0 lds
MM v[116:119],a[60:63],a[212:215],v[116:119]
s_add_u32 s95, s88, 9216
MM v[124:127],a[60:63],a[220:223],v[124:127]
buffer_load_dwordx4 v221, s[32:35], s95 offen offset:1024 lds
MM v[124:127],a[56:59],a[216:219],v[124:127]
s_add_u32 s95, s88, 18432
s_waitcnt vmcnt(17)
s_barrier
MM v[0:3],a[0:3],a[224:227],v[0:3]
TR a[56:57],v199 offset:8
TR a[58:59],v199 offset:264
MM v[0:3],a[4:7],a[228:231],v[0:3]
TR a[60:61],v199 offset:8232
TR a[62:63],v199 offset:8488
MM v[8:11],a[4:7],a[236:239],v[8:11]
buffer_load_dwordx4 v220, s[32:35], s95 offen offset:2048 lds
MM v[8:11],a[0:3],a[232:235],v[8:11]
s_add_u32 s95, s88, 27648
MM v[16:19],a[0:3],a[240:243],v[16:19]
MM v[16:19],a[4:7],a[244:247],v[16:19]
MM v[24:27],a[4:7],a[252:255],v[24:27]
buffer_load_dwordx4 v221, s[32:35], s95 offen offset:3072 lds
MM v[24:27],a[0:3],a[248:251],v[24:27]
s_add_u32 m0, s94, 36864
s_add_u32 s95, s88, 40960
MM v[4:7],a[8:11],a[224:227],v[4:7]
MM v[4:7],a[12:15],a[228:231],v[4:7]
MM v[12:15],a[12:15],a[236:239],v[12:15]
buffer_load_dwordx4 v220, s[32:35], s95 offen offset:0 lds
MM v[12:15],a[8:11],a[232:235],v[12:15]
s_add_u32 s95, s88, 50176
MM v[20:23],a[8:11],a[240:243],v[20:23]
MM v[20:23],a[12:15],a[244:247],v[20:23]
MM v[28:31],a[12:15],a[252:255],v[28:31]
buffer_load_dwordx4 v221, s[32:35], s95 offen offset:1024 lds
MM v[28:31],a[8:11],a[248:251],v[28:31]
s_add_u32 s95, s88, 59392
v_smfmac_f32_16x16x64_bf16 v[184:187], v[236:239], a[0:7], v234
v_smfmac_f32_16x16x64_bf16 v[188:191], v[236:239], a[8:15], v234
MM v[32:35],a[16:19],a[224:227],v[32:35]
TR a[0:1],v196 offset:16448
TR a[2:3],v196 offset:16704
MM v[32:35],a[20:23],a[228:231],v[32:35]
TR a[4:5],v196 offset:24672
TR a[6:7],v196 offset:24928
MM v[40:43],a[20:23],a[236:239],v[40:43]
TR a[8:9],v196 offset:16456
TR a[10:11],v196 offset:16712
buffer_load_dwordx4 v220, s[32:35], s95 offen offset:2048 lds
MM v[40:43],a[16:19],a[232:235],v[40:43]
s_add_u32 s95, s88, 68608
TR a[12:13],v196 offset:24680
TR a[14:15],v196 offset:24936
MM v[48:51],a[16:19],a[240:243],v[48:51]
buffer_load_dwordx4 v221, s[32:35], s95 offen offset:3072 lds
MM v[48:51],a[20:23],a[244:247],v[48:51]
s_add_u32 s87, s89, s85
s_add_u32 s85, s85, 524288
MM v[56:59],a[20:23],a[252:255],v[56:59]
s_cmp_ge_u32 s85, 5242880
s_cselect_b32 s85, 0, s85
s_add_u32 m0, s96, 81984
MM v[56:59],a[16:19],a[248:251],v[56:59]
MM v[36:39],a[24:27],a[224:227],v[36:39]
TR a[16:17],v197 offset:16448
TR a[18:19],v197 offset:16704
MM v[36:39],a[28:31],a[228:231],v[36:39]
TR a[20:21],v197 offset:24672
TR a[22:23],v197 offset:24928
MM v[44:47],a[28:31],a[236:239],v[44:47]
buffer_load_dwordx4 v253, s[16:19], s87 offen offset:0 lds
MM v[44:47],a[24:27],a[232:235],v[44:47]
s_add_u32 m0, s96, 83008
MM v[52:55],a[24:27],a[240:243],v[52:55]
buffer_load_dwordx4 v254, s[16:19], s87 offen offset:0 lds
MM v[52:55],a[28:31],a[244:247],v[52:55]
s_add_u32 s87, s87, 65536
s_add_u32 m0, s96, 84040
MM v[60:63],a[28:31],a[252:255],v[60:63]
MM v[60:63],a[24:27],a[248:251],v[60:63]
MM v[64:67],a[32:35],a[224:227],v[64:67]
TR a[24:25],v197 offset:16456
TR a[26:27],v197 offset:16712
MM v[64:67],a[36:39],a[228:231],v[64:67]
TR a[28:29],v197 offset:24680
TR a[30:31],v197 offset:24936
MM v[72:75],a[36:39],a[236:239],v[72:75]
buffer_load_dwordx4 v253, s[16:19], s87 offen offset:0 lds
MM v[72:75],a[32:35],a[232:235],v[72:75]
s_add_u32 m0, s96, 85064
MM v[80:83],a[32:35],a[240:243],v[80:83]
buffer_load_dwordx4 v254, s[16:19], s87 offen offset:0 lds
MM v[80:83],a[36:39],a[244:247],v[80:83]
s_waitcnt vmcnt(16)
RD a[128:131],v222 offset:0
MM v[88:91],a[36:39],a[252:255],v[88:91]
RD a[132:135],v223 offset:0
MM v[88:91],a[32:35],a[248:251],v[88:91]
RD a[136:139],v217 offset:0
MM v[68:71],a[40:43],a[224:227],v[68:71]
TR a[32:33],v198 offset:16448
TR a[34:35],v198 offset:16704
MM v[68:71],a[44:47],a[228:231],v[68:71]
TR a[36:37],v198 offset:24672
TR a[38:39],v198 offset:24928
MM v[76:79],a[44:47],a[236:239],v[76:79]
RD a[140:143],v218 offset:0
MM v[76:79],a[40:43],a[232:235],v[76:79]
RD a[144:147],v222 offset:4096
MM v[84:87],a[40:43],a[240:243],v[84:87]
RD a[148:151],v223 offset:4096
MM v[84:87],a[44:47],a[244:247],v[84:87]
RD a[152:155],v217 offset:4096
MM v[92:95],a[44:47],a[252:255],v[92:95]
RD a[156:159],v218 offset:4096
MM v[92:95],a[40:43],a[248:251],v[92:95]
s_waitcnt lgkmcnt(15)
MM v[96:99],a[48:51],a[224:227],v[96:99]
TR a[40:41],v198 offset:16456
TR a[42:43],v198 offset:16712
MM v[96:99],a[52:55],a[228:231],v[96:99]
TR a[44:45],v198 offset:24680
TR a[46:47],v198 offset:24936
MM v[104:107],a[52:55],a[236:239],v[104:107]
s_waitcnt lgkmcnt(4)
s_add_u32 s88, s68, s86
s_add_u32 s86, s86, 128
MM v[104:107],a[48:51],a[232:235],v[104:107]
s_cmp_ge_u32 s86, 1280
s_cselect_b32 s86, 0, s86
s_add_u32 m0, s94, 0
MM v[112:115],a[48:51],a[240:243],v[112:115]
s_add_u32 s95, s88, 0
buffer_load_dwordx4 v220, s[32:35], s95 offen offset:0 lds
MM v[112:115],a[52:55],a[244:247],v[112:115]
s_add_u32 s95, s88, 9216
MM v[120:123],a[52:55],a[252:255],v[120:123]
buffer_load_dwordx4 v221, s[32:35], s95 offen offset:1024 lds
MM v[120:123],a[48:51],a[248:251],v[120:123]
s_add_u32 s95, s88, 18432
MM v[100:103],a[56:59],a[224:227],v[100:103]
TR a[48:49],v199 offset:16448
TR a[50:51],v199 offset:16704
MM v[100:103],a[60:63],a[228:231],v[100:103]
TR a[52:53],v199 offset:24672
TR a[54:55],v199 offset:24928
MM v[108:111],a[60:63],a[236:239],v[108:111]
buffer_load_dwordx4 v220, s[32:35], s95 offen offset:2048 lds
MM v[108:111],a[56:59],a[232:235],v[108:111]
s_add_u32 s95, s88, 27648
MM v[116:119],a[56:59],a[240:243],v[116:119]
MM v[116:119],a[60:63],a[244:247],v[116:119]
MM v[124:127],a[60:63],a[252:255],v[124:127]
buffer_load_dwordx4 v221, s[32:35], s95 offen offset:3072 lds
MM v[124:127],a[56:59],a[248:251],v[124:127]
s_add_u32 m0, s94, 4096
s_add_u32 s95, s88, 40960
s_waitcnt vmcnt(16)
s_barrier
MM v[0:3],a[0:3],a[128:131],v[0:3]
TR a[56:57],v199 offset:16456
TR a[58:59],v199 offset:16712
MM v[0:3],a[4:7],a[132:135],v[0:3]
TR a[60:61],v199 offset:24680
TR a[62:63],v199 offset:24936
MM v[8:11],a[4:7],a[140:143],v[8:11]
buffer_load_dwordx4 v220, s[32:35], s95 offen offset:0 lds
MM v[8:11],a[0:3],a[136:139],v[8:11]
s_add_u32 s95, s88, 50176
MM v[16:19],a[0:3],a[144:147],v[16:19]
MM v[16:19],a[4:7],a[148:151],v[16:19]
MM v[24:27],a[4:7],a[156:159],v[24:27]
buffer_load_dwordx4 v221, s[32:35], s95 offen offset:1024 lds
MM v[24:27],a[0:3],a[152:155],v[24:27]
s_add_u32 s95, s88, 59392
MM v[4:7],a[8:11],a[128:131],v[4:7]
MM v[4:7],a[12:15],a[132:135],v[4:7]
MM v[12:15],a[12:15],a[140:143],v[12:15]
)ASM"
R"ASM(
buffer_load_dwordx4 v220, s[32:35], s95 offen offset:2048 lds
MM v[12:15],a[8:11],a[136:139],v[12:15]
s_add_u32 s95, s88, 68608
MM v[20:23],a[8:11],a[144:147],v[20:23]
MM v[20:23],a[12:15],a[148:151],v[20:23]
MM v[28:31],a[12:15],a[156:159],v[28:31]
buffer_load_dwordx4 v221, s[32:35], s95 offen offset:3072 lds
MM v[28:31],a[8:11],a[152:155],v[28:31]
s_add_u32 s87, s89, s85
s_add_u32 s85, s85, 524288
v_smfmac_f32_16x16x64_bf16 v[184:187], v[236:239], a[0:7], v234
s_cmp_ge_u32 s85, 5242880
s_cselect_b32 s85, 0, s85
v_smfmac_f32_16x16x64_bf16 v[188:191], v[236:239], a[8:15], v234
s_add_u32 m0, s96, 98432
MM v[32:35],a[16:19],a[128:131],v[32:35]
TR a[0:1],v192 offset:0
TR a[2:3],v192 offset:256
MM v[32:35],a[20:23],a[132:135],v[32:35]
TR a[4:5],v192 offset:8224
TR a[6:7],v192 offset:8480
MM v[40:43],a[20:23],a[140:143],v[40:43]
TR a[8:9],v192 offset:8
TR a[10:11],v192 offset:264
buffer_load_dwordx4 v253, s[16:19], s87 offen offset:0 lds
MM v[40:43],a[16:19],a[136:139],v[40:43]
s_add_u32 m0, s96, 99456
TR a[12:13],v192 offset:8232
TR a[14:15],v192 offset:8488
MM v[48:51],a[16:19],a[144:147],v[48:51]
buffer_load_dwordx4 v254, s[16:19], s87 offen offset:0 lds
MM v[48:51],a[20:23],a[148:151],v[48:51]
s_add_u32 s87, s87, 65536
s_add_u32 m0, s96, 100488
MM v[56:59],a[20:23],a[156:159],v[56:59]
MM v[56:59],a[16:19],a[152:155],v[56:59]
MM v[36:39],a[24:27],a[128:131],v[36:39]
TR a[16:17],v193 offset:0
TR a[18:19],v193 offset:256
MM v[36:39],a[28:31],a[132:135],v[36:39]
TR a[20:21],v193 offset:8224
TR a[22:23],v193 offset:8480
MM v[44:47],a[28:31],a[140:143],v[44:47]
buffer_load_dwordx4 v253, s[16:19], s87 offen offset:0 lds
MM v[44:47],a[24:27],a[136:139],v[44:47]
s_add_u32 m0, s96, 101512
MM v[52:55],a[24:27],a[144:147],v[52:55]
buffer_load_dwordx4 v254, s[16:19], s87 offen offset:0 lds
MM v[52:55],a[28:31],a[148:151],v[52:55]
s_waitcnt vmcnt(16)
RD a[160:163],v222 offset:32768
MM v[60:63],a[28:31],a[156:159],v[60:63]
RD a[164:167],v223 offset:32768
MM v[60:63],a[24:27],a[152:155],v[60:63]
RD a[168:171],v217 offset:32768
MM v[64:67],a[32:35],a[128:131],v[64:67]
TR a[24:25],v193 offset:8
TR a[26:27],v193 offset:264
MM v[64:67],a[36:39],a[132:135],v[64:67]
TR a[28:29],v193 offset:8232
TR a[30:31],v193 offset:8488
MM v[72:75],a[36:39],a[140:143],v[72:75]
RD a[172:175],v218 offset:32768
MM v[72:75],a[32:35],a[136:139],v[72:75]
RD a[176:179],v222 offset:36864
MM v[80:83],a[32:35],a[144:147],v[80:83]
RD a[180:183],v223 offset:36864
MM v[80:83],a[36:39],a[148:151],v[80:83]
RD a[184:187],v217 offset:36864
MM v[88:91],a[36:39],a[156:159],v[88:91]
RD a[188:191],v218 offset:36864
MM v[88:91],a[32:35],a[152:155],v[88:91]
s_waitcnt lgkmcnt(15)
MM v[68:71],a[40:43],a[128:131],v[68:71]
TR a[32:33],v194 offset:0
TR a[34:35],v194 offset:256
MM v[68:71],a[44:47],a[132:135],v[68:71]
TR a[36:37],v194 offset:8224
TR a[38:39],v194 offset:8480
MM v[76:79],a[44:47],a[140:143],v[76:79]
s_waitcnt lgkmcnt(4)
s_add_u32 s88, s68, s86
s_add_u32 s86, s86, 128
MM v[76:79],a[40:43],a[136:139],v[76:79]
s_cmp_ge_u32 s86, 1280
s_cselect_b32 s86, 0, s86
s_add_u32 m0, s94, 32768
MM v[84:87],a[40:43],a[144:147],v[84:87]
s_add_u32 s95, s88, 0
buffer_load_dwordx4 v220, s[32:35], s95 offen offset:0 lds
MM v[84:87],a[44:47],a[148:151],v[84:87]
s_add_u32 s95, s88, 9216
MM v[92:95],a[44:47],a[156:159],v[92:95]
buffer_load_dwordx4 v221, s[32:35], s95 offen offset:1024 lds
MM v[92:95],a[40:43],a[152:155],v[92:95]
s_add_u32 s95, s88, 18432
MM v[96:99],a[48:51],a[128:131],v[96:99]
TR a[40:41],v194 offset:8
TR a[42:43],v194 offset:264
MM v[96:99],a[52:55],a[132:135],v[96:99]
TR a[44:45],v194 offset:8232
TR a[46:47],v194 offset:8488
MM v[104:107],a[52:55],a[140:143],v[104:107]
buffer_load_dwordx4 v220, s[32:35], s95 offen offset:2048 lds
MM v[104:107],a[48:51],a[136:139],v[104:107]
s_add_u32 s95, s88, 27648
MM v[112:115],a[48:51],a[144:147],v[112:115]
MM v[112:115],a[52:55],a[148:151],v[112:115]
MM v[120:123],a[52:55],a[156:159],v[120:123]
buffer_load_dwordx4 v221, s[32:35], s95 offen offset:3072 lds
MM v[120:123],a[48:51],a[152:155],v[120:123]
s_add_u32 m0, s94, 36864
s_add_u32 s95, s88, 40960
MM v[100:103],a[56:59],a[128:131],v[100:103]
TR a[48:49],v195 offset:0
TR a[50:51],v195 offset:256
MM v[100:103],a[60:63],a[132:135],v[100:103]
TR a[52:53],v195 offset:8224
TR a[54:55],v195 offset:8480
MM v[108:111],a[60:63],a[140:143],v[108:111]
buffer_load_dwordx4 v220, s[32:35], s95 offen offset:0 lds
MM v[108:111],a[56:59],a[136:139],v[108:111]
s_add_u32 s95, s88, 50176
MM v[116:119],a[56:59],a[144:147],v[116:119]
MM v[116:119],a[60:63],a[148:151],v[116:119]
MM v[124:127],a[60:63],a[156:159],v[124:127]
buffer_load_dwordx4 v221, s[32:35], s95 offen offset:1024 lds
MM v[124:127],a[56:59],a[152:155],v[124:127]
s_add_u32 s95, s88, 59392
s_waitcnt vmcnt(18)
s_barrier
MM v[0:3],a[0:3],a[160:163],v[0:3]
TR a[56:57],v195 offset:8
TR a[58:59],v195 offset:264
MM v[0:3],a[4:7],a[164:167],v[0:3]
TR a[60:61],v195 offset:8232
TR a[62:63],v195 offset:8488
MM v[8:11],a[4:7],a[172:175],v[8:11]
buffer_load_dwordx4 v220, s[32:35], s95 offen offset:2048 lds
MM v[8:11],a[0:3],a[168:171],v[8:11]
s_add_u32 s95, s88, 68608
MM v[16:19],a[0:3],a[176:179],v[16:19]
MM v[16:19],a[4:7],a[180:183],v[16:19]
MM v[24:27],a[4:7],a[188:191],v[24:27]
buffer_load_dwordx4 v221, s[32:35], s95 offen offset:3072 lds
MM v[24:27],a[0:3],a[184:187],v[24:27]
s_add_u32 s87, s89, s85
s_add_u32 s85, s85, 524288
MM v[4:7],a[8:11],a[160:163],v[4:7]
s_cmp_ge_u32 s85, 5242880
s_cselect_b32 s85, 0, s85
s_add_u32 m0, s96, 114880
MM v[4:7],a[12:15],a[164:167],v[4:7]
MM v[12:15],a[12:15],a[172:175],v[12:15]
buffer_load_dwordx4 v253, s[16:19], s87 offen offset:0 lds
MM v[12:15],a[8:11],a[168:171],v[12:15]
s_add_u32 m0, s96, 115904
MM v[20:23],a[8:11],a[176:179],v[20:23]
MM v[20:23],a[12:15],a[180:183],v[20:23]
MM v[28:31],a[12:15],a[188:191],v[28:31]
buffer_load_dwordx4 v254, s[16:19], s87 offen offset:0 lds
MM v[28:31],a[8:11],a[184:187],v[28:31]
s_add_u32 s87, s87, 65536
s_add_u32 m0, s96, 116936
v_smfmac_f32_16x16x64_bf16 v[184:187], v[236:239], a[0:7], v234
v_smfmac_f32_16x16x64_bf16 v[188:191], v[236:239], a[8:15], v234
MM v[32:35],a[16:19],a[160:163],v[32:35]
TR a[0:1],v192 offset:16448
TR a[2:3],v192 offset:16704
MM v[32:35],a[20:23],a[164:167],v[32:35]
TR a[4:5],v192 offset:24672
TR a[6:7],v192 offset:24928
MM v[40:43],a[20:23],a[172:175],v[40:43]
TR a[8:9],v192 offset:16456
TR a[10:11],v192 offset:16712
buffer_load_dwordx4 v253, s[16:19], s87 offen offset:0 lds
MM v[40:43],a[16:19],a[168:171],v[40:43]
s_add_u32 m0, s96, 117960
TR a[12:13],v192 offset:24680
TR a[14:15],v192 offset:24936
MM v[48:51],a[16:19],a[176:179],v[48:51]
buffer_load_dwordx4 v254, s[16:19], s87 offen offset:0 lds
MM v[48:51],a[20:23],a[180:183],v[48:51]
s_waitcnt vmcnt(16)
RD a[192:195],v222 offset:0
MM v[56:59],a[20:23],a[188:191],v[56:59]
RD a[196:199],v223 offset:0
MM v[56:59],a[16:19],a[184:187],v[56:59]
RD a[200:203],v217 offset:0
MM v[36:39],a[24:27],a[160:163],v[36:39]
TR a[16:17],v193 offset:16448
TR a[18:19],v193 offset:16704
MM v[36:39],a[28:31],a[164:167],v[36:39]
TR a[20:21],v193 offset:24672
TR a[22:23],v193 offset:24928
MM v[44:47],a[28:31],a[172:175],v[44:47]
RD a[204:207],v218 offset:0
MM v[44:47],a[24:27],a[168:171],v[44:47]
RD a[208:211],v222 offset:4096
MM v[52:55],a[24:27],a[176:179],v[52:55]
RD a[212:215],v223 offset:4096
MM v[52:55],a[28:31],a[180:183],v[52:55]
RD a[216:219],v217 offset:4096
MM v[60:63],a[28:31],a[188:191],v[60:63]
RD a[220:223],v218 offset:4096
MM v[60:63],a[24:27],a[184:187],v[60:63]
s_waitcnt lgkmcnt(15)
MM v[64:67],a[32:35],a[160:163],v[64:67]
TR a[24:25],v193 offset:16456
TR a[26:27],v193 offset:16712
MM v[64:67],a[36:39],a[164:167],v[64:67]
TR a[28:29],v193 offset:24680
TR a[30:31],v193 offset:24936
MM v[72:75],a[36:39],a[172:175],v[72:75]
s_waitcnt lgkmcnt(4)
s_add_u32 s88, s68, s86
s_add_u32 s86, s86, 128
MM v[72:75],a[32:35],a[168:171],v[72:75]
s_cmp_ge_u32 s86, 1280
s_cselect_b32 s86, 0, s86
s_add_u32 m0, s94, 0
MM v[80:83],a[32:35],a[176:179],v[80:83]
s_add_u32 s95, s88, 0
buffer_load_dwordx4 v220, s[32:35], s95 offen offset:0 lds
MM v[80:83],a[36:39],a[180:183],v[80:83]
s_add_u32 s95, s88, 9216
MM v[88:91],a[36:39],a[188:191],v[88:91]
buffer_load_dwordx4 v221, s[32:35], s95 offen offset:1024 lds
MM v[88:91],a[32:35],a[184:187],v[88:91]
s_add_u32 s95, s88, 18432
MM v[68:71],a[40:43],a[160:163],v[68:71]
TR a[32:33],v194 offset:16448
TR a[34:35],v194 offset:16704
MM v[68:71],a[44:47],a[164:167],v[68:71]
TR a[36:37],v194 offset:24672
TR a[38:39],v194 offset:24928
MM v[76:79],a[44:47],a[172:175],v[76:79]
buffer_load_dwordx4 v220, s[32:35], s95 offen offset:2048 lds
MM v[76:79],a[40:43],a[168:171],v[76:79]
s_add_u32 s95, s88, 27648
MM v[84:87],a[40:43],a[176:179],v[84:87]
MM v[84:87],a[44:47],a[180:183],v[84:87]
MM v[92:95],a[44:47],a[188:191],v[92:95]
buffer_load_dwordx4 v221, s[32:35], s95 offen offset:3072 lds
MM v[92:95],a[40:43],a[184:187],v[92:95]
s_add_u32 m0, s94, 4096
s_add_u32 s95, s88, 40960
MM v[96:99],a[48:51],a[160:163],v[96:99]
TR a[40:41],v194 offset:16456
TR a[42:43],v194 offset:16712
MM v[96:99],a[52:55],a[164:167],v[96:99]
TR a[44:45],v194 offset:24680
TR a[46:47],v194 offset:24936
MM v[104:107],a[52:55],a[172:175],v[104:107]
buffer_load_dwordx4 v220, s[32:35], s95 offen offset:0 lds
MM v[104:107],a[48:51],a[168:171],v[104:107]
s_add_u32 s95, s88, 50176
MM v[112:115],a[48:51],a[176:179],v[112:115]
MM v[112:115],a[52:55],a[180:183],v[112:115]
MM v[120:123],a[52:55],a[188:191],v[120:123]
buffer_load_dwordx4 v221, s[32:35], s95 offen offset:1024 lds
MM v[120:123],a[48:51],a[184:187],v[120:123]
s_add_u32 s95, s88, 59392
MM v[100:103],a[56:59],a[160:163],v[100:103]
TR a[48:49],v195 offset:16448
TR a[50:51],v195 offset:16704
MM v[100:103],a[60:63],a[164:167],v[100:103]
TR a[52:53],v195 offset:24672
TR a[54:55],v195 offset:24928
MM v[108:111],a[60:63],a[172:175],v[108:111]
buffer_load_dwordx4 v220, s[32:35], s95 offen offset:2048 lds
MM v[108:111],a[56:59],a[168:171],v[108:111]
s_add_u32 s95, s88, 68608
MM v[116:119],a[56:59],a[176:179],v[116:119]
MM v[116:119],a[60:63],a[180:183],v[116:119]
MM v[124:127],a[60:63],a[188:191],v[124:127]
buffer_load_dwordx4 v221, s[32:35], s95 offen offset:3072 lds
MM v[124:127],a[56:59],a[184:187],v[124:127]
s_add_u32 s87, s89, s85
s_add_u32 s85, s85, 524288
s_waitcnt vmcnt(20)
s_barrier
MM v[0:3],a[0:3],a[192:195],v[0:3]
TR a[56:57],v195 offset:16456
TR a[58:59],v195 offset:16712
MM v[0:3],a[4:7],a[196:199],v[0:3]
TR a[60:61],v195 offset:24680
TR a[62:63],v195 offset:24936
s_cmp_ge_u32 s85, 5242880
s_cselect_b32 s85, 0, s85
MM v[8:11],a[4:7],a[204:207],v[8:11]
s_add_u32 m0, s96, 131328
s_nop 0
buffer_load_dwordx4 v253, s[16:19], s87 offen offset:0 lds
v_and_b32 v208, 240, v248
MM v[8:11],a[0:3],a[200:203],v[8:11]
s_add_u32 m0, s96, 132352
s_add_u32 s93, s93, 36864
MM v[16:19],a[0:3],a[208:211],v[16:19]
MM v[16:19],a[4:7],a[212:215],v[16:19]
MM v[24:27],a[4:7],a[220:223],v[24:27]
LPG3_%=:
s_cbranch_execz LPG7_%=
buffer_load_dwordx4 v254, s[16:19], s87 offen offset:0 lds
MM v[24:27],a[0:3],a[216:219],v[24:27]
s_add_u32 s87, s87, 65536
s_add_u32 m0, s96, 133384
MM v[4:7],a[8:11],a[192:195],v[4:7]
MM v[4:7],a[12:15],a[196:199],v[4:7]
MM v[12:15],a[12:15],a[204:207],v[12:15]
buffer_load_dwordx4 v253, s[16:19], s87 offen offset:0 lds
MM v[12:15],a[8:11],a[200:203],v[12:15]
s_add_u32 m0, s96, 134408
MM v[20:23],a[8:11],a[208:211],v[20:23]
MM v[20:23],a[12:15],a[212:215],v[20:23]
MM v[28:31],a[12:15],a[220:223],v[28:31]
buffer_load_dwordx4 v254, s[16:19], s87 offen offset:0 lds
MM v[28:31],a[8:11],a[216:219],v[28:31]
s_waitcnt vmcnt(16)
RD a[224:227],v222 offset:32768
v_smfmac_f32_16x16x64_bf16 v[184:187], v[236:239], a[0:7], v234
RD a[228:231],v223 offset:32768
v_smfmac_f32_16x16x64_bf16 v[188:191], v[236:239], a[8:15], v234
RD a[232:235],v217 offset:32768
MM v[32:35],a[16:19],a[192:195],v[32:35]
TR a[0:1],v192 offset:32896
TR a[2:3],v192 offset:33152
MM v[32:35],a[20:23],a[196:199],v[32:35]
TR a[4:5],v192 offset:41120
TR a[6:7],v192 offset:41376
)ASM"
R"ASM(
MM v[40:43],a[20:23],a[204:207],v[40:43]
TR a[8:9],v192 offset:32904
TR a[10:11],v192 offset:33160
RD a[236:239],v218 offset:32768
buffer_load_dwordx4 v[200:203], v208, s[20:23], s93 offen offset:0 sc1
MM v[40:43],a[16:19],a[200:203],v[40:43]
TR a[12:13],v192 offset:41128
TR a[14:15],v192 offset:41384
MM v[48:51],a[16:19],a[208:211],v[48:51]
buffer_load_dwordx4 v[204:207], v208, s[20:23], s93 offen offset:256 sc1
MM v[48:51],a[20:23],a[212:215],v[48:51]
RD a[240:243],v222 offset:36864
MM v[56:59],a[20:23],a[220:223],v[56:59]
RD a[244:247],v223 offset:36864
MM v[56:59],a[16:19],a[216:219],v[56:59]
s_waitcnt lgkmcnt(15)
RD a[248:251],v217 offset:36864
MM v[36:39],a[24:27],a[192:195],v[36:39]
TR a[16:17],v193 offset:32896
TR a[18:19],v193 offset:33152
MM v[36:39],a[28:31],a[196:199],v[36:39]
TR a[20:21],v193 offset:41120
TR a[22:23],v193 offset:41376
MM v[44:47],a[28:31],a[204:207],v[44:47]
RD a[252:255],v218 offset:36864
MM v[44:47],a[24:27],a[200:203],v[44:47]
MM v[52:55],a[24:27],a[208:211],v[52:55]
MM v[52:55],a[28:31],a[212:215],v[52:55]
MM v[60:63],a[28:31],a[220:223],v[60:63]
s_waitcnt lgkmcnt(0)
s_add_u32 s88, s68, s86
s_add_u32 s86, s86, 128
MM v[60:63],a[24:27],a[216:219],v[60:63]
s_cmp_ge_u32 s86, 1280
s_cselect_b32 s86, 0, s86
s_add_u32 m0, s94, 32768
MM v[64:67],a[32:35],a[192:195],v[64:67]
s_add_u32 s95, s88, 0
TR a[24:25],v193 offset:32904
TR a[26:27],v193 offset:33160
MM v[64:67],a[36:39],a[196:199],v[64:67]
TR a[28:29],v193 offset:41128
TR a[30:31],v193 offset:41384
MM v[72:75],a[36:39],a[204:207],v[72:75]
buffer_load_dwordx4 v220, s[32:35], s95 offen offset:0 lds
MM v[72:75],a[32:35],a[200:203],v[72:75]
s_add_u32 s95, s88, 9216
MM v[80:83],a[32:35],a[208:211],v[80:83]
buffer_load_dwordx4 v221, s[32:35], s95 offen offset:1024 lds
MM v[80:83],a[36:39],a[212:215],v[80:83]
s_add_u32 s95, s88, 18432
MM v[88:91],a[36:39],a[220:223],v[88:91]
MM v[88:91],a[32:35],a[216:219],v[88:91]
MM v[68:71],a[40:43],a[192:195],v[68:71]
TR a[32:33],v194 offset:32896
TR a[34:35],v194 offset:33152
MM v[68:71],a[44:47],a[196:199],v[68:71]
TR a[36:37],v194 offset:41120
TR a[38:39],v194 offset:41376
MM v[76:79],a[44:47],a[204:207],v[76:79]
buffer_load_dwordx4 v220, s[32:35], s95 offen offset:2048 lds
MM v[76:79],a[40:43],a[200:203],v[76:79]
s_add_u32 s95, s88, 27648
MM v[84:87],a[40:43],a[208:211],v[84:87]
buffer_load_dwordx4 v221, s[32:35], s95 offen offset:3072 lds
MM v[84:87],a[44:47],a[212:215],v[84:87]
s_waitcnt vmcnt(10)
RD a[128:131],v222 offset:0
MM v[92:95],a[44:47],a[220:223],v[92:95]
RD a[132:135],v223 offset:0
MM v[92:95],a[40:43],a[216:219],v[92:95]
RD a[136:139],v217 offset:0
MM v[96:99],a[48:51],a[192:195],v[96:99]
TR a[40:41],v194 offset:32904
TR a[42:43],v194 offset:33160
MM v[96:99],a[52:55],a[196:199],v[96:99]
TR a[44:45],v194 offset:41128
TR a[46:47],v194 offset:41384
MM v[104:107],a[52:55],a[204:207],v[104:107]
RD a[140:143],v218 offset:0
MM v[104:107],a[48:51],a[200:203],v[104:107]
RD a[144:147],v222 offset:4096
MM v[112:115],a[48:51],a[208:211],v[112:115]
RD a[148:151],v223 offset:4096
MM v[112:115],a[52:55],a[212:215],v[112:115]
RD a[152:155],v217 offset:4096
MM v[120:123],a[52:55],a[220:223],v[120:123]
RD a[156:159],v218 offset:4096
MM v[120:123],a[48:51],a[216:219],v[120:123]
s_add_u32 m0, s94, 36864
s_add_u32 s95, s88, 40960
MM v[100:103],a[56:59],a[192:195],v[100:103]
TR a[48:49],v195 offset:32896
TR a[50:51],v195 offset:33152
MM v[100:103],a[60:63],a[196:199],v[100:103]
TR a[52:53],v195 offset:41120
TR a[54:55],v195 offset:41376
buffer_load_dwordx4 v220, s[32:35], s95 offen offset:0 lds
MM v[108:111],a[60:63],a[204:207],v[108:111]
s_add_u32 s95, s88, 50176
MM v[108:111],a[56:59],a[200:203],v[108:111]
buffer_load_dwordx4 v221, s[32:35], s95 offen offset:1024 lds
MM v[116:119],a[56:59],a[208:211],v[116:119]
s_add_u32 s95, s88, 59392
MM v[116:119],a[60:63],a[212:215],v[116:119]
MM v[124:127],a[60:63],a[220:223],v[124:127]
MM v[124:127],a[56:59],a[216:219],v[124:127]
buffer_load_dwordx4 v220, s[32:35], s95 offen offset:2048 lds
s_add_u32 s95, s88, 68608
buffer_load_dwordx4 v221, s[32:35], s95 offen offset:3072 lds
s_waitcnt lgkmcnt(4)
s_waitcnt vmcnt(10)
s_barrier
MM v[0:3],a[0:3],a[224:227],v[0:3]
s_add_u32 s77, s64, 0
TR a[56:57],v195 offset:32904
TR a[58:59],v195 offset:33160
MM v[0:3],a[4:7],a[228:231],v[0:3]
s_add_u32 m0, s65, 0
TR a[60:61],v195 offset:41128
TR a[62:63],v195 offset:41384
MM v[8:11],a[4:7],a[236:239],v[8:11]
s_waitcnt vmcnt(8)
TR a[64:65],v196 offset:0
TR a[66:67],v196 offset:256
MM v[8:11],a[0:3],a[232:235],v[8:11]
s_lshl_b32 s76, %[swave], 7
v_lshrrev_b32 v253, 8, v248
v_lshlrev_b32 v253, 5, v253
MM v[16:19],a[0:3],a[240:243],v[16:19]
s_add_u32 s76, s76, %[slds]
TR a[68:69],v196 offset:8224
TR a[70:71],v196 offset:8480
v_xor_b32 v201, s69, v201
MM v[16:19],a[4:7],a[244:247],v[16:19]
buffer_load_dwordx4 v228, s[8:11], s77 offen offset:0 lds
s_mov_b64 exec, s[54:55]
buffer_store_dwordx2 v[170:171], off, s[20:23], s80 sc1
s_mov_b64 exec, -1
v_add_u32 v253, s76, v253
v_add_u32 v253, 151552, v253
MM v[24:27],a[4:7],a[252:255],v[24:27]
TR a[72:73],v196 offset:16448
TR a[74:75],v196 offset:16704
MM v[24:27],a[0:3],a[248:251],v[24:27]
TR a[76:77],v196 offset:24672
TR a[78:79],v196 offset:24928
v_xor_b32 v203, s69, v203
MM v[4:7],a[8:11],a[224:227],v[4:7]
TR a[80:81],v196 offset:8
TR a[82:83],v196 offset:264
MM v[4:7],a[12:15],a[228:231],v[4:7]
TR a[84:85],v196 offset:8232
TR a[86:87],v196 offset:8488
buffer_load_dwordx4 v229, s[8:11], s77 offen offset:1024 lds
MM v[12:15],a[12:15],a[236:239],v[12:15]
TR a[88:89],v196 offset:16456
TR a[90:91],v196 offset:16712
v_xor_b32 v205, s69, v205
MM v[12:15],a[8:11],a[232:235],v[12:15]
TR a[92:93],v196 offset:24680
TR a[94:95],v196 offset:24936
MM v[20:23],a[8:11],a[240:243],v[20:23]
TR a[96:97],v197 offset:0
TR a[98:99],v197 offset:256
MM v[20:23],a[12:15],a[244:247],v[20:23]
TR a[100:101],v197 offset:8224
TR a[102:103],v197 offset:8480
buffer_load_dwordx4 v230, s[8:11], s77 offen offset:2048 lds
v_xor_b32 v207, s69, v207
MM v[28:31],a[12:15],a[252:255],v[28:31]
TR a[104:105],v197 offset:16448
TR a[106:107],v197 offset:16704
MM v[28:31],a[8:11],a[248:251],v[28:31]
TR a[108:109],v197 offset:24672
TR a[110:111],v197 offset:24928
v_smfmac_f32_16x16x64_bf16 v[184:187], v[236:239], a[0:7], v234
TR a[112:113],v197 offset:8
v_or3_b32 v209, v201, v203, v205
v_smfmac_f32_16x16x64_bf16 v[188:191], v[236:239], a[8:15], v234
TR a[114:115],v197 offset:264
buffer_load_dwordx4 v231, s[8:11], s77 offen offset:3072 lds
MM v[32:35],a[16:19],a[224:227],v[32:35]
s_add_u32 s77, s64, 16384
TR a[116:117],v197 offset:8232
TR a[118:119],v197 offset:8488
MM v[32:35],a[20:23],a[228:231],v[32:35]
s_add_u32 m0, s65, 16384
TR a[120:121],v197 offset:16456
TR a[122:123],v197 offset:16712
v_or_b32 v209, v209, v207
MM v[40:43],a[20:23],a[236:239],v[40:43]
TR a[124:125],v197 offset:24680
TR a[126:127],v197 offset:24936
MM v[40:43],a[16:19],a[232:235],v[40:43]
TR a[0:1],v198 offset:0
TR a[2:3],v198 offset:256
MM v[48:51],a[16:19],a[240:243],v[48:51]
TR a[4:5],v198 offset:8224
TR a[6:7],v198 offset:8480
v_cmp_ne_u32 vcc, 0, v209
s_cbranch_vccnz LRG1o_%=
LRG1ob_%=:
MM v[48:51],a[20:23],a[244:247],v[48:51]
TR a[8:9],v198 offset:16448
TR a[10:11],v198 offset:16704
buffer_load_dwordx4 v228, s[8:11], s77 offen offset:0 lds
MM v[56:59],a[20:23],a[252:255],v[56:59]
TR a[12:13],v198 offset:24672
TR a[14:15],v198 offset:24928
MM v[56:59],a[16:19],a[248:251],v[56:59]
s_bitcmp1_b32 %[sb], 0
s_cselect_b64 exec, -1, 0
v_swap_b32 v200, v202
v_swap_b32 v204, v206
s_mov_b64 exec, -1
MM v[36:39],a[24:27],a[224:227],v[36:39]
v_lshlrev_b32 v240, 16, v200
v_and_b32 v200, 0xffff0000, v200
MM v[36:39],a[28:31],a[228:231],v[36:39]
v_rcp_f32 v246, v200
MM v[44:47],a[28:31],a[236:239],v[44:47]
v_xor_b32 v240, 0x80000000, v240
v_mul_f32 v246, s72, v246
MM v[44:47],a[24:27],a[232:235],v[44:47]
v_lshlrev_b32 v241, 16, v202
v_and_b32 v202, 0xffff0000, v202
MM v[52:55],a[24:27],a[240:243],v[52:55]
v_rcp_f32 v247, v202
MM v[52:55],a[28:31],a[244:247],v[52:55]
buffer_load_dwordx4 v229, s[8:11], s77 offen offset:1024 lds
MM v[60:63],a[28:31],a[252:255],v[60:63]
v_xor_b32 v241, 0x80000000, v241
v_mul_f32 v247, s72, v247
MM v[60:63],a[24:27],a[248:251],v[60:63]
v_lshlrev_b32 v242, 16, v204
v_and_b32 v204, 0xffff0000, v204
MM v[64:67],a[32:35],a[224:227],v[64:67]
TR a[16:17],v198 offset:8
TR a[18:19],v198 offset:264
MM v[64:67],a[36:39],a[228:231],v[64:67]
TR a[20:21],v198 offset:8232
TR a[22:23],v198 offset:8488
v_rcp_f32 v244, v204
MM v[72:75],a[36:39],a[236:239],v[72:75]
TR a[24:25],v198 offset:16456
TR a[26:27],v198 offset:16712
MM v[72:75],a[32:35],a[232:235],v[72:75]
TR a[28:29],v198 offset:24680
TR a[30:31],v198 offset:24936
MM v[80:83],a[32:35],a[240:243],v[80:83]
v_xor_b32 v242, 0x80000000, v242
v_mul_f32 v244, s72, v244
MM v[80:83],a[36:39],a[244:247],v[80:83]
v_lshlrev_b32 v243, 16, v206
v_and_b32 v206, 0xffff0000, v206
MM v[88:91],a[36:39],a[252:255],v[88:91]
v_rcp_f32 v245, v206
MM v[88:91],a[32:35],a[248:251],v[88:91]
s_waitcnt lgkmcnt(15)
buffer_load_dwordx4 v230, s[8:11], s77 offen offset:2048 lds
v_smfmac_f32_16x16x64_bf16 v[184:187], v[236:239], a[64:71], v234
v_xor_b32 v243, 0x80000000, v243
v_smfmac_f32_16x16x64_bf16 v[184:187], v[236:239], a[72:79], v234
v_mul_f32 v245, s72, v245
v_smfmac_f32_16x16x64_bf16 v[188:191], v[236:239], a[80:87], v234
v_smfmac_f32_16x16x64_bf16 v[188:191], v[236:239], a[88:95], v234
MM v[68:71],a[40:43],a[224:227],v[68:71]
v_bfe_u32 v183, v248, 6, 2
v_lshlrev_b32 v183, 5, v183
MM v[68:71],a[44:47],a[228:231],v[68:71]
v_bfe_u32 v182, v248, 4, 2
v_lshl_add_u32 v183, v182, 2, v183
MM v[76:79],a[44:47],a[236:239],v[76:79]
v_add_u32 v183, s76, v183
v_add_u32 v183, 151552, v183
MM v[76:79],a[40:43],a[232:235],v[76:79]
MM v[84:87],a[40:43],a[240:243],v[84:87]
MM v[84:87],a[44:47],a[244:247],v[84:87]
buffer_load_dwordx4 v231, s[8:11], s77 offen offset:3072 lds
MM v[92:95],a[44:47],a[252:255],v[92:95]
s_waitcnt vmcnt(9)
RD a[160:163],v222 offset:32768
MM v[92:95],a[40:43],a[248:251],v[92:95]
v_add_f32 v184, v184, v185
v_add_f32 v188, v188, v189
MM v[96:99],a[48:51],a[224:227],v[96:99]
TR a[32:33],v199 offset:0
TR a[34:35],v199 offset:256
MM v[96:99],a[52:55],a[228:231],v[96:99]
RD a[164:167],v223 offset:32768
ds_write_b32 v183, v184 offset:0
ds_write_b32 v183, v188 offset:16
MM v[104:107],a[52:55],a[236:239],v[104:107]
TR a[36:37],v199 offset:8224
TR a[38:39],v199 offset:8480
MM v[104:107],a[48:51],a[232:235],v[104:107]
TR a[40:41],v199 offset:16448
TR a[42:43],v199 offset:16704
MM v[112:115],a[48:51],a[240:243],v[112:115]
TR a[44:45],v199 offset:24672
TR a[46:47],v199 offset:24928
RD a[168:171],v217 offset:32768
MM v[112:115],a[52:55],a[244:247],v[112:115]
RD a[172:175],v218 offset:32768
MM v[120:123],a[52:55],a[252:255],v[120:123]
RD a[176:179],v222 offset:36864
MM v[120:123],a[48:51],a[248:251],v[120:123]
RD a[180:183],v223 offset:36864
MM v[100:103],a[56:59],a[224:227],v[100:103]
RD a[184:187],v217 offset:36864
MM v[100:103],a[60:63],a[228:231],v[100:103]
RD a[188:191],v218 offset:36864
MM v[108:111],a[60:63],a[236:239],v[108:111]
s_add_u32 s77, s64, 0
s_add_u32 s81, s77, 896
s_add_u32 m0, s65, 65536
MM v[108:111],a[56:59],a[232:235],v[108:111]
buffer_load_dwordx4 v235, s[12:15], s81 offen offset:0 lds
MM v[116:119],a[56:59],a[240:243],v[116:119]
MM v[116:119],a[60:63],a[244:247],v[116:119]
MM v[124:127],a[60:63],a[252:255],v[124:127]
MM v[124:127],a[56:59],a[248:251],v[124:127]
s_waitcnt lgkmcnt(0)
s_barrier
MM v[0:3],a[64:67],a[128:131],v[0:3]
s_mov_b32 s56, 0x000f000f
TR a[48:49],v199 offset:8
TR a[50:51],v199 offset:264
MM v[0:3],a[68:71],a[132:135],v[0:3]
s_mov_b32 s57, s56
TR a[52:53],v199 offset:8232
TR a[54:55],v199 offset:8488
MM v[0:3],a[72:75],a[160:163],v[0:3]
s_lshl_b64 s[58:59], s[56:57], 4
TR a[56:57],v199 offset:16456
TR a[58:59],v199 offset:16712
MM v[0:3],a[76:79],a[164:167],v[0:3]
TR a[60:61],v199 offset:24680
TR a[62:63],v199 offset:24936
buffer_load_dwordx4 v235, s[12:15], s77 offen offset:1024 lds
MM v[8:11],a[76:79],a[172:175],v[8:11]
)ASM"
R"ASM(
v_xor_b32 v151, 0, v253
v_xor_b32 v143, 0, v253
MM v[8:11],a[72:75],a[168:171],v[8:11]
RD v[144:147],v151 offset:0
RD v[148:151],v151 offset:16
MM v[8:11],a[68:71],a[140:143],v[8:11]
RD v[136:139],v143 offset:0
RD v[140:143],v143 offset:16
MM v[8:11],a[64:67],a[136:139],v[8:11]
MM v[4:7],a[80:83],a[128:131],v[4:7]
MM v[4:7],a[84:87],a[132:135],v[4:7]
s_waitcnt lgkmcnt(2)
MM v[4:7],a[88:91],a[160:163],v[4:7]
s_waitcnt lgkmcnt(0)
MM v[4:7],a[92:95],a[164:167],v[4:7]
buffer_load_dwordx4 v235, s[12:15], s81 offen offset:2048 lds
MM v[12:15],a[92:95],a[172:175],v[12:15]
MM v[12:15],a[88:91],a[168:171],v[12:15]
MM v[12:15],a[84:87],a[140:143],v[12:15]
MM v[12:15],a[80:83],a[136:139],v[12:15]
MM v[32:35],a[96:99],a[128:131],v[32:35]
MM v[32:35],a[100:103],a[132:135],v[32:35]
MM v[32:35],a[104:107],a[160:163],v[32:35]
MM v[32:35],a[108:111],a[164:167],v[32:35]
v_pk_fma_f32 v[0:1], v[240:241], v[144:145], v[0:1] op_sel:[0,0,0] op_sel_hi:[0,1,1]
v_pk_fma_f32 v[2:3], v[240:241], v[146:147], v[2:3] op_sel:[0,0,0] op_sel_hi:[0,1,1]
v_pk_fma_f32 v[4:5], v[240:241], v[148:149], v[4:5] op_sel:[0,0,0] op_sel_hi:[0,1,1]
v_pk_fma_f32 v[6:7], v[240:241], v[150:151], v[6:7] op_sel:[0,0,0] op_sel_hi:[0,1,1]
v_pk_mul_f32 v[0:1], v[246:247], v[0:1] op_sel:[0,0] op_sel_hi:[0,1]
v_pk_mul_f32 v[2:3], v[246:247], v[2:3] op_sel:[0,0] op_sel_hi:[0,1]
v_pk_mul_f32 v[4:5], v[246:247], v[4:5] op_sel:[0,0] op_sel_hi:[0,1]
v_pk_mul_f32 v[6:7], v[246:247], v[6:7] op_sel:[0,0] op_sel_hi:[0,1]
MM v[40:43],a[108:111],a[172:175],v[40:43]
CV v160,v0,v1
CV v161,v2,v3
MM v[40:43],a[104:107],a[168:171],v[40:43]
buffer_load_dwordx4 v235, s[12:15], s77 offen offset:3072 lds
MM v[40:43],a[100:103],a[140:143],v[40:43]
s_add_u32 s77, s64, 32768
s_add_u32 m0, s65, 32768
CV v162,v4,v5
MM v[40:43],a[96:99],a[136:139],v[40:43]
CV v163,v6,v7
MM v[36:39],a[112:115],a[128:131],v[36:39]
v_pk_fma_f32 v[8:9], v[240:241], v[144:145], v[8:9] op_sel:[1,0,0] op_sel_hi:[1,1,1]
v_pk_fma_f32 v[10:11], v[240:241], v[146:147], v[10:11] op_sel:[1,0,0] op_sel_hi:[1,1,1]
v_pk_fma_f32 v[12:13], v[240:241], v[148:149], v[12:13] op_sel:[1,0,0] op_sel_hi:[1,1,1]
v_pk_fma_f32 v[14:15], v[240:241], v[150:151], v[14:15] op_sel:[1,0,0] op_sel_hi:[1,1,1]
v_pk_mul_f32 v[8:9], v[246:247], v[8:9] op_sel:[1,0] op_sel_hi:[1,1]
v_pk_mul_f32 v[10:11], v[246:247], v[10:11] op_sel:[1,0] op_sel_hi:[1,1]
v_pk_mul_f32 v[12:13], v[246:247], v[12:13] op_sel:[1,0] op_sel_hi:[1,1]
v_pk_mul_f32 v[14:15], v[246:247], v[14:15] op_sel:[1,0] op_sel_hi:[1,1]
MM v[36:39],a[116:119],a[132:135],v[36:39]
CV v164,v8,v9
CV v165,v10,v11
MM v[36:39],a[120:123],a[160:163],v[36:39]
CV v166,v12,v13
CV v167,v14,v15
MM v[36:39],a[124:127],a[164:167],v[36:39]
v_xor_b32 v159, 128, v253
MM v[44:47],a[124:127],a[172:175],v[44:47]
RD v[152:155],v159 offset:0
RD v[156:159],v159 offset:16
MM v[44:47],a[120:123],a[168:171],v[44:47]
MM v[44:47],a[116:119],a[140:143],v[44:47]
MM v[44:47],a[112:115],a[136:139],v[44:47]
buffer_load_dwordx4 v228, s[8:11], s77 offen offset:0 lds
MM v[64:67],a[0:3],a[128:131],v[64:67]
s_waitcnt lgkmcnt(0)
MM v[64:67],a[4:7],a[132:135],v[64:67]
MM v[64:67],a[8:11],a[160:163],v[64:67]
MM v[64:67],a[12:15],a[164:167],v[64:67]
v_pk_fma_f32 v[32:33], v[240:241], v[152:153], v[32:33] op_sel:[0,0,0] op_sel_hi:[0,1,1]
v_pk_fma_f32 v[34:35], v[240:241], v[154:155], v[34:35] op_sel:[0,0,0] op_sel_hi:[0,1,1]
v_pk_fma_f32 v[36:37], v[240:241], v[156:157], v[36:37] op_sel:[0,0,0] op_sel_hi:[0,1,1]
v_pk_fma_f32 v[38:39], v[240:241], v[158:159], v[38:39] op_sel:[0,0,0] op_sel_hi:[0,1,1]
v_pk_mul_f32 v[32:33], v[246:247], v[32:33] op_sel:[0,0] op_sel_hi:[0,1]
v_pk_mul_f32 v[34:35], v[246:247], v[34:35] op_sel:[0,0] op_sel_hi:[0,1]
v_pk_mul_f32 v[36:37], v[246:247], v[36:37] op_sel:[0,0] op_sel_hi:[0,1]
v_pk_mul_f32 v[38:39], v[246:247], v[38:39] op_sel:[0,0] op_sel_hi:[0,1]
MM v[72:75],a[12:15],a[172:175],v[72:75]
CV v176,v32,v33
CV v177,v34,v35
MM v[72:75],a[8:11],a[168:171],v[72:75]
CV v178,v36,v37
CV v179,v38,v39
MM v[72:75],a[4:7],a[140:143],v[72:75]
v_pk_fma_f32 v[40:41], v[240:241], v[152:153], v[40:41] op_sel:[1,0,0] op_sel_hi:[1,1,1]
v_pk_fma_f32 v[42:43], v[240:241], v[154:155], v[42:43] op_sel:[1,0,0] op_sel_hi:[1,1,1]
v_pk_fma_f32 v[44:45], v[240:241], v[156:157], v[44:45] op_sel:[1,0,0] op_sel_hi:[1,1,1]
v_pk_fma_f32 v[46:47], v[240:241], v[158:159], v[46:47] op_sel:[1,0,0] op_sel_hi:[1,1,1]
v_pk_mul_f32 v[40:41], v[246:247], v[40:41] op_sel:[1,0] op_sel_hi:[1,1]
v_pk_mul_f32 v[42:43], v[246:247], v[42:43] op_sel:[1,0] op_sel_hi:[1,1]
v_pk_mul_f32 v[44:45], v[246:247], v[44:45] op_sel:[1,0] op_sel_hi:[1,1]
v_pk_mul_f32 v[46:47], v[246:247], v[46:47] op_sel:[1,0] op_sel_hi:[1,1]
MM v[72:75],a[0:3],a[136:139],v[72:75]
CV v180,v40,v41
CV v181,v42,v43
MM v[68:71],a[16:19],a[128:131],v[68:71]
CV v182,v44,v45
CV v183,v46,v47
MM v[68:71],a[20:23],a[132:135],v[68:71]
buffer_load_dwordx4 v229, s[8:11], s77 offen offset:1024 lds
MM v[68:71],a[24:27],a[160:163],v[68:71]
v_xor_b32 v151, 256, v253
MM v[68:71],a[28:31],a[164:167],v[68:71]
RD v[144:147],v151 offset:0
RD v[148:151],v151 offset:16
MM v[76:79],a[28:31],a[172:175],v[76:79]
MM v[76:79],a[24:27],a[168:171],v[76:79]
MM v[76:79],a[20:23],a[140:143],v[76:79]
MM v[76:79],a[16:19],a[136:139],v[76:79]
s_waitcnt lgkmcnt(0)
MM v[96:99],a[32:35],a[128:131],v[96:99]
MM v[96:99],a[36:39],a[132:135],v[96:99]
MM v[96:99],a[40:43],a[160:163],v[96:99]
MM v[96:99],a[44:47],a[164:167],v[96:99]
buffer_load_dwordx4 v230, s[8:11], s77 offen offset:2048 lds
MM v[104:107],a[44:47],a[172:175],v[104:107]
v_pk_fma_f32 v[64:65], v[240:241], v[144:145], v[64:65] op_sel:[0,0,0] op_sel_hi:[0,1,1]
v_pk_fma_f32 v[66:67], v[240:241], v[146:147], v[66:67] op_sel:[0,0,0] op_sel_hi:[0,1,1]
v_pk_fma_f32 v[68:69], v[240:241], v[148:149], v[68:69] op_sel:[0,0,0] op_sel_hi:[0,1,1]
v_pk_fma_f32 v[70:71], v[240:241], v[150:151], v[70:71] op_sel:[0,0,0] op_sel_hi:[0,1,1]
v_pk_mul_f32 v[64:65], v[246:247], v[64:65] op_sel:[0,0] op_sel_hi:[0,1]
v_pk_mul_f32 v[66:67], v[246:247], v[66:67] op_sel:[0,0] op_sel_hi:[0,1]
v_pk_mul_f32 v[68:69], v[246:247], v[68:69] op_sel:[0,0] op_sel_hi:[0,1]
v_pk_mul_f32 v[70:71], v[246:247], v[70:71] op_sel:[0,0] op_sel_hi:[0,1]
MM v[104:107],a[40:43],a[168:171],v[104:107]
CV v192,v64,v65
CV v193,v66,v67
MM v[104:107],a[36:39],a[140:143],v[104:107]
CV v194,v68,v69
CV v195,v70,v71
MM v[104:107],a[32:35],a[136:139],v[104:107]
v_pk_fma_f32 v[72:73], v[240:241], v[144:145], v[72:73] op_sel:[1,0,0] op_sel_hi:[1,1,1]
v_pk_fma_f32 v[74:75], v[240:241], v[146:147], v[74:75] op_sel:[1,0,0] op_sel_hi:[1,1,1]
v_pk_fma_f32 v[76:77], v[240:241], v[148:149], v[76:77] op_sel:[1,0,0] op_sel_hi:[1,1,1]
v_pk_fma_f32 v[78:79], v[240:241], v[150:151], v[78:79] op_sel:[1,0,0] op_sel_hi:[1,1,1]
v_pk_mul_f32 v[72:73], v[246:247], v[72:73] op_sel:[1,0] op_sel_hi:[1,1]
v_pk_mul_f32 v[74:75], v[246:247], v[74:75] op_sel:[1,0] op_sel_hi:[1,1]
v_pk_mul_f32 v[76:77], v[246:247], v[76:77] op_sel:[1,0] op_sel_hi:[1,1]
v_pk_mul_f32 v[78:79], v[246:247], v[78:79] op_sel:[1,0] op_sel_hi:[1,1]
MM v[100:103],a[48:51],a[128:131],v[100:103]
CV v196,v72,v73
CV v197,v74,v75
MM v[100:103],a[52:55],a[132:135],v[100:103]
CV v198,v76,v77
CV v199,v78,v79
MM v[100:103],a[56:59],a[160:163],v[100:103]
v_xor_b32 v159, 384, v253
MM v[100:103],a[60:63],a[164:167],v[100:103]
RD v[152:155],v159 offset:0
RD v[156:159],v159 offset:16
MM v[108:111],a[60:63],a[172:175],v[108:111]
MM v[108:111],a[56:59],a[168:171],v[108:111]
buffer_load_dwordx4 v231, s[8:11], s77 offen offset:3072 lds
MM v[108:111],a[52:55],a[140:143],v[108:111]
s_add_u32 s77, s64, 16384
s_add_u32 s81, s77, 896
s_add_u32 m0, s65, 81920
MM v[108:111],a[48:51],a[136:139],v[108:111]
s_waitcnt lgkmcnt(0)
MM v[16:19],a[64:67],a[144:147],v[16:19]
MM v[16:19],a[68:71],a[148:151],v[16:19]
MM v[16:19],a[72:75],a[176:179],v[16:19]
MM v[16:19],a[76:79],a[180:183],v[16:19]
v_pk_fma_f32 v[96:97], v[240:241], v[152:153], v[96:97] op_sel:[0,0,0] op_sel_hi:[0,1,1]
v_pk_fma_f32 v[98:99], v[240:241], v[154:155], v[98:99] op_sel:[0,0,0] op_sel_hi:[0,1,1]
v_pk_fma_f32 v[100:101], v[240:241], v[156:157], v[100:101] op_sel:[0,0,0] op_sel_hi:[0,1,1]
v_pk_fma_f32 v[102:103], v[240:241], v[158:159], v[102:103] op_sel:[0,0,0] op_sel_hi:[0,1,1]
v_pk_mul_f32 v[96:97], v[246:247], v[96:97] op_sel:[0,0] op_sel_hi:[0,1]
v_pk_mul_f32 v[98:99], v[246:247], v[98:99] op_sel:[0,0] op_sel_hi:[0,1]
v_pk_mul_f32 v[100:101], v[246:247], v[100:101] op_sel:[0,0] op_sel_hi:[0,1]
v_pk_mul_f32 v[102:103], v[246:247], v[102:103] op_sel:[0,0] op_sel_hi:[0,1]
s_nop 0
LPG4_%=:
s_cbranch_execz LPG8_%=
MM v[24:27],a[76:79],a[188:191],v[24:27]
CV v208,v96,v97
CV v209,v98,v99
MM v[24:27],a[72:75],a[184:187],v[24:27]
CV v210,v100,v101
CV v211,v102,v103
MM v[24:27],a[68:71],a[156:159],v[24:27]
s_waitcnt vmcnt(12)
s_barrier
RD a[192:195],v224 offset:0
MM v[24:27],a[64:67],a[152:155],v[24:27]
buffer_load_dwordx4 v235, s[12:15], s81 offen offset:0 lds
MM v[20:23],a[80:83],a[144:147],v[20:23]
RD a[196:199],v225 offset:0
RD a[200:203],v226 offset:0
MM v[20:23],a[84:87],a[148:151],v[20:23]
RD a[204:207],v227 offset:0
RD a[208:211],v224 offset:4096
MM v[20:23],a[88:91],a[176:179],v[20:23]
RD a[212:215],v225 offset:4096
RD a[216:219],v226 offset:4096
MM v[20:23],a[92:95],a[180:183],v[20:23]
RD a[220:223],v227 offset:4096
RD a[224:227],v224 offset:8192
MM v[28:31],a[92:95],a[188:191],v[28:31]
RD a[228:231],v225 offset:8192
RD a[232:235],v226 offset:8192
MM v[28:31],a[88:91],a[184:187],v[28:31]
RD a[236:239],v227 offset:8192
RD a[240:243],v224 offset:12288
MM v[28:31],a[84:87],a[156:159],v[28:31]
RD a[244:247],v225 offset:12288
RD a[248:251],v226 offset:12288
MM v[28:31],a[80:83],a[152:155],v[28:31]
RD a[252:255],v227 offset:12288
MM v[48:51],a[96:99],a[144:147],v[48:51]
v_pk_fma_f32 v[104:105], v[240:241], v[152:153], v[104:105] op_sel:[1,0,0] op_sel_hi:[1,1,1]
v_pk_fma_f32 v[106:107], v[240:241], v[154:155], v[106:107] op_sel:[1,0,0] op_sel_hi:[1,1,1]
v_pk_fma_f32 v[108:109], v[240:241], v[156:157], v[108:109] op_sel:[1,0,0] op_sel_hi:[1,1,1]
v_pk_fma_f32 v[110:111], v[240:241], v[158:159], v[110:111] op_sel:[1,0,0] op_sel_hi:[1,1,1]
v_pk_mul_f32 v[104:105], v[246:247], v[104:105] op_sel:[1,0] op_sel_hi:[1,1]
v_pk_mul_f32 v[106:107], v[246:247], v[106:107] op_sel:[1,0] op_sel_hi:[1,1]
v_pk_mul_f32 v[108:109], v[246:247], v[108:109] op_sel:[1,0] op_sel_hi:[1,1]
v_pk_mul_f32 v[110:111], v[246:247], v[110:111] op_sel:[1,0] op_sel_hi:[1,1]
MM v[48:51],a[100:103],a[148:151],v[48:51]
buffer_load_dwordx4 v235, s[12:15], s77 offen offset:1024 lds
MM v[48:51],a[104:107],a[176:179],v[48:51]
CV v212,v104,v105
CV v213,v106,v107
MM v[48:51],a[108:111],a[180:183],v[48:51]
CV v214,v108,v109
CV v215,v110,v111
MM v[56:59],a[108:111],a[188:191],v[56:59]
v_mov_b32 v247, 0
v_mov_b32 v236, 0
MM v[56:59],a[104:107],a[184:187],v[56:59]
v_mov_b32 v237, 0
v_cndmask_b32 v144, 0, v238, s[56:57]
MM v[56:59],a[100:103],a[156:159],v[56:59]
v_cndmask_b32 v145, 0, v238, s[56:57]
v_cndmask_b32 v146, 0, v238, s[56:57]
MM v[56:59],a[96:99],a[152:155],v[56:59]
v_cndmask_b32 v147, 0, v238, s[56:57]
v_cndmask_b32 v148, 0, v238, s[58:59]
MM v[52:55],a[112:115],a[144:147],v[52:55]
v_cndmask_b32 v149, 0, v238, s[58:59]
v_cndmask_b32 v150, 0, v238, s[58:59]
MM v[52:55],a[116:119],a[148:151],v[52:55]
v_pk_fma_f32 v[16:17], v[242:243], v[136:137], v[16:17] op_sel:[0,0,0] op_sel_hi:[0,1,1]
v_pk_fma_f32 v[18:19], v[242:243], v[138:139], v[18:19] op_sel:[0,0,0] op_sel_hi:[0,1,1]
v_pk_fma_f32 v[20:21], v[242:243], v[140:141], v[20:21] op_sel:[0,0,0] op_sel_hi:[0,1,1]
v_pk_fma_f32 v[22:23], v[242:243], v[142:143], v[22:23] op_sel:[0,0,0] op_sel_hi:[0,1,1]
v_pk_mul_f32 v[16:17], v[244:245], v[16:17] op_sel:[0,0] op_sel_hi:[0,1]
v_pk_mul_f32 v[18:19], v[244:245], v[18:19] op_sel:[0,0] op_sel_hi:[0,1]
v_pk_mul_f32 v[20:21], v[244:245], v[20:21] op_sel:[0,0] op_sel_hi:[0,1]
v_pk_mul_f32 v[22:23], v[244:245], v[22:23] op_sel:[0,0] op_sel_hi:[0,1]
MM v[52:55],a[120:123],a[176:179],v[52:55]
v_cndmask_b32 v151, 0, v238, s[58:59]
CV v168,v16,v17
)ASM"
R"ASM(
MM v[52:55],a[124:127],a[180:183],v[52:55]
buffer_load_dwordx4 v235, s[12:15], s81 offen offset:2048 lds
s_waitcnt lgkmcnt(0)
MM v[0:3],a[192:195],v[160:163],0
CV v169,v18,v19
CV v170,v20,v21
MM v[0:3],a[196:199],v[176:179],v[0:3]
CV v171,v22,v23
MM v[0:3],a[200:203],v[192:195],v[0:3]
v_pk_fma_f32 v[24:25], v[242:243], v[136:137], v[24:25] op_sel:[1,0,0] op_sel_hi:[1,1,1]
v_pk_fma_f32 v[26:27], v[242:243], v[138:139], v[26:27] op_sel:[1,0,0] op_sel_hi:[1,1,1]
v_pk_fma_f32 v[28:29], v[242:243], v[140:141], v[28:29] op_sel:[1,0,0] op_sel_hi:[1,1,1]
v_pk_fma_f32 v[30:31], v[242:243], v[142:143], v[30:31] op_sel:[1,0,0] op_sel_hi:[1,1,1]
v_pk_mul_f32 v[24:25], v[244:245], v[24:25] op_sel:[1,0] op_sel_hi:[1,1]
v_pk_mul_f32 v[26:27], v[244:245], v[26:27] op_sel:[1,0] op_sel_hi:[1,1]
v_pk_mul_f32 v[28:29], v[244:245], v[28:29] op_sel:[1,0] op_sel_hi:[1,1]
v_pk_mul_f32 v[30:31], v[244:245], v[30:31] op_sel:[1,0] op_sel_hi:[1,1]
MM v[0:3],a[204:207],v[208:211],v[0:3]
CV v172,v24,v25
CV v173,v26,v27
MM v[8:11],a[192:195],v[164:167],0
CV v174,v28,v29
CV v175,v30,v31
MM v[8:11],a[196:199],v[180:183],v[8:11]
v_xor_b32 v143, 128, v253
MM v[8:11],a[200:203],v[196:199],v[8:11]
RD v[136:139],v143 offset:0
RD v[140:143],v143 offset:16
MM v[8:11],a[204:207],v[212:215],v[8:11]
MM v[4:7],a[208:211],v[160:163],0
v_max3_f32 v152, v0, v1, v2
v_max_f32 v152, v152, v3
MM v[4:7],a[212:215],v[176:179],v[4:7]
buffer_load_dwordx4 v235, s[12:15], s77 offen offset:3072 lds
MM v[4:7],a[216:219],v[192:195],v[4:7]
s_add_u32 s77, s64, 32768
s_add_u32 s81, s77, 896
s_add_u32 m0, s65, 98304
v_mov_b32 v153, v152
MM v[4:7],a[220:223],v[208:211],v[4:7]
s_waitcnt lgkmcnt(0)
v_permlane16_swap_b32 v152, v153
v_max_f32 v152, v152, v153
MM v[12:15],a[208:211],v[164:167],0
s_waitcnt vmcnt(8)
s_barrier
RD a[192:195],v224 offset:16384
v_mov_b32 v153, v152
MM v[12:15],a[212:215],v[180:183],v[12:15]
RD a[196:199],v225 offset:16384
RD a[200:203],v226 offset:16384
v_permlane32_swap_b32 v152, v153
v_max_f32 v152, v152, v153
v_sub_f32 v128, 0, v152
MM v[12:15],a[216:219],v[196:199],v[12:15]
RD a[204:207],v227 offset:16384
v_sub_f32 v129, 0, v152
v_sub_f32 v130, 0, v152
MM v[12:15],a[220:223],v[212:215],v[12:15]
v_sub_f32 v131, 0, v152
v_max3_f32 v154, v8, v9, v10
MM v[32:35],a[224:227],v[160:163],0
RD a[208:211],v224 offset:20480
RD a[212:215],v225 offset:20480
v_max_f32 v154, v154, v11
v_mov_b32 v155, v154
MM v[32:35],a[228:231],v[176:179],v[32:35]
RD a[216:219],v226 offset:20480
RD a[220:223],v227 offset:20480
v_permlane16_swap_b32 v154, v155
v_max_f32 v154, v154, v155
MM v[32:35],a[232:235],v[192:195],v[32:35]
v_mov_b32 v155, v154
v_sub_f32 v0, v0, v152
MM v[32:35],a[236:239],v[208:211],v[32:35]
buffer_load_dwordx4 v235, s[12:15], s81 offen offset:0 lds
v_permlane32_swap_b32 v154, v155
v_max_f32 v154, v154, v155
v_sub_f32 v132, 0, v154
MM v[40:43],a[224:227],v[164:167],0
v_sub_f32 v133, 0, v154
v_sub_f32 v134, 0, v154
v_sub_f32 v135, 0, v154
v_sub_f32 v1, v1, v152
EX v0,v0
MM v[40:43],a[228:231],v[180:183],v[40:43]
EX v1,v1
MM v[40:43],a[232:235],v[196:199],v[40:43]
v_sub_f32 v2, v2, v152
v_sub_f32 v3, v3, v152
MM v[40:43],a[236:239],v[212:215],v[40:43]
EX v2,v2
MM v[36:39],a[240:243],v[160:163],v[128:131]
RD a[224:227],v224 offset:24576
RD a[228:231],v225 offset:24576
EX v3,v3
MM v[36:39],a[244:247],v[176:179],v[36:39]
RD a[232:235],v226 offset:24576
RD a[236:239],v227 offset:24576
CV v0,v0,v1
v_sub_f32 v4, v4, v152
MM v[36:39],a[248:251],v[192:195],v[36:39]
v_sub_f32 v5, v5, v152
EX v4,v4
MM v[36:39],a[252:255],v[208:211],v[36:39]
EX v5,v5
MM v[44:47],a[240:243],v[164:167],v[132:135]
CV v1,v2,v3
v_sub_f32 v6, v6, v152
MM v[44:47],a[244:247],v[180:183],v[44:47]
buffer_load_dwordx4 v235, s[12:15], s77 offen offset:1024 lds
v_sub_f32 v7, v7, v152
MM v[44:47],a[248:251],v[196:199],v[44:47]
EX v6,v6
EX v7,v7
MM v[44:47],a[252:255],v[212:215],v[44:47]
CV v2,v4,v5
v_sub_f32 v8, v8, v154
MM v[60:63],a[124:127],a[188:191],v[60:63]
RD a[240:243],v224 offset:28672
RD a[244:247],v225 offset:28672
v_sub_f32 v9, v9, v154
MM v[60:63],a[120:123],a[184:187],v[60:63]
RD a[248:251],v226 offset:28672
RD a[252:255],v227 offset:28672
EX v8,v8
MM v[60:63],a[116:119],a[156:159],v[60:63]
EX v9,v9
MM v[60:63],a[112:115],a[152:155],v[60:63]
CV v3,v6,v7
v_sub_f32 v10, v10, v154
MM v[80:83],a[0:3],a[144:147],v[80:83]
v_sub_f32 v11, v11, v154
EX v10,v10
MM v[80:83],a[4:7],a[148:151],v[80:83]
EX v11,v11
MM v[80:83],a[8:11],a[176:179],v[80:83]
CV v8,v8,v9
v_sub_f32 v12, v12, v154
MM v[80:83],a[12:15],a[180:183],v[80:83]
buffer_load_dwordx4 v235, s[12:15], s81 offen offset:2048 lds
v_sub_f32 v13, v13, v154
EX v12,v12
MM v[88:91],a[12:15],a[188:191],v[88:91]
EX v13,v13
MM v[88:91],a[8:11],a[184:187],v[88:91]
v_pk_fma_f32 v[48:49], v[242:243], v[136:137], v[48:49] op_sel:[0,0,0] op_sel_hi:[0,1,1]
v_pk_fma_f32 v[50:51], v[242:243], v[138:139], v[50:51] op_sel:[0,0,0] op_sel_hi:[0,1,1]
v_pk_fma_f32 v[52:53], v[242:243], v[140:141], v[52:53] op_sel:[0,0,0] op_sel_hi:[0,1,1]
v_pk_fma_f32 v[54:55], v[242:243], v[142:143], v[54:55] op_sel:[0,0,0] op_sel_hi:[0,1,1]
v_pk_mul_f32 v[48:49], v[244:245], v[48:49] op_sel:[0,0] op_sel_hi:[0,1]
v_pk_mul_f32 v[50:51], v[244:245], v[50:51] op_sel:[0,0] op_sel_hi:[0,1]
v_pk_mul_f32 v[52:53], v[244:245], v[52:53] op_sel:[0,0] op_sel_hi:[0,1]
v_pk_mul_f32 v[54:55], v[244:245], v[54:55] op_sel:[0,0] op_sel_hi:[0,1]
CV v9,v10,v11
MM v[88:91],a[4:7],a[156:159],v[88:91]
v_sub_f32 v14, v14, v154
v_sub_f32 v15, v15, v154
EX v14,v14
MM v[88:91],a[0:3],a[152:155],v[88:91]
EX v15,v15
MM v[84:87],a[16:19],a[144:147],v[84:87]
CV v10,v12,v13
v_sub_f32 v32, v32, v152
MM v[84:87],a[20:23],a[148:151],v[84:87]
v_sub_f32 v33, v33, v152
CV v184,v48,v49
MM v[84:87],a[24:27],a[176:179],v[84:87]
EX v32,v32
MM v[84:87],a[28:31],a[180:183],v[84:87]
EX v33,v33
MM v[92:95],a[28:31],a[188:191],v[92:95]
CV v11,v14,v15
v_sub_f32 v34, v34, v152
v_sub_f32 v35, v35, v152
MM v[92:95],a[24:27],a[184:187],v[92:95]
buffer_load_dwordx4 v235, s[12:15], s77 offen offset:3072 lds
EX v34,v34
MM v[92:95],a[20:23],a[156:159],v[92:95]
s_add_u32 s77, s64, 49152
s_add_u32 m0, s65, 49152
CV v185,v50,v51
EX v35,v35
MM v[92:95],a[16:19],a[152:155],v[92:95]
buffer_load_dwordx4 v228, s[8:11], s77 offen offset:0 lds
CV v4,v32,v33
EX v36,v36
MM v[112:115],a[32:35],a[144:147],v[112:115]
EX v37,v37
MM v[112:115],a[36:39],a[148:151],v[112:115]
CV v5,v34,v35
CV v186,v52,v53
MM v[112:115],a[40:43],a[176:179],v[112:115]
EX v38,v38
EX v39,v39
MM v[112:115],a[44:47],a[180:183],v[112:115]
buffer_load_dwordx4 v229, s[8:11], s77 offen offset:1024 lds
CV v6,v36,v37
MM v[120:123],a[44:47],a[188:191],v[120:123]
v_sub_f32 v40, v40, v154
v_sub_f32 v41, v41, v154
MM v[120:123],a[40:43],a[184:187],v[120:123]
EX v40,v40
MM v[120:123],a[36:39],a[156:159],v[120:123]
EX v41,v41
MM v[120:123],a[32:35],a[152:155],v[120:123]
buffer_load_dwordx4 v230, s[8:11], s77 offen offset:2048 lds
CV v7,v38,v39
v_sub_f32 v42, v42, v154
MM v[116:119],a[48:51],a[144:147],v[116:119]
v_sub_f32 v43, v43, v154
CV v187,v54,v55
EX v42,v42
MM v[116:119],a[52:55],a[148:151],v[116:119]
EX v43,v43
MM v[116:119],a[56:59],a[176:179],v[116:119]
v_pk_fma_f32 v[56:57], v[242:243], v[136:137], v[56:57] op_sel:[1,0,0] op_sel_hi:[1,1,1]
v_pk_fma_f32 v[58:59], v[242:243], v[138:139], v[58:59] op_sel:[1,0,0] op_sel_hi:[1,1,1]
v_pk_fma_f32 v[60:61], v[242:243], v[140:141], v[60:61] op_sel:[1,0,0] op_sel_hi:[1,1,1]
v_pk_fma_f32 v[62:63], v[242:243], v[142:143], v[62:63] op_sel:[1,0,0] op_sel_hi:[1,1,1]
v_pk_mul_f32 v[56:57], v[244:245], v[56:57] op_sel:[1,0] op_sel_hi:[1,1]
v_pk_mul_f32 v[58:59], v[244:245], v[58:59] op_sel:[1,0] op_sel_hi:[1,1]
v_pk_mul_f32 v[60:61], v[244:245], v[60:61] op_sel:[1,0] op_sel_hi:[1,1]
v_pk_mul_f32 v[62:63], v[244:245], v[62:63] op_sel:[1,0] op_sel_hi:[1,1]
CV v12,v40,v41
EX v44,v44
MM v[116:119],a[60:63],a[180:183],v[116:119]
buffer_load_dwordx4 v231, s[8:11], s77 offen offset:3072 lds
EX v45,v45
MM v[124:127],a[60:63],a[188:191],v[124:127]
s_add_u32 s77, s64, 49152
s_add_u32 s81, s77, 896
s_add_u32 m0, s65, 114688
CV v13,v42,v43
MM v[124:127],a[56:59],a[184:187],v[124:127]
EX v46,v46
MM v[124:127],a[52:55],a[156:159],v[124:127]
EX v47,v47
CV v14,v44,v45
CV v15,v46,v47
MM v[124:127],a[48:51],a[152:155],v[124:127]
buffer_load_dwordx4 v235, s[12:15], s81 offen offset:0 lds
v_smfmac_f32_16x16x64_bf16 v[236:239], v[144:147], v[0:7], v234
CV v188,v56,v57
v_smfmac_f32_16x16x64_bf16 v[236:239], v[148:151], v[8:15], v234
s_waitcnt lgkmcnt(0)
CV v189,v58,v59
buffer_load_dwordx4 v235, s[12:15], s77 offen offset:1024 lds
buffer_load_dwordx4 v235, s[12:15], s81 offen offset:2048 lds
buffer_load_dwordx4 v235, s[12:15], s77 offen offset:3072 lds
s_add_u32 s64, s64, 49152
MM v[64:67],a[192:195],v[160:163],v[128:131]
s_add_u32 s64, s64, 16384
TR a[128:129],v232 offset:0
CV v190,v60,v61
MM v[64:67],a[196:199],v[176:179],v[64:67]
s_add_u32 s81, s64, 896
TR a[130:131],v232 offset:4096
CV v191,v62,v63
MM v[64:67],a[200:203],v[192:195],v[64:67]
TR a[132:133],v232 offset:8192
TR a[134:135],v232 offset:12288
MM v[64:67],a[204:207],v[208:211],v[64:67]
v_add_f32 v246, v236, v237
v_xor_b32 v143, 256, v253
MM v[68:71],a[220:223],v[208:211],v[128:131]
TR a[136:137],v233 offset:0
v_cmp_nge_f32 vcc, s71, v246
MM v[68:71],a[216:219],v[192:195],v[68:71]
s_cbranch_vccnz LSITE0_%=
LBACK0_%=:
TR a[138:139],v233 offset:4096
RD v[136:139],v143 offset:0
MM v[68:71],a[212:215],v[176:179],v[68:71]
TR a[140:141],v233 offset:8192
RD v[140:143],v143 offset:16
MM v[68:71],a[208:211],v[160:163],v[68:71]
TR a[142:143],v233 offset:12288
TR a[144:145],v232 offset:512
MM v[76:79],a[208:211],v[164:167],v[132:135]
TR a[146:147],v232 offset:4608
TR a[148:149],v232 offset:8704
MM v[76:79],a[212:215],v[180:183],v[76:79]
TR a[150:151],v232 offset:12800
TR a[152:153],v233 offset:512
MM v[76:79],a[216:219],v[196:199],v[76:79]
s_waitcnt lgkmcnt(6)
v_pk_fma_f32 v[80:81], v[242:243], v[136:137], v[80:81] op_sel:[0,0,0] op_sel_hi:[0,1,1]
v_pk_fma_f32 v[82:83], v[242:243], v[138:139], v[82:83] op_sel:[0,0,0] op_sel_hi:[0,1,1]
v_pk_fma_f32 v[84:85], v[242:243], v[140:141], v[84:85] op_sel:[0,0,0] op_sel_hi:[0,1,1]
v_pk_fma_f32 v[86:87], v[242:243], v[142:143], v[86:87] op_sel:[0,0,0] op_sel_hi:[0,1,1]
v_pk_mul_f32 v[80:81], v[244:245], v[80:81] op_sel:[0,0] op_sel_hi:[0,1]
v_pk_mul_f32 v[82:83], v[244:245], v[82:83] op_sel:[0,0] op_sel_hi:[0,1]
v_pk_mul_f32 v[84:85], v[244:245], v[84:85] op_sel:[0,0] op_sel_hi:[0,1]
v_pk_mul_f32 v[86:87], v[244:245], v[86:87] op_sel:[0,0] op_sel_hi:[0,1]
MM v[76:79],a[220:223],v[212:215],v[76:79]
EX v64,v64
MM v[72:75],a[204:207],v[212:215],v[132:135]
EX v65,v65
MM v[72:75],a[200:203],v[196:199],v[72:75]
TR a[154:155],v233 offset:4608
TR a[156:157],v233 offset:8704
MM v[72:75],a[196:199],v[180:183],v[72:75]
TR a[158:159],v233 offset:12800
TR a[160:161],v232 offset:1024
MM v[72:75],a[192:195],v[164:167],v[72:75]
EX v66,v66
MM v[96:99],a[224:227],v[160:163],v[128:131]
EX v67,v67
MM v[96:99],a[228:231],v[176:179],v[96:99]
TR a[162:163],v232 offset:5120
TR a[164:165],v232 offset:9216
MM v[96:99],a[232:235],v[192:195],v[96:99]
TR a[166:167],v232 offset:13312
CV v200,v80,v81
EX v68,v68
MM v[96:99],a[236:239],v[208:211],v[96:99]
TR a[168:169],v233 offset:1024
CV v201,v82,v83
MM v[100:103],a[252:255],v[208:211],v[128:131]
TR a[170:171],v233 offset:5120
EX v69,v69
MM v[100:103],a[248:251],v[192:195],v[100:103]
TR a[172:173],v233 offset:9216
TR a[174:175],v233 offset:13312
CV v202,v84,v85
MM v[100:103],a[244:247],v[176:179],v[100:103]
TR a[176:177],v232 offset:1536
EX v70,v70
CV v203,v86,v87
MM v[100:103],a[240:243],v[160:163],v[100:103]
TR a[178:179],v232 offset:5632
v_pk_fma_f32 v[88:89], v[242:243], v[136:137], v[88:89] op_sel:[1,0,0] op_sel_hi:[1,1,1]
v_pk_fma_f32 v[90:91], v[242:243], v[138:139], v[90:91] op_sel:[1,0,0] op_sel_hi:[1,1,1]
v_pk_fma_f32 v[92:93], v[242:243], v[140:141], v[92:93] op_sel:[1,0,0] op_sel_hi:[1,1,1]
v_pk_fma_f32 v[94:95], v[242:243], v[142:143], v[94:95] op_sel:[1,0,0] op_sel_hi:[1,1,1]
v_pk_mul_f32 v[88:89], v[244:245], v[88:89] op_sel:[1,0] op_sel_hi:[1,1]
)ASM"
R"ASM(
v_pk_mul_f32 v[90:91], v[244:245], v[90:91] op_sel:[1,0] op_sel_hi:[1,1]
v_pk_mul_f32 v[92:93], v[244:245], v[92:93] op_sel:[1,0] op_sel_hi:[1,1]
v_pk_mul_f32 v[94:95], v[244:245], v[94:95] op_sel:[1,0] op_sel_hi:[1,1]
EX v71,v71
MM v[108:111],a[240:243],v[164:167],v[132:135]
TR a[180:181],v232 offset:9728
CV v64,v64,v65
MM v[108:111],a[244:247],v[180:183],v[108:111]
TR a[182:183],v232 offset:13824
TR a[184:185],v233 offset:1536
EX v72,v72
MM v[108:111],a[248:251],v[196:199],v[108:111]
TR a[186:187],v233 offset:5632
TR a[188:189],v233 offset:9728
TR a[190:191],v233 offset:13824
EX v73,v73
MM v[108:111],a[252:255],v[212:215],v[108:111]
CV v65,v66,v67
CV v66,v68,v69
MM v[104:107],a[236:239],v[212:215],v[132:135]
EX v74,v74
MM v[104:107],a[232:235],v[196:199],v[104:107]
EX v75,v75
MM v[104:107],a[228:231],v[180:183],v[104:107]
CV v67,v70,v71
CV v204,v88,v89
MM v[104:107],a[224:227],v[164:167],v[104:107]
s_waitcnt vmcnt(12) lgkmcnt(0)
s_barrier
CV v205,v90,v91
EX v76,v76
MM a[0:3],a[128:131],v[0:3],0
s_add_u32 m0, s65, 0
EX v77,v77
MM a[0:3],a[132:135],v[4:7],a[0:3]
CV v206,v92,v93
CV v72,v72,v73
MM a[8:11],a[132:135],v[12:15],0
CV v207,v94,v95
v_xor_b32 v143, 384, v253
MM a[8:11],a[128:131],v[8:11],a[8:11]
EX v78,v78
MM a[12:15],a[136:139],v[8:11],0
EX v79,v79
MM a[12:15],a[140:143],v[12:15],a[12:15]
RD v[136:139],v143 offset:0
RD v[140:143],v143 offset:16
MM a[4:7],a[140:143],v[4:7],0
EX v96,v96
MM a[4:7],a[136:139],v[0:3],a[4:7]
RD a[192:195],v224 offset:32768
CV v73,v74,v75
MM a[32:35],a[144:147],v[0:3],0
EX v97,v97
MM a[32:35],a[148:151],v[4:7],a[32:35]
s_waitcnt lgkmcnt(1)
buffer_load_dwordx4 v228, s[8:11], s64 offen offset:0 lds
v_pk_fma_f32 v[112:113], v[242:243], v[136:137], v[112:113] op_sel:[0,0,0] op_sel_hi:[0,1,1]
v_pk_fma_f32 v[114:115], v[242:243], v[138:139], v[114:115] op_sel:[0,0,0] op_sel_hi:[0,1,1]
v_pk_fma_f32 v[116:117], v[242:243], v[140:141], v[116:117] op_sel:[0,0,0] op_sel_hi:[0,1,1]
v_pk_fma_f32 v[118:119], v[242:243], v[142:143], v[118:119] op_sel:[0,0,0] op_sel_hi:[0,1,1]
v_pk_mul_f32 v[112:113], v[244:245], v[112:113] op_sel:[0,0] op_sel_hi:[0,1]
v_pk_mul_f32 v[114:115], v[244:245], v[114:115] op_sel:[0,0] op_sel_hi:[0,1]
v_pk_mul_f32 v[116:117], v[244:245], v[116:117] op_sel:[0,0] op_sel_hi:[0,1]
v_pk_mul_f32 v[118:119], v[244:245], v[118:119] op_sel:[0,0] op_sel_hi:[0,1]
EX v98,v98
MM a[40:43],a[148:151],v[12:15],0
RD a[196:199],v225 offset:32768
CV v74,v76,v77
MM a[40:43],a[144:147],v[8:11],a[40:43]
RD a[200:203],v226 offset:32768
EX v99,v99
MM a[44:47],a[152:155],v[8:11],0
buffer_load_dwordx4 v229, s[8:11], s64 offen offset:1024 lds
MM a[44:47],a[156:159],v[12:15],a[44:47]
RD a[204:207],v227 offset:32768
EX v100,v100
MM a[36:39],a[156:159],v[4:7],0
EX v101,v101
CV v75,v78,v79
MM a[36:39],a[152:155],v[0:3],a[36:39]
RD a[208:211],v224 offset:36864
CV v216,v112,v113
MM a[64:67],a[160:163],v[0:3],0
EX v102,v102
MM a[64:67],a[164:167],v[4:7],a[64:67]
buffer_load_dwordx4 v230, s[8:11], s64 offen offset:2048 lds
RD a[212:215],v225 offset:36864
CV v217,v114,v115
MM a[72:75],a[164:167],v[12:15],0
EX v103,v103
CV v68,v96,v97
CV v218,v116,v117
MM a[72:75],a[160:163],v[8:11],a[72:75]
RD a[216:219],v226 offset:36864
CV v219,v118,v119
EX v104,v104
MM a[76:79],a[168:171],v[8:11],0
buffer_load_dwordx4 v231, s[8:11], s64 offen offset:3072 lds
RD a[220:223],v227 offset:36864
MM a[76:79],a[172:175],v[12:15],a[76:79]
s_add_u32 m0, s65, 65536
v_pk_fma_f32 v[120:121], v[242:243], v[136:137], v[120:121] op_sel:[1,0,0] op_sel_hi:[1,1,1]
v_pk_fma_f32 v[122:123], v[242:243], v[138:139], v[122:123] op_sel:[1,0,0] op_sel_hi:[1,1,1]
v_pk_fma_f32 v[124:125], v[242:243], v[140:141], v[124:125] op_sel:[1,0,0] op_sel_hi:[1,1,1]
v_pk_fma_f32 v[126:127], v[242:243], v[142:143], v[126:127] op_sel:[1,0,0] op_sel_hi:[1,1,1]
v_pk_mul_f32 v[120:121], v[244:245], v[120:121] op_sel:[1,0] op_sel_hi:[1,1]
v_pk_mul_f32 v[122:123], v[244:245], v[122:123] op_sel:[1,0] op_sel_hi:[1,1]
v_pk_mul_f32 v[124:125], v[244:245], v[124:125] op_sel:[1,0] op_sel_hi:[1,1]
v_pk_mul_f32 v[126:127], v[244:245], v[126:127] op_sel:[1,0] op_sel_hi:[1,1]
EX v105,v105
CV v69,v98,v99
MM a[68:71],a[172:175],v[4:7],0
RD a[224:227],v224 offset:40960
CV v70,v100,v101
MM a[68:71],a[168:171],v[0:3],a[68:71]
EX v106,v106
MM a[96:99],a[176:179],v[0:3],0
RD a[228:231],v225 offset:40960
buffer_load_dwordx4 v235, s[12:15], s81 offen offset:0 lds
EX v107,v107
MM a[96:99],a[180:183],v[4:7],a[96:99]
RD a[232:235],v226 offset:40960
CV v71,v102,v103
MM a[104:107],a[180:183],v[12:15],0
EX v108,v108
MM a[104:107],a[176:179],v[8:11],a[104:107]
EX v109,v109
MM a[108:111],a[184:187],v[8:11],0
RD a[236:239],v227 offset:40960
RD a[240:243],v224 offset:45056
buffer_load_dwordx4 v235, s[12:15], s64 offen offset:1024 lds
MM a[108:111],a[188:191],v[12:15],a[108:111]
EX v110,v110
CV v220,v120,v121
MM a[100:103],a[188:191],v[4:7],0
RD a[244:247],v225 offset:45056
RD a[248:251],v226 offset:45056
RD a[252:255],v227 offset:45056
CV v221,v122,v123
EX v111,v111
CV v222,v124,v125
CV v223,v126,v127
MM a[100:103],a[184:187],v[0:3],a[100:103]
buffer_load_dwordx4 v235, s[12:15], s81 offen offset:2048 lds
CV v76,v104,v105
v_smfmac_f32_16x16x64_bf16 v[236:239], v[144:147], v[64:71], v234
CV v77,v106,v107
CV v78,v108,v109
CV v79,v110,v111
s_nop 1
v_smfmac_f32_16x16x64_bf16 v[236:239], v[148:151], v[72:79], v234
s_waitcnt lgkmcnt(0)
buffer_load_dwordx4 v235, s[12:15], s64 offen offset:3072 lds
MM v[0:3],a[192:195],v[160:163],v[128:131]
s_add_u32 s64, s64, 16384
TR a[128:129],v232 offset:16384
TR a[130:131],v232 offset:20480
MM v[0:3],a[196:199],v[176:179],v[0:3]
s_add_u32 s81, s64, 896
TR a[132:133],v232 offset:24576
s_nop 0
LPG5_%=:
s_cbranch_execz LPG9_%=
TR a[134:135],v232 offset:28672
MM v[0:3],a[200:203],v[192:195],v[0:3]
TR a[136:137],v233 offset:16384
TR a[138:139],v233 offset:20480
MM v[0:3],a[204:207],v[208:211],v[0:3]
TR a[140:141],v233 offset:24576
v_add_f32 v247, v236, v237
MM v[4:7],a[220:223],v[208:211],v[128:131]
TR a[142:143],v233 offset:28672
v_cmp_nge_f32 vcc, s71, v247
MM v[4:7],a[216:219],v[192:195],v[4:7]
s_cbranch_vccnz LSITE1_%=
LBACK1_%=:
TR a[144:145],v232 offset:16896
TR a[146:147],v232 offset:20992
MM v[4:7],a[212:215],v[176:179],v[4:7]
s_cmp_lg_u32 %[sb], 0
s_cbranch_scc1 LKA_%=
s_lshl_b32 s90, %[swave], 10
s_add_u32 s90, s90, 81920
buffer_load_dwordx4 v[48:51], v248, s[20:23], s90 offen offset:0 sc1
LKA_%=:
MM v[4:7],a[208:211],v[160:163],v[4:7]
TR a[148:149],v232 offset:25088
TR a[150:151],v232 offset:29184
MM v[12:15],a[208:211],v[164:167],v[132:135]
EX v0,v0
MM v[12:15],a[212:215],v[180:183],v[12:15]
EX v1,v1
MM v[12:15],a[216:219],v[196:199],v[12:15]
TR a[152:153],v233 offset:16896
CV v0,v0,v1
MM v[12:15],a[220:223],v[212:215],v[12:15]
EX v2,v2
MM v[8:11],a[204:207],v[212:215],v[132:135]
TR a[154:155],v233 offset:20992
TR a[156:157],v233 offset:25088
MM v[8:11],a[200:203],v[196:199],v[8:11]
EX v3,v3
MM v[8:11],a[196:199],v[180:183],v[8:11]
TR a[158:159],v233 offset:29184
CV v1,v2,v3
MM v[8:11],a[192:195],v[164:167],v[8:11]
EX v4,v4
MM v[32:35],a[224:227],v[160:163],v[128:131]
TR a[160:161],v232 offset:17408
TR a[162:163],v232 offset:21504
MM v[32:35],a[228:231],v[176:179],v[32:35]
EX v5,v5
MM v[32:35],a[232:235],v[192:195],v[32:35]
TR a[164:165],v232 offset:25600
CV v2,v4,v5
MM v[32:35],a[236:239],v[208:211],v[32:35]
TR a[166:167],v232 offset:29696
TR a[168:169],v233 offset:17408
MM v[36:39],a[252:255],v[208:211],v[128:131]
EX v6,v6
MM v[36:39],a[248:251],v[192:195],v[36:39]
EX v7,v7
MM v[36:39],a[244:247],v[176:179],v[36:39]
TR a[170:171],v233 offset:21504
CV v3,v6,v7
MM v[36:39],a[240:243],v[160:163],v[36:39]
TR a[172:173],v233 offset:25600
TR a[174:175],v233 offset:29696
MM v[44:47],a[240:243],v[164:167],v[132:135]
EX v8,v8
MM v[44:47],a[244:247],v[180:183],v[44:47]
TR a[176:177],v232 offset:17920
TR a[178:179],v232 offset:22016
MM v[44:47],a[248:251],v[196:199],v[44:47]
EX v9,v9
MM v[44:47],a[252:255],v[212:215],v[44:47]
TR a[180:181],v232 offset:26112
CV v8,v8,v9
MM v[40:43],a[236:239],v[212:215],v[132:135]
EX v10,v10
MM v[40:43],a[232:235],v[196:199],v[40:43]
TR a[182:183],v232 offset:30208
TR a[184:185],v233 offset:17920
MM v[40:43],a[228:231],v[180:183],v[40:43]
EX v11,v11
MM v[40:43],a[224:227],v[164:167],v[40:43]
TR a[186:187],v233 offset:22016
CV v9,v10,v11
MM v[16:19],a[192:195],v[168:171],0
EX v12,v12
MM v[16:19],a[196:199],v[184:187],v[16:19]
TR a[188:189],v233 offset:26112
TR a[190:191],v233 offset:30208
MM v[16:19],a[200:203],v[200:203],v[16:19]
EX v13,v13
MM v[16:19],a[204:207],v[216:219],v[16:19]
EX v14,v14
MM v[24:27],a[192:195],v[172:175],0
EX v15,v15
MM v[24:27],a[196:199],v[188:191],v[24:27]
EX v32,v32
MM v[24:27],a[200:203],v[204:207],v[24:27]
EX v33,v33
MM v[24:27],a[204:207],v[220:223],v[24:27]
s_waitcnt vmcnt(12) lgkmcnt(0)
s_barrier
CV v10,v12,v13
MM a[0:3],a[128:131],v[64:67],a[0:3]
s_add_u32 m0, s65, 16384
RD a[192:195],v224 offset:49152
CV v11,v14,v15
MM a[0:3],a[132:135],v[68:71],a[0:3]
CV v4,v32,v33
v_max3_f32 v154, v16, v17, v18
MM a[8:11],a[132:135],v[76:79],a[8:11]
buffer_load_dwordx4 v228, s[8:11], s64 offen offset:0 lds
MM a[8:11],a[128:131],v[72:75],a[8:11]
RD a[196:199],v225 offset:49152
v_max_f32 v154, v154, v19
MM a[12:15],a[136:139],v[72:75],a[12:15]
EX v34,v34
MM a[12:15],a[140:143],v[76:79],a[12:15]
RD a[200:203],v226 offset:49152
v_mov_b32 v155, v154
MM a[4:7],a[140:143],v[68:71],a[4:7]
s_nop 0
v_permlane16_swap_b32 v154, v155
v_max_f32 v154, v154, v155
MM a[4:7],a[136:139],v[64:67],a[4:7]
EX v35,v35
MM a[32:35],a[144:147],v[64:67],a[32:35]
RD a[204:207],v227 offset:49152
v_mov_b32 v155, v154
MM a[32:35],a[148:151],v[68:71],a[32:35]
s_nop 0
v_permlane32_swap_b32 v154, v155
v_max_f32 v154, v154, v155
MM a[40:43],a[148:151],v[76:79],a[40:43]
EX v36,v36
MM a[40:43],a[144:147],v[72:75],a[40:43]
v_sub_f32 v136, 0, v154
v_sub_f32 v137, 0, v154
MM a[44:47],a[152:155],v[72:75],a[44:47]
EX v37,v37
MM a[44:47],a[156:159],v[76:79],a[44:47]
buffer_load_dwordx4 v229, s[8:11], s64 offen offset:1024 lds
v_sub_f32 v138, 0, v154
v_sub_f32 v139, 0, v154
MM a[36:39],a[156:159],v[68:71],a[36:39]
EX v38,v38
MM a[36:39],a[152:155],v[64:67],a[36:39]
RD a[208:211],v224 offset:53248
v_mov_b32_dpp v152, v144 row_ror:8 row_mask:0xf bank_mask:0xf
v_mov_b32_dpp v153, v145 row_ror:8 row_mask:0xf bank_mask:0xf
MM a[64:67],a[160:163],v[64:67],a[64:67]
EX v39,v39
v_mov_b32_dpp v154, v146 row_ror:8 row_mask:0xf bank_mask:0xf
MM a[64:67],a[164:167],v[68:71],a[64:67]
RD a[212:215],v225 offset:53248
buffer_load_dwordx4 v230, s[8:11], s64 offen offset:2048 lds
v_mov_b32_dpp v155, v147 row_ror:8 row_mask:0xf bank_mask:0xf
MM a[72:75],a[164:167],v[76:79],a[72:75]
RD a[216:219],v226 offset:53248
v_max3_f32 v158, v24, v25, v26
EX v40,v40
MM a[72:75],a[160:163],v[72:75],a[72:75]
v_max_f32 v158, v158, v27
v_mov_b32 v159, v158
CV v5,v34,v35
MM a[76:79],a[168:171],v[72:75],a[76:79]
buffer_load_dwordx4 v231, s[8:11], s64 offen offset:3072 lds
RD a[220:223],v227 offset:53248
EX v41,v41
v_permlane16_swap_b32 v158, v159
MM a[76:79],a[172:175],v[76:79],a[76:79]
s_add_u32 m0, s65, 81920
v_max_f32 v158, v158, v159
CV v6,v36,v37
MM a[68:71],a[172:175],v[68:71],a[68:71]
RD a[224:227],v224 offset:57344
EX v42,v42
v_mov_b32 v159, v158
MM a[68:71],a[168:171],v[64:67],a[68:71]
s_nop 0
v_permlane32_swap_b32 v158, v159
v_max_f32 v158, v158, v159
EX v43,v43
MM a[96:99],a[176:179],v[64:67],a[96:99]
RD a[228:231],v225 offset:57344
buffer_load_dwordx4 v235, s[12:15], s81 offen offset:0 lds
v_sub_f32 v140, 0, v158
MM a[96:99],a[180:183],v[68:71],a[96:99]
v_sub_f32 v141, 0, v158
CV v7,v38,v39
EX v44,v44
MM a[104:107],a[180:183],v[76:79],a[104:107]
RD a[232:235],v226 offset:57344
v_sub_f32 v142, 0, v158
MM a[104:107],a[176:179],v[72:75],a[104:107]
v_sub_f32 v143, 0, v158
v_mov_b32_dpp v156, v148 row_ror:8 row_mask:0xf bank_mask:0xf
EX v45,v45
MM a[108:111],a[184:187],v[72:75],a[108:111]
RD a[236:239],v227 offset:57344
buffer_load_dwordx4 v235, s[12:15], s64 offen offset:1024 lds
v_mov_b32_dpp v157, v149 row_ror:8 row_mask:0xf bank_mask:0xf
v_mov_b32_dpp v158, v150 row_ror:8 row_mask:0xf bank_mask:0xf
)ASM"
R"ASM(
v_mov_b32_dpp v159, v151 row_ror:8 row_mask:0xf bank_mask:0xf
MM a[108:111],a[188:191],v[76:79],a[108:111]
EX v46,v46
MM a[100:103],a[188:191],v[68:71],a[100:103]
RD a[240:243],v224 offset:61440
RD a[244:247],v225 offset:61440
RD a[248:251],v226 offset:61440
RD a[252:255],v227 offset:61440
EX v47,v47
MM a[100:103],a[184:187],v[64:67],a[100:103]
buffer_load_dwordx4 v235, s[12:15], s81 offen offset:2048 lds
CV v12,v40,v41
v_smfmac_f32_16x16x64_bf16 v[236:239], v[144:147], v[0:7], v234
CV v13,v42,v43
CV v14,v44,v45
CV v15,v46,v47
s_nop 1
v_smfmac_f32_16x16x64_bf16 v[236:239], v[148:151], v[8:15], v234
s_waitcnt lgkmcnt(0)
buffer_load_dwordx4 v235, s[12:15], s64 offen offset:3072 lds
MM v[64:67],a[192:195],v[160:163],v[128:131]
s_add_u32 s64, s64, 16384
TR a[128:129],v232 offset:32768
s_add_u32 s81, s64, 896
MM v[64:67],a[196:199],v[176:179],v[64:67]
TR a[130:131],v232 offset:36864
TR a[132:133],v232 offset:40960
MM v[64:67],a[200:203],v[192:195],v[64:67]
TR a[134:135],v232 offset:45056
TR a[136:137],v233 offset:32768
MM v[64:67],a[204:207],v[208:211],v[64:67]
TR a[138:139],v233 offset:36864
v_add_f32 v246, v236, v237
MM v[68:71],a[220:223],v[208:211],v[128:131]
TR a[140:141],v233 offset:40960
v_cmp_nge_f32 vcc, s71, v246
MM v[68:71],a[216:219],v[192:195],v[68:71]
s_cbranch_vccnz LSITE2_%=
LBACK2_%=:
TR a[142:143],v233 offset:45056
TR a[144:145],v232 offset:33280
MM v[68:71],a[212:215],v[176:179],v[68:71]
TR a[146:147],v232 offset:37376
TR a[148:149],v232 offset:41472
MM v[68:71],a[208:211],v[160:163],v[68:71]
TR a[150:151],v232 offset:45568
TR a[152:153],v233 offset:33280
MM v[76:79],a[208:211],v[164:167],v[132:135]
EX v64,v64
MM v[76:79],a[212:215],v[180:183],v[76:79]
EX v65,v65
MM v[76:79],a[216:219],v[196:199],v[76:79]
TR a[154:155],v233 offset:37376
CV v64,v64,v65
MM v[76:79],a[220:223],v[212:215],v[76:79]
EX v66,v66
MM v[72:75],a[204:207],v[212:215],v[132:135]
EX v67,v67
MM v[72:75],a[200:203],v[196:199],v[72:75]
TR a[156:157],v233 offset:41472
CV v65,v66,v67
MM v[72:75],a[196:199],v[180:183],v[72:75]
EX v68,v68
MM v[72:75],a[192:195],v[164:167],v[72:75]
EX v69,v69
MM v[80:83],a[192:195],v[168:171],v[136:139]
TR a[158:159],v233 offset:45568
CV v66,v68,v69
MM v[80:83],a[196:199],v[184:187],v[80:83]
EX v70,v70
MM v[80:83],a[200:203],v[200:203],v[80:83]
EX v71,v71
MM v[80:83],a[204:207],v[216:219],v[80:83]
TR a[160:161],v232 offset:33792
CV v67,v70,v71
MM v[84:87],a[220:223],v[216:219],v[136:139]
s_cmp_lg_u32 %[sb], 0
s_cbranch_scc1 LKB_%=
s_waitcnt vmcnt(8)
v_xor_b32 v48, s69, v48
v_xor_b32 v49, s69, v49
v_xor_b32 v50, s69, v50
v_xor_b32 v51, s69, v51
v_or3_b32 v52, v48, v49, v50
v_or_b32 v52, v52, v51
v_cmp_ne_u32 vcc, 0, v52
s_cbranch_vccnz LRFR_%=
LRFRb_%=:
s_lshl_b32 s90, %[swave], 11
s_add_u32 s78, s90, %[slds]
s_add_u32 s78, s78, 155648
s_add_u32 s90, s90, 53248
s_mov_b32 m0, s78
s_nop 0
buffer_load_dwordx4 v248, s[20:23], s90 offen offset:0 sc1 lds
buffer_load_dwordx4 v248, s[20:23], s90 offen offset:1024 sc1 lds
LKB_%=:
MM v[84:87],a[216:219],v[200:203],v[84:87]
EX v72,v72
MM v[84:87],a[212:215],v[184:187],v[84:87]
EX v73,v73
MM v[84:87],a[208:211],v[168:171],v[84:87]
EX v74,v74
MM v[92:95],a[208:211],v[172:175],v[140:143]
EX v75,v75
MM v[92:95],a[212:215],v[188:191],v[92:95]
CV v72,v72,v73
CV v73,v74,v75
MM v[92:95],a[216:219],v[204:207],v[92:95]
EX v76,v76
MM v[92:95],a[220:223],v[220:223],v[92:95]
EX v77,v77
MM v[88:91],a[204:207],v[220:223],v[140:143]
EX v78,v78
MM v[88:91],a[200:203],v[204:207],v[88:91]
EX v79,v79
MM v[88:91],a[196:199],v[188:191],v[88:91]
CV v74,v76,v77
CV v75,v78,v79
MM v[88:91],a[192:195],v[172:175],v[88:91]
EX v80,v80
MM v[96:99],a[224:227],v[160:163],v[128:131]
EX v81,v81
MM v[96:99],a[228:231],v[176:179],v[96:99]
TR a[162:163],v232 offset:37888
TR a[164:165],v232 offset:41984
MM v[96:99],a[232:235],v[192:195],v[96:99]
EX v82,v82
MM v[96:99],a[236:239],v[208:211],v[96:99]
EX v83,v83
MM v[100:103],a[252:255],v[208:211],v[128:131]
TR a[166:167],v232 offset:46080
CV v80,v80,v81
MM v[100:103],a[248:251],v[192:195],v[100:103]
EX v84,v84
MM v[100:103],a[244:247],v[176:179],v[100:103]
EX v85,v85
MM v[100:103],a[240:243],v[160:163],v[100:103]
TR a[168:169],v233 offset:33792
CV v81,v82,v83
MM v[108:111],a[240:243],v[164:167],v[132:135]
EX v86,v86
MM v[108:111],a[244:247],v[180:183],v[108:111]
EX v87,v87
MM v[108:111],a[248:251],v[196:199],v[108:111]
TR a[170:171],v233 offset:37888
CV v82,v84,v85
MM v[108:111],a[252:255],v[212:215],v[108:111]
EX v88,v88
MM v[104:107],a[236:239],v[212:215],v[132:135]
EX v89,v89
MM v[104:107],a[232:235],v[196:199],v[104:107]
TR a[172:173],v233 offset:41984
TR a[174:175],v233 offset:46080
MM v[104:107],a[228:231],v[180:183],v[104:107]
EX v90,v90
MM v[104:107],a[224:227],v[164:167],v[104:107]
EX v91,v91
MM v[112:115],a[224:227],v[168:171],v[136:139]
TR a[176:177],v232 offset:34304
CV v83,v86,v87
MM v[112:115],a[228:231],v[184:187],v[112:115]
EX v92,v92
MM v[112:115],a[232:235],v[200:203],v[112:115]
EX v93,v93
MM v[112:115],a[236:239],v[216:219],v[112:115]
TR a[178:179],v232 offset:38400
CV v88,v88,v89
MM v[116:119],a[252:255],v[216:219],v[136:139]
EX v94,v94
MM v[116:119],a[248:251],v[200:203],v[116:119]
EX v95,v95
MM v[116:119],a[244:247],v[184:187],v[116:119]
TR a[180:181],v232 offset:42496
TR a[182:183],v232 offset:46592
MM v[116:119],a[240:243],v[168:171],v[116:119]
EX v96,v96
MM v[124:127],a[240:243],v[172:175],v[140:143]
EX v97,v97
CV v89,v90,v91
MM v[124:127],a[244:247],v[188:191],v[124:127]
EX v98,v98
MM v[124:127],a[248:251],v[204:207],v[124:127]
TR a[184:185],v233 offset:34304
TR a[186:187],v233 offset:38400
TR a[188:189],v233 offset:42496
TR a[190:191],v233 offset:46592
MM v[124:127],a[252:255],v[220:223],v[124:127]
EX v99,v99
CV v90,v92,v93
MM v[120:123],a[236:239],v[220:223],v[140:143]
EX v100,v100
MM v[120:123],a[232:235],v[204:207],v[120:123]
EX v101,v101
CV v91,v94,v95
MM v[120:123],a[228:231],v[188:191],v[120:123]
EX v102,v102
MM v[120:123],a[224:227],v[172:175],v[120:123]
s_waitcnt vmcnt(12) lgkmcnt(0)
s_barrier
CV v68,v96,v97
MM a[0:3],a[128:131],v[0:3],a[0:3]
s_add_u32 m0, s65, 32768
EX v103,v103
MM a[0:3],a[132:135],v[4:7],a[0:3]
EX v104,v104
MM a[8:11],a[132:135],v[12:15],a[8:11]
CV v69,v98,v99
CV v70,v100,v101
MM a[8:11],a[128:131],v[8:11],a[8:11]
EX v105,v105
MM a[12:15],a[136:139],v[8:11],a[12:15]
EX v106,v106
MM a[12:15],a[140:143],v[12:15],a[12:15]
EX v107,v107
MM a[4:7],a[140:143],v[4:7],a[4:7]
EX v108,v108
MM a[4:7],a[136:139],v[0:3],a[4:7]
RD a[192:195],v224 offset:0
CV v71,v102,v103
MM a[32:35],a[144:147],v[0:3],a[32:35]
EX v109,v109
MM a[32:35],a[148:151],v[4:7],a[32:35]
buffer_load_dwordx4 v228, s[8:11], s64 offen offset:0 lds
MM a[40:43],a[148:151],v[12:15],a[40:43]
RD a[196:199],v225 offset:0
EX v110,v110
MM a[40:43],a[144:147],v[8:11],a[40:43]
EX v111,v111
CV v76,v104,v105
MM a[44:47],a[152:155],v[8:11],a[44:47]
RD a[200:203],v226 offset:0
EX v112,v112
MM a[44:47],a[156:159],v[12:15],a[44:47]
buffer_load_dwordx4 v229, s[8:11], s64 offen offset:1024 lds
MM a[36:39],a[156:159],v[4:7],a[36:39]
RD a[204:207],v227 offset:0
EX v113,v113
CV v77,v106,v107
MM a[36:39],a[152:155],v[0:3],a[36:39]
EX v114,v114
MM a[64:67],a[160:163],v[0:3],a[64:67]
RD a[208:211],v224 offset:4096
EX v115,v115
CV v78,v108,v109
MM a[64:67],a[164:167],v[4:7],a[64:67]
buffer_load_dwordx4 v230, s[8:11], s64 offen offset:2048 lds
MM a[72:75],a[164:167],v[12:15],a[72:75]
RD a[212:215],v225 offset:4096
EX v116,v116
MM a[72:75],a[160:163],v[8:11],a[72:75]
EX v117,v117
CV v79,v110,v111
MM a[76:79],a[168:171],v[8:11],a[76:79]
RD a[216:219],v226 offset:4096
CV v84,v112,v113
EX v118,v118
MM a[76:79],a[172:175],v[12:15],a[76:79]
buffer_load_dwordx4 v231, s[8:11], s64 offen offset:3072 lds
MM a[68:71],a[172:175],v[4:7],a[68:71]
s_add_u32 m0, s65, 98304
RD a[220:223],v227 offset:4096
EX v119,v119
MM a[68:71],a[168:171],v[0:3],a[68:71]
EX v120,v120
MM a[96:99],a[176:179],v[0:3],a[96:99]
RD a[224:227],v224 offset:8192
CV v85,v114,v115
EX v121,v121
MM a[96:99],a[180:183],v[4:7],a[96:99]
RD a[228:231],v225 offset:8192
CV v86,v116,v117
MM a[104:107],a[180:183],v[12:15],a[104:107]
buffer_load_dwordx4 v235, s[12:15], s81 offen offset:0 lds
EX v122,v122
MM a[104:107],a[176:179],v[8:11],a[104:107]
EX v123,v123
MM a[108:111],a[184:187],v[8:11],a[108:111]
RD a[232:235],v226 offset:8192
CV v87,v118,v119
MM a[108:111],a[188:191],v[12:15],a[108:111]
EX v124,v124
MM a[100:103],a[188:191],v[4:7],a[100:103]
RD a[236:239],v227 offset:8192
buffer_load_dwordx4 v235, s[12:15], s64 offen offset:1024 lds
EX v125,v125
MM a[100:103],a[184:187],v[0:3],a[100:103]
EX v126,v126
v_smfmac_f32_16x16x64_bf16 v[236:239], v[144:147], v[64:71], v234
RD a[240:243],v224 offset:12288
RD a[244:247],v225 offset:12288
RD a[248:251],v226 offset:12288
RD a[252:255],v227 offset:12288
EX v127,v127
v_smfmac_f32_16x16x64_bf16 v[236:239], v[148:151], v[72:79], v234
CV v92,v120,v121
v_smfmac_f32_16x16x64_bf16 v[236:239], v[152:155], v[80:87], v234
buffer_load_dwordx4 v235, s[12:15], s81 offen offset:2048 lds
CV v93,v122,v123
CV v94,v124,v125
CV v95,v126,v127
s_nop 1
v_smfmac_f32_16x16x64_bf16 v[236:239], v[156:159], v[88:95], v234
s_waitcnt lgkmcnt(0)
buffer_load_dwordx4 v235, s[12:15], s64 offen offset:3072 lds
MM v[0:3],a[192:195],v[160:163],v[128:131]
s_add_u32 s64, s64, 16384
TR a[128:129],v232 offset:49152
s_add_u32 s81, s64, 896
MM v[0:3],a[196:199],v[176:179],v[0:3]
TR a[130:131],v232 offset:53248
TR a[132:133],v232 offset:57344
MM v[0:3],a[200:203],v[192:195],v[0:3]
TR a[134:135],v232 offset:61440
TR a[136:137],v233 offset:49152
MM v[0:3],a[204:207],v[208:211],v[0:3]
TR a[138:139],v233 offset:53248
v_add_f32 v247, v236, v237
MM v[4:7],a[220:223],v[208:211],v[128:131]
TR a[140:141],v233 offset:57344
v_cmp_nge_f32 vcc, s71, v247
MM v[4:7],a[216:219],v[192:195],v[4:7]
s_cbranch_vccnz LSITE3_%=
LBACK3_%=:
TR a[142:143],v233 offset:61440
TR a[144:145],v232 offset:49664
MM v[4:7],a[212:215],v[176:179],v[4:7]
v_lshrrev_b32 v252, 6, v248
v_mul_u32_u24 v252, 3456, v252
MM v[4:7],a[208:211],v[160:163],v[4:7]
v_and_b32 v251, 48, v248
v_add_u32 v252, v252, v251
MM v[12:15],a[208:211],v[164:167],v[132:135]
s_and_b32 s90, %[swave], 1
EX v0,v0
MM v[12:15],a[212:215],v[180:183],v[12:15]
s_mul_i32 s90, s90, 276480
EX v1,v1
MM v[12:15],a[216:219],v[196:199],v[12:15]
s_bfe_u32 s91, s48, 131077
TR a[146:147],v232 offset:53760
CV v0,v0,v1
MM v[12:15],a[220:223],v[212:215],v[12:15]
s_mul_i32 s91, s91, 552960
EX v2,v2
MM v[8:11],a[204:207],v[212:215],v[132:135]
s_add_u32 s90, s90, s91
EX v3,v3
MM v[8:11],a[200:203],v[196:199],v[8:11]
s_and_b32 s91, s48, 0xffffff80
TR a[148:149],v232 offset:57856
CV v1,v2,v3
MM v[8:11],a[196:199],v[180:183],v[8:11]
s_add_u32 s90, s90, s91
EX v4,v4
MM v[8:11],a[192:195],v[164:167],v[8:11]
s_and_b32 s91, s48, 16
TR a[150:151],v232 offset:61952
TR a[152:153],v233 offset:49664
MM v[16:19],a[192:195],v[168:171],v[136:139]
s_lshl_b32 s91, s91, 2
EX v5,v5
MM v[16:19],a[196:199],v[184:187],v[16:19]
s_add_u32 s66, s90, s91
TR a[154:155],v233 offset:53760
CV v2,v4,v5
MM v[16:19],a[200:203],v[200:203],v[16:19]
s_mul_i32 s91, %[swave], 5120
EX v6,v6
MM v[16:19],a[204:207],v[216:219],v[16:19]
s_add_u32 s91, s91, %[slds]
EX v7,v7
MM v[20:23],a[220:223],v[216:219],v[136:139]
s_add_u32 s67, s91, 131072
TR a[156:157],v233 offset:57856
CV v3,v6,v7
MM v[20:23],a[216:219],v[200:203],v[20:23]
s_add_u32 s85, s66, 221184
EX v8,v8
MM v[20:23],a[212:215],v[184:187],v[20:23]
s_add_u32 s86, s67, 4096
TR a[158:159],v233 offset:61952
TR a[160:161],v232 offset:50176
MM v[20:23],a[208:211],v[168:171],v[20:23]
EX v9,v9
MM v[28:31],a[208:211],v[172:175],v[140:143]
TR a[162:163],v232 offset:54272
CV v8,v8,v9
MM v[28:31],a[212:215],v[188:191],v[28:31]
EX v10,v10
MM v[28:31],a[216:219],v[204:207],v[28:31]
EX v11,v11
MM v[28:31],a[220:223],v[220:223],v[28:31]
TR a[164:165],v232 offset:58368
CV v9,v10,v11
MM v[24:27],a[204:207],v[220:223],v[140:143]
EX v12,v12
MM v[24:27],a[200:203],v[204:207],v[24:27]
TR a[166:167],v232 offset:62464
TR a[168:169],v233 offset:50176
MM v[24:27],a[196:199],v[188:191],v[24:27]
)ASM"
R"ASM(
EX v13,v13
MM v[24:27],a[192:195],v[172:175],v[24:27]
LPG6_%=:
s_cbranch_execz LPG10_%=
TR a[170:171],v233 offset:54272
CV v10,v12,v13
MM v[32:35],a[224:227],v[160:163],v[128:131]
EX v14,v14
MM v[32:35],a[228:231],v[176:179],v[32:35]
EX v15,v15
MM v[32:35],a[232:235],v[192:195],v[32:35]
TR a[172:173],v233 offset:58368
CV v11,v14,v15
MM v[32:35],a[236:239],v[208:211],v[32:35]
TR a[174:175],v233 offset:62464
TR a[176:177],v232 offset:50688
MM v[36:39],a[252:255],v[208:211],v[128:131]
EX v16,v16
MM v[36:39],a[248:251],v[192:195],v[36:39]
EX v17,v17
MM v[36:39],a[244:247],v[176:179],v[36:39]
TR a[178:179],v232 offset:54784
CV v16,v16,v17
MM v[36:39],a[240:243],v[160:163],v[36:39]
EX v18,v18
MM v[44:47],a[240:243],v[164:167],v[132:135]
TR a[180:181],v232 offset:58880
TR a[182:183],v232 offset:62976
MM v[44:47],a[244:247],v[180:183],v[44:47]
EX v19,v19
MM v[44:47],a[248:251],v[196:199],v[44:47]
TR a[184:185],v233 offset:50688
CV v17,v18,v19
MM v[44:47],a[252:255],v[212:215],v[44:47]
EX v20,v20
MM v[40:43],a[236:239],v[212:215],v[132:135]
EX v21,v21
MM v[40:43],a[232:235],v[196:199],v[40:43]
TR a[186:187],v233 offset:54784
CV v18,v20,v21
MM v[40:43],a[228:231],v[180:183],v[40:43]
EX v22,v22
MM v[40:43],a[224:227],v[164:167],v[40:43]
TR a[188:189],v233 offset:58880
TR a[190:191],v233 offset:62976
MM v[48:51],a[224:227],v[168:171],v[136:139]
EX v23,v23
MM v[48:51],a[228:231],v[184:187],v[48:51]
CV v19,v22,v23
MM v[48:51],a[232:235],v[200:203],v[48:51]
EX v24,v24
MM v[48:51],a[236:239],v[216:219],v[48:51]
EX v25,v25
MM v[52:55],a[252:255],v[216:219],v[136:139]
EX v26,v26
MM v[52:55],a[248:251],v[200:203],v[52:55]
EX v27,v27
MM v[52:55],a[244:247],v[184:187],v[52:55]
EX v28,v28
MM v[52:55],a[240:243],v[168:171],v[52:55]
EX v29,v29
MM v[60:63],a[240:243],v[172:175],v[140:143]
EX v30,v30
MM v[60:63],a[244:247],v[188:191],v[60:63]
EX v31,v31
MM v[60:63],a[248:251],v[204:207],v[60:63]
EX v32,v32
MM v[60:63],a[252:255],v[220:223],v[60:63]
EX v33,v33
MM v[56:59],a[236:239],v[220:223],v[140:143]
EX v34,v34
MM v[56:59],a[232:235],v[204:207],v[56:59]
EX v35,v35
MM v[56:59],a[228:231],v[188:191],v[56:59]
EX v36,v36
MM v[56:59],a[224:227],v[172:175],v[56:59]
s_waitcnt vmcnt(8) lgkmcnt(0)
s_barrier
MM a[0:3],a[128:131],v[64:67],a[0:3]
s_add_u32 m0, s65, 49152
CV v24,v24,v25
CV v25,v26,v27
MM a[0:3],a[132:135],v[68:71],a[0:3]
RD a[192:195],v224 offset:16384
CV v26,v28,v29
MM a[8:11],a[132:135],v[76:79],a[8:11]
RD a[196:199],v225 offset:16384
CV v27,v30,v31
MM a[8:11],a[128:131],v[72:75],a[8:11]
buffer_load_dwordx4 v228, s[8:11], s64 offen offset:0 lds
MM a[16:19],a[128:131],v[80:83],0
RD a[200:203],v226 offset:16384
CV v4,v32,v33
MM a[16:19],a[132:135],v[84:87],a[16:19]
RD a[204:207],v227 offset:16384
CV v5,v34,v35
MM a[24:27],a[132:135],v[92:95],0
buffer_load_dwordx4 v229, s[8:11], s64 offen offset:1024 lds
MM a[24:27],a[128:131],v[88:91],a[24:27]
RD a[208:211],v224 offset:20480
RD a[212:215],v225 offset:20480
MM a[28:31],a[136:139],v[88:91],0
buffer_load_dwordx4 v230, s[8:11], s64 offen offset:2048 lds
MM a[28:31],a[140:143],v[92:95],a[28:31]
RD a[216:219],v226 offset:20480
RD a[220:223],v227 offset:20480
MM a[20:23],a[140:143],v[84:87],0
EX v37,v37
MM a[20:23],a[136:139],v[80:83],a[20:23]
RD a[224:227],v224 offset:24576
CV v6,v36,v37
MM a[12:15],a[136:139],v[72:75],a[12:15]
EX v38,v38
MM a[12:15],a[140:143],v[76:79],a[12:15]
buffer_load_dwordx4 v231, s[8:11], s64 offen offset:3072 lds
MM a[4:7],a[140:143],v[68:71],a[4:7]
s_add_u32 m0, s65, 114688
EX v39,v39
MM a[4:7],a[136:139],v[64:67],a[4:7]
RD a[228:231],v225 offset:24576
CV v7,v38,v39
MM a[32:35],a[144:147],v[64:67],a[32:35]
EX v40,v40
MM a[32:35],a[148:151],v[68:71],a[32:35]
EX v41,v41
MM a[40:43],a[148:151],v[76:79],a[40:43]
RD a[232:235],v226 offset:24576
CV v12,v40,v41
MM a[40:43],a[144:147],v[72:75],a[40:43]
EX v42,v42
MM a[48:51],a[144:147],v[80:83],0
buffer_load_dwordx4 v235, s[12:15], s81 offen offset:0 lds
MM a[48:51],a[148:151],v[84:87],a[48:51]
EX v43,v43
MM a[56:59],a[148:151],v[92:95],0
RD a[236:239],v227 offset:24576
CV v13,v42,v43
MM a[56:59],a[144:147],v[88:91],a[56:59]
EX v44,v44
MM a[60:63],a[152:155],v[88:91],0
EX v45,v45
MM a[60:63],a[156:159],v[92:95],a[60:63]
RD a[240:243],v224 offset:28672
CV v14,v44,v45
MM a[52:55],a[156:159],v[84:87],0
EX v46,v46
MM a[52:55],a[152:155],v[80:83],a[52:55]
buffer_load_dwordx4 v235, s[12:15], s64 offen offset:1024 lds
MM a[44:47],a[152:155],v[72:75],a[44:47]
EX v47,v47
MM a[44:47],a[156:159],v[76:79],a[44:47]
RD a[244:247],v225 offset:28672
CV v15,v46,v47
MM a[36:39],a[156:159],v[68:71],a[36:39]
EX v48,v48
MM a[36:39],a[152:155],v[64:67],a[36:39]
EX v49,v49
MM a[64:67],a[160:163],v[64:67],a[64:67]
RD a[248:251],v226 offset:28672
CV v20,v48,v49
MM a[64:67],a[164:167],v[68:71],a[64:67]
EX v50,v50
MM a[72:75],a[164:167],v[76:79],a[72:75]
buffer_load_dwordx4 v235, s[12:15], s81 offen offset:2048 lds
MM a[72:75],a[160:163],v[72:75],a[72:75]
EX v51,v51
MM a[80:83],a[160:163],v[80:83],0
RD a[252:255],v227 offset:28672
CV v21,v50,v51
MM a[80:83],a[164:167],v[84:87],a[80:83]
EX v52,v52
MM a[88:91],a[164:167],v[92:95],0
EX v53,v53
MM a[88:91],a[160:163],v[88:91],a[88:91]
buffer_load_dwordx4 v235, s[12:15], s64 offen offset:3072 lds
MM a[92:95],a[168:171],v[88:91],0
EX v54,v54
MM a[92:95],a[172:175],v[92:95],a[92:95]
EX v55,v55
MM a[84:87],a[172:175],v[84:87],0
EX v56,v56
MM a[84:87],a[168:171],v[80:83],a[84:87]
EX v57,v57
MM a[76:79],a[168:171],v[72:75],a[76:79]
EX v58,v58
MM a[76:79],a[172:175],v[76:79],a[76:79]
EX v59,v59
MM a[68:71],a[172:175],v[68:71],a[68:71]
EX v60,v60
MM a[68:71],a[168:171],v[64:67],a[68:71]
EX v61,v61
MM a[96:99],a[176:179],v[64:67],a[96:99]
EX v62,v62
MM a[96:99],a[180:183],v[68:71],a[96:99]
EX v63,v63
MM a[104:107],a[180:183],v[76:79],a[104:107]
CV v22,v52,v53
CV v23,v54,v55
MM a[104:107],a[176:179],v[72:75],a[104:107]
CV v28,v56,v57
CV v29,v58,v59
MM a[112:115],a[176:179],v[80:83],0
CV v30,v60,v61
CV v31,v62,v63
MM a[112:115],a[180:183],v[84:87],a[112:115]
MM a[120:123],a[180:183],v[92:95],0
MM a[120:123],a[176:179],v[88:91],a[120:123]
MM a[124:127],a[184:187],v[88:91],0
MM a[124:127],a[188:191],v[92:95],a[124:127]
MM a[116:119],a[188:191],v[84:87],0
MM a[116:119],a[184:187],v[80:83],a[116:119]
MM a[108:111],a[184:187],v[72:75],a[108:111]
MM a[108:111],a[188:191],v[76:79],a[108:111]
MM a[100:103],a[188:191],v[68:71],a[100:103]
MM a[100:103],a[184:187],v[64:67],a[100:103]
v_smfmac_f32_16x16x64_bf16 v[236:239], v[144:147], v[0:7], v234
v_smfmac_f32_16x16x64_bf16 v[236:239], v[148:151], v[8:15], v234
v_smfmac_f32_16x16x64_bf16 v[236:239], v[152:155], v[16:23], v234
v_smfmac_f32_16x16x64_bf16 v[236:239], v[156:159], v[24:31], v234
s_waitcnt lgkmcnt(0)
s_cmp_lg_u32 %[sb], 0
s_cbranch_scc1 LXS_%=
s_getpc_b64 s[98:99]
LTA1_%=:
s_lshl_b32 s73, %[swave], 12
s_add_u32 s73, s73, LPG26_%=-LTA1_%=
s_ashr_i32 s74, s73, 31
s_add_u32 s98, s98, s73
s_addc_u32 s99, s99, s74
s_mov_b64 exec, 0
s_setpc_b64 s[98:99]
LTR1_%=:
s_mov_b64 exec, -1
s_branch LXP_%=
LXS_%=:
s_mov_b32 s70, 2
LLA_%=:
MM v[64:67],a[192:195],v[160:163],v[128:131]
s_add_u32 s64, s64, 16384
TR a[128:129],v232 offset:0
s_add_u32 s81, s64, 896
MM v[64:67],a[196:199],v[176:179],v[64:67]
TR a[130:131],v232 offset:4096
TR a[132:133],v232 offset:8192
MM v[64:67],a[200:203],v[192:195],v[64:67]
TR a[134:135],v232 offset:12288
TR a[136:137],v233 offset:0
MM v[64:67],a[204:207],v[208:211],v[64:67]
TR a[138:139],v233 offset:4096
v_add_f32 v246, v236, v237
MM v[68:71],a[220:223],v[208:211],v[128:131]
TR a[140:141],v233 offset:8192
v_cmp_nge_f32 vcc, s71, v246
MM v[68:71],a[216:219],v[192:195],v[68:71]
s_cbranch_vccnz LSITE8_%=
LBACK8_%=:
TR a[142:143],v233 offset:12288
TR a[144:145],v232 offset:512
MM v[68:71],a[212:215],v[176:179],v[68:71]
s_mov_b32 m0, s67
TR a[146:147],v232 offset:4608
TR a[148:149],v232 offset:8704
MM v[68:71],a[208:211],v[160:163],v[68:71]
buffer_load_dwordx4 v252, s[36:39], s66 offen offset:0 lds
MM v[76:79],a[208:211],v[164:167],v[132:135]
s_add_u32 s66, s66, 55296
EX v64,v64
MM v[76:79],a[212:215],v[180:183],v[76:79]
s_min_u32 s66, s66, s85
EX v65,v65
MM v[76:79],a[216:219],v[196:199],v[76:79]
s_add_u32 s67, s67, 1024
TR a[150:151],v232 offset:12800
CV v64,v64,v65
MM v[76:79],a[220:223],v[212:215],v[76:79]
s_min_u32 s67, s67, s86
EX v66,v66
MM v[72:75],a[204:207],v[212:215],v[132:135]
EX v67,v67
MM v[72:75],a[200:203],v[196:199],v[72:75]
TR a[152:153],v233 offset:512
CV v65,v66,v67
MM v[72:75],a[196:199],v[180:183],v[72:75]
EX v68,v68
MM v[72:75],a[192:195],v[164:167],v[72:75]
EX v69,v69
MM v[80:83],a[192:195],v[168:171],v[136:139]
TR a[154:155],v233 offset:4608
CV v66,v68,v69
MM v[80:83],a[196:199],v[184:187],v[80:83]
EX v70,v70
MM v[80:83],a[200:203],v[200:203],v[80:83]
EX v71,v71
MM v[80:83],a[204:207],v[216:219],v[80:83]
TR a[156:157],v233 offset:8704
CV v67,v70,v71
MM v[84:87],a[220:223],v[216:219],v[136:139]
EX v72,v72
MM v[84:87],a[216:219],v[200:203],v[84:87]
TR a[158:159],v233 offset:12800
TR a[160:161],v232 offset:1024
MM v[84:87],a[212:215],v[184:187],v[84:87]
EX v73,v73
MM v[84:87],a[208:211],v[168:171],v[84:87]
TR a[162:163],v232 offset:5120
CV v72,v72,v73
MM v[92:95],a[208:211],v[172:175],v[140:143]
EX v74,v74
MM v[92:95],a[212:215],v[188:191],v[92:95]
TR a[164:165],v232 offset:9216
TR a[166:167],v232 offset:13312
MM v[92:95],a[216:219],v[204:207],v[92:95]
EX v75,v75
MM v[92:95],a[220:223],v[220:223],v[92:95]
TR a[168:169],v233 offset:1024
CV v73,v74,v75
MM v[88:91],a[204:207],v[220:223],v[140:143]
EX v76,v76
MM v[88:91],a[200:203],v[204:207],v[88:91]
EX v77,v77
MM v[88:91],a[196:199],v[188:191],v[88:91]
TR a[170:171],v233 offset:5120
CV v74,v76,v77
MM v[88:91],a[192:195],v[172:175],v[88:91]
EX v78,v78
MM v[96:99],a[224:227],v[160:163],v[128:131]
TR a[172:173],v233 offset:9216
TR a[174:175],v233 offset:13312
MM v[96:99],a[228:231],v[176:179],v[96:99]
EX v79,v79
MM v[96:99],a[232:235],v[192:195],v[96:99]
TR a[176:177],v232 offset:1536
CV v75,v78,v79
MM v[96:99],a[236:239],v[208:211],v[96:99]
EX v80,v80
MM v[100:103],a[252:255],v[208:211],v[128:131]
EX v81,v81
MM v[100:103],a[248:251],v[192:195],v[100:103]
TR a[178:179],v232 offset:5632
CV v80,v80,v81
MM v[100:103],a[244:247],v[176:179],v[100:103]
EX v82,v82
MM v[100:103],a[240:243],v[160:163],v[100:103]
TR a[180:181],v232 offset:9728
TR a[182:183],v232 offset:13824
MM v[108:111],a[240:243],v[164:167],v[132:135]
EX v83,v83
MM v[108:111],a[244:247],v[180:183],v[108:111]
TR a[184:185],v233 offset:1536
CV v81,v82,v83
MM v[108:111],a[248:251],v[196:199],v[108:111]
EX v84,v84
MM v[108:111],a[252:255],v[212:215],v[108:111]
EX v85,v85
MM v[104:107],a[236:239],v[212:215],v[132:135]
TR a[186:187],v233 offset:5632
CV v82,v84,v85
MM v[104:107],a[232:235],v[196:199],v[104:107]
EX v86,v86
MM v[104:107],a[228:231],v[180:183],v[104:107]
TR a[188:189],v233 offset:9728
TR a[190:191],v233 offset:13824
MM v[104:107],a[224:227],v[164:167],v[104:107]
EX v87,v87
MM v[112:115],a[224:227],v[168:171],v[136:139]
CV v83,v86,v87
MM v[112:115],a[228:231],v[184:187],v[112:115]
EX v88,v88
MM v[112:115],a[232:235],v[200:203],v[112:115]
EX v89,v89
MM v[112:115],a[236:239],v[216:219],v[112:115]
EX v90,v90
MM v[116:119],a[252:255],v[216:219],v[136:139]
EX v91,v91
MM v[116:119],a[248:251],v[200:203],v[116:119]
EX v92,v92
MM v[116:119],a[244:247],v[184:187],v[116:119]
EX v93,v93
MM v[116:119],a[240:243],v[168:171],v[116:119]
EX v94,v94
MM v[124:127],a[240:243],v[172:175],v[140:143]
EX v95,v95
MM v[124:127],a[244:247],v[188:191],v[124:127]
EX v96,v96
MM v[124:127],a[248:251],v[204:207],v[124:127]
EX v97,v97
MM v[124:127],a[252:255],v[220:223],v[124:127]
EX v98,v98
MM v[120:123],a[236:239],v[220:223],v[140:143]
EX v99,v99
MM v[120:123],a[232:235],v[204:207],v[120:123]
EX v100,v100
MM v[120:123],a[228:231],v[188:191],v[120:123]
EX v101,v101
MM v[120:123],a[224:227],v[172:175],v[120:123]
s_waitcnt vmcnt(13) lgkmcnt(0)
s_barrier
MM a[0:3],a[128:131],v[0:3],a[0:3]
s_add_u32 m0, s65, 0
CV v88,v88,v89
CV v89,v90,v91
MM a[0:3],a[132:135],v[4:7],a[0:3]
RD a[192:195],v224 offset:32768
CV v90,v92,v93
)ASM"
R"ASM(
MM a[8:11],a[132:135],v[12:15],a[8:11]
RD a[196:199],v225 offset:32768
CV v91,v94,v95
MM a[8:11],a[128:131],v[8:11],a[8:11]
buffer_load_dwordx4 v228, s[8:11], s64 offen offset:0 lds
MM a[16:19],a[128:131],v[16:19],a[16:19]
RD a[200:203],v226 offset:32768
CV v68,v96,v97
MM a[16:19],a[132:135],v[20:23],a[16:19]
RD a[204:207],v227 offset:32768
CV v69,v98,v99
MM a[24:27],a[132:135],v[28:31],a[24:27]
buffer_load_dwordx4 v229, s[8:11], s64 offen offset:1024 lds
MM a[24:27],a[128:131],v[24:27],a[24:27]
RD a[208:211],v224 offset:36864
CV v70,v100,v101
MM a[28:31],a[136:139],v[24:27],a[28:31]
RD a[212:215],v225 offset:36864
RD a[216:219],v226 offset:36864
MM a[28:31],a[140:143],v[28:31],a[28:31]
buffer_load_dwordx4 v230, s[8:11], s64 offen offset:2048 lds
MM a[20:23],a[140:143],v[20:23],a[20:23]
RD a[220:223],v227 offset:36864
RD a[224:227],v224 offset:40960
MM a[20:23],a[136:139],v[16:19],a[20:23]
EX v102,v102
MM a[12:15],a[136:139],v[8:11],a[12:15]
buffer_load_dwordx4 v231, s[8:11], s64 offen offset:3072 lds
MM a[12:15],a[140:143],v[12:15],a[12:15]
s_add_u32 m0, s65, 65536
EX v103,v103
MM a[4:7],a[140:143],v[4:7],a[4:7]
RD a[228:231],v225 offset:40960
CV v71,v102,v103
MM a[4:7],a[136:139],v[0:3],a[4:7]
EX v104,v104
MM a[32:35],a[144:147],v[0:3],a[32:35]
EX v105,v105
MM a[32:35],a[148:151],v[4:7],a[32:35]
RD a[232:235],v226 offset:40960
CV v76,v104,v105
MM a[40:43],a[148:151],v[12:15],a[40:43]
EX v106,v106
MM a[40:43],a[144:147],v[8:11],a[40:43]
buffer_load_dwordx4 v235, s[12:15], s81 offen offset:0 lds
MM a[48:51],a[144:147],v[16:19],a[48:51]
EX v107,v107
MM a[48:51],a[148:151],v[20:23],a[48:51]
RD a[236:239],v227 offset:40960
CV v77,v106,v107
MM a[56:59],a[148:151],v[28:31],a[56:59]
EX v108,v108
MM a[56:59],a[144:147],v[24:27],a[56:59]
EX v109,v109
MM a[60:63],a[152:155],v[24:27],a[60:63]
RD a[240:243],v224 offset:45056
CV v78,v108,v109
MM a[60:63],a[156:159],v[28:31],a[60:63]
buffer_load_dwordx4 v235, s[12:15], s64 offen offset:1024 lds
MM a[52:55],a[156:159],v[20:23],a[52:55]
EX v110,v110
MM a[52:55],a[152:155],v[16:19],a[52:55]
EX v111,v111
MM a[44:47],a[152:155],v[8:11],a[44:47]
RD a[244:247],v225 offset:45056
CV v79,v110,v111
MM a[44:47],a[156:159],v[12:15],a[44:47]
EX v112,v112
MM a[36:39],a[156:159],v[4:7],a[36:39]
EX v113,v113
MM a[36:39],a[152:155],v[0:3],a[36:39]
RD a[248:251],v226 offset:45056
CV v84,v112,v113
MM a[64:67],a[160:163],v[0:3],a[64:67]
buffer_load_dwordx4 v235, s[12:15], s81 offen offset:2048 lds
MM a[64:67],a[164:167],v[4:7],a[64:67]
EX v114,v114
MM a[72:75],a[164:167],v[12:15],a[72:75]
EX v115,v115
MM a[72:75],a[160:163],v[8:11],a[72:75]
RD a[252:255],v227 offset:45056
CV v85,v114,v115
MM a[80:83],a[160:163],v[16:19],a[80:83]
EX v116,v116
MM a[80:83],a[164:167],v[20:23],a[80:83]
buffer_load_dwordx4 v235, s[12:15], s64 offen offset:3072 lds
MM a[88:91],a[164:167],v[28:31],a[88:91]
EX v117,v117
MM a[88:91],a[160:163],v[24:27],a[88:91]
EX v118,v118
MM a[92:95],a[168:171],v[24:27],a[92:95]
EX v119,v119
MM a[92:95],a[172:175],v[28:31],a[92:95]
EX v120,v120
MM a[84:87],a[172:175],v[20:23],a[84:87]
EX v121,v121
MM a[84:87],a[168:171],v[16:19],a[84:87]
EX v122,v122
MM a[76:79],a[168:171],v[8:11],a[76:79]
EX v123,v123
MM a[76:79],a[172:175],v[12:15],a[76:79]
EX v124,v124
MM a[68:71],a[172:175],v[4:7],a[68:71]
EX v125,v125
MM a[68:71],a[168:171],v[0:3],a[68:71]
EX v126,v126
MM a[96:99],a[176:179],v[0:3],a[96:99]
EX v127,v127
MM a[96:99],a[180:183],v[4:7],a[96:99]
CV v86,v116,v117
CV v87,v118,v119
MM a[104:107],a[180:183],v[12:15],a[104:107]
CV v92,v120,v121
CV v93,v122,v123
MM a[104:107],a[176:179],v[8:11],a[104:107]
CV v94,v124,v125
CV v95,v126,v127
MM a[112:115],a[176:179],v[16:19],a[112:115]
MM a[112:115],a[180:183],v[20:23],a[112:115]
MM a[120:123],a[180:183],v[28:31],a[120:123]
MM a[120:123],a[176:179],v[24:27],a[120:123]
MM a[124:127],a[184:187],v[24:27],a[124:127]
MM a[124:127],a[188:191],v[28:31],a[124:127]
MM a[116:119],a[188:191],v[20:23],a[116:119]
MM a[116:119],a[184:187],v[16:19],a[116:119]
MM a[108:111],a[184:187],v[8:11],a[108:111]
MM a[108:111],a[188:191],v[12:15],a[108:111]
MM a[100:103],a[188:191],v[4:7],a[100:103]
MM a[100:103],a[184:187],v[0:3],a[100:103]
v_smfmac_f32_16x16x64_bf16 v[236:239], v[144:147], v[64:71], v234
v_smfmac_f32_16x16x64_bf16 v[236:239], v[148:151], v[72:79], v234
v_smfmac_f32_16x16x64_bf16 v[236:239], v[152:155], v[80:87], v234
v_smfmac_f32_16x16x64_bf16 v[236:239], v[156:159], v[88:95], v234
s_waitcnt lgkmcnt(0)
MM v[0:3],a[192:195],v[160:163],v[128:131]
s_add_u32 s64, s64, 16384
TR a[128:129],v232 offset:16384
s_add_u32 s81, s64, 896
MM v[0:3],a[196:199],v[176:179],v[0:3]
TR a[130:131],v232 offset:20480
TR a[132:133],v232 offset:24576
MM v[0:3],a[200:203],v[192:195],v[0:3]
TR a[134:135],v232 offset:28672
TR a[136:137],v233 offset:16384
MM v[0:3],a[204:207],v[208:211],v[0:3]
TR a[138:139],v233 offset:20480
v_add_f32 v247, v236, v237
MM v[4:7],a[220:223],v[208:211],v[128:131]
TR a[140:141],v233 offset:24576
v_cmp_nge_f32 vcc, s71, v247
MM v[4:7],a[216:219],v[192:195],v[4:7]
s_cbranch_vccnz LSITE9_%=
LBACK9_%=:
TR a[142:143],v233 offset:28672
TR a[144:145],v232 offset:16896
MM v[4:7],a[212:215],v[176:179],v[4:7]
s_mov_b32 m0, s67
TR a[146:147],v232 offset:20992
TR a[148:149],v232 offset:25088
MM v[4:7],a[208:211],v[160:163],v[4:7]
buffer_load_dwordx4 v252, s[36:39], s66 offen offset:0 lds
MM v[12:15],a[208:211],v[164:167],v[132:135]
s_add_u32 s66, s66, 55296
EX v0,v0
MM v[12:15],a[212:215],v[180:183],v[12:15]
s_min_u32 s66, s66, s85
EX v1,v1
MM v[12:15],a[216:219],v[196:199],v[12:15]
s_add_u32 s67, s67, 1024
TR a[150:151],v232 offset:29184
CV v0,v0,v1
MM v[12:15],a[220:223],v[212:215],v[12:15]
s_min_u32 s67, s67, s86
EX v2,v2
MM v[8:11],a[204:207],v[212:215],v[132:135]
EX v3,v3
MM v[8:11],a[200:203],v[196:199],v[8:11]
TR a[152:153],v233 offset:16896
CV v1,v2,v3
MM v[8:11],a[196:199],v[180:183],v[8:11]
EX v4,v4
MM v[8:11],a[192:195],v[164:167],v[8:11]
EX v5,v5
MM v[16:19],a[192:195],v[168:171],v[136:139]
TR a[154:155],v233 offset:20992
CV v2,v4,v5
MM v[16:19],a[196:199],v[184:187],v[16:19]
EX v6,v6
MM v[16:19],a[200:203],v[200:203],v[16:19]
EX v7,v7
MM v[16:19],a[204:207],v[216:219],v[16:19]
TR a[156:157],v233 offset:25088
CV v3,v6,v7
MM v[20:23],a[220:223],v[216:219],v[136:139]
LPG7_%=:
s_cbranch_execz LPG11_%=
EX v8,v8
MM v[20:23],a[216:219],v[200:203],v[20:23]
TR a[158:159],v233 offset:29184
TR a[160:161],v232 offset:17408
MM v[20:23],a[212:215],v[184:187],v[20:23]
EX v9,v9
MM v[20:23],a[208:211],v[168:171],v[20:23]
TR a[162:163],v232 offset:21504
CV v8,v8,v9
MM v[28:31],a[208:211],v[172:175],v[140:143]
EX v10,v10
MM v[28:31],a[212:215],v[188:191],v[28:31]
TR a[164:165],v232 offset:25600
TR a[166:167],v232 offset:29696
MM v[28:31],a[216:219],v[204:207],v[28:31]
EX v11,v11
MM v[28:31],a[220:223],v[220:223],v[28:31]
TR a[168:169],v233 offset:17408
CV v9,v10,v11
MM v[24:27],a[204:207],v[220:223],v[140:143]
EX v12,v12
MM v[24:27],a[200:203],v[204:207],v[24:27]
EX v13,v13
MM v[24:27],a[196:199],v[188:191],v[24:27]
TR a[170:171],v233 offset:21504
CV v10,v12,v13
MM v[24:27],a[192:195],v[172:175],v[24:27]
EX v14,v14
MM v[32:35],a[224:227],v[160:163],v[128:131]
TR a[172:173],v233 offset:25600
TR a[174:175],v233 offset:29696
MM v[32:35],a[228:231],v[176:179],v[32:35]
EX v15,v15
MM v[32:35],a[232:235],v[192:195],v[32:35]
TR a[176:177],v232 offset:17920
CV v11,v14,v15
MM v[32:35],a[236:239],v[208:211],v[32:35]
EX v16,v16
MM v[36:39],a[252:255],v[208:211],v[128:131]
EX v17,v17
MM v[36:39],a[248:251],v[192:195],v[36:39]
TR a[178:179],v232 offset:22016
CV v16,v16,v17
MM v[36:39],a[244:247],v[176:179],v[36:39]
EX v18,v18
MM v[36:39],a[240:243],v[160:163],v[36:39]
TR a[180:181],v232 offset:26112
TR a[182:183],v232 offset:30208
MM v[44:47],a[240:243],v[164:167],v[132:135]
EX v19,v19
MM v[44:47],a[244:247],v[180:183],v[44:47]
TR a[184:185],v233 offset:17920
CV v17,v18,v19
MM v[44:47],a[248:251],v[196:199],v[44:47]
EX v20,v20
MM v[44:47],a[252:255],v[212:215],v[44:47]
EX v21,v21
MM v[40:43],a[236:239],v[212:215],v[132:135]
TR a[186:187],v233 offset:22016
CV v18,v20,v21
MM v[40:43],a[232:235],v[196:199],v[40:43]
EX v22,v22
MM v[40:43],a[228:231],v[180:183],v[40:43]
TR a[188:189],v233 offset:26112
TR a[190:191],v233 offset:30208
MM v[40:43],a[224:227],v[164:167],v[40:43]
EX v23,v23
MM v[48:51],a[224:227],v[168:171],v[136:139]
CV v19,v22,v23
MM v[48:51],a[228:231],v[184:187],v[48:51]
EX v24,v24
MM v[48:51],a[232:235],v[200:203],v[48:51]
EX v25,v25
MM v[48:51],a[236:239],v[216:219],v[48:51]
EX v26,v26
MM v[52:55],a[252:255],v[216:219],v[136:139]
EX v27,v27
MM v[52:55],a[248:251],v[200:203],v[52:55]
EX v28,v28
MM v[52:55],a[244:247],v[184:187],v[52:55]
EX v29,v29
MM v[52:55],a[240:243],v[168:171],v[52:55]
EX v30,v30
MM v[60:63],a[240:243],v[172:175],v[140:143]
EX v31,v31
MM v[60:63],a[244:247],v[188:191],v[60:63]
EX v32,v32
MM v[60:63],a[248:251],v[204:207],v[60:63]
EX v33,v33
MM v[60:63],a[252:255],v[220:223],v[60:63]
EX v34,v34
MM v[56:59],a[236:239],v[220:223],v[140:143]
EX v35,v35
MM v[56:59],a[232:235],v[204:207],v[56:59]
EX v36,v36
MM v[56:59],a[228:231],v[188:191],v[56:59]
EX v37,v37
MM v[56:59],a[224:227],v[172:175],v[56:59]
s_waitcnt vmcnt(13) lgkmcnt(0)
s_barrier
MM a[0:3],a[128:131],v[64:67],a[0:3]
s_add_u32 m0, s65, 16384
CV v24,v24,v25
CV v25,v26,v27
MM a[0:3],a[132:135],v[68:71],a[0:3]
RD a[192:195],v224 offset:49152
CV v26,v28,v29
MM a[8:11],a[132:135],v[76:79],a[8:11]
RD a[196:199],v225 offset:49152
CV v27,v30,v31
MM a[8:11],a[128:131],v[72:75],a[8:11]
buffer_load_dwordx4 v228, s[8:11], s64 offen offset:0 lds
MM a[16:19],a[128:131],v[80:83],a[16:19]
RD a[200:203],v226 offset:49152
CV v4,v32,v33
MM a[16:19],a[132:135],v[84:87],a[16:19]
RD a[204:207],v227 offset:49152
CV v5,v34,v35
MM a[24:27],a[132:135],v[92:95],a[24:27]
buffer_load_dwordx4 v229, s[8:11], s64 offen offset:1024 lds
MM a[24:27],a[128:131],v[88:91],a[24:27]
RD a[208:211],v224 offset:53248
CV v6,v36,v37
MM a[28:31],a[136:139],v[88:91],a[28:31]
RD a[212:215],v225 offset:53248
RD a[216:219],v226 offset:53248
MM a[28:31],a[140:143],v[92:95],a[28:31]
buffer_load_dwordx4 v230, s[8:11], s64 offen offset:2048 lds
MM a[20:23],a[140:143],v[84:87],a[20:23]
RD a[220:223],v227 offset:53248
RD a[224:227],v224 offset:57344
MM a[20:23],a[136:139],v[80:83],a[20:23]
EX v38,v38
MM a[12:15],a[136:139],v[72:75],a[12:15]
buffer_load_dwordx4 v231, s[8:11], s64 offen offset:3072 lds
MM a[12:15],a[140:143],v[76:79],a[12:15]
s_add_u32 m0, s65, 81920
EX v39,v39
MM a[4:7],a[140:143],v[68:71],a[4:7]
RD a[228:231],v225 offset:57344
CV v7,v38,v39
MM a[4:7],a[136:139],v[64:67],a[4:7]
EX v40,v40
MM a[32:35],a[144:147],v[64:67],a[32:35]
EX v41,v41
MM a[32:35],a[148:151],v[68:71],a[32:35]
RD a[232:235],v226 offset:57344
CV v12,v40,v41
MM a[40:43],a[148:151],v[76:79],a[40:43]
EX v42,v42
MM a[40:43],a[144:147],v[72:75],a[40:43]
buffer_load_dwordx4 v235, s[12:15], s81 offen offset:0 lds
MM a[48:51],a[144:147],v[80:83],a[48:51]
EX v43,v43
MM a[48:51],a[148:151],v[84:87],a[48:51]
RD a[236:239],v227 offset:57344
CV v13,v42,v43
MM a[56:59],a[148:151],v[92:95],a[56:59]
EX v44,v44
MM a[56:59],a[144:147],v[88:91],a[56:59]
EX v45,v45
MM a[60:63],a[152:155],v[88:91],a[60:63]
RD a[240:243],v224 offset:61440
CV v14,v44,v45
MM a[60:63],a[156:159],v[92:95],a[60:63]
buffer_load_dwordx4 v235, s[12:15], s64 offen offset:1024 lds
MM a[52:55],a[156:159],v[84:87],a[52:55]
EX v46,v46
MM a[52:55],a[152:155],v[80:83],a[52:55]
EX v47,v47
MM a[44:47],a[152:155],v[72:75],a[44:47]
RD a[244:247],v225 offset:61440
CV v15,v46,v47
MM a[44:47],a[156:159],v[76:79],a[44:47]
EX v48,v48
MM a[36:39],a[156:159],v[68:71],a[36:39]
EX v49,v49
MM a[36:39],a[152:155],v[64:67],a[36:39]
RD a[248:251],v226 offset:61440
CV v20,v48,v49
MM a[64:67],a[160:163],v[64:67],a[64:67]
buffer_load_dwordx4 v235, s[12:15], s81 offen offset:2048 lds
MM a[64:67],a[164:167],v[68:71],a[64:67]
EX v50,v50
MM a[72:75],a[164:167],v[76:79],a[72:75]
EX v51,v51
MM a[72:75],a[160:163],v[72:75],a[72:75]
RD a[252:255],v227 offset:61440
CV v21,v50,v51
MM a[80:83],a[160:163],v[80:83],a[80:83]
)ASM"
R"ASM(
EX v52,v52
MM a[80:83],a[164:167],v[84:87],a[80:83]
buffer_load_dwordx4 v235, s[12:15], s64 offen offset:3072 lds
MM a[88:91],a[164:167],v[92:95],a[88:91]
EX v53,v53
MM a[88:91],a[160:163],v[88:91],a[88:91]
EX v54,v54
MM a[92:95],a[168:171],v[88:91],a[92:95]
EX v55,v55
MM a[92:95],a[172:175],v[92:95],a[92:95]
EX v56,v56
MM a[84:87],a[172:175],v[84:87],a[84:87]
EX v57,v57
MM a[84:87],a[168:171],v[80:83],a[84:87]
EX v58,v58
MM a[76:79],a[168:171],v[72:75],a[76:79]
EX v59,v59
MM a[76:79],a[172:175],v[76:79],a[76:79]
EX v60,v60
MM a[68:71],a[172:175],v[68:71],a[68:71]
EX v61,v61
MM a[68:71],a[168:171],v[64:67],a[68:71]
EX v62,v62
MM a[96:99],a[176:179],v[64:67],a[96:99]
EX v63,v63
MM a[96:99],a[180:183],v[68:71],a[96:99]
CV v22,v52,v53
CV v23,v54,v55
MM a[104:107],a[180:183],v[76:79],a[104:107]
CV v28,v56,v57
CV v29,v58,v59
MM a[104:107],a[176:179],v[72:75],a[104:107]
CV v30,v60,v61
CV v31,v62,v63
MM a[112:115],a[176:179],v[80:83],a[112:115]
MM a[112:115],a[180:183],v[84:87],a[112:115]
MM a[120:123],a[180:183],v[92:95],a[120:123]
MM a[120:123],a[176:179],v[88:91],a[120:123]
MM a[124:127],a[184:187],v[88:91],a[124:127]
MM a[124:127],a[188:191],v[92:95],a[124:127]
MM a[116:119],a[188:191],v[84:87],a[116:119]
MM a[116:119],a[184:187],v[80:83],a[116:119]
MM a[108:111],a[184:187],v[72:75],a[108:111]
MM a[108:111],a[188:191],v[76:79],a[108:111]
MM a[100:103],a[188:191],v[68:71],a[100:103]
MM a[100:103],a[184:187],v[64:67],a[100:103]
v_smfmac_f32_16x16x64_bf16 v[236:239], v[144:147], v[0:7], v234
v_smfmac_f32_16x16x64_bf16 v[236:239], v[148:151], v[8:15], v234
v_smfmac_f32_16x16x64_bf16 v[236:239], v[152:155], v[16:23], v234
v_smfmac_f32_16x16x64_bf16 v[236:239], v[156:159], v[24:31], v234
s_waitcnt lgkmcnt(0)
MM v[64:67],a[192:195],v[160:163],v[128:131]
s_add_u32 s64, s64, 16384
TR a[128:129],v232 offset:32768
s_add_u32 s81, s64, 896
MM v[64:67],a[196:199],v[176:179],v[64:67]
TR a[130:131],v232 offset:36864
TR a[132:133],v232 offset:40960
MM v[64:67],a[200:203],v[192:195],v[64:67]
TR a[134:135],v232 offset:45056
TR a[136:137],v233 offset:32768
MM v[64:67],a[204:207],v[208:211],v[64:67]
TR a[138:139],v233 offset:36864
v_add_f32 v246, v236, v237
MM v[68:71],a[220:223],v[208:211],v[128:131]
TR a[140:141],v233 offset:40960
v_cmp_nge_f32 vcc, s71, v246
MM v[68:71],a[216:219],v[192:195],v[68:71]
s_cbranch_vccnz LSITE10_%=
LBACK10_%=:
TR a[142:143],v233 offset:45056
TR a[144:145],v232 offset:33280
MM v[68:71],a[212:215],v[176:179],v[68:71]
s_mov_b32 m0, s67
TR a[146:147],v232 offset:37376
TR a[148:149],v232 offset:41472
MM v[68:71],a[208:211],v[160:163],v[68:71]
buffer_load_dwordx4 v252, s[36:39], s66 offen offset:0 lds
MM v[76:79],a[208:211],v[164:167],v[132:135]
s_add_u32 s66, s66, 55296
EX v64,v64
MM v[76:79],a[212:215],v[180:183],v[76:79]
s_min_u32 s66, s66, s85
EX v65,v65
MM v[76:79],a[216:219],v[196:199],v[76:79]
s_add_u32 s67, s67, 1024
TR a[150:151],v232 offset:45568
CV v64,v64,v65
MM v[76:79],a[220:223],v[212:215],v[76:79]
s_min_u32 s67, s67, s86
EX v66,v66
MM v[72:75],a[204:207],v[212:215],v[132:135]
EX v67,v67
MM v[72:75],a[200:203],v[196:199],v[72:75]
TR a[152:153],v233 offset:33280
CV v65,v66,v67
MM v[72:75],a[196:199],v[180:183],v[72:75]
EX v68,v68
MM v[72:75],a[192:195],v[164:167],v[72:75]
EX v69,v69
MM v[80:83],a[192:195],v[168:171],v[136:139]
TR a[154:155],v233 offset:37376
CV v66,v68,v69
MM v[80:83],a[196:199],v[184:187],v[80:83]
EX v70,v70
MM v[80:83],a[200:203],v[200:203],v[80:83]
EX v71,v71
MM v[80:83],a[204:207],v[216:219],v[80:83]
TR a[156:157],v233 offset:41472
CV v67,v70,v71
MM v[84:87],a[220:223],v[216:219],v[136:139]
EX v72,v72
MM v[84:87],a[216:219],v[200:203],v[84:87]
TR a[158:159],v233 offset:45568
TR a[160:161],v232 offset:33792
MM v[84:87],a[212:215],v[184:187],v[84:87]
EX v73,v73
MM v[84:87],a[208:211],v[168:171],v[84:87]
TR a[162:163],v232 offset:37888
CV v72,v72,v73
MM v[92:95],a[208:211],v[172:175],v[140:143]
EX v74,v74
MM v[92:95],a[212:215],v[188:191],v[92:95]
TR a[164:165],v232 offset:41984
TR a[166:167],v232 offset:46080
MM v[92:95],a[216:219],v[204:207],v[92:95]
EX v75,v75
MM v[92:95],a[220:223],v[220:223],v[92:95]
TR a[168:169],v233 offset:33792
CV v73,v74,v75
MM v[88:91],a[204:207],v[220:223],v[140:143]
EX v76,v76
MM v[88:91],a[200:203],v[204:207],v[88:91]
EX v77,v77
MM v[88:91],a[196:199],v[188:191],v[88:91]
TR a[170:171],v233 offset:37888
CV v74,v76,v77
MM v[88:91],a[192:195],v[172:175],v[88:91]
EX v78,v78
MM v[96:99],a[224:227],v[160:163],v[128:131]
TR a[172:173],v233 offset:41984
TR a[174:175],v233 offset:46080
MM v[96:99],a[228:231],v[176:179],v[96:99]
EX v79,v79
MM v[96:99],a[232:235],v[192:195],v[96:99]
TR a[176:177],v232 offset:34304
CV v75,v78,v79
MM v[96:99],a[236:239],v[208:211],v[96:99]
EX v80,v80
MM v[100:103],a[252:255],v[208:211],v[128:131]
EX v81,v81
MM v[100:103],a[248:251],v[192:195],v[100:103]
TR a[178:179],v232 offset:38400
CV v80,v80,v81
MM v[100:103],a[244:247],v[176:179],v[100:103]
EX v82,v82
MM v[100:103],a[240:243],v[160:163],v[100:103]
TR a[180:181],v232 offset:42496
TR a[182:183],v232 offset:46592
MM v[108:111],a[240:243],v[164:167],v[132:135]
EX v83,v83
MM v[108:111],a[244:247],v[180:183],v[108:111]
TR a[184:185],v233 offset:34304
CV v81,v82,v83
MM v[108:111],a[248:251],v[196:199],v[108:111]
EX v84,v84
MM v[108:111],a[252:255],v[212:215],v[108:111]
EX v85,v85
MM v[104:107],a[236:239],v[212:215],v[132:135]
TR a[186:187],v233 offset:38400
CV v82,v84,v85
MM v[104:107],a[232:235],v[196:199],v[104:107]
EX v86,v86
MM v[104:107],a[228:231],v[180:183],v[104:107]
TR a[188:189],v233 offset:42496
TR a[190:191],v233 offset:46592
MM v[104:107],a[224:227],v[164:167],v[104:107]
EX v87,v87
MM v[112:115],a[224:227],v[168:171],v[136:139]
CV v83,v86,v87
MM v[112:115],a[228:231],v[184:187],v[112:115]
EX v88,v88
MM v[112:115],a[232:235],v[200:203],v[112:115]
EX v89,v89
MM v[112:115],a[236:239],v[216:219],v[112:115]
EX v90,v90
MM v[116:119],a[252:255],v[216:219],v[136:139]
EX v91,v91
MM v[116:119],a[248:251],v[200:203],v[116:119]
EX v92,v92
MM v[116:119],a[244:247],v[184:187],v[116:119]
EX v93,v93
MM v[116:119],a[240:243],v[168:171],v[116:119]
EX v94,v94
MM v[124:127],a[240:243],v[172:175],v[140:143]
EX v95,v95
MM v[124:127],a[244:247],v[188:191],v[124:127]
EX v96,v96
MM v[124:127],a[248:251],v[204:207],v[124:127]
EX v97,v97
MM v[124:127],a[252:255],v[220:223],v[124:127]
EX v98,v98
MM v[120:123],a[236:239],v[220:223],v[140:143]
EX v99,v99
MM v[120:123],a[232:235],v[204:207],v[120:123]
EX v100,v100
MM v[120:123],a[228:231],v[188:191],v[120:123]
EX v101,v101
MM v[120:123],a[224:227],v[172:175],v[120:123]
s_waitcnt vmcnt(13) lgkmcnt(0)
s_barrier
MM a[0:3],a[128:131],v[0:3],a[0:3]
s_add_u32 m0, s65, 32768
CV v88,v88,v89
CV v89,v90,v91
MM a[0:3],a[132:135],v[4:7],a[0:3]
RD a[192:195],v224 offset:0
CV v90,v92,v93
MM a[8:11],a[132:135],v[12:15],a[8:11]
RD a[196:199],v225 offset:0
CV v91,v94,v95
MM a[8:11],a[128:131],v[8:11],a[8:11]
buffer_load_dwordx4 v228, s[8:11], s64 offen offset:0 lds
MM a[16:19],a[128:131],v[16:19],a[16:19]
RD a[200:203],v226 offset:0
CV v68,v96,v97
MM a[16:19],a[132:135],v[20:23],a[16:19]
RD a[204:207],v227 offset:0
CV v69,v98,v99
MM a[24:27],a[132:135],v[28:31],a[24:27]
buffer_load_dwordx4 v229, s[8:11], s64 offen offset:1024 lds
MM a[24:27],a[128:131],v[24:27],a[24:27]
RD a[208:211],v224 offset:4096
CV v70,v100,v101
MM a[28:31],a[136:139],v[24:27],a[28:31]
RD a[212:215],v225 offset:4096
RD a[216:219],v226 offset:4096
MM a[28:31],a[140:143],v[28:31],a[28:31]
buffer_load_dwordx4 v230, s[8:11], s64 offen offset:2048 lds
MM a[20:23],a[140:143],v[20:23],a[20:23]
RD a[220:223],v227 offset:4096
RD a[224:227],v224 offset:8192
MM a[20:23],a[136:139],v[16:19],a[20:23]
EX v102,v102
MM a[12:15],a[136:139],v[8:11],a[12:15]
buffer_load_dwordx4 v231, s[8:11], s64 offen offset:3072 lds
MM a[12:15],a[140:143],v[12:15],a[12:15]
s_add_u32 m0, s65, 98304
EX v103,v103
MM a[4:7],a[140:143],v[4:7],a[4:7]
RD a[228:231],v225 offset:8192
CV v71,v102,v103
MM a[4:7],a[136:139],v[0:3],a[4:7]
EX v104,v104
MM a[32:35],a[144:147],v[0:3],a[32:35]
EX v105,v105
MM a[32:35],a[148:151],v[4:7],a[32:35]
RD a[232:235],v226 offset:8192
CV v76,v104,v105
MM a[40:43],a[148:151],v[12:15],a[40:43]
EX v106,v106
MM a[40:43],a[144:147],v[8:11],a[40:43]
buffer_load_dwordx4 v235, s[12:15], s81 offen offset:0 lds
MM a[48:51],a[144:147],v[16:19],a[48:51]
EX v107,v107
MM a[48:51],a[148:151],v[20:23],a[48:51]
RD a[236:239],v227 offset:8192
CV v77,v106,v107
MM a[56:59],a[148:151],v[28:31],a[56:59]
EX v108,v108
MM a[56:59],a[144:147],v[24:27],a[56:59]
EX v109,v109
MM a[60:63],a[152:155],v[24:27],a[60:63]
RD a[240:243],v224 offset:12288
CV v78,v108,v109
MM a[60:63],a[156:159],v[28:31],a[60:63]
buffer_load_dwordx4 v235, s[12:15], s64 offen offset:1024 lds
MM a[52:55],a[156:159],v[20:23],a[52:55]
EX v110,v110
MM a[52:55],a[152:155],v[16:19],a[52:55]
EX v111,v111
MM a[44:47],a[152:155],v[8:11],a[44:47]
RD a[244:247],v225 offset:12288
CV v79,v110,v111
MM a[44:47],a[156:159],v[12:15],a[44:47]
EX v112,v112
MM a[36:39],a[156:159],v[4:7],a[36:39]
EX v113,v113
MM a[36:39],a[152:155],v[0:3],a[36:39]
RD a[248:251],v226 offset:12288
CV v84,v112,v113
MM a[64:67],a[160:163],v[0:3],a[64:67]
buffer_load_dwordx4 v235, s[12:15], s81 offen offset:2048 lds
MM a[64:67],a[164:167],v[4:7],a[64:67]
EX v114,v114
MM a[72:75],a[164:167],v[12:15],a[72:75]
EX v115,v115
MM a[72:75],a[160:163],v[8:11],a[72:75]
RD a[252:255],v227 offset:12288
CV v85,v114,v115
MM a[80:83],a[160:163],v[16:19],a[80:83]
EX v116,v116
MM a[80:83],a[164:167],v[20:23],a[80:83]
buffer_load_dwordx4 v235, s[12:15], s64 offen offset:3072 lds
MM a[88:91],a[164:167],v[28:31],a[88:91]
EX v117,v117
MM a[88:91],a[160:163],v[24:27],a[88:91]
EX v118,v118
MM a[92:95],a[168:171],v[24:27],a[92:95]
EX v119,v119
MM a[92:95],a[172:175],v[28:31],a[92:95]
EX v120,v120
MM a[84:87],a[172:175],v[20:23],a[84:87]
EX v121,v121
MM a[84:87],a[168:171],v[16:19],a[84:87]
EX v122,v122
MM a[76:79],a[168:171],v[8:11],a[76:79]
EX v123,v123
MM a[76:79],a[172:175],v[12:15],a[76:79]
EX v124,v124
MM a[68:71],a[172:175],v[4:7],a[68:71]
EX v125,v125
MM a[68:71],a[168:171],v[0:3],a[68:71]
EX v126,v126
MM a[96:99],a[176:179],v[0:3],a[96:99]
EX v127,v127
MM a[96:99],a[180:183],v[4:7],a[96:99]
CV v86,v116,v117
CV v87,v118,v119
MM a[104:107],a[180:183],v[12:15],a[104:107]
CV v92,v120,v121
CV v93,v122,v123
MM a[104:107],a[176:179],v[8:11],a[104:107]
CV v94,v124,v125
CV v95,v126,v127
MM a[112:115],a[176:179],v[16:19],a[112:115]
MM a[112:115],a[180:183],v[20:23],a[112:115]
MM a[120:123],a[180:183],v[28:31],a[120:123]
MM a[120:123],a[176:179],v[24:27],a[120:123]
MM a[124:127],a[184:187],v[24:27],a[124:127]
MM a[124:127],a[188:191],v[28:31],a[124:127]
MM a[116:119],a[188:191],v[20:23],a[116:119]
MM a[116:119],a[184:187],v[16:19],a[116:119]
MM a[108:111],a[184:187],v[8:11],a[108:111]
MM a[108:111],a[188:191],v[12:15],a[108:111]
MM a[100:103],a[188:191],v[4:7],a[100:103]
MM a[100:103],a[184:187],v[0:3],a[100:103]
v_smfmac_f32_16x16x64_bf16 v[236:239], v[144:147], v[64:71], v234
v_smfmac_f32_16x16x64_bf16 v[236:239], v[148:151], v[72:79], v234
v_smfmac_f32_16x16x64_bf16 v[236:239], v[152:155], v[80:87], v234
v_smfmac_f32_16x16x64_bf16 v[236:239], v[156:159], v[88:95], v234
s_waitcnt lgkmcnt(0)
MM v[0:3],a[192:195],v[160:163],v[128:131]
s_add_u32 s64, s64, 16384
TR a[128:129],v232 offset:49152
s_add_u32 s81, s64, 896
MM v[0:3],a[196:199],v[176:179],v[0:3]
TR a[130:131],v232 offset:53248
TR a[132:133],v232 offset:57344
MM v[0:3],a[200:203],v[192:195],v[0:3]
TR a[134:135],v232 offset:61440
TR a[136:137],v233 offset:49152
MM v[0:3],a[204:207],v[208:211],v[0:3]
TR a[138:139],v233 offset:53248
v_add_f32 v247, v236, v237
MM v[4:7],a[220:223],v[208:211],v[128:131]
TR a[140:141],v233 offset:57344
v_cmp_nge_f32 vcc, s71, v247
MM v[4:7],a[216:219],v[192:195],v[4:7]
s_cbranch_vccnz LSITE11_%=
LBACK11_%=:
TR a[142:143],v233 offset:61440
TR a[144:145],v232 offset:49664
MM v[4:7],a[212:215],v[176:179],v[4:7]
s_mov_b32 m0, s67
TR a[146:147],v232 offset:53760
TR a[148:149],v232 offset:57856
MM v[4:7],a[208:211],v[160:163],v[4:7]
)ASM"
R"ASM(
buffer_load_dwordx4 v252, s[36:39], s66 offen offset:0 lds
MM v[12:15],a[208:211],v[164:167],v[132:135]
s_add_u32 s66, s66, 55296
EX v0,v0
MM v[12:15],a[212:215],v[180:183],v[12:15]
s_min_u32 s66, s66, s85
EX v1,v1
MM v[12:15],a[216:219],v[196:199],v[12:15]
s_add_u32 s67, s67, 1024
TR a[150:151],v232 offset:61952
CV v0,v0,v1
MM v[12:15],a[220:223],v[212:215],v[12:15]
s_min_u32 s67, s67, s86
EX v2,v2
MM v[8:11],a[204:207],v[212:215],v[132:135]
EX v3,v3
s_nop 0
LPG8_%=:
s_cbranch_execz LPG12_%=
MM v[8:11],a[200:203],v[196:199],v[8:11]
TR a[152:153],v233 offset:49664
CV v1,v2,v3
MM v[8:11],a[196:199],v[180:183],v[8:11]
EX v4,v4
MM v[8:11],a[192:195],v[164:167],v[8:11]
EX v5,v5
MM v[16:19],a[192:195],v[168:171],v[136:139]
TR a[154:155],v233 offset:53760
CV v2,v4,v5
MM v[16:19],a[196:199],v[184:187],v[16:19]
EX v6,v6
MM v[16:19],a[200:203],v[200:203],v[16:19]
EX v7,v7
MM v[16:19],a[204:207],v[216:219],v[16:19]
TR a[156:157],v233 offset:57856
CV v3,v6,v7
MM v[20:23],a[220:223],v[216:219],v[136:139]
EX v8,v8
MM v[20:23],a[216:219],v[200:203],v[20:23]
TR a[158:159],v233 offset:61952
TR a[160:161],v232 offset:50176
MM v[20:23],a[212:215],v[184:187],v[20:23]
EX v9,v9
MM v[20:23],a[208:211],v[168:171],v[20:23]
TR a[162:163],v232 offset:54272
CV v8,v8,v9
MM v[28:31],a[208:211],v[172:175],v[140:143]
EX v10,v10
MM v[28:31],a[212:215],v[188:191],v[28:31]
TR a[164:165],v232 offset:58368
TR a[166:167],v232 offset:62464
MM v[28:31],a[216:219],v[204:207],v[28:31]
EX v11,v11
MM v[28:31],a[220:223],v[220:223],v[28:31]
TR a[168:169],v233 offset:50176
CV v9,v10,v11
MM v[24:27],a[204:207],v[220:223],v[140:143]
EX v12,v12
MM v[24:27],a[200:203],v[204:207],v[24:27]
EX v13,v13
MM v[24:27],a[196:199],v[188:191],v[24:27]
TR a[170:171],v233 offset:54272
CV v10,v12,v13
MM v[24:27],a[192:195],v[172:175],v[24:27]
EX v14,v14
MM v[32:35],a[224:227],v[160:163],v[128:131]
TR a[172:173],v233 offset:58368
TR a[174:175],v233 offset:62464
MM v[32:35],a[228:231],v[176:179],v[32:35]
EX v15,v15
MM v[32:35],a[232:235],v[192:195],v[32:35]
TR a[176:177],v232 offset:50688
CV v11,v14,v15
MM v[32:35],a[236:239],v[208:211],v[32:35]
EX v16,v16
MM v[36:39],a[252:255],v[208:211],v[128:131]
EX v17,v17
MM v[36:39],a[248:251],v[192:195],v[36:39]
TR a[178:179],v232 offset:54784
CV v16,v16,v17
MM v[36:39],a[244:247],v[176:179],v[36:39]
EX v18,v18
MM v[36:39],a[240:243],v[160:163],v[36:39]
TR a[180:181],v232 offset:58880
TR a[182:183],v232 offset:62976
MM v[44:47],a[240:243],v[164:167],v[132:135]
EX v19,v19
MM v[44:47],a[244:247],v[180:183],v[44:47]
TR a[184:185],v233 offset:50688
CV v17,v18,v19
MM v[44:47],a[248:251],v[196:199],v[44:47]
EX v20,v20
MM v[44:47],a[252:255],v[212:215],v[44:47]
EX v21,v21
MM v[40:43],a[236:239],v[212:215],v[132:135]
TR a[186:187],v233 offset:54784
CV v18,v20,v21
MM v[40:43],a[232:235],v[196:199],v[40:43]
EX v22,v22
MM v[40:43],a[228:231],v[180:183],v[40:43]
TR a[188:189],v233 offset:58880
TR a[190:191],v233 offset:62976
MM v[40:43],a[224:227],v[164:167],v[40:43]
EX v23,v23
MM v[48:51],a[224:227],v[168:171],v[136:139]
CV v19,v22,v23
MM v[48:51],a[228:231],v[184:187],v[48:51]
EX v24,v24
MM v[48:51],a[232:235],v[200:203],v[48:51]
EX v25,v25
MM v[48:51],a[236:239],v[216:219],v[48:51]
EX v26,v26
MM v[52:55],a[252:255],v[216:219],v[136:139]
EX v27,v27
MM v[52:55],a[248:251],v[200:203],v[52:55]
EX v28,v28
MM v[52:55],a[244:247],v[184:187],v[52:55]
EX v29,v29
MM v[52:55],a[240:243],v[168:171],v[52:55]
EX v30,v30
MM v[60:63],a[240:243],v[172:175],v[140:143]
EX v31,v31
MM v[60:63],a[244:247],v[188:191],v[60:63]
EX v32,v32
MM v[60:63],a[248:251],v[204:207],v[60:63]
EX v33,v33
MM v[60:63],a[252:255],v[220:223],v[60:63]
EX v34,v34
MM v[56:59],a[236:239],v[220:223],v[140:143]
EX v35,v35
MM v[56:59],a[232:235],v[204:207],v[56:59]
EX v36,v36
MM v[56:59],a[228:231],v[188:191],v[56:59]
EX v37,v37
MM v[56:59],a[224:227],v[172:175],v[56:59]
s_waitcnt vmcnt(13) lgkmcnt(0)
s_barrier
MM a[0:3],a[128:131],v[64:67],a[0:3]
s_add_u32 m0, s65, 49152
CV v24,v24,v25
CV v25,v26,v27
MM a[0:3],a[132:135],v[68:71],a[0:3]
RD a[192:195],v224 offset:16384
CV v26,v28,v29
MM a[8:11],a[132:135],v[76:79],a[8:11]
RD a[196:199],v225 offset:16384
CV v27,v30,v31
MM a[8:11],a[128:131],v[72:75],a[8:11]
buffer_load_dwordx4 v228, s[8:11], s64 offen offset:0 lds
MM a[16:19],a[128:131],v[80:83],a[16:19]
RD a[200:203],v226 offset:16384
CV v4,v32,v33
MM a[16:19],a[132:135],v[84:87],a[16:19]
RD a[204:207],v227 offset:16384
CV v5,v34,v35
MM a[24:27],a[132:135],v[92:95],a[24:27]
buffer_load_dwordx4 v229, s[8:11], s64 offen offset:1024 lds
MM a[24:27],a[128:131],v[88:91],a[24:27]
RD a[208:211],v224 offset:20480
CV v6,v36,v37
MM a[28:31],a[136:139],v[88:91],a[28:31]
RD a[212:215],v225 offset:20480
RD a[216:219],v226 offset:20480
MM a[28:31],a[140:143],v[92:95],a[28:31]
buffer_load_dwordx4 v230, s[8:11], s64 offen offset:2048 lds
MM a[20:23],a[140:143],v[84:87],a[20:23]
RD a[220:223],v227 offset:20480
RD a[224:227],v224 offset:24576
MM a[20:23],a[136:139],v[80:83],a[20:23]
EX v38,v38
MM a[12:15],a[136:139],v[72:75],a[12:15]
buffer_load_dwordx4 v231, s[8:11], s64 offen offset:3072 lds
MM a[12:15],a[140:143],v[76:79],a[12:15]
s_add_u32 m0, s65, 114688
EX v39,v39
MM a[4:7],a[140:143],v[68:71],a[4:7]
RD a[228:231],v225 offset:24576
CV v7,v38,v39
MM a[4:7],a[136:139],v[64:67],a[4:7]
EX v40,v40
MM a[32:35],a[144:147],v[64:67],a[32:35]
EX v41,v41
MM a[32:35],a[148:151],v[68:71],a[32:35]
RD a[232:235],v226 offset:24576
CV v12,v40,v41
MM a[40:43],a[148:151],v[76:79],a[40:43]
EX v42,v42
MM a[40:43],a[144:147],v[72:75],a[40:43]
buffer_load_dwordx4 v235, s[12:15], s81 offen offset:0 lds
MM a[48:51],a[144:147],v[80:83],a[48:51]
EX v43,v43
MM a[48:51],a[148:151],v[84:87],a[48:51]
RD a[236:239],v227 offset:24576
CV v13,v42,v43
MM a[56:59],a[148:151],v[92:95],a[56:59]
EX v44,v44
MM a[56:59],a[144:147],v[88:91],a[56:59]
EX v45,v45
MM a[60:63],a[152:155],v[88:91],a[60:63]
RD a[240:243],v224 offset:28672
CV v14,v44,v45
MM a[60:63],a[156:159],v[92:95],a[60:63]
buffer_load_dwordx4 v235, s[12:15], s64 offen offset:1024 lds
MM a[52:55],a[156:159],v[84:87],a[52:55]
EX v46,v46
MM a[52:55],a[152:155],v[80:83],a[52:55]
EX v47,v47
MM a[44:47],a[152:155],v[72:75],a[44:47]
RD a[244:247],v225 offset:28672
CV v15,v46,v47
MM a[44:47],a[156:159],v[76:79],a[44:47]
EX v48,v48
MM a[36:39],a[156:159],v[68:71],a[36:39]
EX v49,v49
MM a[36:39],a[152:155],v[64:67],a[36:39]
RD a[248:251],v226 offset:28672
CV v20,v48,v49
MM a[64:67],a[160:163],v[64:67],a[64:67]
buffer_load_dwordx4 v235, s[12:15], s81 offen offset:2048 lds
MM a[64:67],a[164:167],v[68:71],a[64:67]
EX v50,v50
MM a[72:75],a[164:167],v[76:79],a[72:75]
EX v51,v51
MM a[72:75],a[160:163],v[72:75],a[72:75]
RD a[252:255],v227 offset:28672
CV v21,v50,v51
MM a[80:83],a[160:163],v[80:83],a[80:83]
EX v52,v52
MM a[80:83],a[164:167],v[84:87],a[80:83]
buffer_load_dwordx4 v235, s[12:15], s64 offen offset:3072 lds
MM a[88:91],a[164:167],v[92:95],a[88:91]
EX v53,v53
MM a[88:91],a[160:163],v[88:91],a[88:91]
EX v54,v54
MM a[92:95],a[168:171],v[88:91],a[92:95]
EX v55,v55
MM a[92:95],a[172:175],v[92:95],a[92:95]
EX v56,v56
MM a[84:87],a[172:175],v[84:87],a[84:87]
EX v57,v57
MM a[84:87],a[168:171],v[80:83],a[84:87]
EX v58,v58
MM a[76:79],a[168:171],v[72:75],a[76:79]
EX v59,v59
MM a[76:79],a[172:175],v[76:79],a[76:79]
EX v60,v60
MM a[68:71],a[172:175],v[68:71],a[68:71]
EX v61,v61
MM a[68:71],a[168:171],v[64:67],a[68:71]
EX v62,v62
MM a[96:99],a[176:179],v[64:67],a[96:99]
EX v63,v63
MM a[96:99],a[180:183],v[68:71],a[96:99]
CV v22,v52,v53
CV v23,v54,v55
MM a[104:107],a[180:183],v[76:79],a[104:107]
CV v28,v56,v57
CV v29,v58,v59
MM a[104:107],a[176:179],v[72:75],a[104:107]
CV v30,v60,v61
CV v31,v62,v63
MM a[112:115],a[176:179],v[80:83],a[112:115]
MM a[112:115],a[180:183],v[84:87],a[112:115]
MM a[120:123],a[180:183],v[92:95],a[120:123]
MM a[120:123],a[176:179],v[88:91],a[120:123]
MM a[124:127],a[184:187],v[88:91],a[124:127]
MM a[124:127],a[188:191],v[92:95],a[124:127]
MM a[116:119],a[188:191],v[84:87],a[116:119]
MM a[116:119],a[184:187],v[80:83],a[116:119]
MM a[108:111],a[184:187],v[72:75],a[108:111]
MM a[108:111],a[188:191],v[76:79],a[108:111]
MM a[100:103],a[188:191],v[68:71],a[100:103]
MM a[100:103],a[184:187],v[64:67],a[100:103]
v_smfmac_f32_16x16x64_bf16 v[236:239], v[144:147], v[0:7], v234
v_smfmac_f32_16x16x64_bf16 v[236:239], v[148:151], v[8:15], v234
v_smfmac_f32_16x16x64_bf16 v[236:239], v[152:155], v[16:23], v234
v_smfmac_f32_16x16x64_bf16 v[236:239], v[156:159], v[24:31], v234
s_waitcnt lgkmcnt(0)
s_sub_u32 s70, s70, 1
s_cmp_lg_u32 s70, 0
s_cbranch_scc1 LLA_%=
MM v[64:67],a[192:195],v[160:163],v[128:131]
s_add_u32 s64, s64, 16384
TR a[128:129],v232 offset:0
s_add_u32 s81, s64, 896
MM v[64:67],a[196:199],v[176:179],v[64:67]
TR a[130:131],v232 offset:4096
TR a[132:133],v232 offset:8192
MM v[64:67],a[200:203],v[192:195],v[64:67]
TR a[134:135],v232 offset:12288
TR a[136:137],v233 offset:0
MM v[64:67],a[204:207],v[208:211],v[64:67]
TR a[138:139],v233 offset:4096
v_add_f32 v246, v236, v237
MM v[68:71],a[220:223],v[208:211],v[128:131]
TR a[140:141],v233 offset:8192
v_cmp_nge_f32 vcc, s71, v246
MM v[68:71],a[216:219],v[192:195],v[68:71]
s_cbranch_vccnz LSITE12_%=
LBACK12_%=:
TR a[142:143],v233 offset:12288
TR a[144:145],v232 offset:512
MM v[68:71],a[212:215],v[176:179],v[68:71]
v_lshrrev_b32 v52, 1, v248
v_and_b32 v52, 56, v52
MM v[68:71],a[208:211],v[160:163],v[68:71]
s_mov_b32 s90, 86016
buffer_load_dwordx2 v[48:49], v52, s[20:23], s90 offen offset:0 sc1
MM v[76:79],a[208:211],v[164:167],v[132:135]
EX v64,v64
MM v[76:79],a[212:215],v[180:183],v[76:79]
EX v65,v65
MM v[76:79],a[216:219],v[196:199],v[76:79]
TR a[146:147],v232 offset:4608
CV v64,v64,v65
MM v[76:79],a[220:223],v[212:215],v[76:79]
EX v66,v66
MM v[72:75],a[204:207],v[212:215],v[132:135]
EX v67,v67
MM v[72:75],a[200:203],v[196:199],v[72:75]
TR a[148:149],v232 offset:8704
CV v65,v66,v67
MM v[72:75],a[196:199],v[180:183],v[72:75]
EX v68,v68
MM v[72:75],a[192:195],v[164:167],v[72:75]
TR a[150:151],v232 offset:12800
TR a[152:153],v233 offset:512
MM v[80:83],a[192:195],v[168:171],v[136:139]
EX v69,v69
MM v[80:83],a[196:199],v[184:187],v[80:83]
TR a[154:155],v233 offset:4608
CV v66,v68,v69
MM v[80:83],a[200:203],v[200:203],v[80:83]
EX v70,v70
MM v[80:83],a[204:207],v[216:219],v[80:83]
EX v71,v71
MM v[84:87],a[220:223],v[216:219],v[136:139]
TR a[156:157],v233 offset:8704
CV v67,v70,v71
MM v[84:87],a[216:219],v[200:203],v[84:87]
EX v72,v72
MM v[84:87],a[212:215],v[184:187],v[84:87]
TR a[158:159],v233 offset:12800
TR a[160:161],v232 offset:1024
MM v[84:87],a[208:211],v[168:171],v[84:87]
EX v73,v73
MM v[92:95],a[208:211],v[172:175],v[140:143]
TR a[162:163],v232 offset:5120
CV v72,v72,v73
MM v[92:95],a[212:215],v[188:191],v[92:95]
EX v74,v74
MM v[92:95],a[216:219],v[204:207],v[92:95]
EX v75,v75
MM v[92:95],a[220:223],v[220:223],v[92:95]
TR a[164:165],v232 offset:9216
CV v73,v74,v75
MM v[88:91],a[204:207],v[220:223],v[140:143]
EX v76,v76
MM v[88:91],a[200:203],v[204:207],v[88:91]
TR a[166:167],v232 offset:13312
TR a[168:169],v233 offset:1024
MM v[88:91],a[196:199],v[188:191],v[88:91]
EX v77,v77
MM v[88:91],a[192:195],v[172:175],v[88:91]
TR a[170:171],v233 offset:5120
CV v74,v76,v77
MM v[96:99],a[224:227],v[160:163],v[128:131]
EX v78,v78
MM v[96:99],a[228:231],v[176:179],v[96:99]
EX v79,v79
MM v[96:99],a[232:235],v[192:195],v[96:99]
TR a[172:173],v233 offset:9216
CV v75,v78,v79
MM v[96:99],a[236:239],v[208:211],v[96:99]
TR a[174:175],v233 offset:13312
TR a[176:177],v232 offset:1536
MM v[100:103],a[252:255],v[208:211],v[128:131]
EX v80,v80
MM v[100:103],a[248:251],v[192:195],v[100:103]
EX v81,v81
MM v[100:103],a[244:247],v[176:179],v[100:103]
TR a[178:179],v232 offset:5632
CV v80,v80,v81
MM v[100:103],a[240:243],v[160:163],v[100:103]
EX v82,v82
MM v[108:111],a[240:243],v[164:167],v[132:135]
TR a[180:181],v232 offset:9728
TR a[182:183],v232 offset:13824
MM v[108:111],a[244:247],v[180:183],v[108:111]
EX v83,v83
MM v[108:111],a[248:251],v[196:199],v[108:111]
TR a[184:185],v233 offset:1536
CV v81,v82,v83
MM v[108:111],a[252:255],v[212:215],v[108:111]
)ASM"
R"ASM(
EX v84,v84
MM v[104:107],a[236:239],v[212:215],v[132:135]
EX v85,v85
MM v[104:107],a[232:235],v[196:199],v[104:107]
TR a[186:187],v233 offset:5632
CV v82,v84,v85
MM v[104:107],a[228:231],v[180:183],v[104:107]
EX v86,v86
MM v[104:107],a[224:227],v[164:167],v[104:107]
TR a[188:189],v233 offset:9728
TR a[190:191],v233 offset:13824
MM v[112:115],a[224:227],v[168:171],v[136:139]
EX v87,v87
MM v[112:115],a[228:231],v[184:187],v[112:115]
CV v83,v86,v87
MM v[112:115],a[232:235],v[200:203],v[112:115]
EX v88,v88
MM v[112:115],a[236:239],v[216:219],v[112:115]
EX v89,v89
MM v[116:119],a[252:255],v[216:219],v[136:139]
EX v90,v90
MM v[116:119],a[248:251],v[200:203],v[116:119]
EX v91,v91
MM v[116:119],a[244:247],v[184:187],v[116:119]
EX v92,v92
MM v[116:119],a[240:243],v[168:171],v[116:119]
EX v93,v93
MM v[124:127],a[240:243],v[172:175],v[140:143]
EX v94,v94
MM v[124:127],a[244:247],v[188:191],v[124:127]
EX v95,v95
MM v[124:127],a[248:251],v[204:207],v[124:127]
EX v96,v96
MM v[124:127],a[252:255],v[220:223],v[124:127]
EX v97,v97
MM v[120:123],a[236:239],v[220:223],v[140:143]
EX v98,v98
MM v[120:123],a[232:235],v[204:207],v[120:123]
EX v99,v99
MM v[120:123],a[228:231],v[188:191],v[120:123]
EX v100,v100
MM v[120:123],a[224:227],v[172:175],v[120:123]
s_waitcnt vmcnt(14) lgkmcnt(0)
s_barrier
MM a[0:3],a[128:131],v[0:3],a[0:3]
s_waitcnt vmcnt(0)
v_cmp_ne_u32 vcc, s69, v49
s_cbranch_vccnz LRIX_%=
LRIXb_%=:
MM a[0:3],a[132:135],v[4:7],a[0:3]
s_add_u32 m0, s65, 0
v_readlane_b32 s52, v48, 0
v_readlane_b32 s53, v48, 1
MM a[8:11],a[132:135],v[12:15],a[8:11]
v_readlane_b32 s54, v48, 2
v_readlane_b32 s55, v48, 3
MM a[8:11],a[128:131],v[8:11],a[8:11]
v_readlane_b32 s56, v48, 4
v_readlane_b32 s57, v48, 5
MM a[16:19],a[128:131],v[16:19],a[16:19]
v_readlane_b32 s58, v48, 6
v_readlane_b32 s59, v48, 7
MM a[16:19],a[132:135],v[20:23],a[16:19]
s_mul_i32 s91, %[swave], 3072
CV v88,v88,v89
CV v89,v90,v91
MM a[24:27],a[132:135],v[28:31],a[24:27]
s_add_u32 s91, s91, %[slds]
RD a[192:195],v224 offset:32768
CV v90,v92,v93
MM a[24:27],a[128:131],v[24:27],a[24:27]
s_bfe_u32 s90, s48, 131077
RD a[196:199],v225 offset:32768
CV v91,v94,v95
MM a[28:31],a[136:139],v[24:27],a[28:31]
s_mul_i32 s90, s90, 320
buffer_load_dwordx4 v228, s[8:11], s64 offen offset:0 lds
MM a[28:31],a[140:143],v[28:31],a[28:31]
s_add_u32 s90, s90, 131072
RD a[200:203],v226 offset:32768
CV v68,v96,v97
MM a[20:23],a[140:143],v[20:23],a[20:23]
s_mul_i32 s66, s52, 1280
CV v69,v98,v99
MM a[20:23],a[136:139],v[16:19],a[20:23]
s_add_u32 s66, s66, s90
s_add_u32 m0, s91, 151552
s_mov_b64 exec, 0xfffff
buffer_load_dwordx4 v248, s[20:23], s66 offen offset:0 sc1 lds
s_mov_b64 exec, -1
MM a[12:15],a[136:139],v[8:11],a[12:15]
s_mul_i32 s66, s53, 1280
s_add_u32 m0, s65, 0
s_nop 0
buffer_load_dwordx4 v229, s[8:11], s64 offen offset:1024 lds
MM a[12:15],a[140:143],v[12:15],a[12:15]
s_add_u32 s66, s66, s90
EX v101,v101
MM a[4:7],a[140:143],v[4:7],a[4:7]
RD a[204:207],v227 offset:32768
CV v70,v100,v101
MM a[4:7],a[136:139],v[0:3],a[4:7]
s_add_u32 m0, s91, 151872
s_mov_b64 exec, 0xfffff
buffer_load_dwordx4 v248, s[20:23], s66 offen offset:0 sc1 lds
s_mov_b64 exec, -1
MM a[32:35],a[144:147],v[0:3],a[32:35]
s_mul_i32 s66, s54, 1280
EX v102,v102
MM a[32:35],a[148:151],v[4:7],a[32:35]
s_add_u32 s66, s66, s90
EX v103,v103
MM a[40:43],a[148:151],v[12:15],a[40:43]
s_add_u32 m0, s91, 152192
s_mov_b64 exec, 0xfffff
buffer_load_dwordx4 v248, s[20:23], s66 offen offset:0 sc1 lds
s_mov_b64 exec, -1
MM a[40:43],a[144:147],v[8:11],a[40:43]
s_mul_i32 s66, s55, 1280
RD a[208:211],v224 offset:36864
CV v71,v102,v103
MM a[48:51],a[144:147],v[16:19],a[48:51]
s_add_u32 s66, s66, s90
EX v104,v104
MM a[48:51],a[148:151],v[20:23],a[48:51]
s_add_u32 m0, s91, 152512
s_mov_b64 exec, 0xfffff
buffer_load_dwordx4 v248, s[20:23], s66 offen offset:0 sc1 lds
s_mov_b64 exec, -1
MM a[56:59],a[148:151],v[28:31],a[56:59]
s_mul_i32 s66, s56, 1280
EX v105,v105
MM a[56:59],a[144:147],v[24:27],a[56:59]
s_add_u32 s66, s66, s90
EX v106,v106
MM a[60:63],a[152:155],v[24:27],a[60:63]
s_add_u32 m0, s91, 152832
s_mov_b64 exec, 0xfffff
buffer_load_dwordx4 v248, s[20:23], s66 offen offset:0 sc1 lds
s_mov_b64 exec, -1
MM a[60:63],a[156:159],v[28:31],a[60:63]
s_mul_i32 s66, s57, 1280
RD a[212:215],v225 offset:36864
CV v76,v104,v105
MM a[52:55],a[156:159],v[20:23],a[52:55]
s_add_u32 s66, s66, s90
EX v107,v107
MM a[52:55],a[152:155],v[16:19],a[52:55]
s_add_u32 m0, s91, 153152
s_mov_b64 exec, 0xfffff
buffer_load_dwordx4 v248, s[20:23], s66 offen offset:0 sc1 lds
s_mov_b64 exec, -1
MM a[44:47],a[152:155],v[8:11],a[44:47]
s_mul_i32 s66, s58, 1280
EX v108,v108
MM a[44:47],a[156:159],v[12:15],a[44:47]
s_add_u32 s66, s66, s90
EX v109,v109
MM a[36:39],a[156:159],v[4:7],a[36:39]
s_add_u32 m0, s91, 153472
s_mov_b64 exec, 0xfffff
buffer_load_dwordx4 v248, s[20:23], s66 offen offset:0 sc1 lds
s_mov_b64 exec, -1
MM a[36:39],a[152:155],v[0:3],a[36:39]
s_mul_i32 s66, s59, 1280
RD a[216:219],v226 offset:36864
CV v77,v106,v107
MM a[64:67],a[160:163],v[0:3],a[64:67]
s_add_u32 s66, s66, s90
EX v110,v110
MM a[64:67],a[164:167],v[4:7],a[64:67]
s_add_u32 m0, s91, 153792
s_mov_b64 exec, 0xfffff
buffer_load_dwordx4 v248, s[20:23], s66 offen offset:0 sc1 lds
s_mov_b64 exec, -1
MM a[72:75],a[164:167],v[12:15],a[72:75]
EX v111,v111
MM a[72:75],a[160:163],v[8:11],a[72:75]
RD a[220:223],v227 offset:36864
CV v78,v108,v109
MM a[80:83],a[160:163],v[16:19],a[80:83]
s_add_u32 m0, s65, 0
s_nop 0
buffer_load_dwordx4 v230, s[8:11], s64 offen offset:2048 lds
MM a[80:83],a[164:167],v[20:23],a[80:83]
EX v112,v112
MM a[88:91],a[164:167],v[28:31],a[88:91]
EX v113,v113
MM a[88:91],a[160:163],v[24:27],a[88:91]
buffer_load_dwordx4 v231, s[8:11], s64 offen offset:3072 lds
MM a[92:95],a[168:171],v[24:27],a[92:95]
s_add_u32 m0, s65, 65536
EX v114,v114
MM a[92:95],a[172:175],v[28:31],a[92:95]
RD a[224:227],v224 offset:40960
CV v79,v110,v111
MM a[84:87],a[172:175],v[20:23],a[84:87]
EX v115,v115
MM a[84:87],a[168:171],v[16:19],a[84:87]
EX v116,v116
MM a[76:79],a[168:171],v[8:11],a[76:79]
buffer_load_dwordx4 v235, s[12:15], s81 offen offset:0 lds
MM a[76:79],a[172:175],v[12:15],a[76:79]
EX v117,v117
MM a[68:71],a[172:175],v[4:7],a[68:71]
RD a[228:231],v225 offset:40960
CV v84,v112,v113
MM a[68:71],a[168:171],v[0:3],a[68:71]
EX v118,v118
MM a[96:99],a[176:179],v[0:3],a[96:99]
RD a[232:235],v226 offset:40960
CV v85,v114,v115
MM a[96:99],a[180:183],v[4:7],a[96:99]
EX v119,v119
MM a[104:107],a[180:183],v[12:15],a[104:107]
s_nop 0
LPG9_%=:
s_cbranch_execz LTR0_%=
RD a[236:239],v227 offset:40960
CV v86,v116,v117
MM a[104:107],a[176:179],v[8:11],a[104:107]
EX v120,v120
MM a[112:115],a[176:179],v[16:19],a[112:115]
buffer_load_dwordx4 v235, s[12:15], s64 offen offset:1024 lds
MM a[112:115],a[180:183],v[20:23],a[112:115]
EX v121,v121
MM a[120:123],a[180:183],v[28:31],a[120:123]
EX v122,v122
MM a[120:123],a[176:179],v[24:27],a[120:123]
RD a[240:243],v224 offset:45056
CV v87,v118,v119
MM a[124:127],a[184:187],v[24:27],a[124:127]
EX v123,v123
MM a[124:127],a[188:191],v[28:31],a[124:127]
RD a[244:247],v225 offset:45056
CV v92,v120,v121
MM a[116:119],a[188:191],v[20:23],a[116:119]
EX v124,v124
MM a[116:119],a[184:187],v[16:19],a[116:119]
buffer_load_dwordx4 v235, s[12:15], s81 offen offset:2048 lds
MM a[108:111],a[184:187],v[8:11],a[108:111]
EX v125,v125
MM a[108:111],a[188:191],v[12:15],a[108:111]
RD a[248:251],v226 offset:45056
CV v93,v122,v123
MM a[100:103],a[188:191],v[4:7],a[100:103]
EX v126,v126
MM a[100:103],a[184:187],v[0:3],a[100:103]
EX v127,v127
v_smfmac_f32_16x16x64_bf16 v[236:239], v[144:147], v[64:71], v234
RD a[252:255],v227 offset:45056
v_smfmac_f32_16x16x64_bf16 v[236:239], v[148:151], v[72:79], v234
CV v94,v124,v125
v_smfmac_f32_16x16x64_bf16 v[236:239], v[152:155], v[80:87], v234
CV v95,v126,v127
s_nop 1
v_smfmac_f32_16x16x64_bf16 v[236:239], v[156:159], v[88:95], v234
s_waitcnt lgkmcnt(0)
buffer_load_dwordx4 v235, s[12:15], s64 offen offset:3072 lds
MM v[0:3],a[192:195],v[160:163],v[128:131]
s_add_u32 s64, s64, 16384
TR a[128:129],v232 offset:16384
s_add_u32 s81, s64, 896
MM v[0:3],a[196:199],v[176:179],v[0:3]
TR a[130:131],v232 offset:20480
TR a[132:133],v232 offset:24576
MM v[0:3],a[200:203],v[192:195],v[0:3]
TR a[134:135],v232 offset:28672
TR a[136:137],v233 offset:16384
MM v[0:3],a[204:207],v[208:211],v[0:3]
TR a[138:139],v233 offset:20480
v_add_f32 v247, v236, v237
MM v[4:7],a[220:223],v[208:211],v[128:131]
TR a[140:141],v233 offset:24576
v_cmp_nge_f32 vcc, s71, v247
MM v[4:7],a[216:219],v[192:195],v[4:7]
s_cbranch_vccnz LSITE13_%=
LBACK13_%=:
TR a[142:143],v233 offset:28672
TR a[144:145],v232 offset:16896
MM v[4:7],a[212:215],v[176:179],v[4:7]
v_lshrrev_b32 v127, 8, v248
v_lshlrev_b32 v124, 9, v127
MM v[4:7],a[208:211],v[160:163],v[4:7]
v_and_b32 v126, 192, v248
v_add_u32 v124, v124, v126
MM v[12:15],a[208:211],v[164:167],v[132:135]
v_bfe_u32 v126, v248, 4, 2
v_lshl_add_u32 v124, v126, 3, v124
MM v[12:15],a[212:215],v[180:183],v[12:15]
s_lshr_b32 s76, %[swave], 1
EX v0,v0
MM v[12:15],a[216:219],v[196:199],v[12:15]
s_mul_i32 s76, s76, 10240
EX v1,v1
MM v[12:15],a[220:223],v[212:215],v[12:15]
s_and_b32 s77, %[swave], 1
TR a[146:147],v232 offset:20992
CV v0,v0,v1
MM v[8:11],a[204:207],v[212:215],v[132:135]
s_lshl_b32 s77, s77, 5
EX v2,v2
MM v[8:11],a[200:203],v[196:199],v[8:11]
s_add_u32 s76, s76, s77
EX v3,v3
MM v[8:11],a[196:199],v[180:183],v[8:11]
s_add_u32 s76, s76, %[slds]
TR a[148:149],v232 offset:25088
CV v1,v2,v3
MM v[8:11],a[192:195],v[164:167],v[8:11]
s_add_u32 s76, s76, 131072
v_add_u32 v124, s76, v124
v_bfe_u32 v126, v248, 4, 3
MM v[16:19],a[192:195],v[168:171],v[136:139]
v_mul_u32_u24 v125, 320, v126
v_lshl_add_u32 v125, v127, 4, v125
MM v[16:19],a[196:199],v[184:187],v[16:19]
s_mul_i32 s76, %[swave], 3072
EX v4,v4
MM v[16:19],a[200:203],v[200:203],v[16:19]
s_add_u32 s76, s76, %[slds]
EX v5,v5
MM v[16:19],a[204:207],v[216:219],v[16:19]
s_add_u32 s76, s76, 151552
v_add_u32 v125, s76, v125
v_lshl_add_u32 v126, v127, 3, v126
MM v[20:23],a[220:223],v[216:219],v[136:139]
v_lshlrev_b32 v126, 4, v126
CV v2,v4,v5
MM v[20:23],a[216:219],v[200:203],v[20:23]
s_lshr_b32 s76, s48, 7
EX v6,v6
MM v[20:23],a[212:215],v[184:187],v[20:23]
s_lshl_b32 s76, s76, 5
TR a[150:151],v232 offset:29184
TR a[152:153],v233 offset:16896
MM v[20:23],a[208:211],v[168:171],v[20:23]
s_and_b32 s77, s48, 16
EX v7,v7
MM v[28:31],a[208:211],v[172:175],v[140:143]
s_add_u32 s76, s76, s77
EX v8,v8
MM v[28:31],a[212:215],v[188:191],v[28:31]
s_bfe_u32 s77, s48, 131077
TR a[154:155],v233 offset:20992
CV v3,v6,v7
MM v[28:31],a[216:219],v[204:207],v[28:31]
s_lshl_b32 s77, s77, 2
EX v9,v9
MM v[28:31],a[220:223],v[220:223],v[28:31]
s_add_u32 s76, s76, s77
TR a[156:157],v233 offset:25088
TR a[158:159],v233 offset:29184
MM v[24:27],a[204:207],v[220:223],v[140:143]
s_add_u32 s76, s76, %[swave]
EX v10,v10
MM v[24:27],a[200:203],v[204:207],v[24:27]
s_lshl_b32 s76, s76, 9
TR a[160:161],v232 offset:17408
CV v8,v8,v9
MM v[24:27],a[196:199],v[188:191],v[24:27]
s_add_u32 s76, s76, 22183936
EX v11,v11
MM v[24:27],a[192:195],v[172:175],v[24:27]
EX v12,v12
MM v[32:35],a[224:227],v[160:163],v[128:131]
TR a[162:163],v232 offset:21504
CV v9,v10,v11
MM v[32:35],a[228:231],v[176:179],v[32:35]
EX v13,v13
MM v[32:35],a[232:235],v[192:195],v[32:35]
TR a[164:165],v232 offset:25600
CV v10,v12,v13
MM v[32:35],a[236:239],v[208:211],v[32:35]
EX v14,v14
MM v[36:39],a[252:255],v[208:211],v[128:131]
TR a[166:167],v232 offset:29696
TR a[168:169],v233 offset:17408
MM v[36:39],a[248:251],v[192:195],v[36:39]
EX v15,v15
MM v[36:39],a[244:247],v[176:179],v[36:39]
TR a[170:171],v233 offset:21504
CV v11,v14,v15
MM v[36:39],a[240:243],v[160:163],v[36:39]
EX v16,v16
MM v[44:47],a[240:243],v[164:167],v[132:135]
TR a[172:173],v233 offset:25600
TR a[174:175],v233 offset:29696
MM v[44:47],a[244:247],v[180:183],v[44:47]
EX v17,v17
MM v[44:47],a[248:251],v[196:199],v[44:47]
EX v18,v18
MM v[44:47],a[252:255],v[212:215],v[44:47]
TR a[176:177],v232 offset:17920
CV v16,v16,v17
MM v[40:43],a[236:239],v[212:215],v[132:135]
EX v19,v19
MM v[40:43],a[232:235],v[196:199],v[40:43]
)ASM"
R"ASM(
TR a[178:179],v232 offset:22016
CV v17,v18,v19
MM v[40:43],a[228:231],v[180:183],v[40:43]
EX v20,v20
MM v[40:43],a[224:227],v[164:167],v[40:43]
TR a[180:181],v232 offset:26112
TR a[182:183],v232 offset:30208
MM v[48:51],a[224:227],v[168:171],v[136:139]
EX v21,v21
MM v[48:51],a[228:231],v[184:187],v[48:51]
TR a[184:185],v233 offset:17920
CV v18,v20,v21
MM v[48:51],a[232:235],v[200:203],v[48:51]
EX v22,v22
MM v[48:51],a[236:239],v[216:219],v[48:51]
EX v23,v23
MM v[52:55],a[252:255],v[216:219],v[136:139]
TR a[186:187],v233 offset:22016
CV v19,v22,v23
MM v[52:55],a[248:251],v[200:203],v[52:55]
EX v24,v24
MM v[52:55],a[244:247],v[184:187],v[52:55]
TR a[188:189],v233 offset:26112
TR a[190:191],v233 offset:30208
MM v[52:55],a[240:243],v[168:171],v[52:55]
EX v25,v25
MM v[60:63],a[240:243],v[172:175],v[140:143]
EX v26,v26
MM v[60:63],a[244:247],v[188:191],v[60:63]
EX v27,v27
MM v[60:63],a[248:251],v[204:207],v[60:63]
EX v28,v28
MM v[60:63],a[252:255],v[220:223],v[60:63]
EX v29,v29
MM v[56:59],a[236:239],v[220:223],v[140:143]
EX v30,v30
MM v[56:59],a[232:235],v[204:207],v[56:59]
EX v31,v31
MM v[56:59],a[228:231],v[188:191],v[56:59]
EX v32,v32
MM v[56:59],a[224:227],v[172:175],v[56:59]
s_waitcnt lgkmcnt(0)
s_barrier
MM a[0:3],a[128:131],v[64:67],a[0:3]
s_add_u32 m0, s65, 16384
CV v24,v24,v25
CV v25,v26,v27
MM a[0:3],a[132:135],v[68:71],a[0:3]
RD a[192:195],v224 offset:49152
CV v26,v28,v29
MM a[8:11],a[132:135],v[76:79],a[8:11]
RD a[196:199],v225 offset:49152
CV v27,v30,v31
MM a[8:11],a[128:131],v[72:75],a[8:11]
buffer_load_dwordx4 v228, s[8:11], s64 offen offset:0 lds
MM a[16:19],a[128:131],v[80:83],a[16:19]
RD a[200:203],v226 offset:49152
RD a[204:207],v227 offset:49152
MM a[16:19],a[132:135],v[84:87],a[16:19]
EX v33,v33
MM a[24:27],a[132:135],v[92:95],a[24:27]
RD a[208:211],v224 offset:53248
CV v4,v32,v33
MM a[24:27],a[128:131],v[88:91],a[24:27]
EX v34,v34
MM a[28:31],a[136:139],v[88:91],a[28:31]
buffer_load_dwordx4 v229, s[8:11], s64 offen offset:1024 lds
MM a[28:31],a[140:143],v[92:95],a[28:31]
EX v35,v35
MM a[20:23],a[140:143],v[84:87],a[20:23]
RD a[212:215],v225 offset:53248
CV v5,v34,v35
MM a[20:23],a[136:139],v[80:83],a[20:23]
EX v36,v36
MM a[12:15],a[136:139],v[72:75],a[12:15]
s_waitcnt vmcnt(8)
TR v[96:97],v124 offset:0
TR v[98:99],v124 offset:256
MM a[12:15],a[140:143],v[76:79],a[12:15]
RD v[108:111],v125 offset:0
TR v[100:101],v124 offset:2048
MM a[4:7],a[140:143],v[68:71],a[4:7]
TR v[102:103],v124 offset:2304
RD v[112:115],v125 offset:64
MM a[4:7],a[136:139],v[64:67],a[4:7]
TR v[104:105],v124 offset:4096
TR v[106:107],v124 offset:4352
MM a[32:35],a[144:147],v[64:67],a[32:35]
RD v[116:119],v125 offset:128
RD a[216:219],v226 offset:53248
MM a[32:35],a[148:151],v[68:71],a[32:35]
EX v37,v37
MM a[40:43],a[148:151],v[76:79],a[40:43]
EX v38,v38
MM a[40:43],a[144:147],v[72:75],a[40:43]
EX v39,v39
MM a[48:51],a[144:147],v[80:83],a[48:51]
s_waitcnt lgkmcnt(1)
CV v6,v36,v37
CV v7,v38,v39
MM a[48:51],a[148:151],v[84:87],a[48:51]
EX v40,v40
MM v[120:123],v[96:99],v[108:111],0
MM v[120:123],v[100:103],v[112:115],v[120:123]
MM v[120:123],v[104:107],v[116:119],v[120:123]
MM a[56:59],a[148:151],v[92:95],a[56:59]
TR v[96:97],v124 offset:6144
TR v[98:99],v124 offset:6400
MM a[56:59],a[144:147],v[88:91],a[56:59]
RD v[108:111],v125 offset:192
TR v[100:101],v124 offset:8192
MM a[60:63],a[152:155],v[88:91],a[60:63]
TR v[102:103],v124 offset:8448
RD v[112:115],v125 offset:256
EX v41,v41
MM a[60:63],a[156:159],v[92:95],a[60:63]
EX v42,v42
MM a[52:55],a[156:159],v[84:87],a[52:55]
EX v43,v43
MM a[52:55],a[152:155],v[80:83],a[52:55]
buffer_load_dwordx4 v230, s[8:11], s64 offen offset:2048 lds
MM a[44:47],a[152:155],v[72:75],a[44:47]
s_waitcnt lgkmcnt(0)
EX v44,v44
MM a[44:47],a[156:159],v[76:79],a[44:47]
CV v12,v40,v41
CV v13,v42,v43
MM v[120:123],v[96:99],v[108:111],v[120:123]
MM v[120:123],v[100:103],v[112:115],v[120:123]
MM a[36:39],a[156:159],v[68:71],a[36:39]
EX v45,v45
MM a[36:39],a[152:155],v[64:67],a[36:39]
EX v46,v46
MM a[64:67],a[160:163],v[64:67],a[64:67]
buffer_load_dwordx4 v231, s[8:11], s64 offen offset:3072 lds
MM a[64:67],a[164:167],v[68:71],a[64:67]
s_add_u32 m0, s65, 81920
EX v47,v47
MM a[72:75],a[164:167],v[76:79],a[72:75]
RD a[220:223],v227 offset:53248
CV v14,v44,v45
MM a[72:75],a[160:163],v[72:75],a[72:75]
EX v48,v48
MM a[80:83],a[160:163],v[80:83],a[80:83]
s_mov_b32 exec_lo, 0x00ff00ff
s_mov_b32 exec_hi, 0x00ff00ff
buffer_store_dwordx4 v[120:123], v126, s[20:23], s76 offen offset:0 sc1
s_mov_b64 exec, -1
s_nop 0
MM a[80:83],a[164:167],v[84:87],a[80:83]
s_lshl_b32 s76, s50, 2
EX v49,v49
MM a[88:91],a[164:167],v[92:95],a[88:91]
s_add_u32 s76, s76, s84
RD a[224:227],v224 offset:57344
CV v15,v46,v47
MM a[88:91],a[160:163],v[88:91],a[88:91]
s_lshl_b32 s76, s76, 3
EX v50,v50
MM a[92:95],a[168:171],v[88:91],a[92:95]
s_lshr_b32 s77, s49, 2
EX v51,v51
MM a[92:95],a[172:175],v[92:95],a[92:95]
s_add_u32 s76, s76, s77
buffer_load_dwordx4 v235, s[12:15], s81 offen offset:0 lds
MM a[84:87],a[172:175],v[84:87],a[84:87]
s_mul_i32 s95, s76, 82944
RD a[228:231],v225 offset:57344
CV v20,v48,v49
MM a[84:87],a[168:171],v[80:83],a[84:87]
s_mul_i32 s76, %[swave], 20480
EX v52,v52
MM a[76:79],a[168:171],v[72:75],a[76:79]
s_add_u32 s95, s95, s76
EX v53,v53
MM a[76:79],a[172:175],v[76:79],a[76:79]
s_add_u32 s95, s95, 22708224
v_mov_b32 v240, -1
v_mov_b32 v241, -1
MM a[68:71],a[172:175],v[68:71],a[68:71]
v_mov_b32 v242, -1
v_mov_b32 v243, -1
MM a[68:71],a[168:171],v[64:67],a[68:71]
EX v54,v54
MM a[96:99],a[176:179],v[64:67],a[96:99]
RD a[232:235],v226 offset:57344
CV v21,v50,v51
MM a[96:99],a[180:183],v[68:71],a[96:99]
EX v55,v55
MM a[104:107],a[180:183],v[76:79],a[104:107]
RD a[236:239],v227 offset:57344
CV v22,v52,v53
MM a[104:107],a[176:179],v[72:75],a[104:107]
EX v56,v56
MM a[112:115],a[176:179],v[80:83],a[112:115]
buffer_load_dwordx4 v235, s[12:15], s64 offen offset:1024 lds
MM a[112:115],a[180:183],v[84:87],a[112:115]
EX v57,v57
MM a[120:123],a[180:183],v[92:95],a[120:123]
EX v58,v58
MM a[120:123],a[176:179],v[88:91],a[120:123]
RD a[240:243],v224 offset:61440
CV v23,v54,v55
MM a[124:127],a[184:187],v[88:91],a[124:127]
EX v59,v59
MM a[124:127],a[188:191],v[92:95],a[124:127]
RD a[244:247],v225 offset:61440
CV v28,v56,v57
MM a[116:119],a[188:191],v[84:87],a[116:119]
EX v60,v60
MM a[116:119],a[184:187],v[80:83],a[116:119]
buffer_load_dwordx4 v235, s[12:15], s81 offen offset:2048 lds
MM a[108:111],a[184:187],v[72:75],a[108:111]
EX v61,v61
MM a[108:111],a[188:191],v[76:79],a[108:111]
RD a[248:251],v226 offset:61440
CV v29,v58,v59
MM a[100:103],a[188:191],v[68:71],a[100:103]
EX v62,v62
MM a[100:103],a[184:187],v[64:67],a[100:103]
EX v63,v63
v_smfmac_f32_16x16x64_bf16 v[236:239], v[144:147], v[0:7], v234
RD a[252:255],v227 offset:61440
v_smfmac_f32_16x16x64_bf16 v[236:239], v[148:151], v[8:15], v234
CV v30,v60,v61
v_smfmac_f32_16x16x64_bf16 v[236:239], v[152:155], v[16:23], v234
CV v31,v62,v63
s_nop 1
v_smfmac_f32_16x16x64_bf16 v[236:239], v[156:159], v[24:31], v234
s_waitcnt lgkmcnt(0)
buffer_load_dwordx4 v235, s[12:15], s64 offen offset:3072 lds
MM v[64:67],a[192:195],v[160:163],v[128:131]
s_add_u32 s64, s64, 16384
TR a[128:129],v232 offset:32768
s_add_u32 s81, s64, 896
MM v[64:67],a[196:199],v[176:179],v[64:67]
TR a[130:131],v232 offset:36864
TR a[132:133],v232 offset:40960
MM v[64:67],a[200:203],v[192:195],v[64:67]
TR a[134:135],v232 offset:45056
TR a[136:137],v233 offset:32768
MM v[64:67],a[204:207],v[208:211],v[64:67]
TR a[138:139],v233 offset:36864
v_add_f32 v246, v236, v237
MM v[68:71],a[220:223],v[208:211],v[128:131]
TR a[140:141],v233 offset:40960
v_cmp_nge_f32 vcc, s71, v246
MM v[68:71],a[216:219],v[192:195],v[68:71]
s_cbranch_vccnz LSITE14_%=
LBACK14_%=:
TR a[142:143],v233 offset:45056
TR a[144:145],v232 offset:33280
MM v[68:71],a[212:215],v[176:179],v[68:71]
s_lshr_b32 s77, %[sb], 3
TR a[146:147],v232 offset:37376
TR a[148:149],v232 offset:41472
MM v[68:71],a[208:211],v[160:163],v[68:71]
s_lshl_b32 s77, s77, 2
TR a[150:151],v232 offset:45568
TR a[152:153],v233 offset:33280
MM v[76:79],a[208:211],v[164:167],v[132:135]
s_add_u32 s77, s77, %[swave]
EX v64,v64
MM v[76:79],a[212:215],v[180:183],v[76:79]
s_lshl_b32 s77, s77, 9
EX v65,v65
MM v[76:79],a[216:219],v[196:199],v[76:79]
s_add_u32 s77, s77, 0
v_lshrrev_b32 v253, 1, v248
v_add_u32 v253, s77, v253
MM v[76:79],a[220:223],v[212:215],v[76:79]
s_mov_b32 s77, LKEND_ka-LPD0_%=-16
v_min_u32 v253, s77, v253
CV v64,v64,v65
MM v[72:75],a[204:207],v[212:215],v[132:135]
s_getpc_b64 s[98:99]
LPD0_%=:
global_load_dword v253, v253, s[98:99]
MM v[72:75],a[200:203],v[196:199],v[72:75]
EX v66,v66
MM v[72:75],a[196:199],v[180:183],v[72:75]
EX v67,v67
MM v[72:75],a[192:195],v[164:167],v[72:75]
TR a[154:155],v233 offset:37376
CV v65,v66,v67
MM v[80:83],a[192:195],v[168:171],v[136:139]
EX v68,v68
MM v[80:83],a[196:199],v[184:187],v[80:83]
EX v69,v69
MM v[80:83],a[200:203],v[200:203],v[80:83]
TR a[156:157],v233 offset:41472
CV v66,v68,v69
MM v[80:83],a[204:207],v[216:219],v[80:83]
EX v70,v70
MM v[84:87],a[220:223],v[216:219],v[136:139]
EX v71,v71
MM v[84:87],a[216:219],v[200:203],v[84:87]
TR a[158:159],v233 offset:45568
CV v67,v70,v71
MM v[84:87],a[212:215],v[184:187],v[84:87]
EX v72,v72
MM v[84:87],a[208:211],v[168:171],v[84:87]
EX v73,v73
MM v[92:95],a[208:211],v[172:175],v[140:143]
TR a[160:161],v232 offset:33792
CV v72,v72,v73
MM v[92:95],a[212:215],v[188:191],v[92:95]
EX v74,v74
MM v[92:95],a[216:219],v[204:207],v[92:95]
TR a[162:163],v232 offset:37888
TR a[164:165],v232 offset:41984
MM v[92:95],a[220:223],v[220:223],v[92:95]
EX v75,v75
MM v[88:91],a[204:207],v[220:223],v[140:143]
TR a[166:167],v232 offset:46080
CV v73,v74,v75
MM v[88:91],a[200:203],v[204:207],v[88:91]
EX v76,v76
MM v[88:91],a[196:199],v[188:191],v[88:91]
EX v77,v77
MM v[88:91],a[192:195],v[172:175],v[88:91]
TR a[168:169],v233 offset:33792
CV v74,v76,v77
MM v[96:99],a[224:227],v[160:163],v[128:131]
EX v78,v78
MM v[96:99],a[228:231],v[176:179],v[96:99]
TR a[170:171],v233 offset:37888
TR a[172:173],v233 offset:41984
MM v[96:99],a[232:235],v[192:195],v[96:99]
EX v79,v79
MM v[96:99],a[236:239],v[208:211],v[96:99]
TR a[174:175],v233 offset:46080
CV v75,v78,v79
MM v[100:103],a[252:255],v[208:211],v[128:131]
EX v80,v80
MM v[100:103],a[248:251],v[192:195],v[100:103]
EX v81,v81
MM v[100:103],a[244:247],v[176:179],v[100:103]
TR a[176:177],v232 offset:34304
CV v80,v80,v81
MM v[100:103],a[240:243],v[160:163],v[100:103]
EX v82,v82
MM v[108:111],a[240:243],v[164:167],v[132:135]
s_add_u32 s67, s95, 10240
buffer_store_dwordx4 v[240:243], v248, s[20:23], s67 offen offset:0 sc1
MM v[108:111],a[244:247],v[180:183],v[108:111]
TR a[178:179],v232 offset:38400
TR a[180:181],v232 offset:42496
MM v[108:111],a[248:251],v[196:199],v[108:111]
EX v83,v83
MM v[108:111],a[252:255],v[212:215],v[108:111]
TR a[182:183],v232 offset:46592
CV v81,v82,v83
MM v[104:107],a[236:239],v[212:215],v[132:135]
EX v84,v84
MM v[104:107],a[232:235],v[196:199],v[104:107]
EX v85,v85
MM v[104:107],a[228:231],v[180:183],v[104:107]
TR a[184:185],v233 offset:34304
CV v82,v84,v85
MM v[104:107],a[224:227],v[164:167],v[104:107]
EX v86,v86
MM v[112:115],a[224:227],v[168:171],v[136:139]
TR a[186:187],v233 offset:38400
TR a[188:189],v233 offset:42496
MM v[112:115],a[228:231],v[184:187],v[112:115]
EX v87,v87
MM v[112:115],a[232:235],v[200:203],v[112:115]
TR a[190:191],v233 offset:46592
CV v83,v86,v87
MM v[112:115],a[236:239],v[216:219],v[112:115]
EX v88,v88
MM v[116:119],a[252:255],v[216:219],v[136:139]
EX v89,v89
MM v[116:119],a[248:251],v[200:203],v[116:119]
EX v90,v90
MM v[116:119],a[244:247],v[184:187],v[116:119]
EX v91,v91
MM v[116:119],a[240:243],v[168:171],v[116:119]
EX v92,v92
MM v[124:127],a[240:243],v[172:175],v[140:143]
EX v93,v93
MM v[124:127],a[244:247],v[188:191],v[124:127]
EX v94,v94
MM v[124:127],a[248:251],v[204:207],v[124:127]
EX v95,v95
MM v[124:127],a[252:255],v[220:223],v[124:127]
EX v96,v96
MM v[120:123],a[236:239],v[220:223],v[140:143]
EX v97,v97
MM v[120:123],a[232:235],v[204:207],v[120:123]
EX v98,v98
MM v[120:123],a[228:231],v[188:191],v[120:123]
)ASM"
R"ASM(
EX v99,v99
MM v[120:123],a[224:227],v[172:175],v[120:123]
s_waitcnt vmcnt(15) lgkmcnt(0)
s_barrier
MM a[0:3],a[128:131],v[0:3],a[0:3]
s_add_u32 m0, s65, 32768
CV v88,v88,v89
CV v89,v90,v91
MM a[0:3],a[132:135],v[4:7],a[0:3]
RD a[192:195],v224 offset:0
CV v90,v92,v93
MM a[8:11],a[132:135],v[12:15],a[8:11]
RD a[196:199],v225 offset:0
CV v91,v94,v95
MM a[8:11],a[128:131],v[8:11],a[8:11]
buffer_load_dwordx4 v228, s[8:11], s64 offen offset:0 lds
MM a[16:19],a[128:131],v[16:19],a[16:19]
RD a[200:203],v226 offset:0
CV v68,v96,v97
MM a[16:19],a[132:135],v[20:23],a[16:19]
RD a[204:207],v227 offset:0
CV v69,v98,v99
MM a[24:27],a[132:135],v[28:31],a[24:27]
buffer_load_dwordx4 v229, s[8:11], s64 offen offset:1024 lds
s_nop 0
LPG10_%=:
s_cbranch_execz LTR0_%=
MM a[24:27],a[128:131],v[24:27],a[24:27]
RD a[208:211],v224 offset:4096
RD a[212:215],v225 offset:4096
MM a[28:31],a[136:139],v[24:27],a[28:31]
buffer_load_dwordx4 v230, s[8:11], s64 offen offset:2048 lds
MM a[28:31],a[140:143],v[28:31],a[28:31]
EX v100,v100
MM a[20:23],a[140:143],v[20:23],a[20:23]
EX v101,v101
MM a[20:23],a[136:139],v[16:19],a[20:23]
RD a[216:219],v226 offset:4096
CV v70,v100,v101
MM a[12:15],a[136:139],v[8:11],a[12:15]
EX v102,v102
MM a[12:15],a[140:143],v[12:15],a[12:15]
RD a[220:223],v227 offset:4096
RD a[224:227],v224 offset:8192
MM a[4:7],a[140:143],v[4:7],a[4:7]
EX v103,v103
MM a[4:7],a[136:139],v[0:3],a[4:7]
RD a[228:231],v225 offset:8192
CV v71,v102,v103
MM a[32:35],a[144:147],v[0:3],a[32:35]
EX v104,v104
MM a[32:35],a[148:151],v[4:7],a[32:35]
buffer_load_dwordx4 v231, s[8:11], s64 offen offset:3072 lds
MM a[40:43],a[148:151],v[12:15],a[40:43]
s_add_u32 m0, s65, 98304
EX v105,v105
MM a[40:43],a[144:147],v[8:11],a[40:43]
RD a[232:235],v226 offset:8192
CV v76,v104,v105
MM a[48:51],a[144:147],v[16:19],a[48:51]
EX v106,v106
MM a[48:51],a[148:151],v[20:23],a[48:51]
EX v107,v107
MM a[56:59],a[148:151],v[28:31],a[56:59]
RD a[236:239],v227 offset:8192
CV v77,v106,v107
MM a[56:59],a[144:147],v[24:27],a[56:59]
EX v108,v108
MM a[60:63],a[152:155],v[24:27],a[60:63]
buffer_load_dwordx4 v235, s[12:15], s81 offen offset:0 lds
MM a[60:63],a[156:159],v[28:31],a[60:63]
EX v109,v109
MM a[52:55],a[156:159],v[20:23],a[52:55]
RD a[240:243],v224 offset:12288
CV v78,v108,v109
MM a[52:55],a[152:155],v[16:19],a[52:55]
EX v110,v110
MM a[44:47],a[152:155],v[8:11],a[44:47]
EX v111,v111
MM a[44:47],a[156:159],v[12:15],a[44:47]
RD a[244:247],v225 offset:12288
CV v79,v110,v111
MM a[36:39],a[156:159],v[4:7],a[36:39]
buffer_load_dwordx4 v235, s[12:15], s64 offen offset:1024 lds
MM a[36:39],a[152:155],v[0:3],a[36:39]
EX v112,v112
MM a[64:67],a[160:163],v[0:3],a[64:67]
EX v113,v113
MM a[64:67],a[164:167],v[4:7],a[64:67]
RD a[248:251],v226 offset:12288
CV v84,v112,v113
MM a[72:75],a[164:167],v[12:15],a[72:75]
EX v114,v114
MM a[72:75],a[160:163],v[8:11],a[72:75]
EX v115,v115
MM a[80:83],a[160:163],v[16:19],a[80:83]
RD a[252:255],v227 offset:12288
CV v85,v114,v115
MM a[80:83],a[164:167],v[20:23],a[80:83]
buffer_load_dwordx4 v235, s[12:15], s81 offen offset:2048 lds
MM a[88:91],a[164:167],v[28:31],a[88:91]
EX v116,v116
MM a[88:91],a[160:163],v[24:27],a[88:91]
EX v117,v117
MM a[92:95],a[168:171],v[24:27],a[92:95]
EX v118,v118
MM a[92:95],a[172:175],v[28:31],a[92:95]
buffer_load_dwordx4 v235, s[12:15], s64 offen offset:3072 lds
MM a[84:87],a[172:175],v[20:23],a[84:87]
EX v119,v119
MM a[84:87],a[168:171],v[16:19],a[84:87]
EX v120,v120
MM a[76:79],a[168:171],v[8:11],a[76:79]
EX v121,v121
MM a[76:79],a[172:175],v[12:15],a[76:79]
EX v122,v122
MM a[68:71],a[172:175],v[4:7],a[68:71]
EX v123,v123
MM a[68:71],a[168:171],v[0:3],a[68:71]
EX v124,v124
MM a[96:99],a[176:179],v[0:3],a[96:99]
EX v125,v125
MM a[96:99],a[180:183],v[4:7],a[96:99]
EX v126,v126
MM a[104:107],a[180:183],v[12:15],a[104:107]
EX v127,v127
MM a[104:107],a[176:179],v[8:11],a[104:107]
CV v86,v116,v117
CV v87,v118,v119
MM a[112:115],a[176:179],v[16:19],a[112:115]
CV v92,v120,v121
CV v93,v122,v123
MM a[112:115],a[180:183],v[20:23],a[112:115]
CV v94,v124,v125
CV v95,v126,v127
MM a[120:123],a[180:183],v[28:31],a[120:123]
MM a[120:123],a[176:179],v[24:27],a[120:123]
MM a[124:127],a[184:187],v[24:27],a[124:127]
MM a[124:127],a[188:191],v[28:31],a[124:127]
MM a[116:119],a[188:191],v[20:23],a[116:119]
MM a[116:119],a[184:187],v[16:19],a[116:119]
MM a[108:111],a[184:187],v[8:11],a[108:111]
MM a[108:111],a[188:191],v[12:15],a[108:111]
MM a[100:103],a[188:191],v[4:7],a[100:103]
MM a[100:103],a[184:187],v[0:3],a[100:103]
v_smfmac_f32_16x16x64_bf16 v[236:239], v[144:147], v[64:71], v234
v_smfmac_f32_16x16x64_bf16 v[236:239], v[148:151], v[72:79], v234
v_smfmac_f32_16x16x64_bf16 v[236:239], v[152:155], v[80:87], v234
v_smfmac_f32_16x16x64_bf16 v[236:239], v[156:159], v[88:95], v234
s_waitcnt lgkmcnt(0)
MM v[0:3],a[192:195],v[160:163],v[128:131]
s_add_u32 s64, s64, 16384
TR a[128:129],v232 offset:49152
s_add_u32 s81, s64, 896
MM v[0:3],a[196:199],v[176:179],v[0:3]
TR a[130:131],v232 offset:53248
TR a[132:133],v232 offset:57344
MM v[0:3],a[200:203],v[192:195],v[0:3]
TR a[134:135],v232 offset:61440
TR a[136:137],v233 offset:49152
MM v[0:3],a[204:207],v[208:211],v[0:3]
TR a[138:139],v233 offset:53248
v_add_f32 v247, v236, v237
MM v[4:7],a[220:223],v[208:211],v[128:131]
TR a[140:141],v233 offset:57344
v_cmp_nge_f32 vcc, s71, v247
MM v[4:7],a[216:219],v[192:195],v[4:7]
s_cbranch_vccnz LSITE15_%=
LBACK15_%=:
TR a[142:143],v233 offset:61440
TR a[144:145],v232 offset:49664
MM v[4:7],a[212:215],v[176:179],v[4:7]
s_add_u32 s67, s95, 11264
buffer_store_dwordx4 v[240:243], v248, s[20:23], s67 offen offset:0 sc1
MM v[4:7],a[208:211],v[160:163],v[4:7]
TR a[146:147],v232 offset:53760
TR a[148:149],v232 offset:57856
MM v[12:15],a[208:211],v[164:167],v[132:135]
EX v0,v0
MM v[12:15],a[212:215],v[180:183],v[12:15]
EX v1,v1
MM v[12:15],a[216:219],v[196:199],v[12:15]
TR a[150:151],v232 offset:61952
CV v0,v0,v1
MM v[12:15],a[220:223],v[212:215],v[12:15]
EX v2,v2
MM v[8:11],a[204:207],v[212:215],v[132:135]
EX v3,v3
MM v[8:11],a[200:203],v[196:199],v[8:11]
TR a[152:153],v233 offset:49664
CV v1,v2,v3
MM v[8:11],a[196:199],v[180:183],v[8:11]
EX v4,v4
MM v[8:11],a[192:195],v[164:167],v[8:11]
EX v5,v5
MM v[16:19],a[192:195],v[168:171],v[136:139]
TR a[154:155],v233 offset:53760
CV v2,v4,v5
MM v[16:19],a[196:199],v[184:187],v[16:19]
EX v6,v6
MM v[16:19],a[200:203],v[200:203],v[16:19]
EX v7,v7
MM v[16:19],a[204:207],v[216:219],v[16:19]
TR a[156:157],v233 offset:57856
CV v3,v6,v7
MM v[20:23],a[220:223],v[216:219],v[136:139]
EX v8,v8
MM v[20:23],a[216:219],v[200:203],v[20:23]
TR a[158:159],v233 offset:61952
TR a[160:161],v232 offset:50176
MM v[20:23],a[212:215],v[184:187],v[20:23]
EX v9,v9
MM v[20:23],a[208:211],v[168:171],v[20:23]
TR a[162:163],v232 offset:54272
CV v8,v8,v9
MM v[28:31],a[208:211],v[172:175],v[140:143]
EX v10,v10
MM v[28:31],a[212:215],v[188:191],v[28:31]
TR a[164:165],v232 offset:58368
TR a[166:167],v232 offset:62464
MM v[28:31],a[216:219],v[204:207],v[28:31]
EX v11,v11
MM v[28:31],a[220:223],v[220:223],v[28:31]
TR a[168:169],v233 offset:50176
CV v9,v10,v11
MM v[24:27],a[204:207],v[220:223],v[140:143]
EX v12,v12
MM v[24:27],a[200:203],v[204:207],v[24:27]
EX v13,v13
MM v[24:27],a[196:199],v[188:191],v[24:27]
TR a[170:171],v233 offset:54272
CV v10,v12,v13
MM v[24:27],a[192:195],v[172:175],v[24:27]
EX v14,v14
MM v[32:35],a[224:227],v[160:163],v[128:131]
TR a[172:173],v233 offset:58368
TR a[174:175],v233 offset:62464
MM v[32:35],a[228:231],v[176:179],v[32:35]
EX v15,v15
MM v[32:35],a[232:235],v[192:195],v[32:35]
TR a[176:177],v232 offset:50688
CV v11,v14,v15
MM v[32:35],a[236:239],v[208:211],v[32:35]
EX v16,v16
MM v[36:39],a[252:255],v[208:211],v[128:131]
EX v17,v17
MM v[36:39],a[248:251],v[192:195],v[36:39]
TR a[178:179],v232 offset:54784
CV v16,v16,v17
MM v[36:39],a[244:247],v[176:179],v[36:39]
EX v18,v18
MM v[36:39],a[240:243],v[160:163],v[36:39]
TR a[180:181],v232 offset:58880
TR a[182:183],v232 offset:62976
MM v[44:47],a[240:243],v[164:167],v[132:135]
s_add_u32 s67, s95, 12288
buffer_store_dwordx4 v[240:243], v248, s[20:23], s67 offen offset:0 sc1
MM v[44:47],a[244:247],v[180:183],v[44:47]
EX v19,v19
MM v[44:47],a[248:251],v[196:199],v[44:47]
TR a[184:185],v233 offset:50688
CV v17,v18,v19
MM v[44:47],a[252:255],v[212:215],v[44:47]
EX v20,v20
MM v[40:43],a[236:239],v[212:215],v[132:135]
EX v21,v21
MM v[40:43],a[232:235],v[196:199],v[40:43]
TR a[186:187],v233 offset:54784
CV v18,v20,v21
MM v[40:43],a[228:231],v[180:183],v[40:43]
EX v22,v22
MM v[40:43],a[224:227],v[164:167],v[40:43]
TR a[188:189],v233 offset:58880
TR a[190:191],v233 offset:62976
MM v[48:51],a[224:227],v[168:171],v[136:139]
EX v23,v23
MM v[48:51],a[228:231],v[184:187],v[48:51]
CV v19,v22,v23
MM v[48:51],a[232:235],v[200:203],v[48:51]
EX v24,v24
MM v[48:51],a[236:239],v[216:219],v[48:51]
EX v25,v25
MM v[52:55],a[252:255],v[216:219],v[136:139]
EX v26,v26
MM v[52:55],a[248:251],v[200:203],v[52:55]
EX v27,v27
MM v[52:55],a[244:247],v[184:187],v[52:55]
EX v28,v28
MM v[52:55],a[240:243],v[168:171],v[52:55]
EX v29,v29
MM v[60:63],a[240:243],v[172:175],v[140:143]
EX v30,v30
MM v[60:63],a[244:247],v[188:191],v[60:63]
EX v31,v31
MM v[60:63],a[248:251],v[204:207],v[60:63]
EX v32,v32
MM v[60:63],a[252:255],v[220:223],v[60:63]
EX v33,v33
MM v[56:59],a[236:239],v[220:223],v[140:143]
EX v34,v34
MM v[56:59],a[232:235],v[204:207],v[56:59]
EX v35,v35
MM v[56:59],a[228:231],v[188:191],v[56:59]
s_getpc_b64 s[98:99]
LTA2_%=:
s_lshl_b32 s73, %[swave], 12
s_add_u32 s73, s73, LPG14_%=-LTA2_%=
s_add_u32 s98, s98, s73
s_addc_u32 s99, s99, 0
s_mov_b64 exec, 0
s_setpc_b64 s[98:99]
LTR2_%=:
s_mov_b64 exec, -1
s_nop 0
CV v24,v24,v25
MM v[56:59],a[224:227],v[172:175],v[56:59]
s_waitcnt vmcnt(17) lgkmcnt(0)
s_barrier
MM a[0:3],a[128:131],v[64:67],a[0:3]
s_lshr_b32 s91, s48, 7
CV v25,v26,v27
CV v26,v28,v29
MM a[0:3],a[132:135],v[68:71],a[0:3]
s_lshl_b32 s91, s91, 5
RD a[192:195],v224 offset:16384
CV v27,v30,v31
MM a[8:11],a[132:135],v[76:79],a[8:11]
s_and_b32 s77, s48, 16
s_add_u32 m0, s65, 49152
RD a[196:199],v225 offset:16384
MM a[8:11],a[128:131],v[72:75],a[8:11]
s_add_u32 s91, s91, s77
buffer_load_dwordx4 v228, s[8:11], s64 offen offset:0 lds
MM a[16:19],a[128:131],v[80:83],a[16:19]
s_bfe_u32 s77, s48, 131077
RD a[200:203],v226 offset:16384
CV v4,v32,v33
MM a[16:19],a[132:135],v[84:87],a[16:19]
s_lshl_b32 s77, s77, 2
CV v5,v34,v35
MM a[24:27],a[132:135],v[92:95],a[24:27]
s_add_u32 s91, s91, s77
buffer_load_dwordx4 v229, s[8:11], s64 offen offset:1024 lds
MM a[24:27],a[128:131],v[88:91],a[24:27]
s_add_u32 s91, s91, %[swave]
RD a[204:207],v227 offset:16384
RD a[208:211],v224 offset:20480
MM a[28:31],a[136:139],v[88:91],a[28:31]
s_lshl_b32 s91, s91, 2
RD a[212:215],v225 offset:20480
MM a[28:31],a[140:143],v[92:95],a[28:31]
s_add_u32 s91, s91, 77824
EX v36,v36
MM a[20:23],a[140:143],v[84:87],a[20:23]
s_waitcnt vmcnt(18)
v_mov_b32 v250, s69
s_mov_b64 exec, 1
buffer_store_dword v250, off, s[20:23], s91 offset:0 sc1
s_mov_b64 exec, -1
MM a[20:23],a[136:139],v[80:83],a[20:23]
EX v37,v37
MM a[12:15],a[136:139],v[72:75],a[12:15]
RD a[216:219],v226 offset:20480
CV v6,v36,v37
MM a[12:15],a[140:143],v[76:79],a[12:15]
buffer_load_dwordx4 v230, s[8:11], s64 offen offset:2048 lds
MM a[4:7],a[140:143],v[68:71],a[4:7]
EX v38,v38
MM a[4:7],a[136:139],v[64:67],a[4:7]
EX v39,v39
MM a[32:35],a[144:147],v[64:67],a[32:35]
RD a[220:223],v227 offset:20480
CV v7,v38,v39
MM a[32:35],a[148:151],v[68:71],a[32:35]
EX v40,v40
MM a[40:43],a[148:151],v[76:79],a[40:43]
EX v41,v41
MM a[40:43],a[144:147],v[72:75],a[40:43]
RD a[224:227],v224 offset:24576
CV v12,v40,v41
MM a[48:51],a[144:147],v[80:83],a[48:51]
buffer_load_dwordx4 v231, s[8:11], s64 offen offset:3072 lds
MM a[48:51],a[148:151],v[84:87],a[48:51]
s_add_u32 m0, s65, 114688
EX v42,v42
MM a[56:59],a[148:151],v[92:95],a[56:59]
EX v43,v43
MM a[56:59],a[144:147],v[88:91],a[56:59]
RD a[228:231],v225 offset:24576
CV v13,v42,v43
MM a[60:63],a[152:155],v[88:91],a[60:63]
EX v44,v44
MM a[60:63],a[156:159],v[92:95],a[60:63]
buffer_load_dwordx4 v235, s[12:15], s81 offen offset:0 lds
)ASM"
R"ASM(
MM a[52:55],a[156:159],v[84:87],a[52:55]
EX v45,v45
MM a[52:55],a[152:155],v[80:83],a[52:55]
RD a[232:235],v226 offset:24576
CV v14,v44,v45
MM a[44:47],a[152:155],v[72:75],a[44:47]
EX v46,v46
MM a[44:47],a[156:159],v[76:79],a[44:47]
EX v47,v47
MM a[36:39],a[156:159],v[68:71],a[36:39]
RD a[236:239],v227 offset:24576
CV v15,v46,v47
MM a[36:39],a[152:155],v[64:67],a[36:39]
EX v48,v48
MM a[64:67],a[160:163],v[64:67],a[64:67]
buffer_load_dwordx4 v235, s[12:15], s64 offen offset:1024 lds
MM a[64:67],a[164:167],v[68:71],a[64:67]
EX v49,v49
MM a[72:75],a[164:167],v[76:79],a[72:75]
RD a[240:243],v224 offset:28672
CV v20,v48,v49
MM a[72:75],a[160:163],v[72:75],a[72:75]
EX v50,v50
MM a[80:83],a[160:163],v[80:83],a[80:83]
EX v51,v51
MM a[80:83],a[164:167],v[84:87],a[80:83]
RD a[244:247],v225 offset:28672
CV v21,v50,v51
MM a[88:91],a[164:167],v[92:95],a[88:91]
EX v52,v52
MM a[88:91],a[160:163],v[88:91],a[88:91]
buffer_load_dwordx4 v235, s[12:15], s81 offen offset:2048 lds
MM a[92:95],a[168:171],v[88:91],a[92:95]
EX v53,v53
MM a[92:95],a[172:175],v[92:95],a[92:95]
RD a[248:251],v226 offset:28672
CV v22,v52,v53
MM a[84:87],a[172:175],v[84:87],a[84:87]
EX v54,v54
MM a[84:87],a[168:171],v[80:83],a[84:87]
EX v55,v55
MM a[76:79],a[168:171],v[72:75],a[76:79]
RD a[252:255],v227 offset:28672
CV v23,v54,v55
MM a[76:79],a[172:175],v[76:79],a[76:79]
EX v56,v56
MM a[68:71],a[172:175],v[68:71],a[68:71]
buffer_load_dwordx4 v235, s[12:15], s64 offen offset:3072 lds
MM a[68:71],a[168:171],v[64:67],a[68:71]
EX v57,v57
MM a[96:99],a[176:179],v[64:67],a[96:99]
EX v58,v58
MM a[96:99],a[180:183],v[68:71],a[96:99]
EX v59,v59
MM a[104:107],a[180:183],v[76:79],a[104:107]
EX v60,v60
MM a[104:107],a[176:179],v[72:75],a[104:107]
EX v61,v61
MM a[112:115],a[176:179],v[80:83],a[112:115]
EX v62,v62
MM a[112:115],a[180:183],v[84:87],a[112:115]
EX v63,v63
MM a[120:123],a[180:183],v[92:95],a[120:123]
CV v28,v56,v57
CV v29,v58,v59
MM a[120:123],a[176:179],v[88:91],a[120:123]
CV v30,v60,v61
CV v31,v62,v63
MM a[124:127],a[184:187],v[88:91],a[124:127]
MM a[124:127],a[188:191],v[92:95],a[124:127]
MM a[116:119],a[188:191],v[84:87],a[116:119]
MM a[116:119],a[184:187],v[80:83],a[116:119]
MM a[108:111],a[184:187],v[72:75],a[108:111]
MM a[108:111],a[188:191],v[76:79],a[108:111]
MM a[100:103],a[188:191],v[68:71],a[100:103]
MM a[100:103],a[184:187],v[64:67],a[100:103]
v_smfmac_f32_16x16x64_bf16 v[236:239], v[144:147], v[0:7], v234
v_smfmac_f32_16x16x64_bf16 v[236:239], v[148:151], v[8:15], v234
v_smfmac_f32_16x16x64_bf16 v[236:239], v[152:155], v[16:23], v234
v_smfmac_f32_16x16x64_bf16 v[236:239], v[156:159], v[24:31], v234
s_waitcnt lgkmcnt(0)
MM v[64:67],a[192:195],v[160:163],v[128:131]
s_add_u32 s64, s64, 16384
TR a[128:129],v232 offset:0
s_add_u32 s81, s64, 896
MM v[64:67],a[196:199],v[176:179],v[64:67]
TR a[130:131],v232 offset:4096
TR a[132:133],v232 offset:8192
MM v[64:67],a[200:203],v[192:195],v[64:67]
TR a[134:135],v232 offset:12288
TR a[136:137],v233 offset:0
MM v[64:67],a[204:207],v[208:211],v[64:67]
TR a[138:139],v233 offset:4096
v_add_f32 v246, v236, v237
MM v[68:71],a[220:223],v[208:211],v[128:131]
TR a[140:141],v233 offset:8192
v_cmp_nge_f32 vcc, s71, v246
MM v[68:71],a[216:219],v[192:195],v[68:71]
s_cbranch_vccnz LSITE16_%=
LBACK16_%=:
TR a[142:143],v233 offset:12288
TR a[144:145],v232 offset:512
MM v[68:71],a[212:215],v[176:179],v[68:71]
s_add_u32 s67, s95, 13312
buffer_store_dwordx4 v[240:243], v248, s[20:23], s67 offen offset:0 sc1
MM v[68:71],a[208:211],v[160:163],v[68:71]
TR a[146:147],v232 offset:4608
TR a[148:149],v232 offset:8704
MM v[76:79],a[208:211],v[164:167],v[132:135]
EX v64,v64
MM v[76:79],a[212:215],v[180:183],v[76:79]
EX v65,v65
MM v[76:79],a[216:219],v[196:199],v[76:79]
TR a[150:151],v232 offset:12800
CV v64,v64,v65
MM v[76:79],a[220:223],v[212:215],v[76:79]
EX v66,v66
MM v[72:75],a[204:207],v[212:215],v[132:135]
EX v67,v67
MM v[72:75],a[200:203],v[196:199],v[72:75]
TR a[152:153],v233 offset:512
CV v65,v66,v67
MM v[72:75],a[196:199],v[180:183],v[72:75]
EX v68,v68
MM v[72:75],a[192:195],v[164:167],v[72:75]
EX v69,v69
MM v[80:83],a[192:195],v[168:171],v[136:139]
TR a[154:155],v233 offset:4608
CV v66,v68,v69
MM v[80:83],a[196:199],v[184:187],v[80:83]
EX v70,v70
MM v[80:83],a[200:203],v[200:203],v[80:83]
EX v71,v71
MM v[80:83],a[204:207],v[216:219],v[80:83]
TR a[156:157],v233 offset:8704
CV v67,v70,v71
MM v[84:87],a[220:223],v[216:219],v[136:139]
EX v72,v72
MM v[84:87],a[216:219],v[200:203],v[84:87]
TR a[158:159],v233 offset:12800
TR a[160:161],v232 offset:1024
MM v[84:87],a[212:215],v[184:187],v[84:87]
EX v73,v73
MM v[84:87],a[208:211],v[168:171],v[84:87]
TR a[162:163],v232 offset:5120
CV v72,v72,v73
MM v[92:95],a[208:211],v[172:175],v[140:143]
s_and_b32 s44, s50, 7
EX v74,v74
MM v[92:95],a[212:215],v[188:191],v[92:95]
s_lshl_b32 s44, s44, 2
TR a[164:165],v232 offset:9216
TR a[166:167],v232 offset:13312
MM v[92:95],a[216:219],v[204:207],v[92:95]
s_add_u32 s44, s44, s84
EX v75,v75
MM v[92:95],a[220:223],v[220:223],v[92:95]
s_lshl_b32 s44, s44, 2
TR a[168:169],v233 offset:1024
CV v73,v74,v75
MM v[88:91],a[204:207],v[220:223],v[140:143]
s_add_u32 s44, s44, %[swave]
EX v76,v76
MM v[88:91],a[200:203],v[204:207],v[88:91]
s_mul_i32 s44, s44, 5120
EX v77,v77
MM v[88:91],a[196:199],v[188:191],v[88:91]
s_mul_i32 s45, s84, 320
TR a[170:171],v233 offset:5120
CV v74,v76,v77
MM v[88:91],a[192:195],v[172:175],v[88:91]
s_sub_u32 s44, s44, s45
EX v78,v78
MM v[96:99],a[224:227],v[160:163],v[128:131]
s_lshl_b32 s45, %[swave], 10
TR a[172:173],v233 offset:9216
TR a[174:175],v233 offset:13312
MM v[96:99],a[228:231],v[176:179],v[96:99]
s_add_u32 s45, s45, %[slds]
s_add_u32 m0, s45, 147456
s_add_u32 s44, s44, 0
buffer_load_dwordx4 v248, s[24:27], s44 offen offset:0 lds
MM v[96:99],a[232:235],v[192:195],v[96:99]
EX v79,v79
MM v[96:99],a[236:239],v[208:211],v[96:99]
TR a[176:177],v232 offset:1536
CV v75,v78,v79
MM v[100:103],a[252:255],v[208:211],v[128:131]
EX v80,v80
MM v[100:103],a[248:251],v[192:195],v[100:103]
EX v81,v81
MM v[100:103],a[244:247],v[176:179],v[100:103]
TR a[178:179],v232 offset:5632
CV v80,v80,v81
MM v[100:103],a[240:243],v[160:163],v[100:103]
EX v82,v82
MM v[108:111],a[240:243],v[164:167],v[132:135]
s_add_u32 s67, s95, 14336
buffer_store_dwordx4 v[240:243], v248, s[20:23], s67 offen offset:0 sc1
MM v[108:111],a[244:247],v[180:183],v[108:111]
EX v83,v83
MM v[108:111],a[248:251],v[196:199],v[108:111]
TR a[180:181],v232 offset:9728
CV v81,v82,v83
MM v[108:111],a[252:255],v[212:215],v[108:111]
EX v84,v84
MM v[104:107],a[236:239],v[212:215],v[132:135]
TR a[182:183],v232 offset:13824
TR a[184:185],v233 offset:1536
MM v[104:107],a[232:235],v[196:199],v[104:107]
EX v85,v85
MM v[104:107],a[228:231],v[180:183],v[104:107]
TR a[186:187],v233 offset:5632
CV v82,v84,v85
MM v[104:107],a[224:227],v[164:167],v[104:107]
EX v86,v86
MM v[112:115],a[224:227],v[168:171],v[136:139]
TR a[188:189],v233 offset:9728
TR a[190:191],v233 offset:13824
MM v[112:115],a[228:231],v[184:187],v[112:115]
EX v87,v87
MM v[112:115],a[232:235],v[200:203],v[112:115]
CV v83,v86,v87
MM v[112:115],a[236:239],v[216:219],v[112:115]
EX v88,v88
MM v[116:119],a[252:255],v[216:219],v[136:139]
EX v89,v89
LPG11_%=:
s_cbranch_execz LTR0_%=
MM v[116:119],a[248:251],v[200:203],v[116:119]
EX v90,v90
MM v[116:119],a[244:247],v[184:187],v[116:119]
EX v91,v91
MM v[116:119],a[240:243],v[168:171],v[116:119]
EX v92,v92
MM v[124:127],a[240:243],v[172:175],v[140:143]
EX v93,v93
MM v[124:127],a[244:247],v[188:191],v[124:127]
EX v94,v94
MM v[124:127],a[248:251],v[204:207],v[124:127]
EX v95,v95
MM v[124:127],a[252:255],v[220:223],v[124:127]
EX v96,v96
MM v[120:123],a[236:239],v[220:223],v[140:143]
EX v97,v97
MM v[120:123],a[232:235],v[204:207],v[120:123]
EX v98,v98
MM v[120:123],a[228:231],v[188:191],v[120:123]
EX v99,v99
MM v[120:123],a[224:227],v[172:175],v[120:123]
s_waitcnt vmcnt(18) lgkmcnt(0)
s_barrier
MM a[0:3],a[128:131],v[0:3],a[0:3]
s_add_u32 m0, s65, 0
CV v88,v88,v89
CV v89,v90,v91
MM a[0:3],a[132:135],v[4:7],a[0:3]
RD a[192:195],v224 offset:32768
CV v90,v92,v93
MM a[8:11],a[132:135],v[12:15],a[8:11]
RD a[196:199],v225 offset:32768
CV v91,v94,v95
MM a[8:11],a[128:131],v[8:11],a[8:11]
buffer_load_dwordx4 v228, s[8:11], s64 offen offset:0 lds
MM a[16:19],a[128:131],v[16:19],a[16:19]
RD a[200:203],v226 offset:32768
CV v68,v96,v97
MM a[16:19],a[132:135],v[20:23],a[16:19]
RD a[204:207],v227 offset:32768
CV v69,v98,v99
MM a[24:27],a[132:135],v[28:31],a[24:27]
buffer_load_dwordx4 v229, s[8:11], s64 offen offset:1024 lds
MM a[24:27],a[128:131],v[24:27],a[24:27]
RD a[208:211],v224 offset:36864
RD a[212:215],v225 offset:36864
MM a[28:31],a[136:139],v[24:27],a[28:31]
buffer_load_dwordx4 v230, s[8:11], s64 offen offset:2048 lds
MM a[28:31],a[140:143],v[28:31],a[28:31]
EX v100,v100
MM a[20:23],a[140:143],v[20:23],a[20:23]
EX v101,v101
MM a[20:23],a[136:139],v[16:19],a[20:23]
RD a[216:219],v226 offset:36864
CV v70,v100,v101
MM a[12:15],a[136:139],v[8:11],a[12:15]
EX v102,v102
MM a[12:15],a[140:143],v[12:15],a[12:15]
RD a[220:223],v227 offset:36864
RD a[224:227],v224 offset:40960
MM a[4:7],a[140:143],v[4:7],a[4:7]
EX v103,v103
MM a[4:7],a[136:139],v[0:3],a[4:7]
RD a[228:231],v225 offset:40960
CV v71,v102,v103
MM a[32:35],a[144:147],v[0:3],a[32:35]
EX v104,v104
MM a[32:35],a[148:151],v[4:7],a[32:35]
buffer_load_dwordx4 v231, s[8:11], s64 offen offset:3072 lds
MM a[40:43],a[148:151],v[12:15],a[40:43]
s_add_u32 m0, s65, 65536
EX v105,v105
MM a[40:43],a[144:147],v[8:11],a[40:43]
RD a[232:235],v226 offset:40960
CV v76,v104,v105
MM a[48:51],a[144:147],v[16:19],a[48:51]
EX v106,v106
MM a[48:51],a[148:151],v[20:23],a[48:51]
EX v107,v107
MM a[56:59],a[148:151],v[28:31],a[56:59]
RD a[236:239],v227 offset:40960
CV v77,v106,v107
MM a[56:59],a[144:147],v[24:27],a[56:59]
EX v108,v108
MM a[60:63],a[152:155],v[24:27],a[60:63]
buffer_load_dwordx4 v235, s[12:15], s81 offen offset:0 lds
MM a[60:63],a[156:159],v[28:31],a[60:63]
EX v109,v109
MM a[52:55],a[156:159],v[20:23],a[52:55]
RD a[240:243],v224 offset:45056
CV v78,v108,v109
MM a[52:55],a[152:155],v[16:19],a[52:55]
EX v110,v110
MM a[44:47],a[152:155],v[8:11],a[44:47]
EX v111,v111
MM a[44:47],a[156:159],v[12:15],a[44:47]
RD a[244:247],v225 offset:45056
CV v79,v110,v111
MM a[36:39],a[156:159],v[4:7],a[36:39]
buffer_load_dwordx4 v235, s[12:15], s64 offen offset:1024 lds
MM a[36:39],a[152:155],v[0:3],a[36:39]
EX v112,v112
MM a[64:67],a[160:163],v[0:3],a[64:67]
EX v113,v113
MM a[64:67],a[164:167],v[4:7],a[64:67]
RD a[248:251],v226 offset:45056
CV v84,v112,v113
MM a[72:75],a[164:167],v[12:15],a[72:75]
EX v114,v114
MM a[72:75],a[160:163],v[8:11],a[72:75]
EX v115,v115
MM a[80:83],a[160:163],v[16:19],a[80:83]
RD a[252:255],v227 offset:45056
CV v85,v114,v115
MM a[80:83],a[164:167],v[20:23],a[80:83]
buffer_load_dwordx4 v235, s[12:15], s81 offen offset:2048 lds
MM a[88:91],a[164:167],v[28:31],a[88:91]
EX v116,v116
MM a[88:91],a[160:163],v[24:27],a[88:91]
EX v117,v117
MM a[92:95],a[168:171],v[24:27],a[92:95]
EX v118,v118
MM a[92:95],a[172:175],v[28:31],a[92:95]
buffer_load_dwordx4 v235, s[12:15], s64 offen offset:3072 lds
MM a[84:87],a[172:175],v[20:23],a[84:87]
EX v119,v119
MM a[84:87],a[168:171],v[16:19],a[84:87]
EX v120,v120
MM a[76:79],a[168:171],v[8:11],a[76:79]
EX v121,v121
MM a[76:79],a[172:175],v[12:15],a[76:79]
EX v122,v122
MM a[68:71],a[172:175],v[4:7],a[68:71]
EX v123,v123
MM a[68:71],a[168:171],v[0:3],a[68:71]
EX v124,v124
MM a[96:99],a[176:179],v[0:3],a[96:99]
EX v125,v125
MM a[96:99],a[180:183],v[4:7],a[96:99]
EX v126,v126
MM a[104:107],a[180:183],v[12:15],a[104:107]
EX v127,v127
MM a[104:107],a[176:179],v[8:11],a[104:107]
CV v86,v116,v117
CV v87,v118,v119
MM a[112:115],a[176:179],v[16:19],a[112:115]
CV v92,v120,v121
CV v93,v122,v123
MM a[112:115],a[180:183],v[20:23],a[112:115]
CV v94,v124,v125
CV v95,v126,v127
MM a[120:123],a[180:183],v[28:31],a[120:123]
MM a[120:123],a[176:179],v[24:27],a[120:123]
MM a[124:127],a[184:187],v[24:27],a[124:127]
MM a[124:127],a[188:191],v[28:31],a[124:127]
MM a[116:119],a[188:191],v[20:23],a[116:119]
MM a[116:119],a[184:187],v[16:19],a[116:119]
)ASM"
R"ASM(
MM a[108:111],a[184:187],v[8:11],a[108:111]
MM a[108:111],a[188:191],v[12:15],a[108:111]
MM a[100:103],a[188:191],v[4:7],a[100:103]
MM a[100:103],a[184:187],v[0:3],a[100:103]
v_smfmac_f32_16x16x64_bf16 v[236:239], v[144:147], v[64:71], v234
v_smfmac_f32_16x16x64_bf16 v[236:239], v[148:151], v[72:79], v234
v_smfmac_f32_16x16x64_bf16 v[236:239], v[152:155], v[80:87], v234
v_smfmac_f32_16x16x64_bf16 v[236:239], v[156:159], v[88:95], v234
s_waitcnt lgkmcnt(0)
MM v[0:3],a[192:195],v[160:163],v[128:131]
s_add_u32 s64, s64, 16384
TR a[128:129],v232 offset:16384
s_add_u32 s81, s64, 896
MM v[0:3],a[196:199],v[176:179],v[0:3]
TR a[130:131],v232 offset:20480
TR a[132:133],v232 offset:24576
MM v[0:3],a[200:203],v[192:195],v[0:3]
TR a[134:135],v232 offset:28672
TR a[136:137],v233 offset:16384
MM v[0:3],a[204:207],v[208:211],v[0:3]
TR a[138:139],v233 offset:20480
v_add_f32 v247, v236, v237
MM v[4:7],a[220:223],v[208:211],v[128:131]
TR a[140:141],v233 offset:24576
v_cmp_nge_f32 vcc, s71, v247
MM v[4:7],a[216:219],v[192:195],v[4:7]
s_cbranch_vccnz LSITE17_%=
LBACK17_%=:
TR a[142:143],v233 offset:28672
TR a[144:145],v232 offset:16896
MM v[4:7],a[212:215],v[176:179],v[4:7]
TR a[146:147],v232 offset:20992
TR a[148:149],v232 offset:25088
MM v[4:7],a[208:211],v[160:163],v[4:7]
TR a[150:151],v232 offset:29184
TR a[152:153],v233 offset:16896
MM v[12:15],a[208:211],v[164:167],v[132:135]
EX v0,v0
MM v[12:15],a[212:215],v[180:183],v[12:15]
EX v1,v1
MM v[12:15],a[216:219],v[196:199],v[12:15]
TR a[154:155],v233 offset:20992
CV v0,v0,v1
MM v[12:15],a[220:223],v[212:215],v[12:15]
EX v2,v2
MM v[8:11],a[204:207],v[212:215],v[132:135]
EX v3,v3
MM v[8:11],a[200:203],v[196:199],v[8:11]
TR a[156:157],v233 offset:25088
CV v1,v2,v3
MM v[8:11],a[196:199],v[180:183],v[8:11]
EX v4,v4
MM v[8:11],a[192:195],v[164:167],v[8:11]
EX v5,v5
MM v[16:19],a[192:195],v[168:171],v[136:139]
TR a[158:159],v233 offset:29184
CV v2,v4,v5
MM v[16:19],a[196:199],v[184:187],v[16:19]
EX v6,v6
MM v[16:19],a[200:203],v[200:203],v[16:19]
EX v7,v7
MM v[16:19],a[204:207],v[216:219],v[16:19]
TR a[160:161],v232 offset:17408
CV v3,v6,v7
MM v[20:23],a[220:223],v[216:219],v[136:139]
EX v8,v8
MM v[20:23],a[216:219],v[200:203],v[20:23]
EX v9,v9
MM v[20:23],a[212:215],v[184:187],v[20:23]
TR a[162:163],v232 offset:21504
CV v8,v8,v9
MM v[20:23],a[208:211],v[168:171],v[20:23]
EX v10,v10
MM v[28:31],a[208:211],v[172:175],v[140:143]
s_add_u32 m0, s45, 147456
s_add_u32 s44, s44, 1024
buffer_load_dwordx4 v248, s[24:27], s44 offen offset:0 lds
MM v[28:31],a[212:215],v[188:191],v[28:31]
TR a[164:165],v232 offset:25600
TR a[166:167],v232 offset:29696
MM v[28:31],a[216:219],v[204:207],v[28:31]
EX v11,v11
MM v[28:31],a[220:223],v[220:223],v[28:31]
TR a[168:169],v233 offset:17408
CV v9,v10,v11
MM v[24:27],a[204:207],v[220:223],v[140:143]
EX v12,v12
MM v[24:27],a[200:203],v[204:207],v[24:27]
EX v13,v13
MM v[24:27],a[196:199],v[188:191],v[24:27]
TR a[170:171],v233 offset:21504
CV v10,v12,v13
MM v[24:27],a[192:195],v[172:175],v[24:27]
EX v14,v14
MM v[32:35],a[224:227],v[160:163],v[128:131]
TR a[172:173],v233 offset:25600
TR a[174:175],v233 offset:29696
MM v[32:35],a[228:231],v[176:179],v[32:35]
EX v15,v15
MM v[32:35],a[232:235],v[192:195],v[32:35]
TR a[176:177],v232 offset:17920
CV v11,v14,v15
MM v[32:35],a[236:239],v[208:211],v[32:35]
EX v16,v16
MM v[36:39],a[252:255],v[208:211],v[128:131]
EX v17,v17
MM v[36:39],a[248:251],v[192:195],v[36:39]
TR a[178:179],v232 offset:22016
CV v16,v16,v17
MM v[36:39],a[244:247],v[176:179],v[36:39]
EX v18,v18
MM v[36:39],a[240:243],v[160:163],v[36:39]
TR a[180:181],v232 offset:26112
TR a[182:183],v232 offset:30208
MM v[44:47],a[240:243],v[164:167],v[132:135]
v_bfe_u32 v253, v248, 4, 4
v_lshlrev_b32 v253, 2, v253
MM v[44:47],a[244:247],v[180:183],v[44:47]
s_and_b32 s76, s48, 0xffffffc0
EX v19,v19
MM v[44:47],a[248:251],v[196:199],v[44:47]
s_add_u32 s76, s76, 77824
buffer_load_dword v252, v253, s[20:23], s76 offen offset:0 sc1
MM v[44:47],a[252:255],v[212:215],v[44:47]
TR a[184:185],v233 offset:17920
CV v17,v18,v19
MM v[40:43],a[236:239],v[212:215],v[132:135]
EX v20,v20
MM v[40:43],a[232:235],v[196:199],v[40:43]
EX v21,v21
MM v[40:43],a[228:231],v[180:183],v[40:43]
TR a[186:187],v233 offset:22016
CV v18,v20,v21
MM v[40:43],a[224:227],v[164:167],v[40:43]
EX v22,v22
MM v[48:51],a[224:227],v[168:171],v[136:139]
TR a[188:189],v233 offset:26112
TR a[190:191],v233 offset:30208
MM v[48:51],a[228:231],v[184:187],v[48:51]
EX v23,v23
MM v[48:51],a[232:235],v[200:203],v[48:51]
CV v19,v22,v23
MM v[48:51],a[236:239],v[216:219],v[48:51]
EX v24,v24
MM v[52:55],a[252:255],v[216:219],v[136:139]
EX v25,v25
MM v[52:55],a[248:251],v[200:203],v[52:55]
EX v26,v26
MM v[52:55],a[244:247],v[184:187],v[52:55]
EX v27,v27
MM v[52:55],a[240:243],v[168:171],v[52:55]
EX v28,v28
MM v[60:63],a[240:243],v[172:175],v[140:143]
EX v29,v29
MM v[60:63],a[244:247],v[188:191],v[60:63]
EX v30,v30
MM v[60:63],a[248:251],v[204:207],v[60:63]
EX v31,v31
MM v[60:63],a[252:255],v[220:223],v[60:63]
EX v32,v32
MM v[56:59],a[236:239],v[220:223],v[140:143]
EX v33,v33
MM v[56:59],a[232:235],v[204:207],v[56:59]
EX v34,v34
MM v[56:59],a[228:231],v[188:191],v[56:59]
EX v35,v35
MM v[56:59],a[224:227],v[172:175],v[56:59]
s_waitcnt vmcnt(17) lgkmcnt(0)
s_barrier
MM a[0:3],a[128:131],v[64:67],a[0:3]
s_lshl_b32 s60, s50, 2
CV v24,v24,v25
CV v25,v26,v27
MM a[0:3],a[132:135],v[68:71],a[0:3]
s_add_u32 s60, s60, %[swave]
RD a[192:195],v224 offset:49152
CV v26,v28,v29
MM a[8:11],a[132:135],v[76:79],a[8:11]
s_lshl_b32 s60, s60, 2
s_add_u32 m0, s65, 16384
CV v27,v30,v31
MM a[8:11],a[128:131],v[72:75],a[8:11]
s_bfe_u32 s62, s49, 65538
buffer_load_dwordx4 v228, s[8:11], s64 offen offset:0 lds
MM a[16:19],a[128:131],v[80:83],a[16:19]
s_add_u32 s93, s60, s62
RD a[196:199],v225 offset:49152
CV v4,v32,v33
MM a[16:19],a[132:135],v[84:87],a[16:19]
s_lshl_b32 s93, s93, 2
RD a[200:203],v226 offset:49152
CV v5,v34,v35
MM a[24:27],a[132:135],v[92:95],a[24:27]
s_lshr_b32 s61, s49, 3
RD a[204:207],v227 offset:49152
MM a[24:27],a[128:131],v[88:91],a[24:27]
s_add_u32 s93, s93, s61
buffer_load_dwordx4 v229, s[8:11], s64 offen offset:1024 lds
MM a[28:31],a[136:139],v[88:91],a[28:31]
s_mul_i32 s18, s93, 5120
RD a[208:211],v224 offset:53248
MM a[28:31],a[140:143],v[92:95],a[28:31]
s_mul_i32 s60, s84, 320
EX v36,v36
MM a[20:23],a[140:143],v[84:87],a[20:23]
s_add_u32 s18, s18, s60
buffer_load_dwordx4 v230, s[8:11], s64 offen offset:2048 lds
MM a[20:23],a[136:139],v[80:83],a[20:23]
s_and_b32 s60, s84, 1
EX v37,v37
MM a[12:15],a[136:139],v[72:75],a[12:15]
s_xor_b32 s60, s60, 1
RD a[212:215],v225 offset:53248
CV v6,v36,v37
MM a[12:15],a[140:143],v[76:79],a[12:15]
s_lshl_b32 s60, s60, 8
EX v38,v38
MM a[4:7],a[140:143],v[68:71],a[4:7]
s_add_u32 s63, s18, s60
RD a[216:219],v226 offset:53248
MM a[4:7],a[136:139],v[64:67],a[4:7]
s_and_b32 s60, s84, 1
EX v39,v39
MM a[32:35],a[144:147],v[64:67],a[32:35]
s_lshl_b32 s60, s60, 6
RD a[220:223],v227 offset:53248
CV v7,v38,v39
MM a[32:35],a[148:151],v[68:71],a[32:35]
s_add_u32 s18, s18, s60
EX v40,v40
MM a[40:43],a[148:151],v[76:79],a[40:43]
s_lshl_b32 s60, s50, 2
EX v41,v41
MM a[40:43],a[144:147],v[72:75],a[40:43]
s_add_u32 s60, s60, s84
RD a[224:227],v224 offset:57344
CV v12,v40,v41
MM a[48:51],a[144:147],v[80:83],a[48:51]
s_mul_i32 s16, s60, 663552
buffer_load_dwordx4 v231, s[8:11], s64 offen offset:3072 lds
MM a[48:51],a[148:151],v[84:87],a[48:51]
s_lshl_b32 s62, s62, 2
EX v42,v42
MM a[56:59],a[148:151],v[92:95],a[56:59]
s_add_u32 s62, s62, s61
EX v43,v43
MM a[56:59],a[144:147],v[88:91],a[56:59]
s_lshl_b32 s62, s62, 8
s_add_u32 m0, s65, 81920
CV v13,v42,v43
MM a[60:63],a[152:155],v[88:91],a[60:63]
s_add_u32 s16, s16, s62
EX v44,v44
MM a[60:63],a[156:159],v[92:95],a[60:63]
s_mul_i32 s60, %[swave], 20480
RD a[228:231],v225 offset:57344
RD a[232:235],v226 offset:57344
MM a[52:55],a[156:159],v[84:87],a[52:55]
s_add_u32 s16, s16, s60
EX v45,v45
MM a[52:55],a[152:155],v[80:83],a[52:55]
s_lshr_b32 s61, s49, 4
RD a[236:239],v227 offset:57344
CV v14,v44,v45
MM a[44:47],a[152:155],v[72:75],a[44:47]
s_mul_i32 s61, s61, 331776
EX v46,v46
MM a[44:47],a[156:159],v[76:79],a[44:47]
s_add_u32 s16, s16, s61
buffer_load_dwordx4 v235, s[12:15], s81 offen offset:0 lds
MM a[36:39],a[156:159],v[68:71],a[36:39]
s_add_u32 s16, s16, 22708224
EX v47,v47
MM a[36:39],a[152:155],v[64:67],a[36:39]
s_mov_b32 s17, 31
CV v15,v46,v47
MM a[64:67],a[160:163],v[64:67],a[64:67]
s_mov_b32 s72, 0
EX v48,v48
MM a[64:67],a[164:167],v[68:71],a[64:67]
s_lshl_b32 s60, s50, 2
EX v49,v49
MM a[72:75],a[164:167],v[76:79],a[72:75]
s_add_u32 s60, s60, s84
RD a[240:243],v224 offset:61440
CV v20,v48,v49
MM a[72:75],a[160:163],v[72:75],a[72:75]
s_lshl_b32 s60, s60, 2
buffer_load_dwordx4 v235, s[12:15], s64 offen offset:1024 lds
MM a[80:83],a[160:163],v[80:83],a[80:83]
s_add_u32 s60, s60, %[swave]
EX v50,v50
MM a[80:83],a[164:167],v[84:87],a[80:83]
s_lshl_b32 s60, s60, 5
EX v51,v51
MM a[88:91],a[164:167],v[92:95],a[88:91]
s_and_b32 s61, s49, 28
CV v21,v50,v51
MM a[88:91],a[160:163],v[88:91],a[88:91]
s_add_u32 s19, s60, s61
EX v52,v52
MM a[92:95],a[168:171],v[88:91],a[92:95]
s_lshl_b32 s19, s19, 2
EX v53,v53
MM a[92:95],a[172:175],v[92:95],a[92:95]
s_add_u32 s19, s19, 16384
RD a[244:247],v225 offset:61440
CV v22,v52,v53
MM a[84:87],a[172:175],v[84:87],a[84:87]
s_and_b32 s68, s19, 0xffffffc0
buffer_load_dwordx4 v235, s[12:15], s81 offen offset:2048 lds
MM a[84:87],a[168:171],v[80:83],a[84:87]
s_mov_b32 s82, 0
EX v54,v54
MM a[76:79],a[168:171],v[72:75],a[76:79]
s_lshr_b32 s61, s52, 2
EX v55,v55
MM a[76:79],a[172:175],v[76:79],a[76:79]
s_sub_u32 s61, s61, s93
s_cmp_eq_u32 s61, 0
s_cselect_b32 s60, 1, 0
MM a[68:71],a[172:175],v[68:71],a[68:71]
s_or_b32 s82, s82, s60
s_cmp_eq_u32 s61, 8
s_cselect_b32 s60, 256, 0
MM a[68:71],a[168:171],v[64:67],a[68:71]
s_or_b32 s82, s82, s60
RD a[248:251],v226 offset:61440
CV v23,v54,v55
MM a[96:99],a[176:179],v[64:67],a[96:99]
s_lshr_b32 s61, s53, 2
EX v56,v56
MM a[96:99],a[180:183],v[68:71],a[96:99]
s_sub_u32 s61, s61, s93
s_cmp_eq_u32 s61, 0
s_cselect_b32 s60, 2, 0
MM a[104:107],a[180:183],v[76:79],a[104:107]
s_or_b32 s82, s82, s60
s_cmp_eq_u32 s61, 8
s_cselect_b32 s60, 512, 0
MM a[104:107],a[176:179],v[72:75],a[104:107]
s_or_b32 s82, s82, s60
EX v57,v57
MM a[112:115],a[176:179],v[80:83],a[112:115]
s_lshr_b32 s61, s54, 2
CV v28,v56,v57
MM a[112:115],a[180:183],v[84:87],a[112:115]
s_sub_u32 s61, s61, s93
s_cmp_eq_u32 s61, 0
s_cselect_b32 s60, 4, 0
MM a[120:123],a[180:183],v[92:95],a[120:123]
s_or_b32 s82, s82, s60
s_cmp_eq_u32 s61, 8
s_cselect_b32 s60, 1024, 0
MM a[120:123],a[176:179],v[88:91],a[120:123]
s_or_b32 s82, s82, s60
EX v58,v58
MM a[124:127],a[184:187],v[88:91],a[124:127]
s_lshr_b32 s61, s55, 2
EX v59,v59
MM a[124:127],a[188:191],v[92:95],a[124:127]
s_sub_u32 s61, s61, s93
s_cmp_eq_u32 s61, 0
s_cselect_b32 s60, 8, 0
MM a[116:119],a[188:191],v[84:87],a[116:119]
s_or_b32 s82, s82, s60
s_cmp_eq_u32 s61, 8
s_cselect_b32 s60, 2048, 0
MM a[116:119],a[184:187],v[80:83],a[116:119]
s_or_b32 s82, s82, s60
EX v60,v60
MM a[108:111],a[184:187],v[72:75],a[108:111]
s_lshr_b32 s61, s56, 2
EX v61,v61
MM a[108:111],a[188:191],v[76:79],a[108:111]
s_sub_u32 s61, s61, s93
s_cmp_eq_u32 s61, 0
s_cselect_b32 s60, 16, 0
MM a[100:103],a[188:191],v[68:71],a[100:103]
s_or_b32 s82, s82, s60
s_cmp_eq_u32 s61, 8
s_cselect_b32 s60, 4096, 0
MM a[100:103],a[184:187],v[64:67],a[100:103]
s_or_b32 s82, s82, s60
EX v62,v62
v_smfmac_f32_16x16x64_bf16 v[236:239], v[144:147], v[0:7], v234
s_lshr_b32 s61, s57, 2
RD a[252:255],v227 offset:61440
EX v63,v63
v_smfmac_f32_16x16x64_bf16 v[236:239], v[148:151], v[8:15], v234
s_sub_u32 s61, s61, s93
CV v29,v58,v59
v_smfmac_f32_16x16x64_bf16 v[236:239], v[152:155], v[16:23], v234
CV v30,v60,v61
CV v31,v62,v63
s_nop 1
v_smfmac_f32_16x16x64_bf16 v[236:239], v[156:159], v[24:31], v234
s_waitcnt lgkmcnt(0)
buffer_load_dwordx4 v235, s[12:15], s64 offen offset:3072 lds
s_cmp_eq_u32 s61, 0
s_cselect_b32 s60, 32, 0
MM v[64:67],a[192:195],v[160:163],v[128:131]
s_or_b32 s82, s82, s60
s_cmp_eq_u32 s61, 8
s_cselect_b32 s60, 8192, 0
)ASM"
R"ASM(
MM v[64:67],a[196:199],v[176:179],v[64:67]
s_or_b32 s82, s82, s60
TR a[128:129],v232 offset:32768
TR a[130:131],v232 offset:36864
MM v[64:67],a[200:203],v[192:195],v[64:67]
s_lshr_b32 s61, s58, 2
s_add_u32 s64, s64, 16384
s_add_u32 s81, s64, 896
MM v[64:67],a[204:207],v[208:211],v[64:67]
s_sub_u32 s61, s61, s93
TR a[132:133],v232 offset:40960
v_add_f32 v246, v236, v237
MM v[68:71],a[220:223],v[208:211],v[128:131]
s_cmp_eq_u32 s61, 0
s_cselect_b32 s60, 64, 0
v_cmp_nge_f32 vcc, s71, v246
MM v[68:71],a[216:219],v[192:195],v[68:71]
s_cbranch_vccnz LSITE18_%=
LBACK18_%=:
TR a[134:135],v232 offset:45056
TR a[136:137],v233 offset:32768
MM v[68:71],a[212:215],v[176:179],v[68:71]
s_or_b32 s82, s82, s60
s_cmp_eq_u32 s61, 8
s_cselect_b32 s60, 16384, 0
MM v[68:71],a[208:211],v[160:163],v[68:71]
s_or_b32 s82, s82, s60
TR a[138:139],v233 offset:36864
TR a[140:141],v233 offset:40960
MM v[76:79],a[208:211],v[164:167],v[132:135]
s_lshr_b32 s61, s59, 2
EX v64,v64
MM v[76:79],a[212:215],v[180:183],v[76:79]
s_sub_u32 s61, s61, s93
s_cmp_eq_u32 s61, 0
s_cselect_b32 s60, 128, 0
MM v[76:79],a[216:219],v[196:199],v[76:79]
s_or_b32 s82, s82, s60
s_cmp_eq_u32 s61, 8
s_cselect_b32 s60, 32768, 0
MM v[76:79],a[220:223],v[212:215],v[76:79]
s_or_b32 s82, s82, s60
s_and_b32 s60, s82, 255
s_cselect_b32 s51, 0, 31
MM v[72:75],a[204:207],v[212:215],v[132:135]
EX v65,v65
MM v[72:75],a[200:203],v[196:199],v[72:75]
EX v66,v66
MM v[72:75],a[196:199],v[180:183],v[72:75]
TR a[142:143],v233 offset:45056
CV v64,v64,v65
MM v[72:75],a[192:195],v[164:167],v[72:75]
EX v67,v67
MM v[80:83],a[192:195],v[168:171],v[136:139]
TR a[144:145],v232 offset:33280
CV v65,v66,v67
MM v[80:83],a[196:199],v[184:187],v[80:83]
EX v68,v68
LPG12_%=:
s_cbranch_execz LTR0_%=
MM v[80:83],a[200:203],v[200:203],v[80:83]
TR a[146:147],v232 offset:37376
TR a[148:149],v232 offset:41472
MM v[80:83],a[204:207],v[216:219],v[80:83]
EX v69,v69
MM v[84:87],a[220:223],v[216:219],v[136:139]
EX v70,v70
MM v[84:87],a[216:219],v[200:203],v[84:87]
TR a[150:151],v232 offset:45568
CV v66,v68,v69
MM v[84:87],a[212:215],v[184:187],v[84:87]
EX v71,v71
MM v[84:87],a[208:211],v[168:171],v[84:87]
TR a[152:153],v233 offset:33280
CV v67,v70,v71
MM v[92:95],a[208:211],v[172:175],v[140:143]
EX v72,v72
MM v[92:95],a[212:215],v[188:191],v[92:95]
TR a[154:155],v233 offset:37376
TR a[156:157],v233 offset:41472
MM v[92:95],a[216:219],v[204:207],v[92:95]
EX v73,v73
MM v[92:95],a[220:223],v[220:223],v[92:95]
EX v74,v74
MM v[88:91],a[204:207],v[220:223],v[140:143]
TR a[158:159],v233 offset:45568
CV v72,v72,v73
MM v[88:91],a[200:203],v[204:207],v[88:91]
EX v75,v75
MM v[88:91],a[196:199],v[188:191],v[88:91]
TR a[160:161],v232 offset:33792
CV v73,v74,v75
MM v[88:91],a[192:195],v[172:175],v[88:91]
EX v76,v76
MM v[96:99],a[224:227],v[160:163],v[128:131]
TR a[162:163],v232 offset:37888
TR a[164:165],v232 offset:41984
MM v[96:99],a[228:231],v[176:179],v[96:99]
EX v77,v77
MM v[96:99],a[232:235],v[192:195],v[96:99]
TR a[166:167],v232 offset:46080
CV v74,v76,v77
MM v[96:99],a[236:239],v[208:211],v[96:99]
EX v78,v78
MM v[100:103],a[252:255],v[208:211],v[128:131]
TR a[168:169],v233 offset:33792
TR a[170:171],v233 offset:37888
MM v[100:103],a[248:251],v[192:195],v[100:103]
EX v79,v79
MM v[100:103],a[244:247],v[176:179],v[100:103]
TR a[172:173],v233 offset:41984
CV v75,v78,v79
MM v[100:103],a[240:243],v[160:163],v[100:103]
EX v80,v80
MM v[108:111],a[240:243],v[164:167],v[132:135]
EX v81,v81
MM v[108:111],a[244:247],v[180:183],v[108:111]
TR a[174:175],v233 offset:46080
CV v80,v80,v81
MM v[108:111],a[248:251],v[196:199],v[108:111]
EX v82,v82
MM v[108:111],a[252:255],v[212:215],v[108:111]
TR a[176:177],v232 offset:34304
TR a[178:179],v232 offset:38400
MM v[104:107],a[236:239],v[212:215],v[132:135]
EX v83,v83
MM v[104:107],a[232:235],v[196:199],v[104:107]
TR a[180:181],v232 offset:42496
CV v81,v82,v83
MM v[104:107],a[228:231],v[180:183],v[104:107]
EX v84,v84
MM v[104:107],a[224:227],v[164:167],v[104:107]
EX v85,v85
MM v[112:115],a[224:227],v[168:171],v[136:139]
TR a[182:183],v232 offset:46592
CV v82,v84,v85
MM v[112:115],a[228:231],v[184:187],v[112:115]
EX v86,v86
MM v[112:115],a[232:235],v[200:203],v[112:115]
TR a[184:185],v233 offset:34304
TR a[186:187],v233 offset:38400
MM v[112:115],a[236:239],v[216:219],v[112:115]
EX v87,v87
MM v[116:119],a[252:255],v[216:219],v[136:139]
TR a[188:189],v233 offset:42496
CV v83,v86,v87
MM v[116:119],a[248:251],v[200:203],v[116:119]
EX v88,v88
MM v[116:119],a[244:247],v[184:187],v[116:119]
EX v89,v89
MM v[116:119],a[240:243],v[168:171],v[116:119]
TR a[190:191],v233 offset:46592
CV v88,v88,v89
MM v[124:127],a[240:243],v[172:175],v[140:143]
EX v90,v90
MM v[124:127],a[244:247],v[188:191],v[124:127]
EX v91,v91
MM v[124:127],a[248:251],v[204:207],v[124:127]
EX v92,v92
MM v[124:127],a[252:255],v[220:223],v[124:127]
EX v93,v93
MM v[120:123],a[236:239],v[220:223],v[140:143]
EX v94,v94
MM v[120:123],a[232:235],v[204:207],v[120:123]
EX v95,v95
MM v[120:123],a[228:231],v[188:191],v[120:123]
EX v96,v96
MM v[120:123],a[224:227],v[172:175],v[120:123]
s_waitcnt vmcnt(14) lgkmcnt(0)
s_barrier
MM a[0:3],a[128:131],v[0:3],a[0:3]
s_waitcnt vmcnt(8)
v_cmp_ne_u32 vcc, s69, v252
s_cbranch_vccnz LRFG_%=
LRFGb_%=:
MM a[0:3],a[132:135],v[4:7],a[0:3]
s_add_u32 m0, s65, 32768
v_lshrrev_b32 v253, 8, v248
v_lshlrev_b32 v253, 10, v253
MM a[8:11],a[132:135],v[12:15],a[8:11]
v_and_b32 v252, 240, v248
v_add_u32 v253, v253, v252
MM a[8:11],a[128:131],v[8:11],a[8:11]
s_lshr_b32 s76, s48, 6
CV v89,v90,v91
CV v90,v92,v93
MM a[16:19],a[128:131],v[16:19],a[16:19]
s_lshl_b32 s76, s76, 13
RD a[192:195],v224 offset:0
CV v91,v94,v95
MM a[16:19],a[132:135],v[20:23],a[16:19]
s_bfe_u32 s90, s48, 131076
buffer_load_dwordx4 v228, s[8:11], s64 offen offset:0 lds
MM a[24:27],a[132:135],v[28:31],a[24:27]
s_lshl_b32 s90, s90, 8
EX v97,v97
MM a[24:27],a[128:131],v[24:27],a[24:27]
s_add_u32 s76, s76, s90
RD a[196:199],v225 offset:0
CV v68,v96,v97
MM a[28:31],a[136:139],v[24:27],a[28:31]
s_add_u32 s76, s76, 22183936
EX v98,v98
MM a[28:31],a[140:143],v[28:31],a[28:31]
s_lshl_b32 s90, %[swave], 11
RD a[200:203],v226 offset:0
MM a[20:23],a[140:143],v[20:23],a[20:23]
s_add_u32 s90, s90, %[slds]
s_add_u32 m0, s90, 131072
s_nop 0
buffer_load_dwordx4 v253, s[20:23], s76 offen offset:0 sc1 lds
MM a[20:23],a[136:139],v[16:19],a[20:23]
s_add_u32 m0, s90, 132096
s_add_u32 s76, s76, 4096
buffer_load_dwordx4 v253, s[20:23], s76 offen offset:0 sc1 lds
MM a[12:15],a[136:139],v[8:11],a[12:15]
EX v99,v99
MM a[12:15],a[140:143],v[12:15],a[12:15]
EX v100,v100
MM a[4:7],a[140:143],v[4:7],a[4:7]
EX v101,v101
MM a[4:7],a[136:139],v[0:3],a[4:7]
RD a[204:207],v227 offset:0
CV v69,v98,v99
MM a[32:35],a[144:147],v[0:3],a[32:35]
EX v102,v102
MM a[32:35],a[148:151],v[4:7],a[32:35]
s_add_u32 m0, s65, 32768
s_nop 0
buffer_load_dwordx4 v229, s[8:11], s64 offen offset:1024 lds
MM a[40:43],a[148:151],v[12:15],a[40:43]
EX v103,v103
MM a[40:43],a[144:147],v[8:11],a[40:43]
RD a[208:211],v224 offset:4096
CV v70,v100,v101
MM a[48:51],a[144:147],v[16:19],a[48:51]
EX v104,v104
MM a[48:51],a[148:151],v[20:23],a[48:51]
RD a[212:215],v225 offset:4096
CV v71,v102,v103
MM a[56:59],a[148:151],v[28:31],a[56:59]
EX v105,v105
MM a[56:59],a[144:147],v[24:27],a[56:59]
buffer_load_dwordx4 v230, s[8:11], s64 offen offset:2048 lds
MM a[60:63],a[152:155],v[24:27],a[60:63]
EX v106,v106
MM a[60:63],a[156:159],v[28:31],a[60:63]
EX v107,v107
MM a[52:55],a[156:159],v[20:23],a[52:55]
RD a[216:219],v226 offset:4096
CV v76,v104,v105
MM a[52:55],a[152:155],v[16:19],a[52:55]
EX v108,v108
MM a[44:47],a[152:155],v[8:11],a[44:47]
RD a[220:223],v227 offset:4096
CV v77,v106,v107
MM a[44:47],a[156:159],v[12:15],a[44:47]
EX v109,v109
MM a[36:39],a[156:159],v[4:7],a[36:39]
buffer_load_dwordx4 v231, s[8:11], s64 offen offset:3072 lds
MM a[36:39],a[152:155],v[0:3],a[36:39]
s_add_u32 m0, s65, 98304
EX v110,v110
MM a[64:67],a[160:163],v[0:3],a[64:67]
RD a[224:227],v224 offset:8192
CV v78,v108,v109
MM a[64:67],a[164:167],v[4:7],a[64:67]
EX v111,v111
MM a[72:75],a[164:167],v[12:15],a[72:75]
EX v112,v112
MM a[72:75],a[160:163],v[8:11],a[72:75]
RD a[228:231],v225 offset:8192
CV v79,v110,v111
MM a[80:83],a[160:163],v[16:19],a[80:83]
EX v113,v113
MM a[80:83],a[164:167],v[20:23],a[80:83]
buffer_load_dwordx4 v235, s[12:15], s81 offen offset:0 lds
MM a[88:91],a[164:167],v[28:31],a[88:91]
EX v114,v114
MM a[88:91],a[160:163],v[24:27],a[88:91]
RD a[232:235],v226 offset:8192
CV v84,v112,v113
MM a[92:95],a[168:171],v[24:27],a[92:95]
EX v115,v115
MM a[92:95],a[172:175],v[28:31],a[92:95]
RD a[236:239],v227 offset:8192
CV v85,v114,v115
MM a[84:87],a[172:175],v[20:23],a[84:87]
EX v116,v116
MM a[84:87],a[168:171],v[16:19],a[84:87]
EX v117,v117
MM a[76:79],a[168:171],v[8:11],a[76:79]
RD a[240:243],v224 offset:12288
CV v86,v116,v117
MM a[76:79],a[172:175],v[12:15],a[76:79]
EX v118,v118
MM a[68:71],a[172:175],v[4:7],a[68:71]
buffer_load_dwordx4 v235, s[12:15], s64 offen offset:1024 lds
MM a[68:71],a[168:171],v[0:3],a[68:71]
EX v119,v119
MM a[96:99],a[176:179],v[0:3],a[96:99]
RD a[244:247],v225 offset:12288
CV v87,v118,v119
MM a[96:99],a[180:183],v[4:7],a[96:99]
EX v120,v120
MM a[104:107],a[180:183],v[12:15],a[104:107]
EX v121,v121
MM a[104:107],a[176:179],v[8:11],a[104:107]
RD a[248:251],v226 offset:12288
CV v92,v120,v121
MM a[112:115],a[176:179],v[16:19],a[112:115]
buffer_load_dwordx4 v235, s[12:15], s81 offen offset:2048 lds
MM a[112:115],a[180:183],v[20:23],a[112:115]
EX v122,v122
MM a[120:123],a[180:183],v[28:31],a[120:123]
EX v123,v123
MM a[120:123],a[176:179],v[24:27],a[120:123]
RD a[252:255],v227 offset:12288
CV v93,v122,v123
MM a[124:127],a[184:187],v[24:27],a[124:127]
EX v124,v124
MM a[124:127],a[188:191],v[28:31],a[124:127]
EX v125,v125
MM a[116:119],a[188:191],v[20:23],a[116:119]
buffer_load_dwordx4 v235, s[12:15], s64 offen offset:3072 lds
MM a[116:119],a[184:187],v[16:19],a[116:119]
EX v126,v126
MM a[108:111],a[184:187],v[8:11],a[108:111]
EX v127,v127
MM a[108:111],a[188:191],v[12:15],a[108:111]
CV v94,v124,v125
CV v95,v126,v127
MM a[100:103],a[188:191],v[4:7],a[100:103]
MM a[100:103],a[184:187],v[0:3],a[100:103]
v_smfmac_f32_16x16x64_bf16 v[236:239], v[144:147], v[64:71], v234
v_smfmac_f32_16x16x64_bf16 v[236:239], v[148:151], v[72:79], v234
v_smfmac_f32_16x16x64_bf16 v[236:239], v[152:155], v[80:87], v234
v_smfmac_f32_16x16x64_bf16 v[236:239], v[156:159], v[88:95], v234
s_waitcnt lgkmcnt(0)
MM v[0:3],a[192:195],v[160:163],v[128:131]
s_add_u32 s64, s64, 16384
TR a[128:129],v232 offset:49152
s_add_u32 s81, s64, 896
MM v[0:3],a[196:199],v[176:179],v[0:3]
TR a[130:131],v232 offset:53248
TR a[132:133],v232 offset:57344
MM v[0:3],a[200:203],v[192:195],v[0:3]
TR a[134:135],v232 offset:61440
TR a[136:137],v233 offset:49152
MM v[0:3],a[204:207],v[208:211],v[0:3]
TR a[138:139],v233 offset:53248
v_add_f32 v247, v236, v237
MM v[4:7],a[220:223],v[208:211],v[128:131]
TR a[140:141],v233 offset:57344
v_cmp_nge_f32 vcc, s71, v247
MM v[4:7],a[216:219],v[192:195],v[4:7]
s_cbranch_vccnz LSITE19_%=
LBACK19_%=:
TR a[142:143],v233 offset:61440
TR a[144:145],v232 offset:49664
MM v[4:7],a[212:215],v[176:179],v[4:7]
v_lshrrev_b32 v253, 7, v248
v_mul_u32_u24 v253, 1280, v253
MM v[4:7],a[208:211],v[160:163],v[4:7]
v_and_b32 v252, 112, v248
v_add_u32 v253, v253, v252
MM v[12:15],a[208:211],v[164:167],v[132:135]
s_mul_i32 s90, %[swave], 30720
EX v0,v0
MM v[12:15],a[212:215],v[180:183],v[12:15]
s_mul_i32 s91, %[swave], 3072
EX v1,v1
MM v[12:15],a[216:219],v[196:199],v[12:15]
s_add_u32 s91, s91, %[slds]
s_add_u32 m0, s91, 151552
s_add_u32 s77, s90, 0
buffer_load_dwordx4 v253, s[28:31], s77 offen offset:0 lds
MM v[12:15],a[220:223],v[212:215],v[12:15]
TR a[146:147],v232 offset:53760
CV v0,v0,v1
MM v[8:11],a[204:207],v[212:215],v[132:135]
EX v2,v2
MM v[8:11],a[200:203],v[196:199],v[8:11]
s_add_u32 m0, s91, 152576
s_add_u32 s77, s90, 10240
buffer_load_dwordx4 v253, s[28:31], s77 offen offset:0 lds
MM v[8:11],a[196:199],v[180:183],v[8:11]
EX v3,v3
MM v[8:11],a[192:195],v[164:167],v[8:11]
TR a[148:149],v232 offset:57856
CV v1,v2,v3
)ASM"
R"ASM(
MM v[16:19],a[192:195],v[168:171],v[136:139]
s_add_u32 m0, s91, 153600
s_add_u32 s77, s90, 20480
buffer_load_dwordx4 v253, s[28:31], s77 offen offset:0 lds
MM v[16:19],a[196:199],v[184:187],v[16:19]
EX v4,v4
MM v[16:19],a[200:203],v[200:203],v[16:19]
EX v5,v5
MM v[16:19],a[204:207],v[216:219],v[16:19]
TR a[150:151],v232 offset:61952
CV v2,v4,v5
MM v[20:23],a[220:223],v[216:219],v[136:139]
EX v6,v6
MM v[20:23],a[216:219],v[200:203],v[20:23]
TR a[152:153],v233 offset:49664
TR a[154:155],v233 offset:53760
MM v[20:23],a[212:215],v[184:187],v[20:23]
EX v7,v7
MM v[20:23],a[208:211],v[168:171],v[20:23]
TR a[156:157],v233 offset:57856
CV v3,v6,v7
MM v[28:31],a[208:211],v[172:175],v[140:143]
EX v8,v8
MM v[28:31],a[212:215],v[188:191],v[28:31]
EX v9,v9
MM v[28:31],a[216:219],v[204:207],v[28:31]
TR a[158:159],v233 offset:61952
CV v8,v8,v9
MM v[28:31],a[220:223],v[220:223],v[28:31]
EX v10,v10
MM v[24:27],a[204:207],v[220:223],v[140:143]
TR a[160:161],v232 offset:50176
TR a[162:163],v232 offset:54272
MM v[24:27],a[200:203],v[204:207],v[24:27]
EX v11,v11
MM v[24:27],a[196:199],v[188:191],v[24:27]
TR a[164:165],v232 offset:58368
CV v9,v10,v11
MM v[24:27],a[192:195],v[172:175],v[24:27]
EX v12,v12
MM v[32:35],a[224:227],v[160:163],v[128:131]
EX v13,v13
MM v[32:35],a[228:231],v[176:179],v[32:35]
TR a[166:167],v232 offset:62464
CV v10,v12,v13
MM v[32:35],a[232:235],v[192:195],v[32:35]
EX v14,v14
MM v[32:35],a[236:239],v[208:211],v[32:35]
TR a[168:169],v233 offset:50176
TR a[170:171],v233 offset:54272
MM v[36:39],a[252:255],v[208:211],v[128:131]
EX v15,v15
MM v[36:39],a[248:251],v[192:195],v[36:39]
TR a[172:173],v233 offset:58368
CV v11,v14,v15
MM v[36:39],a[244:247],v[176:179],v[36:39]
EX v16,v16
MM v[36:39],a[240:243],v[160:163],v[36:39]
EX v17,v17
MM v[44:47],a[240:243],v[164:167],v[132:135]
TR a[174:175],v233 offset:62464
CV v16,v16,v17
MM v[44:47],a[244:247],v[180:183],v[44:47]
EX v18,v18
MM v[44:47],a[248:251],v[196:199],v[44:47]
TR a[176:177],v232 offset:50688
TR a[178:179],v232 offset:54784
MM v[44:47],a[252:255],v[212:215],v[44:47]
EX v19,v19
MM v[40:43],a[236:239],v[212:215],v[132:135]
TR a[180:181],v232 offset:58880
CV v17,v18,v19
MM v[40:43],a[232:235],v[196:199],v[40:43]
EX v20,v20
MM v[40:43],a[228:231],v[180:183],v[40:43]
TR a[182:183],v232 offset:62976
TR a[184:185],v233 offset:50688
MM v[40:43],a[224:227],v[164:167],v[40:43]
EX v21,v21
MM v[48:51],a[224:227],v[168:171],v[136:139]
TR a[186:187],v233 offset:54784
CV v18,v20,v21
MM v[48:51],a[228:231],v[184:187],v[48:51]
EX v22,v22
MM v[48:51],a[232:235],v[200:203],v[48:51]
EX v23,v23
MM v[48:51],a[236:239],v[216:219],v[48:51]
TR a[188:189],v233 offset:58880
CV v19,v22,v23
MM v[52:55],a[252:255],v[216:219],v[136:139]
EX v24,v24
MM v[52:55],a[248:251],v[200:203],v[52:55]
EX v25,v25
MM v[52:55],a[244:247],v[184:187],v[52:55]
TR a[190:191],v233 offset:62976
CV v24,v24,v25
MM v[52:55],a[240:243],v[168:171],v[52:55]
EX v26,v26
MM v[60:63],a[240:243],v[172:175],v[140:143]
EX v27,v27
MM v[60:63],a[244:247],v[188:191],v[60:63]
EX v28,v28
MM v[60:63],a[248:251],v[204:207],v[60:63]
EX v29,v29
MM v[60:63],a[252:255],v[220:223],v[60:63]
EX v30,v30
MM v[56:59],a[236:239],v[220:223],v[140:143]
EX v31,v31
MM v[56:59],a[232:235],v[204:207],v[56:59]
EX v32,v32
MM v[56:59],a[228:231],v[188:191],v[56:59]
EX v33,v33
MM v[56:59],a[224:227],v[172:175],v[56:59]
s_waitcnt vmcnt(17) lgkmcnt(0)
s_barrier
MM a[0:3],a[128:131],v[64:67],a[0:3]
s_add_u32 m0, s65, 49152
CV v25,v26,v27
CV v26,v28,v29
MM a[0:3],a[132:135],v[68:71],a[0:3]
RD a[192:195],v224 offset:16384
CV v27,v30,v31
MM a[8:11],a[132:135],v[76:79],a[8:11]
buffer_load_dwordx4 v228, s[8:11], s64 offen offset:0 lds
MM a[8:11],a[128:131],v[72:75],a[8:11]
RD a[196:199],v225 offset:16384
CV v4,v32,v33
MM a[16:19],a[128:131],v[80:83],a[16:19]
RD a[200:203],v226 offset:16384
RD a[204:207],v227 offset:16384
MM a[16:19],a[132:135],v[84:87],a[16:19]
buffer_load_dwordx4 v229, s[8:11], s64 offen offset:1024 lds
MM a[24:27],a[132:135],v[92:95],a[24:27]
EX v34,v34
MM a[24:27],a[128:131],v[88:91],a[24:27]
RD a[208:211],v224 offset:20480
RD a[212:215],v225 offset:20480
MM a[28:31],a[136:139],v[88:91],a[28:31]
EX v35,v35
MM a[28:31],a[140:143],v[92:95],a[28:31]
RD a[216:219],v226 offset:20480
CV v5,v34,v35
MM a[20:23],a[140:143],v[84:87],a[20:23]
EX v36,v36
MM a[20:23],a[136:139],v[80:83],a[20:23]
EX v37,v37
MM a[12:15],a[136:139],v[72:75],a[12:15]
RD a[220:223],v227 offset:20480
CV v6,v36,v37
MM a[12:15],a[140:143],v[76:79],a[12:15]
buffer_load_dwordx4 v230, s[8:11], s64 offen offset:2048 lds
MM a[4:7],a[140:143],v[68:71],a[4:7]
EX v38,v38
MM a[4:7],a[136:139],v[64:67],a[4:7]
EX v39,v39
MM a[32:35],a[144:147],v[64:67],a[32:35]
RD a[224:227],v224 offset:24576
CV v7,v38,v39
MM a[32:35],a[148:151],v[68:71],a[32:35]
EX v40,v40
MM a[40:43],a[148:151],v[76:79],a[40:43]
EX v41,v41
MM a[40:43],a[144:147],v[72:75],a[40:43]
RD a[228:231],v225 offset:24576
CV v12,v40,v41
MM a[48:51],a[144:147],v[80:83],a[48:51]
buffer_load_dwordx4 v231, s[8:11], s64 offen offset:3072 lds
MM a[48:51],a[148:151],v[84:87],a[48:51]
s_add_u32 m0, s65, 114688
EX v42,v42
MM a[56:59],a[148:151],v[92:95],a[56:59]
EX v43,v43
MM a[56:59],a[144:147],v[88:91],a[56:59]
RD a[232:235],v226 offset:24576
CV v13,v42,v43
MM a[60:63],a[152:155],v[88:91],a[60:63]
EX v44,v44
MM a[60:63],a[156:159],v[92:95],a[60:63]
buffer_load_dwordx4 v235, s[12:15], s81 offen offset:0 lds
MM a[52:55],a[156:159],v[84:87],a[52:55]
EX v45,v45
MM a[52:55],a[152:155],v[80:83],a[52:55]
RD a[236:239],v227 offset:24576
CV v14,v44,v45
MM a[44:47],a[152:155],v[72:75],a[44:47]
EX v46,v46
MM a[44:47],a[156:159],v[76:79],a[44:47]
EX v47,v47
MM a[36:39],a[156:159],v[68:71],a[36:39]
RD a[240:243],v224 offset:28672
CV v15,v46,v47
MM a[36:39],a[152:155],v[64:67],a[36:39]
EX v48,v48
MM a[64:67],a[160:163],v[64:67],a[64:67]
buffer_load_dwordx4 v235, s[12:15], s64 offen offset:1024 lds
MM a[64:67],a[164:167],v[68:71],a[64:67]
EX v49,v49
MM a[72:75],a[164:167],v[76:79],a[72:75]
RD a[244:247],v225 offset:28672
CV v20,v48,v49
MM a[72:75],a[160:163],v[72:75],a[72:75]
EX v50,v50
MM a[80:83],a[160:163],v[80:83],a[80:83]
EX v51,v51
MM a[80:83],a[164:167],v[84:87],a[80:83]
RD a[248:251],v226 offset:28672
CV v21,v50,v51
MM a[88:91],a[164:167],v[92:95],a[88:91]
EX v52,v52
MM a[88:91],a[160:163],v[88:91],a[88:91]
buffer_load_dwordx4 v235, s[12:15], s81 offen offset:2048 lds
MM a[92:95],a[168:171],v[88:91],a[92:95]
EX v53,v53
MM a[92:95],a[172:175],v[92:95],a[92:95]
RD a[252:255],v227 offset:28672
CV v22,v52,v53
MM a[84:87],a[172:175],v[84:87],a[84:87]
EX v54,v54
MM a[84:87],a[168:171],v[80:83],a[84:87]
EX v55,v55
MM a[76:79],a[168:171],v[72:75],a[76:79]
CV v23,v54,v55
MM a[76:79],a[172:175],v[76:79],a[76:79]
EX v56,v56
MM a[68:71],a[172:175],v[68:71],a[68:71]
buffer_load_dwordx4 v235, s[12:15], s64 offen offset:3072 lds
MM a[68:71],a[168:171],v[64:67],a[68:71]
EX v57,v57
MM a[96:99],a[176:179],v[64:67],a[96:99]
EX v58,v58
MM a[96:99],a[180:183],v[68:71],a[96:99]
EX v59,v59
MM a[104:107],a[180:183],v[76:79],a[104:107]
EX v60,v60
MM a[104:107],a[176:179],v[72:75],a[104:107]
EX v61,v61
MM a[112:115],a[176:179],v[80:83],a[112:115]
EX v62,v62
MM a[112:115],a[180:183],v[84:87],a[112:115]
EX v63,v63
MM a[120:123],a[180:183],v[92:95],a[120:123]
CV v28,v56,v57
CV v29,v58,v59
MM a[120:123],a[176:179],v[88:91],a[120:123]
CV v30,v60,v61
CV v31,v62,v63
MM a[124:127],a[184:187],v[88:91],a[124:127]
MM a[124:127],a[188:191],v[92:95],a[124:127]
MM a[116:119],a[188:191],v[84:87],a[116:119]
MM a[116:119],a[184:187],v[80:83],a[116:119]
MM a[108:111],a[184:187],v[72:75],a[108:111]
MM a[108:111],a[188:191],v[76:79],a[108:111]
MM a[100:103],a[188:191],v[68:71],a[100:103]
MM a[100:103],a[184:187],v[64:67],a[100:103]
v_smfmac_f32_16x16x64_bf16 v[236:239], v[144:147], v[0:7], v234
v_smfmac_f32_16x16x64_bf16 v[236:239], v[148:151], v[8:15], v234
v_smfmac_f32_16x16x64_bf16 v[236:239], v[152:155], v[16:23], v234
LPG13_%=:
s_nop 0
v_smfmac_f32_16x16x64_bf16 v[236:239], v[156:159], v[24:31], v234
s_waitcnt lgkmcnt(0)
MM v[64:67],a[192:195],v[160:163],v[128:131]
s_add_u32 s64, s64, 16384
TR a[128:129],v232 offset:0
s_add_u32 s81, s64, 896
MM v[64:67],a[196:199],v[176:179],v[64:67]
TR a[130:131],v232 offset:4096
TR a[132:133],v232 offset:8192
MM v[64:67],a[200:203],v[192:195],v[64:67]
TR a[134:135],v232 offset:12288
TR a[136:137],v233 offset:0
MM v[64:67],a[204:207],v[208:211],v[64:67]
TR a[138:139],v233 offset:4096
v_add_f32 v246, v236, v237
MM v[68:71],a[220:223],v[208:211],v[128:131]
TR a[140:141],v233 offset:8192
v_cmp_nge_f32 vcc, s71, v246
MM v[68:71],a[216:219],v[192:195],v[68:71]
s_cbranch_vccnz LSITE20_%=
LBACK20_%=:
TR a[142:143],v233 offset:12288
TR a[144:145],v232 offset:512
MM v[68:71],a[212:215],v[176:179],v[68:71]
v_lshrrev_b32 v43, 7, v248
v_bfe_u32 v42, v248, 6, 1
MM v[68:71],a[208:211],v[160:163],v[68:71]
v_lshl_add_u32 v40, v42, 3, v43
v_lshlrev_b32 v40, 4, v40
MM v[76:79],a[208:211],v[164:167],v[132:135]
v_bfe_u32 v42, v248, 4, 2
v_lshl_add_u32 v40, v42, 2, v40
MM v[76:79],a[212:215],v[180:183],v[76:79]
s_lshl_b32 s76, %[swave], 11
EX v64,v64
MM v[76:79],a[216:219],v[196:199],v[76:79]
s_add_u32 s76, s76, %[slds]
EX v65,v65
MM v[76:79],a[220:223],v[212:215],v[76:79]
s_add_u32 s76, s76, 131072
v_add_u32 v40, s76, v40
CV v64,v64,v65
MM v[72:75],a[204:207],v[212:215],v[132:135]
s_waitcnt vmcnt(18)
ds_read_b32 v32, v40 offset:0
ds_read_b32 v36, v40 offset:256
MM v[72:75],a[200:203],v[196:199],v[72:75]
ds_read_b32 v33, v40 offset:512
ds_read_b32 v37, v40 offset:768
MM v[72:75],a[196:199],v[180:183],v[72:75]
ds_read_b32 v34, v40 offset:1024
ds_read_b32 v38, v40 offset:1280
MM v[72:75],a[192:195],v[164:167],v[72:75]
ds_read_b32 v35, v40 offset:1536
ds_read_b32 v39, v40 offset:1792
MM v[80:83],a[192:195],v[168:171],v[136:139]
v_mul_u32_u24 v41, 3456, v43
v_bfe_u32 v42, v248, 4, 3
MM v[80:83],a[196:199],v[184:187],v[80:83]
TR a[146:147],v232 offset:4608
v_lshl_add_u32 v41, v42, 1, v41
MM v[80:83],a[200:203],v[200:203],v[80:83]
s_add_u32 s76, s48, 21626880
EX v66,v66
MM v[80:83],a[204:207],v[216:219],v[80:83]
s_lshl_b32 s77, %[swave], 4
EX v67,v67
MM v[84:87],a[220:223],v[216:219],v[136:139]
EX v68,v68
MM v[84:87],a[216:219],v[200:203],v[84:87]
s_waitcnt lgkmcnt(1)
v_add_f32 v32, v32, v33
v_add_f32 v36, v36, v37
MM v[84:87],a[212:215],v[184:187],v[84:87]
v_add_f32 v34, v34, v35
v_add_f32 v38, v38, v39
EX v69,v69
MM v[84:87],a[208:211],v[168:171],v[84:87]
v_add_f32 v32, v32, v34
v_add_f32 v36, v36, v38
EX v70,v70
MM v[92:95],a[208:211],v[172:175],v[140:143]
CV v42,v32,v36
v_lshlrev_b32 v32, 16, v42
MM v[92:95],a[212:215],v[188:191],v[92:95]
TR a[148:149],v232 offset:8704
v_and_b32 v36, 0xffff0000, v42
v_mul_f32 v42, 0xbfb8aa3b, v32
EX v71,v71
MM v[92:95],a[216:219],v[204:207],v[92:95]
EX v42,v42
MM v[92:95],a[220:223],v[220:223],v[92:95]
TR a[150:151],v232 offset:12800
v_add_f32 v42, 1.0, v42
EX v72,v72
MM v[88:91],a[204:207],v[220:223],v[140:143]
v_rcp_f32 v42, v42
MM v[88:91],a[200:203],v[204:207],v[88:91]
TR a[152:153],v233 offset:512
v_mul_f32 v42, v32, v42
CV v42,v42,v42
EX v73,v73
CV v65,v66,v67
MM v[88:91],a[196:199],v[188:191],v[88:91]
TR a[154:155],v233 offset:4608
v_lshlrev_b32 v42, 16, v42
v_mul_f32 v32, v42, v36
MM v[88:91],a[192:195],v[172:175],v[88:91]
CV v43,v32,v32
CV v66,v68,v69
EX v74,v74
MM v[96:99],a[224:227],v[160:163],v[128:131]
s_lshl_b64 exec, 0xffff, s77
buffer_store_short v43, v41, s[20:23], s76 offen offset:0 sc1
s_mov_b64 exec, -1
s_nop 0
TR a[156:157],v233 offset:8704
EX v75,v75
MM v[96:99],a[228:231],v[176:179],v[96:99]
TR a[158:159],v233 offset:12800
CV v67,v70,v71
MM v[96:99],a[232:235],v[192:195],v[96:99]
EX v76,v76
MM v[96:99],a[236:239],v[208:211],v[96:99]
TR a[160:161],v232 offset:1024
TR a[162:163],v232 offset:5120
MM v[100:103],a[252:255],v[208:211],v[128:131]
EX v77,v77
MM v[100:103],a[248:251],v[192:195],v[100:103]
EX v78,v78
MM v[100:103],a[244:247],v[176:179],v[100:103]
TR a[164:165],v232 offset:9216
CV v72,v72,v73
MM v[100:103],a[240:243],v[160:163],v[100:103]
EX v79,v79
MM v[108:111],a[240:243],v[164:167],v[132:135]
)ASM"
R"ASM(
TR a[166:167],v232 offset:13312
CV v73,v74,v75
MM v[108:111],a[244:247],v[180:183],v[108:111]
EX v80,v80
MM v[108:111],a[248:251],v[196:199],v[108:111]
TR a[168:169],v233 offset:1024
TR a[170:171],v233 offset:5120
MM v[108:111],a[252:255],v[212:215],v[108:111]
EX v81,v81
MM v[104:107],a[236:239],v[212:215],v[132:135]
TR a[172:173],v233 offset:9216
CV v74,v76,v77
MM v[104:107],a[232:235],v[196:199],v[104:107]
EX v82,v82
MM v[104:107],a[228:231],v[180:183],v[104:107]
EX v83,v83
MM v[104:107],a[224:227],v[164:167],v[104:107]
TR a[174:175],v233 offset:13312
CV v75,v78,v79
MM v[112:115],a[224:227],v[168:171],v[136:139]
EX v84,v84
MM v[112:115],a[228:231],v[184:187],v[112:115]
TR a[176:177],v232 offset:1536
TR a[178:179],v232 offset:5632
MM v[112:115],a[232:235],v[200:203],v[112:115]
EX v85,v85
MM v[112:115],a[236:239],v[216:219],v[112:115]
TR a[180:181],v232 offset:9728
CV v80,v80,v81
MM v[116:119],a[252:255],v[216:219],v[136:139]
EX v86,v86
MM v[116:119],a[248:251],v[200:203],v[116:119]
TR a[182:183],v232 offset:13824
CV v81,v82,v83
MM v[116:119],a[244:247],v[184:187],v[116:119]
EX v87,v87
MM v[116:119],a[240:243],v[168:171],v[116:119]
TR a[184:185],v233 offset:1536
CV v82,v84,v85
MM v[124:127],a[240:243],v[172:175],v[140:143]
EX v88,v88
MM v[124:127],a[244:247],v[188:191],v[124:127]
TR a[186:187],v233 offset:5632
TR a[188:189],v233 offset:9728
MM v[124:127],a[248:251],v[204:207],v[124:127]
TR a[190:191],v233 offset:13824
CV v83,v86,v87
MM v[124:127],a[252:255],v[220:223],v[124:127]
EX v89,v89
MM v[120:123],a[236:239],v[220:223],v[140:143]
EX v90,v90
MM v[120:123],a[232:235],v[204:207],v[120:123]
EX v91,v91
MM v[120:123],a[228:231],v[188:191],v[120:123]
EX v92,v92
MM v[120:123],a[224:227],v[172:175],v[120:123]
s_waitcnt vmcnt(16) lgkmcnt(0)
s_barrier
MM a[0:3],a[128:131],v[0:3],a[0:3]
s_add_u32 m0, s65, 0
CV v88,v88,v89
CV v89,v90,v91
MM a[0:3],a[132:135],v[4:7],a[0:3]
EX v93,v93
MM a[8:11],a[132:135],v[12:15],a[8:11]
EX v94,v94
MM a[8:11],a[128:131],v[8:11],a[8:11]
EX v95,v95
MM a[16:19],a[128:131],v[16:19],a[16:19]
RD a[192:195],v224 offset:32768
CV v90,v92,v93
MM a[16:19],a[132:135],v[20:23],a[16:19]
EX v96,v96
MM a[24:27],a[132:135],v[28:31],a[24:27]
RD a[196:199],v225 offset:32768
CV v91,v94,v95
MM a[24:27],a[128:131],v[24:27],a[24:27]
EX v97,v97
MM a[28:31],a[136:139],v[24:27],a[28:31]
buffer_load_dwordx4 v228, s[8:11], s64 offen offset:0 lds
MM a[28:31],a[140:143],v[28:31],a[28:31]
EX v98,v98
MM a[20:23],a[140:143],v[20:23],a[20:23]
EX v99,v99
MM a[20:23],a[136:139],v[16:19],a[20:23]
RD a[200:203],v226 offset:32768
CV v68,v96,v97
MM a[12:15],a[136:139],v[8:11],a[12:15]
EX v100,v100
MM a[12:15],a[140:143],v[12:15],a[12:15]
RD a[204:207],v227 offset:32768
CV v69,v98,v99
MM a[4:7],a[140:143],v[4:7],a[4:7]
EX v101,v101
MM a[4:7],a[136:139],v[0:3],a[4:7]
buffer_load_dwordx4 v229, s[8:11], s64 offen offset:1024 lds
MM a[32:35],a[144:147],v[0:3],a[32:35]
EX v102,v102
MM a[32:35],a[148:151],v[4:7],a[32:35]
RD a[208:211],v224 offset:36864
CV v70,v100,v101
MM a[40:43],a[148:151],v[12:15],a[40:43]
EX v103,v103
MM a[40:43],a[144:147],v[8:11],a[40:43]
RD a[212:215],v225 offset:36864
CV v71,v102,v103
MM a[48:51],a[144:147],v[16:19],a[48:51]
EX v104,v104
MM a[48:51],a[148:151],v[20:23],a[48:51]
EX v105,v105
MM a[56:59],a[148:151],v[28:31],a[56:59]
buffer_load_dwordx4 v230, s[8:11], s64 offen offset:2048 lds
MM a[56:59],a[144:147],v[24:27],a[56:59]
EX v106,v106
MM a[60:63],a[152:155],v[24:27],a[60:63]
RD a[216:219],v226 offset:36864
CV v76,v104,v105
MM a[60:63],a[156:159],v[28:31],a[60:63]
EX v107,v107
MM a[52:55],a[156:159],v[20:23],a[52:55]
RD a[220:223],v227 offset:36864
CV v77,v106,v107
MM a[52:55],a[152:155],v[16:19],a[52:55]
EX v108,v108
MM a[44:47],a[152:155],v[8:11],a[44:47]
EX v109,v109
MM a[44:47],a[156:159],v[12:15],a[44:47]
buffer_load_dwordx4 v231, s[8:11], s64 offen offset:3072 lds
MM a[36:39],a[156:159],v[4:7],a[36:39]
s_add_u32 m0, s65, 65536
RD a[224:227],v224 offset:40960
CV v78,v108,v109
MM a[36:39],a[152:155],v[0:3],a[36:39]
EX v110,v110
MM a[64:67],a[160:163],v[0:3],a[64:67]
EX v111,v111
MM a[64:67],a[164:167],v[4:7],a[64:67]
RD a[228:231],v225 offset:40960
CV v79,v110,v111
MM a[72:75],a[164:167],v[12:15],a[72:75]
EX v112,v112
MM a[72:75],a[160:163],v[8:11],a[72:75]
buffer_load_dwordx4 v235, s[12:15], s81 offen offset:0 lds
MM a[80:83],a[160:163],v[16:19],a[80:83]
EX v113,v113
MM a[80:83],a[164:167],v[20:23],a[80:83]
RD a[232:235],v226 offset:40960
CV v84,v112,v113
MM a[88:91],a[164:167],v[28:31],a[88:91]
EX v114,v114
MM a[88:91],a[160:163],v[24:27],a[88:91]
EX v115,v115
MM a[92:95],a[168:171],v[24:27],a[92:95]
RD a[236:239],v227 offset:40960
CV v85,v114,v115
MM a[92:95],a[172:175],v[28:31],a[92:95]
EX v116,v116
MM a[84:87],a[172:175],v[20:23],a[84:87]
buffer_load_dwordx4 v235, s[12:15], s64 offen offset:1024 lds
MM a[84:87],a[168:171],v[16:19],a[84:87]
EX v117,v117
MM a[76:79],a[168:171],v[8:11],a[76:79]
RD a[240:243],v224 offset:45056
CV v86,v116,v117
MM a[76:79],a[172:175],v[12:15],a[76:79]
EX v118,v118
MM a[68:71],a[172:175],v[4:7],a[68:71]
EX v119,v119
MM a[68:71],a[168:171],v[0:3],a[68:71]
RD a[244:247],v225 offset:45056
CV v87,v118,v119
MM a[96:99],a[176:179],v[0:3],a[96:99]
EX v120,v120
MM a[96:99],a[180:183],v[4:7],a[96:99]
buffer_load_dwordx4 v235, s[12:15], s81 offen offset:2048 lds
MM a[104:107],a[180:183],v[12:15],a[104:107]
EX v121,v121
MM a[104:107],a[176:179],v[8:11],a[104:107]
RD a[248:251],v226 offset:45056
CV v92,v120,v121
MM a[112:115],a[176:179],v[16:19],a[112:115]
EX v122,v122
MM a[112:115],a[180:183],v[20:23],a[112:115]
EX v123,v123
MM a[120:123],a[180:183],v[28:31],a[120:123]
RD a[252:255],v227 offset:45056
CV v93,v122,v123
MM a[120:123],a[176:179],v[24:27],a[120:123]
EX v124,v124
MM a[124:127],a[184:187],v[24:27],a[124:127]
buffer_load_dwordx4 v235, s[12:15], s64 offen offset:3072 lds
MM a[124:127],a[188:191],v[28:31],a[124:127]
EX v125,v125
MM a[116:119],a[188:191],v[20:23],a[116:119]
EX v126,v126
MM a[116:119],a[184:187],v[16:19],a[116:119]
EX v127,v127
MM a[108:111],a[184:187],v[8:11],a[108:111]
CV v94,v124,v125
CV v95,v126,v127
MM a[108:111],a[188:191],v[12:15],a[108:111]
MM a[100:103],a[188:191],v[4:7],a[100:103]
MM a[100:103],a[184:187],v[0:3],a[100:103]
v_smfmac_f32_16x16x64_bf16 v[236:239], v[144:147], v[64:71], v234
v_smfmac_f32_16x16x64_bf16 v[236:239], v[148:151], v[72:79], v234
v_smfmac_f32_16x16x64_bf16 v[236:239], v[152:155], v[80:87], v234
v_smfmac_f32_16x16x64_bf16 v[236:239], v[156:159], v[88:95], v234
s_waitcnt lgkmcnt(0)
MM v[0:3],a[192:195],v[160:163],v[128:131]
s_add_u32 s64, s64, 16384
TR a[128:129],v232 offset:16384
s_add_u32 s81, s64, 896
MM v[0:3],a[196:199],v[176:179],v[0:3]
TR a[130:131],v232 offset:20480
TR a[132:133],v232 offset:24576
MM v[0:3],a[200:203],v[192:195],v[0:3]
TR a[134:135],v232 offset:28672
TR a[136:137],v233 offset:16384
MM v[0:3],a[204:207],v[208:211],v[0:3]
TR a[138:139],v233 offset:20480
v_add_f32 v247, v236, v237
MM v[4:7],a[220:223],v[208:211],v[128:131]
TR a[140:141],v233 offset:24576
v_cmp_nge_f32 vcc, s71, v247
MM v[4:7],a[216:219],v[192:195],v[4:7]
s_cbranch_vccnz LSITE21_%=
LBACK21_%=:
TR a[142:143],v233 offset:28672
TR a[144:145],v232 offset:16896
MM v[4:7],a[212:215],v[176:179],v[4:7]
s_lshr_b32 s77, %[sb], 3
TR a[146:147],v232 offset:20992
TR a[148:149],v232 offset:25088
MM v[4:7],a[208:211],v[160:163],v[4:7]
s_lshl_b32 s77, s77, 2
TR a[150:151],v232 offset:29184
TR a[152:153],v233 offset:16896
MM v[12:15],a[208:211],v[164:167],v[132:135]
s_add_u32 s77, s77, %[swave]
EX v0,v0
MM v[12:15],a[212:215],v[180:183],v[12:15]
s_lshl_b32 s77, s77, 9
EX v1,v1
MM v[12:15],a[216:219],v[196:199],v[12:15]
s_add_u32 s77, s77, 0
v_lshrrev_b32 v253, 1, v248
v_add_u32 v253, s77, v253
MM v[12:15],a[220:223],v[212:215],v[12:15]
s_mov_b32 s77, LKEND_ka-LPD1_%=-16
v_min_u32 v253, s77, v253
CV v0,v0,v1
MM v[8:11],a[204:207],v[212:215],v[132:135]
s_getpc_b64 s[98:99]
LPD1_%=:
global_load_dword v253, v253, s[98:99]
MM v[8:11],a[200:203],v[196:199],v[8:11]
EX v2,v2
MM v[8:11],a[196:199],v[180:183],v[8:11]
EX v3,v3
MM v[8:11],a[192:195],v[164:167],v[8:11]
TR a[154:155],v233 offset:20992
CV v1,v2,v3
MM v[16:19],a[192:195],v[168:171],v[136:139]
EX v4,v4
MM v[16:19],a[196:199],v[184:187],v[16:19]
EX v5,v5
MM v[16:19],a[200:203],v[200:203],v[16:19]
TR a[156:157],v233 offset:25088
CV v2,v4,v5
MM v[16:19],a[204:207],v[216:219],v[16:19]
EX v6,v6
MM v[20:23],a[220:223],v[216:219],v[136:139]
EX v7,v7
MM v[20:23],a[216:219],v[200:203],v[20:23]
TR a[158:159],v233 offset:29184
CV v3,v6,v7
MM v[20:23],a[212:215],v[184:187],v[20:23]
EX v8,v8
MM v[20:23],a[208:211],v[168:171],v[20:23]
EX v9,v9
MM v[28:31],a[208:211],v[172:175],v[140:143]
s_add_u32 m0, s45, 147456
s_add_u32 s44, s44, 1024
buffer_load_dwordx4 v248, s[24:27], s44 offen offset:0 lds
MM v[28:31],a[212:215],v[188:191],v[28:31]
TR a[160:161],v232 offset:17408
CV v8,v8,v9
MM v[28:31],a[216:219],v[204:207],v[28:31]
EX v10,v10
MM v[28:31],a[220:223],v[220:223],v[28:31]
EX v11,v11
MM v[24:27],a[204:207],v[220:223],v[140:143]
TR a[162:163],v232 offset:21504
CV v9,v10,v11
MM v[24:27],a[200:203],v[204:207],v[24:27]
TR a[164:165],v232 offset:25600
TR a[166:167],v232 offset:29696
MM v[24:27],a[196:199],v[188:191],v[24:27]
EX v12,v12
MM v[24:27],a[192:195],v[172:175],v[24:27]
EX v13,v13
MM v[32:35],a[224:227],v[160:163],v[128:131]
TR a[168:169],v233 offset:17408
CV v10,v12,v13
MM v[32:35],a[228:231],v[176:179],v[32:35]
EX v14,v14
MM v[32:35],a[232:235],v[192:195],v[32:35]
TR a[170:171],v233 offset:21504
TR a[172:173],v233 offset:25600
MM v[32:35],a[236:239],v[208:211],v[32:35]
EX v15,v15
MM v[36:39],a[252:255],v[208:211],v[128:131]
TR a[174:175],v233 offset:29696
CV v11,v14,v15
MM v[36:39],a[248:251],v[192:195],v[36:39]
EX v16,v16
MM v[36:39],a[244:247],v[176:179],v[36:39]
EX v17,v17
MM v[36:39],a[240:243],v[160:163],v[36:39]
TR a[176:177],v232 offset:17920
CV v16,v16,v17
MM v[44:47],a[240:243],v[164:167],v[132:135]
s_add_u32 s67, s95, 15360
buffer_store_dwordx4 v[240:243], v248, s[20:23], s67 offen offset:0 sc1
MM v[44:47],a[244:247],v[180:183],v[44:47]
EX v18,v18
MM v[44:47],a[248:251],v[196:199],v[44:47]
TR a[178:179],v232 offset:22016
TR a[180:181],v232 offset:26112
MM v[44:47],a[252:255],v[212:215],v[44:47]
EX v19,v19
MM v[40:43],a[236:239],v[212:215],v[132:135]
TR a[182:183],v232 offset:30208
CV v17,v18,v19
MM v[40:43],a[232:235],v[196:199],v[40:43]
EX v20,v20
MM v[40:43],a[228:231],v[180:183],v[40:43]
EX v21,v21
MM v[40:43],a[224:227],v[164:167],v[40:43]
TR a[184:185],v233 offset:17920
CV v18,v20,v21
MM v[48:51],a[224:227],v[168:171],v[136:139]
EX v22,v22
MM v[48:51],a[228:231],v[184:187],v[48:51]
TR a[186:187],v233 offset:22016
TR a[188:189],v233 offset:26112
MM v[48:51],a[232:235],v[200:203],v[48:51]
EX v23,v23
MM v[48:51],a[236:239],v[216:219],v[48:51]
TR a[190:191],v233 offset:30208
CV v19,v22,v23
MM v[52:55],a[252:255],v[216:219],v[136:139]
EX v24,v24
MM v[52:55],a[248:251],v[200:203],v[52:55]
EX v25,v25
MM v[52:55],a[244:247],v[184:187],v[52:55]
EX v26,v26
MM v[52:55],a[240:243],v[168:171],v[52:55]
EX v27,v27
MM v[60:63],a[240:243],v[172:175],v[140:143]
EX v28,v28
MM v[60:63],a[244:247],v[188:191],v[60:63]
EX v29,v29
MM v[60:63],a[248:251],v[204:207],v[60:63]
EX v30,v30
MM v[60:63],a[252:255],v[220:223],v[60:63]
EX v31,v31
MM v[56:59],a[236:239],v[220:223],v[140:143]
EX v32,v32
MM v[56:59],a[232:235],v[204:207],v[56:59]
EX v33,v33
MM v[56:59],a[228:231],v[188:191],v[56:59]
EX v34,v34
MM v[56:59],a[224:227],v[172:175],v[56:59]
s_waitcnt vmcnt(16) lgkmcnt(0)
s_barrier
MM a[0:3],a[128:131],v[64:67],a[0:3]
s_add_u32 m0, s65, 16384
CV v24,v24,v25
CV v25,v26,v27
MM a[0:3],a[132:135],v[68:71],a[0:3]
RD a[192:195],v224 offset:49152
CV v26,v28,v29
MM a[8:11],a[132:135],v[76:79],a[8:11]
RD a[196:199],v225 offset:49152
CV v27,v30,v31
MM a[8:11],a[128:131],v[72:75],a[8:11]
buffer_load_dwordx4 v228, s[8:11], s64 offen offset:0 lds
MM a[16:19],a[128:131],v[80:83],a[16:19]
RD a[200:203],v226 offset:49152
CV v4,v32,v33
MM a[16:19],a[132:135],v[84:87],a[16:19]
buffer_load_dwordx4 v229, s[8:11], s64 offen offset:1024 lds
)ASM"
R"ASM(
MM a[24:27],a[132:135],v[92:95],a[24:27]
RD a[204:207],v227 offset:49152
RD a[208:211],v224 offset:53248
MM a[24:27],a[128:131],v[88:91],a[24:27]
EX v35,v35
MM a[28:31],a[136:139],v[88:91],a[28:31]
RD a[212:215],v225 offset:53248
CV v5,v34,v35
MM a[28:31],a[140:143],v[92:95],a[28:31]
EX v36,v36
MM a[20:23],a[140:143],v[84:87],a[20:23]
buffer_load_dwordx4 v230, s[8:11], s64 offen offset:2048 lds
MM a[20:23],a[136:139],v[80:83],a[20:23]
EX v37,v37
MM a[12:15],a[136:139],v[72:75],a[12:15]
RD a[216:219],v226 offset:53248
CV v6,v36,v37
MM a[12:15],a[140:143],v[76:79],a[12:15]
EX v38,v38
MM a[4:7],a[140:143],v[68:71],a[4:7]
EX v39,v39
MM a[4:7],a[136:139],v[64:67],a[4:7]
RD a[220:223],v227 offset:53248
CV v7,v38,v39
MM a[32:35],a[144:147],v[64:67],a[32:35]
EX v40,v40
MM a[32:35],a[148:151],v[68:71],a[32:35]
buffer_load_dwordx4 v231, s[8:11], s64 offen offset:3072 lds
MM a[40:43],a[148:151],v[76:79],a[40:43]
s_add_u32 m0, s65, 81920
EX v41,v41
MM a[40:43],a[144:147],v[72:75],a[40:43]
RD a[224:227],v224 offset:57344
CV v12,v40,v41
MM a[48:51],a[144:147],v[80:83],a[48:51]
EX v42,v42
MM a[48:51],a[148:151],v[84:87],a[48:51]
EX v43,v43
MM a[56:59],a[148:151],v[92:95],a[56:59]
RD a[228:231],v225 offset:57344
CV v13,v42,v43
MM a[56:59],a[144:147],v[88:91],a[56:59]
EX v44,v44
MM a[60:63],a[152:155],v[88:91],a[60:63]
buffer_load_dwordx4 v235, s[12:15], s81 offen offset:0 lds
MM a[60:63],a[156:159],v[92:95],a[60:63]
EX v45,v45
MM a[52:55],a[156:159],v[84:87],a[52:55]
RD a[232:235],v226 offset:57344
CV v14,v44,v45
MM a[52:55],a[152:155],v[80:83],a[52:55]
EX v46,v46
MM a[44:47],a[152:155],v[72:75],a[44:47]
EX v47,v47
MM a[44:47],a[156:159],v[76:79],a[44:47]
RD a[236:239],v227 offset:57344
CV v15,v46,v47
MM a[36:39],a[156:159],v[68:71],a[36:39]
buffer_load_dwordx4 v235, s[12:15], s64 offen offset:1024 lds
MM a[36:39],a[152:155],v[64:67],a[36:39]
EX v48,v48
MM a[64:67],a[160:163],v[64:67],a[64:67]
EX v49,v49
MM a[64:67],a[164:167],v[68:71],a[64:67]
RD a[240:243],v224 offset:61440
LPG14_%=:
s_cbranch_execz LTR2_%=
CV v20,v48,v49
MM a[72:75],a[164:167],v[76:79],a[72:75]
EX v50,v50
MM a[72:75],a[160:163],v[72:75],a[72:75]
RD a[244:247],v225 offset:61440
RD a[248:251],v226 offset:61440
MM a[80:83],a[160:163],v[80:83],a[80:83]
EX v51,v51
MM a[80:83],a[164:167],v[84:87],a[80:83]
RD a[252:255],v227 offset:61440
CV v21,v50,v51
MM a[88:91],a[164:167],v[92:95],a[88:91]
EX v52,v52
MM a[88:91],a[160:163],v[88:91],a[88:91]
buffer_load_dwordx4 v235, s[12:15], s81 offen offset:2048 lds
MM a[92:95],a[168:171],v[88:91],a[92:95]
EX v53,v53
MM a[92:95],a[172:175],v[92:95],a[92:95]
EX v54,v54
MM a[84:87],a[172:175],v[84:87],a[84:87]
buffer_load_dwordx4 v235, s[12:15], s64 offen offset:3072 lds
MM a[84:87],a[168:171],v[80:83],a[84:87]
EX v55,v55
MM a[76:79],a[168:171],v[72:75],a[76:79]
EX v56,v56
MM a[76:79],a[172:175],v[76:79],a[76:79]
EX v57,v57
MM a[68:71],a[172:175],v[68:71],a[68:71]
EX v58,v58
MM a[68:71],a[168:171],v[64:67],a[68:71]
EX v59,v59
MM a[96:99],a[176:179],v[64:67],a[96:99]
EX v60,v60
MM a[96:99],a[180:183],v[68:71],a[96:99]
EX v61,v61
MM a[104:107],a[180:183],v[76:79],a[104:107]
EX v62,v62
MM a[104:107],a[176:179],v[72:75],a[104:107]
EX v63,v63
MM a[112:115],a[176:179],v[80:83],a[112:115]
CV v22,v52,v53
CV v23,v54,v55
MM a[112:115],a[180:183],v[84:87],a[112:115]
CV v28,v56,v57
CV v29,v58,v59
MM a[120:123],a[180:183],v[92:95],a[120:123]
CV v30,v60,v61
CV v31,v62,v63
MM a[120:123],a[176:179],v[88:91],a[120:123]
MM a[124:127],a[184:187],v[88:91],a[124:127]
MM a[124:127],a[188:191],v[92:95],a[124:127]
MM a[116:119],a[188:191],v[84:87],a[116:119]
MM a[116:119],a[184:187],v[80:83],a[116:119]
MM a[108:111],a[184:187],v[72:75],a[108:111]
MM a[108:111],a[188:191],v[76:79],a[108:111]
MM a[100:103],a[188:191],v[68:71],a[100:103]
MM a[100:103],a[184:187],v[64:67],a[100:103]
v_smfmac_f32_16x16x64_bf16 v[236:239], v[144:147], v[0:7], v234
v_smfmac_f32_16x16x64_bf16 v[236:239], v[148:151], v[8:15], v234
v_smfmac_f32_16x16x64_bf16 v[236:239], v[152:155], v[16:23], v234
v_smfmac_f32_16x16x64_bf16 v[236:239], v[156:159], v[24:31], v234
s_waitcnt lgkmcnt(0)
MM v[64:67],a[192:195],v[160:163],v[128:131]
s_add_u32 s64, s64, 16384
TR a[128:129],v232 offset:32768
s_add_u32 s81, s64, 896
MM v[64:67],a[196:199],v[176:179],v[64:67]
TR a[130:131],v232 offset:36864
TR a[132:133],v232 offset:40960
MM v[64:67],a[200:203],v[192:195],v[64:67]
TR a[134:135],v232 offset:45056
TR a[136:137],v233 offset:32768
MM v[64:67],a[204:207],v[208:211],v[64:67]
TR a[138:139],v233 offset:36864
v_add_f32 v246, v236, v237
MM v[68:71],a[220:223],v[208:211],v[128:131]
TR a[140:141],v233 offset:40960
v_cmp_nge_f32 vcc, s71, v246
MM v[68:71],a[216:219],v[192:195],v[68:71]
s_cbranch_vccnz LSITE22_%=
LBACK22_%=:
TR a[142:143],v233 offset:45056
TR a[144:145],v232 offset:33280
MM v[68:71],a[212:215],v[176:179],v[68:71]
s_add_u32 s67, s95, 16384
buffer_store_dwordx4 v[240:243], v248, s[20:23], s67 offen offset:0 sc1
MM v[68:71],a[208:211],v[160:163],v[68:71]
TR a[146:147],v232 offset:37376
TR a[148:149],v232 offset:41472
MM v[76:79],a[208:211],v[164:167],v[132:135]
EX v64,v64
MM v[76:79],a[212:215],v[180:183],v[76:79]
EX v65,v65
MM v[76:79],a[216:219],v[196:199],v[76:79]
TR a[150:151],v232 offset:45568
CV v64,v64,v65
MM v[76:79],a[220:223],v[212:215],v[76:79]
EX v66,v66
MM v[72:75],a[204:207],v[212:215],v[132:135]
EX v67,v67
MM v[72:75],a[200:203],v[196:199],v[72:75]
TR a[152:153],v233 offset:33280
CV v65,v66,v67
MM v[72:75],a[196:199],v[180:183],v[72:75]
EX v68,v68
MM v[72:75],a[192:195],v[164:167],v[72:75]
EX v69,v69
MM v[80:83],a[192:195],v[168:171],v[136:139]
TR a[154:155],v233 offset:37376
CV v66,v68,v69
MM v[80:83],a[196:199],v[184:187],v[80:83]
EX v70,v70
MM v[80:83],a[200:203],v[200:203],v[80:83]
EX v71,v71
MM v[80:83],a[204:207],v[216:219],v[80:83]
TR a[156:157],v233 offset:41472
CV v67,v70,v71
MM v[84:87],a[220:223],v[216:219],v[136:139]
EX v72,v72
MM v[84:87],a[216:219],v[200:203],v[84:87]
TR a[158:159],v233 offset:45568
TR a[160:161],v232 offset:33792
MM v[84:87],a[212:215],v[184:187],v[84:87]
EX v73,v73
MM v[84:87],a[208:211],v[168:171],v[84:87]
TR a[162:163],v232 offset:37888
CV v72,v72,v73
MM v[92:95],a[208:211],v[172:175],v[140:143]
EX v74,v74
MM v[92:95],a[212:215],v[188:191],v[92:95]
TR a[164:165],v232 offset:41984
TR a[166:167],v232 offset:46080
MM v[92:95],a[216:219],v[204:207],v[92:95]
EX v75,v75
MM v[92:95],a[220:223],v[220:223],v[92:95]
TR a[168:169],v233 offset:33792
CV v73,v74,v75
MM v[88:91],a[204:207],v[220:223],v[140:143]
EX v76,v76
MM v[88:91],a[200:203],v[204:207],v[88:91]
EX v77,v77
MM v[88:91],a[196:199],v[188:191],v[88:91]
TR a[170:171],v233 offset:37888
CV v74,v76,v77
MM v[88:91],a[192:195],v[172:175],v[88:91]
EX v78,v78
MM v[96:99],a[224:227],v[160:163],v[128:131]
TR a[172:173],v233 offset:41984
TR a[174:175],v233 offset:46080
MM v[96:99],a[228:231],v[176:179],v[96:99]
EX v79,v79
MM v[96:99],a[232:235],v[192:195],v[96:99]
TR a[176:177],v232 offset:34304
CV v75,v78,v79
MM v[96:99],a[236:239],v[208:211],v[96:99]
EX v80,v80
MM v[100:103],a[252:255],v[208:211],v[128:131]
EX v81,v81
MM v[100:103],a[248:251],v[192:195],v[100:103]
TR a[178:179],v232 offset:38400
CV v80,v80,v81
MM v[100:103],a[244:247],v[176:179],v[100:103]
EX v82,v82
MM v[100:103],a[240:243],v[160:163],v[100:103]
TR a[180:181],v232 offset:42496
TR a[182:183],v232 offset:46592
MM v[108:111],a[240:243],v[164:167],v[132:135]
s_add_u32 s67, s95, 17408
buffer_store_dwordx4 v[240:243], v248, s[20:23], s67 offen offset:0 sc1
MM v[108:111],a[244:247],v[180:183],v[108:111]
EX v83,v83
MM v[108:111],a[248:251],v[196:199],v[108:111]
TR a[184:185],v233 offset:34304
CV v81,v82,v83
MM v[108:111],a[252:255],v[212:215],v[108:111]
EX v84,v84
MM v[104:107],a[236:239],v[212:215],v[132:135]
EX v85,v85
MM v[104:107],a[232:235],v[196:199],v[104:107]
TR a[186:187],v233 offset:38400
CV v82,v84,v85
MM v[104:107],a[228:231],v[180:183],v[104:107]
EX v86,v86
MM v[104:107],a[224:227],v[164:167],v[104:107]
TR a[188:189],v233 offset:42496
TR a[190:191],v233 offset:46592
MM v[112:115],a[224:227],v[168:171],v[136:139]
EX v87,v87
MM v[112:115],a[228:231],v[184:187],v[112:115]
CV v83,v86,v87
MM v[112:115],a[232:235],v[200:203],v[112:115]
EX v88,v88
MM v[112:115],a[236:239],v[216:219],v[112:115]
EX v89,v89
MM v[116:119],a[252:255],v[216:219],v[136:139]
EX v90,v90
MM v[116:119],a[248:251],v[200:203],v[116:119]
EX v91,v91
MM v[116:119],a[244:247],v[184:187],v[116:119]
EX v92,v92
MM v[116:119],a[240:243],v[168:171],v[116:119]
EX v93,v93
MM v[124:127],a[240:243],v[172:175],v[140:143]
EX v94,v94
MM v[124:127],a[244:247],v[188:191],v[124:127]
EX v95,v95
MM v[124:127],a[248:251],v[204:207],v[124:127]
EX v96,v96
MM v[124:127],a[252:255],v[220:223],v[124:127]
EX v97,v97
MM v[120:123],a[236:239],v[220:223],v[140:143]
EX v98,v98
MM v[120:123],a[232:235],v[204:207],v[120:123]
EX v99,v99
MM v[120:123],a[228:231],v[188:191],v[120:123]
s_getpc_b64 s[98:99]
LTA3_%=:
s_lshl_b32 s73, %[swave], 12
s_add_u32 s73, s73, LPG18_%=-LTA3_%=
s_add_u32 s98, s98, s73
s_addc_u32 s99, s99, 0
s_mov_b64 exec, 0
s_setpc_b64 s[98:99]
LTR3_%=:
s_mov_b64 exec, -1
s_nop 0
CV v88,v88,v89
MM v[120:123],a[224:227],v[172:175],v[120:123]
s_waitcnt vmcnt(17) lgkmcnt(0)
s_barrier
MM a[0:3],a[128:131],v[0:3],a[0:3]
s_lshl_b32 s91, %[swave], 2
CV v89,v90,v91
CV v90,v92,v93
MM a[0:3],a[132:135],v[4:7],a[0:3]
s_add_u32 s91, s91, s48
RD a[192:195],v224 offset:0
CV v91,v94,v95
MM a[8:11],a[132:135],v[12:15],a[8:11]
s_add_u32 s91, s91, 69632
s_add_u32 m0, s65, 32768
RD a[196:199],v225 offset:0
MM a[8:11],a[128:131],v[8:11],a[8:11]
v_mov_b32 v250, s69
s_mov_b64 exec, 1
buffer_store_dword v250, off, s[20:23], s91 offset:0 sc1
s_mov_b64 exec, -1
MM a[16:19],a[128:131],v[16:19],a[16:19]
buffer_load_dwordx4 v228, s[8:11], s64 offen offset:0 lds
MM a[16:19],a[132:135],v[20:23],a[16:19]
RD a[200:203],v226 offset:0
CV v68,v96,v97
MM a[24:27],a[132:135],v[28:31],a[24:27]
RD a[204:207],v227 offset:0
CV v69,v98,v99
MM a[24:27],a[128:131],v[24:27],a[24:27]
buffer_load_dwordx4 v229, s[8:11], s64 offen offset:1024 lds
MM a[28:31],a[136:139],v[24:27],a[28:31]
RD a[208:211],v224 offset:4096
RD a[212:215],v225 offset:4096
MM a[28:31],a[140:143],v[28:31],a[28:31]
EX v100,v100
MM a[20:23],a[140:143],v[20:23],a[20:23]
buffer_load_dwordx4 v230, s[8:11], s64 offen offset:2048 lds
MM a[20:23],a[136:139],v[16:19],a[20:23]
EX v101,v101
MM a[12:15],a[136:139],v[8:11],a[12:15]
RD a[216:219],v226 offset:4096
CV v70,v100,v101
MM a[12:15],a[140:143],v[12:15],a[12:15]
EX v102,v102
MM a[4:7],a[140:143],v[4:7],a[4:7]
EX v103,v103
MM a[4:7],a[136:139],v[0:3],a[4:7]
RD a[220:223],v227 offset:4096
CV v71,v102,v103
MM a[32:35],a[144:147],v[0:3],a[32:35]
EX v104,v104
MM a[32:35],a[148:151],v[4:7],a[32:35]
buffer_load_dwordx4 v231, s[8:11], s64 offen offset:3072 lds
MM a[40:43],a[148:151],v[12:15],a[40:43]
s_add_u32 m0, s65, 98304
EX v105,v105
MM a[40:43],a[144:147],v[8:11],a[40:43]
RD a[224:227],v224 offset:8192
CV v76,v104,v105
MM a[48:51],a[144:147],v[16:19],a[48:51]
EX v106,v106
MM a[48:51],a[148:151],v[20:23],a[48:51]
EX v107,v107
MM a[56:59],a[148:151],v[28:31],a[56:59]
RD a[228:231],v225 offset:8192
CV v77,v106,v107
MM a[56:59],a[144:147],v[24:27],a[56:59]
EX v108,v108
MM a[60:63],a[152:155],v[24:27],a[60:63]
buffer_load_dwordx4 v235, s[12:15], s81 offen offset:0 lds
MM a[60:63],a[156:159],v[28:31],a[60:63]
EX v109,v109
MM a[52:55],a[156:159],v[20:23],a[52:55]
RD a[232:235],v226 offset:8192
CV v78,v108,v109
MM a[52:55],a[152:155],v[16:19],a[52:55]
EX v110,v110
MM a[44:47],a[152:155],v[8:11],a[44:47]
EX v111,v111
MM a[44:47],a[156:159],v[12:15],a[44:47]
RD a[236:239],v227 offset:8192
CV v79,v110,v111
MM a[36:39],a[156:159],v[4:7],a[36:39]
buffer_load_dwordx4 v235, s[12:15], s64 offen offset:1024 lds
MM a[36:39],a[152:155],v[0:3],a[36:39]
EX v112,v112
MM a[64:67],a[160:163],v[0:3],a[64:67]
EX v113,v113
MM a[64:67],a[164:167],v[4:7],a[64:67]
RD a[240:243],v224 offset:12288
CV v84,v112,v113
MM a[72:75],a[164:167],v[12:15],a[72:75]
EX v114,v114
MM a[72:75],a[160:163],v[8:11],a[72:75]
)ASM"
R"ASM(
RD a[244:247],v225 offset:12288
RD a[248:251],v226 offset:12288
MM a[80:83],a[160:163],v[16:19],a[80:83]
EX v115,v115
MM a[80:83],a[164:167],v[20:23],a[80:83]
RD a[252:255],v227 offset:12288
CV v85,v114,v115
MM a[88:91],a[164:167],v[28:31],a[88:91]
EX v116,v116
MM a[88:91],a[160:163],v[24:27],a[88:91]
buffer_load_dwordx4 v235, s[12:15], s81 offen offset:2048 lds
MM a[92:95],a[168:171],v[24:27],a[92:95]
EX v117,v117
MM a[92:95],a[172:175],v[28:31],a[92:95]
EX v118,v118
MM a[84:87],a[172:175],v[20:23],a[84:87]
buffer_load_dwordx4 v235, s[12:15], s64 offen offset:3072 lds
MM a[84:87],a[168:171],v[16:19],a[84:87]
EX v119,v119
MM a[76:79],a[168:171],v[8:11],a[76:79]
EX v120,v120
MM a[76:79],a[172:175],v[12:15],a[76:79]
EX v121,v121
MM a[68:71],a[172:175],v[4:7],a[68:71]
EX v122,v122
MM a[68:71],a[168:171],v[0:3],a[68:71]
EX v123,v123
MM a[96:99],a[176:179],v[0:3],a[96:99]
EX v124,v124
MM a[96:99],a[180:183],v[4:7],a[96:99]
EX v125,v125
MM a[104:107],a[180:183],v[12:15],a[104:107]
EX v126,v126
MM a[104:107],a[176:179],v[8:11],a[104:107]
EX v127,v127
MM a[112:115],a[176:179],v[16:19],a[112:115]
CV v86,v116,v117
CV v87,v118,v119
MM a[112:115],a[180:183],v[20:23],a[112:115]
CV v92,v120,v121
CV v93,v122,v123
MM a[120:123],a[180:183],v[28:31],a[120:123]
CV v94,v124,v125
CV v95,v126,v127
MM a[120:123],a[176:179],v[24:27],a[120:123]
MM a[124:127],a[184:187],v[24:27],a[124:127]
MM a[124:127],a[188:191],v[28:31],a[124:127]
MM a[116:119],a[188:191],v[20:23],a[116:119]
MM a[116:119],a[184:187],v[16:19],a[116:119]
MM a[108:111],a[184:187],v[8:11],a[108:111]
MM a[108:111],a[188:191],v[12:15],a[108:111]
MM a[100:103],a[188:191],v[4:7],a[100:103]
MM a[100:103],a[184:187],v[0:3],a[100:103]
v_smfmac_f32_16x16x64_bf16 v[236:239], v[144:147], v[64:71], v234
v_smfmac_f32_16x16x64_bf16 v[236:239], v[148:151], v[72:79], v234
v_smfmac_f32_16x16x64_bf16 v[236:239], v[152:155], v[80:87], v234
v_smfmac_f32_16x16x64_bf16 v[236:239], v[156:159], v[88:95], v234
s_waitcnt lgkmcnt(0)
MM v[0:3],a[192:195],v[160:163],v[128:131]
s_add_u32 s64, s64, 16384
TR a[128:129],v232 offset:49152
s_add_u32 s81, s64, 896
MM v[0:3],a[196:199],v[176:179],v[0:3]
TR a[130:131],v232 offset:53248
TR a[132:133],v232 offset:57344
MM v[0:3],a[200:203],v[192:195],v[0:3]
TR a[134:135],v232 offset:61440
TR a[136:137],v233 offset:49152
MM v[0:3],a[204:207],v[208:211],v[0:3]
TR a[138:139],v233 offset:53248
v_add_f32 v247, v236, v237
MM v[4:7],a[220:223],v[208:211],v[128:131]
TR a[140:141],v233 offset:57344
v_cmp_nge_f32 vcc, s71, v247
MM v[4:7],a[216:219],v[192:195],v[4:7]
s_cbranch_vccnz LSITE23_%=
LBACK23_%=:
TR a[142:143],v233 offset:61440
TR a[144:145],v232 offset:49664
MM v[4:7],a[212:215],v[176:179],v[4:7]
s_add_u32 s67, s95, 18432
buffer_store_dwordx4 v[240:243], v248, s[20:23], s67 offen offset:0 sc1
MM v[4:7],a[208:211],v[160:163],v[4:7]
TR a[146:147],v232 offset:53760
TR a[148:149],v232 offset:57856
MM v[12:15],a[208:211],v[164:167],v[132:135]
EX v0,v0
MM v[12:15],a[212:215],v[180:183],v[12:15]
EX v1,v1
MM v[12:15],a[216:219],v[196:199],v[12:15]
TR a[150:151],v232 offset:61952
CV v0,v0,v1
MM v[12:15],a[220:223],v[212:215],v[12:15]
EX v2,v2
MM v[8:11],a[204:207],v[212:215],v[132:135]
EX v3,v3
MM v[8:11],a[200:203],v[196:199],v[8:11]
TR a[152:153],v233 offset:49664
CV v1,v2,v3
MM v[8:11],a[196:199],v[180:183],v[8:11]
EX v4,v4
MM v[8:11],a[192:195],v[164:167],v[8:11]
EX v5,v5
MM v[16:19],a[192:195],v[168:171],v[136:139]
TR a[154:155],v233 offset:53760
CV v2,v4,v5
MM v[16:19],a[196:199],v[184:187],v[16:19]
EX v6,v6
MM v[16:19],a[200:203],v[200:203],v[16:19]
EX v7,v7
MM v[16:19],a[204:207],v[216:219],v[16:19]
TR a[156:157],v233 offset:57856
CV v3,v6,v7
MM v[20:23],a[220:223],v[216:219],v[136:139]
EX v8,v8
MM v[20:23],a[216:219],v[200:203],v[20:23]
TR a[158:159],v233 offset:61952
TR a[160:161],v232 offset:50176
MM v[20:23],a[212:215],v[184:187],v[20:23]
EX v9,v9
MM v[20:23],a[208:211],v[168:171],v[20:23]
TR a[162:163],v232 offset:54272
CV v8,v8,v9
MM v[28:31],a[208:211],v[172:175],v[140:143]
s_add_u32 m0, s45, 147456
s_add_u32 s44, s44, 1024
buffer_load_dwordx4 v248, s[24:27], s44 offen offset:0 lds
MM v[28:31],a[212:215],v[188:191],v[28:31]
EX v10,v10
MM v[28:31],a[216:219],v[204:207],v[28:31]
EX v11,v11
MM v[28:31],a[220:223],v[220:223],v[28:31]
TR a[164:165],v232 offset:58368
CV v9,v10,v11
MM v[24:27],a[204:207],v[220:223],v[140:143]
EX v12,v12
MM v[24:27],a[200:203],v[204:207],v[24:27]
TR a[166:167],v232 offset:62464
TR a[168:169],v233 offset:50176
MM v[24:27],a[196:199],v[188:191],v[24:27]
EX v13,v13
MM v[24:27],a[192:195],v[172:175],v[24:27]
TR a[170:171],v233 offset:54272
CV v10,v12,v13
MM v[32:35],a[224:227],v[160:163],v[128:131]
EX v14,v14
MM v[32:35],a[228:231],v[176:179],v[32:35]
EX v15,v15
MM v[32:35],a[232:235],v[192:195],v[32:35]
TR a[172:173],v233 offset:58368
CV v11,v14,v15
MM v[32:35],a[236:239],v[208:211],v[32:35]
TR a[174:175],v233 offset:62464
TR a[176:177],v232 offset:50688
MM v[36:39],a[252:255],v[208:211],v[128:131]
EX v16,v16
MM v[36:39],a[248:251],v[192:195],v[36:39]
EX v17,v17
MM v[36:39],a[244:247],v[176:179],v[36:39]
TR a[178:179],v232 offset:54784
CV v16,v16,v17
MM v[36:39],a[240:243],v[160:163],v[36:39]
EX v18,v18
MM v[44:47],a[240:243],v[164:167],v[132:135]
s_add_u32 s67, s95, 19456
buffer_store_dwordx4 v[240:243], v248, s[20:23], s67 offen offset:0 sc1
MM v[44:47],a[244:247],v[180:183],v[44:47]
EX v19,v19
MM v[44:47],a[248:251],v[196:199],v[44:47]
TR a[180:181],v232 offset:58880
CV v17,v18,v19
MM v[44:47],a[252:255],v[212:215],v[44:47]
EX v20,v20
MM v[40:43],a[236:239],v[212:215],v[132:135]
TR a[182:183],v232 offset:62976
TR a[184:185],v233 offset:50688
MM v[40:43],a[232:235],v[196:199],v[40:43]
EX v21,v21
MM v[40:43],a[228:231],v[180:183],v[40:43]
TR a[186:187],v233 offset:54784
CV v18,v20,v21
MM v[40:43],a[224:227],v[164:167],v[40:43]
EX v22,v22
MM v[48:51],a[224:227],v[168:171],v[136:139]
TR a[188:189],v233 offset:58880
TR a[190:191],v233 offset:62976
MM v[48:51],a[228:231],v[184:187],v[48:51]
EX v23,v23
MM v[48:51],a[232:235],v[200:203],v[48:51]
CV v19,v22,v23
MM v[48:51],a[236:239],v[216:219],v[48:51]
EX v24,v24
MM v[52:55],a[252:255],v[216:219],v[136:139]
EX v25,v25
MM v[52:55],a[248:251],v[200:203],v[52:55]
EX v26,v26
MM v[52:55],a[244:247],v[184:187],v[52:55]
EX v27,v27
MM v[52:55],a[240:243],v[168:171],v[52:55]
EX v28,v28
MM v[60:63],a[240:243],v[172:175],v[140:143]
EX v29,v29
MM v[60:63],a[244:247],v[188:191],v[60:63]
EX v30,v30
MM v[60:63],a[248:251],v[204:207],v[60:63]
EX v31,v31
MM v[60:63],a[252:255],v[220:223],v[60:63]
EX v32,v32
MM v[56:59],a[236:239],v[220:223],v[140:143]
EX v33,v33
MM v[56:59],a[232:235],v[204:207],v[56:59]
EX v34,v34
MM v[56:59],a[228:231],v[188:191],v[56:59]
EX v35,v35
MM v[56:59],a[224:227],v[172:175],v[56:59]
s_waitcnt vmcnt(18) lgkmcnt(0)
s_barrier
MM a[0:3],a[128:131],v[64:67],a[0:3]
s_add_u32 m0, s65, 49152
CV v24,v24,v25
CV v25,v26,v27
MM a[0:3],a[132:135],v[68:71],a[0:3]
RD a[192:195],v224 offset:16384
CV v26,v28,v29
MM a[8:11],a[132:135],v[76:79],a[8:11]
RD a[196:199],v225 offset:16384
CV v27,v30,v31
MM a[8:11],a[128:131],v[72:75],a[8:11]
buffer_load_dwordx4 v228, s[8:11], s64 offen offset:0 lds
MM a[16:19],a[128:131],v[80:83],a[16:19]
RD a[200:203],v226 offset:16384
CV v4,v32,v33
MM a[16:19],a[132:135],v[84:87],a[16:19]
RD a[204:207],v227 offset:16384
CV v5,v34,v35
MM a[24:27],a[132:135],v[92:95],a[24:27]
buffer_load_dwordx4 v229, s[8:11], s64 offen offset:1024 lds
MM a[24:27],a[128:131],v[88:91],a[24:27]
RD a[208:211],v224 offset:20480
RD a[212:215],v225 offset:20480
MM a[28:31],a[136:139],v[88:91],a[28:31]
buffer_load_dwordx4 v230, s[8:11], s64 offen offset:2048 lds
MM a[28:31],a[140:143],v[92:95],a[28:31]
EX v36,v36
MM a[20:23],a[140:143],v[84:87],a[20:23]
EX v37,v37
MM a[20:23],a[136:139],v[80:83],a[20:23]
RD a[216:219],v226 offset:20480
CV v6,v36,v37
MM a[12:15],a[136:139],v[72:75],a[12:15]
EX v38,v38
MM a[12:15],a[140:143],v[76:79],a[12:15]
RD a[220:223],v227 offset:20480
RD a[224:227],v224 offset:24576
MM a[4:7],a[140:143],v[68:71],a[4:7]
EX v39,v39
MM a[4:7],a[136:139],v[64:67],a[4:7]
RD a[228:231],v225 offset:24576
CV v7,v38,v39
MM a[32:35],a[144:147],v[64:67],a[32:35]
EX v40,v40
MM a[32:35],a[148:151],v[68:71],a[32:35]
buffer_load_dwordx4 v231, s[8:11], s64 offen offset:3072 lds
MM a[40:43],a[148:151],v[76:79],a[40:43]
s_add_u32 m0, s65, 114688
EX v41,v41
MM a[40:43],a[144:147],v[72:75],a[40:43]
LPG15_%=:
s_cbranch_execz LTR2_%=
RD a[232:235],v226 offset:24576
CV v12,v40,v41
MM a[48:51],a[144:147],v[80:83],a[48:51]
EX v42,v42
MM a[48:51],a[148:151],v[84:87],a[48:51]
EX v43,v43
MM a[56:59],a[148:151],v[92:95],a[56:59]
RD a[236:239],v227 offset:24576
CV v13,v42,v43
MM a[56:59],a[144:147],v[88:91],a[56:59]
EX v44,v44
MM a[60:63],a[152:155],v[88:91],a[60:63]
buffer_load_dwordx4 v235, s[12:15], s81 offen offset:0 lds
MM a[60:63],a[156:159],v[92:95],a[60:63]
EX v45,v45
MM a[52:55],a[156:159],v[84:87],a[52:55]
RD a[240:243],v224 offset:28672
CV v14,v44,v45
MM a[52:55],a[152:155],v[80:83],a[52:55]
EX v46,v46
MM a[44:47],a[152:155],v[72:75],a[44:47]
EX v47,v47
MM a[44:47],a[156:159],v[76:79],a[44:47]
RD a[244:247],v225 offset:28672
CV v15,v46,v47
MM a[36:39],a[156:159],v[68:71],a[36:39]
buffer_load_dwordx4 v235, s[12:15], s64 offen offset:1024 lds
MM a[36:39],a[152:155],v[64:67],a[36:39]
EX v48,v48
MM a[64:67],a[160:163],v[64:67],a[64:67]
EX v49,v49
MM a[64:67],a[164:167],v[68:71],a[64:67]
RD a[248:251],v226 offset:28672
CV v20,v48,v49
MM a[72:75],a[164:167],v[76:79],a[72:75]
EX v50,v50
MM a[72:75],a[160:163],v[72:75],a[72:75]
EX v51,v51
MM a[80:83],a[160:163],v[80:83],a[80:83]
RD a[252:255],v227 offset:28672
CV v21,v50,v51
MM a[80:83],a[164:167],v[84:87],a[80:83]
buffer_load_dwordx4 v235, s[12:15], s81 offen offset:2048 lds
MM a[88:91],a[164:167],v[92:95],a[88:91]
EX v52,v52
MM a[88:91],a[160:163],v[88:91],a[88:91]
EX v53,v53
MM a[92:95],a[168:171],v[88:91],a[92:95]
EX v54,v54
MM a[92:95],a[172:175],v[92:95],a[92:95]
buffer_load_dwordx4 v235, s[12:15], s64 offen offset:3072 lds
MM a[84:87],a[172:175],v[84:87],a[84:87]
EX v55,v55
MM a[84:87],a[168:171],v[80:83],a[84:87]
EX v56,v56
MM a[76:79],a[168:171],v[72:75],a[76:79]
EX v57,v57
MM a[76:79],a[172:175],v[76:79],a[76:79]
EX v58,v58
MM a[68:71],a[172:175],v[68:71],a[68:71]
EX v59,v59
MM a[68:71],a[168:171],v[64:67],a[68:71]
EX v60,v60
MM a[96:99],a[176:179],v[64:67],a[96:99]
EX v61,v61
MM a[96:99],a[180:183],v[68:71],a[96:99]
EX v62,v62
MM a[104:107],a[180:183],v[76:79],a[104:107]
EX v63,v63
MM a[104:107],a[176:179],v[72:75],a[104:107]
CV v22,v52,v53
CV v23,v54,v55
MM a[112:115],a[176:179],v[80:83],a[112:115]
CV v28,v56,v57
CV v29,v58,v59
MM a[112:115],a[180:183],v[84:87],a[112:115]
CV v30,v60,v61
CV v31,v62,v63
MM a[120:123],a[180:183],v[92:95],a[120:123]
MM a[120:123],a[176:179],v[88:91],a[120:123]
MM a[124:127],a[184:187],v[88:91],a[124:127]
MM a[124:127],a[188:191],v[92:95],a[124:127]
MM a[116:119],a[188:191],v[84:87],a[116:119]
MM a[116:119],a[184:187],v[80:83],a[116:119]
MM a[108:111],a[184:187],v[72:75],a[108:111]
MM a[108:111],a[188:191],v[76:79],a[108:111]
MM a[100:103],a[188:191],v[68:71],a[100:103]
MM a[100:103],a[184:187],v[64:67],a[100:103]
v_smfmac_f32_16x16x64_bf16 v[236:239], v[144:147], v[0:7], v234
v_smfmac_f32_16x16x64_bf16 v[236:239], v[148:151], v[8:15], v234
v_smfmac_f32_16x16x64_bf16 v[236:239], v[152:155], v[16:23], v234
v_smfmac_f32_16x16x64_bf16 v[236:239], v[156:159], v[24:31], v234
s_waitcnt lgkmcnt(0)
MM v[64:67],a[192:195],v[160:163],v[128:131]
s_add_u32 s64, s64, 16384
TR a[128:129],v232 offset:0
s_add_u32 s81, s64, 896
MM v[64:67],a[196:199],v[176:179],v[64:67]
TR a[130:131],v232 offset:4096
TR a[132:133],v232 offset:8192
MM v[64:67],a[200:203],v[192:195],v[64:67]
TR a[134:135],v232 offset:12288
TR a[136:137],v233 offset:0
MM v[64:67],a[204:207],v[208:211],v[64:67]
TR a[138:139],v233 offset:4096
v_add_f32 v246, v236, v237
MM v[68:71],a[220:223],v[208:211],v[128:131]
TR a[140:141],v233 offset:8192
v_cmp_nge_f32 vcc, s71, v246
MM v[68:71],a[216:219],v[192:195],v[68:71]
s_cbranch_vccnz LSITE24_%=
)ASM"
R"ASM(
LBACK24_%=:
TR a[142:143],v233 offset:12288
TR a[144:145],v232 offset:512
MM v[68:71],a[212:215],v[176:179],v[68:71]
v_bfe_u32 v242, v248, 6, 3
v_mul_u32_u24 v242, 1280, v242
MM v[68:71],a[208:211],v[160:163],v[68:71]
v_and_b32 v240, 48, v248
v_bfe_u32 v243, v248, 8, 1
MM v[76:79],a[208:211],v[164:167],v[132:135]
v_lshlrev_b32 v243, 5, v243
v_xor_b32 v240, v240, v243
MM v[76:79],a[212:215],v[180:183],v[76:79]
v_add_u32 v240, v240, v242
v_lshrrev_b32 v243, 9, v248
MM v[76:79],a[216:219],v[196:199],v[76:79]
v_mul_u32_u24 v242, 10240, v243
v_add_u32 v241, v240, v242
MM v[76:79],a[220:223],v[212:215],v[76:79]
TR a[146:147],v232 offset:4608
v_lshl_add_u32 v240, v243, 6, v240
MM v[72:75],a[204:207],v[212:215],v[132:135]
s_and_b32 s66, s84, 1
EX v64,v64
MM v[72:75],a[200:203],v[196:199],v[72:75]
s_lshl_b32 s94, s66, 6
TR a[148:149],v232 offset:8704
v_add_u32 v240, s94, v240
MM v[72:75],a[196:199],v[180:183],v[72:75]
s_xor_b32 s66, s66, 1
EX v65,v65
MM v[72:75],a[192:195],v[164:167],v[72:75]
s_lshl_b32 s66, s66, 8
v_add_u32 v241, s66, v241
CV v64,v64,v65
MM v[80:83],a[192:195],v[168:171],v[136:139]
s_mul_i32 s89, %[swave], 20480
EX v66,v66
MM v[80:83],a[196:199],v[184:187],v[80:83]
s_lshl_b32 s94, %[swave], 10
EX v67,v67
MM v[80:83],a[200:203],v[200:203],v[80:83]
s_add_u32 s94, s94, %[slds]
EX v68,v68
MM v[80:83],a[204:207],v[216:219],v[80:83]
s_xor_b32 s66, s84, 0
EX v69,v69
MM v[84:87],a[220:223],v[216:219],v[136:139]
s_mul_i32 s66, s66, 163840
CV v65,v66,v67
CV v66,v68,v69
MM v[84:87],a[216:219],v[200:203],v[84:87]
s_add_u32 s85, s66, s89
EX v70,v70
MM v[84:87],a[212:215],v[184:187],v[84:87]
s_xor_b32 s66, s84, 1
TR a[150:151],v232 offset:12800
TR a[152:153],v233 offset:512
MM v[84:87],a[208:211],v[168:171],v[84:87]
s_mul_i32 s66, s66, 163840
EX v71,v71
MM v[92:95],a[208:211],v[172:175],v[140:143]
s_add_u32 s86, s66, s89
s_add_u32 m0, s45, 147456
s_add_u32 s44, s44, 1024
buffer_load_dwordx4 v248, s[24:27], s44 offen offset:0 lds
MM v[92:95],a[212:215],v[188:191],v[92:95]
s_xor_b32 s66, s84, 2
EX v72,v72
MM v[92:95],a[216:219],v[204:207],v[92:95]
s_mul_i32 s66, s66, 163840
TR a[154:155],v233 offset:4608
CV v67,v70,v71
MM v[92:95],a[220:223],v[220:223],v[92:95]
s_add_u32 s87, s66, s89
EX v73,v73
MM v[88:91],a[204:207],v[220:223],v[140:143]
s_xor_b32 s66, s84, 3
TR a[156:157],v233 offset:8704
TR a[158:159],v233 offset:12800
MM v[88:91],a[200:203],v[204:207],v[88:91]
s_mul_i32 s66, s66, 163840
EX v74,v74
MM v[88:91],a[196:199],v[188:191],v[88:91]
s_add_u32 s88, s66, s89
EX v75,v75
MM v[88:91],a[192:195],v[172:175],v[88:91]
s_lshl_b32 s89, %[swave], 11
TR a[160:161],v232 offset:1024
CV v72,v72,v73
MM v[96:99],a[224:227],v[160:163],v[128:131]
s_add_u32 s89, s89, %[slds]
EX v76,v76
MM v[96:99],a[228:231],v[176:179],v[96:99]
s_lshr_b32 s96, s49, 2
TR a[162:163],v232 offset:5120
CV v73,v74,v75
MM v[96:99],a[232:235],v[192:195],v[96:99]
s_lshl_b32 s97, s50, 3
EX v77,v77
MM v[96:99],a[236:239],v[208:211],v[96:99]
s_add_u32 s96, s96, s97
TR a[164:165],v232 offset:9216
TR a[166:167],v232 offset:13312
MM v[100:103],a[252:255],v[208:211],v[128:131]
s_lshl_b32 s96, s96, 2
EX v78,v78
MM v[100:103],a[248:251],v[192:195],v[100:103]
s_add_u32 s96, s96, %[swave]
EX v79,v79
MM v[100:103],a[244:247],v[176:179],v[100:103]
s_lshl_b32 s96, s96, 6
TR a[168:169],v233 offset:1024
CV v74,v76,v77
MM v[100:103],a[240:243],v[160:163],v[100:103]
s_add_u32 s96, s96, 0
buffer_load_dword v242, off, s[20:23], s96 sc1
MM v[108:111],a[240:243],v[164:167],v[132:135]
buffer_load_dword v243, off, s[20:23], s19 sc1
MM v[108:111],a[244:247],v[180:183],v[108:111]
EX v80,v80
MM v[108:111],a[248:251],v[196:199],v[108:111]
EX v81,v81
MM v[108:111],a[252:255],v[212:215],v[108:111]
TR a[170:171],v233 offset:5120
TR a[172:173],v233 offset:9216
MM v[104:107],a[236:239],v[212:215],v[132:135]
EX v82,v82
MM v[104:107],a[232:235],v[196:199],v[104:107]
TR a[174:175],v233 offset:13312
CV v75,v78,v79
MM v[104:107],a[228:231],v[180:183],v[104:107]
EX v83,v83
MM v[104:107],a[224:227],v[164:167],v[104:107]
TR a[176:177],v232 offset:1536
CV v80,v80,v81
MM v[112:115],a[224:227],v[168:171],v[136:139]
EX v84,v84
MM v[112:115],a[228:231],v[184:187],v[112:115]
TR a[178:179],v232 offset:5632
TR a[180:181],v232 offset:9728
MM v[112:115],a[232:235],v[200:203],v[112:115]
EX v85,v85
MM v[112:115],a[236:239],v[216:219],v[112:115]
EX v86,v86
MM v[116:119],a[252:255],v[216:219],v[136:139]
TR a[182:183],v232 offset:13824
CV v81,v82,v83
MM v[116:119],a[248:251],v[200:203],v[116:119]
EX v87,v87
MM v[116:119],a[244:247],v[184:187],v[116:119]
TR a[184:185],v233 offset:1536
CV v82,v84,v85
MM v[116:119],a[240:243],v[168:171],v[116:119]
EX v88,v88
MM v[124:127],a[240:243],v[172:175],v[140:143]
TR a[186:187],v233 offset:5632
CV v83,v86,v87
MM v[124:127],a[244:247],v[188:191],v[124:127]
EX v89,v89
MM v[124:127],a[248:251],v[204:207],v[124:127]
TR a[188:189],v233 offset:9728
TR a[190:191],v233 offset:13824
MM v[124:127],a[252:255],v[220:223],v[124:127]
EX v90,v90
MM v[120:123],a[236:239],v[220:223],v[140:143]
EX v91,v91
MM v[120:123],a[232:235],v[204:207],v[120:123]
EX v92,v92
MM v[120:123],a[228:231],v[188:191],v[120:123]
EX v93,v93
MM v[120:123],a[224:227],v[172:175],v[120:123]
s_waitcnt vmcnt(18) lgkmcnt(0)
s_barrier
MM a[0:3],a[128:131],v[0:3],a[0:3]
CV v88,v88,v89
CV v89,v90,v91
MM a[0:3],a[132:135],v[4:7],a[0:3]
s_mul_i32 s76, s46, 192
EX v94,v94
MM a[8:11],a[132:135],v[12:15],a[8:11]
s_add_u32 s76, s76, 69632
v_lshrrev_b32 v253, 2, v248
v_min_u32 v253, 188, v253
MM a[8:11],a[128:131],v[8:11],a[8:11]
s_add_u32 m0, s65, 0
buffer_load_dword v252, v253, s[20:23], s76 offen offset:0 sc1
MM a[16:19],a[128:131],v[16:19],a[16:19]
EX v95,v95
MM a[16:19],a[132:135],v[20:23],a[16:19]
RD a[192:195],v224 offset:32768
CV v90,v92,v93
MM a[24:27],a[132:135],v[28:31],a[24:27]
EX v96,v96
MM a[24:27],a[128:131],v[24:27],a[24:27]
EX v97,v97
MM a[28:31],a[136:139],v[24:27],a[28:31]
RD a[196:199],v225 offset:32768
CV v91,v94,v95
MM a[28:31],a[140:143],v[28:31],a[28:31]
EX v98,v98
MM a[20:23],a[140:143],v[20:23],a[20:23]
buffer_load_dwordx4 v228, s[8:11], s64 offen offset:0 lds
MM a[20:23],a[136:139],v[16:19],a[20:23]
EX v99,v99
MM a[12:15],a[136:139],v[8:11],a[12:15]
RD a[200:203],v226 offset:32768
CV v68,v96,v97
MM a[12:15],a[140:143],v[12:15],a[12:15]
EX v100,v100
MM a[4:7],a[140:143],v[4:7],a[4:7]
EX v101,v101
MM a[4:7],a[136:139],v[0:3],a[4:7]
RD a[204:207],v227 offset:32768
CV v69,v98,v99
MM a[32:35],a[144:147],v[0:3],a[32:35]
EX v102,v102
MM a[32:35],a[148:151],v[4:7],a[32:35]
buffer_load_dwordx4 v229, s[8:11], s64 offen offset:1024 lds
MM a[40:43],a[148:151],v[12:15],a[40:43]
EX v103,v103
MM a[40:43],a[144:147],v[8:11],a[40:43]
RD a[208:211],v224 offset:36864
CV v70,v100,v101
MM a[48:51],a[144:147],v[16:19],a[48:51]
EX v104,v104
MM a[48:51],a[148:151],v[20:23],a[48:51]
RD a[212:215],v225 offset:36864
CV v71,v102,v103
MM a[56:59],a[148:151],v[28:31],a[56:59]
EX v105,v105
MM a[56:59],a[144:147],v[24:27],a[56:59]
buffer_load_dwordx4 v230, s[8:11], s64 offen offset:2048 lds
MM a[60:63],a[152:155],v[24:27],a[60:63]
EX v106,v106
MM a[60:63],a[156:159],v[28:31],a[60:63]
EX v107,v107
MM a[52:55],a[156:159],v[20:23],a[52:55]
RD a[216:219],v226 offset:36864
CV v76,v104,v105
MM a[52:55],a[152:155],v[16:19],a[52:55]
EX v108,v108
MM a[44:47],a[152:155],v[8:11],a[44:47]
RD a[220:223],v227 offset:36864
CV v77,v106,v107
MM a[44:47],a[156:159],v[12:15],a[44:47]
EX v109,v109
MM a[36:39],a[156:159],v[4:7],a[36:39]
buffer_load_dwordx4 v231, s[8:11], s64 offen offset:3072 lds
MM a[36:39],a[152:155],v[0:3],a[36:39]
s_add_u32 m0, s65, 65536
EX v110,v110
MM a[64:67],a[160:163],v[0:3],a[64:67]
RD a[224:227],v224 offset:40960
CV v78,v108,v109
MM a[64:67],a[164:167],v[4:7],a[64:67]
EX v111,v111
MM a[72:75],a[164:167],v[12:15],a[72:75]
EX v112,v112
MM a[72:75],a[160:163],v[8:11],a[72:75]
RD a[228:231],v225 offset:40960
CV v79,v110,v111
MM a[80:83],a[160:163],v[16:19],a[80:83]
EX v113,v113
MM a[80:83],a[164:167],v[20:23],a[80:83]
buffer_load_dwordx4 v235, s[12:15], s81 offen offset:0 lds
MM a[88:91],a[164:167],v[28:31],a[88:91]
EX v114,v114
MM a[88:91],a[160:163],v[24:27],a[88:91]
RD a[232:235],v226 offset:40960
CV v84,v112,v113
MM a[92:95],a[168:171],v[24:27],a[92:95]
EX v115,v115
MM a[92:95],a[172:175],v[28:31],a[92:95]
RD a[236:239],v227 offset:40960
CV v85,v114,v115
MM a[84:87],a[172:175],v[20:23],a[84:87]
EX v116,v116
MM a[84:87],a[168:171],v[16:19],a[84:87]
EX v117,v117
MM a[76:79],a[168:171],v[8:11],a[76:79]
RD a[240:243],v224 offset:45056
CV v86,v116,v117
MM a[76:79],a[172:175],v[12:15],a[76:79]
EX v118,v118
MM a[68:71],a[172:175],v[4:7],a[68:71]
buffer_load_dwordx4 v235, s[12:15], s64 offen offset:1024 lds
MM a[68:71],a[168:171],v[0:3],a[68:71]
EX v119,v119
MM a[96:99],a[176:179],v[0:3],a[96:99]
RD a[244:247],v225 offset:45056
CV v87,v118,v119
MM a[96:99],a[180:183],v[4:7],a[96:99]
EX v120,v120
MM a[104:107],a[180:183],v[12:15],a[104:107]
EX v121,v121
MM a[104:107],a[176:179],v[8:11],a[104:107]
RD a[248:251],v226 offset:45056
CV v92,v120,v121
MM a[112:115],a[176:179],v[16:19],a[112:115]
buffer_load_dwordx4 v235, s[12:15], s81 offen offset:2048 lds
MM a[112:115],a[180:183],v[20:23],a[112:115]
EX v122,v122
MM a[120:123],a[180:183],v[28:31],a[120:123]
EX v123,v123
MM a[120:123],a[176:179],v[24:27],a[120:123]
RD a[252:255],v227 offset:45056
CV v93,v122,v123
MM a[124:127],a[184:187],v[24:27],a[124:127]
EX v124,v124
MM a[124:127],a[188:191],v[28:31],a[124:127]
EX v125,v125
MM a[116:119],a[188:191],v[20:23],a[116:119]
buffer_load_dwordx4 v235, s[12:15], s64 offen offset:3072 lds
MM a[116:119],a[184:187],v[16:19],a[116:119]
EX v126,v126
MM a[108:111],a[184:187],v[8:11],a[108:111]
EX v127,v127
MM a[108:111],a[188:191],v[12:15],a[108:111]
CV v94,v124,v125
CV v95,v126,v127
MM a[100:103],a[188:191],v[4:7],a[100:103]
MM a[100:103],a[184:187],v[0:3],a[100:103]
v_smfmac_f32_16x16x64_bf16 v[236:239], v[144:147], v[64:71], v234
v_smfmac_f32_16x16x64_bf16 v[236:239], v[148:151], v[72:79], v234
v_smfmac_f32_16x16x64_bf16 v[236:239], v[152:155], v[80:87], v234
v_smfmac_f32_16x16x64_bf16 v[236:239], v[156:159], v[88:95], v234
s_waitcnt lgkmcnt(0)
MM v[0:3],a[192:195],v[160:163],v[128:131]
s_add_u32 s64, s64, 16384
TR a[128:129],v232 offset:16384
s_add_u32 s81, s64, 896
MM v[0:3],a[196:199],v[176:179],v[0:3]
TR a[130:131],v232 offset:20480
TR a[132:133],v232 offset:24576
MM v[0:3],a[200:203],v[192:195],v[0:3]
TR a[134:135],v232 offset:28672
TR a[136:137],v233 offset:16384
MM v[0:3],a[204:207],v[208:211],v[0:3]
TR a[138:139],v233 offset:20480
v_add_f32 v247, v236, v237
MM v[4:7],a[220:223],v[208:211],v[128:131]
TR a[140:141],v233 offset:24576
v_cmp_nge_f32 vcc, s71, v247
MM v[4:7],a[216:219],v[192:195],v[4:7]
s_cbranch_vccnz LSITE25_%=
LBACK25_%=:
TR a[142:143],v233 offset:28672
TR a[144:145],v232 offset:16896
MM v[4:7],a[212:215],v[176:179],v[4:7]
TR a[146:147],v232 offset:20992
TR a[148:149],v232 offset:25088
MM v[4:7],a[208:211],v[160:163],v[4:7]
TR a[150:151],v232 offset:29184
TR a[152:153],v233 offset:16896
MM v[12:15],a[208:211],v[164:167],v[132:135]
EX v0,v0
MM v[12:15],a[212:215],v[180:183],v[12:15]
EX v1,v1
MM v[12:15],a[216:219],v[196:199],v[12:15]
TR a[154:155],v233 offset:20992
CV v0,v0,v1
MM v[12:15],a[220:223],v[212:215],v[12:15]
EX v2,v2
MM v[8:11],a[204:207],v[212:215],v[132:135]
EX v3,v3
MM v[8:11],a[200:203],v[196:199],v[8:11]
TR a[156:157],v233 offset:25088
CV v1,v2,v3
MM v[8:11],a[196:199],v[180:183],v[8:11]
EX v4,v4
MM v[8:11],a[192:195],v[164:167],v[8:11]
EX v5,v5
MM v[16:19],a[192:195],v[168:171],v[136:139]
TR a[158:159],v233 offset:29184
CV v2,v4,v5
MM v[16:19],a[196:199],v[184:187],v[16:19]
EX v6,v6
MM v[16:19],a[200:203],v[200:203],v[16:19]
EX v7,v7
MM v[16:19],a[204:207],v[216:219],v[16:19]
TR a[160:161],v232 offset:17408
CV v3,v6,v7
MM v[20:23],a[220:223],v[216:219],v[136:139]
EX v8,v8
MM v[20:23],a[216:219],v[200:203],v[20:23]
EX v9,v9
MM v[20:23],a[212:215],v[184:187],v[20:23]
TR a[162:163],v232 offset:21504
CV v8,v8,v9
MM v[20:23],a[208:211],v[168:171],v[20:23]
EX v10,v10
MM v[28:31],a[208:211],v[172:175],v[140:143]
)ASM"
R"ASM(
TR a[164:165],v232 offset:25600
TR a[166:167],v232 offset:29696
MM v[28:31],a[212:215],v[188:191],v[28:31]
EX v11,v11
MM v[28:31],a[216:219],v[204:207],v[28:31]
TR a[168:169],v233 offset:17408
CV v9,v10,v11
MM v[28:31],a[220:223],v[220:223],v[28:31]
EX v12,v12
MM v[24:27],a[204:207],v[220:223],v[140:143]
EX v13,v13
MM v[24:27],a[200:203],v[204:207],v[24:27]
TR a[170:171],v233 offset:21504
CV v10,v12,v13
MM v[24:27],a[196:199],v[188:191],v[24:27]
EX v14,v14
MM v[24:27],a[192:195],v[172:175],v[24:27]
TR a[172:173],v233 offset:25600
TR a[174:175],v233 offset:29696
MM v[32:35],a[224:227],v[160:163],v[128:131]
EX v15,v15
MM v[32:35],a[228:231],v[176:179],v[32:35]
TR a[176:177],v232 offset:17920
CV v11,v14,v15
MM v[32:35],a[232:235],v[192:195],v[32:35]
EX v16,v16
MM v[32:35],a[236:239],v[208:211],v[32:35]
TR a[178:179],v232 offset:22016
TR a[180:181],v232 offset:26112
MM v[36:39],a[252:255],v[208:211],v[128:131]
EX v17,v17
MM v[36:39],a[248:251],v[192:195],v[36:39]
TR a[182:183],v232 offset:30208
CV v16,v16,v17
MM v[36:39],a[244:247],v[176:179],v[36:39]
EX v18,v18
MM v[36:39],a[240:243],v[160:163],v[36:39]
EX v19,v19
MM v[44:47],a[240:243],v[164:167],v[132:135]
TR a[184:185],v233 offset:17920
CV v17,v18,v19
MM v[44:47],a[244:247],v[180:183],v[44:47]
EX v20,v20
MM v[44:47],a[248:251],v[196:199],v[44:47]
TR a[186:187],v233 offset:22016
TR a[188:189],v233 offset:26112
MM v[44:47],a[252:255],v[212:215],v[44:47]
EX v21,v21
MM v[40:43],a[236:239],v[212:215],v[132:135]
TR a[190:191],v233 offset:30208
CV v18,v20,v21
MM v[40:43],a[232:235],v[196:199],v[40:43]
EX v22,v22
MM v[40:43],a[228:231],v[180:183],v[40:43]
EX v23,v23
MM v[40:43],a[224:227],v[164:167],v[40:43]
CV v19,v22,v23
MM v[48:51],a[224:227],v[168:171],v[136:139]
EX v24,v24
MM v[48:51],a[228:231],v[184:187],v[48:51]
EX v25,v25
MM v[48:51],a[232:235],v[200:203],v[48:51]
EX v26,v26
MM v[48:51],a[236:239],v[216:219],v[48:51]
EX v27,v27
MM v[52:55],a[252:255],v[216:219],v[136:139]
EX v28,v28
MM v[52:55],a[248:251],v[200:203],v[52:55]
EX v29,v29
MM v[52:55],a[244:247],v[184:187],v[52:55]
EX v30,v30
MM v[52:55],a[240:243],v[168:171],v[52:55]
EX v31,v31
MM v[60:63],a[240:243],v[172:175],v[140:143]
EX v32,v32
MM v[60:63],a[244:247],v[188:191],v[60:63]
EX v33,v33
MM v[60:63],a[248:251],v[204:207],v[60:63]
EX v34,v34
MM v[60:63],a[252:255],v[220:223],v[60:63]
EX v35,v35
MM v[56:59],a[236:239],v[220:223],v[140:143]
EX v36,v36
MM v[56:59],a[232:235],v[204:207],v[56:59]
EX v37,v37
MM v[56:59],a[228:231],v[188:191],v[56:59]
EX v38,v38
MM v[56:59],a[224:227],v[172:175],v[56:59]
s_waitcnt vmcnt(16) lgkmcnt(0)
s_barrier
MM a[0:3],a[128:131],v[64:67],a[0:3]
s_waitcnt vmcnt(8)
v_cmp_ne_u32 vcc, s69, v252
s_cbranch_vccnz LRFH_%=
LRFHb_%=:
MM a[0:3],a[132:135],v[68:71],a[0:3]
s_add_u32 m0, s65, 16384
LPG16_%=:
s_cbranch_execz LTR2_%=
v_lshrrev_b32 v253, 7, v248
v_mul_u32_u24 v253, 3328, v253
MM a[8:11],a[132:135],v[76:79],a[8:11]
v_add_u32 v253, v253, v248
v_lshrrev_b32 v252, 6, v248
MM a[8:11],a[128:131],v[72:75],a[8:11]
v_mul_u32_u24 v252, 3392, v252
v_add_u32 v252, v252, v248
MM a[16:19],a[128:131],v[80:83],a[16:19]
s_add_u32 s76, s76, 21557248
CV v24,v24,v25
CV v25,v26,v27
MM a[16:19],a[132:135],v[84:87],a[16:19]
s_lshl_b32 s90, %[swave], 11
RD a[192:195],v224 offset:49152
CV v26,v28,v29
MM a[24:27],a[132:135],v[92:95],a[24:27]
s_add_u32 s90, s90, %[slds]
s_add_u32 m0, s90, 139264
s_nop 0
buffer_load_dwordx4 v253, s[20:23], s76 offen offset:0 sc1 lds
MM a[24:27],a[128:131],v[88:91],a[24:27]
RD a[196:199],v225 offset:49152
CV v27,v30,v31
MM a[28:31],a[136:139],v[88:91],a[28:31]
s_add_u32 s76, s76, 128
s_add_u32 m0, s90, 140288
s_mov_b32 exec_hi, 0
buffer_load_dwordx4 v252, s[20:23], s76 offen offset:0 sc1 lds
s_mov_b64 exec, -1
MM a[28:31],a[140:143],v[92:95],a[28:31]
RD a[200:203],v226 offset:49152
CV v4,v32,v33
MM a[20:23],a[140:143],v[84:87],a[20:23]
s_add_u32 m0, s65, 16384
s_nop 0
buffer_load_dwordx4 v228, s[8:11], s64 offen offset:0 lds
MM a[20:23],a[136:139],v[80:83],a[20:23]
RD a[204:207],v227 offset:49152
CV v5,v34,v35
MM a[12:15],a[136:139],v[72:75],a[12:15]
RD a[208:211],v224 offset:53248
CV v6,v36,v37
MM a[12:15],a[140:143],v[76:79],a[12:15]
buffer_load_dwordx4 v229, s[8:11], s64 offen offset:1024 lds
MM a[4:7],a[140:143],v[68:71],a[4:7]
RD a[212:215],v225 offset:53248
RD a[216:219],v226 offset:53248
MM a[4:7],a[136:139],v[64:67],a[4:7]
EX v39,v39
MM a[32:35],a[144:147],v[64:67],a[32:35]
RD a[220:223],v227 offset:53248
CV v7,v38,v39
MM a[32:35],a[148:151],v[68:71],a[32:35]
buffer_load_dwordx4 v230, s[8:11], s64 offen offset:2048 lds
MM a[40:43],a[148:151],v[76:79],a[40:43]
EX v40,v40
MM a[40:43],a[144:147],v[72:75],a[40:43]
EX v41,v41
MM a[48:51],a[144:147],v[80:83],a[48:51]
RD a[224:227],v224 offset:57344
CV v12,v40,v41
MM a[48:51],a[148:151],v[84:87],a[48:51]
EX v42,v42
MM a[56:59],a[148:151],v[92:95],a[56:59]
buffer_load_dwordx4 v231, s[8:11], s64 offen offset:3072 lds
MM a[56:59],a[144:147],v[88:91],a[56:59]
s_add_u32 m0, s65, 81920
EX v43,v43
MM a[60:63],a[152:155],v[88:91],a[60:63]
RD a[228:231],v225 offset:57344
CV v13,v42,v43
MM a[60:63],a[156:159],v[92:95],a[60:63]
EX v44,v44
MM a[52:55],a[156:159],v[84:87],a[52:55]
EX v45,v45
MM a[52:55],a[152:155],v[80:83],a[52:55]
RD a[232:235],v226 offset:57344
CV v14,v44,v45
MM a[44:47],a[152:155],v[72:75],a[44:47]
EX v46,v46
MM a[44:47],a[156:159],v[76:79],a[44:47]
buffer_load_dwordx4 v235, s[12:15], s81 offen offset:0 lds
MM a[36:39],a[156:159],v[68:71],a[36:39]
EX v47,v47
MM a[36:39],a[152:155],v[64:67],a[36:39]
RD a[236:239],v227 offset:57344
CV v15,v46,v47
MM a[64:67],a[160:163],v[64:67],a[64:67]
EX v48,v48
MM a[64:67],a[164:167],v[68:71],a[64:67]
EX v49,v49
MM a[72:75],a[164:167],v[76:79],a[72:75]
RD a[240:243],v224 offset:61440
CV v20,v48,v49
MM a[72:75],a[160:163],v[72:75],a[72:75]
buffer_load_dwordx4 v235, s[12:15], s64 offen offset:1024 lds
MM a[80:83],a[160:163],v[80:83],a[80:83]
EX v50,v50
MM a[80:83],a[164:167],v[84:87],a[80:83]
EX v51,v51
MM a[88:91],a[164:167],v[92:95],a[88:91]
RD a[244:247],v225 offset:61440
CV v21,v50,v51
MM a[88:91],a[160:163],v[88:91],a[88:91]
EX v52,v52
MM a[92:95],a[168:171],v[88:91],a[92:95]
EX v53,v53
MM a[92:95],a[172:175],v[92:95],a[92:95]
RD a[248:251],v226 offset:61440
CV v22,v52,v53
MM a[84:87],a[172:175],v[84:87],a[84:87]
buffer_load_dwordx4 v235, s[12:15], s81 offen offset:2048 lds
MM a[84:87],a[168:171],v[80:83],a[84:87]
EX v54,v54
MM a[76:79],a[168:171],v[72:75],a[76:79]
EX v55,v55
MM a[76:79],a[172:175],v[76:79],a[76:79]
RD a[252:255],v227 offset:61440
CV v23,v54,v55
MM a[68:71],a[172:175],v[68:71],a[68:71]
EX v56,v56
MM a[68:71],a[168:171],v[64:67],a[68:71]
EX v57,v57
MM a[96:99],a[176:179],v[64:67],a[96:99]
buffer_load_dwordx4 v235, s[12:15], s64 offen offset:3072 lds
MM a[96:99],a[180:183],v[68:71],a[96:99]
EX v58,v58
MM a[104:107],a[180:183],v[76:79],a[104:107]
EX v59,v59
MM a[104:107],a[176:179],v[72:75],a[104:107]
EX v60,v60
MM a[112:115],a[176:179],v[80:83],a[112:115]
EX v61,v61
MM a[112:115],a[180:183],v[84:87],a[112:115]
EX v62,v62
MM a[120:123],a[180:183],v[92:95],a[120:123]
EX v63,v63
MM a[120:123],a[176:179],v[88:91],a[120:123]
CV v28,v56,v57
CV v29,v58,v59
MM a[124:127],a[184:187],v[88:91],a[124:127]
CV v30,v60,v61
CV v31,v62,v63
MM a[124:127],a[188:191],v[92:95],a[124:127]
MM a[116:119],a[188:191],v[84:87],a[116:119]
MM a[116:119],a[184:187],v[80:83],a[116:119]
MM a[108:111],a[184:187],v[72:75],a[108:111]
MM a[108:111],a[188:191],v[76:79],a[108:111]
MM a[100:103],a[188:191],v[68:71],a[100:103]
MM a[100:103],a[184:187],v[64:67],a[100:103]
v_smfmac_f32_16x16x64_bf16 v[236:239], v[144:147], v[0:7], v234
v_smfmac_f32_16x16x64_bf16 v[236:239], v[148:151], v[8:15], v234
v_smfmac_f32_16x16x64_bf16 v[236:239], v[152:155], v[16:23], v234
v_smfmac_f32_16x16x64_bf16 v[236:239], v[156:159], v[24:31], v234
s_waitcnt lgkmcnt(0)
MM v[64:67],a[192:195],v[160:163],v[128:131]
s_add_u32 s64, s64, 16384
TR a[128:129],v232 offset:32768
s_add_u32 s81, s64, 896
MM v[64:67],a[196:199],v[176:179],v[64:67]
TR a[130:131],v232 offset:36864
TR a[132:133],v232 offset:40960
MM v[64:67],a[200:203],v[192:195],v[64:67]
TR a[134:135],v232 offset:45056
TR a[136:137],v233 offset:32768
MM v[64:67],a[204:207],v[208:211],v[64:67]
TR a[138:139],v233 offset:36864
v_add_f32 v246, v236, v237
MM v[68:71],a[220:223],v[208:211],v[128:131]
TR a[140:141],v233 offset:40960
v_cmp_nge_f32 vcc, s71, v246
MM v[68:71],a[216:219],v[192:195],v[68:71]
s_cbranch_vccnz LSITE26_%=
LBACK26_%=:
TR a[142:143],v233 offset:45056
TR a[144:145],v232 offset:33280
MM v[68:71],a[212:215],v[176:179],v[68:71]
s_add_u32 m0, s94, 131072
s_add_u32 s66, s85, 0
buffer_load_dwordx4 v241, s[24:27], s66 offen offset:0 lds
MM v[68:71],a[208:211],v[160:163],v[68:71]
TR a[146:147],v232 offset:37376
TR a[148:149],v232 offset:41472
MM v[76:79],a[208:211],v[164:167],v[132:135]
EX v64,v64
MM v[76:79],a[212:215],v[180:183],v[76:79]
EX v65,v65
MM v[76:79],a[216:219],v[196:199],v[76:79]
TR a[150:151],v232 offset:45568
CV v64,v64,v65
MM v[76:79],a[220:223],v[212:215],v[76:79]
EX v66,v66
MM v[72:75],a[204:207],v[212:215],v[132:135]
EX v67,v67
MM v[72:75],a[200:203],v[196:199],v[72:75]
TR a[152:153],v233 offset:33280
CV v65,v66,v67
MM v[72:75],a[196:199],v[180:183],v[72:75]
s_add_u32 m0, s94, 135168
s_add_u32 s66, s85, 81920
buffer_load_dwordx4 v241, s[24:27], s66 offen offset:0 lds
MM v[72:75],a[192:195],v[164:167],v[72:75]
EX v68,v68
MM v[80:83],a[192:195],v[168:171],v[136:139]
EX v69,v69
MM v[80:83],a[196:199],v[184:187],v[80:83]
TR a[154:155],v233 offset:37376
CV v66,v68,v69
MM v[80:83],a[200:203],v[200:203],v[80:83]
EX v70,v70
MM v[80:83],a[204:207],v[216:219],v[80:83]
EX v71,v71
MM v[84:87],a[220:223],v[216:219],v[136:139]
TR a[156:157],v233 offset:41472
CV v67,v70,v71
MM v[84:87],a[216:219],v[200:203],v[84:87]
EX v72,v72
MM v[84:87],a[212:215],v[184:187],v[84:87]
s_add_u32 m0, s94, 147456
s_add_u32 s66, s87, 0
buffer_load_dwordx4 v241, s[24:27], s66 offen offset:0 lds
MM v[84:87],a[208:211],v[168:171],v[84:87]
TR a[158:159],v233 offset:45568
TR a[160:161],v232 offset:33792
MM v[92:95],a[208:211],v[172:175],v[140:143]
EX v73,v73
MM v[92:95],a[212:215],v[188:191],v[92:95]
TR a[162:163],v232 offset:37888
CV v72,v72,v73
MM v[92:95],a[216:219],v[204:207],v[92:95]
EX v74,v74
MM v[92:95],a[220:223],v[220:223],v[92:95]
EX v75,v75
MM v[88:91],a[204:207],v[220:223],v[140:143]
TR a[164:165],v232 offset:41984
CV v73,v74,v75
MM v[88:91],a[200:203],v[204:207],v[88:91]
EX v76,v76
MM v[88:91],a[196:199],v[188:191],v[88:91]
TR a[166:167],v232 offset:46080
TR a[168:169],v233 offset:33792
MM v[88:91],a[192:195],v[172:175],v[88:91]
EX v77,v77
MM v[96:99],a[224:227],v[160:163],v[128:131]
TR a[170:171],v233 offset:37888
CV v74,v76,v77
MM v[96:99],a[228:231],v[176:179],v[96:99]
EX v78,v78
MM v[96:99],a[232:235],v[192:195],v[96:99]
EX v79,v79
MM v[96:99],a[236:239],v[208:211],v[96:99]
TR a[172:173],v233 offset:41984
CV v75,v78,v79
MM v[100:103],a[252:255],v[208:211],v[128:131]
EX v80,v80
MM v[100:103],a[248:251],v[192:195],v[100:103]
TR a[174:175],v233 offset:46080
TR a[176:177],v232 offset:34304
MM v[100:103],a[244:247],v[176:179],v[100:103]
EX v81,v81
MM v[100:103],a[240:243],v[160:163],v[100:103]
TR a[178:179],v232 offset:38400
CV v80,v80,v81
MM v[108:111],a[240:243],v[164:167],v[132:135]
EX v82,v82
MM v[108:111],a[244:247],v[180:183],v[108:111]
EX v83,v83
MM v[108:111],a[248:251],v[196:199],v[108:111]
TR a[180:181],v232 offset:42496
CV v81,v82,v83
MM v[108:111],a[252:255],v[212:215],v[108:111]
EX v84,v84
MM v[104:107],a[236:239],v[212:215],v[132:135]
TR a[182:183],v232 offset:46592
TR a[184:185],v233 offset:34304
MM v[104:107],a[232:235],v[196:199],v[104:107]
EX v85,v85
MM v[104:107],a[228:231],v[180:183],v[104:107]
TR a[186:187],v233 offset:38400
CV v82,v84,v85
MM v[104:107],a[224:227],v[164:167],v[104:107]
EX v86,v86
MM v[112:115],a[224:227],v[168:171],v[136:139]
TR a[188:189],v233 offset:42496
TR a[190:191],v233 offset:46592
MM v[112:115],a[228:231],v[184:187],v[112:115]
EX v87,v87
MM v[112:115],a[232:235],v[200:203],v[112:115]
)ASM"
R"ASM(
CV v83,v86,v87
MM v[112:115],a[236:239],v[216:219],v[112:115]
EX v88,v88
MM v[116:119],a[252:255],v[216:219],v[136:139]
EX v89,v89
MM v[116:119],a[248:251],v[200:203],v[116:119]
EX v90,v90
MM v[116:119],a[244:247],v[184:187],v[116:119]
EX v91,v91
MM v[116:119],a[240:243],v[168:171],v[116:119]
EX v92,v92
MM v[124:127],a[240:243],v[172:175],v[140:143]
EX v93,v93
MM v[124:127],a[244:247],v[188:191],v[124:127]
EX v94,v94
MM v[124:127],a[248:251],v[204:207],v[124:127]
EX v95,v95
MM v[124:127],a[252:255],v[220:223],v[124:127]
EX v96,v96
MM v[120:123],a[236:239],v[220:223],v[140:143]
EX v97,v97
MM v[120:123],a[232:235],v[204:207],v[120:123]
EX v98,v98
MM v[120:123],a[228:231],v[188:191],v[120:123]
EX v99,v99
MM v[120:123],a[224:227],v[172:175],v[120:123]
s_waitcnt vmcnt(17) lgkmcnt(0)
s_barrier
MM a[0:3],a[128:131],v[0:3],a[0:3]
s_add_u32 m0, s65, 32768
CV v88,v88,v89
CV v89,v90,v91
MM a[0:3],a[132:135],v[4:7],a[0:3]
RD a[192:195],v224 offset:0
CV v90,v92,v93
MM a[8:11],a[132:135],v[12:15],a[8:11]
RD a[196:199],v225 offset:0
CV v91,v94,v95
MM a[8:11],a[128:131],v[8:11],a[8:11]
buffer_load_dwordx4 v228, s[8:11], s64 offen offset:0 lds
MM a[16:19],a[128:131],v[16:19],a[16:19]
RD a[200:203],v226 offset:0
CV v68,v96,v97
MM a[16:19],a[132:135],v[20:23],a[16:19]
RD a[204:207],v227 offset:0
CV v69,v98,v99
MM a[24:27],a[132:135],v[28:31],a[24:27]
buffer_load_dwordx4 v229, s[8:11], s64 offen offset:1024 lds
MM a[24:27],a[128:131],v[24:27],a[24:27]
RD a[208:211],v224 offset:4096
RD a[212:215],v225 offset:4096
MM a[28:31],a[136:139],v[24:27],a[28:31]
buffer_load_dwordx4 v230, s[8:11], s64 offen offset:2048 lds
MM a[28:31],a[140:143],v[28:31],a[28:31]
EX v100,v100
MM a[20:23],a[140:143],v[20:23],a[20:23]
EX v101,v101
MM a[20:23],a[136:139],v[16:19],a[20:23]
RD a[216:219],v226 offset:4096
CV v70,v100,v101
MM a[12:15],a[136:139],v[8:11],a[12:15]
EX v102,v102
MM a[12:15],a[140:143],v[12:15],a[12:15]
RD a[220:223],v227 offset:4096
RD a[224:227],v224 offset:8192
MM a[4:7],a[140:143],v[4:7],a[4:7]
EX v103,v103
MM a[4:7],a[136:139],v[0:3],a[4:7]
RD a[228:231],v225 offset:8192
CV v71,v102,v103
MM a[32:35],a[144:147],v[0:3],a[32:35]
EX v104,v104
MM a[32:35],a[148:151],v[4:7],a[32:35]
buffer_load_dwordx4 v231, s[8:11], s64 offen offset:3072 lds
MM a[40:43],a[148:151],v[12:15],a[40:43]
s_add_u32 m0, s65, 98304
EX v105,v105
MM a[40:43],a[144:147],v[8:11],a[40:43]
RD a[232:235],v226 offset:8192
CV v76,v104,v105
MM a[48:51],a[144:147],v[16:19],a[48:51]
EX v106,v106
MM a[48:51],a[148:151],v[20:23],a[48:51]
EX v107,v107
MM a[56:59],a[148:151],v[28:31],a[56:59]
RD a[236:239],v227 offset:8192
CV v77,v106,v107
MM a[56:59],a[144:147],v[24:27],a[56:59]
EX v108,v108
MM a[60:63],a[152:155],v[24:27],a[60:63]
buffer_load_dwordx4 v235, s[12:15], s81 offen offset:0 lds
MM a[60:63],a[156:159],v[28:31],a[60:63]
EX v109,v109
MM a[52:55],a[156:159],v[20:23],a[52:55]
RD a[240:243],v224 offset:12288
CV v78,v108,v109
MM a[52:55],a[152:155],v[16:19],a[52:55]
EX v110,v110
MM a[44:47],a[152:155],v[8:11],a[44:47]
EX v111,v111
MM a[44:47],a[156:159],v[12:15],a[44:47]
RD a[244:247],v225 offset:12288
CV v79,v110,v111
MM a[36:39],a[156:159],v[4:7],a[36:39]
buffer_load_dwordx4 v235, s[12:15], s64 offen offset:1024 lds
MM a[36:39],a[152:155],v[0:3],a[36:39]
EX v112,v112
MM a[64:67],a[160:163],v[0:3],a[64:67]
EX v113,v113
MM a[64:67],a[164:167],v[4:7],a[64:67]
RD a[248:251],v226 offset:12288
CV v84,v112,v113
MM a[72:75],a[164:167],v[12:15],a[72:75]
EX v114,v114
MM a[72:75],a[160:163],v[8:11],a[72:75]
EX v115,v115
MM a[80:83],a[160:163],v[16:19],a[80:83]
RD a[252:255],v227 offset:12288
CV v85,v114,v115
MM a[80:83],a[164:167],v[20:23],a[80:83]
buffer_load_dwordx4 v235, s[12:15], s81 offen offset:2048 lds
MM a[88:91],a[164:167],v[28:31],a[88:91]
EX v116,v116
MM a[88:91],a[160:163],v[24:27],a[88:91]
EX v117,v117
MM a[92:95],a[168:171],v[24:27],a[92:95]
EX v118,v118
MM a[92:95],a[172:175],v[28:31],a[92:95]
buffer_load_dwordx4 v235, s[12:15], s64 offen offset:3072 lds
MM a[84:87],a[172:175],v[20:23],a[84:87]
EX v119,v119
MM a[84:87],a[168:171],v[16:19],a[84:87]
EX v120,v120
MM a[76:79],a[168:171],v[8:11],a[76:79]
EX v121,v121
MM a[76:79],a[172:175],v[12:15],a[76:79]
EX v122,v122
MM a[68:71],a[172:175],v[4:7],a[68:71]
EX v123,v123
MM a[68:71],a[168:171],v[0:3],a[68:71]
EX v124,v124
MM a[96:99],a[176:179],v[0:3],a[96:99]
EX v125,v125
MM a[96:99],a[180:183],v[4:7],a[96:99]
EX v126,v126
MM a[104:107],a[180:183],v[12:15],a[104:107]
EX v127,v127
MM a[104:107],a[176:179],v[8:11],a[104:107]
CV v86,v116,v117
CV v87,v118,v119
MM a[112:115],a[176:179],v[16:19],a[112:115]
CV v92,v120,v121
CV v93,v122,v123
MM a[112:115],a[180:183],v[20:23],a[112:115]
CV v94,v124,v125
CV v95,v126,v127
MM a[120:123],a[180:183],v[28:31],a[120:123]
MM a[120:123],a[176:179],v[24:27],a[120:123]
MM a[124:127],a[184:187],v[24:27],a[124:127]
MM a[124:127],a[188:191],v[28:31],a[124:127]
MM a[116:119],a[188:191],v[20:23],a[116:119]
MM a[116:119],a[184:187],v[16:19],a[116:119]
MM a[108:111],a[184:187],v[8:11],a[108:111]
MM a[108:111],a[188:191],v[12:15],a[108:111]
MM a[100:103],a[188:191],v[4:7],a[100:103]
MM a[100:103],a[184:187],v[0:3],a[100:103]
v_smfmac_f32_16x16x64_bf16 v[236:239], v[144:147], v[64:71], v234
v_smfmac_f32_16x16x64_bf16 v[236:239], v[148:151], v[72:79], v234
v_smfmac_f32_16x16x64_bf16 v[236:239], v[152:155], v[80:87], v234
v_smfmac_f32_16x16x64_bf16 v[236:239], v[156:159], v[88:95], v234
s_waitcnt lgkmcnt(0)
MM v[0:3],a[192:195],v[160:163],v[128:131]
s_add_u32 s64, s64, 16384
TR a[128:129],v232 offset:49152
s_add_u32 s81, s64, 896
MM v[0:3],a[196:199],v[176:179],v[0:3]
TR a[130:131],v232 offset:53248
TR a[132:133],v232 offset:57344
MM v[0:3],a[200:203],v[192:195],v[0:3]
TR a[134:135],v232 offset:61440
TR a[136:137],v233 offset:49152
MM v[0:3],a[204:207],v[208:211],v[0:3]
TR a[138:139],v233 offset:53248
v_add_f32 v247, v236, v237
MM v[4:7],a[220:223],v[208:211],v[128:131]
TR a[140:141],v233 offset:57344
v_cmp_nge_f32 vcc, s71, v247
MM v[4:7],a[216:219],v[192:195],v[4:7]
s_cbranch_vccnz LSITE27_%=
LBACK27_%=:
TR a[142:143],v233 offset:61440
TR a[144:145],v232 offset:49664
MM v[4:7],a[212:215],v[176:179],v[4:7]
v_lshrrev_b32 v125, 8, v248
v_lshlrev_b32 v124, 10, v125
MM v[4:7],a[208:211],v[160:163],v[4:7]
v_bfe_u32 v126, v248, 6, 2
v_lshl_add_u32 v124, v126, 7, v124
MM v[12:15],a[208:211],v[164:167],v[132:135]
v_bfe_u32 v126, v248, 4, 2
v_lshl_add_u32 v124, v126, 3, v124
MM v[12:15],a[212:215],v[180:183],v[12:15]
s_lshl_b32 s91, %[swave], 5
EX v0,v0
MM v[12:15],a[216:219],v[196:199],v[12:15]
s_add_u32 s91, s91, %[slds]
EX v1,v1
MM v[12:15],a[220:223],v[212:215],v[12:15]
s_add_u32 s91, s91, 151552
v_add_u32 v124, s91, v124
v_and_b32 v126, 112, v248
MM v[8:11],a[204:207],v[212:215],v[132:135]
s_lshl_b32 s91, %[swave], 11
EX v2,v2
MM v[8:11],a[200:203],v[196:199],v[8:11]
s_add_u32 s91, s91, %[slds]
TR a[146:147],v232 offset:53760
CV v0,v0,v1
MM v[8:11],a[196:199],v[180:183],v[8:11]
s_add_u32 s91, s91, 139264
v_lshl_add_u32 v127, v125, 4, s91
v_lshl_add_u32 v127, v126, 2, v127
MM v[8:11],a[192:195],v[164:167],v[8:11]
TR a[148:149],v232 offset:57856
v_lshl_add_u32 v126, v126, 2, v127
MM v[16:19],a[192:195],v[168:171],v[136:139]
s_waitcnt vmcnt(19)
TR v[96:97],v124 offset:0
TR v[98:99],v124 offset:512
MM v[16:19],a[196:199],v[184:187],v[16:19]
RD v[108:111],v126 offset:0
TR v[100:101],v124 offset:4096
MM v[16:19],a[200:203],v[200:203],v[16:19]
TR v[102:103],v124 offset:4608
RD v[112:115],v126 offset:64
MM v[16:19],a[204:207],v[216:219],v[16:19]
TR v[104:105],v124 offset:8192
TR v[106:107],v124 offset:8704
MM v[20:23],a[220:223],v[216:219],v[136:139]
RD v[116:119],v127 offset:1024
v_bfe_u32 v127, v248, 4, 3
EX v3,v3
MM v[20:23],a[216:219],v[200:203],v[20:23]
v_mul_u32_u24 v127, 2560, v127
v_lshl_add_u32 v127, v125, 4, v127
EX v4,v4
MM v[20:23],a[212:215],v[184:187],v[20:23]
s_lshl_b32 s90, %[swave], 6
EX v5,v5
MM v[20:23],a[208:211],v[168:171],v[20:23]
s_lshl_b32 s91, s47, 8
EX v6,v6
MM v[28:31],a[208:211],v[172:175],v[140:143]
s_add_u32 s90, s90, s91
EX v7,v7
MM v[28:31],a[212:215],v[188:191],v[28:31]
s_mul_i32 s91, s46, 17920
TR a[150:151],v232 offset:61952
CV v1,v2,v3
MM v[28:31],a[216:219],v[204:207],v[28:31]
s_add_u32 s90, s90, s91
EX v8,v8
MM v[28:31],a[220:223],v[220:223],v[28:31]
s_add_u32 s90, s90, 21659648
TR a[152:153],v233 offset:49664
CV v2,v4,v5
MM v[24:27],a[204:207],v[220:223],v[140:143]
EX v9,v9
MM v[24:27],a[200:203],v[204:207],v[24:27]
s_waitcnt lgkmcnt(2)
TR a[154:155],v233 offset:53760
TR a[156:157],v233 offset:57856
MM v[24:27],a[196:199],v[188:191],v[24:27]
EX v10,v10
MM v[120:123],v[96:99],v[108:111],0
MM v[120:123],v[100:103],v[112:115],v[120:123]
MM v[120:123],v[104:107],v[116:119],v[120:123]
MM v[24:27],a[192:195],v[172:175],v[24:27]
EX v11,v11
MM v[32:35],a[224:227],v[160:163],v[128:131]
TR a[158:159],v233 offset:61952
CV v3,v6,v7
MM v[32:35],a[228:231],v[176:179],v[32:35]
EX v12,v12
MM v[32:35],a[232:235],v[192:195],v[32:35]
TR a[160:161],v232 offset:50176
CV v8,v8,v9
MM v[32:35],a[236:239],v[208:211],v[32:35]
EX v13,v13
MM v[36:39],a[252:255],v[208:211],v[128:131]
TR a[162:163],v232 offset:54272
TR a[164:165],v232 offset:58368
MM v[36:39],a[248:251],v[192:195],v[36:39]
s_nop 0
LPG17_%=:
s_cbranch_execz LTR2_%=
s_mov_b32 exec_lo, 0x00ff00ff
s_mov_b32 exec_hi, 0x00ff00ff
buffer_store_dwordx4 v[120:123], v127, s[20:23], s90 offen offset:0 sc1
s_mov_b64 exec, -1
s_nop 0
MM v[36:39],a[244:247],v[176:179],v[36:39]
EX v14,v14
MM v[36:39],a[240:243],v[160:163],v[36:39]
EX v15,v15
MM v[44:47],a[240:243],v[164:167],v[132:135]
TR a[166:167],v232 offset:62464
CV v9,v10,v11
MM v[44:47],a[244:247],v[180:183],v[44:47]
EX v16,v16
MM v[44:47],a[248:251],v[196:199],v[44:47]
TR a[168:169],v233 offset:50176
TR a[170:171],v233 offset:54272
MM v[44:47],a[252:255],v[212:215],v[44:47]
EX v17,v17
MM v[40:43],a[236:239],v[212:215],v[132:135]
TR a[172:173],v233 offset:58368
CV v10,v12,v13
MM v[40:43],a[232:235],v[196:199],v[40:43]
EX v18,v18
MM v[40:43],a[228:231],v[180:183],v[40:43]
EX v19,v19
MM v[40:43],a[224:227],v[164:167],v[40:43]
TR a[174:175],v233 offset:62464
CV v11,v14,v15
MM v[48:51],a[224:227],v[168:171],v[136:139]
EX v20,v20
MM v[48:51],a[228:231],v[184:187],v[48:51]
TR a[176:177],v232 offset:50688
TR a[178:179],v232 offset:54784
MM v[48:51],a[232:235],v[200:203],v[48:51]
EX v21,v21
MM v[48:51],a[236:239],v[216:219],v[48:51]
TR a[180:181],v232 offset:58880
CV v16,v16,v17
MM v[52:55],a[252:255],v[216:219],v[136:139]
EX v22,v22
MM v[52:55],a[248:251],v[200:203],v[52:55]
TR a[182:183],v232 offset:62976
CV v17,v18,v19
MM v[52:55],a[244:247],v[184:187],v[52:55]
EX v23,v23
MM v[52:55],a[240:243],v[168:171],v[52:55]
TR a[184:185],v233 offset:50688
CV v18,v20,v21
MM v[60:63],a[240:243],v[172:175],v[140:143]
EX v24,v24
MM v[60:63],a[244:247],v[188:191],v[60:63]
TR a[186:187],v233 offset:54784
TR a[188:189],v233 offset:58880
MM v[60:63],a[248:251],v[204:207],v[60:63]
TR a[190:191],v233 offset:62976
CV v19,v22,v23
MM v[60:63],a[252:255],v[220:223],v[60:63]
EX v25,v25
MM v[56:59],a[236:239],v[220:223],v[140:143]
EX v26,v26
MM v[56:59],a[232:235],v[204:207],v[56:59]
EX v27,v27
MM v[56:59],a[228:231],v[188:191],v[56:59]
EX v28,v28
MM v[56:59],a[224:227],v[172:175],v[56:59]
s_waitcnt vmcnt(16) lgkmcnt(0)
s_barrier
MM a[0:3],a[128:131],v[64:67],a[0:3]
s_add_u32 m0, s65, 49152
CV v24,v24,v25
CV v25,v26,v27
MM a[0:3],a[132:135],v[68:71],a[0:3]
EX v29,v29
MM a[8:11],a[132:135],v[76:79],a[8:11]
EX v30,v30
MM a[8:11],a[128:131],v[72:75],a[8:11]
EX v31,v31
MM a[16:19],a[128:131],v[80:83],a[16:19]
RD a[192:195],v224 offset:16384
CV v26,v28,v29
MM a[16:19],a[132:135],v[84:87],a[16:19]
EX v32,v32
MM a[24:27],a[132:135],v[92:95],a[24:27]
RD a[196:199],v225 offset:16384
CV v27,v30,v31
MM a[24:27],a[128:131],v[88:91],a[24:27]
EX v33,v33
MM a[28:31],a[136:139],v[88:91],a[28:31]
buffer_load_dwordx4 v228, s[8:11], s64 offen offset:0 lds
MM a[28:31],a[140:143],v[92:95],a[28:31]
EX v34,v34
MM a[20:23],a[140:143],v[84:87],a[20:23]
EX v35,v35
MM a[20:23],a[136:139],v[80:83],a[20:23]
RD a[200:203],v226 offset:16384
)ASM"
R"ASM(
CV v4,v32,v33
MM a[12:15],a[136:139],v[72:75],a[12:15]
EX v36,v36
MM a[12:15],a[140:143],v[76:79],a[12:15]
RD a[204:207],v227 offset:16384
CV v5,v34,v35
MM a[4:7],a[140:143],v[68:71],a[4:7]
EX v37,v37
MM a[4:7],a[136:139],v[64:67],a[4:7]
buffer_load_dwordx4 v229, s[8:11], s64 offen offset:1024 lds
MM a[32:35],a[144:147],v[64:67],a[32:35]
EX v38,v38
MM a[32:35],a[148:151],v[68:71],a[32:35]
RD a[208:211],v224 offset:20480
CV v6,v36,v37
MM a[40:43],a[148:151],v[76:79],a[40:43]
EX v39,v39
MM a[40:43],a[144:147],v[72:75],a[40:43]
RD a[212:215],v225 offset:20480
CV v7,v38,v39
MM a[48:51],a[144:147],v[80:83],a[48:51]
EX v40,v40
MM a[48:51],a[148:151],v[84:87],a[48:51]
EX v41,v41
MM a[56:59],a[148:151],v[92:95],a[56:59]
buffer_load_dwordx4 v230, s[8:11], s64 offen offset:2048 lds
MM a[56:59],a[144:147],v[88:91],a[56:59]
EX v42,v42
MM a[60:63],a[152:155],v[88:91],a[60:63]
RD a[216:219],v226 offset:20480
CV v12,v40,v41
MM a[60:63],a[156:159],v[92:95],a[60:63]
EX v43,v43
MM a[52:55],a[156:159],v[84:87],a[52:55]
RD a[220:223],v227 offset:20480
CV v13,v42,v43
MM a[52:55],a[152:155],v[80:83],a[52:55]
EX v44,v44
MM a[44:47],a[152:155],v[72:75],a[44:47]
EX v45,v45
MM a[44:47],a[156:159],v[76:79],a[44:47]
buffer_load_dwordx4 v231, s[8:11], s64 offen offset:3072 lds
MM a[36:39],a[156:159],v[68:71],a[36:39]
s_add_u32 m0, s65, 114688
RD a[224:227],v224 offset:24576
CV v14,v44,v45
MM a[36:39],a[152:155],v[64:67],a[36:39]
EX v46,v46
MM a[64:67],a[160:163],v[64:67],a[64:67]
EX v47,v47
MM a[64:67],a[164:167],v[68:71],a[64:67]
RD a[228:231],v225 offset:24576
CV v15,v46,v47
MM a[72:75],a[164:167],v[76:79],a[72:75]
EX v48,v48
MM a[72:75],a[160:163],v[72:75],a[72:75]
buffer_load_dwordx4 v235, s[12:15], s81 offen offset:0 lds
MM a[80:83],a[160:163],v[80:83],a[80:83]
EX v49,v49
MM a[80:83],a[164:167],v[84:87],a[80:83]
RD a[232:235],v226 offset:24576
CV v20,v48,v49
MM a[88:91],a[164:167],v[92:95],a[88:91]
EX v50,v50
MM a[88:91],a[160:163],v[88:91],a[88:91]
EX v51,v51
MM a[92:95],a[168:171],v[88:91],a[92:95]
RD a[236:239],v227 offset:24576
CV v21,v50,v51
MM a[92:95],a[172:175],v[92:95],a[92:95]
EX v52,v52
MM a[84:87],a[172:175],v[84:87],a[84:87]
buffer_load_dwordx4 v235, s[12:15], s64 offen offset:1024 lds
MM a[84:87],a[168:171],v[80:83],a[84:87]
EX v53,v53
MM a[76:79],a[168:171],v[72:75],a[76:79]
RD a[240:243],v224 offset:28672
CV v22,v52,v53
MM a[76:79],a[172:175],v[76:79],a[76:79]
EX v54,v54
MM a[68:71],a[172:175],v[68:71],a[68:71]
EX v55,v55
MM a[68:71],a[168:171],v[64:67],a[68:71]
RD a[244:247],v225 offset:28672
CV v23,v54,v55
MM a[96:99],a[176:179],v[64:67],a[96:99]
EX v56,v56
MM a[96:99],a[180:183],v[68:71],a[96:99]
buffer_load_dwordx4 v235, s[12:15], s81 offen offset:2048 lds
MM a[104:107],a[180:183],v[76:79],a[104:107]
EX v57,v57
MM a[104:107],a[176:179],v[72:75],a[104:107]
RD a[248:251],v226 offset:28672
CV v28,v56,v57
MM a[112:115],a[176:179],v[80:83],a[112:115]
EX v58,v58
MM a[112:115],a[180:183],v[84:87],a[112:115]
EX v59,v59
MM a[120:123],a[180:183],v[92:95],a[120:123]
RD a[252:255],v227 offset:28672
CV v29,v58,v59
MM a[120:123],a[176:179],v[88:91],a[120:123]
EX v60,v60
MM a[124:127],a[184:187],v[88:91],a[124:127]
buffer_load_dwordx4 v235, s[12:15], s64 offen offset:3072 lds
MM a[124:127],a[188:191],v[92:95],a[124:127]
EX v61,v61
MM a[116:119],a[188:191],v[84:87],a[116:119]
EX v62,v62
MM a[116:119],a[184:187],v[80:83],a[116:119]
EX v63,v63
MM a[108:111],a[184:187],v[72:75],a[108:111]
CV v30,v60,v61
CV v31,v62,v63
MM a[108:111],a[188:191],v[76:79],a[108:111]
MM a[100:103],a[188:191],v[68:71],a[100:103]
MM a[100:103],a[184:187],v[64:67],a[100:103]
v_smfmac_f32_16x16x64_bf16 v[236:239], v[144:147], v[0:7], v234
v_smfmac_f32_16x16x64_bf16 v[236:239], v[148:151], v[8:15], v234
v_smfmac_f32_16x16x64_bf16 v[236:239], v[152:155], v[16:23], v234
v_smfmac_f32_16x16x64_bf16 v[236:239], v[156:159], v[24:31], v234
s_waitcnt lgkmcnt(0)
MM v[64:67],a[192:195],v[160:163],v[128:131]
s_sub_u32 s64, s64, 507904
TR a[128:129],v232 offset:0
s_add_u32 s81, s64, 896
MM v[64:67],a[196:199],v[176:179],v[64:67]
TR a[130:131],v232 offset:4096
TR a[132:133],v232 offset:8192
MM v[64:67],a[200:203],v[192:195],v[64:67]
TR a[134:135],v232 offset:12288
TR a[136:137],v233 offset:0
MM v[64:67],a[204:207],v[208:211],v[64:67]
TR a[138:139],v233 offset:4096
v_add_f32 v246, v236, v237
MM v[68:71],a[220:223],v[208:211],v[128:131]
TR a[140:141],v233 offset:8192
v_cmp_nge_f32 vcc, s71, v246
MM v[68:71],a[216:219],v[192:195],v[68:71]
s_cbranch_vccnz LSITE28_%=
LBACK28_%=:
TR a[142:143],v233 offset:12288
TR a[144:145],v232 offset:512
MM v[68:71],a[212:215],v[176:179],v[68:71]
TR a[146:147],v232 offset:4608
TR a[148:149],v232 offset:8704
MM v[68:71],a[208:211],v[160:163],v[68:71]
TR a[150:151],v232 offset:12800
TR a[152:153],v233 offset:512
MM v[76:79],a[208:211],v[164:167],v[132:135]
EX v64,v64
MM v[76:79],a[212:215],v[180:183],v[76:79]
EX v65,v65
MM v[76:79],a[216:219],v[196:199],v[76:79]
TR a[154:155],v233 offset:4608
CV v64,v64,v65
MM v[76:79],a[220:223],v[212:215],v[76:79]
EX v66,v66
MM v[72:75],a[204:207],v[212:215],v[132:135]
EX v67,v67
MM v[72:75],a[200:203],v[196:199],v[72:75]
TR a[156:157],v233 offset:8704
CV v65,v66,v67
MM v[72:75],a[196:199],v[180:183],v[72:75]
EX v68,v68
MM v[72:75],a[192:195],v[164:167],v[72:75]
EX v69,v69
MM v[80:83],a[192:195],v[168:171],v[136:139]
TR a[158:159],v233 offset:12800
CV v66,v68,v69
MM v[80:83],a[196:199],v[184:187],v[80:83]
EX v70,v70
MM v[80:83],a[200:203],v[200:203],v[80:83]
EX v71,v71
MM v[80:83],a[204:207],v[216:219],v[80:83]
TR a[160:161],v232 offset:1024
CV v67,v70,v71
MM v[84:87],a[220:223],v[216:219],v[136:139]
EX v72,v72
MM v[84:87],a[216:219],v[200:203],v[84:87]
EX v73,v73
MM v[84:87],a[212:215],v[184:187],v[84:87]
TR a[162:163],v232 offset:5120
CV v72,v72,v73
MM v[84:87],a[208:211],v[168:171],v[84:87]
EX v74,v74
MM v[92:95],a[208:211],v[172:175],v[140:143]
TR a[164:165],v232 offset:9216
TR a[166:167],v232 offset:13312
MM v[92:95],a[212:215],v[188:191],v[92:95]
EX v75,v75
MM v[92:95],a[216:219],v[204:207],v[92:95]
TR a[168:169],v233 offset:1024
CV v73,v74,v75
MM v[92:95],a[220:223],v[220:223],v[92:95]
EX v76,v76
MM v[88:91],a[204:207],v[220:223],v[140:143]
EX v77,v77
MM v[88:91],a[200:203],v[204:207],v[88:91]
TR a[170:171],v233 offset:5120
CV v74,v76,v77
MM v[88:91],a[196:199],v[188:191],v[88:91]
EX v78,v78
MM v[88:91],a[192:195],v[172:175],v[88:91]
TR a[172:173],v233 offset:9216
TR a[174:175],v233 offset:13312
MM v[96:99],a[224:227],v[160:163],v[128:131]
EX v79,v79
MM v[96:99],a[228:231],v[176:179],v[96:99]
TR a[176:177],v232 offset:1536
CV v75,v78,v79
MM v[96:99],a[232:235],v[192:195],v[96:99]
EX v80,v80
MM v[96:99],a[236:239],v[208:211],v[96:99]
TR a[178:179],v232 offset:5632
TR a[180:181],v232 offset:9728
MM v[100:103],a[252:255],v[208:211],v[128:131]
EX v81,v81
MM v[100:103],a[248:251],v[192:195],v[100:103]
TR a[182:183],v232 offset:13824
CV v80,v80,v81
MM v[100:103],a[244:247],v[176:179],v[100:103]
EX v82,v82
MM v[100:103],a[240:243],v[160:163],v[100:103]
EX v83,v83
MM v[108:111],a[240:243],v[164:167],v[132:135]
TR a[184:185],v233 offset:1536
CV v81,v82,v83
MM v[108:111],a[244:247],v[180:183],v[108:111]
EX v84,v84
MM v[108:111],a[248:251],v[196:199],v[108:111]
TR a[186:187],v233 offset:5632
TR a[188:189],v233 offset:9728
MM v[108:111],a[252:255],v[212:215],v[108:111]
EX v85,v85
MM v[104:107],a[236:239],v[212:215],v[132:135]
TR a[190:191],v233 offset:13824
CV v82,v84,v85
MM v[104:107],a[232:235],v[196:199],v[104:107]
EX v86,v86
MM v[104:107],a[228:231],v[180:183],v[104:107]
EX v87,v87
MM v[104:107],a[224:227],v[164:167],v[104:107]
CV v83,v86,v87
MM v[112:115],a[224:227],v[168:171],v[136:139]
EX v88,v88
MM v[112:115],a[228:231],v[184:187],v[112:115]
EX v89,v89
MM v[112:115],a[232:235],v[200:203],v[112:115]
EX v90,v90
MM v[112:115],a[236:239],v[216:219],v[112:115]
EX v91,v91
MM v[116:119],a[252:255],v[216:219],v[136:139]
EX v92,v92
MM v[116:119],a[248:251],v[200:203],v[116:119]
EX v93,v93
MM v[116:119],a[244:247],v[184:187],v[116:119]
EX v94,v94
MM v[116:119],a[240:243],v[168:171],v[116:119]
EX v95,v95
MM v[124:127],a[240:243],v[172:175],v[140:143]
EX v96,v96
MM v[124:127],a[244:247],v[188:191],v[124:127]
EX v97,v97
MM v[124:127],a[248:251],v[204:207],v[124:127]
EX v98,v98
MM v[124:127],a[252:255],v[220:223],v[124:127]
EX v99,v99
MM v[120:123],a[236:239],v[220:223],v[140:143]
EX v100,v100
MM v[120:123],a[232:235],v[204:207],v[120:123]
EX v101,v101
MM v[120:123],a[228:231],v[188:191],v[120:123]
EX v102,v102
MM v[120:123],a[224:227],v[172:175],v[120:123]
s_waitcnt vmcnt(13) lgkmcnt(0)
s_barrier
MM a[0:3],a[128:131],v[0:3],a[0:3]
s_add_u32 m0, s65, 0
s_add_u32 m0, s94, 139264
s_add_u32 s66, s86, 0
buffer_load_dwordx4 v241, s[24:27], s66 offen offset:0 lds
MM a[0:3],a[132:135],v[4:7],a[0:3]
CV v88,v88,v89
CV v89,v90,v91
MM a[8:11],a[132:135],v[12:15],a[8:11]
RD a[192:195],v224 offset:32768
CV v90,v92,v93
MM a[8:11],a[128:131],v[8:11],a[8:11]
RD a[196:199],v225 offset:32768
CV v91,v94,v95
MM a[16:19],a[128:131],v[16:19],a[16:19]
s_add_u32 m0, s65, 0
s_nop 0
buffer_load_dwordx4 v228, s[8:11], s64 offen offset:0 lds
MM a[16:19],a[132:135],v[20:23],a[16:19]
RD a[200:203],v226 offset:32768
CV v68,v96,v97
MM a[24:27],a[132:135],v[28:31],a[24:27]
RD a[204:207],v227 offset:32768
CV v69,v98,v99
MM a[24:27],a[128:131],v[24:27],a[24:27]
buffer_load_dwordx4 v229, s[8:11], s64 offen offset:1024 lds
MM a[28:31],a[136:139],v[24:27],a[28:31]
s_add_u32 m0, s94, 143360
s_add_u32 s66, s86, 81920
buffer_load_dwordx4 v241, s[24:27], s66 offen offset:0 lds
MM a[28:31],a[140:143],v[28:31],a[28:31]
RD a[208:211],v224 offset:36864
CV v70,v100,v101
MM a[20:23],a[140:143],v[20:23],a[20:23]
RD a[212:215],v225 offset:36864
RD a[216:219],v226 offset:36864
MM a[20:23],a[136:139],v[16:19],a[20:23]
s_add_u32 m0, s65, 0
s_nop 0
buffer_load_dwordx4 v230, s[8:11], s64 offen offset:2048 lds
MM a[12:15],a[136:139],v[8:11],a[12:15]
RD a[220:223],v227 offset:36864
RD a[224:227],v224 offset:40960
MM a[12:15],a[140:143],v[12:15],a[12:15]
EX v103,v103
MM a[4:7],a[140:143],v[4:7],a[4:7]
RD a[228:231],v225 offset:40960
CV v71,v102,v103
MM a[4:7],a[136:139],v[0:3],a[4:7]
buffer_load_dwordx4 v231, s[8:11], s64 offen offset:3072 lds
MM a[32:35],a[144:147],v[0:3],a[32:35]
s_add_u32 m0, s65, 65536
EX v104,v104
MM a[32:35],a[148:151],v[4:7],a[32:35]
EX v105,v105
MM a[40:43],a[148:151],v[12:15],a[40:43]
RD a[232:235],v226 offset:40960
CV v76,v104,v105
MM a[40:43],a[144:147],v[8:11],a[40:43]
EX v106,v106
MM a[48:51],a[144:147],v[16:19],a[48:51]
buffer_load_dwordx4 v235, s[12:15], s81 offen offset:0 lds
MM a[48:51],a[148:151],v[20:23],a[48:51]
EX v107,v107
MM a[56:59],a[148:151],v[28:31],a[56:59]
RD a[236:239],v227 offset:40960
CV v77,v106,v107
MM a[56:59],a[144:147],v[24:27],a[56:59]
EX v108,v108
MM a[60:63],a[152:155],v[24:27],a[60:63]
EX v109,v109
MM a[60:63],a[156:159],v[28:31],a[60:63]
RD a[240:243],v224 offset:45056
CV v78,v108,v109
MM a[52:55],a[156:159],v[20:23],a[52:55]
EX v110,v110
MM a[52:55],a[152:155],v[16:19],a[52:55]
buffer_load_dwordx4 v235, s[12:15], s64 offen offset:1024 lds
MM a[44:47],a[152:155],v[8:11],a[44:47]
EX v111,v111
MM a[44:47],a[156:159],v[12:15],a[44:47]
RD a[244:247],v225 offset:45056
CV v79,v110,v111
MM a[36:39],a[156:159],v[4:7],a[36:39]
EX v112,v112
MM a[36:39],a[152:155],v[0:3],a[36:39]
EX v113,v113
MM a[64:67],a[160:163],v[0:3],a[64:67]
RD a[248:251],v226 offset:45056
CV v84,v112,v113
MM a[64:67],a[164:167],v[4:7],a[64:67]
EX v114,v114
MM a[72:75],a[164:167],v[12:15],a[72:75]
buffer_load_dwordx4 v235, s[12:15], s81 offen offset:2048 lds
MM a[72:75],a[160:163],v[8:11],a[72:75]
EX v115,v115
MM a[80:83],a[160:163],v[16:19],a[80:83]
RD a[252:255],v227 offset:45056
CV v85,v114,v115
MM a[80:83],a[164:167],v[20:23],a[80:83]
EX v116,v116
MM a[88:91],a[164:167],v[28:31],a[88:91]
EX v117,v117
MM a[88:91],a[160:163],v[24:27],a[88:91]
buffer_load_dwordx4 v235, s[12:15], s64 offen offset:3072 lds
MM a[92:95],a[168:171],v[24:27],a[92:95]
EX v118,v118
MM a[92:95],a[172:175],v[28:31],a[92:95]
EX v119,v119
MM a[84:87],a[172:175],v[20:23],a[84:87]
)ASM"
R"ASM(
EX v120,v120
MM a[84:87],a[168:171],v[16:19],a[84:87]
EX v121,v121
MM a[76:79],a[168:171],v[8:11],a[76:79]
EX v122,v122
MM a[76:79],a[172:175],v[12:15],a[76:79]
EX v123,v123
MM a[68:71],a[172:175],v[4:7],a[68:71]
EX v124,v124
MM a[68:71],a[168:171],v[0:3],a[68:71]
EX v125,v125
MM a[96:99],a[176:179],v[0:3],a[96:99]
EX v126,v126
MM a[96:99],a[180:183],v[4:7],a[96:99]
EX v127,v127
MM a[104:107],a[180:183],v[12:15],a[104:107]
CV v86,v116,v117
CV v87,v118,v119
MM a[104:107],a[176:179],v[8:11],a[104:107]
CV v92,v120,v121
CV v93,v122,v123
MM a[112:115],a[176:179],v[16:19],a[112:115]
CV v94,v124,v125
CV v95,v126,v127
MM a[112:115],a[180:183],v[20:23],a[112:115]
MM a[120:123],a[180:183],v[28:31],a[120:123]
MM a[120:123],a[176:179],v[24:27],a[120:123]
MM a[124:127],a[184:187],v[24:27],a[124:127]
MM a[124:127],a[188:191],v[28:31],a[124:127]
MM a[116:119],a[188:191],v[20:23],a[116:119]
MM a[116:119],a[184:187],v[16:19],a[116:119]
MM a[108:111],a[184:187],v[8:11],a[108:111]
MM a[108:111],a[188:191],v[12:15],a[108:111]
MM a[100:103],a[188:191],v[4:7],a[100:103]
MM a[100:103],a[184:187],v[0:3],a[100:103]
v_smfmac_f32_16x16x64_bf16 v[236:239], v[144:147], v[64:71], v234
v_smfmac_f32_16x16x64_bf16 v[236:239], v[148:151], v[72:79], v234
v_smfmac_f32_16x16x64_bf16 v[236:239], v[152:155], v[80:87], v234
v_smfmac_f32_16x16x64_bf16 v[236:239], v[156:159], v[88:95], v234
s_waitcnt lgkmcnt(0)
MM v[0:3],a[192:195],v[160:163],v[128:131]
s_add_u32 s64, s64, 16384
TR a[128:129],v232 offset:16384
s_add_u32 s81, s64, 896
MM v[0:3],a[196:199],v[176:179],v[0:3]
TR a[130:131],v232 offset:20480
TR a[132:133],v232 offset:24576
MM v[0:3],a[200:203],v[192:195],v[0:3]
TR a[134:135],v232 offset:28672
TR a[136:137],v233 offset:16384
MM v[0:3],a[204:207],v[208:211],v[0:3]
TR a[138:139],v233 offset:20480
v_add_f32 v247, v236, v237
MM v[4:7],a[220:223],v[208:211],v[128:131]
TR a[140:141],v233 offset:24576
v_cmp_nge_f32 vcc, s71, v247
MM v[4:7],a[216:219],v[192:195],v[4:7]
s_cbranch_vccnz LSITE29_%=
LBACK29_%=:
TR a[142:143],v233 offset:28672
TR a[144:145],v232 offset:16896
MM v[4:7],a[212:215],v[176:179],v[4:7]
s_add_u32 m0, s94, 151552
s_add_u32 s66, s87, 81920
buffer_load_dwordx4 v241, s[24:27], s66 offen offset:0 lds
MM v[4:7],a[208:211],v[160:163],v[4:7]
v_bfe_u32 v96, v248, 6, 2
v_mul_u32_u24 v96, 1280, v96
MM v[12:15],a[208:211],v[164:167],v[132:135]
v_bfe_u32 v97, v248, 8, 1
v_mul_u32_u24 v97, 40960, v97
MM v[12:15],a[212:215],v[180:183],v[12:15]
v_add_u32 v96, v96, v97
v_and_b32 v97, 48, v248
MM v[12:15],a[216:219],v[196:199],v[12:15]
v_mul_u32_u24 v97, 6, v97
v_add_u32 v96, v96, v97
MM v[12:15],a[220:223],v[212:215],v[12:15]
s_min_u32 s60, s18, s63
EX v0,v0
MM v[8:11],a[204:207],v[212:215],v[132:135]
s_add_u32 s61, s60, 131072
s_mov_b32 exec_hi, 0
buffer_load_dword v100, v96, s[32:35], s60 offen offset:0
s_mov_b64 exec, -1
MM v[8:11],a[200:203],v[196:199],v[8:11]
EX v1,v1
MM v[8:11],a[196:199],v[180:183],v[8:11]
s_add_u32 m0, s94, 155648
s_add_u32 s66, s88, 0
buffer_load_dwordx4 v241, s[24:27], s66 offen offset:0 lds
MM v[8:11],a[192:195],v[164:167],v[8:11]
s_mov_b32 exec_hi, 0
buffer_load_dword v101, v96, s[20:23], s61 offen offset:0 sc1
s_mov_b64 exec, -1
MM v[16:19],a[192:195],v[168:171],v[136:139]
EX v2,v2
MM v[16:19],a[196:199],v[184:187],v[16:19]
EX v3,v3
MM v[16:19],a[200:203],v[200:203],v[16:19]
EX v4,v4
MM v[16:19],a[204:207],v[216:219],v[16:19]
TR a[146:147],v232 offset:20992
CV v0,v0,v1
MM v[20:23],a[220:223],v[216:219],v[136:139]
LPG18_%=:
s_cbranch_execz LPG22_%=
EX v5,v5
MM v[20:23],a[216:219],v[200:203],v[20:23]
TR a[148:149],v232 offset:25088
CV v1,v2,v3
MM v[20:23],a[212:215],v[184:187],v[20:23]
s_add_u32 m0, s94, 159744
s_add_u32 s66, s88, 81920
buffer_load_dwordx4 v241, s[24:27], s66 offen offset:0 lds
MM v[20:23],a[208:211],v[168:171],v[20:23]
EX v6,v6
MM v[28:31],a[208:211],v[172:175],v[140:143]
EX v7,v7
MM v[28:31],a[212:215],v[188:191],v[28:31]
TR a[150:151],v232 offset:29184
TR a[152:153],v233 offset:16896
MM v[28:31],a[216:219],v[204:207],v[28:31]
EX v8,v8
MM v[28:31],a[220:223],v[220:223],v[28:31]
EX v9,v9
MM v[24:27],a[204:207],v[220:223],v[140:143]
TR a[154:155],v233 offset:20992
CV v2,v4,v5
MM v[24:27],a[200:203],v[204:207],v[24:27]
EX v10,v10
MM v[24:27],a[196:199],v[188:191],v[24:27]
TR a[156:157],v233 offset:25088
CV v3,v6,v7
MM v[24:27],a[192:195],v[172:175],v[24:27]
EX v11,v11
MM v[32:35],a[224:227],v[160:163],v[128:131]
TR a[158:159],v233 offset:29184
TR a[160:161],v232 offset:17408
MM v[32:35],a[228:231],v[176:179],v[32:35]
EX v12,v12
MM v[32:35],a[232:235],v[192:195],v[32:35]
TR a[162:163],v232 offset:21504
CV v8,v8,v9
MM v[32:35],a[236:239],v[208:211],v[32:35]
EX v13,v13
MM v[36:39],a[252:255],v[208:211],v[128:131]
TR a[164:165],v232 offset:25600
CV v9,v10,v11
MM v[36:39],a[248:251],v[192:195],v[36:39]
EX v14,v14
MM v[36:39],a[244:247],v[176:179],v[36:39]
EX v15,v15
MM v[36:39],a[240:243],v[160:163],v[36:39]
TR a[166:167],v232 offset:29696
TR a[168:169],v233 offset:17408
MM v[44:47],a[240:243],v[164:167],v[132:135]
EX v16,v16
MM v[44:47],a[244:247],v[180:183],v[44:47]
TR a[170:171],v233 offset:21504
CV v10,v12,v13
MM v[44:47],a[248:251],v[196:199],v[44:47]
EX v17,v17
MM v[44:47],a[252:255],v[212:215],v[44:47]
TR a[172:173],v233 offset:25600
CV v11,v14,v15
MM v[40:43],a[236:239],v[212:215],v[132:135]
EX v18,v18
MM v[40:43],a[232:235],v[196:199],v[40:43]
TR a[174:175],v233 offset:29696
TR a[176:177],v232 offset:17920
MM v[40:43],a[228:231],v[180:183],v[40:43]
EX v19,v19
MM v[40:43],a[224:227],v[164:167],v[40:43]
EX v20,v20
MM v[48:51],a[224:227],v[168:171],v[136:139]
TR a[178:179],v232 offset:22016
CV v16,v16,v17
MM v[48:51],a[228:231],v[184:187],v[48:51]
EX v21,v21
MM v[48:51],a[232:235],v[200:203],v[48:51]
TR a[180:181],v232 offset:26112
CV v17,v18,v19
MM v[48:51],a[236:239],v[216:219],v[48:51]
EX v22,v22
MM v[52:55],a[252:255],v[216:219],v[136:139]
TR a[182:183],v232 offset:30208
CV v18,v20,v21
MM v[52:55],a[248:251],v[200:203],v[52:55]
EX v23,v23
MM v[52:55],a[244:247],v[184:187],v[52:55]
TR a[184:185],v233 offset:17920
TR a[186:187],v233 offset:22016
MM v[52:55],a[240:243],v[168:171],v[52:55]
EX v24,v24
MM v[60:63],a[240:243],v[172:175],v[140:143]
EX v25,v25
MM v[60:63],a[244:247],v[188:191],v[60:63]
TR a[188:189],v233 offset:26112
CV v19,v22,v23
MM v[60:63],a[248:251],v[204:207],v[60:63]
TR a[190:191],v233 offset:30208
CV v24,v24,v25
MM v[60:63],a[252:255],v[220:223],v[60:63]
EX v26,v26
MM v[56:59],a[236:239],v[220:223],v[140:143]
EX v27,v27
MM v[56:59],a[232:235],v[204:207],v[56:59]
EX v28,v28
MM v[56:59],a[228:231],v[188:191],v[56:59]
EX v29,v29
MM v[56:59],a[224:227],v[172:175],v[56:59]
s_waitcnt vmcnt(19) lgkmcnt(0)
s_barrier
MM a[0:3],a[128:131],v[64:67],a[0:3]
CV v25,v26,v27
CV v26,v28,v29
MM a[0:3],a[132:135],v[68:71],a[0:3]
s_lshl_b32 s91, s47, 2
EX v30,v30
MM a[8:11],a[132:135],v[76:79],a[8:11]
s_add_u32 s91, s91, %[swave]
EX v31,v31
MM a[8:11],a[128:131],v[72:75],a[8:11]
s_lshl_b32 s91, s91, 2
RD a[192:195],v224 offset:49152
CV v27,v30,v31
MM a[16:19],a[128:131],v[80:83],a[16:19]
s_add_u32 s91, s91, 73728
s_add_u32 m0, s65, 16384
RD a[196:199],v225 offset:49152
MM a[16:19],a[132:135],v[84:87],a[16:19]
v_mov_b32 v250, s69
s_mov_b64 exec, 1
buffer_store_dword v250, off, s[20:23], s91 offset:0 sc1
s_mov_b64 exec, -1
MM a[24:27],a[132:135],v[92:95],a[24:27]
s_lshr_b32 s77, %[sb], 3
EX v32,v32
MM a[24:27],a[128:131],v[88:91],a[24:27]
s_lshl_b32 s77, s77, 2
EX v33,v33
MM a[28:31],a[136:139],v[88:91],a[28:31]
s_add_u32 s77, s77, %[swave]
buffer_load_dwordx4 v228, s[8:11], s64 offen offset:0 lds
MM a[28:31],a[140:143],v[92:95],a[28:31]
s_lshl_b32 s77, s77, 9
EX v34,v34
MM a[20:23],a[140:143],v[84:87],a[20:23]
s_add_u32 s77, s77, 0
v_lshrrev_b32 v253, 1, v248
v_add_u32 v253, s77, v253
MM a[20:23],a[136:139],v[80:83],a[20:23]
s_mov_b32 s77, LKEND_ka-LPD2_%=-16
v_min_u32 v253, s77, v253
CV v4,v32,v33
MM a[12:15],a[136:139],v[72:75],a[12:15]
s_getpc_b64 s[98:99]
LPD2_%=:
global_load_dword v253, v253, s[98:99]
MM a[12:15],a[140:143],v[76:79],a[12:15]
EX v35,v35
MM a[4:7],a[140:143],v[68:71],a[4:7]
EX v36,v36
MM a[4:7],a[136:139],v[64:67],a[4:7]
EX v37,v37
MM a[32:35],a[144:147],v[64:67],a[32:35]
RD a[200:203],v226 offset:49152
CV v5,v34,v35
MM a[32:35],a[148:151],v[68:71],a[32:35]
EX v38,v38
MM a[40:43],a[148:151],v[76:79],a[40:43]
buffer_load_dwordx4 v229, s[8:11], s64 offen offset:1024 lds
MM a[40:43],a[144:147],v[72:75],a[40:43]
EX v39,v39
MM a[48:51],a[144:147],v[80:83],a[48:51]
RD a[204:207],v227 offset:49152
CV v6,v36,v37
MM a[48:51],a[148:151],v[84:87],a[48:51]
EX v40,v40
MM a[56:59],a[148:151],v[92:95],a[56:59]
EX v41,v41
MM a[56:59],a[144:147],v[88:91],a[56:59]
RD a[208:211],v224 offset:53248
CV v7,v38,v39
MM a[60:63],a[152:155],v[88:91],a[60:63]
EX v42,v42
MM a[60:63],a[156:159],v[92:95],a[60:63]
buffer_load_dwordx4 v230, s[8:11], s64 offen offset:2048 lds
MM a[52:55],a[156:159],v[84:87],a[52:55]
EX v43,v43
MM a[52:55],a[152:155],v[80:83],a[52:55]
RD a[212:215],v225 offset:53248
CV v12,v40,v41
MM a[44:47],a[152:155],v[72:75],a[44:47]
EX v44,v44
MM a[44:47],a[156:159],v[76:79],a[44:47]
EX v45,v45
MM a[36:39],a[156:159],v[68:71],a[36:39]
RD a[216:219],v226 offset:53248
CV v13,v42,v43
MM a[36:39],a[152:155],v[64:67],a[36:39]
EX v46,v46
MM a[64:67],a[160:163],v[64:67],a[64:67]
buffer_load_dwordx4 v231, s[8:11], s64 offen offset:3072 lds
MM a[64:67],a[164:167],v[68:71],a[64:67]
s_add_u32 m0, s65, 81920
EX v47,v47
MM a[72:75],a[164:167],v[76:79],a[72:75]
RD a[220:223],v227 offset:53248
CV v14,v44,v45
MM a[72:75],a[160:163],v[72:75],a[72:75]
EX v48,v48
MM a[80:83],a[160:163],v[80:83],a[80:83]
RD a[224:227],v224 offset:57344
CV v15,v46,v47
MM a[80:83],a[164:167],v[84:87],a[80:83]
EX v49,v49
MM a[88:91],a[164:167],v[92:95],a[88:91]
EX v50,v50
MM a[88:91],a[160:163],v[88:91],a[88:91]
buffer_load_dwordx4 v235, s[12:15], s81 offen offset:0 lds
MM a[92:95],a[168:171],v[88:91],a[92:95]
EX v51,v51
MM a[92:95],a[172:175],v[92:95],a[92:95]
RD a[228:231],v225 offset:57344
CV v20,v48,v49
MM a[84:87],a[172:175],v[84:87],a[84:87]
EX v52,v52
MM a[84:87],a[168:171],v[80:83],a[84:87]
RD a[232:235],v226 offset:57344
CV v21,v50,v51
MM a[76:79],a[168:171],v[72:75],a[76:79]
EX v53,v53
MM a[76:79],a[172:175],v[76:79],a[76:79]
RD a[236:239],v227 offset:57344
CV v22,v52,v53
MM a[68:71],a[172:175],v[68:71],a[68:71]
EX v54,v54
MM a[68:71],a[168:171],v[64:67],a[68:71]
buffer_load_dwordx4 v235, s[12:15], s64 offen offset:1024 lds
MM a[96:99],a[176:179],v[64:67],a[96:99]
EX v55,v55
MM a[96:99],a[180:183],v[68:71],a[96:99]
EX v56,v56
MM a[104:107],a[180:183],v[76:79],a[104:107]
RD a[240:243],v224 offset:61440
CV v23,v54,v55
MM a[104:107],a[176:179],v[72:75],a[104:107]
EX v57,v57
MM a[112:115],a[176:179],v[80:83],a[112:115]
RD a[244:247],v225 offset:61440
CV v28,v56,v57
MM a[112:115],a[180:183],v[84:87],a[112:115]
EX v58,v58
MM a[120:123],a[180:183],v[92:95],a[120:123]
s_waitcnt vmcnt(9)
buffer_load_dwordx4 v235, s[12:15], s81 offen offset:2048 lds
MM a[120:123],a[176:179],v[88:91],a[120:123]
EX v59,v59
MM a[124:127],a[184:187],v[88:91],a[124:127]
RD a[248:251],v226 offset:61440
CV v29,v58,v59
MM a[124:127],a[188:191],v[92:95],a[124:127]
EX v60,v60
MM a[116:119],a[188:191],v[84:87],a[116:119]
EX v61,v61
MM a[116:119],a[184:187],v[80:83],a[116:119]
RD a[252:255],v227 offset:61440
CV v30,v60,v61
MM a[108:111],a[184:187],v[72:75],a[108:111]
EX v62,v62
MM a[108:111],a[188:191],v[76:79],a[108:111]
buffer_load_dwordx4 v235, s[12:15], s64 offen offset:3072 lds
MM a[100:103],a[188:191],v[68:71],a[100:103]
EX v63,v63
MM a[100:103],a[184:187],v[64:67],a[100:103]
CV v31,v62,v63
v_smfmac_f32_16x16x64_bf16 v[236:239], v[144:147], v[0:7], v234
v_smfmac_f32_16x16x64_bf16 v[236:239], v[148:151], v[8:15], v234
v_smfmac_f32_16x16x64_bf16 v[236:239], v[152:155], v[16:23], v234
v_smfmac_f32_16x16x64_bf16 v[236:239], v[156:159], v[24:31], v234
s_waitcnt lgkmcnt(0)
MM v[64:67],a[192:195],v[160:163],v[128:131]
s_add_u32 s64, s64, 16384
TR a[128:129],v232 offset:32768
s_add_u32 s81, s64, 896
MM v[64:67],a[196:199],v[176:179],v[64:67]
)ASM"
R"ASM(
TR a[130:131],v232 offset:36864
TR a[132:133],v232 offset:40960
MM v[64:67],a[200:203],v[192:195],v[64:67]
TR a[134:135],v232 offset:45056
TR a[136:137],v233 offset:32768
MM v[64:67],a[204:207],v[208:211],v[64:67]
TR a[138:139],v233 offset:36864
v_add_f32 v246, v236, v237
MM v[68:71],a[220:223],v[208:211],v[128:131]
TR a[140:141],v233 offset:40960
v_cmp_nge_f32 vcc, s71, v246
MM v[68:71],a[216:219],v[192:195],v[68:71]
s_cbranch_vccnz LSITE30_%=
LBACK30_%=:
TR a[142:143],v233 offset:45056
TR a[144:145],v232 offset:33280
MM v[68:71],a[212:215],v[176:179],v[68:71]
TR a[146:147],v232 offset:37376
TR a[148:149],v232 offset:41472
MM v[68:71],a[208:211],v[160:163],v[68:71]
TR a[150:151],v232 offset:45568
TR a[152:153],v233 offset:33280
MM v[76:79],a[208:211],v[164:167],v[132:135]
EX v64,v64
MM v[76:79],a[212:215],v[180:183],v[76:79]
EX v65,v65
MM v[76:79],a[216:219],v[196:199],v[76:79]
TR a[154:155],v233 offset:37376
CV v64,v64,v65
MM v[76:79],a[220:223],v[212:215],v[76:79]
EX v66,v66
MM v[72:75],a[204:207],v[212:215],v[132:135]
EX v67,v67
MM v[72:75],a[200:203],v[196:199],v[72:75]
TR a[156:157],v233 offset:41472
CV v65,v66,v67
MM v[72:75],a[196:199],v[180:183],v[72:75]
EX v68,v68
MM v[72:75],a[192:195],v[164:167],v[72:75]
EX v69,v69
MM v[80:83],a[192:195],v[168:171],v[136:139]
TR a[158:159],v233 offset:45568
CV v66,v68,v69
MM v[80:83],a[196:199],v[184:187],v[80:83]
EX v70,v70
MM v[80:83],a[200:203],v[200:203],v[80:83]
EX v71,v71
MM v[80:83],a[204:207],v[216:219],v[80:83]
TR a[160:161],v232 offset:33792
CV v67,v70,v71
MM v[84:87],a[220:223],v[216:219],v[136:139]
EX v72,v72
MM v[84:87],a[216:219],v[200:203],v[84:87]
EX v73,v73
MM v[84:87],a[212:215],v[184:187],v[84:87]
TR a[162:163],v232 offset:37888
CV v72,v72,v73
MM v[84:87],a[208:211],v[168:171],v[84:87]
EX v74,v74
MM v[92:95],a[208:211],v[172:175],v[140:143]
TR a[164:165],v232 offset:41984
TR a[166:167],v232 offset:46080
MM v[92:95],a[212:215],v[188:191],v[92:95]
EX v75,v75
MM v[92:95],a[216:219],v[204:207],v[92:95]
TR a[168:169],v233 offset:33792
CV v73,v74,v75
MM v[92:95],a[220:223],v[220:223],v[92:95]
EX v76,v76
MM v[88:91],a[204:207],v[220:223],v[140:143]
EX v77,v77
MM v[88:91],a[200:203],v[204:207],v[88:91]
TR a[170:171],v233 offset:37888
CV v74,v76,v77
MM v[88:91],a[196:199],v[188:191],v[88:91]
EX v78,v78
MM v[88:91],a[192:195],v[172:175],v[88:91]
TR a[172:173],v233 offset:41984
TR a[174:175],v233 offset:46080
MM v[96:99],a[224:227],v[160:163],v[128:131]
EX v79,v79
MM v[96:99],a[228:231],v[176:179],v[96:99]
TR a[176:177],v232 offset:34304
CV v75,v78,v79
MM v[96:99],a[232:235],v[192:195],v[96:99]
EX v80,v80
MM v[96:99],a[236:239],v[208:211],v[96:99]
TR a[178:179],v232 offset:38400
TR a[180:181],v232 offset:42496
MM v[100:103],a[252:255],v[208:211],v[128:131]
EX v81,v81
MM v[100:103],a[248:251],v[192:195],v[100:103]
TR a[182:183],v232 offset:46592
CV v80,v80,v81
MM v[100:103],a[244:247],v[176:179],v[100:103]
EX v82,v82
MM v[100:103],a[240:243],v[160:163],v[100:103]
EX v83,v83
MM v[108:111],a[240:243],v[164:167],v[132:135]
TR a[184:185],v233 offset:34304
CV v81,v82,v83
MM v[108:111],a[244:247],v[180:183],v[108:111]
EX v84,v84
MM v[108:111],a[248:251],v[196:199],v[108:111]
TR a[186:187],v233 offset:38400
TR a[188:189],v233 offset:42496
MM v[108:111],a[252:255],v[212:215],v[108:111]
EX v85,v85
MM v[104:107],a[236:239],v[212:215],v[132:135]
TR a[190:191],v233 offset:46592
CV v82,v84,v85
MM v[104:107],a[232:235],v[196:199],v[104:107]
EX v86,v86
MM v[104:107],a[228:231],v[180:183],v[104:107]
EX v87,v87
MM v[104:107],a[224:227],v[164:167],v[104:107]
CV v83,v86,v87
MM v[112:115],a[224:227],v[168:171],v[136:139]
EX v88,v88
MM v[112:115],a[228:231],v[184:187],v[112:115]
EX v89,v89
MM v[112:115],a[232:235],v[200:203],v[112:115]
EX v90,v90
MM v[112:115],a[236:239],v[216:219],v[112:115]
EX v91,v91
MM v[116:119],a[252:255],v[216:219],v[136:139]
EX v92,v92
MM v[116:119],a[248:251],v[200:203],v[116:119]
EX v93,v93
MM v[116:119],a[244:247],v[184:187],v[116:119]
EX v94,v94
MM v[116:119],a[240:243],v[168:171],v[116:119]
EX v95,v95
MM v[124:127],a[240:243],v[172:175],v[140:143]
EX v96,v96
MM v[124:127],a[244:247],v[188:191],v[124:127]
EX v97,v97
MM v[124:127],a[248:251],v[204:207],v[124:127]
EX v98,v98
MM v[124:127],a[252:255],v[220:223],v[124:127]
EX v99,v99
MM v[120:123],a[236:239],v[220:223],v[140:143]
EX v100,v100
MM v[120:123],a[232:235],v[204:207],v[120:123]
EX v101,v101
MM v[120:123],a[228:231],v[188:191],v[120:123]
EX v102,v102
MM v[120:123],a[224:227],v[172:175],v[120:123]
s_waitcnt lgkmcnt(0)
s_barrier
MM a[0:3],a[128:131],v[0:3],a[0:3]
s_add_u32 m0, s65, 32768
CV v88,v88,v89
CV v89,v90,v91
MM a[0:3],a[132:135],v[4:7],a[0:3]
RD a[192:195],v224 offset:0
CV v90,v92,v93
MM a[8:11],a[132:135],v[12:15],a[8:11]
RD a[196:199],v225 offset:0
CV v91,v94,v95
MM a[8:11],a[128:131],v[8:11],a[8:11]
buffer_load_dwordx4 v228, s[8:11], s64 offen offset:0 lds
MM a[16:19],a[128:131],v[16:19],a[16:19]
RD a[200:203],v226 offset:0
CV v68,v96,v97
MM a[16:19],a[132:135],v[20:23],a[16:19]
RD a[204:207],v227 offset:0
CV v69,v98,v99
MM a[24:27],a[132:135],v[28:31],a[24:27]
buffer_load_dwordx4 v229, s[8:11], s64 offen offset:1024 lds
MM a[24:27],a[128:131],v[24:27],a[24:27]
RD a[208:211],v224 offset:4096
CV v70,v100,v101
MM a[28:31],a[136:139],v[24:27],a[28:31]
RD a[212:215],v225 offset:4096
RD a[216:219],v226 offset:4096
MM a[28:31],a[140:143],v[28:31],a[28:31]
buffer_load_dwordx4 v230, s[8:11], s64 offen offset:2048 lds
MM a[20:23],a[140:143],v[20:23],a[20:23]
RD a[220:223],v227 offset:4096
RD a[224:227],v224 offset:8192
MM a[20:23],a[136:139],v[16:19],a[20:23]
buffer_load_dwordx4 v231, s[8:11], s64 offen offset:3072 lds
MM a[12:15],a[136:139],v[8:11],a[12:15]
s_add_u32 m0, s65, 98304
EX v103,v103
MM a[12:15],a[140:143],v[12:15],a[12:15]
RD a[228:231],v225 offset:8192
CV v71,v102,v103
MM a[4:7],a[140:143],v[4:7],a[4:7]
EX v104,v104
MM a[4:7],a[136:139],v[0:3],a[4:7]
buffer_load_dwordx4 v235, s[12:15], s81 offen offset:0 lds
MM a[32:35],a[144:147],v[0:3],a[32:35]
EX v105,v105
MM a[32:35],a[148:151],v[4:7],a[32:35]
RD a[232:235],v226 offset:8192
CV v76,v104,v105
MM a[40:43],a[148:151],v[12:15],a[40:43]
EX v106,v106
MM a[40:43],a[144:147],v[8:11],a[40:43]
EX v107,v107
MM a[48:51],a[144:147],v[16:19],a[48:51]
RD a[236:239],v227 offset:8192
CV v77,v106,v107
MM a[48:51],a[148:151],v[20:23],a[48:51]
EX v108,v108
MM a[56:59],a[148:151],v[28:31],a[56:59]
buffer_load_dwordx4 v235, s[12:15], s64 offen offset:1024 lds
MM a[56:59],a[144:147],v[24:27],a[56:59]
EX v109,v109
MM a[60:63],a[152:155],v[24:27],a[60:63]
RD a[240:243],v224 offset:12288
CV v78,v108,v109
MM a[60:63],a[156:159],v[28:31],a[60:63]
EX v110,v110
MM a[52:55],a[156:159],v[20:23],a[52:55]
EX v111,v111
MM a[52:55],a[152:155],v[16:19],a[52:55]
RD a[244:247],v225 offset:12288
CV v79,v110,v111
MM a[44:47],a[152:155],v[8:11],a[44:47]
EX v112,v112
MM a[44:47],a[156:159],v[12:15],a[44:47]
buffer_load_dwordx4 v235, s[12:15], s81 offen offset:2048 lds
MM a[36:39],a[156:159],v[4:7],a[36:39]
EX v113,v113
MM a[36:39],a[152:155],v[0:3],a[36:39]
RD a[248:251],v226 offset:12288
CV v84,v112,v113
MM a[64:67],a[160:163],v[0:3],a[64:67]
EX v114,v114
MM a[64:67],a[164:167],v[4:7],a[64:67]
EX v115,v115
MM a[72:75],a[164:167],v[12:15],a[72:75]
RD a[252:255],v227 offset:12288
CV v85,v114,v115
MM a[72:75],a[160:163],v[8:11],a[72:75]
EX v116,v116
MM a[80:83],a[160:163],v[16:19],a[80:83]
buffer_load_dwordx4 v235, s[12:15], s64 offen offset:3072 lds
MM a[80:83],a[164:167],v[20:23],a[80:83]
EX v117,v117
MM a[88:91],a[164:167],v[28:31],a[88:91]
EX v118,v118
MM a[88:91],a[160:163],v[24:27],a[88:91]
EX v119,v119
MM a[92:95],a[168:171],v[24:27],a[92:95]
EX v120,v120
MM a[92:95],a[172:175],v[28:31],a[92:95]
EX v121,v121
MM a[84:87],a[172:175],v[20:23],a[84:87]
EX v122,v122
MM a[84:87],a[168:171],v[16:19],a[84:87]
EX v123,v123
MM a[76:79],a[168:171],v[8:11],a[76:79]
EX v124,v124
MM a[76:79],a[172:175],v[12:15],a[76:79]
EX v125,v125
MM a[68:71],a[172:175],v[4:7],a[68:71]
EX v126,v126
MM a[68:71],a[168:171],v[0:3],a[68:71]
EX v127,v127
MM a[96:99],a[176:179],v[0:3],a[96:99]
CV v86,v116,v117
CV v87,v118,v119
MM a[96:99],a[180:183],v[4:7],a[96:99]
CV v92,v120,v121
CV v93,v122,v123
MM a[104:107],a[180:183],v[12:15],a[104:107]
CV v94,v124,v125
CV v95,v126,v127
MM a[104:107],a[176:179],v[8:11],a[104:107]
MM a[112:115],a[176:179],v[16:19],a[112:115]
MM a[112:115],a[180:183],v[20:23],a[112:115]
MM a[120:123],a[180:183],v[28:31],a[120:123]
MM a[120:123],a[176:179],v[24:27],a[120:123]
MM a[124:127],a[184:187],v[24:27],a[124:127]
MM a[124:127],a[188:191],v[28:31],a[124:127]
MM a[116:119],a[188:191],v[20:23],a[116:119]
MM a[116:119],a[184:187],v[16:19],a[116:119]
MM a[108:111],a[184:187],v[8:11],a[108:111]
MM a[108:111],a[188:191],v[12:15],a[108:111]
MM a[100:103],a[188:191],v[4:7],a[100:103]
MM a[100:103],a[184:187],v[0:3],a[100:103]
v_smfmac_f32_16x16x64_bf16 v[236:239], v[144:147], v[64:71], v234
v_smfmac_f32_16x16x64_bf16 v[236:239], v[148:151], v[72:79], v234
v_smfmac_f32_16x16x64_bf16 v[236:239], v[152:155], v[80:87], v234
v_smfmac_f32_16x16x64_bf16 v[236:239], v[156:159], v[88:95], v234
s_waitcnt lgkmcnt(0)
MM v[16:19],a[192:195],v[168:171],v[136:139]
TR a[128:129],v232 offset:49152
TR a[130:131],v232 offset:53248
MM v[16:19],a[196:199],v[184:187],v[16:19]
TR a[132:133],v232 offset:57344
TR a[134:135],v232 offset:61440
MM v[16:19],a[200:203],v[200:203],v[16:19]
TR a[136:137],v233 offset:49152
TR a[138:139],v233 offset:53248
MM v[16:19],a[204:207],v[216:219],v[16:19]
TR a[140:141],v233 offset:57344
v_add_f32 v247, v236, v237
MM v[20:23],a[220:223],v[216:219],v[136:139]
TR a[142:143],v233 offset:61440
v_cmp_nge_f32 vcc, s71, v247
MM v[20:23],a[216:219],v[200:203],v[20:23]
s_cbranch_vccnz LSITE31_%=
LBACK31_%=:
TR a[144:145],v232 offset:49664
LPG19_%=:
s_cbranch_execz LPG23_%=
TR a[146:147],v232 offset:53760
MM v[20:23],a[212:215],v[184:187],v[20:23]
v_rcp_f32 v145, v247
MM v[20:23],a[208:211],v[168:171],v[20:23]
TR a[148:149],v232 offset:57856
TR a[150:151],v232 offset:61952
MM v[28:31],a[208:211],v[172:175],v[140:143]
EX v16,v16
MM v[28:31],a[212:215],v[188:191],v[28:31]
TR a[152:153],v233 offset:49664
TR a[154:155],v233 offset:53760
MM v[28:31],a[216:219],v[204:207],v[28:31]
EX v17,v17
MM v[28:31],a[220:223],v[220:223],v[28:31]
TR a[156:157],v233 offset:57856
CV v16,v16,v17
MM v[24:27],a[204:207],v[220:223],v[140:143]
TR a[158:159],v233 offset:61952
TR a[160:161],v232 offset:50176
MM v[24:27],a[200:203],v[204:207],v[24:27]
TR a[162:163],v232 offset:54272
TR a[164:165],v232 offset:58368
MM v[24:27],a[196:199],v[188:191],v[24:27]
EX v18,v18
MM v[24:27],a[192:195],v[172:175],v[24:27]
TR a[166:167],v232 offset:62464
TR a[168:169],v233 offset:50176
MM v[48:51],a[224:227],v[168:171],v[136:139]
EX v19,v19
MM v[48:51],a[228:231],v[184:187],v[48:51]
TR a[170:171],v233 offset:54272
CV v17,v18,v19
MM v[48:51],a[232:235],v[200:203],v[48:51]
TR a[172:173],v233 offset:58368
TR a[174:175],v233 offset:62464
MM v[48:51],a[236:239],v[216:219],v[48:51]
TR a[176:177],v232 offset:50688
TR a[178:179],v232 offset:54784
MM v[52:55],a[252:255],v[216:219],v[136:139]
EX v20,v20
MM v[52:55],a[248:251],v[200:203],v[52:55]
TR a[180:181],v232 offset:58880
TR a[182:183],v232 offset:62976
MM v[52:55],a[244:247],v[184:187],v[52:55]
EX v21,v21
MM v[52:55],a[240:243],v[168:171],v[52:55]
TR a[184:185],v233 offset:50688
TR a[186:187],v233 offset:54784
MM v[60:63],a[240:243],v[172:175],v[140:143]
TR a[188:189],v233 offset:58880
CV v18,v20,v21
MM v[60:63],a[244:247],v[188:191],v[60:63]
TR a[190:191],v233 offset:62976
v_mov_b32 v144, v145
MM v[60:63],a[248:251],v[204:207],v[60:63]
EX v22,v22
MM v[60:63],a[252:255],v[220:223],v[60:63]
EX v23,v23
MM v[56:59],a[236:239],v[220:223],v[140:143]
CV v19,v22,v23
v_permlane32_swap_b32 v145, v144
MM v[56:59],a[232:235],v[204:207],v[56:59]
EX v24,v24
MM v[56:59],a[228:231],v[188:191],v[56:59]
EX v25,v25
MM v[56:59],a[224:227],v[172:175],v[56:59]
s_waitcnt vmcnt(12) lgkmcnt(0)
s_barrier
CV v24,v24,v25
MM a[0:3],a[128:131],v[64:67],a[0:3]
s_add_u32 m0, s89, 49152
)ASM"
R"ASM(
RD a[192:195],v224 offset:16384
RD a[196:199],v225 offset:16384
MM a[0:3],a[132:135],v[68:71],a[0:3]
s_add_u32 s66, s85, 0
buffer_load_dwordx4 v240, s[24:27], s66 offen offset:0 lds
MM a[8:11],a[132:135],v[76:79],a[8:11]
s_add_u32 m0, s89, 50176
EX v26,v26
MM a[8:11],a[128:131],v[72:75],a[8:11]
s_add_u32 s66, s85, 10240
RD a[200:203],v226 offset:16384
RD a[204:207],v227 offset:16384
MM a[12:15],a[136:139],v[72:75],a[12:15]
EX v27,v27
MM a[12:15],a[140:143],v[76:79],a[12:15]
RD a[208:211],v224 offset:20480
CV v25,v26,v27
MM a[4:7],a[140:143],v[68:71],a[4:7]
buffer_load_dwordx4 v240, s[24:27], s66 offen offset:0 lds
MM a[4:7],a[136:139],v[64:67],a[4:7]
s_add_u32 m0, s89, 57344
EX v28,v28
MM a[32:35],a[144:147],v[64:67],a[32:35]
s_add_u32 s66, s85, 81920
EX v29,v29
MM a[32:35],a[148:151],v[68:71],a[32:35]
CV v26,v28,v29
v_mov_b32 v149, v145
MM a[40:43],a[148:151],v[76:79],a[40:43]
RD a[212:215],v225 offset:20480
v_permlane16_swap_b32 v145, v149
MM a[40:43],a[144:147],v[72:75],a[40:43]
s_mul_i32 s78, s49, 17408
s_add_u32 s78, s83, s78
s_add_u32 s78, s78, 0
MM a[44:47],a[152:155],v[72:75],a[44:47]
s_add_u32 s79, s78, 4096
AR v0,a0
AR v1,a1
MM a[44:47],a[156:159],v[76:79],a[44:47]
AR v2,a2
AR v3,a3
MM a[36:39],a[156:159],v[68:71],a[36:39]
AR v4,a4
AR v5,a5
MM a[36:39],a[152:155],v[64:67],a[36:39]
AR v6,a6
AR v7,a7
MM a[64:67],a[160:163],v[64:67],a[64:67]
EX v30,v30
MM a[64:67],a[164:167],v[68:71],a[64:67]
v_mul_f32 v0, v0, v145
v_mul_f32 v1, v1, v145
MM a[72:75],a[164:167],v[76:79],a[72:75]
v_mul_f32 v2, v2, v145
v_mul_f32 v3, v3, v145
MM a[72:75],a[160:163],v[72:75],a[72:75]
v_mul_f32 v4, v4, v145
v_mul_f32 v5, v5, v145
MM a[76:79],a[168:171],v[72:75],a[76:79]
v_mul_f32 v6, v6, v145
v_mul_f32 v7, v7, v145
MM a[76:79],a[172:175],v[76:79],a[76:79]
EX v31,v31
MM a[68:71],a[172:175],v[68:71],a[68:71]
CV v160,v0,v1
CV v161,v2,v3
MM a[68:71],a[168:171],v[64:67],a[68:71]
CV v162,v4,v5
CV v163,v6,v7
MM a[96:99],a[176:179],v[64:67],a[96:99]
buffer_store_dwordx4 v[160:163], v248, s[20:23], s78 offen offset:0 sc1
EX v48,v48
MM a[96:99],a[180:183],v[68:71],a[96:99]
AR v8,a8
AR v9,a9
MM a[104:107],a[180:183],v[76:79],a[104:107]
AR v10,a10
AR v11,a11
MM a[104:107],a[176:179],v[72:75],a[104:107]
AR v12,a12
AR v13,a13
EX v49,v49
MM a[108:111],a[184:187],v[72:75],a[108:111]
AR v14,a14
AR v15,a15
MM a[108:111],a[188:191],v[76:79],a[108:111]
v_mul_f32 v8, v8, v149
v_mul_f32 v9, v9, v149
EX v50,v50
MM a[100:103],a[188:191],v[68:71],a[100:103]
v_mul_f32 v10, v10, v149
v_mul_f32 v11, v11, v149
MM a[100:103],a[184:187],v[64:67],a[100:103]
buffer_load_dwordx4 v240, s[24:27], s66 offen offset:0 lds
v_mul_f32 v12, v12, v149
v_mul_f32 v13, v13, v149
MM a[16:19],a[128:131],v[80:83],a[16:19]
s_add_u32 m0, s89, 58368
v_mul_f32 v14, v14, v149
v_mul_f32 v15, v15, v149
EX v51,v51
MM a[16:19],a[132:135],v[84:87],a[16:19]
s_add_u32 s66, s85, 92160
CV v164,v8,v9
CV v27,v30,v31
MM a[24:27],a[132:135],v[92:95],a[24:27]
CV v165,v10,v11
CV v166,v12,v13
MM a[24:27],a[128:131],v[88:91],a[24:27]
RD a[216:219],v226 offset:20480
CV v167,v14,v15
EX v52,v52
MM a[28:31],a[136:139],v[88:91],a[28:31]
buffer_store_dwordx4 v[164:167], v248, s[20:23], s79 offen offset:0 sc1
AR v32,a32
MM a[28:31],a[140:143],v[92:95],a[28:31]
AR v33,a33
AR v34,a34
EX v53,v53
MM a[20:23],a[140:143],v[84:87],a[20:23]
AR v35,a35
AR v36,a36
MM a[20:23],a[136:139],v[80:83],a[20:23]
buffer_load_dwordx4 v240, s[24:27], s66 offen offset:0 lds
AR v37,a37
AR v38,a38
MM a[48:51],a[144:147],v[80:83],a[48:51]
s_add_u32 m0, s89, 114688
AR v39,a39
v_mul_f32 v32, v32, v145
EX v54,v54
MM a[48:51],a[148:151],v[84:87],a[48:51]
s_add_u32 s66, s86, 0
v_mul_f32 v33, v33, v145
CV v20,v48,v49
MM a[56:59],a[148:151],v[92:95],a[56:59]
RD a[220:223],v227 offset:20480
v_mul_f32 v34, v34, v145
v_mul_f32 v35, v35, v145
EX v55,v55
MM a[56:59],a[144:147],v[88:91],a[56:59]
v_mul_f32 v36, v36, v145
v_mul_f32 v37, v37, v145
MM a[60:63],a[152:155],v[88:91],a[60:63]
v_mul_f32 v38, v38, v145
v_mul_f32 v39, v39, v145
MM a[60:63],a[156:159],v[92:95],a[60:63]
CV v176,v32,v33
CV v177,v34,v35
EX v56,v56
MM a[52:55],a[156:159],v[84:87],a[52:55]
RD a[224:227],v224 offset:24576
buffer_load_dwordx4 v240, s[24:27], s66 offen offset:0 lds
CV v178,v36,v37
CV v179,v38,v39
CV v21,v50,v51
MM a[52:55],a[152:155],v[80:83],a[52:55]
s_add_u32 m0, s89, 115712
buffer_store_dwordx4 v[176:179], v248, s[20:23], s78 offen offset:1024 sc1
MM a[80:83],a[160:163],v[80:83],a[80:83]
s_add_u32 s66, s86, 10240
AR v40,a40
AR v41,a41
EX v57,v57
MM a[80:83],a[164:167],v[84:87],a[80:83]
AR v42,a42
AR v43,a43
MM a[88:91],a[164:167],v[92:95],a[88:91]
RD a[228:231],v225 offset:24576
AR v44,a44
AR v45,a45
MM a[88:91],a[160:163],v[88:91],a[88:91]
AR v46,a46
CV v22,v52,v53
EX v58,v58
MM a[92:95],a[168:171],v[88:91],a[92:95]
AR v47,a47
v_mul_f32 v40, v40, v149
MM a[92:95],a[172:175],v[92:95],a[92:95]
v_mul_f32 v41, v41, v149
v_mul_f32 v42, v42, v149
EX v59,v59
MM a[84:87],a[172:175],v[84:87],a[84:87]
RD a[232:235],v226 offset:24576
buffer_load_dwordx4 v240, s[24:27], s66 offen offset:0 lds
v_mul_f32 v43, v43, v149
v_mul_f32 v44, v44, v149
MM a[84:87],a[168:171],v[80:83],a[84:87]
s_add_u32 m0, s89, 122880
v_mul_f32 v45, v45, v149
v_mul_f32 v46, v46, v149
MM a[112:115],a[176:179],v[80:83],a[112:115]
s_add_u32 s66, s86, 81920
v_mul_f32 v47, v47, v149
CV v180,v40,v41
EX v60,v60
CV v23,v54,v55
MM a[112:115],a[180:183],v[84:87],a[112:115]
CV v181,v42,v43
CV v182,v44,v45
MM a[120:123],a[180:183],v[92:95],a[120:123]
RD a[236:239],v227 offset:24576
CV v183,v46,v47
EX v61,v61
MM a[120:123],a[176:179],v[88:91],a[120:123]
buffer_store_dwordx4 v[180:183], v248, s[20:23], s79 offen offset:1024 sc1
AR v0,a64
MM a[124:127],a[184:187],v[88:91],a[124:127]
AR v1,a65
AR v2,a66
MM a[124:127],a[188:191],v[92:95],a[124:127]
AR v3,a67
AR v4,a68
EX v62,v62
MM a[116:119],a[188:191],v[84:87],a[116:119]
RD a[240:243],v224 offset:28672
RD a[244:247],v225 offset:28672
RD a[248:251],v226 offset:28672
RD a[252:255],v227 offset:28672
buffer_load_dwordx4 v240, s[24:27], s66 offen offset:0 lds
EX v63,v63
AR v5,a69
CV v28,v56,v57
MM a[116:119],a[184:187],v[80:83],a[116:119]
s_add_u32 m0, s89, 123904
AR v6,a70
AR v7,a71
v_smfmac_f32_16x16x64_bf16 v[236:239], v[152:155], v[16:23], v234
s_add_u32 s66, s86, 92160
buffer_load_dwordx4 v240, s[24:27], s66 offen offset:0 lds
CV v29,v58,v59
CV v30,v60,v61
CV v31,v62,v63
v_mul_f32 v0, v0, v145
v_mul_f32 v1, v1, v145
v_smfmac_f32_16x16x64_bf16 v[236:239], v[156:159], v[24:31], v234
s_waitcnt lgkmcnt(0)
v_mul_f32 v2, v2, v145
v_mul_f32 v3, v3, v145
MM v[80:83],a[192:195],v[168:171],v[136:139]
v_mul_f32 v4, v4, v145
v_mul_f32 v5, v5, v145
MM v[80:83],a[196:199],v[184:187],v[80:83]
v_mul_f32 v6, v6, v145
v_mul_f32 v7, v7, v145
MM v[80:83],a[200:203],v[200:203],v[80:83]
CV v192,v0,v1
CV v193,v2,v3
MM v[80:83],a[204:207],v[216:219],v[80:83]
TR a[128:129],v232 offset:0
v_add_f32 v246, v236, v237
CV v194,v4,v5
CV v195,v6,v7
MM v[84:87],a[220:223],v[216:219],v[136:139]
TR a[130:131],v232 offset:4096
buffer_store_dwordx4 v[192:195], v248, s[20:23], s78 offen offset:2048 sc1
TR a[132:133],v232 offset:8192
v_cmp_nge_f32 vcc, s71, v246
MM v[84:87],a[216:219],v[200:203],v[84:87]
s_cbranch_vccnz LSITE32_%=
LBACK32_%=:
TR a[134:135],v232 offset:12288
AR v8,a72
AR v9,a73
MM v[84:87],a[212:215],v[184:187],v[84:87]
TR a[136:137],v233 offset:0
AR v10,a74
AR v11,a75
MM v[84:87],a[208:211],v[168:171],v[84:87]
TR a[138:139],v233 offset:4096
AR v12,a76
MM v[92:95],a[208:211],v[172:175],v[140:143]
TR a[140:141],v233 offset:8192
TR a[142:143],v233 offset:12288
AR v13,a77
AR v14,a78
MM v[92:95],a[212:215],v[188:191],v[92:95]
TR a[144:145],v232 offset:512
AR v15,a79
v_mul_f32 v8, v8, v149
MM v[92:95],a[216:219],v[204:207],v[92:95]
TR a[146:147],v232 offset:4608
v_mul_f32 v9, v9, v149
v_mul_f32 v10, v10, v149
MM v[92:95],a[220:223],v[220:223],v[92:95]
TR a[148:149],v232 offset:8704
v_mul_f32 v11, v11, v149
v_mul_f32 v12, v12, v149
MM v[88:91],a[204:207],v[220:223],v[140:143]
TR a[150:151],v232 offset:12800
v_mul_f32 v13, v13, v149
EX v80,v80
v_mul_f32 v14, v14, v149
MM v[88:91],a[200:203],v[204:207],v[88:91]
TR a[152:153],v233 offset:512
TR a[154:155],v233 offset:4608
v_mul_f32 v15, v15, v149
CV v196,v8,v9
EX v81,v81
MM v[88:91],a[196:199],v[188:191],v[88:91]
TR a[156:157],v233 offset:8704
CV v197,v10,v11
CV v198,v12,v13
MM v[88:91],a[192:195],v[172:175],v[88:91]
TR a[158:159],v233 offset:12800
CV v199,v14,v15
EX v82,v82
MM v[112:115],a[224:227],v[168:171],v[136:139]
buffer_store_dwordx4 v[196:199], v248, s[20:23], s79 offen offset:2048 sc1
TR a[160:161],v232 offset:1024
AR v32,a96
EX v83,v83
MM v[112:115],a[228:231],v[184:187],v[112:115]
TR a[162:163],v232 offset:5120
TR a[164:165],v232 offset:9216
AR v33,a97
AR v34,a98
MM v[112:115],a[232:235],v[200:203],v[112:115]
TR a[166:167],v232 offset:13312
AR v35,a99
EX v84,v84
MM v[112:115],a[236:239],v[216:219],v[112:115]
TR a[168:169],v233 offset:1024
AR v36,a100
AR v37,a101
MM v[116:119],a[252:255],v[216:219],v[136:139]
TR a[170:171],v233 offset:5120
AR v38,a102
EX v85,v85
AR v39,a103
MM v[116:119],a[248:251],v[200:203],v[116:119]
TR a[172:173],v233 offset:9216
TR a[174:175],v233 offset:13312
v_mul_f32 v32, v32, v145
v_mul_f32 v33, v33, v145
MM v[116:119],a[244:247],v[184:187],v[116:119]
TR a[176:177],v232 offset:1536
v_mul_f32 v34, v34, v145
v_mul_f32 v35, v35, v145
EX v86,v86
MM v[116:119],a[240:243],v[168:171],v[116:119]
TR a[178:179],v232 offset:5632
v_mul_f32 v36, v36, v145
v_mul_f32 v37, v37, v145
EX v87,v87
MM v[124:127],a[240:243],v[172:175],v[140:143]
TR a[180:181],v232 offset:9728
v_mul_f32 v38, v38, v145
v_mul_f32 v39, v39, v145
CV v80,v80,v81
MM v[124:127],a[244:247],v[188:191],v[124:127]
TR a[182:183],v232 offset:13824
CV v208,v32,v33
EX v88,v88
CV v209,v34,v35
MM v[124:127],a[248:251],v[204:207],v[124:127]
TR a[184:185],v233 offset:1536
TR a[186:187],v233 offset:5632
TR a[188:189],v233 offset:9728
TR a[190:191],v233 offset:13824
CV v210,v36,v37
CV v211,v38,v39
EX v89,v89
MM v[124:127],a[252:255],v[220:223],v[124:127]
buffer_store_dwordx4 v[208:211], v248, s[20:23], s78 offen offset:3072 sc1
CV v81,v82,v83
MM v[120:123],a[236:239],v[220:223],v[140:143]
AR v40,a104
AR v41,a105
EX v90,v90
MM v[120:123],a[232:235],v[204:207],v[120:123]
AR v42,a106
AR v43,a107
MM v[120:123],a[228:231],v[188:191],v[120:123]
EX v91,v91
AR v44,a108
CV v82,v84,v85
MM v[120:123],a[224:227],v[172:175],v[120:123]
s_waitcnt vmcnt(19) lgkmcnt(0)
s_barrier
AR v45,a109
AR v46,a110
EX v92,v92
MM a[16:19],a[128:131],v[16:19],a[16:19]
s_add_u32 m0, s89, 0
AR v47,a111
v_mul_f32 v40, v40, v149
MM a[16:19],a[132:135],v[20:23],a[16:19]
s_add_u32 s66, s87, 0
v_mul_f32 v41, v41, v149
v_mul_f32 v42, v42, v149
EX v93,v93
CV v83,v86,v87
MM a[24:27],a[132:135],v[28:31],a[24:27]
v_mul_f32 v43, v43, v149
v_mul_f32 v44, v44, v149
MM a[24:27],a[128:131],v[24:27],a[24:27]
v_mul_f32 v45, v45, v149
v_mul_f32 v46, v46, v149
EX v94,v94
MM a[28:31],a[136:139],v[24:27],a[28:31]
v_mul_f32 v47, v47, v149
CV v212,v40,v41
EX v95,v95
MM a[28:31],a[140:143],v[28:31],a[28:31]
CV v213,v42,v43
CV v88,v88,v89
CV v214,v44,v45
MM a[20:23],a[140:143],v[20:23],a[20:23]
CV v215,v46,v47
CV v89,v90,v91
EX v112,v112
MM a[20:23],a[136:139],v[16:19],a[20:23]
buffer_store_dwordx4 v[212:215], v248, s[20:23], s79 offen offset:3072 sc1
MM a[48:51],a[144:147],v[16:19],a[48:51]
buffer_load_dwordx4 v240, s[24:27], s66 offen offset:0 lds
RD a[192:195],v224 offset:32768
EX v113,v113
MM a[48:51],a[148:151],v[20:23],a[48:51]
s_add_u32 m0, s89, 1024
EX v114,v114
MM a[56:59],a[148:151],v[28:31],a[56:59]
s_add_u32 s66, s87, 10240
RD a[196:199],v225 offset:32768
CV v90,v92,v93
MM a[56:59],a[144:147],v[24:27],a[56:59]
buffer_load_dwordx4 v240, s[24:27], s66 offen offset:0 lds
RD a[200:203],v226 offset:32768
EX v115,v115
MM a[60:63],a[152:155],v[24:27],a[60:63]
s_add_u32 m0, s89, 8192
EX v116,v116
MM a[60:63],a[156:159],v[28:31],a[60:63]
s_add_u32 s66, s87, 81920
RD a[204:207],v227 offset:32768
CV v91,v94,v95
MM a[52:55],a[156:159],v[20:23],a[52:55]
EX v117,v117
MM a[52:55],a[152:155],v[16:19],a[52:55]
)ASM"
R"ASM(
RD a[208:211],v224 offset:36864
RD a[212:215],v225 offset:36864
buffer_load_dwordx4 v240, s[24:27], s66 offen offset:0 lds
MM a[80:83],a[160:163],v[16:19],a[80:83]
s_add_u32 m0, s89, 9216
EX v118,v118
MM a[80:83],a[164:167],v[20:23],a[80:83]
s_add_u32 s66, s87, 92160
RD a[216:219],v226 offset:36864
CV v84,v112,v113
MM a[88:91],a[164:167],v[28:31],a[88:91]
EX v119,v119
MM a[88:91],a[160:163],v[24:27],a[88:91]
buffer_load_dwordx4 v240, s[24:27], s66 offen offset:0 lds
EX v120,v120
MM a[92:95],a[168:171],v[24:27],a[92:95]
s_add_u32 m0, s89, 65536
RD a[220:223],v227 offset:36864
CV v85,v114,v115
MM a[92:95],a[172:175],v[28:31],a[92:95]
s_add_u32 s66, s88, 0
EX v121,v121
MM a[84:87],a[172:175],v[20:23],a[84:87]
RD a[224:227],v224 offset:40960
CV v86,v116,v117
MM a[84:87],a[168:171],v[16:19],a[84:87]
buffer_load_dwordx4 v240, s[24:27], s66 offen offset:0 lds
EX v122,v122
MM a[112:115],a[176:179],v[16:19],a[112:115]
s_add_u32 m0, s89, 66560
RD a[228:231],v225 offset:40960
EX v123,v123
MM a[112:115],a[180:183],v[20:23],a[112:115]
s_add_u32 s66, s88, 10240
RD a[232:235],v226 offset:40960
CV v87,v118,v119
MM a[120:123],a[180:183],v[28:31],a[120:123]
EX v124,v124
MM a[120:123],a[176:179],v[24:27],a[120:123]
buffer_load_dwordx4 v240, s[24:27], s66 offen offset:0 lds
MM a[124:127],a[184:187],v[24:27],a[124:127]
s_add_u32 m0, s89, 73728
RD a[236:239],v227 offset:40960
EX v125,v125
MM a[124:127],a[188:191],v[28:31],a[124:127]
s_add_u32 s66, s88, 81920
EX v126,v126
MM a[116:119],a[188:191],v[20:23],a[116:119]
RD a[240:243],v224 offset:45056
RD a[244:247],v225 offset:45056
RD a[248:251],v226 offset:45056
RD a[252:255],v227 offset:45056
EX v127,v127
MM a[116:119],a[184:187],v[16:19],a[116:119]
buffer_load_dwordx4 v240, s[24:27], s66 offen offset:0 lds
CV v92,v120,v121
v_smfmac_f32_16x16x64_bf16 v[236:239], v[152:155], v[80:87], v234
s_add_u32 m0, s89, 74752
CV v93,v122,v123
CV v94,v124,v125
CV v95,v126,v127
s_nop 1
v_smfmac_f32_16x16x64_bf16 v[236:239], v[156:159], v[88:95], v234
s_waitcnt lgkmcnt(0)
s_add_u32 s66, s88, 92160
buffer_load_dwordx4 v240, s[24:27], s66 offen offset:0 lds
v_lshrrev_b32 v230, 9, v248
v_lshlrev_b32 v230, 9, v230
v_sub_u32 v244, v232, v230
v_sub_u32 v235, v233, v230
v_sub_u32 v244, v244, v230
v_sub_u32 v235, v235, v230
v_sub_u32 v228, v244, v230
v_sub_u32 v229, v235, v230
v_add_u32 v228, 65536, v228
v_add_u32 v229, 65536, v229
v_subrev_u32 v230, 65536, v244
v_subrev_u32 v231, 65536, v235
MM v[16:19],a[192:195],v[168:171],v[136:139]
TR a[128:129],v232 offset:16384
s_nop 0
LPG20_%=:
s_cbranch_execz LPG24_%=
TR a[130:131],v232 offset:20480
MM v[16:19],a[196:199],v[184:187],v[16:19]
TR a[132:133],v232 offset:24576
TR a[134:135],v232 offset:28672
MM v[16:19],a[200:203],v[200:203],v[16:19]
TR a[136:137],v233 offset:16384
TR a[138:139],v233 offset:20480
MM v[16:19],a[204:207],v[216:219],v[16:19]
TR a[140:141],v233 offset:24576
v_add_f32 v247, v236, v237
MM v[20:23],a[220:223],v[216:219],v[136:139]
TR a[142:143],v233 offset:28672
v_cmp_nge_f32 vcc, s71, v247
MM v[20:23],a[216:219],v[200:203],v[20:23]
s_cbranch_vccnz LSITE33_%=
LBACK33_%=:
TR a[144:145],v232 offset:16896
TR a[146:147],v232 offset:20992
MM v[20:23],a[212:215],v[184:187],v[20:23]
TR a[148:149],v232 offset:25088
TR a[150:151],v232 offset:29184
MM v[20:23],a[208:211],v[168:171],v[20:23]
TR a[152:153],v233 offset:16896
TR a[154:155],v233 offset:20992
MM v[28:31],a[208:211],v[172:175],v[140:143]
EX v16,v16
MM v[28:31],a[212:215],v[188:191],v[28:31]
EX v17,v17
MM v[28:31],a[216:219],v[204:207],v[28:31]
s_lshl_b32 s97, s84, 2
s_add_u32 s97, s97, s96
s_add_u32 s97, s97, 0
MM v[28:31],a[220:223],v[220:223],v[28:31]
s_waitcnt vmcnt(8)
v_mov_b32 v242, s69
s_mov_b64 exec, 1
buffer_store_dword v242, off, s[20:23], s97 offset:0 sc1
s_mov_b64 exec, -1
MM v[24:27],a[204:207],v[220:223],v[140:143]
TR a[156:157],v233 offset:25088
TR a[158:159],v233 offset:29184
MM v[24:27],a[200:203],v[204:207],v[24:27]
EX v18,v18
MM v[24:27],a[196:199],v[188:191],v[24:27]
TR a[160:161],v232 offset:17408
TR a[162:163],v232 offset:21504
MM v[24:27],a[192:195],v[172:175],v[24:27]
EX v19,v19
MM v[48:51],a[224:227],v[168:171],v[136:139]
TR a[164:165],v232 offset:25600
TR a[166:167],v232 offset:29696
MM v[48:51],a[228:231],v[184:187],v[48:51]
EX v20,v20
MM v[48:51],a[232:235],v[200:203],v[48:51]
TR a[168:169],v233 offset:17408
TR a[170:171],v233 offset:21504
MM v[48:51],a[236:239],v[216:219],v[48:51]
EX v21,v21
MM v[52:55],a[252:255],v[216:219],v[136:139]
TR a[172:173],v233 offset:25600
CV v16,v16,v17
MM v[52:55],a[248:251],v[200:203],v[52:55]
TR a[174:175],v233 offset:29696
TR a[176:177],v232 offset:17920
MM v[52:55],a[244:247],v[184:187],v[52:55]
EX v22,v22
MM v[52:55],a[240:243],v[168:171],v[52:55]
TR a[178:179],v232 offset:22016
TR a[180:181],v232 offset:26112
EX v23,v23
MM v[60:63],a[240:243],v[172:175],v[140:143]
TR a[182:183],v232 offset:30208
CV v17,v18,v19
MM v[60:63],a[244:247],v[188:191],v[60:63]
EX v24,v24
MM v[60:63],a[248:251],v[204:207],v[60:63]
TR a[184:185],v233 offset:17920
TR a[186:187],v233 offset:22016
TR a[188:189],v233 offset:26112
TR a[190:191],v233 offset:30208
EX v25,v25
MM v[60:63],a[252:255],v[220:223],v[60:63]
EX v26,v26
MM v[56:59],a[236:239],v[220:223],v[140:143]
EX v27,v27
MM v[56:59],a[232:235],v[204:207],v[56:59]
EX v28,v28
MM v[56:59],a[228:231],v[188:191],v[56:59]
EX v29,v29
CV v18,v20,v21
MM v[56:59],a[224:227],v[172:175],v[56:59]
s_waitcnt lgkmcnt(0)
s_barrier
CV v19,v22,v23
MM a[16:19],a[128:131],v[80:83],a[16:19]
s_add_u32 m0, s89, 16384
EX v30,v30
MM a[16:19],a[132:135],v[84:87],a[16:19]
s_add_u32 s66, s85, 128
EX v31,v31
MM a[24:27],a[132:135],v[92:95],a[24:27]
buffer_load_dwordx4 v240, s[24:27], s66 offen offset:0 lds
MM a[24:27],a[128:131],v[88:91],a[24:27]
s_add_u32 m0, s89, 17408
EX v48,v48
MM a[28:31],a[136:139],v[88:91],a[28:31]
s_add_u32 s66, s85, 10368
CV v24,v24,v25
CV v25,v26,v27
MM a[28:31],a[140:143],v[92:95],a[28:31]
CV v26,v28,v29
CV v27,v30,v31
MM a[20:23],a[140:143],v[84:87],a[20:23]
EX v49,v49
MM a[20:23],a[136:139],v[80:83],a[20:23]
EX v50,v50
MM a[48:51],a[144:147],v[80:83],a[48:51]
buffer_load_dwordx4 v240, s[24:27], s66 offen offset:0 lds
MM a[48:51],a[148:151],v[84:87],a[48:51]
s_add_u32 m0, s89, 24576
EX v51,v51
MM a[56:59],a[148:151],v[92:95],a[56:59]
s_add_u32 s66, s85, 82048
TR a[128:129],v232 offset:32768
CV v20,v48,v49
MM a[56:59],a[144:147],v[88:91],a[56:59]
EX v52,v52
MM a[60:63],a[152:155],v[88:91],a[60:63]
TR a[130:131],v232 offset:36864
TR a[132:133],v232 offset:40960
MM a[60:63],a[156:159],v[92:95],a[60:63]
EX v53,v53
MM a[52:55],a[156:159],v[84:87],a[52:55]
TR a[134:135],v232 offset:45056
TR a[136:137],v233 offset:32768
MM a[52:55],a[152:155],v[80:83],a[52:55]
buffer_load_dwordx4 v240, s[24:27], s66 offen offset:0 lds
MM a[80:83],a[160:163],v[80:83],a[80:83]
s_add_u32 m0, s89, 25600
TR a[138:139],v233 offset:36864
TR a[140:141],v233 offset:40960
EX v54,v54
MM a[80:83],a[164:167],v[84:87],a[80:83]
s_add_u32 s66, s85, 92288
TR a[142:143],v233 offset:45056
TR a[144:145],v232 offset:33280
MM a[88:91],a[164:167],v[92:95],a[88:91]
EX v55,v55
MM a[88:91],a[160:163],v[88:91],a[88:91]
TR a[146:147],v232 offset:37376
TR a[148:149],v232 offset:41472
buffer_load_dwordx4 v240, s[24:27], s66 offen offset:0 lds
EX v56,v56
MM a[92:95],a[168:171],v[88:91],a[92:95]
s_add_u32 m0, s89, 81920
TR a[150:151],v232 offset:45568
CV v21,v50,v51
MM a[92:95],a[172:175],v[92:95],a[92:95]
s_add_u32 s66, s86, 128
TR a[152:153],v233 offset:33280
EX v57,v57
MM a[84:87],a[172:175],v[84:87],a[84:87]
TR a[154:155],v233 offset:37376
TR a[156:157],v233 offset:41472
MM a[84:87],a[168:171],v[80:83],a[84:87]
TR a[158:159],v233 offset:45568
buffer_load_dwordx4 v240, s[24:27], s66 offen offset:0 lds
EX v58,v58
MM a[112:115],a[176:179],v[80:83],a[112:115]
s_add_u32 m0, s89, 82944
TR a[160:161],v232 offset:33792
CV v22,v52,v53
EX v59,v59
MM a[112:115],a[180:183],v[84:87],a[112:115]
s_add_u32 s66, s86, 10368
TR a[162:163],v232 offset:37888
TR a[164:165],v232 offset:41984
MM a[120:123],a[180:183],v[92:95],a[120:123]
TR a[166:167],v232 offset:46080
CV v23,v54,v55
EX v60,v60
MM a[120:123],a[176:179],v[88:91],a[120:123]
TR a[168:169],v233 offset:33792
TR a[170:171],v233 offset:37888
buffer_load_dwordx4 v240, s[24:27], s66 offen offset:0 lds
MM a[124:127],a[184:187],v[88:91],a[124:127]
s_add_u32 m0, s89, 90112
TR a[172:173],v233 offset:41984
v_lshrrev_b32 v243, 2, v248
v_min_u32 v243, 12, v243
EX v61,v61
MM a[124:127],a[188:191],v[92:95],a[124:127]
s_add_u32 s66, s86, 82048
buffer_load_dword v242, v243, s[20:23], s96 offen offset:0 sc1
TR a[174:175],v233 offset:46080
EX v62,v62
MM a[116:119],a[188:191],v[84:87],a[116:119]
TR a[176:177],v232 offset:34304
TR a[178:179],v232 offset:38400
EX v63,v63
MM a[116:119],a[184:187],v[80:83],a[116:119]
TR a[180:181],v232 offset:42496
buffer_load_dwordx4 v240, s[24:27], s66 offen offset:0 lds
CV v28,v56,v57
v_smfmac_f32_16x16x64_bf16 v[236:239], v[152:155], v[16:23], v234
s_add_u32 m0, s89, 91136
TR a[182:183],v232 offset:46592
CV v29,v58,v59
CV v30,v60,v61
CV v31,v62,v63
s_nop 1
v_smfmac_f32_16x16x64_bf16 v[236:239], v[156:159], v[24:31], v234
TR a[184:185],v233 offset:34304
TR a[186:187],v233 offset:38400
TR a[188:189],v233 offset:42496
TR a[190:191],v233 offset:46592
s_waitcnt lgkmcnt(0)
s_add_u32 s66, s86, 92288
buffer_load_dwordx4 v240, s[24:27], s66 offen offset:0 lds
s_nop 6
v_add_f32 v246, v236, v237
v_cmp_nge_f32 vcc, s71, v246
s_cbranch_vccnz LSITE34_%=
LBACK34_%=:
s_waitcnt lgkmcnt(0)
MM a[16:19],a[128:131],v[16:19],a[16:19]
s_waitcnt vmcnt(10)
s_barrier
MM a[16:19],a[132:135],v[20:23],a[16:19]
v_rcp_f32 v152, v246
MM a[24:27],a[132:135],v[28:31],a[24:27]
v_mov_b32 v153, v152
MM a[24:27],a[128:131],v[24:27],a[24:27]
s_nop 0
v_permlane32_swap_b32 v152, v153
MM a[28:31],a[136:139],v[24:27],a[28:31]
v_mov_b32 v157, v153
MM a[28:31],a[140:143],v[28:31],a[28:31]
s_nop 0
v_permlane16_swap_b32 v153, v157
MM a[20:23],a[140:143],v[20:23],a[20:23]
s_mul_i32 s78, s49, 17408
s_add_u32 s78, s83, s78
s_add_u32 s78, s78, 8192
MM a[20:23],a[136:139],v[16:19],a[20:23]
s_add_u32 s79, s78, 4096
MM a[48:51],a[144:147],v[16:19],a[48:51]
TR a[128:129],v230 offset:49152
TR a[130:131],v230 offset:51200
MM a[48:51],a[148:151],v[20:23],a[48:51]
MM a[56:59],a[148:151],v[28:31],a[56:59]
TR a[132:133],v230 offset:53248
TR a[134:135],v230 offset:55296
MM a[56:59],a[144:147],v[24:27],a[56:59]
AR v80,a16
AR v81,a17
MM a[60:63],a[152:155],v[24:27],a[60:63]
TR a[136:137],v230 offset:57344
TR a[138:139],v230 offset:59392
MM a[60:63],a[156:159],v[28:31],a[60:63]
AR v82,a18
AR v83,a19
MM a[52:55],a[156:159],v[20:23],a[52:55]
TR a[140:141],v230 offset:61440
TR a[142:143],v230 offset:63488
MM a[52:55],a[152:155],v[16:19],a[52:55]
AR v84,a20
AR v85,a21
MM a[80:83],a[160:163],v[16:19],a[80:83]
TR a[144:145],v231 offset:49152
TR a[146:147],v231 offset:51200
MM a[80:83],a[164:167],v[20:23],a[80:83]
AR v86,a22
AR v87,a23
MM a[88:91],a[164:167],v[28:31],a[88:91]
TR a[148:149],v231 offset:53248
TR a[150:151],v231 offset:55296
MM a[88:91],a[160:163],v[24:27],a[88:91]
s_waitcnt vmcnt(2)
v_cmp_ne_u32 vcc, s69, v242
s_cbranch_vccnz LRPO0_%=
LRPO0b_%=:
v_mul_f32 v80, v80, v153
MM a[92:95],a[168:171],v[24:27],a[92:95]
s_xor_b32 s92, s49, 1
TR a[152:153],v231 offset:57344
TR a[154:155],v231 offset:59392
v_mul_f32 v81, v81, v153
v_mul_f32 v82, v82, v153
MM a[92:95],a[172:175],v[28:31],a[92:95]
s_mul_i32 s92, s92, 17408
s_add_u32 s92, s83, s92
v_mul_f32 v83, v83, v153
MM a[84:87],a[172:175],v[20:23],a[84:87]
TR a[156:157],v231 offset:61440
TR a[158:159],v231 offset:63488
v_mul_f32 v84, v84, v153
v_mul_f32 v85, v85, v153
MM a[84:87],a[168:171],v[16:19],a[84:87]
buffer_load_dwordx4 a[0:3], v248, s[20:23], s92 offen offset:0 sc1
v_mul_f32 v86, v86, v153
v_mul_f32 v87, v87, v153
MM a[112:115],a[176:179],v[16:19],a[112:115]
TR a[160:161],v230 offset:49664
TR a[162:163],v230 offset:51712
CV v168,v80,v81
MM a[112:115],a[180:183],v[20:23],a[112:115]
buffer_load_dwordx4 a[32:35], v248, s[20:23], s92 offen offset:1024 sc1
CV v169,v82,v83
)ASM"
R"ASM(
CV v170,v84,v85
MM a[120:123],a[180:183],v[28:31],a[120:123]
TR a[164:165],v230 offset:53760
TR a[166:167],v230 offset:55808
CV v171,v86,v87
MM a[120:123],a[176:179],v[24:27],a[120:123]
buffer_load_dwordx4 a[64:67], v248, s[20:23], s92 offen offset:2048 sc1
MM a[124:127],a[184:187],v[24:27],a[124:127]
TR a[168:169],v230 offset:57856
TR a[170:171],v230 offset:59904
MM a[124:127],a[188:191],v[28:31],a[124:127]
MM a[116:119],a[188:191],v[20:23],a[116:119]
buffer_store_dwordx4 v[168:171], v248, s[20:23], s78 offen offset:0 sc1
TR a[172:173],v230 offset:61952
TR a[174:175],v230 offset:64000
AR v88,a24
AR v89,a25
AR v90,a26
AR v91,a27
AR v92,a28
MM a[116:119],a[184:187],v[16:19],a[116:119]
AR v93,a29
AR v94,a30
s_waitcnt lgkmcnt(15)
MM v[0:3],a[128:131],v[160:163],0
TR a[176:177],v231 offset:49664
TR a[178:179],v231 offset:51712
AR v95,a31
MM v[0:3],a[132:135],v[176:179],v[0:3]
v_bfe_u32 v249, v248, 6, 2
v_lshlrev_b32 v249, 8, v249
v_mul_f32 v88, v88, v157
v_mul_f32 v89, v89, v157
MM v[0:3],a[136:139],v[192:195],v[0:3]
TR a[180:181],v231 offset:53760
TR a[182:183],v231 offset:55808
v_mul_f32 v90, v90, v157
v_mul_f32 v91, v91, v157
MM v[0:3],a[140:143],v[208:211],v[0:3]
buffer_load_dwordx4 a[96:99], v248, s[20:23], s92 offen offset:3072 sc1
v_lshrrev_b32 v250, 8, v248
v_lshl_add_u32 v249, v250, 6, v249
v_mul_f32 v92, v92, v157
MM v[8:11],a[140:143],v[212:215],0
s_add_u32 s92, s92, 4096
TR a[184:185],v231 offset:57856
TR a[186:187],v231 offset:59904
v_mul_f32 v93, v93, v157
v_mul_f32 v94, v94, v157
MM v[8:11],a[136:139],v[196:199],v[8:11]
v_and_b32 v251, 48, v248
v_add_u32 v249, v249, v251
v_mul_f32 v95, v95, v157
CV v172,v88,v89
MM v[8:11],a[132:135],v[180:183],v[8:11]
s_lshr_b32 s45, s49, 3
buffer_load_dwordx4 a[4:7], v248, s[20:23], s92 offen offset:0 sc1
TR a[188:189],v231 offset:61952
TR a[190:191],v231 offset:64000
CV v173,v90,v91
MM v[8:11],a[128:131],v[164:167],v[8:11]
s_waitcnt lgkmcnt(15)
v_bfe_u32 v245, v248, 6, 2
v_xor_b32 v251, s45, v245
CV v174,v92,v93
CV v175,v94,v95
MM v[4:7],a[144:147],v[160:163],0
buffer_store_dwordx4 v[172:175], v248, s[20:23], s79 offen offset:0 sc1
TR a[128:129],v228 offset:0
TR a[130:131],v228 offset:1024
MM v[4:7],a[148:151],v[176:179],v[4:7]
v_add_u32 v251, -1, v251
v_and_b32 v251, 0x40000000, v251
AR v112,a48
MM v[4:7],a[152:155],v[192:195],v[4:7]
TR a[132:133],v228 offset:2048
TR a[134:135],v228 offset:3072
AR v113,a49
AR v114,a50
MM v[4:7],a[156:159],v[208:211],v[4:7]
v_lshlrev_b32 v245, 6, v245
v_lshl_add_u32 v245, s45, 4, v245
AR v115,a51
MM v[12:15],a[156:159],v[212:215],0
TR a[136:137],v228 offset:4096
TR a[138:139],v228 offset:5120
AR v116,a52
AR v117,a53
MM v[12:15],a[152:155],v[196:199],v[12:15]
v_mov_b32 v234, v249
v_add_u32 v249, v249, v251
AR v118,a54
AR v119,a55
MM v[12:15],a[148:151],v[180:183],v[12:15]
buffer_load_dwordx4 a[36:39], v248, s[20:23], s92 offen offset:1024 sc1
TR a[140:141],v228 offset:6144
TR a[142:143],v228 offset:7168
v_mul_f32 v112, v112, v153
MM v[12:15],a[144:147],v[164:167],v[12:15]
s_waitcnt lgkmcnt(15)
v_bfe_u32 v251, v248, 4, 2
v_lshl_add_u32 v245, v251, 2, v245
v_mul_f32 v113, v113, v153
v_mul_f32 v114, v114, v153
MM v[32:35],a[160:163],v[160:163],0
s_lshl_b32 s47, %[sb], 10
TR a[144:145],v229 offset:0
TR a[146:147],v229 offset:1024
v_mul_f32 v115, v115, v153
v_mul_f32 v116, v116, v153
MM v[32:35],a[164:167],v[176:179],v[32:35]
s_and_b32 s47, s47, 1024
s_add_u32 s46, s95, s47
s_sub_u32 s47, s95, s47
v_mul_f32 v117, v117, v153
MM v[32:35],a[168:171],v[192:195],v[32:35]
TR a[148:149],v229 offset:2048
TR a[150:151],v229 offset:3072
v_mul_f32 v118, v118, v153
v_mul_f32 v119, v119, v153
MM v[32:35],a[172:175],v[208:211],v[32:35]
s_bitcmp1_b32 s49, 4
s_cselect_b32 s44, -1, 1
s_mul_i32 s44, s44, 331776
buffer_load_dwordx4 a[68:71], v248, s[20:23], s92 offen offset:2048 sc1
CV v184,v112,v113
MM v[40:43],a[172:175],v[212:215],0
s_add_u32 s44, s44, s16
TR a[152:153],v229 offset:4096
TR a[154:155],v229 offset:5120
CV v185,v114,v115
CV v186,v116,v117
MM v[40:43],a[168:171],v[196:199],v[40:43]
s_xor_b32 s45, s68, 64
buffer_load_dwordx4 a[100:103], v248, s[20:23], s92 offen offset:3072 sc1
CV v187,v118,v119
MM v[40:43],a[164:167],v[180:183],v[40:43]
s_bfe_u32 s80, s49, 131074
TR a[156:157],v229 offset:6144
TR a[158:159],v229 offset:7168
v_xor_b32 v250, s80, v250
v_mul_u32_u24 v250, 82944, v250
v_and_b32 v252, 240, v248
v_add_u32 v250, v250, v252
v_lshrrev_b32 v251, 8, v248
v_mul_u32_u24 v251, 1280, v251
v_bfe_u32 v252, v248, 4, 3
v_lshl_add_u32 v251, v252, 3, v251
v_bfe_u32 v252, v248, 7, 1
v_mul_u32_u24 v254, 40960, v252
v_add_u32 v254, v254, v251
v_lshl_add_u32 v251, v252, 6, v251
v_lshrrev_b32 v253, 8, v248
MM v[40:43],a[160:163],v[164:167],v[40:43]
s_waitcnt lgkmcnt(15)
v_and_b32 v252, 240, v248
v_lshl_add_u32 v232, v253, 2, v252
s_waitcnt vmcnt(10)
s_barrier
MM v[36:39],a[176:179],v[160:163],0
TR a[160:161],v230 offset:16384
TR a[162:163],v230 offset:18432
MM v[36:39],a[180:183],v[176:179],v[36:39]
buffer_store_dwordx4 v[184:187], v248, s[20:23], s78 offen offset:1024 sc1
AR v120,a56
AR v121,a57
AR v122,a58
AR v123,a59
AR v124,a60
v_xor_b32 v253, s80, v253
MM v[36:39],a[184:187],v[192:195],v[36:39]
TR a[164:165],v230 offset:20480
TR a[166:167],v230 offset:22528
AR v125,a61
AR v126,a62
MM v[36:39],a[188:191],v[208:211],v[36:39]
AR v127,a63
v_lshlrev_b32 v253, 4, v253
v_mul_f32 v120, v120, v157
MM v[44:47],a[188:191],v[212:215],0
TR a[168:169],v230 offset:24576
TR a[170:171],v230 offset:26624
v_mul_f32 v121, v121, v157
v_mov_b32 v233, s69
MM v[44:47],a[184:187],v[196:199],v[44:47]
s_add_u32 s80, s18, 131072
v_mul_f32 v122, v122, v157
v_mul_f32 v123, v123, v157
MM v[44:47],a[180:183],v[180:183],v[44:47]
buffer_load_dwordx2 v[224:225], v251, s[20:23], s80 offen offset:0 sc1
TR a[172:173],v230 offset:28672
TR a[174:175],v230 offset:30720
v_mul_f32 v124, v124, v157
MM v[44:47],a[176:179],v[164:167],v[44:47]
s_waitcnt lgkmcnt(15)
v_mul_f32 v125, v125, v157
v_mul_f32 v126, v126, v157
MM v[128:131],a[128:131],v[160:163],0
s_add_u32 s80, s18, 131200
TR a[176:177],v231 offset:16384
TR a[178:179],v231 offset:18432
v_mul_f32 v127, v127, v157
CV v188,v120,v121
MM v[128:131],a[132:135],v[176:179],v[128:131]
buffer_load_dwordx2 v[226:227], v251, s[20:23], s80 offen offset:0 sc1
CV v189,v122,v123
CV v190,v124,v125
MM v[128:131],a[136:139],v[192:195],v[128:131]
TR a[180:181],v231 offset:20480
TR a[182:183],v231 offset:22528
CV v191,v126,v127
MM v[128:131],a[140:143],v[208:211],v[128:131]
s_add_u32 s80, s18, 172032
buffer_load_dwordx2 v[236:237], v251, s[20:23], s80 offen offset:0 sc1
MM v[136:139],a[140:143],v[212:215],0
TR a[184:185],v231 offset:24576
TR a[186:187],v231 offset:26624
MM v[136:139],a[136:139],v[196:199],v[136:139]
s_add_u32 s80, s18, 172160
buffer_load_dwordx2 v[238:239], v251, s[20:23], s80 offen offset:0 sc1
MM v[136:139],a[132:135],v[180:183],v[136:139]
buffer_store_dwordx4 v[188:191], v248, s[20:23], s79 offen offset:1024 sc1
TR a[188:189],v231 offset:28672
TR a[190:191],v231 offset:30720
AR v80,a80
AR v81,a81
AR v82,a82
AR v83,a83
AR v84,a84
MM v[136:139],a[128:131],v[164:167],v[136:139]
s_waitcnt lgkmcnt(15)
AR v85,a85
AR v86,a86
MM v[132:135],a[144:147],v[160:163],0
s_add_u32 s80, s63, 131072
buffer_load_dwordx2 v[246:247], v254, s[20:23], s80 offen offset:0 sc1
TR a[128:129],v230 offset:16896
TR a[130:131],v230 offset:18944
AR v87,a87
MM v[132:135],a[148:151],v[176:179],v[132:135]
v_mul_f32 v80, v80, v153
v_mul_f32 v81, v81, v153
MM v[132:135],a[152:155],v[192:195],v[132:135]
TR a[132:133],v230 offset:20992
TR a[134:135],v230 offset:23040
v_mul_f32 v82, v82, v153
MM v[132:135],a[156:159],v[208:211],v[132:135]
v_mul_f32 v83, v83, v153
v_mul_f32 v84, v84, v153
MM v[140:143],a[156:159],v[212:215],0
TR a[136:137],v230 offset:25088
TR a[138:139],v230 offset:27136
v_mul_f32 v85, v85, v153
MM v[140:143],a[152:155],v[196:199],v[140:143]
v_mul_f32 v86, v86, v153
v_mul_f32 v87, v87, v153
MM v[140:143],a[148:151],v[180:183],v[140:143]
TR a[140:141],v230 offset:29184
TR a[142:143],v230 offset:31232
CV v200,v80,v81
CV v201,v82,v83
MM v[140:143],a[144:147],v[164:167],v[140:143]
s_waitcnt lgkmcnt(15)
CV v202,v84,v85
CV v203,v86,v87
MM v[64:67],a[160:163],v[160:163],0
TR a[144:145],v231 offset:16896
TR a[146:147],v231 offset:18944
MM v[64:67],a[164:167],v[176:179],v[64:67]
MM v[64:67],a[168:171],v[192:195],v[64:67]
TR a[148:149],v231 offset:20992
TR a[150:151],v231 offset:23040
MM v[64:67],a[172:175],v[208:211],v[64:67]
buffer_store_dwordx4 v[200:203], v248, s[20:23], s78 offen offset:2048 sc1
AR v88,a88
AR v89,a89
AR v90,a90
LPG21_%=:
s_cbranch_execz LPG25_%=
AR v91,a91
MM v[72:75],a[172:175],v[212:215],0
TR a[152:153],v231 offset:25088
TR a[154:155],v231 offset:27136
AR v92,a92
MM v[72:75],a[168:171],v[196:199],v[72:75]
AR v93,a93
AR v94,a94
MM v[72:75],a[164:167],v[180:183],v[72:75]
TR a[156:157],v231 offset:29184
TR a[158:159],v231 offset:31232
AR v95,a95
v_mul_f32 v88, v88, v157
MM v[72:75],a[160:163],v[164:167],v[72:75]
s_waitcnt lgkmcnt(15)
v_mul_f32 v89, v89, v157
v_mul_f32 v90, v90, v157
MM v[68:71],a[176:179],v[160:163],0
TR a[160:161],v244 offset:49152
TR a[162:163],v244 offset:51200
v_mul_f32 v91, v91, v157
MM v[68:71],a[180:183],v[176:179],v[68:71]
v_mul_f32 v92, v92, v157
v_mul_f32 v93, v93, v157
MM v[68:71],a[184:187],v[192:195],v[68:71]
TR a[164:165],v244 offset:53248
TR a[166:167],v244 offset:55296
v_mul_f32 v94, v94, v157
MM v[68:71],a[188:191],v[208:211],v[68:71]
v_mul_f32 v95, v95, v157
CV v204,v88,v89
MM v[76:79],a[188:191],v[212:215],0
TR a[168:169],v244 offset:57344
TR a[170:171],v244 offset:59392
CV v205,v90,v91
CV v206,v92,v93
MM v[76:79],a[184:187],v[196:199],v[76:79]
CV v207,v94,v95
MM v[76:79],a[180:183],v[180:183],v[76:79]
TR a[172:173],v244 offset:61440
TR a[174:175],v244 offset:63488
MM v[76:79],a[176:179],v[164:167],v[76:79]
s_waitcnt lgkmcnt(15)
MM v[96:99],a[128:131],v[160:163],0
s_xor_b32 s92, s49, 2
buffer_store_dwordx4 v[204:207], v248, s[20:23], s79 offen offset:2048 sc1
TR a[176:177],v235 offset:49152
TR a[178:179],v235 offset:51200
AR v112,a112
AR v113,a113
AR v114,a114
MM v[96:99],a[132:135],v[176:179],v[96:99]
s_mul_i32 s92, s92, 17408
s_add_u32 s92, s83, s92
AR v115,a115
AR v116,a116
MM v[96:99],a[136:139],v[192:195],v[96:99]
TR a[180:181],v235 offset:53248
TR a[182:183],v235 offset:55296
AR v117,a117
MM v[96:99],a[140:143],v[208:211],v[96:99]
buffer_load_dwordx4 a[8:11], v248, s[20:23], s92 offen offset:0 sc1
AR v118,a118
AR v119,a119
MM v[104:107],a[140:143],v[212:215],0
TR a[184:185],v235 offset:57344
TR a[186:187],v235 offset:59392
v_mul_f32 v112, v112, v153
v_mul_f32 v113, v113, v153
MM v[104:107],a[136:139],v[196:199],v[104:107]
buffer_load_dwordx4 a[40:43], v248, s[20:23], s92 offen offset:1024 sc1
v_mul_f32 v114, v114, v153
MM v[104:107],a[132:135],v[180:183],v[104:107]
TR a[188:189],v235 offset:61440
TR a[190:191],v235 offset:63488
v_mul_f32 v115, v115, v153
v_mul_f32 v116, v116, v153
MM v[104:107],a[128:131],v[164:167],v[104:107]
s_waitcnt lgkmcnt(15)
buffer_load_dwordx4 a[72:75], v248, s[20:23], s92 offen offset:2048 sc1
v_mul_f32 v117, v117, v153
v_mul_f32 v118, v118, v153
MM v[100:103],a[144:147],v[160:163],0
TR a[128:129],v244 offset:49664
TR a[130:131],v244 offset:51712
v_mul_f32 v119, v119, v153
MM v[100:103],a[148:151],v[176:179],v[100:103]
CV v216,v112,v113
CV v217,v114,v115
MM v[100:103],a[152:155],v[192:195],v[100:103]
TR a[132:133],v244 offset:53760
TR a[134:135],v244 offset:55808
CV v218,v116,v117
MM v[100:103],a[156:159],v[208:211],v[100:103]
buffer_load_dwordx4 a[104:107], v248, s[20:23], s92 offen offset:3072 sc1
CV v219,v118,v119
MM v[108:111],a[156:159],v[212:215],0
s_add_u32 s92, s92, 4096
TR a[136:137],v244 offset:57856
TR a[138:139],v244 offset:59904
MM v[108:111],a[152:155],v[196:199],v[108:111]
buffer_load_dwordx4 a[12:15], v248, s[20:23], s92 offen offset:0 sc1
MM v[108:111],a[148:151],v[180:183],v[108:111]
TR a[140:141],v244 offset:61952
TR a[142:143],v244 offset:64000
MM v[108:111],a[144:147],v[164:167],v[108:111]
s_waitcnt lgkmcnt(15)
s_waitcnt vmcnt(19)
)ASM"
R"ASM(
MM v[0:3],a[160:163],a[0:3],v[0:3]
buffer_store_dwordx4 v[216:219], v248, s[20:23], s78 offen offset:3072 sc1
TR a[144:145],v235 offset:49664
TR a[146:147],v235 offset:51712
AR v120,a120
AR v121,a121
AR v122,a122
AR v123,a123
AR v124,a124
AR v125,a125
AR v126,a126
MM v[0:3],a[164:167],a[32:35],v[0:3]
AR v127,a127
v_mul_f32 v120, v120, v157
MM v[0:3],a[168:171],a[64:67],v[0:3]
TR a[148:149],v235 offset:53760
TR a[150:151],v235 offset:55808
v_mul_f32 v121, v121, v157
v_mul_f32 v122, v122, v157
MM v[0:3],a[172:175],a[96:99],v[0:3]
s_waitcnt vmcnt(15)
buffer_load_dwordx4 a[44:47], v248, s[20:23], s92 offen offset:1024 sc1
v_mul_f32 v123, v123, v157
MM v[8:11],a[172:175],a[100:103],v[8:11]
TR a[152:153],v235 offset:57856
TR a[154:155],v235 offset:59904
v_mul_f32 v124, v124, v157
v_mul_f32 v125, v125, v157
MM v[8:11],a[168:171],a[68:71],v[8:11]
v_mul_f32 v126, v126, v157
v_mul_f32 v127, v127, v157
MM v[8:11],a[164:167],a[36:39],v[8:11]
TR a[156:157],v235 offset:61952
TR a[158:159],v235 offset:64000
CV v220,v120,v121
MM v[8:11],a[160:163],a[4:7],v[8:11]
s_waitcnt lgkmcnt(15)
buffer_load_dwordx4 a[76:79], v248, s[20:23], s92 offen offset:2048 sc1
CV v221,v122,v123
CV v222,v124,v125
MM v[4:7],a[176:179],a[0:3],v[4:7]
TR a[160:161],v228 offset:8192
TR a[162:163],v228 offset:9216
CV v223,v126,v127
MM v[4:7],a[180:183],a[32:35],v[4:7]
buffer_load_dwordx4 a[108:111], v248, s[20:23], s92 offen offset:3072 sc1
MM v[4:7],a[184:187],a[64:67],v[4:7]
TR a[164:165],v228 offset:10240
TR a[166:167],v228 offset:11264
MM v[4:7],a[188:191],a[96:99],v[4:7]
MM v[12:15],a[188:191],a[100:103],v[12:15]
buffer_store_dwordx4 v[220:223], v248, s[20:23], s79 offen offset:3072 sc1
TR a[168:169],v228 offset:12288
TR a[170:171],v228 offset:13312
MM v[12:15],a[184:187],a[68:71],v[12:15]
MM v[12:15],a[180:183],a[36:39],v[12:15]
TR a[172:173],v228 offset:14336
TR a[174:175],v228 offset:15360
MM v[12:15],a[176:179],a[4:7],v[12:15]
s_waitcnt lgkmcnt(15)
MM v[32:35],a[128:131],a[0:3],v[32:35]
TR a[176:177],v229 offset:8192
TR a[178:179],v229 offset:9216
MM v[32:35],a[132:135],a[32:35],v[32:35]
MM v[32:35],a[136:139],a[64:67],v[32:35]
TR a[180:181],v229 offset:10240
TR a[182:183],v229 offset:11264
MM v[32:35],a[140:143],a[96:99],v[32:35]
MM v[40:43],a[140:143],a[100:103],v[40:43]
TR a[184:185],v229 offset:12288
TR a[186:187],v229 offset:13312
MM v[40:43],a[136:139],a[68:71],v[40:43]
MM v[40:43],a[132:135],a[36:39],v[40:43]
TR a[188:189],v229 offset:14336
TR a[190:191],v229 offset:15360
MM v[40:43],a[128:131],a[4:7],v[40:43]
s_waitcnt lgkmcnt(15)
MM v[36:39],a[144:147],a[0:3],v[36:39]
s_waitcnt vmcnt(12)
TR a[128:129],v230 offset:0
TR a[130:131],v230 offset:2048
MM v[36:39],a[148:151],a[32:35],v[36:39]
v_lshlrev_b32 v112, 16, v224
v_and_b32 v113, 0xffff0000, v224
MM v[36:39],a[152:155],a[64:67],v[36:39]
TR a[132:133],v230 offset:4096
TR a[134:135],v230 offset:6144
MM v[36:39],a[156:159],a[96:99],v[36:39]
v_lshlrev_b32 v114, 16, v225
v_and_b32 v115, 0xffff0000, v225
MM v[44:47],a[156:159],a[100:103],v[44:47]
TR a[136:137],v230 offset:8192
TR a[138:139],v230 offset:10240
MM v[44:47],a[152:155],a[68:71],v[44:47]
v_mul_f32 v116, 0xbfb8aa3b, v112
v_mul_f32 v117, 0xbfb8aa3b, v113
MM v[44:47],a[148:151],a[36:39],v[44:47]
TR a[140:141],v230 offset:12288
TR a[142:143],v230 offset:14336
MM v[44:47],a[144:147],a[4:7],v[44:47]
s_waitcnt lgkmcnt(15)
v_mul_f32 v118, 0xbfb8aa3b, v114
v_mul_f32 v119, 0xbfb8aa3b, v115
MM v[128:131],a[160:163],a[0:3],v[128:131]
TR a[144:145],v231 offset:0
TR a[146:147],v231 offset:2048
MM v[128:131],a[164:167],a[32:35],v[128:131]
EX v116,v116
MM v[128:131],a[168:171],a[64:67],v[128:131]
TR a[148:149],v231 offset:4096
TR a[150:151],v231 offset:6144
MM v[128:131],a[172:175],a[96:99],v[128:131]
EX v117,v117
MM v[136:139],a[172:175],a[100:103],v[136:139]
TR a[152:153],v231 offset:8192
TR a[154:155],v231 offset:10240
MM v[136:139],a[168:171],a[68:71],v[136:139]
EX v118,v118
MM v[136:139],a[164:167],a[36:39],v[136:139]
TR a[156:157],v231 offset:12288
TR a[158:159],v231 offset:14336
MM v[136:139],a[160:163],a[4:7],v[136:139]
s_waitcnt lgkmcnt(15)
EX v119,v119
MM v[132:135],a[176:179],a[0:3],v[132:135]
s_xor_b32 s92, s49, 3
TR a[160:161],v230 offset:512
TR a[162:163],v230 offset:2560
MM v[132:135],a[180:183],a[32:35],v[132:135]
s_mul_i32 s92, s92, 17408
s_add_u32 s92, s83, s92
v_add_f32 v116, 1.0, v116
MM v[132:135],a[184:187],a[64:67],v[132:135]
TR a[164:165],v230 offset:4608
TR a[166:167],v230 offset:6656
MM v[132:135],a[188:191],a[96:99],v[132:135]
buffer_load_dwordx4 a[192:195], v248, s[20:23], s92 offen offset:0 sc1
MM v[140:143],a[188:191],a[100:103],v[140:143]
TR a[168:169],v230 offset:8704
TR a[170:171],v230 offset:10752
MM v[140:143],a[184:187],a[68:71],v[140:143]
buffer_load_dwordx4 a[208:211], v248, s[20:23], s92 offen offset:1024 sc1
MM v[140:143],a[180:183],a[36:39],v[140:143]
TR a[172:173],v230 offset:12800
TR a[174:175],v230 offset:14848
MM v[140:143],a[176:179],a[4:7],v[140:143]
s_waitcnt lgkmcnt(15)
buffer_load_dwordx4 a[224:227], v248, s[20:23], s92 offen offset:2048 sc1
s_waitcnt vmcnt(9)
MM v[0:3],a[128:131],a[8:11],v[0:3]
TR a[176:177],v231 offset:512
TR a[178:179],v231 offset:2560
MM v[0:3],a[132:135],a[40:43],v[0:3]
v_add_f32 v117, 1.0, v117
v_add_f32 v118, 1.0, v118
MM v[0:3],a[136:139],a[72:75],v[0:3]
TR a[180:181],v231 offset:4608
TR a[182:183],v231 offset:6656
MM v[0:3],a[140:143],a[104:107],v[0:3]
s_waitcnt vmcnt(4)
buffer_load_dwordx4 a[240:243], v248, s[20:23], s92 offen offset:3072 sc1
MM v[8:11],a[140:143],a[108:111],v[8:11]
s_add_u32 s92, s92, 4096
TR a[184:185],v231 offset:8704
TR a[186:187],v231 offset:10752
MM v[8:11],a[136:139],a[76:79],v[8:11]
buffer_load_dwordx4 a[196:199], v248, s[20:23], s92 offen offset:0 sc1
MM v[8:11],a[132:135],a[44:47],v[8:11]
TR a[188:189],v231 offset:12800
TR a[190:191],v231 offset:14848
MM v[8:11],a[128:131],a[12:15],v[8:11]
s_waitcnt lgkmcnt(15)
v_add_f32 v119, 1.0, v119
MM v[4:7],a[144:147],a[8:11],v[4:7]
TR a[128:129],v228 offset:16384
TR a[130:131],v228 offset:17408
MM v[4:7],a[148:151],a[40:43],v[4:7]
buffer_load_dwordx4 a[212:215], v248, s[20:23], s92 offen offset:1024 sc1
MM v[4:7],a[152:155],a[72:75],v[4:7]
TR a[132:133],v228 offset:18432
TR a[134:135],v228 offset:19456
MM v[4:7],a[156:159],a[104:107],v[4:7]
buffer_load_dwordx4 a[228:231], v248, s[20:23], s92 offen offset:2048 sc1
MM v[12:15],a[156:159],a[108:111],v[12:15]
TR a[136:137],v228 offset:20480
TR a[138:139],v228 offset:21504
MM v[12:15],a[152:155],a[76:79],v[12:15]
v_rcp_f32 v116, v116
MM v[12:15],a[148:151],a[44:47],v[12:15]
TR a[140:141],v228 offset:22528
TR a[142:143],v228 offset:23552
MM v[12:15],a[144:147],a[12:15],v[12:15]
s_waitcnt lgkmcnt(15)
buffer_load_dwordx4 a[244:247], v248, s[20:23], s92 offen offset:3072 sc1
MM v[32:35],a[160:163],a[8:11],v[32:35]
TR a[144:145],v229 offset:16384
TR a[146:147],v229 offset:17408
MM v[32:35],a[164:167],a[40:43],v[32:35]
s_add_u32 m0, s89, 32768
v_rcp_f32 v117, v117
MM v[32:35],a[168:171],a[72:75],v[32:35]
s_add_u32 s66, s87, 128
TR a[148:149],v229 offset:18432
TR a[150:151],v229 offset:19456
MM v[32:35],a[172:175],a[104:107],v[32:35]
buffer_load_dwordx4 v240, s[24:27], s66 offen offset:0 lds
MM v[40:43],a[172:175],a[108:111],v[40:43]
s_add_u32 m0, s89, 33792
TR a[152:153],v229 offset:20480
TR a[154:155],v229 offset:21504
MM v[40:43],a[168:171],a[76:79],v[40:43]
s_add_u32 s66, s87, 10368
buffer_load_dwordx4 v240, s[24:27], s66 offen offset:0 lds
MM v[40:43],a[164:167],a[44:47],v[40:43]
s_add_u32 m0, s89, 40960
TR a[156:157],v229 offset:22528
TR a[158:159],v229 offset:23552
MM v[40:43],a[160:163],a[12:15],v[40:43]
s_waitcnt lgkmcnt(15)
v_rcp_f32 v118, v118
MM v[36:39],a[176:179],a[8:11],v[36:39]
s_add_u32 s66, s87, 82048
TR a[160:161],v244 offset:0
TR a[162:163],v244 offset:2048
MM v[36:39],a[180:183],a[40:43],v[36:39]
buffer_load_dwordx4 v240, s[24:27], s66 offen offset:0 lds
MM v[36:39],a[184:187],a[72:75],v[36:39]
s_add_u32 m0, s89, 41984
TR a[164:165],v244 offset:4096
TR a[166:167],v244 offset:6144
MM v[36:39],a[188:191],a[104:107],v[36:39]
s_add_u32 s66, s87, 92288
buffer_load_dwordx4 v240, s[24:27], s66 offen offset:0 lds
MM v[44:47],a[188:191],a[108:111],v[44:47]
s_add_u32 m0, s89, 98304
TR a[168:169],v244 offset:8192
TR a[170:171],v244 offset:10240
MM v[44:47],a[184:187],a[76:79],v[44:47]
s_add_u32 s66, s88, 128
buffer_load_dwordx4 v240, s[24:27], s66 offen offset:0 lds
MM v[44:47],a[180:183],a[44:47],v[44:47]
s_add_u32 m0, s89, 99328
TR a[172:173],v244 offset:12288
TR a[174:175],v244 offset:14336
MM v[44:47],a[176:179],a[12:15],v[44:47]
s_waitcnt lgkmcnt(15)
v_rcp_f32 v119, v119
MM v[128:131],a[128:131],a[8:11],v[128:131]
s_add_u32 s66, s88, 10368
TR a[176:177],v235 offset:0
TR a[178:179],v235 offset:2048
MM v[128:131],a[132:135],a[40:43],v[128:131]
buffer_load_dwordx4 v240, s[24:27], s66 offen offset:0 lds
MM v[128:131],a[136:139],a[72:75],v[128:131]
s_add_u32 m0, s89, 106496
TR a[180:181],v235 offset:4096
TR a[182:183],v235 offset:6144
MM v[128:131],a[140:143],a[104:107],v[128:131]
s_add_u32 s66, s88, 82048
buffer_load_dwordx4 v240, s[24:27], s66 offen offset:0 lds
MM v[136:139],a[140:143],a[108:111],v[136:139]
s_add_u32 m0, s89, 107520
TR a[184:185],v235 offset:8192
TR a[186:187],v235 offset:10240
MM v[136:139],a[136:139],a[76:79],v[136:139]
s_add_u32 s66, s88, 92288
v_mul_f32 v116, v112, v116
v_mul_f32 v117, v113, v117
MM v[136:139],a[132:135],a[44:47],v[136:139]
TR a[188:189],v235 offset:12288
TR a[190:191],v235 offset:14336
MM v[136:139],a[128:131],a[12:15],v[136:139]
s_waitcnt lgkmcnt(15)
buffer_load_dwordx4 v240, s[24:27], s66 offen offset:0 lds
MM v[132:135],a[144:147],a[8:11],v[132:135]
s_lshl_b32 s97, s84, 2
TR a[128:129],v244 offset:512
TR a[130:131],v244 offset:2560
MM v[132:135],a[148:151],a[40:43],v[132:135]
s_add_u32 s97, s97, s96
s_add_u32 s97, s97, 16
s_waitcnt vmcnt(16)
MM v[132:135],a[152:155],a[72:75],v[132:135]
TR a[132:133],v244 offset:4608
TR a[134:135],v244 offset:6656
MM v[132:135],a[156:159],a[104:107],v[132:135]
v_mov_b32 v242, s69
s_mov_b64 exec, 1
buffer_store_dword v242, off, s[20:23], s97 offset:0 sc1
s_mov_b64 exec, -1
MM v[140:143],a[156:159],a[108:111],v[140:143]
TR a[136:137],v244 offset:8704
TR a[138:139],v244 offset:10752
MM v[140:143],a[152:155],a[76:79],v[140:143]
v_mul_f32 v118, v114, v118
v_mul_f32 v119, v115, v119
MM v[140:143],a[148:151],a[44:47],v[140:143]
TR a[140:141],v244 offset:12800
TR a[142:143],v244 offset:14848
MM v[140:143],a[144:147],a[12:15],v[140:143]
s_waitcnt lgkmcnt(15)
s_waitcnt vmcnt(13)
MM v[0:3],a[160:163],a[192:195],v[0:3]
TR a[144:145],v235 offset:512
TR a[146:147],v235 offset:2560
MM v[0:3],a[164:167],a[208:211],v[0:3]
MM v[0:3],a[168:171],a[224:227],v[0:3]
TR a[148:149],v235 offset:4608
TR a[150:151],v235 offset:6656
MM v[0:3],a[172:175],a[240:243],v[0:3]
s_waitcnt vmcnt(9)
MM v[8:11],a[172:175],a[244:247],v[8:11]
TR a[152:153],v235 offset:8704
TR a[154:155],v235 offset:10752
MM v[8:11],a[168:171],a[228:231],v[8:11]
MM v[8:11],a[164:167],a[212:215],v[8:11]
TR a[156:157],v235 offset:12800
TR a[158:159],v235 offset:14848
MM v[8:11],a[160:163],a[196:199],v[8:11]
s_waitcnt lgkmcnt(15)
MM v[4:7],a[176:179],a[192:195],v[4:7]
TR a[160:161],v228 offset:24576
TR a[162:163],v228 offset:25600
MM v[4:7],a[180:183],a[208:211],v[4:7]
MM v[4:7],a[184:187],a[224:227],v[4:7]
TR a[164:165],v228 offset:26624
TR a[166:167],v228 offset:27648
MM v[4:7],a[188:191],a[240:243],v[4:7]
MM v[12:15],a[188:191],a[244:247],v[12:15]
TR a[168:169],v228 offset:28672
TR a[170:171],v228 offset:29696
MM v[12:15],a[184:187],a[228:231],v[12:15]
MM v[12:15],a[180:183],a[212:215],v[12:15]
TR a[172:173],v228 offset:30720
TR a[174:175],v228 offset:31744
MM v[12:15],a[176:179],a[196:199],v[12:15]
s_waitcnt lgkmcnt(15)
MM v[32:35],a[128:131],a[192:195],v[32:35]
TR a[176:177],v229 offset:24576
TR a[178:179],v229 offset:25600
MM v[32:35],a[132:135],a[208:211],v[32:35]
MM v[32:35],a[136:139],a[224:227],v[32:35]
TR a[180:181],v229 offset:26624
TR a[182:183],v229 offset:27648
MM v[32:35],a[140:143],a[240:243],v[32:35]
)ASM"
R"ASM(
s_add_u32 s67, s46, 0
CV v0,v0,v1
CV v1,v2,v3
MM v[40:43],a[140:143],a[244:247],v[40:43]
TR a[184:185],v229 offset:28672
TR a[186:187],v229 offset:29696
MM v[40:43],a[136:139],a[228:231],v[40:43]
CV v2,v4,v5
CV v3,v6,v7
MM v[40:43],a[132:135],a[212:215],v[40:43]
TR a[188:189],v229 offset:30720
TR a[190:191],v229 offset:31744
MM v[40:43],a[128:131],a[196:199],v[40:43]
s_waitcnt lgkmcnt(15)
buffer_store_dwordx4 v[0:3], v249, s[20:23], s67 offen offset:0 sc1
MM v[36:39],a[144:147],a[192:195],v[36:39]
s_add_u32 s67, s47, 0
TR a[128:129],v244 offset:16384
TR a[130:131],v244 offset:18432
MM v[36:39],a[148:151],a[208:211],v[36:39]
CV v8,v8,v9
CV v9,v10,v11
MM v[36:39],a[152:155],a[224:227],v[36:39]
TR a[132:133],v244 offset:20480
TR a[134:135],v244 offset:22528
MM v[36:39],a[156:159],a[240:243],v[36:39]
CV v10,v12,v13
CV v11,v14,v15
MM v[44:47],a[156:159],a[244:247],v[44:47]
TR a[136:137],v244 offset:24576
TR a[138:139],v244 offset:26624
MM v[44:47],a[152:155],a[228:231],v[44:47]
buffer_store_dwordx4 v[8:11], v234, s[20:23], s67 offen offset:1024 sc1
MM v[44:47],a[148:151],a[212:215],v[44:47]
TR a[140:141],v244 offset:28672
TR a[142:143],v244 offset:30720
MM v[44:47],a[144:147],a[196:199],v[44:47]
s_waitcnt lgkmcnt(15)
ds_bpermute_b32 v160, v245, v0
ds_bpermute_b32 v161, v245, v1
MM v[128:131],a[160:163],a[192:195],v[128:131]
TR a[144:145],v235 offset:16384
TR a[146:147],v235 offset:18432
MM v[128:131],a[164:167],a[208:211],v[128:131]
ds_bpermute_b32 v162, v245, v2
ds_bpermute_b32 v163, v245, v3
MM v[128:131],a[168:171],a[224:227],v[128:131]
TR a[148:149],v235 offset:20480
TR a[150:151],v235 offset:22528
MM v[128:131],a[172:175],a[240:243],v[128:131]
CV v0,v116,v117
CV v1,v118,v119
MM v[136:139],a[172:175],a[244:247],v[136:139]
s_add_u32 s67, s46, 0
TR a[152:153],v235 offset:24576
TR a[154:155],v235 offset:26624
MM v[136:139],a[168:171],a[228:231],v[136:139]
CV v32,v32,v33
CV v33,v34,v35
MM v[136:139],a[164:167],a[212:215],v[136:139]
TR a[156:157],v235 offset:28672
TR a[158:159],v235 offset:30720
MM v[136:139],a[160:163],a[196:199],v[136:139]
s_waitcnt lgkmcnt(15)
CV v34,v36,v37
CV v35,v38,v39
s_waitcnt vmcnt(3)
s_barrier
MM v[132:135],a[176:179],a[192:195],v[132:135]
TR a[160:161],v230 offset:32768
TR a[162:163],v230 offset:34816
MM v[132:135],a[180:183],a[208:211],v[132:135]
buffer_store_dwordx4 v[32:35], v249, s[20:23], s67 offen offset:2048 sc1
MM v[132:135],a[184:187],a[224:227],v[132:135]
s_add_u32 s67, s47, 0
TR a[164:165],v230 offset:36864
TR a[166:167],v230 offset:38912
v_lshrrev_b32 v243, 2, v248
MM v[132:135],a[188:191],a[240:243],v[132:135]
CV v40,v40,v41
CV v41,v42,v43
MM v[140:143],a[188:191],a[244:247],v[140:143]
TR a[168:169],v230 offset:40960
TR a[170:171],v230 offset:43008
MM v[140:143],a[184:187],a[228:231],v[140:143]
CV v42,v44,v45
CV v43,v46,v47
v_min_u32 v243, 12, v243
MM v[140:143],a[180:183],a[212:215],v[140:143]
TR a[172:173],v230 offset:45056
TR a[174:175],v230 offset:47104
MM v[140:143],a[176:179],a[196:199],v[140:143]
s_waitcnt lgkmcnt(15)
buffer_store_dwordx4 v[40:43], v234, s[20:23], s67 offen offset:3072 sc1
MM v[64:67],a[128:131],a[0:3],v[64:67]
buffer_load_dword v242, v243, s[20:23], s96 offen offset:16 sc1
TR a[176:177],v231 offset:32768
TR a[178:179],v231 offset:34816
MM v[64:67],a[132:135],a[32:35],v[64:67]
ds_bpermute_b32 v176, v245, v32
ds_bpermute_b32 v177, v245, v33
MM v[64:67],a[136:139],a[64:67],v[64:67]
TR a[180:181],v231 offset:36864
s_nop 0
LPG22_%=:
s_cbranch_execz LTR3_%=
TR a[182:183],v231 offset:38912
MM v[64:67],a[140:143],a[96:99],v[64:67]
ds_bpermute_b32 v178, v245, v34
ds_bpermute_b32 v179, v245, v35
MM v[72:75],a[140:143],a[100:103],v[72:75]
TR a[184:185],v231 offset:40960
TR a[186:187],v231 offset:43008
MM v[72:75],a[136:139],a[68:71],v[72:75]
v_lshlrev_b32 v112, 16, v226
v_and_b32 v113, 0xffff0000, v226
MM v[72:75],a[132:135],a[36:39],v[72:75]
s_add_u32 s67, s46, 8192
TR a[188:189],v231 offset:45056
TR a[190:191],v231 offset:47104
MM v[72:75],a[128:131],a[4:7],v[72:75]
s_waitcnt lgkmcnt(15)
CV v128,v128,v129
CV v129,v130,v131
MM v[68:71],a[144:147],a[0:3],v[68:71]
TR a[128:129],v244 offset:32768
TR a[130:131],v244 offset:34816
MM v[68:71],a[148:151],a[32:35],v[68:71]
CV v130,v132,v133
CV v131,v134,v135
MM v[68:71],a[152:155],a[64:67],v[68:71]
TR a[132:133],v244 offset:36864
TR a[134:135],v244 offset:38912
MM v[68:71],a[156:159],a[96:99],v[68:71]
buffer_store_dwordx4 v[128:131], v249, s[20:23], s67 offen offset:0 sc1
MM v[76:79],a[156:159],a[100:103],v[76:79]
s_add_u32 s67, s47, 8192
TR a[136:137],v244 offset:40960
TR a[138:139],v244 offset:43008
MM v[76:79],a[152:155],a[68:71],v[76:79]
CV v136,v136,v137
CV v137,v138,v139
MM v[76:79],a[148:151],a[36:39],v[76:79]
TR a[140:141],v244 offset:45056
TR a[142:143],v244 offset:47104
MM v[76:79],a[144:147],a[4:7],v[76:79]
s_waitcnt lgkmcnt(15)
CV v138,v140,v141
CV v139,v142,v143
MM v[64:67],a[160:163],a[8:11],v[64:67]
TR a[144:145],v235 offset:32768
TR a[146:147],v235 offset:34816
MM v[64:67],a[164:167],a[40:43],v[64:67]
buffer_store_dwordx4 v[136:139], v234, s[20:23], s67 offen offset:1024 sc1
MM v[64:67],a[168:171],a[72:75],v[64:67]
TR a[148:149],v235 offset:36864
TR a[150:151],v235 offset:38912
MM v[64:67],a[172:175],a[104:107],v[64:67]
ds_bpermute_b32 v164, v245, v128
ds_bpermute_b32 v165, v245, v129
MM v[72:75],a[172:175],a[108:111],v[72:75]
TR a[152:153],v235 offset:40960
TR a[154:155],v235 offset:43008
MM v[72:75],a[168:171],a[76:79],v[72:75]
ds_bpermute_b32 v166, v245, v130
ds_bpermute_b32 v167, v245, v131
MM v[72:75],a[164:167],a[44:47],v[72:75]
TR a[156:157],v235 offset:45056
TR a[158:159],v235 offset:47104
MM v[72:75],a[160:163],a[12:15],v[72:75]
s_waitcnt lgkmcnt(15)
v_lshlrev_b32 v114, 16, v227
v_and_b32 v115, 0xffff0000, v227
MM v[68:71],a[176:179],a[8:11],v[68:71]
TR a[160:161],v244 offset:16896
TR a[162:163],v244 offset:18944
MM v[68:71],a[180:183],a[40:43],v[68:71]
v_mul_f32 v116, 0xbfb8aa3b, v112
v_mul_f32 v117, 0xbfb8aa3b, v113
MM v[68:71],a[184:187],a[72:75],v[68:71]
TR a[164:165],v244 offset:20992
TR a[166:167],v244 offset:23040
MM v[68:71],a[188:191],a[104:107],v[68:71]
v_mul_f32 v118, 0xbfb8aa3b, v114
v_mul_f32 v119, 0xbfb8aa3b, v115
MM v[76:79],a[188:191],a[108:111],v[76:79]
TR a[168:169],v244 offset:25088
TR a[170:171],v244 offset:27136
MM v[76:79],a[184:187],a[76:79],v[76:79]
EX v116,v116
MM v[76:79],a[180:183],a[44:47],v[76:79]
TR a[172:173],v244 offset:29184
TR a[174:175],v244 offset:31232
MM v[76:79],a[176:179],a[12:15],v[76:79]
s_waitcnt lgkmcnt(15)
EX v117,v117
MM v[64:67],a[128:131],a[192:195],v[64:67]
TR a[176:177],v235 offset:16896
TR a[178:179],v235 offset:18944
MM v[64:67],a[132:135],a[208:211],v[64:67]
EX v118,v118
MM v[64:67],a[136:139],a[224:227],v[64:67]
TR a[180:181],v235 offset:20992
TR a[182:183],v235 offset:23040
MM v[64:67],a[140:143],a[240:243],v[64:67]
EX v119,v119
MM v[72:75],a[140:143],a[244:247],v[72:75]
TR a[184:185],v235 offset:25088
TR a[186:187],v235 offset:27136
MM v[72:75],a[136:139],a[228:231],v[72:75]
v_add_f32 v116, 1.0, v116
v_add_f32 v117, 1.0, v117
MM v[72:75],a[132:135],a[212:215],v[72:75]
TR a[188:189],v235 offset:29184
TR a[190:191],v235 offset:31232
MM v[72:75],a[128:131],a[196:199],v[72:75]
s_waitcnt lgkmcnt(15)
v_add_f32 v118, 1.0, v118
v_add_f32 v119, 1.0, v119
MM v[68:71],a[144:147],a[192:195],v[68:71]
TR a[128:129],v230 offset:33280
TR a[130:131],v230 offset:35328
MM v[68:71],a[148:151],a[208:211],v[68:71]
v_rcp_f32 v116, v116
MM v[68:71],a[152:155],a[224:227],v[68:71]
TR a[132:133],v230 offset:37376
TR a[134:135],v230 offset:39424
MM v[68:71],a[156:159],a[240:243],v[68:71]
v_rcp_f32 v117, v117
MM v[76:79],a[156:159],a[244:247],v[76:79]
TR a[136:137],v230 offset:41472
TR a[138:139],v230 offset:43520
MM v[76:79],a[152:155],a[228:231],v[76:79]
v_rcp_f32 v118, v118
MM v[76:79],a[148:151],a[212:215],v[76:79]
TR a[140:141],v230 offset:45568
TR a[142:143],v230 offset:47616
MM v[76:79],a[144:147],a[196:199],v[76:79]
s_waitcnt lgkmcnt(15)
v_rcp_f32 v119, v119
MM v[96:99],a[160:163],a[0:3],v[96:99]
s_waitcnt vmcnt(2)
TR a[144:145],v231 offset:33280
TR a[146:147],v231 offset:35328
MM v[96:99],a[164:167],a[32:35],v[96:99]
v_cmp_ne_u32 vcc, s69, v242
s_cbranch_vccnz LRPO1_%=
LRPO1b_%=:
MM v[96:99],a[168:171],a[64:67],v[96:99]
s_xor_b32 s92, s49, 1
TR a[148:149],v231 offset:37376
TR a[150:151],v231 offset:39424
MM v[96:99],a[172:175],a[96:99],v[96:99]
s_add_u32 s67, s46, 4096
CV v64,v64,v65
CV v65,v66,v67
MM v[104:107],a[172:175],a[100:103],v[104:107]
s_mul_i32 s92, s92, 17408
TR a[152:153],v231 offset:41472
TR a[154:155],v231 offset:43520
MM v[104:107],a[168:171],a[68:71],v[104:107]
s_add_u32 s92, s83, s92
CV v66,v68,v69
CV v67,v70,v71
MM v[104:107],a[164:167],a[36:39],v[104:107]
s_add_u32 s92, s92, 8192
TR a[156:157],v231 offset:45568
TR a[158:159],v231 offset:47616
MM v[104:107],a[160:163],a[4:7],v[104:107]
s_waitcnt lgkmcnt(15)
buffer_store_dwordx4 v[64:67], v249, s[20:23], s67 offen offset:0 sc1
MM v[100:103],a[176:179],a[0:3],v[100:103]
s_add_u32 s67, s47, 4096
TR a[160:161],v244 offset:33280
TR a[162:163],v244 offset:35328
MM v[100:103],a[180:183],a[32:35],v[100:103]
s_cmp_eq_u32 s82, 0
s_cbranch_scc1 LY0_%=
v_lshrrev_b32 v135, 4, v248
v_min_u32 v135, 59, v135
v_mul_u32_u24 v132, 205, v135
v_lshrrev_b32 v132, 11, v132
v_mul_u32_u24 v132, 30, v132
v_add_lshl_u32 v135, v135, v132, 2
s_mul_i32 s78, s84, 40
s_add_u32 s78, s78, 73728
buffer_load_dword v132, v135, s[20:23], s78 offen offset:0 sc1
buffer_load_dword v133, v135, s[20:23], s78 offen offset:960 sc1
buffer_load_dword v134, v135, s[20:23], s78 offen offset:1920 sc1
LY0_%=:
buffer_load_dwordx4 a[16:19], v248, s[20:23], s92 offen offset:0 sc1
CV v72,v72,v73
CV v73,v74,v75
MM v[100:103],a[184:187],a[64:67],v[100:103]
TR a[164:165],v244 offset:37376
TR a[166:167],v244 offset:39424
MM v[100:103],a[188:191],a[96:99],v[100:103]
CV v74,v76,v77
CV v75,v78,v79
MM v[108:111],a[188:191],a[100:103],v[108:111]
TR a[168:169],v244 offset:41472
TR a[170:171],v244 offset:43520
MM v[108:111],a[184:187],a[68:71],v[108:111]
buffer_store_dwordx4 v[72:75], v234, s[20:23], s67 offen offset:1024 sc1
MM v[108:111],a[180:183],a[36:39],v[108:111]
buffer_load_dwordx4 a[48:51], v248, s[20:23], s92 offen offset:1024 sc1
TR a[172:173],v244 offset:45568
TR a[174:175],v244 offset:47616
MM v[108:111],a[176:179],a[4:7],v[108:111]
s_waitcnt lgkmcnt(15)
ds_bpermute_b32 v192, v245, v64
ds_bpermute_b32 v193, v245, v65
MM v[96:99],a[128:131],a[8:11],v[96:99]
TR a[176:177],v235 offset:33280
TR a[178:179],v235 offset:35328
MM v[96:99],a[132:135],a[40:43],v[96:99]
ds_bpermute_b32 v194, v245, v66
ds_bpermute_b32 v195, v245, v67
MM v[96:99],a[136:139],a[72:75],v[96:99]
TR a[180:181],v235 offset:37376
TR a[182:183],v235 offset:39424
MM v[96:99],a[140:143],a[104:107],v[96:99]
buffer_load_dwordx4 a[80:83], v248, s[20:23], s92 offen offset:2048 sc1
MM v[104:107],a[140:143],a[108:111],v[104:107]
TR a[184:185],v235 offset:41472
TR a[186:187],v235 offset:43520
MM v[104:107],a[136:139],a[76:79],v[104:107]
buffer_load_dwordx4 a[112:115], v248, s[20:23], s92 offen offset:3072 sc1
MM v[104:107],a[132:135],a[44:47],v[104:107]
s_add_u32 s92, s92, 4096
TR a[188:189],v235 offset:45568
TR a[190:191],v235 offset:47616
MM v[104:107],a[128:131],a[12:15],v[104:107]
s_waitcnt lgkmcnt(15)
buffer_load_dwordx4 a[20:23], v248, s[20:23], s92 offen offset:0 sc1
MM v[100:103],a[144:147],a[8:11],v[100:103]
TR a[128:129],v230 offset:49152
TR a[130:131],v230 offset:51200
MM v[100:103],a[148:151],a[40:43],v[100:103]
v_mov_b32 v252, s69
s_mov_b64 exec, 1
buffer_store_dword v252, off, s[20:23], s19 offset:0 sc1
s_mov_b64 exec, -1
MM v[100:103],a[152:155],a[72:75],v[100:103]
TR a[132:133],v230 offset:53248
TR a[134:135],v230 offset:55296
MM v[100:103],a[156:159],a[104:107],v[100:103]
buffer_load_dwordx4 a[52:55], v248, s[20:23], s92 offen offset:1024 sc1
MM v[108:111],a[156:159],a[108:111],v[108:111]
TR a[136:137],v230 offset:57344
TR a[138:139],v230 offset:59392
MM v[108:111],a[152:155],a[76:79],v[108:111]
buffer_load_dwordx4 a[84:87], v248, s[20:23], s92 offen offset:2048 sc1
)ASM"
R"ASM(
MM v[108:111],a[148:151],a[44:47],v[108:111]
TR a[140:141],v230 offset:61440
TR a[142:143],v230 offset:63488
MM v[108:111],a[144:147],a[12:15],v[108:111]
s_waitcnt lgkmcnt(15)
v_mul_f32 v116, v112, v116
v_mul_f32 v117, v113, v117
MM v[96:99],a[160:163],a[192:195],v[96:99]
TR a[144:145],v231 offset:49152
TR a[146:147],v231 offset:51200
MM v[96:99],a[164:167],a[208:211],v[96:99]
buffer_load_dwordx4 a[116:119], v248, s[20:23], s92 offen offset:3072 sc1
MM v[96:99],a[168:171],a[224:227],v[96:99]
TR a[148:149],v231 offset:53248
TR a[150:151],v231 offset:55296
MM v[96:99],a[172:175],a[240:243],v[96:99]
v_mul_f32 v118, v114, v118
v_mul_f32 v119, v115, v119
MM v[104:107],a[172:175],a[244:247],v[104:107]
TR a[152:153],v231 offset:57344
TR a[154:155],v231 offset:59392
MM v[104:107],a[168:171],a[228:231],v[104:107]
CV v2,v116,v117
CV v3,v118,v119
MM v[104:107],a[164:167],a[212:215],v[104:107]
TR a[156:157],v231 offset:61440
TR a[158:159],v231 offset:63488
MM v[104:107],a[160:163],a[196:199],v[104:107]
s_waitcnt lgkmcnt(15)
v_lshlrev_b32 v10, 16, v236
v_and_b32 v11, 0xffff0000, v236
MM v[100:103],a[176:179],a[192:195],v[100:103]
TR a[160:161],v230 offset:49664
TR a[162:163],v230 offset:51712
MM v[100:103],a[180:183],a[208:211],v[100:103]
v_lshlrev_b32 v12, 16, v237
v_and_b32 v13, 0xffff0000, v237
MM v[100:103],a[184:187],a[224:227],v[100:103]
TR a[164:165],v230 offset:53760
TR a[166:167],v230 offset:55808
MM v[100:103],a[188:191],a[240:243],v[100:103]
v_mul_f32 v14, 0xbfb8aa3b, v10
v_mul_f32 v15, 0xbfb8aa3b, v11
MM v[108:111],a[188:191],a[244:247],v[108:111]
TR a[168:169],v230 offset:57856
TR a[170:171],v230 offset:59904
MM v[108:111],a[184:187],a[228:231],v[108:111]
v_mul_f32 v42, 0xbfb8aa3b, v12
v_mul_f32 v43, 0xbfb8aa3b, v13
MM v[108:111],a[180:183],a[212:215],v[108:111]
TR a[172:173],v230 offset:61952
TR a[174:175],v230 offset:64000
MM v[108:111],a[176:179],a[196:199],v[108:111]
s_waitcnt lgkmcnt(15)
EX v14,v14
MM v[16:19],a[128:131],v[168:171],0
TR a[176:177],v231 offset:49664
TR a[178:179],v231 offset:51712
MM v[16:19],a[132:135],v[184:187],v[16:19]
AW a3,-1
AW a7,-1
MM v[16:19],a[136:139],v[200:203],v[16:19]
TR a[180:181],v231 offset:53760
TR a[182:183],v231 offset:55808
MM v[16:19],a[140:143],v[216:219],v[16:19]
s_add_u32 s67, s46, 4096
CV v96,v96,v97
CV v97,v98,v99
AW a11,-1
MM v[24:27],a[140:143],v[220:223],0
TR a[184:185],v231 offset:57856
TR a[186:187],v231 offset:59904
MM v[24:27],a[136:139],v[204:207],v[24:27]
CV v98,v100,v101
CV v99,v102,v103
MM v[24:27],a[132:135],v[188:191],v[24:27]
TR a[188:189],v231 offset:61952
TR a[190:191],v231 offset:64000
AW a15,-1
MM v[24:27],a[128:131],v[172:175],v[24:27]
s_waitcnt lgkmcnt(15)
buffer_store_dwordx4 v[96:99], v249, s[20:23], s67 offen offset:2048 sc1
MM v[20:23],a[144:147],v[168:171],0
s_add_u32 s67, s47, 4096
TR a[128:129],v230 offset:16384
TR a[130:131],v230 offset:18432
MM v[20:23],a[148:151],v[184:187],v[20:23]
CV v104,v104,v105
CV v105,v106,v107
AW a35,-1
MM v[20:23],a[152:155],v[200:203],v[20:23]
TR a[132:133],v230 offset:20480
TR a[134:135],v230 offset:22528
MM v[20:23],a[156:159],v[216:219],v[20:23]
CV v106,v108,v109
CV v107,v110,v111
s_mov_b64 exec, 0xffff
AW a0,v160
AW a1,v161
AW a2,v162
AW a3,v163
s_mov_b64 exec, -1
MM v[28:31],a[156:159],v[220:223],0
TR a[136:137],v230 offset:24576
TR a[138:139],v230 offset:26624
AW a39,-1
MM v[28:31],a[152:155],v[204:207],v[28:31]
buffer_store_dwordx4 v[104:107], v234, s[20:23], s67 offen offset:3072 sc1
MM v[28:31],a[148:151],v[188:191],v[28:31]
TR a[140:141],v230 offset:28672
TR a[142:143],v230 offset:30720
s_mov_b64 exec, 0xffff
AW a8,v176
AW a9,v177
AW a10,v178
AW a11,v179
s_mov_b64 exec, -1
MM v[28:31],a[144:147],v[172:175],v[28:31]
s_waitcnt lgkmcnt(15)
ds_bpermute_b32 v208, v245, v96
ds_bpermute_b32 v209, v245, v97
AW a43,-1
MM v[48:51],a[160:163],v[168:171],0
TR a[144:145],v231 offset:16384
TR a[146:147],v231 offset:18432
MM v[48:51],a[164:167],v[184:187],v[48:51]
ds_bpermute_b32 v210, v245, v98
ds_bpermute_b32 v211, v245, v99
MM v[48:51],a[168:171],v[200:203],v[48:51]
TR a[148:149],v231 offset:20480
TR a[150:151],v231 offset:22528
AW a47,-1
MM v[48:51],a[172:175],v[216:219],v[48:51]
buffer_load_dword v240, v253, s[20:23], s68 offen offset:0 sc1
buffer_load_dword v140, v253, s[20:23], s45 offen offset:0 sc1
MM v[56:59],a[172:175],v[220:223],0
TR a[152:153],v231 offset:24576
TR a[154:155],v231 offset:26624
MM v[56:59],a[168:171],v[204:207],v[56:59]
AW a71,-1
AW a79,-1
MM v[56:59],a[164:167],v[188:191],v[56:59]
TR a[156:157],v231 offset:28672
TR a[158:159],v231 offset:30720
MM v[56:59],a[160:163],v[172:175],v[56:59]
s_waitcnt lgkmcnt(15)
AW a103,-1
AW a111,-1
MM v[52:55],a[176:179],v[168:171],0
TR a[160:161],v230 offset:16896
TR a[162:163],v230 offset:18944
MM v[52:55],a[180:183],v[184:187],v[52:55]
AW a195,-1
AW a199,-1
MM v[52:55],a[184:187],v[200:203],v[52:55]
TR a[164:165],v230 offset:20992
TR a[166:167],v230 offset:23040
MM v[52:55],a[188:191],v[216:219],v[52:55]
AW a215,-1
MM v[60:63],a[188:191],v[220:223],0
TR a[168:169],v230 offset:25088
TR a[170:171],v230 offset:27136
MM v[60:63],a[184:187],v[204:207],v[60:63]
s_mov_b64 exec, 0xffff
AW a32,v192
AW a33,v193
AW a34,v194
AW a35,v195
s_mov_b64 exec, -1
MM v[60:63],a[180:183],v[188:191],v[60:63]
s_waitcnt lgkmcnt(12)
TR a[172:173],v230 offset:29184
TR a[174:175],v230 offset:31232
MM v[60:63],a[176:179],v[172:175],v[60:63]
s_mov_b64 exec, 0xffff
AW a40,v208
AW a41,v209
AW a42,v210
AW a43,v211
s_mov_b64 exec, -1
MM v[80:83],a[128:131],v[168:171],0
TR a[176:177],v231 offset:16896
TR a[178:179],v231 offset:18944
MM v[80:83],a[132:135],v[184:187],v[80:83]
s_mov_b64 exec, 0xffff
AW a192,v164
AW a193,v165
AW a194,v166
AW a195,v167
s_mov_b64 exec, -1
MM v[80:83],a[136:139],v[200:203],v[80:83]
TR a[180:181],v231 offset:20992
TR a[182:183],v231 offset:23040
MM v[80:83],a[140:143],v[216:219],v[80:83]
v_lshrrev_b32 v104, 8, v248
v_min_u32 v104, 1, v104
MM v[88:91],a[140:143],v[220:223],0
TR a[184:185],v231 offset:25088
TR a[186:187],v231 offset:27136
MM v[88:91],a[136:139],v[204:207],v[88:91]
EX v15,v15
v_sub_u32 v104, 0, v104
MM v[88:91],a[132:135],v[188:191],v[88:91]
TR a[188:189],v231 offset:29184
TR a[190:191],v231 offset:31232
MM v[88:91],a[128:131],v[172:175],v[88:91]
s_waitcnt lgkmcnt(15)
EX v42,v42
MM v[84:87],a[144:147],v[168:171],0
TR a[128:129],v228 offset:0
TR a[130:131],v228 offset:1024
MM v[84:87],a[148:151],v[184:187],v[84:87]
EX v43,v43
MM v[84:87],a[152:155],v[200:203],v[84:87]
TR a[132:133],v228 offset:2048
TR a[134:135],v228 offset:3072
MM v[84:87],a[156:159],v[216:219],v[84:87]
v_add_f32 v14, 1.0, v14
v_add_f32 v15, 1.0, v15
MM v[92:95],a[156:159],v[220:223],0
TR a[136:137],v228 offset:4096
TR a[138:139],v228 offset:5120
MM v[92:95],a[152:155],v[204:207],v[92:95]
v_add_f32 v42, 1.0, v42
v_add_f32 v43, 1.0, v43
MM v[92:95],a[148:151],v[188:191],v[92:95]
s_xor_b32 s92, s49, 2
TR a[140:141],v228 offset:6144
TR a[142:143],v228 offset:7168
MM v[92:95],a[144:147],v[172:175],v[92:95]
s_waitcnt lgkmcnt(15)
s_mul_i32 s92, s92, 17408
s_add_u32 s92, s83, s92
MM v[112:115],a[160:163],v[168:171],0
s_add_u32 s92, s92, 8192
TR a[144:145],v229 offset:0
TR a[146:147],v229 offset:1024
MM v[112:115],a[164:167],v[184:187],v[112:115]
buffer_load_dwordx4 a[24:27], v248, s[20:23], s92 offen offset:0 sc1
MM v[112:115],a[168:171],v[200:203],v[112:115]
TR a[148:149],v229 offset:2048
TR a[150:151],v229 offset:3072
MM v[112:115],a[172:175],v[216:219],v[112:115]
buffer_load_dwordx4 a[56:59], v248, s[20:23], s92 offen offset:1024 sc1
MM v[120:123],a[172:175],v[220:223],0
TR a[152:153],v229 offset:4096
TR a[154:155],v229 offset:5120
MM v[120:123],a[168:171],v[204:207],v[120:123]
buffer_load_dwordx4 a[88:91], v248, s[20:23], s92 offen offset:2048 sc1
MM v[120:123],a[164:167],v[188:191],v[120:123]
TR a[156:157],v229 offset:6144
TR a[158:159],v229 offset:7168
MM v[120:123],a[160:163],v[172:175],v[120:123]
s_waitcnt lgkmcnt(15)
v_rcp_f32 v14, v14
MM v[116:119],a[176:179],v[168:171],0
TR a[160:161],v244 offset:49152
TR a[162:163],v244 offset:51200
MM v[116:119],a[180:183],v[184:187],v[116:119]
buffer_load_dwordx4 a[120:123], v248, s[20:23], s92 offen offset:3072 sc1
MM v[116:119],a[184:187],v[200:203],v[116:119]
s_add_u32 s92, s92, 4096
TR a[164:165],v244 offset:53248
TR a[166:167],v244 offset:55296
MM v[116:119],a[188:191],v[216:219],v[116:119]
buffer_load_dwordx4 a[28:31], v248, s[20:23], s92 offen offset:0 sc1
MM v[124:127],a[188:191],v[220:223],0
TR a[168:169],v244 offset:57344
TR a[170:171],v244 offset:59392
MM v[124:127],a[184:187],v[204:207],v[124:127]
v_rcp_f32 v15, v15
MM v[124:127],a[180:183],v[188:191],v[124:127]
TR a[172:173],v244 offset:61440
TR a[174:175],v244 offset:63488
MM v[124:127],a[176:179],v[172:175],v[124:127]
s_waitcnt lgkmcnt(15)
buffer_load_dwordx4 a[60:63], v248, s[20:23], s92 offen offset:1024 sc1
MM v[144:147],a[128:131],v[168:171],0
TR a[176:177],v235 offset:49152
TR a[178:179],v235 offset:51200
MM v[144:147],a[132:135],v[184:187],v[144:147]
buffer_load_dwordx4 a[92:95], v248, s[20:23], s92 offen offset:2048 sc1
MM v[144:147],a[136:139],v[200:203],v[144:147]
TR a[180:181],v235 offset:53248
TR a[182:183],v235 offset:55296
MM v[144:147],a[140:143],v[216:219],v[144:147]
v_rcp_f32 v42, v42
MM v[152:155],a[140:143],v[220:223],0
TR a[184:185],v235 offset:57344
TR a[186:187],v235 offset:59392
MM v[152:155],a[136:139],v[204:207],v[152:155]
buffer_load_dwordx4 a[124:127], v248, s[20:23], s92 offen offset:3072 sc1
MM v[152:155],a[132:135],v[188:191],v[152:155]
s_xor_b32 s92, s49, 3
TR a[188:189],v235 offset:61440
TR a[190:191],v235 offset:63488
MM v[152:155],a[128:131],v[172:175],v[152:155]
s_waitcnt lgkmcnt(15)
s_mul_i32 s92, s92, 17408
s_add_u32 s92, s83, s92
MM v[148:151],a[144:147],v[168:171],0
s_add_u32 s92, s92, 8192
TR a[128:129],v244 offset:49664
TR a[130:131],v244 offset:51712
MM v[148:151],a[148:151],v[184:187],v[148:151]
buffer_load_dwordx4 a[200:203], v248, s[20:23], s92 offen offset:0 sc1
LPG23_%=:
s_cbranch_execz LTR3_%=
MM v[148:151],a[152:155],v[200:203],v[148:151]
TR a[132:133],v244 offset:53760
TR a[134:135],v244 offset:55808
MM v[148:151],a[156:159],v[216:219],v[148:151]
buffer_load_dwordx4 a[216:219], v248, s[20:23], s92 offen offset:1024 sc1
MM v[156:159],a[156:159],v[220:223],0
TR a[136:137],v244 offset:57856
TR a[138:139],v244 offset:59904
MM v[156:159],a[152:155],v[204:207],v[156:159]
buffer_load_dwordx4 a[232:235], v248, s[20:23], s92 offen offset:2048 sc1
MM v[156:159],a[148:151],v[188:191],v[156:159]
TR a[140:141],v244 offset:61952
TR a[142:143],v244 offset:64000
MM v[156:159],a[144:147],v[172:175],v[156:159]
s_waitcnt lgkmcnt(15)
s_waitcnt vmcnt(11)
v_cmp_eq_u32 s[36:37], s69, v240
MM v[16:19],a[160:163],a[16:19],v[16:19]
TR a[144:145],v235 offset:49664
TR a[146:147],v235 offset:51712
MM v[16:19],a[164:167],a[48:51],v[16:19]
buffer_load_dwordx4 a[248:251], v248, s[20:23], s92 offen offset:3072 sc1
v_cmp_eq_u32 s[38:39], s69, v140
MM v[16:19],a[168:171],a[80:83],v[16:19]
s_add_u32 s92, s92, 4096
TR a[148:149],v235 offset:53760
TR a[150:151],v235 offset:55808
MM v[16:19],a[172:175],a[112:115],v[16:19]
s_nop 0
buffer_load_dwordx4 a[204:207], v248, s[20:23], s92 offen offset:0 sc1
MM v[24:27],a[172:175],a[116:119],v[24:27]
s_and_b32 s36, s36, 0xffff0000
TR a[152:153],v235 offset:57856
TR a[154:155],v235 offset:59904
MM v[24:27],a[168:171],a[84:87],v[24:27]
v_rcp_f32 v43, v43
MM v[24:27],a[164:167],a[52:55],v[24:27]
TR a[156:157],v235 offset:61952
TR a[158:159],v235 offset:64000
MM v[24:27],a[160:163],a[20:23],v[24:27]
s_waitcnt lgkmcnt(15)
buffer_load_dwordx4 a[220:223], v248, s[20:23], s92 offen offset:1024 sc1
MM v[20:23],a[176:179],a[16:19],v[20:23]
TR a[160:161],v244 offset:16384
TR a[162:163],v244 offset:18432
MM v[20:23],a[180:183],a[48:51],v[20:23]
buffer_load_dwordx4 a[236:239], v248, s[20:23], s92 offen offset:2048 sc1
MM v[20:23],a[184:187],a[80:83],v[20:23]
v_mov_b32 v252, s69
s_mov_b64 exec, 1
buffer_store_dword v252, off, s[20:23], s19 offset:4 sc1
s_mov_b64 exec, -1
TR a[164:165],v244 offset:20480
)ASM"
R"ASM(
TR a[166:167],v244 offset:22528
MM v[20:23],a[188:191],a[112:115],v[20:23]
v_mul_f32 v14, v10, v14
v_mul_f32 v15, v11, v15
MM v[28:31],a[188:191],a[116:119],v[28:31]
TR a[168:169],v244 offset:24576
TR a[170:171],v244 offset:26624
MM v[28:31],a[184:187],a[84:87],v[28:31]
buffer_load_dwordx4 a[252:255], v248, s[20:23], s92 offen offset:3072 sc1
MM v[28:31],a[180:183],a[52:55],v[28:31]
TR a[172:173],v244 offset:28672
TR a[174:175],v244 offset:30720
MM v[28:31],a[176:179],a[20:23],v[28:31]
s_waitcnt lgkmcnt(15)
s_add_u32 s66, s16, 0
v_mul_f32 v42, v12, v42
MM v[48:51],a[128:131],a[16:19],v[48:51]
TR a[176:177],v235 offset:16384
TR a[178:179],v235 offset:18432
MM v[48:51],a[132:135],a[48:51],v[48:51]
s_mov_b64 exec, s[36:37]
buffer_load_dwordx4 a[0:3], v250, s[20:23], s66 offen offset:0 sc1
s_mov_b64 exec, -1
MM v[48:51],a[136:139],a[80:83],v[48:51]
s_add_u32 s66, s44, 0
TR a[180:181],v235 offset:20480
TR a[182:183],v235 offset:22528
MM v[48:51],a[140:143],a[112:115],v[48:51]
s_mov_b64 exec, s[38:39]
buffer_load_dwordx4 a[4:7], v250, s[20:23], s66 offen offset:0 sc1
s_mov_b64 exec, -1
MM v[56:59],a[140:143],a[116:119],v[56:59]
s_add_u32 s66, s16, 2048
TR a[184:185],v235 offset:24576
TR a[186:187],v235 offset:26624
MM v[56:59],a[136:139],a[84:87],v[56:59]
s_mov_b64 exec, s[36:37]
buffer_load_dwordx4 a[8:11], v250, s[20:23], s66 offen offset:0 sc1
s_mov_b64 exec, -1
MM v[56:59],a[132:135],a[52:55],v[56:59]
s_add_u32 s66, s44, 2048
TR a[188:189],v235 offset:28672
TR a[190:191],v235 offset:30720
MM v[56:59],a[128:131],a[20:23],v[56:59]
s_waitcnt lgkmcnt(15)
v_mul_f32 v43, v13, v43
CV v4,v14,v15
MM v[52:55],a[144:147],a[16:19],v[52:55]
TR a[128:129],v244 offset:16896
TR a[130:131],v244 offset:18944
MM v[52:55],a[148:151],a[48:51],v[52:55]
s_mov_b64 exec, s[38:39]
buffer_load_dwordx4 a[12:15], v250, s[20:23], s66 offen offset:0 sc1
s_mov_b64 exec, -1
MM v[52:55],a[152:155],a[80:83],v[52:55]
TR a[132:133],v244 offset:20992
TR a[134:135],v244 offset:23040
MM v[52:55],a[156:159],a[112:115],v[52:55]
CV v5,v42,v43
v_lshlrev_b32 v10, 16, v238
MM v[60:63],a[156:159],a[116:119],v[60:63]
TR a[136:137],v244 offset:25088
TR a[138:139],v244 offset:27136
MM v[60:63],a[152:155],a[84:87],v[60:63]
v_and_b32 v11, 0xffff0000, v238
v_lshlrev_b32 v12, 16, v239
MM v[60:63],a[148:151],a[52:55],v[60:63]
TR a[140:141],v244 offset:29184
TR a[142:143],v244 offset:31232
MM v[60:63],a[144:147],a[20:23],v[60:63]
s_waitcnt lgkmcnt(15)
v_and_b32 v13, 0xffff0000, v239
v_mul_f32 v14, 0xbfb8aa3b, v10
MM v[80:83],a[160:163],a[16:19],v[80:83]
TR a[144:145],v235 offset:16896
TR a[146:147],v235 offset:18944
MM v[80:83],a[164:167],a[48:51],v[80:83]
v_mul_f32 v15, 0xbfb8aa3b, v11
v_mul_f32 v42, 0xbfb8aa3b, v12
MM v[80:83],a[168:171],a[80:83],v[80:83]
TR a[148:149],v235 offset:20992
TR a[150:151],v235 offset:23040
MM v[80:83],a[172:175],a[112:115],v[80:83]
v_mul_f32 v43, 0xbfb8aa3b, v13
MM v[88:91],a[172:175],a[116:119],v[88:91]
TR a[152:153],v235 offset:25088
TR a[154:155],v235 offset:27136
MM v[88:91],a[168:171],a[84:87],v[88:91]
EX v14,v14
MM v[88:91],a[164:167],a[52:55],v[88:91]
TR a[156:157],v235 offset:29184
TR a[158:159],v235 offset:31232
MM v[88:91],a[160:163],a[20:23],v[88:91]
s_waitcnt lgkmcnt(15)
EX v15,v15
MM v[84:87],a[176:179],a[16:19],v[84:87]
TR a[160:161],v228 offset:8192
TR a[162:163],v228 offset:9216
MM v[84:87],a[180:183],a[48:51],v[84:87]
EX v42,v42
MM v[84:87],a[184:187],a[80:83],v[84:87]
TR a[164:165],v228 offset:10240
TR a[166:167],v228 offset:11264
MM v[84:87],a[188:191],a[112:115],v[84:87]
EX v43,v43
MM v[92:95],a[188:191],a[116:119],v[92:95]
TR a[168:169],v228 offset:12288
TR a[170:171],v228 offset:13312
MM v[92:95],a[184:187],a[84:87],v[92:95]
v_add_f32 v14, 1.0, v14
v_add_f32 v15, 1.0, v15
MM v[92:95],a[180:183],a[52:55],v[92:95]
TR a[172:173],v228 offset:14336
TR a[174:175],v228 offset:15360
MM v[92:95],a[176:179],a[20:23],v[92:95]
s_waitcnt lgkmcnt(15)
v_add_f32 v42, 1.0, v42
v_add_f32 v43, 1.0, v43
MM v[112:115],a[128:131],a[16:19],v[112:115]
TR a[176:177],v229 offset:8192
TR a[178:179],v229 offset:9216
MM v[112:115],a[132:135],a[48:51],v[112:115]
v_rcp_f32 v14, v14
MM v[112:115],a[136:139],a[80:83],v[112:115]
TR a[180:181],v229 offset:10240
TR a[182:183],v229 offset:11264
MM v[112:115],a[140:143],a[112:115],v[112:115]
buffer_load_dword v241, v253, s[20:23], s68 offen offset:4 sc1
buffer_load_dword v141, v253, s[20:23], s45 offen offset:4 sc1
MM v[120:123],a[140:143],a[116:119],v[120:123]
TR a[184:185],v229 offset:12288
TR a[186:187],v229 offset:13312
MM v[120:123],a[136:139],a[84:87],v[120:123]
v_rcp_f32 v15, v15
MM v[120:123],a[132:135],a[52:55],v[120:123]
TR a[188:189],v229 offset:14336
TR a[190:191],v229 offset:15360
MM v[120:123],a[128:131],a[20:23],v[120:123]
s_waitcnt lgkmcnt(15)
v_rcp_f32 v42, v42
MM v[116:119],a[144:147],a[16:19],v[116:119]
TR a[128:129],v230 offset:0
TR a[130:131],v230 offset:2048
MM v[116:119],a[148:151],a[48:51],v[116:119]
v_rcp_f32 v43, v43
MM v[116:119],a[152:155],a[80:83],v[116:119]
TR a[132:133],v230 offset:4096
TR a[134:135],v230 offset:6144
MM v[116:119],a[156:159],a[112:115],v[116:119]
v_mul_f32 v14, v10, v14
v_mul_f32 v15, v11, v15
MM v[124:127],a[156:159],a[116:119],v[124:127]
TR a[136:137],v230 offset:8192
TR a[138:139],v230 offset:10240
MM v[124:127],a[152:155],a[84:87],v[124:127]
v_mul_f32 v42, v12, v42
v_mul_f32 v43, v13, v43
MM v[124:127],a[148:151],a[52:55],v[124:127]
TR a[140:141],v230 offset:12288
TR a[142:143],v230 offset:14336
MM v[124:127],a[144:147],a[20:23],v[124:127]
s_waitcnt lgkmcnt(15)
CV v6,v14,v15
CV v7,v42,v43
MM v[144:147],a[160:163],a[16:19],v[144:147]
s_add_u32 s80, s18, 0
TR a[144:145],v244 offset:0
TR a[146:147],v244 offset:2048
MM v[144:147],a[164:167],a[48:51],v[144:147]
buffer_load_dwordx2 v[32:33], v251, s[32:35], s80 offen offset:0
MM v[144:147],a[168:171],a[80:83],v[144:147]
s_add_u32 s80, s18, 128
TR a[148:149],v244 offset:4096
TR a[150:151],v244 offset:6144
MM v[144:147],a[172:175],a[112:115],v[144:147]
buffer_load_dwordx2 v[34:35], v251, s[32:35], s80 offen offset:0
MM v[152:155],a[172:175],a[116:119],v[152:155]
s_add_u32 s80, s18, 40960
TR a[152:153],v244 offset:8192
TR a[154:155],v244 offset:10240
MM v[152:155],a[168:171],a[84:87],v[152:155]
buffer_load_dwordx2 v[36:37], v251, s[32:35], s80 offen offset:0
MM v[152:155],a[164:167],a[52:55],v[152:155]
s_add_u32 s80, s18, 41088
TR a[156:157],v244 offset:12288
TR a[158:159],v244 offset:14336
MM v[152:155],a[160:163],a[20:23],v[152:155]
s_waitcnt lgkmcnt(15)
buffer_load_dwordx2 v[38:39], v251, s[32:35], s80 offen offset:0
MM v[148:151],a[176:179],a[16:19],v[148:151]
s_add_u32 s80, s63, 0
TR a[160:161],v231 offset:0
TR a[162:163],v231 offset:2048
MM v[148:151],a[180:183],a[48:51],v[148:151]
buffer_load_dwordx2 v[40:41], v254, s[32:35], s80 offen offset:0
MM v[148:151],a[184:187],a[80:83],v[148:151]
TR a[164:165],v231 offset:4096
TR a[166:167],v231 offset:6144
MM v[148:151],a[188:191],a[112:115],v[148:151]
v_mov_b32 v77, 0x3f80
v_and_b32 v76, 16, v248
MM v[156:159],a[188:191],a[116:119],v[156:159]
TR a[168:169],v231 offset:8192
TR a[170:171],v231 offset:10240
MM v[156:159],a[184:187],a[84:87],v[156:159]
v_lshlrev_b32 v76, v76, v77
s_mov_b32 s28, 0x30003
MM v[156:159],a[180:183],a[52:55],v[156:159]
s_mov_b32 s29, s28
TR a[172:173],v231 offset:12288
TR a[174:175],v231 offset:14336
MM v[156:159],a[176:179],a[20:23],v[156:159]
s_waitcnt lgkmcnt(15)
v_cndmask_b32 v64, 0, v76, s[28:29]
s_lshl_b64 s[28:29], s[28:29], 2
s_waitcnt vmcnt(24)
MM v[16:19],a[128:131],a[24:27],v[16:19]
TR a[176:177],v235 offset:0
TR a[178:179],v235 offset:2048
MM v[16:19],a[132:135],a[56:59],v[16:19]
v_cndmask_b32 v65, 0, v76, s[28:29]
s_lshl_b64 s[28:29], s[28:29], 2
MM v[16:19],a[136:139],a[88:91],v[16:19]
TR a[180:181],v235 offset:4096
TR a[182:183],v235 offset:6144
MM v[16:19],a[140:143],a[120:123],v[16:19]
s_waitcnt vmcnt(20)
v_cndmask_b32 v66, 0, v76, s[28:29]
s_lshl_b64 s[28:29], s[28:29], 2
MM v[24:27],a[140:143],a[124:127],v[24:27]
TR a[184:185],v235 offset:8192
TR a[186:187],v235 offset:10240
MM v[24:27],a[136:139],a[92:95],v[24:27]
v_cndmask_b32 v67, 0, v76, s[28:29]
s_lshl_b64 s[28:29], s[28:29], 2
MM v[24:27],a[132:135],a[60:63],v[24:27]
TR a[188:189],v235 offset:12288
TR a[190:191],v235 offset:14336
MM v[24:27],a[128:131],a[28:31],v[24:27]
s_waitcnt lgkmcnt(15)
v_cndmask_b32 v68, 0, v76, s[28:29]
s_lshl_b64 s[28:29], s[28:29], 2
s_waitcnt vmcnt(16)
MM v[16:19],a[144:147],a[200:203],v[16:19]
TR a[128:129],v230 offset:512
TR a[130:131],v230 offset:2560
MM v[16:19],a[148:151],a[216:219],v[16:19]
v_cndmask_b32 v69, 0, v76, s[28:29]
s_lshl_b64 s[28:29], s[28:29], 2
MM v[16:19],a[152:155],a[232:235],v[16:19]
TR a[132:133],v230 offset:4608
TR a[134:135],v230 offset:6656
MM v[16:19],a[156:159],a[248:251],v[16:19]
s_waitcnt vmcnt(11)
v_cndmask_b32 v70, 0, v76, s[28:29]
s_lshl_b64 s[28:29], s[28:29], 2
MM v[24:27],a[156:159],a[252:255],v[24:27]
TR a[136:137],v230 offset:8704
TR a[138:139],v230 offset:10752
MM v[24:27],a[152:155],a[236:239],v[24:27]
v_cndmask_b32 v71, 0, v76, s[28:29]
MM v[24:27],a[148:151],a[220:223],v[24:27]
TR a[140:141],v230 offset:12800
TR a[142:143],v230 offset:14848
MM v[24:27],a[144:147],a[204:207],v[24:27]
s_waitcnt lgkmcnt(15)
MM v[20:23],a[160:163],a[24:27],v[20:23]
TR a[144:145],v244 offset:512
TR a[146:147],v244 offset:2560
MM v[20:23],a[164:167],a[56:59],v[20:23]
MM v[20:23],a[168:171],a[88:91],v[20:23]
TR a[148:149],v244 offset:4608
TR a[150:151],v244 offset:6656
MM v[20:23],a[172:175],a[120:123],v[20:23]
MM v[28:31],a[172:175],a[124:127],v[28:31]
TR a[152:153],v244 offset:8704
TR a[154:155],v244 offset:10752
MM v[28:31],a[168:171],a[92:95],v[28:31]
MM v[28:31],a[164:167],a[60:63],v[28:31]
TR a[156:157],v244 offset:12800
TR a[158:159],v244 offset:14848
MM v[28:31],a[160:163],a[28:31],v[28:31]
s_waitcnt lgkmcnt(15)
MM v[28:31],a[188:191],a[252:255],v[28:31]
TR a[160:161],v231 offset:512
TR a[162:163],v231 offset:2560
MM v[28:31],a[184:187],a[236:239],v[28:31]
MM v[28:31],a[180:183],a[220:223],v[28:31]
TR a[164:165],v231 offset:4608
TR a[166:167],v231 offset:6656
MM v[28:31],a[176:179],a[204:207],v[28:31]
MM v[20:23],a[176:179],a[200:203],v[20:23]
TR a[168:169],v231 offset:8704
TR a[170:171],v231 offset:10752
MM v[20:23],a[180:183],a[216:219],v[20:23]
MM v[20:23],a[184:187],a[232:235],v[20:23]
TR a[172:173],v231 offset:12800
TR a[174:175],v231 offset:14848
MM v[20:23],a[188:191],a[248:251],v[20:23]
s_waitcnt lgkmcnt(15)
s_waitcnt vmcnt(5)
v_cmp_eq_u32 s[8:9], s69, v241
MM v[48:51],a[128:131],a[24:27],v[48:51]
TR a[176:177],v235 offset:512
TR a[178:179],v235 offset:2560
MM v[48:51],a[132:135],a[56:59],v[48:51]
v_cmp_eq_u32 s[10:11], s69, v141
MM v[48:51],a[136:139],a[88:91],v[48:51]
TR a[180:181],v235 offset:4608
TR a[182:183],v235 offset:6656
MM v[48:51],a[140:143],a[120:123],v[48:51]
s_add_u32 s67, s46, 8192
CV v16,v16,v17
CV v17,v18,v19
MM v[56:59],a[140:143],a[124:127],v[56:59]
s_nop 0
TR a[184:185],v235 offset:8704
TR a[186:187],v235 offset:10752
MM v[56:59],a[136:139],a[92:95],v[56:59]
s_and_b32 s8, s8, 0xffff0000
CV v18,v20,v21
CV v19,v22,v23
MM v[56:59],a[132:135],a[60:63],v[56:59]
s_add_u32 s66, s16, 4096
TR a[188:189],v235 offset:12800
TR a[190:191],v235 offset:14848
MM v[56:59],a[128:131],a[28:31],v[56:59]
s_waitcnt lgkmcnt(15)
buffer_store_dwordx4 v[16:19], v249, s[20:23], s67 offen offset:2048 sc1
MM v[56:59],a[156:159],a[252:255],v[56:59]
s_add_u32 s67, s47, 8192
TR a[128:129],v230 offset:32768
TR a[130:131],v230 offset:34816
MM v[56:59],a[152:155],a[236:239],v[56:59]
s_mov_b64 exec, s[8:9]
buffer_load_dwordx4 a[32:35], v250, s[20:23], s66 offen offset:0 sc1
s_mov_b64 exec, -1
CV v24,v24,v25
CV v25,v26,v27
MM v[56:59],a[148:151],a[220:223],v[56:59]
s_add_u32 s66, s44, 4096
TR a[132:133],v230 offset:36864
TR a[134:135],v230 offset:38912
MM v[56:59],a[144:147],a[204:207],v[56:59]
CV v26,v28,v29
CV v27,v30,v31
MM v[48:51],a[144:147],a[200:203],v[48:51]
TR a[136:137],v230 offset:40960
TR a[138:139],v230 offset:43008
MM v[48:51],a[148:151],a[216:219],v[48:51]
buffer_store_dwordx4 v[24:27], v234, s[20:23], s67 offen offset:3072 sc1
)ASM"
R"ASM(
MM v[48:51],a[152:155],a[232:235],v[48:51]
TR a[140:141],v230 offset:45056
TR a[142:143],v230 offset:47104
MM v[48:51],a[156:159],a[248:251],v[48:51]
s_waitcnt lgkmcnt(15)
ds_bpermute_b32 v16, v245, v16
ds_bpermute_b32 v17, v245, v17
MM v[52:55],a[160:163],a[24:27],v[52:55]
TR a[144:145],v244 offset:32768
TR a[146:147],v244 offset:34816
MM v[52:55],a[164:167],a[56:59],v[52:55]
s_mov_b64 exec, s[10:11]
buffer_load_dwordx4 a[36:39], v250, s[20:23], s66 offen offset:0 sc1
s_mov_b64 exec, -1
ds_bpermute_b32 v18, v245, v18
MM v[52:55],a[168:171],a[88:91],v[52:55]
s_add_u32 s66, s16, 6144
TR a[148:149],v244 offset:36864
TR a[150:151],v244 offset:38912
MM v[52:55],a[172:175],a[120:123],v[52:55]
ds_bpermute_b32 v19, v245, v19
MM v[60:63],a[172:175],a[124:127],v[60:63]
TR a[152:153],v244 offset:40960
TR a[154:155],v244 offset:43008
MM v[60:63],a[168:171],a[92:95],v[60:63]
MM v[60:63],a[164:167],a[60:63],v[60:63]
TR a[156:157],v244 offset:45056
TR a[158:159],v244 offset:47104
MM v[60:63],a[160:163],a[28:31],v[60:63]
s_waitcnt lgkmcnt(15)
s_mov_b64 exec, s[8:9]
buffer_load_dwordx4 a[40:43], v250, s[20:23], s66 offen offset:0 sc1
s_mov_b64 exec, -1
MM v[60:63],a[188:191],a[252:255],v[60:63]
s_add_u32 s66, s44, 6144
TR a[160:161],v231 offset:32768
TR a[162:163],v231 offset:34816
MM v[60:63],a[184:187],a[236:239],v[60:63]
s_waitcnt lgkmcnt(6)
v_or_b32 v19, v104, v19
MM v[60:63],a[180:183],a[220:223],v[60:63]
TR a[164:165],v231 offset:36864
TR a[166:167],v231 offset:38912
MM v[60:63],a[176:179],a[204:207],v[60:63]
MM v[52:55],a[176:179],a[200:203],v[52:55]
TR a[168:169],v231 offset:40960
TR a[170:171],v231 offset:43008
MM v[52:55],a[180:183],a[216:219],v[52:55]
s_mov_b64 exec, s[10:11]
buffer_load_dwordx4 a[44:47], v250, s[20:23], s66 offen offset:0 sc1
s_mov_b64 exec, -1
MM v[52:55],a[184:187],a[232:235],v[52:55]
s_add_u32 s66, s16, 8192
TR a[172:173],v231 offset:45056
TR a[174:175],v231 offset:47104
MM v[52:55],a[188:191],a[248:251],v[52:55]
MM v[80:83],a[128:131],a[24:27],v[80:83]
TR a[176:177],v235 offset:32768
TR a[178:179],v235 offset:34816
MM v[80:83],a[132:135],a[56:59],v[80:83]
v_mov_b32 v252, s69
s_mov_b64 exec, 1
buffer_store_dword v252, off, s[20:23], s19 offset:8 sc1
s_mov_b64 exec, -1
MM v[80:83],a[136:139],a[88:91],v[80:83]
TR a[180:181],v235 offset:36864
TR a[182:183],v235 offset:38912
MM v[80:83],a[140:143],a[120:123],v[80:83]
s_add_u32 s67, s46, 12288
CV v48,v48,v49
CV v49,v50,v51
MM v[88:91],a[140:143],a[124:127],v[88:91]
TR a[184:185],v235 offset:40960
TR a[186:187],v235 offset:43008
MM v[88:91],a[136:139],a[92:95],v[88:91]
s_mov_b64 exec, s[8:9]
buffer_load_dwordx4 a[192:195], v250, s[20:23], s66 offen offset:0 sc1
s_mov_b64 exec, -1
CV v50,v52,v53
CV v51,v54,v55
MM v[88:91],a[132:135],a[60:63],v[88:91]
s_add_u32 s66, s44, 8192
TR a[188:189],v235 offset:45056
TR a[190:191],v235 offset:47104
MM v[88:91],a[128:131],a[28:31],v[88:91]
s_waitcnt lgkmcnt(15)
buffer_store_dwordx4 v[48:51], v249, s[20:23], s67 offen offset:0 sc1
MM v[88:91],a[156:159],a[252:255],v[88:91]
s_add_u32 s67, s47, 12288
TR a[128:129],v230 offset:33280
TR a[130:131],v230 offset:35328
MM v[88:91],a[152:155],a[236:239],v[88:91]
s_waitcnt vmcnt(9)
CV v56,v56,v57
CV v57,v58,v59
MM v[88:91],a[148:151],a[220:223],v[88:91]
TR a[132:133],v230 offset:37376
TR a[134:135],v230 offset:39424
MM v[72:75],v[64:67],a[0:3],0
MM v[72:75],v[64:67],a[4:7],v[72:75]
MM v[72:75],v[68:71],a[8:11],v[72:75]
MM v[72:75],v[68:71],a[12:15],v[72:75]
MM v[88:91],a[144:147],a[204:207],v[88:91]
CV v58,v60,v61
CV v59,v62,v63
MM v[80:83],a[144:147],a[200:203],v[80:83]
TR a[136:137],v230 offset:41472
TR a[138:139],v230 offset:43520
MM v[80:83],a[148:151],a[216:219],v[80:83]
MM v[80:83],a[152:155],a[232:235],v[80:83]
TR a[140:141],v230 offset:45568
TR a[142:143],v230 offset:47616
MM v[80:83],a[156:159],a[248:251],v[80:83]
s_waitcnt lgkmcnt(15)
MM v[84:87],a[160:163],a[24:27],v[84:87]
TR a[144:145],v244 offset:33280
TR a[146:147],v244 offset:35328
MM v[84:87],a[164:167],a[56:59],v[84:87]
buffer_store_dwordx4 v[56:59], v234, s[20:23], s67 offen offset:1024 sc1
MM v[84:87],a[168:171],a[88:91],v[84:87]
TR a[148:149],v244 offset:37376
TR a[150:151],v244 offset:39424
MM v[84:87],a[172:175],a[120:123],v[84:87]
ds_bpermute_b32 v48, v245, v48
ds_bpermute_b32 v49, v245, v49
MM v[92:95],a[172:175],a[124:127],v[92:95]
TR a[152:153],v244 offset:41472
TR a[154:155],v244 offset:43520
MM v[92:95],a[168:171],a[92:95],v[92:95]
ds_bpermute_b32 v50, v245, v50
ds_bpermute_b32 v51, v245, v51
MM v[92:95],a[164:167],a[60:63],v[92:95]
TR a[156:157],v244 offset:45568
TR a[158:159],v244 offset:47616
MM v[92:95],a[160:163],a[28:31],v[92:95]
s_waitcnt lgkmcnt(15)
v_cmp_u_f32 s[30:31], v72, v72
CV v76,v72,v73
MM v[92:95],a[188:191],a[252:255],v[92:95]
TR a[160:161],v231 offset:33280
TR a[162:163],v231 offset:35328
MM v[92:95],a[184:187],a[236:239],v[92:95]
CV v77,v74,v75
MM v[92:95],a[180:183],a[220:223],v[92:95]
TR a[164:165],v231 offset:37376
TR a[166:167],v231 offset:39424
MM v[92:95],a[176:179],a[204:207],v[92:95]
s_waitcnt lgkmcnt(6)
ds_bpermute_b32 v76, v232, v76
v_or_b32 v51, v104, v51
MM v[84:87],a[176:179],a[200:203],v[84:87]
TR a[168:169],v231 offset:41472
TR a[170:171],v231 offset:43520
MM v[84:87],a[180:183],a[216:219],v[84:87]
ds_bpermute_b32 v77, v232, v77
LPG24_%=:
s_cbranch_execz LTR3_%=
v_lshlrev_b32 v10, 16, v0
MM v[84:87],a[184:187],a[232:235],v[84:87]
TR a[172:173],v231 offset:45568
TR a[174:175],v231 offset:47616
MM v[84:87],a[188:191],a[248:251],v[84:87]
v_and_b32 v11, 0xffff0000, v0
v_lshlrev_b32 v12, 16, v1
MM v[112:115],a[128:131],a[24:27],v[112:115]
s_cmp_eq_u32 s82, 0
s_cbranch_scc1 LY1_%=
v_xor_b32 v132, s69, v132
v_xor_b32 v133, s69, v133
v_xor_b32 v134, s69, v134
v_or3_b32 v139, v132, v133, v134
v_cmp_ne_u32 vcc, 0, v139
s_cbranch_vccnz LRYF_%=
LRYFb_%=:
s_ff1_i32_b32 s76, s82
s_and_b32 s76, s76, 7
s_mov_b32 s79, s52
s_cmp_eq_u32 s76, 1
s_cselect_b32 s79, s53, s79
s_cmp_eq_u32 s76, 2
s_cselect_b32 s79, s54, s79
s_cmp_eq_u32 s76, 3
s_cselect_b32 s79, s55, s79
s_cmp_eq_u32 s76, 4
s_cselect_b32 s79, s56, s79
s_cmp_eq_u32 s76, 5
s_cselect_b32 s79, s57, s79
s_cmp_eq_u32 s76, 6
s_cselect_b32 s79, s58, s79
s_cmp_eq_u32 s76, 7
s_cselect_b32 s79, s59, s79
s_mul_i32 s76, s76, 2560
s_mul_i32 s90, s84, 640
s_add_u32 s76, s76, s90
s_add_u32 s76, s76, 21659648
s_lshl_b32 s90, s79, 2
s_add_u32 s90, s90, 53248
buffer_load_dword v138, off, s[20:23], s90 sc1
s_mul_i32 s90, s79, 1280
s_mul_i32 s77, s84, 320
s_add_u32 s90, s90, s77
s_add_u32 s90, s90, 131072
v_lshrrev_b32 v139, 1, v248
s_mov_b32 exec_lo, -1
s_mov_b32 exec_hi, 255
buffer_load_dwordx2 v[136:137], v139, s[20:23], s90 offen offset:0 sc1
s_mov_b32 exec_lo, -1
s_mov_b32 exec_hi, 255
buffer_load_dwordx4 v[160:163], v248, s[20:23], s76 offen offset:0 sc1
s_add_u32 s76, s76, 20480
buffer_load_dwordx4 v[164:167], v248, s[20:23], s76 offen offset:0 sc1
s_add_u32 s76, s76, 20480
buffer_load_dwordx4 v[168:171], v248, s[20:23], s76 offen offset:0 sc1
s_add_u32 s76, s76, 20480
buffer_load_dwordx4 v[172:175], v248, s[20:23], s76 offen offset:0 sc1
s_add_u32 s76, s76, 20480
buffer_load_dwordx4 v[176:179], v248, s[20:23], s76 offen offset:0 sc1
s_add_u32 s76, s76, 20480
buffer_load_dwordx4 v[180:183], v248, s[20:23], s76 offen offset:0 sc1
s_add_u32 s76, s76, 20480
s_mov_b64 exec, -1
LY1_%=:
TR a[176:177],v235 offset:33280
TR a[178:179],v235 offset:35328
MM v[112:115],a[132:135],a[56:59],v[112:115]
s_mov_b64 exec, s[10:11]
buffer_load_dwordx4 a[196:199], v250, s[20:23], s66 offen offset:0 sc1
s_mov_b64 exec, -1
MM v[112:115],a[136:139],a[88:91],v[112:115]
TR a[180:181],v235 offset:37376
TR a[182:183],v235 offset:39424
MM v[112:115],a[140:143],a[120:123],v[112:115]
s_add_u32 s67, s46, 12288
CV v80,v80,v81
CV v81,v82,v83
MM v[120:123],a[140:143],a[124:127],v[120:123]
TR a[184:185],v235 offset:41472
TR a[186:187],v235 offset:43520
MM v[120:123],a[136:139],a[92:95],v[120:123]
CV v82,v84,v85
CV v83,v86,v87
MM v[120:123],a[132:135],a[60:63],v[120:123]
TR a[188:189],v235 offset:45568
TR a[190:191],v235 offset:47616
MM v[120:123],a[128:131],a[28:31],v[120:123]
s_waitcnt lgkmcnt(15)
buffer_store_dwordx4 v[80:83], v249, s[20:23], s67 offen offset:2048 sc1
MM v[120:123],a[156:159],a[252:255],v[120:123]
s_add_u32 s67, s47, 12288
TR a[128:129],v228 offset:16384
TR a[130:131],v228 offset:17408
MM v[120:123],a[152:155],a[236:239],v[120:123]
CV v88,v88,v89
CV v89,v90,v91
MM v[120:123],a[148:151],a[220:223],v[120:123]
TR a[132:133],v228 offset:18432
TR a[134:135],v228 offset:19456
MM v[120:123],a[144:147],a[204:207],v[120:123]
CV v90,v92,v93
CV v91,v94,v95
MM v[112:115],a[144:147],a[200:203],v[112:115]
TR a[136:137],v228 offset:20480
TR a[138:139],v228 offset:21504
MM v[112:115],a[148:151],a[216:219],v[112:115]
v_and_b32 v13, 0xffff0000, v1
v_lshlrev_b32 v14, 16, v32
MM v[112:115],a[152:155],a[232:235],v[112:115]
TR a[140:141],v228 offset:22528
TR a[142:143],v228 offset:23552
MM v[112:115],a[156:159],a[248:251],v[112:115]
s_waitcnt lgkmcnt(15)
buffer_store_dwordx4 v[88:91], v234, s[20:23], s67 offen offset:3072 sc1
MM v[116:119],a[160:163],a[24:27],v[116:119]
TR a[144:145],v228 offset:24576
TR a[146:147],v228 offset:25600
MM v[116:119],a[164:167],a[56:59],v[116:119]
buffer_load_dword v242, v253, s[20:23], s68 offen offset:8 sc1
buffer_load_dword v142, v253, s[20:23], s45 offen offset:8 sc1
MM v[116:119],a[168:171],a[88:91],v[116:119]
TR a[148:149],v228 offset:26624
TR a[150:151],v228 offset:27648
MM v[116:119],a[172:175],a[120:123],v[116:119]
ds_bpermute_b32 v80, v245, v80
ds_bpermute_b32 v81, v245, v81
MM v[124:127],a[172:175],a[124:127],v[124:127]
TR a[152:153],v228 offset:28672
TR a[154:155],v228 offset:29696
MM v[124:127],a[168:171],a[92:95],v[124:127]
ds_bpermute_b32 v82, v245, v82
ds_bpermute_b32 v83, v245, v83
MM v[124:127],a[164:167],a[60:63],v[124:127]
TR a[156:157],v228 offset:30720
TR a[158:159],v228 offset:31744
MM v[124:127],a[160:163],a[28:31],v[124:127]
s_waitcnt lgkmcnt(15)
v_and_b32 v15, 0xffff0000, v32
v_lshlrev_b32 v42, 16, v33
MM v[124:127],a[188:191],a[252:255],v[124:127]
s_cmp_eq_u32 s82, 0
s_cbranch_scc1 LY2_%=
s_mov_b32 exec_lo, -1
s_mov_b32 exec_hi, 255
buffer_load_dwordx4 v[184:187], v248, s[20:23], s76 offen offset:0 sc1
s_add_u32 s76, s76, 20480
buffer_load_dwordx4 v[188:191], v248, s[20:23], s76 offen offset:0 sc1
s_add_u32 s76, s76, 20480
buffer_load_dwordx4 v[192:195], v248, s[20:23], s76 offen offset:0 sc1
s_add_u32 s76, s76, 20480
buffer_load_dwordx4 v[196:199], v248, s[20:23], s76 offen offset:0 sc1
s_add_u32 s76, s76, 20480
buffer_load_dwordx4 v[200:203], v248, s[20:23], s76 offen offset:0 sc1
s_add_u32 s76, s76, 20480
buffer_load_dwordx4 v[204:207], v248, s[20:23], s76 offen offset:0 sc1
s_add_u32 s76, s76, 20480
s_mov_b64 exec, -1
LY2_%=:
TR a[160:161],v229 offset:16384
TR a[162:163],v229 offset:17408
MM v[124:127],a[184:187],a[236:239],v[124:127]
v_and_b32 v43, 0xffff0000, v33
s_add_u32 s80, s18, 0
MM v[124:127],a[180:183],a[220:223],v[124:127]
TR a[164:165],v229 offset:18432
TR a[166:167],v229 offset:19456
MM v[124:127],a[176:179],a[204:207],v[124:127]
s_waitcnt lgkmcnt(6)
v_or_b32 v83, v104, v83
v_lshlrev_b32 v72, 16, v76
MM v[116:119],a[176:179],a[200:203],v[116:119]
TR a[168:169],v229 offset:20480
TR a[170:171],v229 offset:21504
MM v[116:119],a[180:183],a[216:219],v[116:119]
v_and_b32 v73, 0xffff0000, v76
v_lshlrev_b32 v74, 16, v77
MM v[116:119],a[184:187],a[232:235],v[116:119]
TR a[172:173],v229 offset:22528
TR a[174:175],v229 offset:23552
MM v[116:119],a[188:191],a[248:251],v[116:119]
v_and_b32 v75, 0xffff0000, v77
v_mul_f32 v72, v72, v10
MM v[144:147],a[128:131],a[24:27],v[144:147]
TR a[176:177],v229 offset:24576
TR a[178:179],v229 offset:25600
MM v[144:147],a[132:135],a[56:59],v[144:147]
v_mul_f32 v73, v73, v11
v_mul_f32 v74, v74, v12
MM v[144:147],a[136:139],a[88:91],v[144:147]
TR a[180:181],v229 offset:26624
TR a[182:183],v229 offset:27648
MM v[144:147],a[140:143],a[120:123],v[144:147]
s_add_u32 s67, s46, 16384
CV v112,v112,v113
CV v113,v114,v115
MM v[152:155],a[140:143],a[124:127],v[152:155]
)ASM"
R"ASM(
TR a[184:185],v229 offset:28672
TR a[186:187],v229 offset:29696
MM v[152:155],a[136:139],a[92:95],v[152:155]
CV v114,v116,v117
CV v115,v118,v119
MM v[152:155],a[132:135],a[60:63],v[152:155]
TR a[188:189],v229 offset:30720
TR a[190:191],v229 offset:31744
MM v[152:155],a[128:131],a[28:31],v[152:155]
s_waitcnt lgkmcnt(15)
buffer_store_dwordx4 v[112:115], v249, s[20:23], s67 offen offset:0 sc1
MM v[152:155],a[156:159],a[252:255],v[152:155]
s_add_u32 s67, s47, 16384
CV v120,v120,v121
CV v121,v122,v123
MM v[152:155],a[152:155],a[236:239],v[152:155]
CV v122,v124,v125
CV v123,v126,v127
MM v[152:155],a[148:151],a[220:223],v[152:155]
buffer_store_dwordx4 v[120:123], v234, s[20:23], s67 offen offset:1024 sc1
MM v[152:155],a[144:147],a[204:207],v[152:155]
v_mul_f32 v75, v75, v13
CV v76,v72,v73
MM v[144:147],a[144:147],a[200:203],v[144:147]
ds_bpermute_b32 v112, v245, v112
ds_bpermute_b32 v113, v245, v113
MM v[144:147],a[148:151],a[216:219],v[144:147]
ds_bpermute_b32 v114, v245, v114
ds_bpermute_b32 v115, v245, v115
MM v[144:147],a[152:155],a[232:235],v[144:147]
CV v77,v74,v75
v_lshlrev_b32 v72, 16, v76
MM v[144:147],a[156:159],a[248:251],v[144:147]
s_waitcnt lgkmcnt(12)
v_and_b32 v73, 0xffff0000, v76
v_lshlrev_b32 v74, 16, v77
MM v[148:151],a[160:163],a[24:27],v[148:151]
s_cmp_eq_u32 s82, 0
s_cbranch_scc1 LY3_%=
s_mov_b32 exec_lo, -1
s_mov_b32 exec_hi, 255
buffer_load_dwordx4 v[208:211], v248, s[20:23], s76 offen offset:0 sc1
s_add_u32 s76, s76, 20480
buffer_load_dwordx4 v[212:215], v248, s[20:23], s76 offen offset:0 sc1
s_add_u32 s76, s76, 20480
buffer_load_dwordx4 v[216:219], v248, s[20:23], s76 offen offset:0 sc1
s_add_u32 s76, s76, 20480
buffer_load_dwordx4 v[220:223], v248, s[20:23], s76 offen offset:0 sc1
s_add_u32 s76, s76, 20480
buffer_load_dwordx4 v[96:99], v248, s[20:23], s76 offen offset:0 sc1
s_add_u32 s76, s76, 20480
buffer_load_dwordx4 v[100:103], v248, s[20:23], s76 offen offset:0 sc1
s_mov_b64 exec, -1
LY3_%=:
v_and_b32 v75, 0xffff0000, v77
v_add_f32 v72, v72, v14
MM v[148:151],a[164:167],a[56:59],v[148:151]
v_add_f32 v73, v73, v15
v_add_f32 v74, v74, v42
MM v[148:151],a[168:171],a[88:91],v[148:151]
v_add_f32 v75, v75, v43
CV v44,v72,v73
MM v[148:151],a[172:175],a[120:123],v[148:151]
s_waitcnt lgkmcnt(0)
v_or_b32 v115, v104, v115
CV v45,v74,v75
MM v[156:159],a[172:175],a[124:127],v[156:159]
s_cmp_eq_u64 s[30:31], 0
s_cselect_b32 s94, s51, 0
s_and_b32 s94, s94, 1
MM v[156:159],a[168:171],a[92:95],v[156:159]
s_xor_b32 s17, s17, s94
s_cmp_lg_u32 s94, 0
s_cselect_b64 exec, -1, 0
buffer_store_dwordx2 v[44:45], v251, s[40:43], s80 offen offset:0 nt
s_mov_b64 exec, -1
MM v[156:159],a[164:167],a[60:63],v[156:159]
MM v[156:159],a[160:163],a[28:31],v[156:159]
MM v[156:159],a[188:191],a[252:255],v[156:159]
MM v[156:159],a[184:187],a[236:239],v[156:159]
MM v[156:159],a[180:183],a[220:223],v[156:159]
MM v[156:159],a[176:179],a[204:207],v[156:159]
MM v[148:151],a[176:179],a[200:203],v[148:151]
MM v[148:151],a[180:183],a[216:219],v[148:151]
MM v[148:151],a[184:187],a[232:235],v[148:151]
MM v[148:151],a[188:191],a[248:251],v[148:151]
s_nop 5
s_add_u32 s67, s46, 16384
CV v144,v144,v145
CV v145,v146,v147
CV v146,v148,v149
CV v147,v150,v151
buffer_store_dwordx4 v[144:147], v249, s[20:23], s67 offen offset:2048 sc1
s_add_u32 s67, s47, 16384
CV v152,v152,v153
CV v153,v154,v155
CV v154,v156,v157
CV v155,v158,v159
buffer_store_dwordx4 v[152:155], v234, s[20:23], s67 offen offset:3072 sc1
ds_bpermute_b32 v144, v245, v144
ds_bpermute_b32 v145, v245, v145
ds_bpermute_b32 v146, v245, v146
ds_bpermute_b32 v147, v245, v147
s_waitcnt vmcnt(5)
v_cmp_eq_u32 s[12:13], s69, v242
v_cmp_eq_u32 s[14:15], s69, v142
s_nop 0
s_and_b32 s12, s12, 0xffff0000
s_nop 0
s_add_u32 s66, s16, 10240
s_mov_b64 exec, s[12:13]
buffer_load_dwordx4 v[16:19], v250, s[20:23], s66 offen offset:0 sc1
s_mov_b64 exec, -1
s_add_u32 s66, s44, 10240
s_mov_b64 exec, s[14:15]
buffer_load_dwordx4 a[68:71], v250, s[20:23], s66 offen offset:0 sc1
s_mov_b64 exec, -1
s_add_u32 s66, s16, 12288
s_mov_b64 exec, s[12:13]
buffer_load_dwordx4 v[48:51], v250, s[20:23], s66 offen offset:0 sc1
s_mov_b64 exec, -1
s_add_u32 s66, s44, 12288
s_mov_b64 exec, s[14:15]
buffer_load_dwordx4 a[76:79], v250, s[20:23], s66 offen offset:0 sc1
s_mov_b64 exec, -1
s_add_u32 s66, s16, 14336
s_mov_b64 exec, s[12:13]
buffer_load_dwordx4 v[80:83], v250, s[20:23], s66 offen offset:0 sc1
s_mov_b64 exec, -1
s_add_u32 s66, s44, 14336
s_mov_b64 exec, s[14:15]
buffer_load_dwordx4 a[100:103], v250, s[20:23], s66 offen offset:0 sc1
s_mov_b64 exec, -1
s_add_u32 s66, s16, 16384
s_mov_b64 exec, s[12:13]
buffer_load_dwordx4 v[112:115], v250, s[20:23], s66 offen offset:0 sc1
s_mov_b64 exec, -1
s_add_u32 s66, s44, 16384
s_mov_b64 exec, s[14:15]
buffer_load_dwordx4 a[108:111], v250, s[20:23], s66 offen offset:0 sc1
s_mov_b64 exec, -1
s_waitcnt lgkmcnt(0)
v_or_b32 v147, v104, v147
s_add_u32 s66, s16, 18432
s_mov_b64 exec, s[12:13]
buffer_load_dwordx4 v[144:147], v250, s[20:23], s66 offen offset:0 sc1
s_mov_b64 exec, -1
s_add_u32 s66, s44, 18432
s_mov_b64 exec, s[14:15]
buffer_load_dwordx4 a[212:215], v250, s[20:23], s66 offen offset:0 sc1
s_mov_b64 exec, -1
buffer_load_dwordx4 v[240:243], v253, s[20:23], s68 offen offset:0 sc1
buffer_load_dwordx4 v[140:143], v253, s[20:23], s45 offen offset:0 sc1
v_lshlrev_b32 v10, 16, v246
v_and_b32 v11, 0xffff0000, v246
v_lshlrev_b32 v12, 16, v247
v_and_b32 v13, 0xffff0000, v247
v_mul_f32 v14, 0xbfb8aa3b, v10
v_mul_f32 v15, 0xbfb8aa3b, v11
v_mul_f32 v42, 0xbfb8aa3b, v12
v_mul_f32 v43, 0xbfb8aa3b, v13
EX v14,v14
EX v15,v15
EX v42,v42
EX v43,v43
v_add_f32 v14, 1.0, v14
v_add_f32 v15, 1.0, v15
v_add_f32 v42, 1.0, v42
v_add_f32 v43, 1.0, v43
v_rcp_f32 v14, v14
v_rcp_f32 v15, v15
v_rcp_f32 v42, v42
v_rcp_f32 v43, v43
v_mul_f32 v14, v10, v14
v_mul_f32 v15, v11, v15
v_mul_f32 v42, v12, v42
v_mul_f32 v43, v13, v43
CV v8,v14,v15
CV v9,v42,v43
s_cmp_lg_u32 s82, 0
s_cbranch_scc1 LYS_%=
LYE_%=:
s_mov_b32 s73, 30001
s_mov_b32 s65, 1
s_bitcmp0_b32 s17, 0
s_cbranch_scc1 LSA0_%=
MM v[72:75],v[64:67],a[0:3],0
MM v[72:75],v[64:67],a[4:7],v[72:75]
MM v[72:75],v[68:71],a[8:11],v[72:75]
MM v[72:75],v[68:71],a[12:15],v[72:75]
s_nop 7
v_cmp_u_f32 s[30:31], v72, v72
s_nop 1
s_cmp_lg_u64 s[30:31], 0
s_cbranch_scc1 LSA0_%=
s_bitset0_b32 s17, 0
CV v76,v72,v73
CV v77,v74,v75
ds_bpermute_b32 v76, v232, v76
ds_bpermute_b32 v77, v232, v77
v_lshlrev_b32 v10, 16, v0
v_and_b32 v11, 0xffff0000, v0
v_lshlrev_b32 v12, 16, v1
v_and_b32 v13, 0xffff0000, v1
v_lshlrev_b32 v14, 16, v32
v_and_b32 v15, 0xffff0000, v32
v_lshlrev_b32 v42, 16, v33
v_and_b32 v43, 0xffff0000, v33
s_add_u32 s80, s18, 0
s_waitcnt lgkmcnt(0)
v_lshlrev_b32 v72, 16, v76
v_and_b32 v73, 0xffff0000, v76
v_lshlrev_b32 v74, 16, v77
v_and_b32 v75, 0xffff0000, v77
v_mul_f32 v72, v72, v10
v_mul_f32 v73, v73, v11
v_mul_f32 v74, v74, v12
v_mul_f32 v75, v75, v13
CV v76,v72,v73
CV v77,v74,v75
v_lshlrev_b32 v72, 16, v76
v_and_b32 v73, 0xffff0000, v76
v_lshlrev_b32 v74, 16, v77
v_and_b32 v75, 0xffff0000, v77
v_add_f32 v72, v72, v14
v_add_f32 v73, v73, v15
v_add_f32 v74, v74, v42
v_add_f32 v75, v75, v43
CV v44,v72,v73
CV v45,v74,v75
buffer_store_dwordx2 v[44:45], v251, s[40:43], s80 offen offset:0 nt
s_nop 1
LSA0_%=:
s_bitcmp0_b32 s17, 1
s_cbranch_scc1 LSA1_%=
MM v[72:75],v[64:67],a[32:35],0
MM v[72:75],v[64:67],a[36:39],v[72:75]
MM v[72:75],v[68:71],a[40:43],v[72:75]
MM v[72:75],v[68:71],a[44:47],v[72:75]
s_nop 7
v_cmp_u_f32 s[30:31], v72, v72
s_nop 1
s_cmp_lg_u64 s[30:31], 0
s_cbranch_scc1 LSA1_%=
s_bitset0_b32 s17, 1
CV v76,v72,v73
CV v77,v74,v75
ds_bpermute_b32 v76, v232, v76
ds_bpermute_b32 v77, v232, v77
v_lshlrev_b32 v10, 16, v2
v_and_b32 v11, 0xffff0000, v2
v_lshlrev_b32 v12, 16, v3
v_and_b32 v13, 0xffff0000, v3
v_lshlrev_b32 v14, 16, v34
v_and_b32 v15, 0xffff0000, v34
v_lshlrev_b32 v42, 16, v35
v_and_b32 v43, 0xffff0000, v35
s_add_u32 s80, s18, 128
s_waitcnt lgkmcnt(0)
v_lshlrev_b32 v72, 16, v76
v_and_b32 v73, 0xffff0000, v76
v_lshlrev_b32 v74, 16, v77
v_and_b32 v75, 0xffff0000, v77
v_mul_f32 v72, v72, v10
v_mul_f32 v73, v73, v11
v_mul_f32 v74, v74, v12
v_mul_f32 v75, v75, v13
CV v76,v72,v73
CV v77,v74,v75
v_lshlrev_b32 v72, 16, v76
v_and_b32 v73, 0xffff0000, v76
v_lshlrev_b32 v74, 16, v77
v_and_b32 v75, 0xffff0000, v77
v_add_f32 v72, v72, v14
v_add_f32 v73, v73, v15
v_add_f32 v74, v74, v42
v_add_f32 v75, v75, v43
CV v46,v72,v73
CV v47,v74,v75
buffer_store_dwordx2 v[46:47], v251, s[40:43], s80 offen offset:0 nt
s_nop 1
LSA1_%=:
s_waitcnt vmcnt(0)
s_mov_b64 exec, 1
buffer_store_dword v233, off, s[20:23], s19 offset:12 sc1
s_mov_b64 exec, -1
MM v[72:75],v[64:67],a[192:195],0
MM v[100:103],v[64:67],v[80:83],0
MM v[96:99],v[64:67],v[16:19],0
MM v[72:75],v[64:67],a[196:199],v[72:75]
MM v[100:103],v[64:67],a[100:103],v[100:103]
MM v[96:99],v[64:67],a[68:71],v[96:99]
MM v[72:75],v[68:71],v[144:147],v[72:75]
MM v[100:103],v[68:71],v[112:115],v[100:103]
MM v[96:99],v[68:71],v[48:51],v[96:99]
MM v[72:75],v[68:71],a[212:215],v[72:75]
MM v[100:103],v[68:71],a[108:111],v[100:103]
MM v[96:99],v[68:71],a[76:79],v[96:99]
s_mov_b32 s65, 0
s_mov_b32 s90, 250
s_nop 5
v_cmp_u_f32 vcc, v72, v100
v_cmp_u_f32 s[30:31], v96, v96
s_nop 1
s_or_b64 vcc, vcc, s[30:31]
s_cbranch_scc1 LQS_%=
CV v76,v72,v73
CV v77,v74,v75
CV v108,v100,v101
CV v109,v102,v103
CV v78,v96,v97
CV v79,v98,v99
ds_bpermute_b32 v76, v232, v76
ds_bpermute_b32 v77, v232, v77
ds_bpermute_b32 v108, v232, v108
ds_bpermute_b32 v109, v232, v109
ds_bpermute_b32 v78, v232, v78
ds_bpermute_b32 v79, v232, v79
v_lshlrev_b32 v10, 16, v8
v_and_b32 v11, 0xffff0000, v8
v_lshlrev_b32 v12, 16, v9
v_and_b32 v13, 0xffff0000, v9
v_lshlrev_b32 v14, 16, v40
v_and_b32 v15, 0xffff0000, v40
v_lshlrev_b32 v42, 16, v41
v_and_b32 v43, 0xffff0000, v41
s_add_u32 s80, s63, 0
s_waitcnt lgkmcnt(4)
v_lshlrev_b32 v72, 16, v76
v_and_b32 v73, 0xffff0000, v76
v_lshlrev_b32 v74, 16, v77
v_and_b32 v75, 0xffff0000, v77
v_mul_f32 v72, v72, v10
v_mul_f32 v73, v73, v11
v_mul_f32 v74, v74, v12
v_mul_f32 v75, v75, v13
CV v76,v72,v73
CV v77,v74,v75
v_lshlrev_b32 v72, 16, v76
v_and_b32 v73, 0xffff0000, v76
v_lshlrev_b32 v74, 16, v77
v_and_b32 v75, 0xffff0000, v77
v_add_f32 v72, v72, v14
v_add_f32 v73, v73, v15
v_add_f32 v74, v74, v42
v_add_f32 v75, v75, v43
CV v44,v72,v73
CV v45,v74,v75
buffer_store_dwordx2 v[44:45], v254, s[40:43], s80 offen offset:0 nt
v_lshlrev_b32 v10, 16, v6
v_and_b32 v11, 0xffff0000, v6
v_lshlrev_b32 v12, 16, v7
v_and_b32 v13, 0xffff0000, v7
v_lshlrev_b32 v14, 16, v38
v_and_b32 v15, 0xffff0000, v38
v_lshlrev_b32 v42, 16, v39
v_and_b32 v43, 0xffff0000, v39
s_add_u32 s80, s18, 41088
s_waitcnt lgkmcnt(2)
v_lshlrev_b32 v100, 16, v108
v_and_b32 v101, 0xffff0000, v108
v_lshlrev_b32 v102, 16, v109
v_and_b32 v103, 0xffff0000, v109
v_mul_f32 v100, v100, v10
v_mul_f32 v101, v101, v11
v_mul_f32 v102, v102, v12
v_mul_f32 v103, v103, v13
CV v108,v100,v101
CV v109,v102,v103
v_lshlrev_b32 v100, 16, v108
v_and_b32 v101, 0xffff0000, v108
v_lshlrev_b32 v102, 16, v109
v_and_b32 v103, 0xffff0000, v109
v_add_f32 v100, v100, v14
v_add_f32 v101, v101, v15
v_add_f32 v102, v102, v42
v_add_f32 v103, v103, v43
CV v46,v100,v101
CV v47,v102,v103
buffer_store_dwordx2 v[46:47], v251, s[40:43], s80 offen offset:0 nt
v_lshlrev_b32 v10, 16, v4
v_and_b32 v11, 0xffff0000, v4
v_lshlrev_b32 v12, 16, v5
v_and_b32 v13, 0xffff0000, v5
v_lshlrev_b32 v14, 16, v36
v_and_b32 v15, 0xffff0000, v36
v_lshlrev_b32 v42, 16, v37
v_and_b32 v43, 0xffff0000, v37
s_add_u32 s80, s18, 40960
s_waitcnt lgkmcnt(0)
v_lshlrev_b32 v96, 16, v78
v_and_b32 v97, 0xffff0000, v78
v_lshlrev_b32 v98, 16, v79
v_and_b32 v99, 0xffff0000, v79
v_mul_f32 v96, v96, v10
v_mul_f32 v97, v97, v11
v_mul_f32 v98, v98, v12
v_mul_f32 v99, v99, v13
CV v78,v96,v97
CV v79,v98,v99
v_lshlrev_b32 v96, 16, v78
v_and_b32 v97, 0xffff0000, v78
v_lshlrev_b32 v98, 16, v79
v_and_b32 v99, 0xffff0000, v79
v_add_f32 v96, v96, v14
v_add_f32 v97, v97, v15
v_add_f32 v98, v98, v42
v_add_f32 v99, v99, v43
CV v44,v96,v97
CV v45,v98,v99
buffer_store_dwordx2 v[44:45], v251, s[40:43], s80 offen offset:0 nt
s_and_b32 s17, s17, 3
)ASM"
R"ASM(
s_cbranch_scc0 LQE_%=
LQS_%=:
v_cmp_eq_u32 s[12:13], s69, v242
v_cmp_eq_u32 s[14:15], s69, v142
v_cmp_eq_u32 s[8:9], s69, v241
v_cmp_eq_u32 s[10:11], s69, v141
v_cmp_eq_u32 s[36:37], s69, v240
v_cmp_eq_u32 s[38:39], s69, v140
s_mov_b32 s94, 0
s_sub_u32 s90, s90, 1
s_cbranch_scc1 LSW_%=
s_and_b64 vcc, s[12:13], s[14:15]
s_and_b32 s12, s12, 0xffff0000
s_and_b32 s8, s8, 0xffff0000
s_and_b32 s36, s36, 0xffff0000
s_bitcmp0_b32 s17, 4
s_cbranch_scc1 LQG4_%=
AR v108,a195
AR v109,a199
AR v111,a215
v_cmp_eq_u32 s[24:25], -1, v108
v_cmp_eq_u32 s[26:27], -1, v109
v_cmp_eq_u32 s[28:29], -1, v147
v_cmp_eq_u32 s[46:47], -1, v111
s_nop 0
s_or_b64 s[30:31], s[24:25], s[26:27]
s_or_b64 s[30:31], s[30:31], s[28:29]
s_or_b64 s[30:31], s[30:31], s[46:47]
s_cselect_b32 s76, 256, 16
s_or_b32 s94, s94, s76
s_bitcmp1_b32 s94, 4
s_cbranch_scc1 LQG4_%=
s_nop 0
s_nop 0
s_nop 0
s_nop 0
s_nop 0
LPG25_%=:
s_cbranch_execz LTR3_%=
s_and_b64 exec, s[46:47], s[14:15]
s_cbranch_scc0 LQK43_%=
s_add_u32 s66, s44, 18432
buffer_load_dwordx4 a[212:215], v250, s[20:23], s66 offen offset:0 sc1
LQK43_%=:
s_and_b64 exec, s[28:29], s[12:13]
s_cbranch_scc0 LQK42_%=
s_add_u32 s66, s16, 18432
buffer_load_dwordx4 v[144:147], v250, s[20:23], s66 offen offset:0 sc1
LQK42_%=:
s_and_b64 exec, s[26:27], s[10:11]
s_cbranch_scc0 LQK41_%=
s_add_u32 s66, s44, 8192
buffer_load_dwordx4 a[196:199], v250, s[20:23], s66 offen offset:0 sc1
LQK41_%=:
s_and_b64 exec, s[24:25], s[8:9]
s_cbranch_scc0 LQK40_%=
s_add_u32 s66, s16, 8192
buffer_load_dwordx4 a[192:195], v250, s[20:23], s66 offen offset:0 sc1
LQK40_%=:
s_mov_b64 exec, -1
LQG4_%=:
s_bitcmp0_b32 s17, 3
s_cbranch_scc1 LQG3_%=
AR v109,a103
AR v111,a111
v_cmp_eq_u32 s[24:25], -1, v83
v_cmp_eq_u32 s[26:27], -1, v109
v_cmp_eq_u32 s[28:29], -1, v115
v_cmp_eq_u32 s[46:47], -1, v111
s_nop 0
s_or_b64 s[30:31], s[24:25], s[26:27]
s_or_b64 s[30:31], s[30:31], s[28:29]
s_or_b64 s[30:31], s[30:31], s[46:47]
s_cselect_b32 s76, 256, 8
s_or_b32 s94, s94, s76
s_bitcmp1_b32 s94, 3
s_cbranch_scc1 LQG3_%=
s_and_b64 exec, s[46:47], s[14:15]
s_cbranch_scc0 LQK33_%=
s_add_u32 s66, s44, 16384
buffer_load_dwordx4 a[108:111], v250, s[20:23], s66 offen offset:0 sc1
LQK33_%=:
s_and_b64 exec, s[28:29], s[12:13]
s_cbranch_scc0 LQK32_%=
s_add_u32 s66, s16, 16384
buffer_load_dwordx4 v[112:115], v250, s[20:23], s66 offen offset:0 sc1
LQK32_%=:
s_and_b64 exec, s[26:27], s[14:15]
s_cbranch_scc0 LQK31_%=
s_add_u32 s66, s44, 14336
buffer_load_dwordx4 a[100:103], v250, s[20:23], s66 offen offset:0 sc1
LQK31_%=:
s_and_b64 exec, s[24:25], s[12:13]
s_cbranch_scc0 LQK30_%=
s_add_u32 s66, s16, 14336
buffer_load_dwordx4 v[80:83], v250, s[20:23], s66 offen offset:0 sc1
LQK30_%=:
s_mov_b64 exec, -1
LQG3_%=:
s_bitcmp0_b32 s17, 2
s_cbranch_scc1 LQG2_%=
AR v109,a71
AR v111,a79
v_cmp_eq_u32 s[24:25], -1, v19
v_cmp_eq_u32 s[26:27], -1, v109
v_cmp_eq_u32 s[28:29], -1, v51
v_cmp_eq_u32 s[46:47], -1, v111
s_nop 0
s_or_b64 s[30:31], s[24:25], s[26:27]
s_or_b64 s[30:31], s[30:31], s[28:29]
s_or_b64 s[30:31], s[30:31], s[46:47]
s_cselect_b32 s76, 256, 4
s_or_b32 s94, s94, s76
s_bitcmp1_b32 s94, 2
s_cbranch_scc1 LQG2_%=
s_and_b64 exec, s[46:47], s[14:15]
s_cbranch_scc0 LQK23_%=
s_add_u32 s66, s44, 12288
buffer_load_dwordx4 a[76:79], v250, s[20:23], s66 offen offset:0 sc1
LQK23_%=:
s_and_b64 exec, s[28:29], s[12:13]
s_cbranch_scc0 LQK22_%=
s_add_u32 s66, s16, 12288
buffer_load_dwordx4 v[48:51], v250, s[20:23], s66 offen offset:0 sc1
LQK22_%=:
s_and_b64 exec, s[26:27], s[14:15]
s_cbranch_scc0 LQK21_%=
s_add_u32 s66, s44, 10240
buffer_load_dwordx4 a[68:71], v250, s[20:23], s66 offen offset:0 sc1
LQK21_%=:
s_and_b64 exec, s[24:25], s[12:13]
s_cbranch_scc0 LQK20_%=
s_add_u32 s66, s16, 10240
buffer_load_dwordx4 v[16:19], v250, s[20:23], s66 offen offset:0 sc1
LQK20_%=:
s_mov_b64 exec, -1
LQG2_%=:
s_and_b32 s76, s17, 3
s_cbranch_scc1 LQA_%=
LQB_%=:
s_cmp_eq_u64 vcc, -1
s_cbranch_scc1 LQF_%=
buffer_load_dwordx4 v[240:243], v253, s[20:23], s68 offen offset:0 sc1
buffer_load_dwordx4 v[140:143], v253, s[20:23], s45 offen offset:0 sc1
LQF_%=:
s_and_b32 s76, s94, 3
s_cbranch_scc1 LQC_%=
LQD_%=:
s_bitcmp0_b32 s94, 2
s_cbranch_scc1 LQN2_%=
MM v[72:75],v[64:67],v[16:19],0
MM v[72:75],v[64:67],a[68:71],v[72:75]
MM v[72:75],v[68:71],v[48:51],v[72:75]
MM v[72:75],v[68:71],a[76:79],v[72:75]
s_bitset0_b32 s17, 2
s_nop 6
CV v76,v72,v73
CV v77,v74,v75
ds_bpermute_b32 v76, v232, v76
ds_bpermute_b32 v77, v232, v77
v_lshlrev_b32 v10, 16, v4
v_and_b32 v11, 0xffff0000, v4
v_lshlrev_b32 v12, 16, v5
v_and_b32 v13, 0xffff0000, v5
v_lshlrev_b32 v14, 16, v36
v_and_b32 v15, 0xffff0000, v36
v_lshlrev_b32 v42, 16, v37
v_and_b32 v43, 0xffff0000, v37
s_add_u32 s80, s18, 40960
s_waitcnt lgkmcnt(0)
v_lshlrev_b32 v72, 16, v76
v_and_b32 v73, 0xffff0000, v76
v_lshlrev_b32 v74, 16, v77
v_and_b32 v75, 0xffff0000, v77
v_mul_f32 v72, v72, v10
v_mul_f32 v73, v73, v11
v_mul_f32 v74, v74, v12
v_mul_f32 v75, v75, v13
CV v76,v72,v73
CV v77,v74,v75
v_lshlrev_b32 v72, 16, v76
v_and_b32 v73, 0xffff0000, v76
v_lshlrev_b32 v74, 16, v77
v_and_b32 v75, 0xffff0000, v77
v_add_f32 v72, v72, v14
v_add_f32 v73, v73, v15
v_add_f32 v74, v74, v42
v_add_f32 v75, v75, v43
CV v44,v72,v73
CV v45,v74,v75
buffer_store_dwordx2 v[44:45], v251, s[40:43], s80 offen offset:0 nt
s_bitcmp0_b32 s94, 8
s_cbranch_scc1 LQN2_%=
s_waitcnt vmcnt(1)
s_branch LQS_%=
LQN2_%=:
s_bitcmp0_b32 s94, 3
s_cbranch_scc1 LQN3_%=
MM v[72:75],v[64:67],v[80:83],0
MM v[72:75],v[64:67],a[100:103],v[72:75]
MM v[72:75],v[68:71],v[112:115],v[72:75]
MM v[72:75],v[68:71],a[108:111],v[72:75]
s_bitset0_b32 s17, 3
s_nop 6
CV v76,v72,v73
CV v77,v74,v75
ds_bpermute_b32 v76, v232, v76
ds_bpermute_b32 v77, v232, v77
v_lshlrev_b32 v10, 16, v6
v_and_b32 v11, 0xffff0000, v6
v_lshlrev_b32 v12, 16, v7
v_and_b32 v13, 0xffff0000, v7
v_lshlrev_b32 v14, 16, v38
v_and_b32 v15, 0xffff0000, v38
v_lshlrev_b32 v42, 16, v39
v_and_b32 v43, 0xffff0000, v39
s_add_u32 s80, s18, 41088
s_waitcnt lgkmcnt(0)
v_lshlrev_b32 v72, 16, v76
v_and_b32 v73, 0xffff0000, v76
v_lshlrev_b32 v74, 16, v77
v_and_b32 v75, 0xffff0000, v77
v_mul_f32 v72, v72, v10
v_mul_f32 v73, v73, v11
v_mul_f32 v74, v74, v12
v_mul_f32 v75, v75, v13
CV v76,v72,v73
CV v77,v74,v75
v_lshlrev_b32 v72, 16, v76
v_and_b32 v73, 0xffff0000, v76
v_lshlrev_b32 v74, 16, v77
v_and_b32 v75, 0xffff0000, v77
v_add_f32 v72, v72, v14
v_add_f32 v73, v73, v15
v_add_f32 v74, v74, v42
v_add_f32 v75, v75, v43
CV v46,v72,v73
CV v47,v74,v75
buffer_store_dwordx2 v[46:47], v251, s[40:43], s80 offen offset:0 nt
s_bitcmp0_b32 s94, 8
s_cbranch_scc1 LQN3_%=
s_waitcnt vmcnt(1)
s_branch LQS_%=
LQN3_%=:
s_bitcmp0_b32 s94, 4
s_cbranch_scc1 LQN4_%=
MM v[72:75],v[64:67],a[192:195],0
MM v[72:75],v[64:67],a[196:199],v[72:75]
MM v[72:75],v[68:71],v[144:147],v[72:75]
MM v[72:75],v[68:71],a[212:215],v[72:75]
s_bitset0_b32 s17, 4
s_nop 6
CV v76,v72,v73
CV v77,v74,v75
ds_bpermute_b32 v76, v232, v76
ds_bpermute_b32 v77, v232, v77
v_lshlrev_b32 v10, 16, v8
v_and_b32 v11, 0xffff0000, v8
v_lshlrev_b32 v12, 16, v9
v_and_b32 v13, 0xffff0000, v9
v_lshlrev_b32 v14, 16, v40
v_and_b32 v15, 0xffff0000, v40
v_lshlrev_b32 v42, 16, v41
v_and_b32 v43, 0xffff0000, v41
s_add_u32 s80, s63, 0
s_waitcnt lgkmcnt(0)
v_lshlrev_b32 v72, 16, v76
v_and_b32 v73, 0xffff0000, v76
v_lshlrev_b32 v74, 16, v77
v_and_b32 v75, 0xffff0000, v77
v_mul_f32 v72, v72, v10
v_mul_f32 v73, v73, v11
v_mul_f32 v74, v74, v12
v_mul_f32 v75, v75, v13
CV v76,v72,v73
CV v77,v74,v75
v_lshlrev_b32 v72, 16, v76
v_and_b32 v73, 0xffff0000, v76
v_lshlrev_b32 v74, 16, v77
v_and_b32 v75, 0xffff0000, v77
v_add_f32 v72, v72, v14
v_add_f32 v73, v73, v15
v_add_f32 v74, v74, v42
v_add_f32 v75, v75, v43
CV v44,v72,v73
CV v45,v74,v75
buffer_store_dwordx2 v[44:45], v254, s[40:43], s80 offen offset:0 nt
s_bitcmp0_b32 s94, 8
s_cbranch_scc1 LQN4_%=
s_waitcnt vmcnt(1)
s_branch LQS_%=
LQN4_%=:
s_cmp_eq_u32 s17, 0
s_cbranch_scc1 LQE_%=
s_waitcnt vmcnt(0)
s_branch LQS_%=
LQE_%=:
s_endpgm
LYS_%=:
s_waitcnt vmcnt(15)
LYT_%=:
v_ashrrev_i32 v139, 31, v138
v_not_b32 v139, v139
v_or_b32 v139, 0x80000000, v139
v_xor_b32 v138, v138, v139
v_and_b32 v138, 0xffff0000, v138
v_mul_f32 v138, 0xbfb8aa3b, v138
EX v138,v138
v_pk_add_f32 v[160:161], v[160:161], v[196:197]
v_pk_add_f32 v[162:163], v[162:163], v[198:199]
v_pk_add_f32 v[164:165], v[164:165], v[200:201]
v_pk_add_f32 v[166:167], v[166:167], v[202:203]
v_pk_add_f32 v[168:169], v[168:169], v[204:205]
v_pk_add_f32 v[170:171], v[170:171], v[206:207]
v_pk_add_f32 v[172:173], v[172:173], v[208:209]
v_pk_add_f32 v[174:175], v[174:175], v[210:211]
v_pk_add_f32 v[176:177], v[176:177], v[212:213]
v_pk_add_f32 v[178:179], v[178:179], v[214:215]
v_pk_add_f32 v[180:181], v[180:181], v[216:217]
v_pk_add_f32 v[182:183], v[182:183], v[218:219]
v_pk_add_f32 v[184:185], v[184:185], v[220:221]
v_pk_add_f32 v[186:187], v[186:187], v[222:223]
v_pk_add_f32 v[188:189], v[188:189], v[96:97]
v_pk_add_f32 v[190:191], v[190:191], v[98:99]
v_pk_add_f32 v[192:193], v[192:193], v[100:101]
v_pk_add_f32 v[194:195], v[194:195], v[102:103]
v_pk_add_f32 v[160:161], v[160:161], v[192:193]
v_pk_add_f32 v[162:163], v[162:163], v[194:195]
v_add_f32 v138, 1.0, v138
v_rcp_f32 v138, v138
v_pk_add_f32 v[160:161], v[160:161], v[176:177]
v_pk_add_f32 v[162:163], v[162:163], v[178:179]
v_pk_add_f32 v[164:165], v[164:165], v[180:181]
v_pk_add_f32 v[166:167], v[166:167], v[182:183]
v_pk_add_f32 v[168:169], v[168:169], v[184:185]
v_pk_add_f32 v[170:171], v[170:171], v[186:187]
v_pk_add_f32 v[172:173], v[172:173], v[188:189]
v_pk_add_f32 v[174:175], v[174:175], v[190:191]
CV v138,v138,v138
v_and_b32 v138, 0xffff0000, v138
v_pk_add_f32 v[160:161], v[160:161], v[168:169]
v_pk_add_f32 v[162:163], v[162:163], v[170:171]
v_pk_add_f32 v[164:165], v[164:165], v[172:173]
v_pk_add_f32 v[166:167], v[166:167], v[174:175]
v_pk_add_f32 v[160:161], v[160:161], v[164:165]
v_pk_add_f32 v[162:163], v[162:163], v[166:167]
CV v164,v160,v161
CV v165,v162,v163
v_lshlrev_b32 v160, 16, v164
v_and_b32 v161, 0xffff0000, v164
v_lshlrev_b32 v162, 16, v165
v_and_b32 v163, 0xffff0000, v165
v_mul_f32 v160, v160, v138
v_mul_f32 v161, v161, v138
v_mul_f32 v162, v162, v138
v_mul_f32 v163, v163, v138
CV v164,v160,v161
CV v165,v162,v163
v_lshlrev_b32 v160, 16, v164
v_and_b32 v161, 0xffff0000, v164
v_lshlrev_b32 v162, 16, v165
v_and_b32 v163, 0xffff0000, v165
v_lshlrev_b32 v168, 16, v136
v_and_b32 v169, 0xffff0000, v136
v_lshlrev_b32 v170, 16, v137
v_and_b32 v171, 0xffff0000, v137
v_add_f32 v160, v160, v168
v_add_f32 v161, v161, v169
v_add_f32 v162, v162, v170
v_add_f32 v163, v163, v171
CV v164,v160,v161
CV v165,v162,v163
v_lshlrev_b32 v160, 16, v164
v_and_b32 v161, 0xffff0000, v164
v_lshlrev_b32 v162, 16, v165
v_and_b32 v163, 0xffff0000, v165
v_mul_f32 v168, 0xbfb8aa3b, v160
v_mul_f32 v169, 0xbfb8aa3b, v161
v_mul_f32 v170, 0xbfb8aa3b, v162
v_mul_f32 v171, 0xbfb8aa3b, v163
EX v168,v168
EX v169,v169
EX v170,v170
EX v171,v171
v_bfe_u32 v139, v248, 2, 6
v_and_b32 v132, 28, v139
v_add_f32 v168, 1.0, v168
v_add_f32 v169, 1.0, v169
v_add_f32 v170, 1.0, v170
v_add_f32 v171, 1.0, v171
v_rcp_f32 v168, v168
v_rcp_f32 v169, v169
v_rcp_f32 v170, v170
v_rcp_f32 v171, v171
s_and_b32 s77, s84, 1
s_lshl_b32 s76, s77, 5
v_add_u32 v139, s76, v139
s_xor_b32 s77, s77, 1
s_lshl_b32 s77, s77, 7
v_add_u32 v132, s77, v132
v_mul_f32 v168, v160, v168
v_mul_f32 v169, v161, v169
v_mul_f32 v170, v162, v170
v_mul_f32 v171, v163, v171
CV v172,v168,v169
CV v173,v170,v171
ds_bpermute_b32 v176, v139, v172 offset:0
ds_bpermute_b32 v177, v139, v173 offset:0
ds_bpermute_b32 v180, v139, v172 offset:64
ds_bpermute_b32 v181, v139, v173 offset:64
ds_bpermute_b32 v184, v132, v172 offset:0
ds_bpermute_b32 v185, v132, v173 offset:0
s_ff1_i32_b32 s76, s82
s_bitset0_b32 s82, s76
s_and_b32 s77, s79, 3
s_lshl_b32 s77, s77, 4
s_and_b32 s76, s76, 8
s_waitcnt lgkmcnt(0)
s_lshl_b64 vcc, 0xffff, s77
s_cmp_eq_u32 s76, 0
s_cselect_b64 exec, vcc, 0
v_mov_b32 v0, v176
v_mov_b32 v1, v177
v_mov_b32 v2, v180
v_mov_b32 v3, v181
s_cselect_b64 exec, 0, vcc
v_mov_b32 v4, v176
v_mov_b32 v5, v177
v_mov_b32 v6, v180
v_mov_b32 v7, v181
)ASM"
R"ASM(
s_add_u32 s77, s77, s76
s_lshl_b64 exec, 0xff, s77
v_mov_b32 v8, v184
v_mov_b32 v9, v185
s_mov_b64 exec, -1
s_cmp_eq_u32 s82, 0
s_cbranch_scc1 LYE_%=
s_ff1_i32_b32 s76, s82
s_and_b32 s76, s76, 7
s_mov_b32 s79, s52
s_cmp_eq_u32 s76, 1
s_cselect_b32 s79, s53, s79
s_cmp_eq_u32 s76, 2
s_cselect_b32 s79, s54, s79
s_cmp_eq_u32 s76, 3
s_cselect_b32 s79, s55, s79
s_cmp_eq_u32 s76, 4
s_cselect_b32 s79, s56, s79
s_cmp_eq_u32 s76, 5
s_cselect_b32 s79, s57, s79
s_cmp_eq_u32 s76, 6
s_cselect_b32 s79, s58, s79
s_cmp_eq_u32 s76, 7
s_cselect_b32 s79, s59, s79
s_mul_i32 s76, s76, 2560
s_mul_i32 s90, s84, 640
s_add_u32 s76, s76, s90
s_add_u32 s76, s76, 21659648
s_lshl_b32 s90, s79, 2
s_add_u32 s90, s90, 53248
buffer_load_dword v138, off, s[20:23], s90 sc1
s_mul_i32 s90, s79, 1280
s_mul_i32 s77, s84, 320
s_add_u32 s90, s90, s77
s_add_u32 s90, s90, 131072
v_lshrrev_b32 v139, 1, v248
s_mov_b32 exec_lo, -1
s_mov_b32 exec_hi, 255
buffer_load_dwordx2 v[136:137], v139, s[20:23], s90 offen offset:0 sc1
s_mov_b32 exec_lo, -1
s_mov_b32 exec_hi, 255
buffer_load_dwordx4 v[160:163], v248, s[20:23], s76 offen offset:0 sc1
s_add_u32 s76, s76, 20480
buffer_load_dwordx4 v[164:167], v248, s[20:23], s76 offen offset:0 sc1
s_add_u32 s76, s76, 20480
buffer_load_dwordx4 v[168:171], v248, s[20:23], s76 offen offset:0 sc1
s_add_u32 s76, s76, 20480
buffer_load_dwordx4 v[172:175], v248, s[20:23], s76 offen offset:0 sc1
s_add_u32 s76, s76, 20480
buffer_load_dwordx4 v[176:179], v248, s[20:23], s76 offen offset:0 sc1
s_add_u32 s76, s76, 20480
buffer_load_dwordx4 v[180:183], v248, s[20:23], s76 offen offset:0 sc1
s_add_u32 s76, s76, 20480
buffer_load_dwordx4 v[184:187], v248, s[20:23], s76 offen offset:0 sc1
s_add_u32 s76, s76, 20480
buffer_load_dwordx4 v[188:191], v248, s[20:23], s76 offen offset:0 sc1
s_add_u32 s76, s76, 20480
buffer_load_dwordx4 v[192:195], v248, s[20:23], s76 offen offset:0 sc1
s_add_u32 s76, s76, 20480
buffer_load_dwordx4 v[196:199], v248, s[20:23], s76 offen offset:0 sc1
s_add_u32 s76, s76, 20480
buffer_load_dwordx4 v[200:203], v248, s[20:23], s76 offen offset:0 sc1
s_add_u32 s76, s76, 20480
buffer_load_dwordx4 v[204:207], v248, s[20:23], s76 offen offset:0 sc1
s_add_u32 s76, s76, 20480
buffer_load_dwordx4 v[208:211], v248, s[20:23], s76 offen offset:0 sc1
s_add_u32 s76, s76, 20480
buffer_load_dwordx4 v[212:215], v248, s[20:23], s76 offen offset:0 sc1
s_add_u32 s76, s76, 20480
buffer_load_dwordx4 v[216:219], v248, s[20:23], s76 offen offset:0 sc1
s_add_u32 s76, s76, 20480
buffer_load_dwordx4 v[220:223], v248, s[20:23], s76 offen offset:0 sc1
s_add_u32 s76, s76, 20480
buffer_load_dwordx4 v[96:99], v248, s[20:23], s76 offen offset:0 sc1
s_add_u32 s76, s76, 20480
buffer_load_dwordx4 v[100:103], v248, s[20:23], s76 offen offset:0 sc1
s_mov_b64 exec, -1
s_waitcnt vmcnt(0)
s_branch LYT_%=
.fill 358, 4, 0xbf800000
LXP_%=:
LPG26_%=:
s_cbranch_execz LTR1_%=
v_mov_b32 v250, s69
s_mov_b32 s76, 32768
s_mov_b64 exec, 1
buffer_store_dword v250, off, s[20:23], s76 sc1
s_mov_b64 exec, -1
s_add_u32 s79, %[slds], 154112
s_lshl_b32 s80, %[swave], 2
MM v[64:67],a[192:195],v[160:163],v[128:131]
s_add_u32 s64, s64, 16384
TR a[128:129],v232 offset:0
s_add_u32 s81, s64, 896
MM v[64:67],a[196:199],v[176:179],v[64:67]
TR a[130:131],v232 offset:4096
TR a[132:133],v232 offset:8192
MM v[64:67],a[200:203],v[192:195],v[64:67]
TR a[134:135],v232 offset:12288
TR a[136:137],v233 offset:0
MM v[64:67],a[204:207],v[208:211],v[64:67]
TR a[138:139],v233 offset:4096
v_add_f32 v246, v236, v237
MM v[68:71],a[220:223],v[208:211],v[128:131]
TR a[140:141],v233 offset:8192
v_cmp_nge_f32 vcc, s71, v246
MM v[68:71],a[216:219],v[192:195],v[68:71]
s_cbranch_vccnz LSITE4_%=
LBACK4_%=:
TR a[142:143],v233 offset:12288
TR a[144:145],v232 offset:512
MM v[68:71],a[212:215],v[176:179],v[68:71]
s_mov_b32 m0, s67
TR a[146:147],v232 offset:4608
TR a[148:149],v232 offset:8704
MM v[68:71],a[208:211],v[160:163],v[68:71]
buffer_load_dwordx4 v252, s[36:39], s66 offen offset:0 lds
MM v[76:79],a[208:211],v[164:167],v[132:135]
s_add_u32 s66, s66, 55296
EX v64,v64
MM v[76:79],a[212:215],v[180:183],v[76:79]
s_min_u32 s66, s66, s85
EX v65,v65
MM v[76:79],a[216:219],v[196:199],v[76:79]
s_add_u32 s67, s67, 1024
TR a[150:151],v232 offset:12800
CV v64,v64,v65
MM v[76:79],a[220:223],v[212:215],v[76:79]
s_min_u32 s67, s67, s86
EX v66,v66
MM v[72:75],a[204:207],v[212:215],v[132:135]
EX v67,v67
MM v[72:75],a[200:203],v[196:199],v[72:75]
TR a[152:153],v233 offset:512
CV v65,v66,v67
MM v[72:75],a[196:199],v[180:183],v[72:75]
EX v68,v68
MM v[72:75],a[192:195],v[164:167],v[72:75]
EX v69,v69
MM v[80:83],a[192:195],v[168:171],v[136:139]
TR a[154:155],v233 offset:4608
CV v66,v68,v69
MM v[80:83],a[196:199],v[184:187],v[80:83]
EX v70,v70
MM v[80:83],a[200:203],v[200:203],v[80:83]
EX v71,v71
MM v[80:83],a[204:207],v[216:219],v[80:83]
TR a[156:157],v233 offset:8704
CV v67,v70,v71
MM v[84:87],a[220:223],v[216:219],v[136:139]
EX v72,v72
MM v[84:87],a[216:219],v[200:203],v[84:87]
TR a[158:159],v233 offset:12800
TR a[160:161],v232 offset:1024
MM v[84:87],a[212:215],v[184:187],v[84:87]
EX v73,v73
MM v[84:87],a[208:211],v[168:171],v[84:87]
TR a[162:163],v232 offset:5120
CV v72,v72,v73
MM v[92:95],a[208:211],v[172:175],v[140:143]
EX v74,v74
MM v[92:95],a[212:215],v[188:191],v[92:95]
TR a[164:165],v232 offset:9216
TR a[166:167],v232 offset:13312
MM v[92:95],a[216:219],v[204:207],v[92:95]
EX v75,v75
MM v[92:95],a[220:223],v[220:223],v[92:95]
TR a[168:169],v233 offset:1024
CV v73,v74,v75
MM v[88:91],a[204:207],v[220:223],v[140:143]
EX v76,v76
MM v[88:91],a[200:203],v[204:207],v[88:91]
EX v77,v77
MM v[88:91],a[196:199],v[188:191],v[88:91]
TR a[170:171],v233 offset:5120
CV v74,v76,v77
MM v[88:91],a[192:195],v[172:175],v[88:91]
EX v78,v78
MM v[96:99],a[224:227],v[160:163],v[128:131]
TR a[172:173],v233 offset:9216
TR a[174:175],v233 offset:13312
MM v[96:99],a[228:231],v[176:179],v[96:99]
EX v79,v79
MM v[96:99],a[232:235],v[192:195],v[96:99]
TR a[176:177],v232 offset:1536
CV v75,v78,v79
MM v[96:99],a[236:239],v[208:211],v[96:99]
EX v80,v80
MM v[100:103],a[252:255],v[208:211],v[128:131]
EX v81,v81
MM v[100:103],a[248:251],v[192:195],v[100:103]
TR a[178:179],v232 offset:5632
CV v80,v80,v81
MM v[100:103],a[244:247],v[176:179],v[100:103]
EX v82,v82
MM v[100:103],a[240:243],v[160:163],v[100:103]
TR a[180:181],v232 offset:9728
TR a[182:183],v232 offset:13824
MM v[108:111],a[240:243],v[164:167],v[132:135]
v_add_u32 v48, s78, v248
v_mov_b32 v49, s79
MM v[108:111],a[244:247],v[180:183],v[108:111]
EX v83,v83
MM v[108:111],a[248:251],v[196:199],v[108:111]
TR a[184:185],v233 offset:1536
CV v81,v82,v83
MM v[108:111],a[252:255],v[212:215],v[108:111]
EX v84,v84
MM v[104:107],a[236:239],v[212:215],v[132:135]
EX v85,v85
MM v[104:107],a[232:235],v[196:199],v[104:107]
TR a[186:187],v233 offset:5632
CV v82,v84,v85
MM v[104:107],a[228:231],v[180:183],v[104:107]
EX v86,v86
MM v[104:107],a[224:227],v[164:167],v[104:107]
TR a[188:189],v233 offset:9728
TR a[190:191],v233 offset:13824
MM v[112:115],a[224:227],v[168:171],v[136:139]
EX v87,v87
MM v[112:115],a[228:231],v[184:187],v[112:115]
CV v83,v86,v87
MM v[112:115],a[232:235],v[200:203],v[112:115]
EX v88,v88
MM v[112:115],a[236:239],v[216:219],v[112:115]
EX v89,v89
MM v[116:119],a[252:255],v[216:219],v[136:139]
EX v90,v90
MM v[116:119],a[248:251],v[200:203],v[116:119]
EX v91,v91
MM v[116:119],a[244:247],v[184:187],v[116:119]
EX v92,v92
MM v[116:119],a[240:243],v[168:171],v[116:119]
EX v93,v93
MM v[124:127],a[240:243],v[172:175],v[140:143]
EX v94,v94
MM v[124:127],a[244:247],v[188:191],v[124:127]
EX v95,v95
MM v[124:127],a[248:251],v[204:207],v[124:127]
EX v96,v96
MM v[124:127],a[252:255],v[220:223],v[124:127]
EX v97,v97
MM v[120:123],a[236:239],v[220:223],v[140:143]
EX v98,v98
MM v[120:123],a[232:235],v[204:207],v[120:123]
EX v99,v99
MM v[120:123],a[228:231],v[188:191],v[120:123]
EX v100,v100
MM v[120:123],a[224:227],v[172:175],v[120:123]
s_waitcnt vmcnt(13) lgkmcnt(0)
s_barrier
MM a[0:3],a[128:131],v[0:3],a[0:3]
s_add_u32 m0, s65, 0
RD v[32:35],v48 offset:0
RD v[36:39],v48 offset:1024
MM a[0:3],a[132:135],v[4:7],a[0:3]
s_mov_b32 s92, -1
CV v88,v88,v89
CV v89,v90,v91
MM a[8:11],a[132:135],v[12:15],a[8:11]
RD a[192:195],v224 offset:32768
CV v90,v92,v93
MM a[8:11],a[128:131],v[8:11],a[8:11]
RD a[196:199],v225 offset:32768
CV v91,v94,v95
MM a[16:19],a[128:131],v[16:19],a[16:19]
s_waitcnt lgkmcnt(2)
v_sub_u32 v32, s92, v32
v_sub_u32 v33, s92, v33
MM a[16:19],a[132:135],v[20:23],a[16:19]
v_sub_u32 v34, s92, v34
v_sub_u32 v35, s92, v35
MM a[24:27],a[132:135],v[28:31],a[24:27]
v_sub_u32 v36, s92, v36
v_sub_u32 v37, s92, v37
MM a[24:27],a[128:131],v[24:27],a[24:27]
v_sub_u32 v38, s92, v38
v_sub_u32 v39, s92, v39
MM a[28:31],a[136:139],v[24:27],a[28:31]
v_min3_u32 v52, v32, v33, v34
v_min3_u32 v51, v35, v36, v37
MM a[28:31],a[140:143],v[28:31],a[28:31]
v_min3_u32 v52, v52, v38, v39
v_min_u32 v52, v52, v51
MM a[20:23],a[140:143],v[20:23],a[20:23]
s_nop 0
v_min_u32_dpp v52, v52, v52 row_shr:1 row_mask:0xf bank_mask:0xf
CV v68,v96,v97
MM a[20:23],a[136:139],v[16:19],a[20:23]
RD a[200:203],v226 offset:32768
v_min_u32_dpp v52, v52, v52 row_shr:2 row_mask:0xf bank_mask:0xf
MM a[12:15],a[136:139],v[8:11],a[12:15]
s_nop 0
v_min_u32_dpp v52, v52, v52 row_shr:4 row_mask:0xf bank_mask:0xf
CV v69,v98,v99
MM a[12:15],a[140:143],v[12:15],a[12:15]
RD a[204:207],v227 offset:32768
v_min_u32_dpp v52, v52, v52 row_shr:8 row_mask:0xf bank_mask:0xf
MM a[4:7],a[140:143],v[4:7],a[4:7]
RD a[208:211],v224 offset:36864
v_min_u32_dpp v52, v52, v52 row_bcast:15 row_mask:0xa bank_mask:0xf
MM a[4:7],a[136:139],v[0:3],a[4:7]
RD a[212:215],v225 offset:36864
v_min_u32_dpp v52, v52, v52 row_bcast:31 row_mask:0xc bank_mask:0xf
MM a[32:35],a[144:147],v[0:3],a[32:35]
v_readlane_b32 s76, v52, 63
v_lshl_add_u32 v49, s80, 1, v49
MM a[32:35],a[148:151],v[4:7],a[32:35]
s_add_u32 s76, s76, 1
buffer_load_dwordx4 v228, s[8:11], s64 offen offset:0 lds
v_subrev_u32 v32, s76, v32
v_subrev_u32 v33, s76, v33
EX v101,v101
MM a[40:43],a[148:151],v[12:15],a[40:43]
v_subrev_u32 v34, s76, v34
v_subrev_u32 v35, s76, v35
MM a[40:43],a[144:147],v[8:11],a[40:43]
v_subrev_u32 v36, s76, v36
v_subrev_u32 v37, s76, v37
EX v102,v102
MM a[48:51],a[144:147],v[16:19],a[48:51]
v_subrev_u32 v38, s76, v38
v_subrev_u32 v39, s76, v39
EX v103,v103
MM a[48:51],a[148:151],v[20:23],a[48:51]
v_min3_u32 v53, v32, v33, v34
v_min3_u32 v51, v35, v36, v37
MM a[56:59],a[148:151],v[28:31],a[56:59]
v_min3_u32 v53, v53, v38, v39
v_min_u32 v53, v53, v51
EX v104,v104
MM a[56:59],a[144:147],v[24:27],a[56:59]
v_min_u32_dpp v53, v53, v53 row_shr:1 row_mask:0xf bank_mask:0xf
CV v70,v100,v101
MM a[60:63],a[152:155],v[24:27],a[60:63]
v_min_u32_dpp v53, v53, v53 row_shr:2 row_mask:0xf bank_mask:0xf
CV v71,v102,v103
EX v105,v105
MM a[60:63],a[156:159],v[28:31],a[60:63]
buffer_load_dwordx4 v229, s[8:11], s64 offen offset:1024 lds
v_min_u32_dpp v53, v53, v53 row_shr:4 row_mask:0xf bank_mask:0xf
CV v76,v104,v105
MM a[52:55],a[156:159],v[20:23],a[52:55]
RD a[216:219],v226 offset:36864
v_min_u32_dpp v53, v53, v53 row_shr:8 row_mask:0xf bank_mask:0xf
EX v106,v106
MM a[52:55],a[152:155],v[16:19],a[52:55]
RD a[220:223],v227 offset:36864
v_min_u32_dpp v53, v53, v53 row_bcast:15 row_mask:0xa bank_mask:0xf
MM a[44:47],a[152:155],v[8:11],a[44:47]
RD a[224:227],v224 offset:40960
v_min_u32_dpp v53, v53, v53 row_bcast:31 row_mask:0xc bank_mask:0xf
EX v107,v107
MM a[44:47],a[156:159],v[12:15],a[44:47]
v_add_u32 v53, s76, v53
CV v77,v106,v107
MM a[36:39],a[156:159],v[4:7],a[36:39]
s_mov_b32 exec_lo, 0
s_mov_b32 exec_hi, 0x80000000
ds_write_b64 v49, v[52:53] offset:0
s_mov_b64 exec, -1
EX v108,v108
MM a[36:39],a[152:155],v[0:3],a[36:39]
EX v109,v109
MM a[64:67],a[160:163],v[0:3],a[64:67]
buffer_load_dwordx4 v230, s[8:11], s64 offen offset:2048 lds
MM a[64:67],a[164:167],v[4:7],a[64:67]
EX v110,v110
MM a[72:75],a[164:167],v[12:15],a[72:75]
EX v111,v111
MM a[72:75],a[160:163],v[8:11],a[72:75]
buffer_load_dwordx4 v231, s[8:11], s64 offen offset:3072 lds
MM a[80:83],a[160:163],v[16:19],a[80:83]
s_add_u32 m0, s65, 65536
)ASM"
R"ASM(
EX v112,v112
MM a[80:83],a[164:167],v[20:23],a[80:83]
CV v78,v108,v109
CV v79,v110,v111
MM a[88:91],a[164:167],v[28:31],a[88:91]
EX v113,v113
MM a[88:91],a[160:163],v[24:27],a[88:91]
EX v114,v114
MM a[92:95],a[168:171],v[24:27],a[92:95]
EX v115,v115
MM a[92:95],a[172:175],v[28:31],a[92:95]
buffer_load_dwordx4 v235, s[12:15], s81 offen offset:0 lds
MM a[84:87],a[172:175],v[20:23],a[84:87]
RD a[228:231],v225 offset:40960
CV v84,v112,v113
MM a[84:87],a[168:171],v[16:19],a[84:87]
EX v116,v116
MM a[76:79],a[168:171],v[8:11],a[76:79]
EX v117,v117
MM a[76:79],a[172:175],v[12:15],a[76:79]
RD a[232:235],v226 offset:40960
CV v85,v114,v115
MM a[68:71],a[172:175],v[4:7],a[68:71]
EX v118,v118
MM a[68:71],a[168:171],v[0:3],a[68:71]
RD a[236:239],v227 offset:40960
CV v86,v116,v117
MM a[96:99],a[176:179],v[0:3],a[96:99]
EX v119,v119
MM a[96:99],a[180:183],v[4:7],a[96:99]
buffer_load_dwordx4 v235, s[12:15], s64 offen offset:1024 lds
MM a[104:107],a[180:183],v[12:15],a[104:107]
EX v120,v120
MM a[104:107],a[176:179],v[8:11],a[104:107]
RD a[240:243],v224 offset:45056
CV v87,v118,v119
MM a[112:115],a[176:179],v[16:19],a[112:115]
EX v121,v121
MM a[112:115],a[180:183],v[20:23],a[112:115]
EX v122,v122
MM a[120:123],a[180:183],v[28:31],a[120:123]
RD a[244:247],v225 offset:45056
CV v92,v120,v121
MM a[120:123],a[176:179],v[24:27],a[120:123]
EX v123,v123
MM a[124:127],a[184:187],v[24:27],a[124:127]
buffer_load_dwordx4 v235, s[12:15], s81 offen offset:2048 lds
MM a[124:127],a[188:191],v[28:31],a[124:127]
EX v124,v124
MM a[116:119],a[188:191],v[20:23],a[116:119]
RD a[248:251],v226 offset:45056
CV v93,v122,v123
MM a[116:119],a[184:187],v[16:19],a[116:119]
EX v125,v125
MM a[108:111],a[184:187],v[8:11],a[108:111]
EX v126,v126
MM a[108:111],a[188:191],v[12:15],a[108:111]
RD a[252:255],v227 offset:45056
CV v94,v124,v125
MM a[100:103],a[188:191],v[4:7],a[100:103]
EX v127,v127
MM a[100:103],a[184:187],v[0:3],a[100:103]
buffer_load_dwordx4 v235, s[12:15], s64 offen offset:3072 lds
v_smfmac_f32_16x16x64_bf16 v[236:239], v[144:147], v[64:71], v234
CV v95,v126,v127
v_smfmac_f32_16x16x64_bf16 v[236:239], v[148:151], v[72:79], v234
v_smfmac_f32_16x16x64_bf16 v[236:239], v[152:155], v[80:87], v234
v_smfmac_f32_16x16x64_bf16 v[236:239], v[156:159], v[88:95], v234
s_waitcnt lgkmcnt(0)
MM v[0:3],a[192:195],v[160:163],v[128:131]
s_add_u32 s64, s64, 16384
TR a[128:129],v232 offset:16384
s_add_u32 s81, s64, 896
MM v[0:3],a[196:199],v[176:179],v[0:3]
TR a[130:131],v232 offset:20480
TR a[132:133],v232 offset:24576
MM v[0:3],a[200:203],v[192:195],v[0:3]
TR a[134:135],v232 offset:28672
TR a[136:137],v233 offset:16384
MM v[0:3],a[204:207],v[208:211],v[0:3]
TR a[138:139],v233 offset:20480
v_add_f32 v247, v236, v237
MM v[4:7],a[220:223],v[208:211],v[128:131]
TR a[140:141],v233 offset:24576
v_cmp_nge_f32 vcc, s71, v247
MM v[4:7],a[216:219],v[192:195],v[4:7]
s_cbranch_vccnz LSITE5_%=
LBACK5_%=:
TR a[142:143],v233 offset:28672
TR a[144:145],v232 offset:16896
MM v[4:7],a[212:215],v[176:179],v[4:7]
s_mov_b32 m0, s67
TR a[146:147],v232 offset:20992
TR a[148:149],v232 offset:25088
MM v[4:7],a[208:211],v[160:163],v[4:7]
buffer_load_dwordx4 v252, s[36:39], s66 offen offset:0 lds
MM v[12:15],a[208:211],v[164:167],v[132:135]
s_add_u32 s66, s66, 55296
EX v0,v0
MM v[12:15],a[212:215],v[180:183],v[12:15]
s_min_u32 s66, s66, s85
EX v1,v1
MM v[12:15],a[216:219],v[196:199],v[12:15]
s_add_u32 s67, s67, 1024
TR a[150:151],v232 offset:29184
CV v0,v0,v1
MM v[12:15],a[220:223],v[212:215],v[12:15]
s_min_u32 s67, s67, s86
EX v2,v2
MM v[8:11],a[204:207],v[212:215],v[132:135]
EX v3,v3
MM v[8:11],a[200:203],v[196:199],v[8:11]
TR a[152:153],v233 offset:16896
CV v1,v2,v3
MM v[8:11],a[196:199],v[180:183],v[8:11]
EX v4,v4
MM v[8:11],a[192:195],v[164:167],v[8:11]
EX v5,v5
MM v[16:19],a[192:195],v[168:171],v[136:139]
TR a[154:155],v233 offset:20992
CV v2,v4,v5
MM v[16:19],a[196:199],v[184:187],v[16:19]
EX v6,v6
MM v[16:19],a[200:203],v[200:203],v[16:19]
EX v7,v7
MM v[16:19],a[204:207],v[216:219],v[16:19]
TR a[156:157],v233 offset:25088
CV v3,v6,v7
MM v[20:23],a[220:223],v[216:219],v[136:139]
EX v8,v8
MM v[20:23],a[216:219],v[200:203],v[20:23]
TR a[158:159],v233 offset:29184
TR a[160:161],v232 offset:17408
MM v[20:23],a[212:215],v[184:187],v[20:23]
EX v9,v9
MM v[20:23],a[208:211],v[168:171],v[20:23]
TR a[162:163],v232 offset:21504
CV v8,v8,v9
MM v[28:31],a[208:211],v[172:175],v[140:143]
EX v10,v10
MM v[28:31],a[212:215],v[188:191],v[28:31]
TR a[164:165],v232 offset:25600
TR a[166:167],v232 offset:29696
MM v[28:31],a[216:219],v[204:207],v[28:31]
EX v11,v11
MM v[28:31],a[220:223],v[220:223],v[28:31]
TR a[168:169],v233 offset:17408
CV v9,v10,v11
MM v[24:27],a[204:207],v[220:223],v[140:143]
EX v12,v12
MM v[24:27],a[200:203],v[204:207],v[24:27]
EX v13,v13
MM v[24:27],a[196:199],v[188:191],v[24:27]
TR a[170:171],v233 offset:21504
CV v10,v12,v13
MM v[24:27],a[192:195],v[172:175],v[24:27]
EX v14,v14
MM v[32:35],a[224:227],v[160:163],v[128:131]
TR a[172:173],v233 offset:25600
TR a[174:175],v233 offset:29696
MM v[32:35],a[228:231],v[176:179],v[32:35]
EX v15,v15
MM v[32:35],a[232:235],v[192:195],v[32:35]
TR a[176:177],v232 offset:17920
CV v11,v14,v15
MM v[32:35],a[236:239],v[208:211],v[32:35]
EX v16,v16
MM v[36:39],a[252:255],v[208:211],v[128:131]
EX v17,v17
MM v[36:39],a[248:251],v[192:195],v[36:39]
TR a[178:179],v232 offset:22016
CV v16,v16,v17
MM v[36:39],a[244:247],v[176:179],v[36:39]
EX v18,v18
MM v[36:39],a[240:243],v[160:163],v[36:39]
TR a[180:181],v232 offset:26112
TR a[182:183],v232 offset:30208
MM v[44:47],a[240:243],v[164:167],v[132:135]
v_add_u32 v112, s78, v248
v_mov_b32 v113, s79
MM v[44:47],a[244:247],v[180:183],v[44:47]
EX v19,v19
MM v[44:47],a[248:251],v[196:199],v[44:47]
TR a[184:185],v233 offset:17920
CV v17,v18,v19
MM v[44:47],a[252:255],v[212:215],v[44:47]
EX v20,v20
MM v[40:43],a[236:239],v[212:215],v[132:135]
EX v21,v21
MM v[40:43],a[232:235],v[196:199],v[40:43]
TR a[186:187],v233 offset:22016
CV v18,v20,v21
MM v[40:43],a[228:231],v[180:183],v[40:43]
EX v22,v22
MM v[40:43],a[224:227],v[164:167],v[40:43]
TR a[188:189],v233 offset:26112
TR a[190:191],v233 offset:30208
MM v[48:51],a[224:227],v[168:171],v[136:139]
EX v23,v23
MM v[48:51],a[228:231],v[184:187],v[48:51]
CV v19,v22,v23
MM v[48:51],a[232:235],v[200:203],v[48:51]
EX v24,v24
MM v[48:51],a[236:239],v[216:219],v[48:51]
EX v25,v25
MM v[52:55],a[252:255],v[216:219],v[136:139]
EX v26,v26
MM v[52:55],a[248:251],v[200:203],v[52:55]
EX v27,v27
MM v[52:55],a[244:247],v[184:187],v[52:55]
EX v28,v28
MM v[52:55],a[240:243],v[168:171],v[52:55]
EX v29,v29
MM v[60:63],a[240:243],v[172:175],v[140:143]
EX v30,v30
MM v[60:63],a[244:247],v[188:191],v[60:63]
EX v31,v31
MM v[60:63],a[248:251],v[204:207],v[60:63]
EX v32,v32
MM v[60:63],a[252:255],v[220:223],v[60:63]
EX v33,v33
MM v[56:59],a[236:239],v[220:223],v[140:143]
EX v34,v34
MM v[56:59],a[232:235],v[204:207],v[56:59]
EX v35,v35
MM v[56:59],a[228:231],v[188:191],v[56:59]
EX v36,v36
MM v[56:59],a[224:227],v[172:175],v[56:59]
s_waitcnt vmcnt(13) lgkmcnt(0)
s_barrier
MM a[0:3],a[128:131],v[64:67],a[0:3]
s_add_u32 m0, s65, 16384
RD v[104:107],v113 offset:0
RD v[108:111],v113 offset:16
MM a[0:3],a[132:135],v[68:71],a[0:3]
RD v[96:99],v112 offset:0
RD v[100:103],v112 offset:1024
MM a[8:11],a[132:135],v[76:79],a[8:11]
CV v24,v24,v25
CV v25,v26,v27
MM a[8:11],a[128:131],v[72:75],a[8:11]
RD a[192:195],v224 offset:49152
CV v26,v28,v29
MM a[16:19],a[128:131],v[80:83],a[16:19]
RD a[196:199],v225 offset:49152
CV v27,v30,v31
MM a[16:19],a[132:135],v[84:87],a[16:19]
s_waitcnt lgkmcnt(4)
v_min3_u32 v114, v104, v105, v106
v_min3_u32 v115, v107, v108, v109
MM a[24:27],a[132:135],v[92:95],a[24:27]
v_min3_u32 v114, v114, v110, v111
v_min_u32 v114, v114, v115
MM a[24:27],a[128:131],v[88:91],a[24:27]
v_readfirstlane_b32 s76, v114
CV v4,v32,v33
MM a[28:31],a[136:139],v[88:91],a[28:31]
s_sub_u32 s92, s92, s76
buffer_load_dwordx4 v228, s[8:11], s64 offen offset:0 lds
MM a[28:31],a[140:143],v[92:95],a[28:31]
s_add_u32 s76, s76, 1
v_subrev_u32 v104, s76, v104
v_subrev_u32 v105, s76, v105
MM a[20:23],a[140:143],v[84:87],a[20:23]
v_subrev_u32 v106, s76, v106
v_subrev_u32 v107, s76, v107
MM a[20:23],a[136:139],v[80:83],a[20:23]
v_subrev_u32 v108, s76, v108
v_subrev_u32 v109, s76, v109
MM a[12:15],a[136:139],v[72:75],a[12:15]
v_subrev_u32 v110, s76, v110
v_subrev_u32 v111, s76, v111
MM a[12:15],a[140:143],v[76:79],a[12:15]
s_and_b32 s77, s92, 65535
RD a[200:203],v226 offset:49152
CV v5,v34,v35
MM a[4:7],a[140:143],v[68:71],a[4:7]
s_sub_u32 s52, 65535, s77
v_min3_u32 v114, v104, v105, v106
v_min3_u32 v115, v107, v108, v109
MM a[4:7],a[136:139],v[64:67],a[4:7]
v_min3_u32 v114, v114, v110, v111
v_min_u32 v114, v114, v115
MM a[32:35],a[144:147],v[64:67],a[32:35]
RD a[204:207],v227 offset:49152
v_readfirstlane_b32 s76, v114
MM a[32:35],a[148:151],v[68:71],a[32:35]
s_sub_u32 s92, s92, s76
EX v37,v37
MM a[40:43],a[148:151],v[76:79],a[40:43]
s_sub_u32 s92, s92, 1
EX v38,v38
MM a[40:43],a[144:147],v[72:75],a[40:43]
s_and_b32 s77, s92, 65535
EX v39,v39
MM a[48:51],a[144:147],v[80:83],a[48:51]
s_sub_u32 s53, 65535, s77
buffer_load_dwordx4 v229, s[8:11], s64 offen offset:1024 lds
MM a[48:51],a[148:151],v[84:87],a[48:51]
s_sub_u32 s92, s92, 1
EX v40,v40
MM a[56:59],a[148:151],v[92:95],a[56:59]
s_waitcnt lgkmcnt(4)
v_sub_u32 v96, s92, v96
v_sub_u32 v97, s92, v97
MM a[56:59],a[144:147],v[88:91],a[56:59]
v_sub_u32 v98, s92, v98
v_sub_u32 v99, s92, v99
MM a[60:63],a[152:155],v[88:91],a[60:63]
v_sub_u32 v100, s92, v100
v_sub_u32 v101, s92, v101
EX v41,v41
MM a[60:63],a[156:159],v[92:95],a[60:63]
v_sub_u32 v102, s92, v102
v_sub_u32 v103, s92, v103
LPG27_%=:
s_cbranch_execz LTR1_%=
MM a[52:55],a[156:159],v[84:87],a[52:55]
v_min3_u32 v116, v96, v97, v98
v_min3_u32 v115, v99, v100, v101
EX v42,v42
MM a[52:55],a[152:155],v[80:83],a[52:55]
v_min3_u32 v116, v116, v102, v103
v_min_u32 v116, v116, v115
CV v6,v36,v37
MM a[44:47],a[152:155],v[72:75],a[44:47]
v_min_u32_dpp v116, v116, v116 row_shr:1 row_mask:0xf bank_mask:0xf
CV v7,v38,v39
EX v43,v43
MM a[44:47],a[156:159],v[76:79],a[44:47]
RD a[208:211],v224 offset:53248
v_min_u32_dpp v116, v116, v116 row_shr:2 row_mask:0xf bank_mask:0xf
MM a[36:39],a[156:159],v[68:71],a[36:39]
s_nop 0
v_min_u32_dpp v116, v116, v116 row_shr:4 row_mask:0xf bank_mask:0xf
CV v12,v40,v41
EX v44,v44
MM a[36:39],a[152:155],v[64:67],a[36:39]
RD a[212:215],v225 offset:53248
v_min_u32_dpp v116, v116, v116 row_shr:8 row_mask:0xf bank_mask:0xf
MM a[64:67],a[160:163],v[64:67],a[64:67]
s_nop 0
v_min_u32_dpp v116, v116, v116 row_bcast:15 row_mask:0xa bank_mask:0xf
CV v13,v42,v43
EX v45,v45
MM a[64:67],a[164:167],v[68:71],a[64:67]
RD a[216:219],v226 offset:53248
v_min_u32_dpp v116, v116, v116 row_bcast:31 row_mask:0xc bank_mask:0xf
MM a[72:75],a[164:167],v[76:79],a[72:75]
buffer_load_dwordx4 v230, s[8:11], s64 offen offset:2048 lds
v_readlane_b32 s76, v116, 63
v_lshl_add_u32 v113, s80, 1, v113
EX v46,v46
MM a[72:75],a[160:163],v[72:75],a[72:75]
s_add_u32 s76, s76, 1
v_subrev_u32 v96, s76, v96
v_subrev_u32 v97, s76, v97
EX v47,v47
MM a[80:83],a[160:163],v[80:83],a[80:83]
v_subrev_u32 v98, s76, v98
v_subrev_u32 v99, s76, v99
MM a[80:83],a[164:167],v[84:87],a[80:83]
v_subrev_u32 v100, s76, v100
v_subrev_u32 v101, s76, v101
EX v48,v48
MM a[88:91],a[164:167],v[92:95],a[88:91]
v_subrev_u32 v102, s76, v102
v_subrev_u32 v103, s76, v103
MM a[88:91],a[160:163],v[88:91],a[88:91]
v_min3_u32 v117, v96, v97, v98
v_min3_u32 v115, v99, v100, v101
EX v49,v49
MM a[92:95],a[168:171],v[88:91],a[92:95]
v_min3_u32 v117, v117, v102, v103
v_min_u32 v117, v117, v115
MM a[92:95],a[172:175],v[92:95],a[92:95]
s_nop 0
v_min_u32_dpp v117, v117, v117 row_shr:1 row_mask:0xf bank_mask:0xf
CV v14,v44,v45
EX v50,v50
MM a[84:87],a[172:175],v[84:87],a[84:87]
RD a[220:223],v227 offset:53248
buffer_load_dwordx4 v231, s[8:11], s64 offen offset:3072 lds
v_min_u32_dpp v117, v117, v117 row_shr:2 row_mask:0xf bank_mask:0xf
MM a[84:87],a[168:171],v[80:83],a[84:87]
s_add_u32 m0, s65, 81920
v_min_u32_dpp v117, v117, v117 row_shr:4 row_mask:0xf bank_mask:0xf
)ASM"
R"ASM(
CV v15,v46,v47
EX v51,v51
MM a[76:79],a[168:171],v[72:75],a[76:79]
RD a[224:227],v224 offset:57344
v_min_u32_dpp v117, v117, v117 row_shr:8 row_mask:0xf bank_mask:0xf
MM a[76:79],a[172:175],v[76:79],a[76:79]
s_nop 0
v_min_u32_dpp v117, v117, v117 row_bcast:15 row_mask:0xa bank_mask:0xf
CV v20,v48,v49
EX v52,v52
MM a[68:71],a[172:175],v[68:71],a[68:71]
RD a[228:231],v225 offset:57344
v_min_u32_dpp v117, v117, v117 row_bcast:31 row_mask:0xc bank_mask:0xf
EX v53,v53
MM a[68:71],a[168:171],v[64:67],a[68:71]
v_add_u32 v117, s76, v117
CV v21,v50,v51
MM a[96:99],a[176:179],v[64:67],a[96:99]
s_mov_b32 exec_lo, 0
s_mov_b32 exec_hi, 0x80000000
ds_write_b64 v113, v[116:117] offset:32
s_mov_b64 exec, -1
EX v54,v54
MM a[96:99],a[180:183],v[68:71],a[96:99]
buffer_load_dwordx4 v235, s[12:15], s81 offen offset:0 lds
MM a[104:107],a[180:183],v[76:79],a[104:107]
EX v55,v55
MM a[104:107],a[176:179],v[72:75],a[104:107]
EX v56,v56
MM a[112:115],a[176:179],v[80:83],a[112:115]
RD a[232:235],v226 offset:57344
CV v22,v52,v53
MM a[112:115],a[180:183],v[84:87],a[112:115]
EX v57,v57
MM a[120:123],a[180:183],v[92:95],a[120:123]
buffer_load_dwordx4 v235, s[12:15], s64 offen offset:1024 lds
MM a[120:123],a[176:179],v[88:91],a[120:123]
EX v58,v58
MM a[124:127],a[184:187],v[88:91],a[124:127]
RD a[236:239],v227 offset:57344
CV v23,v54,v55
MM a[124:127],a[188:191],v[92:95],a[124:127]
EX v59,v59
MM a[116:119],a[188:191],v[84:87],a[116:119]
EX v60,v60
MM a[116:119],a[184:187],v[80:83],a[116:119]
RD a[240:243],v224 offset:61440
CV v28,v56,v57
MM a[108:111],a[184:187],v[72:75],a[108:111]
EX v61,v61
MM a[108:111],a[188:191],v[76:79],a[108:111]
buffer_load_dwordx4 v235, s[12:15], s81 offen offset:2048 lds
MM a[100:103],a[188:191],v[68:71],a[100:103]
EX v62,v62
MM a[100:103],a[184:187],v[64:67],a[100:103]
RD a[244:247],v225 offset:61440
CV v29,v58,v59
v_smfmac_f32_16x16x64_bf16 v[236:239], v[144:147], v[0:7], v234
RD a[248:251],v226 offset:61440
RD a[252:255],v227 offset:61440
EX v63,v63
v_smfmac_f32_16x16x64_bf16 v[236:239], v[148:151], v[8:15], v234
CV v30,v60,v61
v_smfmac_f32_16x16x64_bf16 v[236:239], v[152:155], v[16:23], v234
CV v31,v62,v63
s_nop 1
v_smfmac_f32_16x16x64_bf16 v[236:239], v[156:159], v[24:31], v234
s_waitcnt lgkmcnt(0)
buffer_load_dwordx4 v235, s[12:15], s64 offen offset:3072 lds
MM v[64:67],a[192:195],v[160:163],v[128:131]
s_add_u32 s64, s64, 16384
TR a[128:129],v232 offset:32768
s_add_u32 s81, s64, 896
MM v[64:67],a[196:199],v[176:179],v[64:67]
TR a[130:131],v232 offset:36864
TR a[132:133],v232 offset:40960
MM v[64:67],a[200:203],v[192:195],v[64:67]
TR a[134:135],v232 offset:45056
TR a[136:137],v233 offset:32768
MM v[64:67],a[204:207],v[208:211],v[64:67]
TR a[138:139],v233 offset:36864
v_add_f32 v246, v236, v237
MM v[68:71],a[220:223],v[208:211],v[128:131]
TR a[140:141],v233 offset:40960
v_cmp_nge_f32 vcc, s71, v246
MM v[68:71],a[216:219],v[192:195],v[68:71]
s_cbranch_vccnz LSITE6_%=
LBACK6_%=:
TR a[142:143],v233 offset:45056
TR a[144:145],v232 offset:33280
MM v[68:71],a[212:215],v[176:179],v[68:71]
s_mov_b32 m0, s67
TR a[146:147],v232 offset:37376
TR a[148:149],v232 offset:41472
MM v[68:71],a[208:211],v[160:163],v[68:71]
buffer_load_dwordx4 v252, s[36:39], s66 offen offset:0 lds
MM v[76:79],a[208:211],v[164:167],v[132:135]
s_add_u32 s66, s66, 55296
EX v64,v64
MM v[76:79],a[212:215],v[180:183],v[76:79]
s_min_u32 s66, s66, s85
EX v65,v65
MM v[76:79],a[216:219],v[196:199],v[76:79]
s_add_u32 s67, s67, 1024
TR a[150:151],v232 offset:45568
CV v64,v64,v65
MM v[76:79],a[220:223],v[212:215],v[76:79]
s_min_u32 s67, s67, s86
EX v66,v66
MM v[72:75],a[204:207],v[212:215],v[132:135]
EX v67,v67
MM v[72:75],a[200:203],v[196:199],v[72:75]
TR a[152:153],v233 offset:33280
CV v65,v66,v67
MM v[72:75],a[196:199],v[180:183],v[72:75]
EX v68,v68
MM v[72:75],a[192:195],v[164:167],v[72:75]
EX v69,v69
MM v[80:83],a[192:195],v[168:171],v[136:139]
TR a[154:155],v233 offset:37376
CV v66,v68,v69
MM v[80:83],a[196:199],v[184:187],v[80:83]
EX v70,v70
MM v[80:83],a[200:203],v[200:203],v[80:83]
EX v71,v71
MM v[80:83],a[204:207],v[216:219],v[80:83]
TR a[156:157],v233 offset:41472
CV v67,v70,v71
MM v[84:87],a[220:223],v[216:219],v[136:139]
EX v72,v72
MM v[84:87],a[216:219],v[200:203],v[84:87]
TR a[158:159],v233 offset:45568
TR a[160:161],v232 offset:33792
MM v[84:87],a[212:215],v[184:187],v[84:87]
EX v73,v73
MM v[84:87],a[208:211],v[168:171],v[84:87]
TR a[162:163],v232 offset:37888
CV v72,v72,v73
MM v[92:95],a[208:211],v[172:175],v[140:143]
EX v74,v74
MM v[92:95],a[212:215],v[188:191],v[92:95]
TR a[164:165],v232 offset:41984
TR a[166:167],v232 offset:46080
MM v[92:95],a[216:219],v[204:207],v[92:95]
EX v75,v75
MM v[92:95],a[220:223],v[220:223],v[92:95]
TR a[168:169],v233 offset:33792
CV v73,v74,v75
MM v[88:91],a[204:207],v[220:223],v[140:143]
EX v76,v76
MM v[88:91],a[200:203],v[204:207],v[88:91]
EX v77,v77
MM v[88:91],a[196:199],v[188:191],v[88:91]
TR a[170:171],v233 offset:37888
CV v74,v76,v77
MM v[88:91],a[192:195],v[172:175],v[88:91]
EX v78,v78
MM v[96:99],a[224:227],v[160:163],v[128:131]
TR a[172:173],v233 offset:41984
TR a[174:175],v233 offset:46080
MM v[96:99],a[228:231],v[176:179],v[96:99]
EX v79,v79
MM v[96:99],a[232:235],v[192:195],v[96:99]
TR a[176:177],v232 offset:34304
CV v75,v78,v79
MM v[96:99],a[236:239],v[208:211],v[96:99]
EX v80,v80
MM v[100:103],a[252:255],v[208:211],v[128:131]
EX v81,v81
MM v[100:103],a[248:251],v[192:195],v[100:103]
TR a[178:179],v232 offset:38400
CV v80,v80,v81
MM v[100:103],a[244:247],v[176:179],v[100:103]
EX v82,v82
MM v[100:103],a[240:243],v[160:163],v[100:103]
TR a[180:181],v232 offset:42496
TR a[182:183],v232 offset:46592
MM v[108:111],a[240:243],v[164:167],v[132:135]
v_add_u32 v48, s78, v248
v_mov_b32 v49, s79
MM v[108:111],a[244:247],v[180:183],v[108:111]
EX v83,v83
MM v[108:111],a[248:251],v[196:199],v[108:111]
TR a[184:185],v233 offset:34304
CV v81,v82,v83
MM v[108:111],a[252:255],v[212:215],v[108:111]
EX v84,v84
MM v[104:107],a[236:239],v[212:215],v[132:135]
EX v85,v85
MM v[104:107],a[232:235],v[196:199],v[104:107]
TR a[186:187],v233 offset:38400
CV v82,v84,v85
MM v[104:107],a[228:231],v[180:183],v[104:107]
EX v86,v86
MM v[104:107],a[224:227],v[164:167],v[104:107]
TR a[188:189],v233 offset:42496
TR a[190:191],v233 offset:46592
MM v[112:115],a[224:227],v[168:171],v[136:139]
EX v87,v87
MM v[112:115],a[228:231],v[184:187],v[112:115]
CV v83,v86,v87
MM v[112:115],a[232:235],v[200:203],v[112:115]
EX v88,v88
MM v[112:115],a[236:239],v[216:219],v[112:115]
EX v89,v89
MM v[116:119],a[252:255],v[216:219],v[136:139]
EX v90,v90
MM v[116:119],a[248:251],v[200:203],v[116:119]
EX v91,v91
MM v[116:119],a[244:247],v[184:187],v[116:119]
EX v92,v92
MM v[116:119],a[240:243],v[168:171],v[116:119]
EX v93,v93
MM v[124:127],a[240:243],v[172:175],v[140:143]
EX v94,v94
MM v[124:127],a[244:247],v[188:191],v[124:127]
EX v95,v95
MM v[124:127],a[248:251],v[204:207],v[124:127]
EX v96,v96
MM v[124:127],a[252:255],v[220:223],v[124:127]
EX v97,v97
MM v[120:123],a[236:239],v[220:223],v[140:143]
EX v98,v98
MM v[120:123],a[232:235],v[204:207],v[120:123]
EX v99,v99
MM v[120:123],a[228:231],v[188:191],v[120:123]
EX v100,v100
MM v[120:123],a[224:227],v[172:175],v[120:123]
s_waitcnt vmcnt(13) lgkmcnt(0)
s_barrier
MM a[0:3],a[128:131],v[0:3],a[0:3]
s_add_u32 m0, s65, 32768
RD v[40:43],v49 offset:32
RD v[44:47],v49 offset:48
MM a[0:3],a[132:135],v[4:7],a[0:3]
RD v[32:35],v48 offset:0
RD v[36:39],v48 offset:1024
MM a[8:11],a[132:135],v[12:15],a[8:11]
CV v88,v88,v89
CV v89,v90,v91
MM a[8:11],a[128:131],v[8:11],a[8:11]
RD a[192:195],v224 offset:0
CV v90,v92,v93
MM a[16:19],a[128:131],v[16:19],a[16:19]
RD a[196:199],v225 offset:0
CV v91,v94,v95
MM a[16:19],a[132:135],v[20:23],a[16:19]
s_waitcnt lgkmcnt(4)
v_min3_u32 v50, v40, v41, v42
v_min3_u32 v51, v43, v44, v45
MM a[24:27],a[132:135],v[28:31],a[24:27]
v_min3_u32 v50, v50, v46, v47
v_min_u32 v50, v50, v51
MM a[24:27],a[128:131],v[24:27],a[24:27]
v_readfirstlane_b32 s76, v50
CV v68,v96,v97
MM a[28:31],a[136:139],v[24:27],a[28:31]
s_sub_u32 s92, s92, s76
buffer_load_dwordx4 v228, s[8:11], s64 offen offset:0 lds
MM a[28:31],a[140:143],v[28:31],a[28:31]
s_add_u32 s76, s76, 1
v_subrev_u32 v40, s76, v40
v_subrev_u32 v41, s76, v41
MM a[20:23],a[140:143],v[20:23],a[20:23]
v_subrev_u32 v42, s76, v42
v_subrev_u32 v43, s76, v43
MM a[20:23],a[136:139],v[16:19],a[20:23]
v_subrev_u32 v44, s76, v44
v_subrev_u32 v45, s76, v45
MM a[12:15],a[136:139],v[8:11],a[12:15]
v_subrev_u32 v46, s76, v46
v_subrev_u32 v47, s76, v47
MM a[12:15],a[140:143],v[12:15],a[12:15]
s_and_b32 s77, s92, 65535
RD a[200:203],v226 offset:0
CV v69,v98,v99
MM a[4:7],a[140:143],v[4:7],a[4:7]
s_sub_u32 s54, 65535, s77
v_min3_u32 v50, v40, v41, v42
v_min3_u32 v51, v43, v44, v45
MM a[4:7],a[136:139],v[0:3],a[4:7]
v_min3_u32 v50, v50, v46, v47
v_min_u32 v50, v50, v51
MM a[32:35],a[144:147],v[0:3],a[32:35]
RD a[204:207],v227 offset:0
v_readfirstlane_b32 s76, v50
MM a[32:35],a[148:151],v[4:7],a[32:35]
s_sub_u32 s92, s92, s76
EX v101,v101
MM a[40:43],a[148:151],v[12:15],a[40:43]
s_sub_u32 s92, s92, 1
EX v102,v102
MM a[40:43],a[144:147],v[8:11],a[40:43]
s_and_b32 s77, s92, 65535
EX v103,v103
MM a[48:51],a[144:147],v[16:19],a[48:51]
s_sub_u32 s55, 65535, s77
buffer_load_dwordx4 v229, s[8:11], s64 offen offset:1024 lds
MM a[48:51],a[148:151],v[20:23],a[48:51]
s_sub_u32 s92, s92, 1
EX v104,v104
MM a[56:59],a[148:151],v[28:31],a[56:59]
s_waitcnt lgkmcnt(4)
v_sub_u32 v32, s92, v32
v_sub_u32 v33, s92, v33
MM a[56:59],a[144:147],v[24:27],a[56:59]
v_sub_u32 v34, s92, v34
v_sub_u32 v35, s92, v35
MM a[60:63],a[152:155],v[24:27],a[60:63]
v_sub_u32 v36, s92, v36
v_sub_u32 v37, s92, v37
EX v105,v105
MM a[60:63],a[156:159],v[28:31],a[60:63]
v_sub_u32 v38, s92, v38
v_sub_u32 v39, s92, v39
MM a[52:55],a[156:159],v[20:23],a[52:55]
v_min3_u32 v52, v32, v33, v34
v_min3_u32 v51, v35, v36, v37
EX v106,v106
MM a[52:55],a[152:155],v[16:19],a[52:55]
v_min3_u32 v52, v52, v38, v39
v_min_u32 v52, v52, v51
CV v70,v100,v101
MM a[44:47],a[152:155],v[8:11],a[44:47]
v_min_u32_dpp v52, v52, v52 row_shr:1 row_mask:0xf bank_mask:0xf
CV v71,v102,v103
EX v107,v107
MM a[44:47],a[156:159],v[12:15],a[44:47]
RD a[208:211],v224 offset:4096
v_min_u32_dpp v52, v52, v52 row_shr:2 row_mask:0xf bank_mask:0xf
MM a[36:39],a[156:159],v[4:7],a[36:39]
s_nop 0
v_min_u32_dpp v52, v52, v52 row_shr:4 row_mask:0xf bank_mask:0xf
CV v76,v104,v105
EX v108,v108
MM a[36:39],a[152:155],v[0:3],a[36:39]
RD a[212:215],v225 offset:4096
v_min_u32_dpp v52, v52, v52 row_shr:8 row_mask:0xf bank_mask:0xf
MM a[64:67],a[160:163],v[0:3],a[64:67]
s_nop 0
v_min_u32_dpp v52, v52, v52 row_bcast:15 row_mask:0xa bank_mask:0xf
CV v77,v106,v107
EX v109,v109
MM a[64:67],a[164:167],v[4:7],a[64:67]
RD a[216:219],v226 offset:4096
v_min_u32_dpp v52, v52, v52 row_bcast:31 row_mask:0xc bank_mask:0xf
MM a[72:75],a[164:167],v[12:15],a[72:75]
buffer_load_dwordx4 v230, s[8:11], s64 offen offset:2048 lds
v_readlane_b32 s76, v52, 63
v_lshl_add_u32 v49, s80, 1, v49
EX v110,v110
MM a[72:75],a[160:163],v[8:11],a[72:75]
s_add_u32 s76, s76, 1
v_subrev_u32 v32, s76, v32
v_subrev_u32 v33, s76, v33
EX v111,v111
MM a[80:83],a[160:163],v[16:19],a[80:83]
v_subrev_u32 v34, s76, v34
v_subrev_u32 v35, s76, v35
MM a[80:83],a[164:167],v[20:23],a[80:83]
v_subrev_u32 v36, s76, v36
v_subrev_u32 v37, s76, v37
EX v112,v112
MM a[88:91],a[164:167],v[28:31],a[88:91]
v_subrev_u32 v38, s76, v38
v_subrev_u32 v39, s76, v39
MM a[88:91],a[160:163],v[24:27],a[88:91]
v_min3_u32 v53, v32, v33, v34
v_min3_u32 v51, v35, v36, v37
EX v113,v113
MM a[92:95],a[168:171],v[24:27],a[92:95]
v_min3_u32 v53, v53, v38, v39
v_min_u32 v53, v53, v51
MM a[92:95],a[172:175],v[28:31],a[92:95]
s_nop 0
v_min_u32_dpp v53, v53, v53 row_shr:1 row_mask:0xf bank_mask:0xf
CV v78,v108,v109
EX v114,v114
MM a[84:87],a[172:175],v[20:23],a[84:87]
RD a[220:223],v227 offset:4096
buffer_load_dwordx4 v231, s[8:11], s64 offen offset:3072 lds
v_min_u32_dpp v53, v53, v53 row_shr:2 row_mask:0xf bank_mask:0xf
MM a[84:87],a[168:171],v[16:19],a[84:87]
s_add_u32 m0, s65, 98304
v_min_u32_dpp v53, v53, v53 row_shr:4 row_mask:0xf bank_mask:0xf
)ASM"
R"ASM(
CV v79,v110,v111
EX v115,v115
MM a[76:79],a[168:171],v[8:11],a[76:79]
RD a[224:227],v224 offset:8192
v_min_u32_dpp v53, v53, v53 row_shr:8 row_mask:0xf bank_mask:0xf
MM a[76:79],a[172:175],v[12:15],a[76:79]
s_nop 0
v_min_u32_dpp v53, v53, v53 row_bcast:15 row_mask:0xa bank_mask:0xf
CV v84,v112,v113
EX v116,v116
MM a[68:71],a[172:175],v[4:7],a[68:71]
RD a[228:231],v225 offset:8192
v_min_u32_dpp v53, v53, v53 row_bcast:31 row_mask:0xc bank_mask:0xf
EX v117,v117
MM a[68:71],a[168:171],v[0:3],a[68:71]
v_add_u32 v53, s76, v53
CV v85,v114,v115
MM a[96:99],a[176:179],v[0:3],a[96:99]
s_mov_b32 exec_lo, 0
s_mov_b32 exec_hi, 0x80000000
ds_write_b64 v49, v[52:53] offset:0
s_mov_b64 exec, -1
EX v118,v118
MM a[96:99],a[180:183],v[4:7],a[96:99]
buffer_load_dwordx4 v235, s[12:15], s81 offen offset:0 lds
MM a[104:107],a[180:183],v[12:15],a[104:107]
EX v119,v119
MM a[104:107],a[176:179],v[8:11],a[104:107]
EX v120,v120
MM a[112:115],a[176:179],v[16:19],a[112:115]
RD a[232:235],v226 offset:8192
CV v86,v116,v117
MM a[112:115],a[180:183],v[20:23],a[112:115]
EX v121,v121
MM a[120:123],a[180:183],v[28:31],a[120:123]
buffer_load_dwordx4 v235, s[12:15], s64 offen offset:1024 lds
MM a[120:123],a[176:179],v[24:27],a[120:123]
EX v122,v122
MM a[124:127],a[184:187],v[24:27],a[124:127]
RD a[236:239],v227 offset:8192
CV v87,v118,v119
MM a[124:127],a[188:191],v[28:31],a[124:127]
EX v123,v123
MM a[116:119],a[188:191],v[20:23],a[116:119]
EX v124,v124
MM a[116:119],a[184:187],v[16:19],a[116:119]
RD a[240:243],v224 offset:12288
CV v92,v120,v121
MM a[108:111],a[184:187],v[8:11],a[108:111]
EX v125,v125
MM a[108:111],a[188:191],v[12:15],a[108:111]
buffer_load_dwordx4 v235, s[12:15], s81 offen offset:2048 lds
MM a[100:103],a[188:191],v[4:7],a[100:103]
EX v126,v126
MM a[100:103],a[184:187],v[0:3],a[100:103]
RD a[244:247],v225 offset:12288
CV v93,v122,v123
v_smfmac_f32_16x16x64_bf16 v[236:239], v[144:147], v[64:71], v234
RD a[248:251],v226 offset:12288
RD a[252:255],v227 offset:12288
EX v127,v127
v_smfmac_f32_16x16x64_bf16 v[236:239], v[148:151], v[72:79], v234
CV v94,v124,v125
v_smfmac_f32_16x16x64_bf16 v[236:239], v[152:155], v[80:87], v234
CV v95,v126,v127
s_nop 1
v_smfmac_f32_16x16x64_bf16 v[236:239], v[156:159], v[88:95], v234
s_waitcnt lgkmcnt(0)
buffer_load_dwordx4 v235, s[12:15], s64 offen offset:3072 lds
MM v[0:3],a[192:195],v[160:163],v[128:131]
s_add_u32 s64, s64, 16384
TR a[128:129],v232 offset:49152
s_add_u32 s81, s64, 896
MM v[0:3],a[196:199],v[176:179],v[0:3]
TR a[130:131],v232 offset:53248
TR a[132:133],v232 offset:57344
MM v[0:3],a[200:203],v[192:195],v[0:3]
TR a[134:135],v232 offset:61440
TR a[136:137],v233 offset:49152
MM v[0:3],a[204:207],v[208:211],v[0:3]
TR a[138:139],v233 offset:53248
v_add_f32 v247, v236, v237
MM v[4:7],a[220:223],v[208:211],v[128:131]
TR a[140:141],v233 offset:57344
v_cmp_nge_f32 vcc, s71, v247
MM v[4:7],a[216:219],v[192:195],v[4:7]
s_cbranch_vccnz LSITE7_%=
LBACK7_%=:
TR a[142:143],v233 offset:61440
TR a[144:145],v232 offset:49664
MM v[4:7],a[212:215],v[176:179],v[4:7]
s_mov_b32 m0, s67
TR a[146:147],v232 offset:53760
TR a[148:149],v232 offset:57856
MM v[4:7],a[208:211],v[160:163],v[4:7]
buffer_load_dwordx4 v252, s[36:39], s66 offen offset:0 lds
MM v[12:15],a[208:211],v[164:167],v[132:135]
s_add_u32 s66, s66, 55296
EX v0,v0
MM v[12:15],a[212:215],v[180:183],v[12:15]
s_min_u32 s66, s66, s85
EX v1,v1
MM v[12:15],a[216:219],v[196:199],v[12:15]
s_add_u32 s67, s67, 1024
TR a[150:151],v232 offset:61952
CV v0,v0,v1
MM v[12:15],a[220:223],v[212:215],v[12:15]
s_min_u32 s67, s67, s86
EX v2,v2
MM v[8:11],a[204:207],v[212:215],v[132:135]
EX v3,v3
MM v[8:11],a[200:203],v[196:199],v[8:11]
TR a[152:153],v233 offset:49664
CV v1,v2,v3
MM v[8:11],a[196:199],v[180:183],v[8:11]
EX v4,v4
MM v[8:11],a[192:195],v[164:167],v[8:11]
EX v5,v5
MM v[16:19],a[192:195],v[168:171],v[136:139]
TR a[154:155],v233 offset:53760
CV v2,v4,v5
MM v[16:19],a[196:199],v[184:187],v[16:19]
EX v6,v6
MM v[16:19],a[200:203],v[200:203],v[16:19]
EX v7,v7
MM v[16:19],a[204:207],v[216:219],v[16:19]
TR a[156:157],v233 offset:57856
CV v3,v6,v7
MM v[20:23],a[220:223],v[216:219],v[136:139]
EX v8,v8
MM v[20:23],a[216:219],v[200:203],v[20:23]
TR a[158:159],v233 offset:61952
TR a[160:161],v232 offset:50176
MM v[20:23],a[212:215],v[184:187],v[20:23]
EX v9,v9
MM v[20:23],a[208:211],v[168:171],v[20:23]
TR a[162:163],v232 offset:54272
CV v8,v8,v9
MM v[28:31],a[208:211],v[172:175],v[140:143]
EX v10,v10
MM v[28:31],a[212:215],v[188:191],v[28:31]
TR a[164:165],v232 offset:58368
TR a[166:167],v232 offset:62464
MM v[28:31],a[216:219],v[204:207],v[28:31]
EX v11,v11
MM v[28:31],a[220:223],v[220:223],v[28:31]
TR a[168:169],v233 offset:50176
CV v9,v10,v11
MM v[24:27],a[204:207],v[220:223],v[140:143]
LPG28_%=:
s_cbranch_execz LTR1_%=
EX v12,v12
MM v[24:27],a[200:203],v[204:207],v[24:27]
EX v13,v13
MM v[24:27],a[196:199],v[188:191],v[24:27]
TR a[170:171],v233 offset:54272
CV v10,v12,v13
MM v[24:27],a[192:195],v[172:175],v[24:27]
EX v14,v14
MM v[32:35],a[224:227],v[160:163],v[128:131]
TR a[172:173],v233 offset:58368
TR a[174:175],v233 offset:62464
MM v[32:35],a[228:231],v[176:179],v[32:35]
EX v15,v15
MM v[32:35],a[232:235],v[192:195],v[32:35]
TR a[176:177],v232 offset:50688
CV v11,v14,v15
MM v[32:35],a[236:239],v[208:211],v[32:35]
EX v16,v16
MM v[36:39],a[252:255],v[208:211],v[128:131]
EX v17,v17
MM v[36:39],a[248:251],v[192:195],v[36:39]
TR a[178:179],v232 offset:54784
CV v16,v16,v17
MM v[36:39],a[244:247],v[176:179],v[36:39]
EX v18,v18
MM v[36:39],a[240:243],v[160:163],v[36:39]
TR a[180:181],v232 offset:58880
TR a[182:183],v232 offset:62976
MM v[44:47],a[240:243],v[164:167],v[132:135]
v_add_u32 v112, s78, v248
v_mov_b32 v113, s79
MM v[44:47],a[244:247],v[180:183],v[44:47]
EX v19,v19
MM v[44:47],a[248:251],v[196:199],v[44:47]
TR a[184:185],v233 offset:50688
CV v17,v18,v19
MM v[44:47],a[252:255],v[212:215],v[44:47]
EX v20,v20
MM v[40:43],a[236:239],v[212:215],v[132:135]
EX v21,v21
MM v[40:43],a[232:235],v[196:199],v[40:43]
TR a[186:187],v233 offset:54784
CV v18,v20,v21
MM v[40:43],a[228:231],v[180:183],v[40:43]
EX v22,v22
MM v[40:43],a[224:227],v[164:167],v[40:43]
TR a[188:189],v233 offset:58880
TR a[190:191],v233 offset:62976
MM v[48:51],a[224:227],v[168:171],v[136:139]
EX v23,v23
MM v[48:51],a[228:231],v[184:187],v[48:51]
CV v19,v22,v23
MM v[48:51],a[232:235],v[200:203],v[48:51]
EX v24,v24
MM v[48:51],a[236:239],v[216:219],v[48:51]
EX v25,v25
MM v[52:55],a[252:255],v[216:219],v[136:139]
EX v26,v26
MM v[52:55],a[248:251],v[200:203],v[52:55]
EX v27,v27
MM v[52:55],a[244:247],v[184:187],v[52:55]
EX v28,v28
MM v[52:55],a[240:243],v[168:171],v[52:55]
EX v29,v29
MM v[60:63],a[240:243],v[172:175],v[140:143]
EX v30,v30
MM v[60:63],a[244:247],v[188:191],v[60:63]
EX v31,v31
MM v[60:63],a[248:251],v[204:207],v[60:63]
EX v32,v32
MM v[60:63],a[252:255],v[220:223],v[60:63]
EX v33,v33
MM v[56:59],a[236:239],v[220:223],v[140:143]
EX v34,v34
MM v[56:59],a[232:235],v[204:207],v[56:59]
EX v35,v35
MM v[56:59],a[228:231],v[188:191],v[56:59]
EX v36,v36
MM v[56:59],a[224:227],v[172:175],v[56:59]
s_waitcnt vmcnt(13) lgkmcnt(0)
s_barrier
MM a[0:3],a[128:131],v[64:67],a[0:3]
s_add_u32 m0, s65, 49152
RD v[104:107],v113 offset:0
RD v[108:111],v113 offset:16
MM a[0:3],a[132:135],v[68:71],a[0:3]
RD v[96:99],v112 offset:0
RD v[100:103],v112 offset:1024
MM a[8:11],a[132:135],v[76:79],a[8:11]
CV v24,v24,v25
CV v25,v26,v27
MM a[8:11],a[128:131],v[72:75],a[8:11]
RD a[192:195],v224 offset:16384
CV v26,v28,v29
MM a[16:19],a[128:131],v[80:83],a[16:19]
RD a[196:199],v225 offset:16384
CV v27,v30,v31
MM a[16:19],a[132:135],v[84:87],a[16:19]
s_waitcnt lgkmcnt(4)
v_min3_u32 v114, v104, v105, v106
v_min3_u32 v115, v107, v108, v109
MM a[24:27],a[132:135],v[92:95],a[24:27]
v_min3_u32 v114, v114, v110, v111
v_min_u32 v114, v114, v115
MM a[24:27],a[128:131],v[88:91],a[24:27]
v_readfirstlane_b32 s76, v114
CV v4,v32,v33
MM a[28:31],a[136:139],v[88:91],a[28:31]
s_sub_u32 s92, s92, s76
buffer_load_dwordx4 v228, s[8:11], s64 offen offset:0 lds
MM a[28:31],a[140:143],v[92:95],a[28:31]
s_add_u32 s76, s76, 1
v_subrev_u32 v104, s76, v104
v_subrev_u32 v105, s76, v105
MM a[20:23],a[140:143],v[84:87],a[20:23]
v_subrev_u32 v106, s76, v106
v_subrev_u32 v107, s76, v107
MM a[20:23],a[136:139],v[80:83],a[20:23]
v_subrev_u32 v108, s76, v108
v_subrev_u32 v109, s76, v109
MM a[12:15],a[136:139],v[72:75],a[12:15]
v_subrev_u32 v110, s76, v110
v_subrev_u32 v111, s76, v111
MM a[12:15],a[140:143],v[76:79],a[12:15]
s_and_b32 s77, s92, 65535
RD a[200:203],v226 offset:16384
CV v5,v34,v35
MM a[4:7],a[140:143],v[68:71],a[4:7]
s_sub_u32 s56, 65535, s77
v_min3_u32 v114, v104, v105, v106
v_min3_u32 v115, v107, v108, v109
MM a[4:7],a[136:139],v[64:67],a[4:7]
v_min3_u32 v114, v114, v110, v111
v_min_u32 v114, v114, v115
MM a[32:35],a[144:147],v[64:67],a[32:35]
RD a[204:207],v227 offset:16384
v_readfirstlane_b32 s76, v114
MM a[32:35],a[148:151],v[68:71],a[32:35]
s_sub_u32 s92, s92, s76
EX v37,v37
MM a[40:43],a[148:151],v[76:79],a[40:43]
s_sub_u32 s92, s92, 1
EX v38,v38
MM a[40:43],a[144:147],v[72:75],a[40:43]
s_and_b32 s77, s92, 65535
EX v39,v39
MM a[48:51],a[144:147],v[80:83],a[48:51]
s_sub_u32 s57, 65535, s77
buffer_load_dwordx4 v229, s[8:11], s64 offen offset:1024 lds
MM a[48:51],a[148:151],v[84:87],a[48:51]
s_sub_u32 s92, s92, 1
EX v40,v40
MM a[56:59],a[148:151],v[92:95],a[56:59]
s_waitcnt lgkmcnt(4)
v_sub_u32 v96, s92, v96
v_sub_u32 v97, s92, v97
MM a[56:59],a[144:147],v[88:91],a[56:59]
v_sub_u32 v98, s92, v98
v_sub_u32 v99, s92, v99
MM a[60:63],a[152:155],v[88:91],a[60:63]
v_sub_u32 v100, s92, v100
v_sub_u32 v101, s92, v101
EX v41,v41
MM a[60:63],a[156:159],v[92:95],a[60:63]
v_sub_u32 v102, s92, v102
v_sub_u32 v103, s92, v103
MM a[52:55],a[156:159],v[84:87],a[52:55]
v_min3_u32 v116, v96, v97, v98
v_min3_u32 v115, v99, v100, v101
EX v42,v42
MM a[52:55],a[152:155],v[80:83],a[52:55]
v_min3_u32 v116, v116, v102, v103
v_min_u32 v116, v116, v115
CV v6,v36,v37
MM a[44:47],a[152:155],v[72:75],a[44:47]
v_min_u32_dpp v116, v116, v116 row_shr:1 row_mask:0xf bank_mask:0xf
CV v7,v38,v39
EX v43,v43
MM a[44:47],a[156:159],v[76:79],a[44:47]
RD a[208:211],v224 offset:20480
v_min_u32_dpp v116, v116, v116 row_shr:2 row_mask:0xf bank_mask:0xf
MM a[36:39],a[156:159],v[68:71],a[36:39]
s_nop 0
v_min_u32_dpp v116, v116, v116 row_shr:4 row_mask:0xf bank_mask:0xf
CV v12,v40,v41
EX v44,v44
MM a[36:39],a[152:155],v[64:67],a[36:39]
RD a[212:215],v225 offset:20480
v_min_u32_dpp v116, v116, v116 row_shr:8 row_mask:0xf bank_mask:0xf
MM a[64:67],a[160:163],v[64:67],a[64:67]
s_nop 0
v_min_u32_dpp v116, v116, v116 row_bcast:15 row_mask:0xa bank_mask:0xf
CV v13,v42,v43
EX v45,v45
MM a[64:67],a[164:167],v[68:71],a[64:67]
RD a[216:219],v226 offset:20480
v_min_u32_dpp v116, v116, v116 row_bcast:31 row_mask:0xc bank_mask:0xf
MM a[72:75],a[164:167],v[76:79],a[72:75]
buffer_load_dwordx4 v230, s[8:11], s64 offen offset:2048 lds
v_readlane_b32 s76, v116, 63
v_lshl_add_u32 v113, s80, 1, v113
EX v46,v46
MM a[72:75],a[160:163],v[72:75],a[72:75]
s_add_u32 s76, s76, 1
v_subrev_u32 v96, s76, v96
v_subrev_u32 v97, s76, v97
EX v47,v47
MM a[80:83],a[160:163],v[80:83],a[80:83]
v_subrev_u32 v98, s76, v98
v_subrev_u32 v99, s76, v99
MM a[80:83],a[164:167],v[84:87],a[80:83]
v_subrev_u32 v100, s76, v100
v_subrev_u32 v101, s76, v101
EX v48,v48
MM a[88:91],a[164:167],v[92:95],a[88:91]
v_subrev_u32 v102, s76, v102
v_subrev_u32 v103, s76, v103
MM a[88:91],a[160:163],v[88:91],a[88:91]
v_min3_u32 v117, v96, v97, v98
v_min3_u32 v115, v99, v100, v101
EX v49,v49
MM a[92:95],a[168:171],v[88:91],a[92:95]
v_min3_u32 v117, v117, v102, v103
v_min_u32 v117, v117, v115
MM a[92:95],a[172:175],v[92:95],a[92:95]
s_nop 0
v_min_u32_dpp v117, v117, v117 row_shr:1 row_mask:0xf bank_mask:0xf
CV v14,v44,v45
EX v50,v50
MM a[84:87],a[172:175],v[84:87],a[84:87]
RD a[220:223],v227 offset:20480
buffer_load_dwordx4 v231, s[8:11], s64 offen offset:3072 lds
v_min_u32_dpp v117, v117, v117 row_shr:2 row_mask:0xf bank_mask:0xf
MM a[84:87],a[168:171],v[80:83],a[84:87]
s_add_u32 m0, s65, 114688
v_min_u32_dpp v117, v117, v117 row_shr:4 row_mask:0xf bank_mask:0xf
)ASM"
R"ASM(
CV v15,v46,v47
EX v51,v51
MM a[76:79],a[168:171],v[72:75],a[76:79]
RD a[224:227],v224 offset:24576
v_min_u32_dpp v117, v117, v117 row_shr:8 row_mask:0xf bank_mask:0xf
MM a[76:79],a[172:175],v[76:79],a[76:79]
s_nop 0
v_min_u32_dpp v117, v117, v117 row_bcast:15 row_mask:0xa bank_mask:0xf
CV v20,v48,v49
EX v52,v52
MM a[68:71],a[172:175],v[68:71],a[68:71]
RD a[228:231],v225 offset:24576
v_min_u32_dpp v117, v117, v117 row_bcast:31 row_mask:0xc bank_mask:0xf
EX v53,v53
MM a[68:71],a[168:171],v[64:67],a[68:71]
v_add_u32 v117, s76, v117
CV v21,v50,v51
MM a[96:99],a[176:179],v[64:67],a[96:99]
s_mov_b32 exec_lo, 0
s_mov_b32 exec_hi, 0x80000000
ds_write_b64 v113, v[116:117] offset:32
s_mov_b64 exec, -1
EX v54,v54
MM a[96:99],a[180:183],v[68:71],a[96:99]
buffer_load_dwordx4 v235, s[12:15], s81 offen offset:0 lds
MM a[104:107],a[180:183],v[76:79],a[104:107]
EX v55,v55
MM a[104:107],a[176:179],v[72:75],a[104:107]
EX v56,v56
MM a[112:115],a[176:179],v[80:83],a[112:115]
RD a[232:235],v226 offset:24576
CV v22,v52,v53
MM a[112:115],a[180:183],v[84:87],a[112:115]
EX v57,v57
MM a[120:123],a[180:183],v[92:95],a[120:123]
buffer_load_dwordx4 v235, s[12:15], s64 offen offset:1024 lds
MM a[120:123],a[176:179],v[88:91],a[120:123]
EX v58,v58
MM a[124:127],a[184:187],v[88:91],a[124:127]
RD a[236:239],v227 offset:24576
CV v23,v54,v55
MM a[124:127],a[188:191],v[92:95],a[124:127]
EX v59,v59
MM a[116:119],a[188:191],v[84:87],a[116:119]
EX v60,v60
MM a[116:119],a[184:187],v[80:83],a[116:119]
RD a[240:243],v224 offset:28672
CV v28,v56,v57
MM a[108:111],a[184:187],v[72:75],a[108:111]
EX v61,v61
MM a[108:111],a[188:191],v[76:79],a[108:111]
buffer_load_dwordx4 v235, s[12:15], s81 offen offset:2048 lds
MM a[100:103],a[188:191],v[68:71],a[100:103]
EX v62,v62
MM a[100:103],a[184:187],v[64:67],a[100:103]
RD a[244:247],v225 offset:28672
CV v29,v58,v59
v_smfmac_f32_16x16x64_bf16 v[236:239], v[144:147], v[0:7], v234
RD a[248:251],v226 offset:28672
RD a[252:255],v227 offset:28672
EX v63,v63
v_smfmac_f32_16x16x64_bf16 v[236:239], v[148:151], v[8:15], v234
CV v30,v60,v61
v_smfmac_f32_16x16x64_bf16 v[236:239], v[152:155], v[16:23], v234
CV v31,v62,v63
s_nop 1
v_smfmac_f32_16x16x64_bf16 v[236:239], v[156:159], v[24:31], v234
s_waitcnt lgkmcnt(0)
buffer_load_dwordx4 v235, s[12:15], s64 offen offset:3072 lds
s_waitcnt lgkmcnt(0)
s_barrier
v_mov_b32 v113, s79
RD v[104:107],v113 offset:32
RD v[108:111],v113 offset:48
s_waitcnt lgkmcnt(0)
v_min3_u32 v114, v104, v105, v106
v_min3_u32 v115, v107, v108, v109
v_min3_u32 v114, v114, v110, v111
v_min_u32 v114, v114, v115
s_nop 0
v_readfirstlane_b32 s76, v114
s_nop 0
s_sub_u32 s92, s92, s76
s_add_u32 s76, s76, 1
v_subrev_u32 v104, s76, v104
v_subrev_u32 v105, s76, v105
v_subrev_u32 v106, s76, v106
v_subrev_u32 v107, s76, v107
v_subrev_u32 v108, s76, v108
v_subrev_u32 v109, s76, v109
v_subrev_u32 v110, s76, v110
v_subrev_u32 v111, s76, v111
s_and_b32 s77, s92, 65535
s_sub_u32 s58, 65535, s77
v_min3_u32 v114, v104, v105, v106
v_min3_u32 v115, v107, v108, v109
v_min3_u32 v114, v114, v110, v111
v_min_u32 v114, v114, v115
s_nop 0
v_readfirstlane_b32 s76, v114
s_nop 0
s_sub_u32 s92, s92, s76
s_sub_u32 s92, s92, 1
s_and_b32 s77, s92, 65535
s_sub_u32 s59, 65535, s77
s_sub_u32 s92, s92, 1
s_lshl_b32 s90, %[swave], 8
s_add_u32 s90, s90, %[sb]
s_cmp_lg_u32 s90, 0
s_cbranch_scc1 LIX_%=
v_writelane_b32 v112, s52, 0
v_writelane_b32 v112, s53, 1
v_writelane_b32 v112, s54, 2
v_writelane_b32 v112, s55, 3
v_writelane_b32 v112, s56, 4
v_writelane_b32 v112, s57, 5
v_writelane_b32 v112, s58, 6
v_writelane_b32 v112, s59, 7
v_mov_b32 v113, s69
v_lshrrev_b32 v116, 1, v248
s_mov_b32 s90, 86016
s_mov_b64 exec, 0xff
buffer_store_dwordx2 v[112:113], v116, s[20:23], s90 offen offset:0 sc1
s_mov_b64 exec, -1
s_nop 1
LIX_%=:
s_mov_b32 s70, 1
s_branch LLA_%=
.fill 435, 4, 0xbf800000
LPG29_%=:
s_cbranch_execz LTR1_%=
s_nop 0
LRG1o_%=:
s_mov_b32 s70, 30001
LRG1ol_%=:
s_sleep 2
buffer_load_dwordx4 v[200:203], v208, s[20:23], s93 offen offset:0 sc1
buffer_load_dwordx4 v[204:207], v208, s[20:23], s93 offen offset:256 sc1
s_waitcnt vmcnt(0)
v_xor_b32 v201, s69, v201
v_xor_b32 v203, s69, v203
v_xor_b32 v205, s69, v205
v_xor_b32 v207, s69, v207
v_or3_b32 v209, v201, v203, v205
v_or_b32 v209, v209, v207
v_cmp_ne_u32 vcc, 0, v209
s_cbranch_vccz LRG1ob_%=
s_sub_u32 s70, s70, 1
s_cbranch_scc0 LRG1ol_%=
s_branch LRG1ob_%=
LRFR_%=:
s_mov_b32 s91, 30001
LRFRl_%=:
s_sleep 2
buffer_load_dwordx4 v[48:51], v248, s[20:23], s90 offen offset:0 sc1
s_waitcnt vmcnt(0)
v_xor_b32 v48, s69, v48
v_xor_b32 v49, s69, v49
v_xor_b32 v50, s69, v50
v_xor_b32 v51, s69, v51
v_or3_b32 v52, v48, v49, v50
v_or_b32 v52, v52, v51
v_cmp_ne_u32 vcc, 0, v52
s_cbranch_vccz LRFRb_%=
s_sub_u32 s91, s91, 1
s_cbranch_scc0 LRFRl_%=
s_branch LRFRb_%=
LRIX_%=:
s_mov_b32 s91, 30001
LRIXl_%=:
s_sleep 2
buffer_load_dwordx2 v[48:49], v52, s[20:23], s90 offen offset:0 sc1
s_waitcnt vmcnt(0)
v_cmp_ne_u32 vcc, s69, v49
s_cbranch_vccz LRIXb_%=
s_sub_u32 s91, s91, 1
s_cbranch_scc0 LRIXl_%=
s_branch LRIXb_%=
LRFG_%=:
s_mov_b32 s91, 30001
LRFGl_%=:
s_sleep 2
buffer_load_dword v252, v253, s[20:23], s76 offen offset:0 sc1
s_waitcnt vmcnt(0)
v_cmp_ne_u32 vcc, s69, v252
s_cbranch_vccz LRFGb_%=
s_sub_u32 s91, s91, 1
s_cbranch_scc0 LRFGl_%=
s_branch LRFGb_%=
LRFH_%=:
s_mov_b32 s91, 30001
LRFHl_%=:
s_sleep 2
buffer_load_dword v252, v253, s[20:23], s76 offen offset:0 sc1
s_waitcnt vmcnt(0)
v_cmp_ne_u32 vcc, s69, v252
s_cbranch_vccz LRFHb_%=
s_sub_u32 s91, s91, 1
s_cbranch_scc0 LRFHl_%=
s_branch LRFHb_%=
LRPO0_%=:
s_mov_b32 s97, 30001
LRPO0l_%=:
s_sleep 2
buffer_load_dword v242, v243, s[20:23], s96 offen offset:0 sc1
s_waitcnt vmcnt(0)
v_cmp_ne_u32 vcc, s69, v242
s_cbranch_vccz LRPO0b_%=
s_sub_u32 s97, s97, 1
s_cbranch_scc0 LRPO0l_%=
s_branch LRPO0b_%=
LRPO1_%=:
s_mov_b32 s97, 30001
LRPO1l_%=:
s_sleep 2
buffer_load_dword v242, v243, s[20:23], s96 offen offset:16 sc1
s_waitcnt vmcnt(0)
v_cmp_ne_u32 vcc, s69, v242
s_cbranch_vccz LRPO1b_%=
s_sub_u32 s97, s97, 1
s_cbranch_scc0 LRPO1l_%=
s_branch LRPO1b_%=
LRYF_%=:
s_mov_b32 s91, 30001
LRYFl_%=:
s_sleep 2
buffer_load_dword v132, v135, s[20:23], s78 offen offset:0 sc1
buffer_load_dword v133, v135, s[20:23], s78 offen offset:960 sc1
buffer_load_dword v134, v135, s[20:23], s78 offen offset:1920 sc1
s_waitcnt vmcnt(0)
v_xor_b32 v132, s69, v132
v_xor_b32 v133, s69, v133
v_xor_b32 v134, s69, v134
v_or3_b32 v139, v132, v133, v134
v_cmp_ne_u32 vcc, 0, v139
s_cbranch_vccz LRYFb_%=
s_sub_u32 s91, s91, 1
s_cbranch_scc0 LRYFl_%=
s_branch LRYFb_%=
LQA_%=:
s_bitcmp0_b32 s17, 1
s_cbranch_scc1 LQG1_%=
AR v108,a35
AR v109,a39
AR v110,a43
AR v111,a47
v_cmp_eq_u32 s[24:25], -1, v108
v_cmp_eq_u32 s[26:27], -1, v109
v_cmp_eq_u32 s[28:29], -1, v110
v_cmp_eq_u32 s[46:47], -1, v111
s_nop 0
s_or_b64 s[30:31], s[24:25], s[26:27]
s_or_b64 s[30:31], s[30:31], s[28:29]
s_or_b64 s[30:31], s[30:31], s[46:47]
s_cselect_b32 s76, 256, 2
s_or_b32 s94, s94, s76
s_bitcmp1_b32 s94, 1
s_cbranch_scc1 LQG1_%=
s_and_b64 exec, s[46:47], s[10:11]
s_cbranch_scc0 LQK13_%=
s_add_u32 s66, s44, 6144
buffer_load_dwordx4 a[44:47], v250, s[20:23], s66 offen offset:0 sc1
LQK13_%=:
s_and_b64 exec, s[28:29], s[8:9]
s_cbranch_scc0 LQK12_%=
s_add_u32 s66, s16, 6144
buffer_load_dwordx4 a[40:43], v250, s[20:23], s66 offen offset:0 sc1
LQK12_%=:
s_and_b64 exec, s[26:27], s[10:11]
s_cbranch_scc0 LQK11_%=
s_add_u32 s66, s44, 4096
buffer_load_dwordx4 a[36:39], v250, s[20:23], s66 offen offset:0 sc1
LQK11_%=:
s_and_b64 exec, s[24:25], s[8:9]
s_cbranch_scc0 LQK10_%=
s_add_u32 s66, s16, 4096
buffer_load_dwordx4 a[32:35], v250, s[20:23], s66 offen offset:0 sc1
LQK10_%=:
s_mov_b64 exec, -1
LQG1_%=:
s_bitcmp0_b32 s17, 0
s_cbranch_scc1 LQG0_%=
AR v108,a3
AR v109,a7
AR v110,a11
AR v111,a15
v_cmp_eq_u32 s[24:25], -1, v108
v_cmp_eq_u32 s[26:27], -1, v109
v_cmp_eq_u32 s[28:29], -1, v110
v_cmp_eq_u32 s[46:47], -1, v111
s_nop 0
s_or_b64 s[30:31], s[24:25], s[26:27]
s_or_b64 s[30:31], s[30:31], s[28:29]
s_or_b64 s[30:31], s[30:31], s[46:47]
s_cselect_b32 s76, 256, 1
s_or_b32 s94, s94, s76
s_bitcmp1_b32 s94, 0
s_cbranch_scc1 LQG0_%=
s_and_b64 exec, s[46:47], s[38:39]
s_cbranch_scc0 LQK03_%=
s_add_u32 s66, s44, 2048
buffer_load_dwordx4 a[12:15], v250, s[20:23], s66 offen offset:0 sc1
LQK03_%=:
s_and_b64 exec, s[28:29], s[36:37]
s_cbranch_scc0 LQK02_%=
s_add_u32 s66, s16, 2048
buffer_load_dwordx4 a[8:11], v250, s[20:23], s66 offen offset:0 sc1
LQK02_%=:
s_and_b64 exec, s[26:27], s[38:39]
s_cbranch_scc0 LQK01_%=
s_add_u32 s66, s44, 0
buffer_load_dwordx4 a[4:7], v250, s[20:23], s66 offen offset:0 sc1
LQK01_%=:
s_and_b64 exec, s[24:25], s[36:37]
s_cbranch_scc0 LQK00_%=
s_add_u32 s66, s16, 0
buffer_load_dwordx4 a[0:3], v250, s[20:23], s66 offen offset:0 sc1
LQK00_%=:
s_mov_b64 exec, -1
LQG0_%=:
s_branch LQB_%=
LQC_%=:
s_bitcmp0_b32 s94, 0
s_cbranch_scc1 LQN0_%=
MM v[72:75],v[64:67],a[0:3],0
MM v[72:75],v[64:67],a[4:7],v[72:75]
MM v[72:75],v[68:71],a[8:11],v[72:75]
MM v[72:75],v[68:71],a[12:15],v[72:75]
s_bitset0_b32 s17, 0
s_nop 6
CV v76,v72,v73
CV v77,v74,v75
ds_bpermute_b32 v76, v232, v76
ds_bpermute_b32 v77, v232, v77
v_lshlrev_b32 v10, 16, v0
v_and_b32 v11, 0xffff0000, v0
v_lshlrev_b32 v12, 16, v1
v_and_b32 v13, 0xffff0000, v1
v_lshlrev_b32 v14, 16, v32
v_and_b32 v15, 0xffff0000, v32
v_lshlrev_b32 v42, 16, v33
v_and_b32 v43, 0xffff0000, v33
s_add_u32 s80, s18, 0
s_waitcnt lgkmcnt(0)
v_lshlrev_b32 v72, 16, v76
v_and_b32 v73, 0xffff0000, v76
v_lshlrev_b32 v74, 16, v77
v_and_b32 v75, 0xffff0000, v77
v_mul_f32 v72, v72, v10
v_mul_f32 v73, v73, v11
v_mul_f32 v74, v74, v12
v_mul_f32 v75, v75, v13
CV v76,v72,v73
CV v77,v74,v75
v_lshlrev_b32 v72, 16, v76
v_and_b32 v73, 0xffff0000, v76
v_lshlrev_b32 v74, 16, v77
v_and_b32 v75, 0xffff0000, v77
v_add_f32 v72, v72, v14
v_add_f32 v73, v73, v15
v_add_f32 v74, v74, v42
v_add_f32 v75, v75, v43
CV v44,v72,v73
CV v45,v74,v75
buffer_store_dwordx2 v[44:45], v251, s[40:43], s80 offen offset:0 nt
s_bitcmp0_b32 s94, 8
s_cbranch_scc1 LQN0_%=
s_waitcnt vmcnt(1)
s_branch LQS_%=
LQN0_%=:
s_bitcmp0_b32 s94, 1
s_cbranch_scc1 LQN1_%=
MM v[72:75],v[64:67],a[32:35],0
MM v[72:75],v[64:67],a[36:39],v[72:75]
MM v[72:75],v[68:71],a[40:43],v[72:75]
MM v[72:75],v[68:71],a[44:47],v[72:75]
s_bitset0_b32 s17, 1
s_nop 6
CV v76,v72,v73
CV v77,v74,v75
ds_bpermute_b32 v76, v232, v76
ds_bpermute_b32 v77, v232, v77
v_lshlrev_b32 v10, 16, v2
v_and_b32 v11, 0xffff0000, v2
v_lshlrev_b32 v12, 16, v3
v_and_b32 v13, 0xffff0000, v3
v_lshlrev_b32 v14, 16, v34
v_and_b32 v15, 0xffff0000, v34
v_lshlrev_b32 v42, 16, v35
v_and_b32 v43, 0xffff0000, v35
s_add_u32 s80, s18, 128
s_waitcnt lgkmcnt(0)
v_lshlrev_b32 v72, 16, v76
v_and_b32 v73, 0xffff0000, v76
v_lshlrev_b32 v74, 16, v77
v_and_b32 v75, 0xffff0000, v77
v_mul_f32 v72, v72, v10
v_mul_f32 v73, v73, v11
v_mul_f32 v74, v74, v12
v_mul_f32 v75, v75, v13
CV v76,v72,v73
CV v77,v74,v75
v_lshlrev_b32 v72, 16, v76
v_and_b32 v73, 0xffff0000, v76
v_lshlrev_b32 v74, 16, v77
v_and_b32 v75, 0xffff0000, v77
v_add_f32 v72, v72, v14
v_add_f32 v73, v73, v15
v_add_f32 v74, v74, v42
v_add_f32 v75, v75, v43
CV v46,v72,v73
CV v47,v74,v75
buffer_store_dwordx2 v[46:47], v251, s[40:43], s80 offen offset:0 nt
s_bitcmp0_b32 s94, 8
s_cbranch_scc1 LQN1_%=
s_waitcnt vmcnt(1)
s_branch LQS_%=
LQN1_%=:
s_branch LQD_%=
LSW_%=:
s_sleep 1
s_waitcnt vmcnt(0)
s_cmp_eq_u32 s65, 0
s_cbranch_scc1 LSX_%=
s_mov_b64 exec, 1
buffer_store_dword v233, off, s[20:23], s19 offset:12 sc1
s_mov_b64 exec, -1
s_mov_b32 s65, 0
LSX_%=:
v_cmp_ne_u32 vcc, s69, v243
v_cmp_ne_u32 s[36:37], s69, v143
s_nop 1
s_or_b64 vcc, vcc, s[36:37]
s_cselect_b32 s76, 0, 1
s_add_u32 s72, s72, s76
s_sub_u32 s73, s73, 1
s_cselect_b32 s72, 2, s72
s_bitcmp0_b32 s17, 4
s_cbranch_scc1 LSN4_%=
MM v[72:75],v[64:67],a[192:195],0
MM v[72:75],v[64:67],a[196:199],v[72:75]
MM v[72:75],v[68:71],v[144:147],v[72:75]
MM v[72:75],v[68:71],a[212:215],v[72:75]
AR v76,a195
AR v77,a199
v_mov_b32 v78, v147
AR v79,a215
s_nop 3
v_cmp_u_f32 s[30:31], v72, v72
s_nop 1
s_cmp_eq_u64 s[30:31], 0
s_cbranch_scc1 LSF4_%=
s_cmp_ge_u32 s72, 2
s_cbranch_scc1 LSF4_%=
v_cmp_eq_u32 vcc, s69, v242
)ASM"
R"ASM(
v_cmp_eq_u32 s[36:37], -1, v78
s_add_u32 s66, s16, 18432
s_and_b64 vcc, vcc, s[36:37]
s_cmp_eq_u32 s72, 0
s_cselect_b64 exec, vcc, -1
s_and_b32 exec_lo, exec_lo, 0xffff0000
buffer_load_dwordx4 v[144:147], v250, s[20:23], s66 offen offset:0 sc1
s_mov_b64 exec, -1
v_cmp_eq_u32 vcc, s69, v142
v_cmp_eq_u32 s[36:37], -1, v79
s_add_u32 s66, s44, 18432
s_and_b64 vcc, vcc, s[36:37]
s_cmp_eq_u32 s72, 0
s_cselect_b64 exec, vcc, -1
buffer_load_dwordx4 a[212:215], v250, s[20:23], s66 offen offset:0 sc1
s_mov_b64 exec, -1
v_cmp_eq_u32 vcc, s69, v241
v_cmp_eq_u32 s[36:37], -1, v76
s_add_u32 s66, s16, 8192
s_and_b64 vcc, vcc, s[36:37]
s_cmp_eq_u32 s72, 0
s_cselect_b64 exec, vcc, -1
s_and_b32 exec_lo, exec_lo, 0xffff0000
buffer_load_dwordx4 a[192:195], v250, s[20:23], s66 offen offset:0 sc1
s_mov_b64 exec, -1
v_cmp_eq_u32 vcc, s69, v141
v_cmp_eq_u32 s[36:37], -1, v77
s_add_u32 s66, s44, 8192
s_and_b64 vcc, vcc, s[36:37]
s_cmp_eq_u32 s72, 0
s_cselect_b64 exec, vcc, -1
buffer_load_dwordx4 a[196:199], v250, s[20:23], s66 offen offset:0 sc1
s_mov_b64 exec, -1
s_branch LSN4_%=
LSF4_%=:
s_bitset0_b32 s17, 4
CV v76,v72,v73
CV v77,v74,v75
ds_bpermute_b32 v76, v232, v76
ds_bpermute_b32 v77, v232, v77
v_lshlrev_b32 v10, 16, v8
v_and_b32 v11, 0xffff0000, v8
v_lshlrev_b32 v12, 16, v9
v_and_b32 v13, 0xffff0000, v9
v_lshlrev_b32 v14, 16, v40
v_and_b32 v15, 0xffff0000, v40
v_lshlrev_b32 v42, 16, v41
v_and_b32 v43, 0xffff0000, v41
s_add_u32 s80, s63, 0
s_waitcnt lgkmcnt(0)
v_lshlrev_b32 v72, 16, v76
v_and_b32 v73, 0xffff0000, v76
v_lshlrev_b32 v74, 16, v77
v_and_b32 v75, 0xffff0000, v77
v_mul_f32 v72, v72, v10
v_mul_f32 v73, v73, v11
v_mul_f32 v74, v74, v12
v_mul_f32 v75, v75, v13
CV v76,v72,v73
CV v77,v74,v75
v_lshlrev_b32 v72, 16, v76
v_and_b32 v73, 0xffff0000, v76
v_lshlrev_b32 v74, 16, v77
v_and_b32 v75, 0xffff0000, v77
v_add_f32 v72, v72, v14
v_add_f32 v73, v73, v15
v_add_f32 v74, v74, v42
v_add_f32 v75, v75, v43
CV v44,v72,v73
CV v45,v74,v75
buffer_store_dwordx2 v[44:45], v254, s[40:43], s80 offen offset:0 nt
s_nop 1
LSN4_%=:
s_bitcmp0_b32 s17, 3
s_cbranch_scc1 LSN3_%=
MM v[72:75],v[64:67],v[80:83],0
MM v[72:75],v[64:67],a[100:103],v[72:75]
MM v[72:75],v[68:71],v[112:115],v[72:75]
MM v[72:75],v[68:71],a[108:111],v[72:75]
v_mov_b32 v76, v83
AR v77,a103
v_mov_b32 v78, v115
AR v79,a111
s_nop 3
v_cmp_u_f32 s[30:31], v72, v72
s_nop 1
s_cmp_eq_u64 s[30:31], 0
s_cbranch_scc1 LSF3_%=
s_cmp_ge_u32 s72, 2
s_cbranch_scc1 LSF3_%=
v_cmp_eq_u32 vcc, s69, v242
v_cmp_eq_u32 s[36:37], -1, v78
s_add_u32 s66, s16, 16384
s_and_b64 vcc, vcc, s[36:37]
s_cmp_eq_u32 s72, 0
s_cselect_b64 exec, vcc, -1
s_and_b32 exec_lo, exec_lo, 0xffff0000
buffer_load_dwordx4 v[112:115], v250, s[20:23], s66 offen offset:0 sc1
s_mov_b64 exec, -1
v_cmp_eq_u32 vcc, s69, v142
v_cmp_eq_u32 s[36:37], -1, v79
s_add_u32 s66, s44, 16384
s_and_b64 vcc, vcc, s[36:37]
s_cmp_eq_u32 s72, 0
s_cselect_b64 exec, vcc, -1
buffer_load_dwordx4 a[108:111], v250, s[20:23], s66 offen offset:0 sc1
s_mov_b64 exec, -1
v_cmp_eq_u32 vcc, s69, v242
v_cmp_eq_u32 s[36:37], -1, v76
s_add_u32 s66, s16, 14336
s_and_b64 vcc, vcc, s[36:37]
s_cmp_eq_u32 s72, 0
s_cselect_b64 exec, vcc, -1
s_and_b32 exec_lo, exec_lo, 0xffff0000
buffer_load_dwordx4 v[80:83], v250, s[20:23], s66 offen offset:0 sc1
s_mov_b64 exec, -1
v_cmp_eq_u32 vcc, s69, v142
v_cmp_eq_u32 s[36:37], -1, v77
s_add_u32 s66, s44, 14336
s_and_b64 vcc, vcc, s[36:37]
s_cmp_eq_u32 s72, 0
s_cselect_b64 exec, vcc, -1
buffer_load_dwordx4 a[100:103], v250, s[20:23], s66 offen offset:0 sc1
s_mov_b64 exec, -1
s_branch LSN3_%=
LSF3_%=:
s_bitset0_b32 s17, 3
CV v76,v72,v73
CV v77,v74,v75
ds_bpermute_b32 v76, v232, v76
ds_bpermute_b32 v77, v232, v77
v_lshlrev_b32 v10, 16, v6
v_and_b32 v11, 0xffff0000, v6
v_lshlrev_b32 v12, 16, v7
v_and_b32 v13, 0xffff0000, v7
v_lshlrev_b32 v14, 16, v38
v_and_b32 v15, 0xffff0000, v38
v_lshlrev_b32 v42, 16, v39
v_and_b32 v43, 0xffff0000, v39
s_add_u32 s80, s18, 41088
s_waitcnt lgkmcnt(0)
v_lshlrev_b32 v72, 16, v76
v_and_b32 v73, 0xffff0000, v76
v_lshlrev_b32 v74, 16, v77
v_and_b32 v75, 0xffff0000, v77
v_mul_f32 v72, v72, v10
v_mul_f32 v73, v73, v11
v_mul_f32 v74, v74, v12
v_mul_f32 v75, v75, v13
CV v76,v72,v73
CV v77,v74,v75
v_lshlrev_b32 v72, 16, v76
v_and_b32 v73, 0xffff0000, v76
v_lshlrev_b32 v74, 16, v77
v_and_b32 v75, 0xffff0000, v77
v_add_f32 v72, v72, v14
v_add_f32 v73, v73, v15
v_add_f32 v74, v74, v42
v_add_f32 v75, v75, v43
CV v46,v72,v73
CV v47,v74,v75
buffer_store_dwordx2 v[46:47], v251, s[40:43], s80 offen offset:0 nt
s_nop 1
LSN3_%=:
s_bitcmp0_b32 s17, 2
s_cbranch_scc1 LSN2_%=
MM v[72:75],v[64:67],v[16:19],0
MM v[72:75],v[64:67],a[68:71],v[72:75]
MM v[72:75],v[68:71],v[48:51],v[72:75]
MM v[72:75],v[68:71],a[76:79],v[72:75]
v_mov_b32 v76, v19
AR v77,a71
v_mov_b32 v78, v51
AR v79,a79
s_nop 3
v_cmp_u_f32 s[30:31], v72, v72
s_nop 1
s_cmp_eq_u64 s[30:31], 0
s_cbranch_scc1 LSF2_%=
s_cmp_ge_u32 s72, 2
s_cbranch_scc1 LSF2_%=
v_cmp_eq_u32 vcc, s69, v242
v_cmp_eq_u32 s[36:37], -1, v78
s_add_u32 s66, s16, 12288
s_and_b64 vcc, vcc, s[36:37]
s_cmp_eq_u32 s72, 0
s_cselect_b64 exec, vcc, -1
s_and_b32 exec_lo, exec_lo, 0xffff0000
buffer_load_dwordx4 v[48:51], v250, s[20:23], s66 offen offset:0 sc1
s_mov_b64 exec, -1
v_cmp_eq_u32 vcc, s69, v142
v_cmp_eq_u32 s[36:37], -1, v79
s_add_u32 s66, s44, 12288
s_and_b64 vcc, vcc, s[36:37]
s_cmp_eq_u32 s72, 0
s_cselect_b64 exec, vcc, -1
buffer_load_dwordx4 a[76:79], v250, s[20:23], s66 offen offset:0 sc1
s_mov_b64 exec, -1
v_cmp_eq_u32 vcc, s69, v242
v_cmp_eq_u32 s[36:37], -1, v76
s_add_u32 s66, s16, 10240
s_and_b64 vcc, vcc, s[36:37]
s_cmp_eq_u32 s72, 0
s_cselect_b64 exec, vcc, -1
s_and_b32 exec_lo, exec_lo, 0xffff0000
buffer_load_dwordx4 v[16:19], v250, s[20:23], s66 offen offset:0 sc1
s_mov_b64 exec, -1
v_cmp_eq_u32 vcc, s69, v142
v_cmp_eq_u32 s[36:37], -1, v77
s_add_u32 s66, s44, 10240
s_and_b64 vcc, vcc, s[36:37]
s_cmp_eq_u32 s72, 0
s_cselect_b64 exec, vcc, -1
buffer_load_dwordx4 a[68:71], v250, s[20:23], s66 offen offset:0 sc1
s_mov_b64 exec, -1
s_branch LSN2_%=
LSF2_%=:
s_bitset0_b32 s17, 2
CV v76,v72,v73
CV v77,v74,v75
ds_bpermute_b32 v76, v232, v76
ds_bpermute_b32 v77, v232, v77
v_lshlrev_b32 v10, 16, v4
v_and_b32 v11, 0xffff0000, v4
v_lshlrev_b32 v12, 16, v5
v_and_b32 v13, 0xffff0000, v5
v_lshlrev_b32 v14, 16, v36
v_and_b32 v15, 0xffff0000, v36
v_lshlrev_b32 v42, 16, v37
v_and_b32 v43, 0xffff0000, v37
s_add_u32 s80, s18, 40960
s_waitcnt lgkmcnt(0)
v_lshlrev_b32 v72, 16, v76
v_and_b32 v73, 0xffff0000, v76
v_lshlrev_b32 v74, 16, v77
v_and_b32 v75, 0xffff0000, v77
v_mul_f32 v72, v72, v10
v_mul_f32 v73, v73, v11
v_mul_f32 v74, v74, v12
v_mul_f32 v75, v75, v13
CV v76,v72,v73
CV v77,v74,v75
v_lshlrev_b32 v72, 16, v76
v_and_b32 v73, 0xffff0000, v76
v_lshlrev_b32 v74, 16, v77
v_and_b32 v75, 0xffff0000, v77
v_add_f32 v72, v72, v14
v_add_f32 v73, v73, v15
v_add_f32 v74, v74, v42
v_add_f32 v75, v75, v43
CV v44,v72,v73
CV v45,v74,v75
buffer_store_dwordx2 v[44:45], v251, s[40:43], s80 offen offset:0 nt
s_nop 1
LSN2_%=:
s_bitcmp0_b32 s17, 1
s_cbranch_scc1 LSN1_%=
MM v[72:75],v[64:67],a[32:35],0
MM v[72:75],v[64:67],a[36:39],v[72:75]
MM v[72:75],v[68:71],a[40:43],v[72:75]
MM v[72:75],v[68:71],a[44:47],v[72:75]
AR v76,a35
AR v77,a39
AR v78,a43
AR v79,a47
s_nop 3
v_cmp_u_f32 s[30:31], v72, v72
s_nop 1
s_cmp_eq_u64 s[30:31], 0
s_cbranch_scc1 LSF1_%=
s_cmp_ge_u32 s72, 2
s_cbranch_scc1 LSF1_%=
v_cmp_eq_u32 vcc, s69, v241
v_cmp_eq_u32 s[36:37], -1, v78
s_add_u32 s66, s16, 6144
s_and_b64 vcc, vcc, s[36:37]
s_cmp_eq_u32 s72, 0
s_cselect_b64 exec, vcc, -1
s_and_b32 exec_lo, exec_lo, 0xffff0000
buffer_load_dwordx4 a[40:43], v250, s[20:23], s66 offen offset:0 sc1
s_mov_b64 exec, -1
v_cmp_eq_u32 vcc, s69, v141
v_cmp_eq_u32 s[36:37], -1, v79
s_add_u32 s66, s44, 6144
s_and_b64 vcc, vcc, s[36:37]
s_cmp_eq_u32 s72, 0
s_cselect_b64 exec, vcc, -1
buffer_load_dwordx4 a[44:47], v250, s[20:23], s66 offen offset:0 sc1
s_mov_b64 exec, -1
v_cmp_eq_u32 vcc, s69, v241
v_cmp_eq_u32 s[36:37], -1, v76
s_add_u32 s66, s16, 4096
s_and_b64 vcc, vcc, s[36:37]
s_cmp_eq_u32 s72, 0
s_cselect_b64 exec, vcc, -1
s_and_b32 exec_lo, exec_lo, 0xffff0000
buffer_load_dwordx4 a[32:35], v250, s[20:23], s66 offen offset:0 sc1
s_mov_b64 exec, -1
v_cmp_eq_u32 vcc, s69, v141
v_cmp_eq_u32 s[36:37], -1, v77
s_add_u32 s66, s44, 4096
s_and_b64 vcc, vcc, s[36:37]
s_cmp_eq_u32 s72, 0
s_cselect_b64 exec, vcc, -1
buffer_load_dwordx4 a[36:39], v250, s[20:23], s66 offen offset:0 sc1
s_mov_b64 exec, -1
s_branch LSN1_%=
LSF1_%=:
s_bitset0_b32 s17, 1
CV v76,v72,v73
CV v77,v74,v75
ds_bpermute_b32 v76, v232, v76
ds_bpermute_b32 v77, v232, v77
v_lshlrev_b32 v10, 16, v2
v_and_b32 v11, 0xffff0000, v2
v_lshlrev_b32 v12, 16, v3
v_and_b32 v13, 0xffff0000, v3
v_lshlrev_b32 v14, 16, v34
v_and_b32 v15, 0xffff0000, v34
v_lshlrev_b32 v42, 16, v35
v_and_b32 v43, 0xffff0000, v35
s_add_u32 s80, s18, 128
s_waitcnt lgkmcnt(0)
v_lshlrev_b32 v72, 16, v76
v_and_b32 v73, 0xffff0000, v76
v_lshlrev_b32 v74, 16, v77
v_and_b32 v75, 0xffff0000, v77
v_mul_f32 v72, v72, v10
v_mul_f32 v73, v73, v11
v_mul_f32 v74, v74, v12
v_mul_f32 v75, v75, v13
CV v76,v72,v73
CV v77,v74,v75
v_lshlrev_b32 v72, 16, v76
v_and_b32 v73, 0xffff0000, v76
v_lshlrev_b32 v74, 16, v77
v_and_b32 v75, 0xffff0000, v77
v_add_f32 v72, v72, v14
v_add_f32 v73, v73, v15
v_add_f32 v74, v74, v42
v_add_f32 v75, v75, v43
CV v46,v72,v73
CV v47,v74,v75
buffer_store_dwordx2 v[46:47], v251, s[40:43], s80 offen offset:0 nt
s_nop 1
LSN1_%=:
s_bitcmp0_b32 s17, 0
s_cbranch_scc1 LSN0_%=
MM v[72:75],v[64:67],a[0:3],0
MM v[72:75],v[64:67],a[4:7],v[72:75]
MM v[72:75],v[68:71],a[8:11],v[72:75]
MM v[72:75],v[68:71],a[12:15],v[72:75]
AR v76,a3
AR v77,a7
AR v78,a11
AR v79,a15
s_nop 3
v_cmp_u_f32 s[30:31], v72, v72
s_nop 1
s_cmp_eq_u64 s[30:31], 0
s_cbranch_scc1 LSF0_%=
s_cmp_ge_u32 s72, 2
s_cbranch_scc1 LSF0_%=
v_cmp_eq_u32 vcc, s69, v240
v_cmp_eq_u32 s[36:37], -1, v78
s_add_u32 s66, s16, 2048
s_and_b64 vcc, vcc, s[36:37]
s_cmp_eq_u32 s72, 0
s_cselect_b64 exec, vcc, -1
s_and_b32 exec_lo, exec_lo, 0xffff0000
buffer_load_dwordx4 a[8:11], v250, s[20:23], s66 offen offset:0 sc1
s_mov_b64 exec, -1
v_cmp_eq_u32 vcc, s69, v140
v_cmp_eq_u32 s[36:37], -1, v79
s_add_u32 s66, s44, 2048
s_and_b64 vcc, vcc, s[36:37]
s_cmp_eq_u32 s72, 0
s_cselect_b64 exec, vcc, -1
buffer_load_dwordx4 a[12:15], v250, s[20:23], s66 offen offset:0 sc1
s_mov_b64 exec, -1
v_cmp_eq_u32 vcc, s69, v240
v_cmp_eq_u32 s[36:37], -1, v76
s_add_u32 s66, s16, 0
s_and_b64 vcc, vcc, s[36:37]
s_cmp_eq_u32 s72, 0
s_cselect_b64 exec, vcc, -1
s_and_b32 exec_lo, exec_lo, 0xffff0000
buffer_load_dwordx4 a[0:3], v250, s[20:23], s66 offen offset:0 sc1
s_mov_b64 exec, -1
v_cmp_eq_u32 vcc, s69, v140
v_cmp_eq_u32 s[36:37], -1, v77
s_add_u32 s66, s44, 0
s_and_b64 vcc, vcc, s[36:37]
s_cmp_eq_u32 s72, 0
s_cselect_b64 exec, vcc, -1
buffer_load_dwordx4 a[4:7], v250, s[20:23], s66 offen offset:0 sc1
s_mov_b64 exec, -1
s_branch LSN0_%=
LSF0_%=:
s_bitset0_b32 s17, 0
CV v76,v72,v73
CV v77,v74,v75
ds_bpermute_b32 v76, v232, v76
ds_bpermute_b32 v77, v232, v77
v_lshlrev_b32 v10, 16, v0
v_and_b32 v11, 0xffff0000, v0
v_lshlrev_b32 v12, 16, v1
v_and_b32 v13, 0xffff0000, v1
v_lshlrev_b32 v14, 16, v32
v_and_b32 v15, 0xffff0000, v32
v_lshlrev_b32 v42, 16, v33
v_and_b32 v43, 0xffff0000, v33
s_add_u32 s80, s18, 0
s_waitcnt lgkmcnt(0)
v_lshlrev_b32 v72, 16, v76
v_and_b32 v73, 0xffff0000, v76
v_lshlrev_b32 v74, 16, v77
v_and_b32 v75, 0xffff0000, v77
v_mul_f32 v72, v72, v10
v_mul_f32 v73, v73, v11
v_mul_f32 v74, v74, v12
v_mul_f32 v75, v75, v13
CV v76,v72,v73
CV v77,v74,v75
v_lshlrev_b32 v72, 16, v76
v_and_b32 v73, 0xffff0000, v76
v_lshlrev_b32 v74, 16, v77
v_and_b32 v75, 0xffff0000, v77
v_add_f32 v72, v72, v14
v_add_f32 v73, v73, v15
v_add_f32 v74, v74, v42
v_add_f32 v75, v75, v43
CV v44,v72,v73
CV v45,v74,v75
buffer_store_dwordx2 v[44:45], v251, s[40:43], s80 offen offset:0 nt
s_nop 1
LSN0_%=:
v_cmp_ne_u32 vcc, s69, v242
v_cmp_ne_u32 s[36:37], s69, v142
s_nop 1
s_or_b64 vcc, vcc, s[36:37]
s_cselect_b32 s76, 0, 15
s_and_b32 s76, s76, s73
s_cbranch_scc1 LSU_%=
buffer_load_dwordx4 v[240:243], v253, s[20:23], s68 offen offset:0 sc1
)ASM"
R"ASM(
buffer_load_dwordx4 v[140:143], v253, s[20:23], s45 offen offset:0 sc1
LSU_%=:
s_or_b32 s76, s17, s65
s_cbranch_scc1 LSW_%=
s_branch LQE_%=
LKEND_ka:
LSITE0_%=:
s_mov_b32 s73, 0
s_mov_b32 s74, 0
s_mov_b32 s75, 16384
s_branch LRARE002_%=
LSITE2_%=:
s_mov_b32 s73, 2
s_mov_b32 s74, 32768
s_mov_b32 s75, 49152
s_branch LRARE002_%=
LRARE002_%=:
s_nop 15
s_waitcnt vmcnt(0) lgkmcnt(0)
v_add_u32 v249, s74, v224
RD a[192:195],v249 offset:0
RD a[208:211],v249 offset:4096
RD a[224:227],v249 offset:8192
RD a[240:243],v249 offset:12288
v_add_u32 v250, s74, v225
RD a[196:199],v250 offset:0
RD a[212:215],v250 offset:4096
RD a[228:231],v250 offset:8192
RD a[244:247],v250 offset:12288
v_add_u32 v251, s74, v226
RD a[200:203],v251 offset:0
RD a[216:219],v251 offset:4096
RD a[232:235],v251 offset:8192
RD a[248:251],v251 offset:12288
v_add_u32 v254, s74, v227
RD a[204:207],v254 offset:0
RD a[220:223],v254 offset:4096
RD a[236:239],v254 offset:8192
RD a[252:255],v254 offset:12288
s_waitcnt lgkmcnt(0)
MM v[0:3],a[192:195],v[160:163],v[128:131]
MM v[0:3],a[196:199],v[176:179],v[0:3]
MM v[0:3],a[200:203],v[192:195],v[0:3]
MM v[0:3],a[204:207],v[208:211],v[0:3]
MM v[4:7],a[220:223],v[208:211],v[128:131]
MM v[4:7],a[216:219],v[192:195],v[4:7]
MM v[4:7],a[212:215],v[176:179],v[4:7]
MM v[4:7],a[208:211],v[160:163],v[4:7]
MM v[12:15],a[208:211],v[164:167],v[132:135]
MM v[12:15],a[212:215],v[180:183],v[12:15]
MM v[12:15],a[216:219],v[196:199],v[12:15]
MM v[12:15],a[220:223],v[212:215],v[12:15]
MM v[8:11],a[204:207],v[212:215],v[132:135]
MM v[8:11],a[200:203],v[196:199],v[8:11]
MM v[8:11],a[196:199],v[180:183],v[8:11]
MM v[8:11],a[192:195],v[164:167],v[8:11]
MM v[32:35],a[224:227],v[160:163],v[128:131]
MM v[32:35],a[228:231],v[176:179],v[32:35]
MM v[32:35],a[232:235],v[192:195],v[32:35]
MM v[32:35],a[236:239],v[208:211],v[32:35]
MM v[36:39],a[252:255],v[208:211],v[128:131]
MM v[36:39],a[248:251],v[192:195],v[36:39]
MM v[36:39],a[244:247],v[176:179],v[36:39]
MM v[36:39],a[240:243],v[160:163],v[36:39]
MM v[44:47],a[240:243],v[164:167],v[132:135]
MM v[44:47],a[244:247],v[180:183],v[44:47]
MM v[44:47],a[248:251],v[196:199],v[44:47]
MM v[44:47],a[252:255],v[212:215],v[44:47]
MM v[40:43],a[236:239],v[212:215],v[132:135]
MM v[40:43],a[232:235],v[196:199],v[40:43]
MM v[40:43],a[228:231],v[180:183],v[40:43]
MM v[40:43],a[224:227],v[164:167],v[40:43]
s_nop 15
v_add_u32 v249, s75, v224
RD a[192:195],v249 offset:0
RD a[208:211],v249 offset:4096
RD a[224:227],v249 offset:8192
RD a[240:243],v249 offset:12288
v_add_u32 v250, s75, v225
RD a[196:199],v250 offset:0
RD a[212:215],v250 offset:4096
RD a[228:231],v250 offset:8192
RD a[244:247],v250 offset:12288
v_add_u32 v251, s75, v226
RD a[200:203],v251 offset:0
RD a[216:219],v251 offset:4096
RD a[232:235],v251 offset:8192
RD a[248:251],v251 offset:12288
v_add_u32 v254, s75, v227
RD a[204:207],v254 offset:0
RD a[220:223],v254 offset:4096
RD a[236:239],v254 offset:8192
RD a[252:255],v254 offset:12288
s_waitcnt lgkmcnt(0)
v_mov_b32 v236, v247
v_mov_b32 v237, 0
v_max3_f32 v249, v0, v1, v2
v_max3_f32 v251, v32, v33, v34
v_max3_f32 v249, v249, v3, v4
v_max3_f32 v251, v251, v35, v36
v_max3_f32 v249, v249, v5, v6
v_max3_f32 v251, v251, v37, v38
v_max3_f32 v249, v249, v7, v39
v_max_f32 v249, v249, v251
v_log_f32 v250, v247
s_lshl_b64 exec, 0xffff, 0
v_max_f32 v249, v249, v250
s_mov_b64 exec, -1
v_mov_b32 v251, v249
s_nop 1
v_permlane16_swap_b32 v249, v251
v_max_f32 v249, v249, v251
v_mov_b32 v251, v249
s_nop 1
v_permlane32_swap_b32 v249, v251
v_max_f32 v249, v249, v251
v_max_f32 v249, 0, v249
v_sub_f32 v250, 0, v249
EX v250,v250
v_sub_f32 v128, v128, v249
v_sub_f32 v129, v129, v249
v_sub_f32 v130, v130, v249
v_sub_f32 v131, v131, v249
v_sub_f32 v64, v64, v249
v_sub_f32 v0, v0, v249
v_sub_f32 v65, v65, v249
v_sub_f32 v1, v1, v249
v_sub_f32 v66, v66, v249
v_sub_f32 v2, v2, v249
v_sub_f32 v67, v67, v249
v_sub_f32 v3, v3, v249
v_sub_f32 v68, v68, v249
v_sub_f32 v4, v4, v249
v_sub_f32 v69, v69, v249
v_sub_f32 v5, v5, v249
v_sub_f32 v70, v70, v249
v_sub_f32 v6, v6, v249
v_sub_f32 v71, v71, v249
v_sub_f32 v7, v7, v249
v_sub_f32 v96, v96, v249
v_sub_f32 v32, v32, v249
v_sub_f32 v97, v97, v249
v_sub_f32 v33, v33, v249
v_sub_f32 v98, v98, v249
v_sub_f32 v34, v34, v249
v_sub_f32 v99, v99, v249
v_sub_f32 v35, v35, v249
v_sub_f32 v100, v100, v249
v_sub_f32 v36, v36, v249
v_sub_f32 v101, v101, v249
v_sub_f32 v37, v37, v249
v_sub_f32 v102, v102, v249
v_sub_f32 v38, v38, v249
v_sub_f32 v103, v103, v249
v_sub_f32 v39, v39, v249
s_lshl_b64 exec, 0xffff, 0
v_mul_f32 v236, v236, v250
s_mov_b64 exec, -1
AR v254,a0
v_mul_f32 v254, v254, v250
AW a0,v254
AR v251,a1
v_mul_f32 v251, v251, v250
AW a1,v251
AR v254,a2
v_mul_f32 v254, v254, v250
AW a2,v254
AR v251,a3
v_mul_f32 v251, v251, v250
AW a3,v251
AR v254,a4
v_mul_f32 v254, v254, v250
AW a4,v254
AR v251,a5
v_mul_f32 v251, v251, v250
AW a5,v251
AR v254,a6
v_mul_f32 v254, v254, v250
AW a6,v254
AR v251,a7
v_mul_f32 v251, v251, v250
AW a7,v251
AR v254,a32
v_mul_f32 v254, v254, v250
AW a32,v254
AR v251,a33
v_mul_f32 v251, v251, v250
AW a33,v251
AR v254,a34
v_mul_f32 v254, v254, v250
AW a34,v254
AR v251,a35
v_mul_f32 v251, v251, v250
AW a35,v251
AR v254,a36
v_mul_f32 v254, v254, v250
AW a36,v254
AR v251,a37
v_mul_f32 v251, v251, v250
AW a37,v251
AR v254,a38
v_mul_f32 v254, v254, v250
AW a38,v254
AR v251,a39
v_mul_f32 v251, v251, v250
AW a39,v251
AR v254,a64
v_mul_f32 v254, v254, v250
AW a64,v254
AR v251,a65
v_mul_f32 v251, v251, v250
AW a65,v251
AR v254,a66
v_mul_f32 v254, v254, v250
AW a66,v254
AR v251,a67
v_mul_f32 v251, v251, v250
AW a67,v251
AR v254,a68
v_mul_f32 v254, v254, v250
AW a68,v254
AR v251,a69
v_mul_f32 v251, v251, v250
AW a69,v251
AR v254,a70
v_mul_f32 v254, v254, v250
AW a70,v254
AR v251,a71
v_mul_f32 v251, v251, v250
AW a71,v251
AR v254,a96
v_mul_f32 v254, v254, v250
AW a96,v254
AR v251,a97
v_mul_f32 v251, v251, v250
AW a97,v251
AR v254,a98
v_mul_f32 v254, v254, v250
AW a98,v254
AR v251,a99
v_mul_f32 v251, v251, v250
AW a99,v251
AR v254,a100
v_mul_f32 v254, v254, v250
AW a100,v254
AR v251,a101
v_mul_f32 v251, v251, v250
AW a101,v251
AR v254,a102
v_mul_f32 v254, v254, v250
AW a102,v254
AR v251,a103
v_mul_f32 v251, v251, v250
AW a103,v251
v_max3_f32 v249, v8, v9, v10
v_max3_f32 v251, v40, v41, v42
v_max3_f32 v249, v249, v11, v12
v_max3_f32 v251, v251, v43, v44
v_max3_f32 v249, v249, v13, v14
v_max3_f32 v251, v251, v45, v46
v_max3_f32 v249, v249, v15, v47
v_max_f32 v249, v249, v251
v_log_f32 v250, v247
s_lshl_b64 exec, 0xffff, 16
v_max_f32 v249, v249, v250
s_mov_b64 exec, -1
v_mov_b32 v251, v249
s_nop 1
v_permlane16_swap_b32 v249, v251
v_max_f32 v249, v249, v251
v_mov_b32 v251, v249
s_nop 1
v_permlane32_swap_b32 v249, v251
v_max_f32 v249, v249, v251
v_max_f32 v249, 0, v249
v_sub_f32 v250, 0, v249
EX v250,v250
v_sub_f32 v132, v132, v249
v_sub_f32 v133, v133, v249
v_sub_f32 v134, v134, v249
v_sub_f32 v135, v135, v249
v_sub_f32 v72, v72, v249
v_sub_f32 v8, v8, v249
v_sub_f32 v73, v73, v249
v_sub_f32 v9, v9, v249
v_sub_f32 v74, v74, v249
v_sub_f32 v10, v10, v249
v_sub_f32 v75, v75, v249
v_sub_f32 v11, v11, v249
v_sub_f32 v76, v76, v249
v_sub_f32 v12, v12, v249
v_sub_f32 v77, v77, v249
v_sub_f32 v13, v13, v249
v_sub_f32 v78, v78, v249
v_sub_f32 v14, v14, v249
v_sub_f32 v79, v79, v249
v_sub_f32 v15, v15, v249
v_sub_f32 v104, v104, v249
v_sub_f32 v40, v40, v249
v_sub_f32 v105, v105, v249
v_sub_f32 v41, v41, v249
v_sub_f32 v106, v106, v249
v_sub_f32 v42, v42, v249
v_sub_f32 v107, v107, v249
v_sub_f32 v43, v43, v249
v_sub_f32 v108, v108, v249
v_sub_f32 v44, v44, v249
v_sub_f32 v109, v109, v249
v_sub_f32 v45, v45, v249
v_sub_f32 v110, v110, v249
v_sub_f32 v46, v46, v249
v_sub_f32 v111, v111, v249
v_sub_f32 v47, v47, v249
s_lshl_b64 exec, 0xffff, 16
v_mul_f32 v236, v236, v250
s_mov_b64 exec, -1
AR v254,a8
v_mul_f32 v254, v254, v250
AW a8,v254
AR v251,a9
v_mul_f32 v251, v251, v250
AW a9,v251
AR v254,a10
v_mul_f32 v254, v254, v250
AW a10,v254
AR v251,a11
v_mul_f32 v251, v251, v250
AW a11,v251
AR v254,a12
v_mul_f32 v254, v254, v250
AW a12,v254
AR v251,a13
v_mul_f32 v251, v251, v250
AW a13,v251
AR v254,a14
v_mul_f32 v254, v254, v250
AW a14,v254
AR v251,a15
v_mul_f32 v251, v251, v250
AW a15,v251
AR v254,a40
v_mul_f32 v254, v254, v250
AW a40,v254
AR v251,a41
v_mul_f32 v251, v251, v250
AW a41,v251
AR v254,a42
v_mul_f32 v254, v254, v250
AW a42,v254
AR v251,a43
v_mul_f32 v251, v251, v250
AW a43,v251
AR v254,a44
v_mul_f32 v254, v254, v250
AW a44,v254
AR v251,a45
v_mul_f32 v251, v251, v250
AW a45,v251
AR v254,a46
v_mul_f32 v254, v254, v250
AW a46,v254
AR v251,a47
v_mul_f32 v251, v251, v250
AW a47,v251
AR v254,a72
v_mul_f32 v254, v254, v250
AW a72,v254
AR v251,a73
v_mul_f32 v251, v251, v250
AW a73,v251
AR v254,a74
v_mul_f32 v254, v254, v250
AW a74,v254
AR v251,a75
v_mul_f32 v251, v251, v250
AW a75,v251
AR v254,a76
v_mul_f32 v254, v254, v250
AW a76,v254
AR v251,a77
v_mul_f32 v251, v251, v250
AW a77,v251
AR v254,a78
v_mul_f32 v254, v254, v250
AW a78,v254
AR v251,a79
v_mul_f32 v251, v251, v250
AW a79,v251
AR v254,a104
v_mul_f32 v254, v254, v250
AW a104,v254
AR v251,a105
v_mul_f32 v251, v251, v250
AW a105,v251
AR v254,a106
v_mul_f32 v254, v254, v250
AW a106,v254
AR v251,a107
v_mul_f32 v251, v251, v250
AW a107,v251
AR v254,a108
v_mul_f32 v254, v254, v250
AW a108,v254
AR v251,a109
v_mul_f32 v251, v251, v250
AW a109,v251
AR v254,a110
v_mul_f32 v254, v254, v250
AW a110,v254
AR v251,a111
v_mul_f32 v251, v251, v250
AW a111,v251
EX v0,v0
EX v1,v1
EX v2,v2
EX v3,v3
EX v4,v4
EX v5,v5
EX v6,v6
EX v7,v7
EX v8,v8
EX v9,v9
EX v10,v10
EX v11,v11
EX v12,v12
EX v13,v13
EX v14,v14
EX v15,v15
EX v32,v32
EX v33,v33
EX v34,v34
EX v35,v35
EX v36,v36
EX v37,v37
EX v38,v38
EX v39,v39
EX v40,v40
EX v41,v41
EX v42,v42
EX v43,v43
EX v44,v44
EX v45,v45
EX v46,v46
EX v47,v47
CV v0,v0,v1
CV v1,v2,v3
CV v2,v4,v5
CV v3,v6,v7
CV v8,v8,v9
CV v9,v10,v11
CV v10,v12,v13
CV v11,v14,v15
CV v4,v32,v33
CV v5,v34,v35
CV v6,v36,v37
CV v7,v38,v39
CV v12,v40,v41
CV v13,v42,v43
CV v14,v44,v45
CV v15,v46,v47
v_smfmac_f32_16x16x64_bf16 v[236:239], v[144:147], v[0:7], v234
s_nop 0
v_smfmac_f32_16x16x64_bf16 v[236:239], v[148:151], v[8:15], v234
s_nop 9
v_add_f32 v246, v236, v237
s_cmp_eq_u32 s73, 0
s_cbranch_scc1 LBACK0_%=
s_cmp_eq_u32 s73, 2
s_cbranch_scc1 LBACK2_%=
s_endpgm
LSITE1_%=:
s_mov_b32 s73, 1
s_mov_b32 s74, 16384
s_mov_b32 s75, 32768
s_branch LRARE102_%=
LRARE102_%=:
s_nop 15
s_waitcnt vmcnt(0) lgkmcnt(0)
v_add_u32 v249, s74, v224
RD a[192:195],v249 offset:0
RD a[208:211],v249 offset:4096
RD a[224:227],v249 offset:8192
RD a[240:243],v249 offset:12288
v_add_u32 v250, s74, v225
RD a[196:199],v250 offset:0
RD a[212:215],v250 offset:4096
RD a[228:231],v250 offset:8192
RD a[244:247],v250 offset:12288
v_add_u32 v251, s74, v226
RD a[200:203],v251 offset:0
RD a[216:219],v251 offset:4096
RD a[232:235],v251 offset:8192
RD a[248:251],v251 offset:12288
v_add_u32 v254, s74, v227
RD a[204:207],v254 offset:0
RD a[220:223],v254 offset:4096
RD a[236:239],v254 offset:8192
RD a[252:255],v254 offset:12288
s_waitcnt lgkmcnt(0)
MM v[64:67],a[192:195],v[160:163],v[128:131]
MM v[64:67],a[196:199],v[176:179],v[64:67]
MM v[64:67],a[200:203],v[192:195],v[64:67]
MM v[64:67],a[204:207],v[208:211],v[64:67]
MM v[68:71],a[220:223],v[208:211],v[128:131]
MM v[68:71],a[216:219],v[192:195],v[68:71]
MM v[68:71],a[212:215],v[176:179],v[68:71]
MM v[68:71],a[208:211],v[160:163],v[68:71]
MM v[76:79],a[208:211],v[164:167],v[132:135]
MM v[76:79],a[212:215],v[180:183],v[76:79]
MM v[76:79],a[216:219],v[196:199],v[76:79]
MM v[76:79],a[220:223],v[212:215],v[76:79]
MM v[72:75],a[204:207],v[212:215],v[132:135]
MM v[72:75],a[200:203],v[196:199],v[72:75]
MM v[72:75],a[196:199],v[180:183],v[72:75]
MM v[72:75],a[192:195],v[164:167],v[72:75]
MM v[96:99],a[224:227],v[160:163],v[128:131]
MM v[96:99],a[228:231],v[176:179],v[96:99]
MM v[96:99],a[232:235],v[192:195],v[96:99]
MM v[96:99],a[236:239],v[208:211],v[96:99]
MM v[100:103],a[252:255],v[208:211],v[128:131]
MM v[100:103],a[248:251],v[192:195],v[100:103]
MM v[100:103],a[244:247],v[176:179],v[100:103]
MM v[100:103],a[240:243],v[160:163],v[100:103]
)ASM"
R"ASM(
MM v[108:111],a[240:243],v[164:167],v[132:135]
MM v[108:111],a[244:247],v[180:183],v[108:111]
MM v[108:111],a[248:251],v[196:199],v[108:111]
MM v[108:111],a[252:255],v[212:215],v[108:111]
MM v[104:107],a[236:239],v[212:215],v[132:135]
MM v[104:107],a[232:235],v[196:199],v[104:107]
MM v[104:107],a[228:231],v[180:183],v[104:107]
MM v[104:107],a[224:227],v[164:167],v[104:107]
s_nop 15
v_add_u32 v249, s75, v224
RD a[192:195],v249 offset:0
RD a[208:211],v249 offset:4096
RD a[224:227],v249 offset:8192
RD a[240:243],v249 offset:12288
v_add_u32 v250, s75, v225
RD a[196:199],v250 offset:0
RD a[212:215],v250 offset:4096
RD a[228:231],v250 offset:8192
RD a[244:247],v250 offset:12288
v_add_u32 v251, s75, v226
RD a[200:203],v251 offset:0
RD a[216:219],v251 offset:4096
RD a[232:235],v251 offset:8192
RD a[248:251],v251 offset:12288
v_add_u32 v254, s75, v227
RD a[204:207],v254 offset:0
RD a[220:223],v254 offset:4096
RD a[236:239],v254 offset:8192
RD a[252:255],v254 offset:12288
s_waitcnt lgkmcnt(0)
v_mov_b32 v236, v246
v_mov_b32 v237, 0
v_max3_f32 v249, v64, v65, v66
v_max3_f32 v251, v96, v97, v98
v_max3_f32 v249, v249, v67, v68
v_max3_f32 v251, v251, v99, v100
v_max3_f32 v249, v249, v69, v70
v_max3_f32 v251, v251, v101, v102
v_max3_f32 v249, v249, v71, v103
v_max_f32 v249, v249, v251
v_log_f32 v250, v246
s_lshl_b64 exec, 0xffff, 0
v_max_f32 v249, v249, v250
s_mov_b64 exec, -1
v_mov_b32 v251, v249
s_nop 1
v_permlane16_swap_b32 v249, v251
v_max_f32 v249, v249, v251
v_mov_b32 v251, v249
s_nop 1
v_permlane32_swap_b32 v249, v251
v_max_f32 v249, v249, v251
v_max_f32 v249, 0, v249
v_sub_f32 v250, 0, v249
EX v250,v250
v_sub_f32 v128, v128, v249
v_sub_f32 v129, v129, v249
v_sub_f32 v130, v130, v249
v_sub_f32 v131, v131, v249
v_sub_f32 v0, v0, v249
v_sub_f32 v64, v64, v249
v_sub_f32 v1, v1, v249
v_sub_f32 v65, v65, v249
v_sub_f32 v2, v2, v249
v_sub_f32 v66, v66, v249
v_sub_f32 v3, v3, v249
v_sub_f32 v67, v67, v249
v_sub_f32 v4, v4, v249
v_sub_f32 v68, v68, v249
v_sub_f32 v5, v5, v249
v_sub_f32 v69, v69, v249
v_sub_f32 v6, v6, v249
v_sub_f32 v70, v70, v249
v_sub_f32 v7, v7, v249
v_sub_f32 v71, v71, v249
v_sub_f32 v32, v32, v249
v_sub_f32 v96, v96, v249
v_sub_f32 v33, v33, v249
v_sub_f32 v97, v97, v249
v_sub_f32 v34, v34, v249
v_sub_f32 v98, v98, v249
v_sub_f32 v35, v35, v249
v_sub_f32 v99, v99, v249
v_sub_f32 v36, v36, v249
v_sub_f32 v100, v100, v249
v_sub_f32 v37, v37, v249
v_sub_f32 v101, v101, v249
v_sub_f32 v38, v38, v249
v_sub_f32 v102, v102, v249
v_sub_f32 v39, v39, v249
v_sub_f32 v103, v103, v249
s_lshl_b64 exec, 0xffff, 0
v_mul_f32 v236, v236, v250
s_mov_b64 exec, -1
AR v254,a0
v_mul_f32 v254, v254, v250
AW a0,v254
AR v251,a1
v_mul_f32 v251, v251, v250
AW a1,v251
AR v254,a2
v_mul_f32 v254, v254, v250
AW a2,v254
AR v251,a3
v_mul_f32 v251, v251, v250
AW a3,v251
AR v254,a4
v_mul_f32 v254, v254, v250
AW a4,v254
AR v251,a5
v_mul_f32 v251, v251, v250
AW a5,v251
AR v254,a6
v_mul_f32 v254, v254, v250
AW a6,v254
AR v251,a7
v_mul_f32 v251, v251, v250
AW a7,v251
AR v254,a32
v_mul_f32 v254, v254, v250
AW a32,v254
AR v251,a33
v_mul_f32 v251, v251, v250
AW a33,v251
AR v254,a34
v_mul_f32 v254, v254, v250
AW a34,v254
AR v251,a35
v_mul_f32 v251, v251, v250
AW a35,v251
AR v254,a36
v_mul_f32 v254, v254, v250
AW a36,v254
AR v251,a37
v_mul_f32 v251, v251, v250
AW a37,v251
AR v254,a38
v_mul_f32 v254, v254, v250
AW a38,v254
AR v251,a39
v_mul_f32 v251, v251, v250
AW a39,v251
AR v254,a64
v_mul_f32 v254, v254, v250
AW a64,v254
AR v251,a65
v_mul_f32 v251, v251, v250
AW a65,v251
AR v254,a66
v_mul_f32 v254, v254, v250
AW a66,v254
AR v251,a67
v_mul_f32 v251, v251, v250
AW a67,v251
AR v254,a68
v_mul_f32 v254, v254, v250
AW a68,v254
AR v251,a69
v_mul_f32 v251, v251, v250
AW a69,v251
AR v254,a70
v_mul_f32 v254, v254, v250
AW a70,v254
AR v251,a71
v_mul_f32 v251, v251, v250
AW a71,v251
AR v254,a96
v_mul_f32 v254, v254, v250
AW a96,v254
AR v251,a97
v_mul_f32 v251, v251, v250
AW a97,v251
AR v254,a98
v_mul_f32 v254, v254, v250
AW a98,v254
AR v251,a99
v_mul_f32 v251, v251, v250
AW a99,v251
AR v254,a100
v_mul_f32 v254, v254, v250
AW a100,v254
AR v251,a101
v_mul_f32 v251, v251, v250
AW a101,v251
AR v254,a102
v_mul_f32 v254, v254, v250
AW a102,v254
AR v251,a103
v_mul_f32 v251, v251, v250
AW a103,v251
v_max3_f32 v249, v72, v73, v74
v_max3_f32 v251, v104, v105, v106
v_max3_f32 v249, v249, v75, v76
v_max3_f32 v251, v251, v107, v108
v_max3_f32 v249, v249, v77, v78
v_max3_f32 v251, v251, v109, v110
v_max3_f32 v249, v249, v79, v111
v_max_f32 v249, v249, v251
v_log_f32 v250, v246
s_lshl_b64 exec, 0xffff, 16
v_max_f32 v249, v249, v250
s_mov_b64 exec, -1
v_mov_b32 v251, v249
s_nop 1
v_permlane16_swap_b32 v249, v251
v_max_f32 v249, v249, v251
v_mov_b32 v251, v249
s_nop 1
v_permlane32_swap_b32 v249, v251
v_max_f32 v249, v249, v251
v_max_f32 v249, 0, v249
v_sub_f32 v250, 0, v249
EX v250,v250
v_sub_f32 v132, v132, v249
v_sub_f32 v133, v133, v249
v_sub_f32 v134, v134, v249
v_sub_f32 v135, v135, v249
v_sub_f32 v8, v8, v249
v_sub_f32 v72, v72, v249
v_sub_f32 v9, v9, v249
v_sub_f32 v73, v73, v249
v_sub_f32 v10, v10, v249
v_sub_f32 v74, v74, v249
v_sub_f32 v11, v11, v249
v_sub_f32 v75, v75, v249
v_sub_f32 v12, v12, v249
v_sub_f32 v76, v76, v249
v_sub_f32 v13, v13, v249
v_sub_f32 v77, v77, v249
v_sub_f32 v14, v14, v249
v_sub_f32 v78, v78, v249
v_sub_f32 v15, v15, v249
v_sub_f32 v79, v79, v249
v_sub_f32 v40, v40, v249
v_sub_f32 v104, v104, v249
v_sub_f32 v41, v41, v249
v_sub_f32 v105, v105, v249
v_sub_f32 v42, v42, v249
v_sub_f32 v106, v106, v249
v_sub_f32 v43, v43, v249
v_sub_f32 v107, v107, v249
v_sub_f32 v44, v44, v249
v_sub_f32 v108, v108, v249
v_sub_f32 v45, v45, v249
v_sub_f32 v109, v109, v249
v_sub_f32 v46, v46, v249
v_sub_f32 v110, v110, v249
v_sub_f32 v47, v47, v249
v_sub_f32 v111, v111, v249
s_lshl_b64 exec, 0xffff, 16
v_mul_f32 v236, v236, v250
s_mov_b64 exec, -1
AR v254,a8
v_mul_f32 v254, v254, v250
AW a8,v254
AR v251,a9
v_mul_f32 v251, v251, v250
AW a9,v251
AR v254,a10
v_mul_f32 v254, v254, v250
AW a10,v254
AR v251,a11
v_mul_f32 v251, v251, v250
AW a11,v251
AR v254,a12
v_mul_f32 v254, v254, v250
AW a12,v254
AR v251,a13
v_mul_f32 v251, v251, v250
AW a13,v251
AR v254,a14
v_mul_f32 v254, v254, v250
AW a14,v254
AR v251,a15
v_mul_f32 v251, v251, v250
AW a15,v251
AR v254,a40
v_mul_f32 v254, v254, v250
AW a40,v254
AR v251,a41
v_mul_f32 v251, v251, v250
AW a41,v251
AR v254,a42
v_mul_f32 v254, v254, v250
AW a42,v254
AR v251,a43
v_mul_f32 v251, v251, v250
AW a43,v251
AR v254,a44
v_mul_f32 v254, v254, v250
AW a44,v254
AR v251,a45
v_mul_f32 v251, v251, v250
AW a45,v251
AR v254,a46
v_mul_f32 v254, v254, v250
AW a46,v254
AR v251,a47
v_mul_f32 v251, v251, v250
AW a47,v251
AR v254,a72
v_mul_f32 v254, v254, v250
AW a72,v254
AR v251,a73
v_mul_f32 v251, v251, v250
AW a73,v251
AR v254,a74
v_mul_f32 v254, v254, v250
AW a74,v254
AR v251,a75
v_mul_f32 v251, v251, v250
AW a75,v251
AR v254,a76
v_mul_f32 v254, v254, v250
AW a76,v254
AR v251,a77
v_mul_f32 v251, v251, v250
AW a77,v251
AR v254,a78
v_mul_f32 v254, v254, v250
AW a78,v254
AR v251,a79
v_mul_f32 v251, v251, v250
AW a79,v251
AR v254,a104
v_mul_f32 v254, v254, v250
AW a104,v254
AR v251,a105
v_mul_f32 v251, v251, v250
AW a105,v251
AR v254,a106
v_mul_f32 v254, v254, v250
AW a106,v254
AR v251,a107
v_mul_f32 v251, v251, v250
AW a107,v251
AR v254,a108
v_mul_f32 v254, v254, v250
AW a108,v254
AR v251,a109
v_mul_f32 v251, v251, v250
AW a109,v251
AR v254,a110
v_mul_f32 v254, v254, v250
AW a110,v254
AR v251,a111
v_mul_f32 v251, v251, v250
AW a111,v251
EX v64,v64
EX v65,v65
EX v66,v66
EX v67,v67
EX v68,v68
EX v69,v69
EX v70,v70
EX v71,v71
EX v72,v72
EX v73,v73
EX v74,v74
EX v75,v75
EX v76,v76
EX v77,v77
EX v78,v78
EX v79,v79
EX v96,v96
EX v97,v97
EX v98,v98
EX v99,v99
EX v100,v100
EX v101,v101
EX v102,v102
EX v103,v103
EX v104,v104
EX v105,v105
EX v106,v106
EX v107,v107
EX v108,v108
EX v109,v109
EX v110,v110
EX v111,v111
CV v64,v64,v65
CV v65,v66,v67
CV v66,v68,v69
CV v67,v70,v71
CV v72,v72,v73
CV v73,v74,v75
CV v74,v76,v77
CV v75,v78,v79
CV v68,v96,v97
CV v69,v98,v99
CV v70,v100,v101
CV v71,v102,v103
CV v76,v104,v105
CV v77,v106,v107
CV v78,v108,v109
CV v79,v110,v111
v_smfmac_f32_16x16x64_bf16 v[236:239], v[144:147], v[64:71], v234
s_nop 0
v_smfmac_f32_16x16x64_bf16 v[236:239], v[148:151], v[72:79], v234
s_nop 9
v_add_f32 v247, v236, v237
s_cmp_eq_u32 s73, 1
s_cbranch_scc1 LBACK1_%=
s_endpgm
LSITE3_%=:
s_mov_b32 s73, 3
s_mov_b32 s74, 49152
s_mov_b32 s75, 0
s_branch LRARE104_%=
LSITE5_%=:
s_mov_b32 s73, 5
s_mov_b32 s74, 16384
s_mov_b32 s75, 32768
s_branch LRARE104_%=
LSITE7_%=:
s_mov_b32 s73, 7
s_mov_b32 s74, 49152
s_mov_b32 s75, 0
s_branch LRARE104_%=
LSITE9_%=:
s_mov_b32 s73, 9
s_mov_b32 s74, 16384
s_mov_b32 s75, 32768
s_branch LRARE104_%=
LSITE11_%=:
s_mov_b32 s73, 11
s_mov_b32 s74, 49152
s_mov_b32 s75, 0
s_branch LRARE104_%=
LSITE13_%=:
s_mov_b32 s73, 13
s_mov_b32 s74, 16384
s_mov_b32 s75, 32768
s_branch LRARE104_%=
LSITE15_%=:
s_mov_b32 s73, 15
s_mov_b32 s74, 49152
s_mov_b32 s75, 0
s_branch LRARE104_%=
LSITE17_%=:
s_mov_b32 s73, 17
s_mov_b32 s74, 16384
s_mov_b32 s75, 32768
s_branch LRARE104_%=
LSITE19_%=:
s_mov_b32 s73, 19
s_mov_b32 s74, 49152
s_mov_b32 s75, 0
s_branch LRARE104_%=
LSITE21_%=:
s_mov_b32 s73, 21
s_mov_b32 s74, 16384
s_mov_b32 s75, 32768
s_branch LRARE104_%=
LSITE23_%=:
s_mov_b32 s73, 23
s_mov_b32 s74, 49152
s_mov_b32 s75, 0
s_branch LRARE104_%=
LSITE25_%=:
s_mov_b32 s73, 25
s_mov_b32 s74, 16384
s_mov_b32 s75, 32768
s_branch LRARE104_%=
LSITE27_%=:
s_mov_b32 s73, 27
s_mov_b32 s74, 49152
s_mov_b32 s75, 0
s_branch LRARE104_%=
LSITE29_%=:
s_mov_b32 s73, 29
s_mov_b32 s74, 16384
s_mov_b32 s75, 32768
s_branch LRARE104_%=
LSITE31_%=:
s_mov_b32 s73, 31
s_mov_b32 s74, 49152
s_mov_b32 s75, 0
s_branch LRARE104_%=
LRARE104_%=:
s_nop 15
s_waitcnt vmcnt(0) lgkmcnt(0)
v_add_u32 v249, s74, v224
RD a[192:195],v249 offset:0
RD a[208:211],v249 offset:4096
RD a[224:227],v249 offset:8192
RD a[240:243],v249 offset:12288
v_add_u32 v250, s74, v225
RD a[196:199],v250 offset:0
RD a[212:215],v250 offset:4096
RD a[228:231],v250 offset:8192
RD a[244:247],v250 offset:12288
v_add_u32 v251, s74, v226
RD a[200:203],v251 offset:0
RD a[216:219],v251 offset:4096
RD a[232:235],v251 offset:8192
RD a[248:251],v251 offset:12288
v_add_u32 v254, s74, v227
RD a[204:207],v254 offset:0
RD a[220:223],v254 offset:4096
RD a[236:239],v254 offset:8192
RD a[252:255],v254 offset:12288
s_waitcnt lgkmcnt(0)
MM v[64:67],a[192:195],v[160:163],v[128:131]
MM v[64:67],a[196:199],v[176:179],v[64:67]
MM v[64:67],a[200:203],v[192:195],v[64:67]
MM v[64:67],a[204:207],v[208:211],v[64:67]
MM v[68:71],a[220:223],v[208:211],v[128:131]
MM v[68:71],a[216:219],v[192:195],v[68:71]
MM v[68:71],a[212:215],v[176:179],v[68:71]
MM v[68:71],a[208:211],v[160:163],v[68:71]
MM v[76:79],a[208:211],v[164:167],v[132:135]
MM v[76:79],a[212:215],v[180:183],v[76:79]
MM v[76:79],a[216:219],v[196:199],v[76:79]
MM v[76:79],a[220:223],v[212:215],v[76:79]
MM v[72:75],a[204:207],v[212:215],v[132:135]
MM v[72:75],a[200:203],v[196:199],v[72:75]
MM v[72:75],a[196:199],v[180:183],v[72:75]
MM v[72:75],a[192:195],v[164:167],v[72:75]
MM v[80:83],a[192:195],v[168:171],v[136:139]
MM v[80:83],a[196:199],v[184:187],v[80:83]
MM v[80:83],a[200:203],v[200:203],v[80:83]
MM v[80:83],a[204:207],v[216:219],v[80:83]
MM v[84:87],a[220:223],v[216:219],v[136:139]
MM v[84:87],a[216:219],v[200:203],v[84:87]
MM v[84:87],a[212:215],v[184:187],v[84:87]
MM v[84:87],a[208:211],v[168:171],v[84:87]
MM v[92:95],a[208:211],v[172:175],v[140:143]
MM v[92:95],a[212:215],v[188:191],v[92:95]
MM v[92:95],a[216:219],v[204:207],v[92:95]
MM v[92:95],a[220:223],v[220:223],v[92:95]
MM v[88:91],a[204:207],v[220:223],v[140:143]
MM v[88:91],a[200:203],v[204:207],v[88:91]
MM v[88:91],a[196:199],v[188:191],v[88:91]
MM v[88:91],a[192:195],v[172:175],v[88:91]
MM v[96:99],a[224:227],v[160:163],v[128:131]
MM v[96:99],a[228:231],v[176:179],v[96:99]
MM v[96:99],a[232:235],v[192:195],v[96:99]
MM v[96:99],a[236:239],v[208:211],v[96:99]
MM v[100:103],a[252:255],v[208:211],v[128:131]
MM v[100:103],a[248:251],v[192:195],v[100:103]
MM v[100:103],a[244:247],v[176:179],v[100:103]
)ASM"
R"ASM(
MM v[100:103],a[240:243],v[160:163],v[100:103]
MM v[108:111],a[240:243],v[164:167],v[132:135]
MM v[108:111],a[244:247],v[180:183],v[108:111]
MM v[108:111],a[248:251],v[196:199],v[108:111]
MM v[108:111],a[252:255],v[212:215],v[108:111]
MM v[104:107],a[236:239],v[212:215],v[132:135]
MM v[104:107],a[232:235],v[196:199],v[104:107]
MM v[104:107],a[228:231],v[180:183],v[104:107]
MM v[104:107],a[224:227],v[164:167],v[104:107]
MM v[112:115],a[224:227],v[168:171],v[136:139]
MM v[112:115],a[228:231],v[184:187],v[112:115]
MM v[112:115],a[232:235],v[200:203],v[112:115]
MM v[112:115],a[236:239],v[216:219],v[112:115]
MM v[116:119],a[252:255],v[216:219],v[136:139]
MM v[116:119],a[248:251],v[200:203],v[116:119]
MM v[116:119],a[244:247],v[184:187],v[116:119]
MM v[116:119],a[240:243],v[168:171],v[116:119]
MM v[124:127],a[240:243],v[172:175],v[140:143]
MM v[124:127],a[244:247],v[188:191],v[124:127]
MM v[124:127],a[248:251],v[204:207],v[124:127]
MM v[124:127],a[252:255],v[220:223],v[124:127]
MM v[120:123],a[236:239],v[220:223],v[140:143]
MM v[120:123],a[232:235],v[204:207],v[120:123]
MM v[120:123],a[228:231],v[188:191],v[120:123]
MM v[120:123],a[224:227],v[172:175],v[120:123]
s_nop 15
v_add_u32 v249, s75, v224
RD a[192:195],v249 offset:0
RD a[208:211],v249 offset:4096
RD a[224:227],v249 offset:8192
RD a[240:243],v249 offset:12288
v_add_u32 v250, s75, v225
RD a[196:199],v250 offset:0
RD a[212:215],v250 offset:4096
RD a[228:231],v250 offset:8192
RD a[244:247],v250 offset:12288
v_add_u32 v251, s75, v226
RD a[200:203],v251 offset:0
RD a[216:219],v251 offset:4096
RD a[232:235],v251 offset:8192
RD a[248:251],v251 offset:12288
v_add_u32 v254, s75, v227
RD a[204:207],v254 offset:0
RD a[220:223],v254 offset:4096
RD a[236:239],v254 offset:8192
RD a[252:255],v254 offset:12288
s_waitcnt lgkmcnt(0)
v_mov_b32 v236, v246
v_mov_b32 v237, 0
v_max3_f32 v249, v64, v65, v66
v_max3_f32 v251, v96, v97, v98
v_max3_f32 v249, v249, v67, v68
v_max3_f32 v251, v251, v99, v100
v_max3_f32 v249, v249, v69, v70
v_max3_f32 v251, v251, v101, v102
v_max3_f32 v249, v249, v71, v103
v_max_f32 v249, v249, v251
v_log_f32 v250, v246
s_lshl_b64 exec, 0xffff, 0
v_max_f32 v249, v249, v250
s_mov_b64 exec, -1
v_mov_b32 v251, v249
s_nop 1
v_permlane16_swap_b32 v249, v251
v_max_f32 v249, v249, v251
v_mov_b32 v251, v249
s_nop 1
v_permlane32_swap_b32 v249, v251
v_max_f32 v249, v249, v251
v_max_f32 v249, 0, v249
v_sub_f32 v250, 0, v249
EX v250,v250
v_sub_f32 v128, v128, v249
v_sub_f32 v129, v129, v249
v_sub_f32 v130, v130, v249
v_sub_f32 v131, v131, v249
v_sub_f32 v0, v0, v249
v_sub_f32 v64, v64, v249
v_sub_f32 v1, v1, v249
v_sub_f32 v65, v65, v249
v_sub_f32 v2, v2, v249
v_sub_f32 v66, v66, v249
v_sub_f32 v3, v3, v249
v_sub_f32 v67, v67, v249
v_sub_f32 v4, v4, v249
v_sub_f32 v68, v68, v249
v_sub_f32 v5, v5, v249
v_sub_f32 v69, v69, v249
v_sub_f32 v6, v6, v249
v_sub_f32 v70, v70, v249
v_sub_f32 v7, v7, v249
v_sub_f32 v71, v71, v249
v_sub_f32 v32, v32, v249
v_sub_f32 v96, v96, v249
v_sub_f32 v33, v33, v249
v_sub_f32 v97, v97, v249
v_sub_f32 v34, v34, v249
v_sub_f32 v98, v98, v249
v_sub_f32 v35, v35, v249
v_sub_f32 v99, v99, v249
v_sub_f32 v36, v36, v249
v_sub_f32 v100, v100, v249
v_sub_f32 v37, v37, v249
v_sub_f32 v101, v101, v249
v_sub_f32 v38, v38, v249
v_sub_f32 v102, v102, v249
v_sub_f32 v39, v39, v249
v_sub_f32 v103, v103, v249
s_lshl_b64 exec, 0xffff, 0
v_mul_f32 v236, v236, v250
s_mov_b64 exec, -1
AR v254,a0
v_mul_f32 v254, v254, v250
AW a0,v254
AR v251,a1
v_mul_f32 v251, v251, v250
AW a1,v251
AR v254,a2
v_mul_f32 v254, v254, v250
AW a2,v254
AR v251,a3
v_mul_f32 v251, v251, v250
AW a3,v251
AR v254,a4
v_mul_f32 v254, v254, v250
AW a4,v254
AR v251,a5
v_mul_f32 v251, v251, v250
AW a5,v251
AR v254,a6
v_mul_f32 v254, v254, v250
AW a6,v254
AR v251,a7
v_mul_f32 v251, v251, v250
AW a7,v251
AR v254,a32
v_mul_f32 v254, v254, v250
AW a32,v254
AR v251,a33
v_mul_f32 v251, v251, v250
AW a33,v251
AR v254,a34
v_mul_f32 v254, v254, v250
AW a34,v254
AR v251,a35
v_mul_f32 v251, v251, v250
AW a35,v251
AR v254,a36
v_mul_f32 v254, v254, v250
AW a36,v254
AR v251,a37
v_mul_f32 v251, v251, v250
AW a37,v251
AR v254,a38
v_mul_f32 v254, v254, v250
AW a38,v254
AR v251,a39
v_mul_f32 v251, v251, v250
AW a39,v251
AR v254,a64
v_mul_f32 v254, v254, v250
AW a64,v254
AR v251,a65
v_mul_f32 v251, v251, v250
AW a65,v251
AR v254,a66
v_mul_f32 v254, v254, v250
AW a66,v254
AR v251,a67
v_mul_f32 v251, v251, v250
AW a67,v251
AR v254,a68
v_mul_f32 v254, v254, v250
AW a68,v254
AR v251,a69
v_mul_f32 v251, v251, v250
AW a69,v251
AR v254,a70
v_mul_f32 v254, v254, v250
AW a70,v254
AR v251,a71
v_mul_f32 v251, v251, v250
AW a71,v251
AR v254,a96
v_mul_f32 v254, v254, v250
AW a96,v254
AR v251,a97
v_mul_f32 v251, v251, v250
AW a97,v251
AR v254,a98
v_mul_f32 v254, v254, v250
AW a98,v254
AR v251,a99
v_mul_f32 v251, v251, v250
AW a99,v251
AR v254,a100
v_mul_f32 v254, v254, v250
AW a100,v254
AR v251,a101
v_mul_f32 v251, v251, v250
AW a101,v251
AR v254,a102
v_mul_f32 v254, v254, v250
AW a102,v254
AR v251,a103
v_mul_f32 v251, v251, v250
AW a103,v251
v_max3_f32 v249, v72, v73, v74
v_max3_f32 v251, v104, v105, v106
v_max3_f32 v249, v249, v75, v76
v_max3_f32 v251, v251, v107, v108
v_max3_f32 v249, v249, v77, v78
v_max3_f32 v251, v251, v109, v110
v_max3_f32 v249, v249, v79, v111
v_max_f32 v249, v249, v251
v_log_f32 v250, v246
s_lshl_b64 exec, 0xffff, 16
v_max_f32 v249, v249, v250
s_mov_b64 exec, -1
v_mov_b32 v251, v249
s_nop 1
v_permlane16_swap_b32 v249, v251
v_max_f32 v249, v249, v251
v_mov_b32 v251, v249
s_nop 1
v_permlane32_swap_b32 v249, v251
v_max_f32 v249, v249, v251
v_max_f32 v249, 0, v249
v_sub_f32 v250, 0, v249
EX v250,v250
v_sub_f32 v132, v132, v249
v_sub_f32 v133, v133, v249
v_sub_f32 v134, v134, v249
v_sub_f32 v135, v135, v249
v_sub_f32 v8, v8, v249
v_sub_f32 v72, v72, v249
v_sub_f32 v9, v9, v249
v_sub_f32 v73, v73, v249
v_sub_f32 v10, v10, v249
v_sub_f32 v74, v74, v249
v_sub_f32 v11, v11, v249
v_sub_f32 v75, v75, v249
v_sub_f32 v12, v12, v249
v_sub_f32 v76, v76, v249
v_sub_f32 v13, v13, v249
v_sub_f32 v77, v77, v249
v_sub_f32 v14, v14, v249
v_sub_f32 v78, v78, v249
v_sub_f32 v15, v15, v249
v_sub_f32 v79, v79, v249
v_sub_f32 v40, v40, v249
v_sub_f32 v104, v104, v249
v_sub_f32 v41, v41, v249
v_sub_f32 v105, v105, v249
v_sub_f32 v42, v42, v249
v_sub_f32 v106, v106, v249
v_sub_f32 v43, v43, v249
v_sub_f32 v107, v107, v249
v_sub_f32 v44, v44, v249
v_sub_f32 v108, v108, v249
v_sub_f32 v45, v45, v249
v_sub_f32 v109, v109, v249
v_sub_f32 v46, v46, v249
v_sub_f32 v110, v110, v249
v_sub_f32 v47, v47, v249
v_sub_f32 v111, v111, v249
s_lshl_b64 exec, 0xffff, 16
v_mul_f32 v236, v236, v250
s_mov_b64 exec, -1
AR v254,a8
v_mul_f32 v254, v254, v250
AW a8,v254
AR v251,a9
v_mul_f32 v251, v251, v250
AW a9,v251
AR v254,a10
v_mul_f32 v254, v254, v250
AW a10,v254
AR v251,a11
v_mul_f32 v251, v251, v250
AW a11,v251
AR v254,a12
v_mul_f32 v254, v254, v250
AW a12,v254
AR v251,a13
v_mul_f32 v251, v251, v250
AW a13,v251
AR v254,a14
v_mul_f32 v254, v254, v250
AW a14,v254
AR v251,a15
v_mul_f32 v251, v251, v250
AW a15,v251
AR v254,a40
v_mul_f32 v254, v254, v250
AW a40,v254
AR v251,a41
v_mul_f32 v251, v251, v250
AW a41,v251
AR v254,a42
v_mul_f32 v254, v254, v250
AW a42,v254
AR v251,a43
v_mul_f32 v251, v251, v250
AW a43,v251
AR v254,a44
v_mul_f32 v254, v254, v250
AW a44,v254
AR v251,a45
v_mul_f32 v251, v251, v250
AW a45,v251
AR v254,a46
v_mul_f32 v254, v254, v250
AW a46,v254
AR v251,a47
v_mul_f32 v251, v251, v250
AW a47,v251
AR v254,a72
v_mul_f32 v254, v254, v250
AW a72,v254
AR v251,a73
v_mul_f32 v251, v251, v250
AW a73,v251
AR v254,a74
v_mul_f32 v254, v254, v250
AW a74,v254
AR v251,a75
v_mul_f32 v251, v251, v250
AW a75,v251
AR v254,a76
v_mul_f32 v254, v254, v250
AW a76,v254
AR v251,a77
v_mul_f32 v251, v251, v250
AW a77,v251
AR v254,a78
v_mul_f32 v254, v254, v250
AW a78,v254
AR v251,a79
v_mul_f32 v251, v251, v250
AW a79,v251
AR v254,a104
v_mul_f32 v254, v254, v250
AW a104,v254
AR v251,a105
v_mul_f32 v251, v251, v250
AW a105,v251
AR v254,a106
v_mul_f32 v254, v254, v250
AW a106,v254
AR v251,a107
v_mul_f32 v251, v251, v250
AW a107,v251
AR v254,a108
v_mul_f32 v254, v254, v250
AW a108,v254
AR v251,a109
v_mul_f32 v251, v251, v250
AW a109,v251
AR v254,a110
v_mul_f32 v254, v254, v250
AW a110,v254
AR v251,a111
v_mul_f32 v251, v251, v250
AW a111,v251
v_max3_f32 v249, v80, v81, v82
v_max3_f32 v251, v112, v113, v114
v_max3_f32 v249, v249, v83, v84
v_max3_f32 v251, v251, v115, v116
v_max3_f32 v249, v249, v85, v86
v_max3_f32 v251, v251, v117, v118
v_max3_f32 v249, v249, v87, v119
v_max_f32 v249, v249, v251
v_log_f32 v250, v246
s_lshl_b64 exec, 0xffff, 32
v_max_f32 v249, v249, v250
s_mov_b64 exec, -1
v_mov_b32 v251, v249
s_nop 1
v_permlane16_swap_b32 v249, v251
v_max_f32 v249, v249, v251
v_mov_b32 v251, v249
s_nop 1
v_permlane32_swap_b32 v249, v251
v_max_f32 v249, v249, v251
v_max_f32 v249, 0, v249
v_sub_f32 v250, 0, v249
EX v250,v250
v_sub_f32 v136, v136, v249
v_sub_f32 v137, v137, v249
v_sub_f32 v138, v138, v249
v_sub_f32 v139, v139, v249
v_sub_f32 v16, v16, v249
v_sub_f32 v80, v80, v249
v_sub_f32 v17, v17, v249
v_sub_f32 v81, v81, v249
v_sub_f32 v18, v18, v249
v_sub_f32 v82, v82, v249
v_sub_f32 v19, v19, v249
v_sub_f32 v83, v83, v249
v_sub_f32 v20, v20, v249
v_sub_f32 v84, v84, v249
v_sub_f32 v21, v21, v249
v_sub_f32 v85, v85, v249
v_sub_f32 v22, v22, v249
v_sub_f32 v86, v86, v249
v_sub_f32 v23, v23, v249
v_sub_f32 v87, v87, v249
v_sub_f32 v48, v48, v249
v_sub_f32 v112, v112, v249
v_sub_f32 v49, v49, v249
v_sub_f32 v113, v113, v249
v_sub_f32 v50, v50, v249
v_sub_f32 v114, v114, v249
v_sub_f32 v51, v51, v249
v_sub_f32 v115, v115, v249
v_sub_f32 v52, v52, v249
v_sub_f32 v116, v116, v249
v_sub_f32 v53, v53, v249
v_sub_f32 v117, v117, v249
v_sub_f32 v54, v54, v249
v_sub_f32 v118, v118, v249
v_sub_f32 v55, v55, v249
v_sub_f32 v119, v119, v249
s_lshl_b64 exec, 0xffff, 32
v_mul_f32 v236, v236, v250
s_mov_b64 exec, -1
AR v254,a16
v_mul_f32 v254, v254, v250
AW a16,v254
AR v251,a17
v_mul_f32 v251, v251, v250
AW a17,v251
AR v254,a18
v_mul_f32 v254, v254, v250
AW a18,v254
AR v251,a19
v_mul_f32 v251, v251, v250
AW a19,v251
AR v254,a20
v_mul_f32 v254, v254, v250
AW a20,v254
AR v251,a21
v_mul_f32 v251, v251, v250
AW a21,v251
AR v254,a22
v_mul_f32 v254, v254, v250
AW a22,v254
AR v251,a23
v_mul_f32 v251, v251, v250
AW a23,v251
AR v254,a48
v_mul_f32 v254, v254, v250
AW a48,v254
AR v251,a49
v_mul_f32 v251, v251, v250
AW a49,v251
AR v254,a50
v_mul_f32 v254, v254, v250
AW a50,v254
AR v251,a51
v_mul_f32 v251, v251, v250
AW a51,v251
AR v254,a52
v_mul_f32 v254, v254, v250
AW a52,v254
AR v251,a53
v_mul_f32 v251, v251, v250
AW a53,v251
AR v254,a54
v_mul_f32 v254, v254, v250
AW a54,v254
AR v251,a55
v_mul_f32 v251, v251, v250
AW a55,v251
AR v254,a80
v_mul_f32 v254, v254, v250
AW a80,v254
AR v251,a81
v_mul_f32 v251, v251, v250
AW a81,v251
AR v254,a82
v_mul_f32 v254, v254, v250
AW a82,v254
AR v251,a83
v_mul_f32 v251, v251, v250
AW a83,v251
AR v254,a84
v_mul_f32 v254, v254, v250
AW a84,v254
AR v251,a85
v_mul_f32 v251, v251, v250
AW a85,v251
AR v254,a86
v_mul_f32 v254, v254, v250
AW a86,v254
AR v251,a87
v_mul_f32 v251, v251, v250
AW a87,v251
AR v254,a112
v_mul_f32 v254, v254, v250
AW a112,v254
AR v251,a113
v_mul_f32 v251, v251, v250
AW a113,v251
AR v254,a114
v_mul_f32 v254, v254, v250
AW a114,v254
AR v251,a115
v_mul_f32 v251, v251, v250
AW a115,v251
AR v254,a116
v_mul_f32 v254, v254, v250
AW a116,v254
AR v251,a117
v_mul_f32 v251, v251, v250
AW a117,v251
AR v254,a118
v_mul_f32 v254, v254, v250
AW a118,v254
AR v251,a119
v_mul_f32 v251, v251, v250
AW a119,v251
v_max3_f32 v249, v88, v89, v90
v_max3_f32 v251, v120, v121, v122
v_max3_f32 v249, v249, v91, v92
v_max3_f32 v251, v251, v123, v124
v_max3_f32 v249, v249, v93, v94
v_max3_f32 v251, v251, v125, v126
v_max3_f32 v249, v249, v95, v127
v_max_f32 v249, v249, v251
v_log_f32 v250, v246
s_lshl_b64 exec, 0xffff, 48
v_max_f32 v249, v249, v250
s_mov_b64 exec, -1
v_mov_b32 v251, v249
s_nop 1
v_permlane16_swap_b32 v249, v251
v_max_f32 v249, v249, v251
v_mov_b32 v251, v249
s_nop 1
v_permlane32_swap_b32 v249, v251
v_max_f32 v249, v249, v251
)ASM"
R"ASM(
v_max_f32 v249, 0, v249
v_sub_f32 v250, 0, v249
EX v250,v250
v_sub_f32 v140, v140, v249
v_sub_f32 v141, v141, v249
v_sub_f32 v142, v142, v249
v_sub_f32 v143, v143, v249
v_sub_f32 v24, v24, v249
v_sub_f32 v88, v88, v249
v_sub_f32 v25, v25, v249
v_sub_f32 v89, v89, v249
v_sub_f32 v26, v26, v249
v_sub_f32 v90, v90, v249
v_sub_f32 v27, v27, v249
v_sub_f32 v91, v91, v249
v_sub_f32 v28, v28, v249
v_sub_f32 v92, v92, v249
v_sub_f32 v29, v29, v249
v_sub_f32 v93, v93, v249
v_sub_f32 v30, v30, v249
v_sub_f32 v94, v94, v249
v_sub_f32 v31, v31, v249
v_sub_f32 v95, v95, v249
v_sub_f32 v56, v56, v249
v_sub_f32 v120, v120, v249
v_sub_f32 v57, v57, v249
v_sub_f32 v121, v121, v249
v_sub_f32 v58, v58, v249
v_sub_f32 v122, v122, v249
v_sub_f32 v59, v59, v249
v_sub_f32 v123, v123, v249
v_sub_f32 v60, v60, v249
v_sub_f32 v124, v124, v249
v_sub_f32 v61, v61, v249
v_sub_f32 v125, v125, v249
v_sub_f32 v62, v62, v249
v_sub_f32 v126, v126, v249
v_sub_f32 v63, v63, v249
v_sub_f32 v127, v127, v249
s_lshl_b64 exec, 0xffff, 48
v_mul_f32 v236, v236, v250
s_mov_b64 exec, -1
AR v254,a24
v_mul_f32 v254, v254, v250
AW a24,v254
AR v251,a25
v_mul_f32 v251, v251, v250
AW a25,v251
AR v254,a26
v_mul_f32 v254, v254, v250
AW a26,v254
AR v251,a27
v_mul_f32 v251, v251, v250
AW a27,v251
AR v254,a28
v_mul_f32 v254, v254, v250
AW a28,v254
AR v251,a29
v_mul_f32 v251, v251, v250
AW a29,v251
AR v254,a30
v_mul_f32 v254, v254, v250
AW a30,v254
AR v251,a31
v_mul_f32 v251, v251, v250
AW a31,v251
AR v254,a56
v_mul_f32 v254, v254, v250
AW a56,v254
AR v251,a57
v_mul_f32 v251, v251, v250
AW a57,v251
AR v254,a58
v_mul_f32 v254, v254, v250
AW a58,v254
AR v251,a59
v_mul_f32 v251, v251, v250
AW a59,v251
AR v254,a60
v_mul_f32 v254, v254, v250
AW a60,v254
AR v251,a61
v_mul_f32 v251, v251, v250
AW a61,v251
AR v254,a62
v_mul_f32 v254, v254, v250
AW a62,v254
AR v251,a63
v_mul_f32 v251, v251, v250
AW a63,v251
AR v254,a88
v_mul_f32 v254, v254, v250
AW a88,v254
AR v251,a89
v_mul_f32 v251, v251, v250
AW a89,v251
AR v254,a90
v_mul_f32 v254, v254, v250
AW a90,v254
AR v251,a91
v_mul_f32 v251, v251, v250
AW a91,v251
AR v254,a92
v_mul_f32 v254, v254, v250
AW a92,v254
AR v251,a93
v_mul_f32 v251, v251, v250
AW a93,v251
AR v254,a94
v_mul_f32 v254, v254, v250
AW a94,v254
AR v251,a95
v_mul_f32 v251, v251, v250
AW a95,v251
AR v254,a120
v_mul_f32 v254, v254, v250
AW a120,v254
AR v251,a121
v_mul_f32 v251, v251, v250
AW a121,v251
AR v254,a122
v_mul_f32 v254, v254, v250
AW a122,v254
AR v251,a123
v_mul_f32 v251, v251, v250
AW a123,v251
AR v254,a124
v_mul_f32 v254, v254, v250
AW a124,v254
AR v251,a125
v_mul_f32 v251, v251, v250
AW a125,v251
AR v254,a126
v_mul_f32 v254, v254, v250
AW a126,v254
AR v251,a127
v_mul_f32 v251, v251, v250
AW a127,v251
EX v64,v64
EX v65,v65
EX v66,v66
EX v67,v67
EX v68,v68
EX v69,v69
EX v70,v70
EX v71,v71
EX v72,v72
EX v73,v73
EX v74,v74
EX v75,v75
EX v76,v76
EX v77,v77
EX v78,v78
EX v79,v79
EX v80,v80
EX v81,v81
EX v82,v82
EX v83,v83
EX v84,v84
EX v85,v85
EX v86,v86
EX v87,v87
EX v88,v88
EX v89,v89
EX v90,v90
EX v91,v91
EX v92,v92
EX v93,v93
EX v94,v94
EX v95,v95
EX v96,v96
EX v97,v97
EX v98,v98
EX v99,v99
EX v100,v100
EX v101,v101
EX v102,v102
EX v103,v103
EX v104,v104
EX v105,v105
EX v106,v106
EX v107,v107
EX v108,v108
EX v109,v109
EX v110,v110
EX v111,v111
EX v112,v112
EX v113,v113
EX v114,v114
EX v115,v115
EX v116,v116
EX v117,v117
EX v118,v118
EX v119,v119
EX v120,v120
EX v121,v121
EX v122,v122
EX v123,v123
EX v124,v124
EX v125,v125
EX v126,v126
EX v127,v127
CV v64,v64,v65
CV v65,v66,v67
CV v66,v68,v69
CV v67,v70,v71
CV v72,v72,v73
CV v73,v74,v75
CV v74,v76,v77
CV v75,v78,v79
CV v80,v80,v81
CV v81,v82,v83
CV v82,v84,v85
CV v83,v86,v87
CV v88,v88,v89
CV v89,v90,v91
CV v90,v92,v93
CV v91,v94,v95
CV v68,v96,v97
CV v69,v98,v99
CV v70,v100,v101
CV v71,v102,v103
CV v76,v104,v105
CV v77,v106,v107
CV v78,v108,v109
CV v79,v110,v111
CV v84,v112,v113
CV v85,v114,v115
CV v86,v116,v117
CV v87,v118,v119
CV v92,v120,v121
CV v93,v122,v123
CV v94,v124,v125
CV v95,v126,v127
v_smfmac_f32_16x16x64_bf16 v[236:239], v[144:147], v[64:71], v234
v_smfmac_f32_16x16x64_bf16 v[236:239], v[148:151], v[72:79], v234
v_smfmac_f32_16x16x64_bf16 v[236:239], v[152:155], v[80:87], v234
v_smfmac_f32_16x16x64_bf16 v[236:239], v[156:159], v[88:95], v234
s_nop 9
v_add_f32 v247, v236, v237
s_cmp_eq_u32 s73, 3
s_cbranch_scc1 LBACK3_%=
s_cmp_eq_u32 s73, 5
s_cbranch_scc1 LBACK5_%=
s_cmp_eq_u32 s73, 7
s_cbranch_scc1 LBACK7_%=
s_cmp_eq_u32 s73, 9
s_cbranch_scc1 LBACK9_%=
s_cmp_eq_u32 s73, 11
s_cbranch_scc1 LBACK11_%=
s_cmp_eq_u32 s73, 13
s_cbranch_scc1 LBACK13_%=
s_cmp_eq_u32 s73, 15
s_cbranch_scc1 LBACK15_%=
s_cmp_eq_u32 s73, 17
s_cbranch_scc1 LBACK17_%=
s_cmp_eq_u32 s73, 19
s_cbranch_scc1 LBACK19_%=
s_cmp_eq_u32 s73, 21
s_cbranch_scc1 LBACK21_%=
s_cmp_eq_u32 s73, 23
s_cbranch_scc1 LBACK23_%=
s_cmp_eq_u32 s73, 25
s_cbranch_scc1 LBACK25_%=
s_cmp_eq_u32 s73, 27
s_cbranch_scc1 LBACK27_%=
s_cmp_eq_u32 s73, 29
s_cbranch_scc1 LBACK29_%=
s_cmp_eq_u32 s73, 31
s_cbranch_scc1 LBACK31_%=
s_endpgm
LSITE4_%=:
s_mov_b32 s73, 4
s_mov_b32 s74, 0
s_mov_b32 s75, 16384
s_branch LRARE004_%=
LSITE6_%=:
s_mov_b32 s73, 6
s_mov_b32 s74, 32768
s_mov_b32 s75, 49152
s_branch LRARE004_%=
LSITE8_%=:
s_mov_b32 s73, 8
s_mov_b32 s74, 0
s_mov_b32 s75, 16384
s_branch LRARE004_%=
LSITE10_%=:
s_mov_b32 s73, 10
s_mov_b32 s74, 32768
s_mov_b32 s75, 49152
s_branch LRARE004_%=
LSITE12_%=:
s_mov_b32 s73, 12
s_mov_b32 s74, 0
s_mov_b32 s75, 16384
s_branch LRARE004_%=
LSITE14_%=:
s_mov_b32 s73, 14
s_mov_b32 s74, 32768
s_mov_b32 s75, 49152
s_branch LRARE004_%=
LSITE16_%=:
s_mov_b32 s73, 16
s_mov_b32 s74, 0
s_mov_b32 s75, 16384
s_branch LRARE004_%=
LSITE18_%=:
s_mov_b32 s73, 18
s_mov_b32 s74, 32768
s_mov_b32 s75, 49152
s_branch LRARE004_%=
LSITE20_%=:
s_mov_b32 s73, 20
s_mov_b32 s74, 0
s_mov_b32 s75, 16384
s_branch LRARE004_%=
LSITE22_%=:
s_mov_b32 s73, 22
s_mov_b32 s74, 32768
s_mov_b32 s75, 49152
s_branch LRARE004_%=
LSITE24_%=:
s_mov_b32 s73, 24
s_mov_b32 s74, 0
s_mov_b32 s75, 16384
s_branch LRARE004_%=
LSITE26_%=:
s_mov_b32 s73, 26
s_mov_b32 s74, 32768
s_mov_b32 s75, 49152
s_branch LRARE004_%=
LSITE28_%=:
s_mov_b32 s73, 28
s_mov_b32 s74, 0
s_mov_b32 s75, 16384
s_branch LRARE004_%=
LSITE30_%=:
s_mov_b32 s73, 30
s_mov_b32 s74, 32768
s_mov_b32 s75, 49152
s_branch LRARE004_%=
LRARE004_%=:
s_nop 15
s_waitcnt vmcnt(0) lgkmcnt(0)
v_add_u32 v249, s74, v224
RD a[192:195],v249 offset:0
RD a[208:211],v249 offset:4096
RD a[224:227],v249 offset:8192
RD a[240:243],v249 offset:12288
v_add_u32 v250, s74, v225
RD a[196:199],v250 offset:0
RD a[212:215],v250 offset:4096
RD a[228:231],v250 offset:8192
RD a[244:247],v250 offset:12288
v_add_u32 v251, s74, v226
RD a[200:203],v251 offset:0
RD a[216:219],v251 offset:4096
RD a[232:235],v251 offset:8192
RD a[248:251],v251 offset:12288
v_add_u32 v254, s74, v227
RD a[204:207],v254 offset:0
RD a[220:223],v254 offset:4096
RD a[236:239],v254 offset:8192
RD a[252:255],v254 offset:12288
s_waitcnt lgkmcnt(0)
MM v[0:3],a[192:195],v[160:163],v[128:131]
MM v[0:3],a[196:199],v[176:179],v[0:3]
MM v[0:3],a[200:203],v[192:195],v[0:3]
MM v[0:3],a[204:207],v[208:211],v[0:3]
MM v[4:7],a[220:223],v[208:211],v[128:131]
MM v[4:7],a[216:219],v[192:195],v[4:7]
MM v[4:7],a[212:215],v[176:179],v[4:7]
MM v[4:7],a[208:211],v[160:163],v[4:7]
MM v[12:15],a[208:211],v[164:167],v[132:135]
MM v[12:15],a[212:215],v[180:183],v[12:15]
MM v[12:15],a[216:219],v[196:199],v[12:15]
MM v[12:15],a[220:223],v[212:215],v[12:15]
MM v[8:11],a[204:207],v[212:215],v[132:135]
MM v[8:11],a[200:203],v[196:199],v[8:11]
MM v[8:11],a[196:199],v[180:183],v[8:11]
MM v[8:11],a[192:195],v[164:167],v[8:11]
MM v[16:19],a[192:195],v[168:171],v[136:139]
MM v[16:19],a[196:199],v[184:187],v[16:19]
MM v[16:19],a[200:203],v[200:203],v[16:19]
MM v[16:19],a[204:207],v[216:219],v[16:19]
MM v[20:23],a[220:223],v[216:219],v[136:139]
MM v[20:23],a[216:219],v[200:203],v[20:23]
MM v[20:23],a[212:215],v[184:187],v[20:23]
MM v[20:23],a[208:211],v[168:171],v[20:23]
MM v[28:31],a[208:211],v[172:175],v[140:143]
MM v[28:31],a[212:215],v[188:191],v[28:31]
MM v[28:31],a[216:219],v[204:207],v[28:31]
MM v[28:31],a[220:223],v[220:223],v[28:31]
MM v[24:27],a[204:207],v[220:223],v[140:143]
MM v[24:27],a[200:203],v[204:207],v[24:27]
MM v[24:27],a[196:199],v[188:191],v[24:27]
MM v[24:27],a[192:195],v[172:175],v[24:27]
MM v[32:35],a[224:227],v[160:163],v[128:131]
MM v[32:35],a[228:231],v[176:179],v[32:35]
MM v[32:35],a[232:235],v[192:195],v[32:35]
MM v[32:35],a[236:239],v[208:211],v[32:35]
MM v[36:39],a[252:255],v[208:211],v[128:131]
MM v[36:39],a[248:251],v[192:195],v[36:39]
MM v[36:39],a[244:247],v[176:179],v[36:39]
MM v[36:39],a[240:243],v[160:163],v[36:39]
MM v[44:47],a[240:243],v[164:167],v[132:135]
MM v[44:47],a[244:247],v[180:183],v[44:47]
MM v[44:47],a[248:251],v[196:199],v[44:47]
MM v[44:47],a[252:255],v[212:215],v[44:47]
MM v[40:43],a[236:239],v[212:215],v[132:135]
MM v[40:43],a[232:235],v[196:199],v[40:43]
MM v[40:43],a[228:231],v[180:183],v[40:43]
MM v[40:43],a[224:227],v[164:167],v[40:43]
MM v[48:51],a[224:227],v[168:171],v[136:139]
MM v[48:51],a[228:231],v[184:187],v[48:51]
MM v[48:51],a[232:235],v[200:203],v[48:51]
MM v[48:51],a[236:239],v[216:219],v[48:51]
MM v[52:55],a[252:255],v[216:219],v[136:139]
MM v[52:55],a[248:251],v[200:203],v[52:55]
MM v[52:55],a[244:247],v[184:187],v[52:55]
MM v[52:55],a[240:243],v[168:171],v[52:55]
MM v[60:63],a[240:243],v[172:175],v[140:143]
MM v[60:63],a[244:247],v[188:191],v[60:63]
MM v[60:63],a[248:251],v[204:207],v[60:63]
MM v[60:63],a[252:255],v[220:223],v[60:63]
MM v[56:59],a[236:239],v[220:223],v[140:143]
MM v[56:59],a[232:235],v[204:207],v[56:59]
MM v[56:59],a[228:231],v[188:191],v[56:59]
MM v[56:59],a[224:227],v[172:175],v[56:59]
s_nop 15
v_add_u32 v249, s75, v224
RD a[192:195],v249 offset:0
RD a[208:211],v249 offset:4096
RD a[224:227],v249 offset:8192
RD a[240:243],v249 offset:12288
v_add_u32 v250, s75, v225
RD a[196:199],v250 offset:0
RD a[212:215],v250 offset:4096
RD a[228:231],v250 offset:8192
RD a[244:247],v250 offset:12288
v_add_u32 v251, s75, v226
RD a[200:203],v251 offset:0
RD a[216:219],v251 offset:4096
RD a[232:235],v251 offset:8192
RD a[248:251],v251 offset:12288
v_add_u32 v254, s75, v227
RD a[204:207],v254 offset:0
RD a[220:223],v254 offset:4096
RD a[236:239],v254 offset:8192
RD a[252:255],v254 offset:12288
s_waitcnt lgkmcnt(0)
v_mov_b32 v236, v247
v_mov_b32 v237, 0
v_max3_f32 v249, v0, v1, v2
v_max3_f32 v251, v32, v33, v34
v_max3_f32 v249, v249, v3, v4
v_max3_f32 v251, v251, v35, v36
v_max3_f32 v249, v249, v5, v6
v_max3_f32 v251, v251, v37, v38
v_max3_f32 v249, v249, v7, v39
v_max_f32 v249, v249, v251
v_log_f32 v250, v247
s_lshl_b64 exec, 0xffff, 0
v_max_f32 v249, v249, v250
s_mov_b64 exec, -1
v_mov_b32 v251, v249
s_nop 1
v_permlane16_swap_b32 v249, v251
v_max_f32 v249, v249, v251
v_mov_b32 v251, v249
s_nop 1
v_permlane32_swap_b32 v249, v251
v_max_f32 v249, v249, v251
v_max_f32 v249, 0, v249
v_sub_f32 v250, 0, v249
EX v250,v250
v_sub_f32 v128, v128, v249
v_sub_f32 v129, v129, v249
v_sub_f32 v130, v130, v249
v_sub_f32 v131, v131, v249
v_sub_f32 v64, v64, v249
v_sub_f32 v0, v0, v249
v_sub_f32 v65, v65, v249
v_sub_f32 v1, v1, v249
v_sub_f32 v66, v66, v249
v_sub_f32 v2, v2, v249
v_sub_f32 v67, v67, v249
v_sub_f32 v3, v3, v249
v_sub_f32 v68, v68, v249
v_sub_f32 v4, v4, v249
v_sub_f32 v69, v69, v249
v_sub_f32 v5, v5, v249
v_sub_f32 v70, v70, v249
v_sub_f32 v6, v6, v249
v_sub_f32 v71, v71, v249
v_sub_f32 v7, v7, v249
v_sub_f32 v96, v96, v249
v_sub_f32 v32, v32, v249
v_sub_f32 v97, v97, v249
v_sub_f32 v33, v33, v249
v_sub_f32 v98, v98, v249
v_sub_f32 v34, v34, v249
v_sub_f32 v99, v99, v249
v_sub_f32 v35, v35, v249
v_sub_f32 v100, v100, v249
v_sub_f32 v36, v36, v249
v_sub_f32 v101, v101, v249
v_sub_f32 v37, v37, v249
v_sub_f32 v102, v102, v249
v_sub_f32 v38, v38, v249
v_sub_f32 v103, v103, v249
v_sub_f32 v39, v39, v249
s_lshl_b64 exec, 0xffff, 0
v_mul_f32 v236, v236, v250
s_mov_b64 exec, -1
AR v254,a0
v_mul_f32 v254, v254, v250
AW a0,v254
AR v251,a1
v_mul_f32 v251, v251, v250
AW a1,v251
AR v254,a2
v_mul_f32 v254, v254, v250
)ASM"
R"ASM(
AW a2,v254
AR v251,a3
v_mul_f32 v251, v251, v250
AW a3,v251
AR v254,a4
v_mul_f32 v254, v254, v250
AW a4,v254
AR v251,a5
v_mul_f32 v251, v251, v250
AW a5,v251
AR v254,a6
v_mul_f32 v254, v254, v250
AW a6,v254
AR v251,a7
v_mul_f32 v251, v251, v250
AW a7,v251
AR v254,a32
v_mul_f32 v254, v254, v250
AW a32,v254
AR v251,a33
v_mul_f32 v251, v251, v250
AW a33,v251
AR v254,a34
v_mul_f32 v254, v254, v250
AW a34,v254
AR v251,a35
v_mul_f32 v251, v251, v250
AW a35,v251
AR v254,a36
v_mul_f32 v254, v254, v250
AW a36,v254
AR v251,a37
v_mul_f32 v251, v251, v250
AW a37,v251
AR v254,a38
v_mul_f32 v254, v254, v250
AW a38,v254
AR v251,a39
v_mul_f32 v251, v251, v250
AW a39,v251
AR v254,a64
v_mul_f32 v254, v254, v250
AW a64,v254
AR v251,a65
v_mul_f32 v251, v251, v250
AW a65,v251
AR v254,a66
v_mul_f32 v254, v254, v250
AW a66,v254
AR v251,a67
v_mul_f32 v251, v251, v250
AW a67,v251
AR v254,a68
v_mul_f32 v254, v254, v250
AW a68,v254
AR v251,a69
v_mul_f32 v251, v251, v250
AW a69,v251
AR v254,a70
v_mul_f32 v254, v254, v250
AW a70,v254
AR v251,a71
v_mul_f32 v251, v251, v250
AW a71,v251
AR v254,a96
v_mul_f32 v254, v254, v250
AW a96,v254
AR v251,a97
v_mul_f32 v251, v251, v250
AW a97,v251
AR v254,a98
v_mul_f32 v254, v254, v250
AW a98,v254
AR v251,a99
v_mul_f32 v251, v251, v250
AW a99,v251
AR v254,a100
v_mul_f32 v254, v254, v250
AW a100,v254
AR v251,a101
v_mul_f32 v251, v251, v250
AW a101,v251
AR v254,a102
v_mul_f32 v254, v254, v250
AW a102,v254
AR v251,a103
v_mul_f32 v251, v251, v250
AW a103,v251
v_max3_f32 v249, v8, v9, v10
v_max3_f32 v251, v40, v41, v42
v_max3_f32 v249, v249, v11, v12
v_max3_f32 v251, v251, v43, v44
v_max3_f32 v249, v249, v13, v14
v_max3_f32 v251, v251, v45, v46
v_max3_f32 v249, v249, v15, v47
v_max_f32 v249, v249, v251
v_log_f32 v250, v247
s_lshl_b64 exec, 0xffff, 16
v_max_f32 v249, v249, v250
s_mov_b64 exec, -1
v_mov_b32 v251, v249
s_nop 1
v_permlane16_swap_b32 v249, v251
v_max_f32 v249, v249, v251
v_mov_b32 v251, v249
s_nop 1
v_permlane32_swap_b32 v249, v251
v_max_f32 v249, v249, v251
v_max_f32 v249, 0, v249
v_sub_f32 v250, 0, v249
EX v250,v250
v_sub_f32 v132, v132, v249
v_sub_f32 v133, v133, v249
v_sub_f32 v134, v134, v249
v_sub_f32 v135, v135, v249
v_sub_f32 v72, v72, v249
v_sub_f32 v8, v8, v249
v_sub_f32 v73, v73, v249
v_sub_f32 v9, v9, v249
v_sub_f32 v74, v74, v249
v_sub_f32 v10, v10, v249
v_sub_f32 v75, v75, v249
v_sub_f32 v11, v11, v249
v_sub_f32 v76, v76, v249
v_sub_f32 v12, v12, v249
v_sub_f32 v77, v77, v249
v_sub_f32 v13, v13, v249
v_sub_f32 v78, v78, v249
v_sub_f32 v14, v14, v249
v_sub_f32 v79, v79, v249
v_sub_f32 v15, v15, v249
v_sub_f32 v104, v104, v249
v_sub_f32 v40, v40, v249
v_sub_f32 v105, v105, v249
v_sub_f32 v41, v41, v249
v_sub_f32 v106, v106, v249
v_sub_f32 v42, v42, v249
v_sub_f32 v107, v107, v249
v_sub_f32 v43, v43, v249
v_sub_f32 v108, v108, v249
v_sub_f32 v44, v44, v249
v_sub_f32 v109, v109, v249
v_sub_f32 v45, v45, v249
v_sub_f32 v110, v110, v249
v_sub_f32 v46, v46, v249
v_sub_f32 v111, v111, v249
v_sub_f32 v47, v47, v249
s_lshl_b64 exec, 0xffff, 16
v_mul_f32 v236, v236, v250
s_mov_b64 exec, -1
AR v254,a8
v_mul_f32 v254, v254, v250
AW a8,v254
AR v251,a9
v_mul_f32 v251, v251, v250
AW a9,v251
AR v254,a10
v_mul_f32 v254, v254, v250
AW a10,v254
AR v251,a11
v_mul_f32 v251, v251, v250
AW a11,v251
AR v254,a12
v_mul_f32 v254, v254, v250
AW a12,v254
AR v251,a13
v_mul_f32 v251, v251, v250
AW a13,v251
AR v254,a14
v_mul_f32 v254, v254, v250
AW a14,v254
AR v251,a15
v_mul_f32 v251, v251, v250
AW a15,v251
AR v254,a40
v_mul_f32 v254, v254, v250
AW a40,v254
AR v251,a41
v_mul_f32 v251, v251, v250
AW a41,v251
AR v254,a42
v_mul_f32 v254, v254, v250
AW a42,v254
AR v251,a43
v_mul_f32 v251, v251, v250
AW a43,v251
AR v254,a44
v_mul_f32 v254, v254, v250
AW a44,v254
AR v251,a45
v_mul_f32 v251, v251, v250
AW a45,v251
AR v254,a46
v_mul_f32 v254, v254, v250
AW a46,v254
AR v251,a47
v_mul_f32 v251, v251, v250
AW a47,v251
AR v254,a72
v_mul_f32 v254, v254, v250
AW a72,v254
AR v251,a73
v_mul_f32 v251, v251, v250
AW a73,v251
AR v254,a74
v_mul_f32 v254, v254, v250
AW a74,v254
AR v251,a75
v_mul_f32 v251, v251, v250
AW a75,v251
AR v254,a76
v_mul_f32 v254, v254, v250
AW a76,v254
AR v251,a77
v_mul_f32 v251, v251, v250
AW a77,v251
AR v254,a78
v_mul_f32 v254, v254, v250
AW a78,v254
AR v251,a79
v_mul_f32 v251, v251, v250
AW a79,v251
AR v254,a104
v_mul_f32 v254, v254, v250
AW a104,v254
AR v251,a105
v_mul_f32 v251, v251, v250
AW a105,v251
AR v254,a106
v_mul_f32 v254, v254, v250
AW a106,v254
AR v251,a107
v_mul_f32 v251, v251, v250
AW a107,v251
AR v254,a108
v_mul_f32 v254, v254, v250
AW a108,v254
AR v251,a109
v_mul_f32 v251, v251, v250
AW a109,v251
AR v254,a110
v_mul_f32 v254, v254, v250
AW a110,v254
AR v251,a111
v_mul_f32 v251, v251, v250
AW a111,v251
v_max3_f32 v249, v16, v17, v18
v_max3_f32 v251, v48, v49, v50
v_max3_f32 v249, v249, v19, v20
v_max3_f32 v251, v251, v51, v52
v_max3_f32 v249, v249, v21, v22
v_max3_f32 v251, v251, v53, v54
v_max3_f32 v249, v249, v23, v55
v_max_f32 v249, v249, v251
v_log_f32 v250, v247
s_lshl_b64 exec, 0xffff, 32
v_max_f32 v249, v249, v250
s_mov_b64 exec, -1
v_mov_b32 v251, v249
s_nop 1
v_permlane16_swap_b32 v249, v251
v_max_f32 v249, v249, v251
v_mov_b32 v251, v249
s_nop 1
v_permlane32_swap_b32 v249, v251
v_max_f32 v249, v249, v251
v_max_f32 v249, 0, v249
v_sub_f32 v250, 0, v249
EX v250,v250
v_sub_f32 v136, v136, v249
v_sub_f32 v137, v137, v249
v_sub_f32 v138, v138, v249
v_sub_f32 v139, v139, v249
v_sub_f32 v80, v80, v249
v_sub_f32 v16, v16, v249
v_sub_f32 v81, v81, v249
v_sub_f32 v17, v17, v249
v_sub_f32 v82, v82, v249
v_sub_f32 v18, v18, v249
v_sub_f32 v83, v83, v249
v_sub_f32 v19, v19, v249
v_sub_f32 v84, v84, v249
v_sub_f32 v20, v20, v249
v_sub_f32 v85, v85, v249
v_sub_f32 v21, v21, v249
v_sub_f32 v86, v86, v249
v_sub_f32 v22, v22, v249
v_sub_f32 v87, v87, v249
v_sub_f32 v23, v23, v249
v_sub_f32 v112, v112, v249
v_sub_f32 v48, v48, v249
v_sub_f32 v113, v113, v249
v_sub_f32 v49, v49, v249
v_sub_f32 v114, v114, v249
v_sub_f32 v50, v50, v249
v_sub_f32 v115, v115, v249
v_sub_f32 v51, v51, v249
v_sub_f32 v116, v116, v249
v_sub_f32 v52, v52, v249
v_sub_f32 v117, v117, v249
v_sub_f32 v53, v53, v249
v_sub_f32 v118, v118, v249
v_sub_f32 v54, v54, v249
v_sub_f32 v119, v119, v249
v_sub_f32 v55, v55, v249
s_lshl_b64 exec, 0xffff, 32
v_mul_f32 v236, v236, v250
s_mov_b64 exec, -1
AR v254,a16
v_mul_f32 v254, v254, v250
AW a16,v254
AR v251,a17
v_mul_f32 v251, v251, v250
AW a17,v251
AR v254,a18
v_mul_f32 v254, v254, v250
AW a18,v254
AR v251,a19
v_mul_f32 v251, v251, v250
AW a19,v251
AR v254,a20
v_mul_f32 v254, v254, v250
AW a20,v254
AR v251,a21
v_mul_f32 v251, v251, v250
AW a21,v251
AR v254,a22
v_mul_f32 v254, v254, v250
AW a22,v254
AR v251,a23
v_mul_f32 v251, v251, v250
AW a23,v251
AR v254,a48
v_mul_f32 v254, v254, v250
AW a48,v254
AR v251,a49
v_mul_f32 v251, v251, v250
AW a49,v251
AR v254,a50
v_mul_f32 v254, v254, v250
AW a50,v254
AR v251,a51
v_mul_f32 v251, v251, v250
AW a51,v251
AR v254,a52
v_mul_f32 v254, v254, v250
AW a52,v254
AR v251,a53
v_mul_f32 v251, v251, v250
AW a53,v251
AR v254,a54
v_mul_f32 v254, v254, v250
AW a54,v254
AR v251,a55
v_mul_f32 v251, v251, v250
AW a55,v251
AR v254,a80
v_mul_f32 v254, v254, v250
AW a80,v254
AR v251,a81
v_mul_f32 v251, v251, v250
AW a81,v251
AR v254,a82
v_mul_f32 v254, v254, v250
AW a82,v254
AR v251,a83
v_mul_f32 v251, v251, v250
AW a83,v251
AR v254,a84
v_mul_f32 v254, v254, v250
AW a84,v254
AR v251,a85
v_mul_f32 v251, v251, v250
AW a85,v251
AR v254,a86
v_mul_f32 v254, v254, v250
AW a86,v254
AR v251,a87
v_mul_f32 v251, v251, v250
AW a87,v251
AR v254,a112
v_mul_f32 v254, v254, v250
AW a112,v254
AR v251,a113
v_mul_f32 v251, v251, v250
AW a113,v251
AR v254,a114
v_mul_f32 v254, v254, v250
AW a114,v254
AR v251,a115
v_mul_f32 v251, v251, v250
AW a115,v251
AR v254,a116
v_mul_f32 v254, v254, v250
AW a116,v254
AR v251,a117
v_mul_f32 v251, v251, v250
AW a117,v251
AR v254,a118
v_mul_f32 v254, v254, v250
AW a118,v254
AR v251,a119
v_mul_f32 v251, v251, v250
AW a119,v251
v_max3_f32 v249, v24, v25, v26
v_max3_f32 v251, v56, v57, v58
v_max3_f32 v249, v249, v27, v28
v_max3_f32 v251, v251, v59, v60
v_max3_f32 v249, v249, v29, v30
v_max3_f32 v251, v251, v61, v62
v_max3_f32 v249, v249, v31, v63
v_max_f32 v249, v249, v251
v_log_f32 v250, v247
s_lshl_b64 exec, 0xffff, 48
v_max_f32 v249, v249, v250
s_mov_b64 exec, -1
v_mov_b32 v251, v249
s_nop 1
v_permlane16_swap_b32 v249, v251
v_max_f32 v249, v249, v251
v_mov_b32 v251, v249
s_nop 1
v_permlane32_swap_b32 v249, v251
v_max_f32 v249, v249, v251
v_max_f32 v249, 0, v249
v_sub_f32 v250, 0, v249
EX v250,v250
v_sub_f32 v140, v140, v249
v_sub_f32 v141, v141, v249
v_sub_f32 v142, v142, v249
v_sub_f32 v143, v143, v249
v_sub_f32 v88, v88, v249
v_sub_f32 v24, v24, v249
v_sub_f32 v89, v89, v249
v_sub_f32 v25, v25, v249
v_sub_f32 v90, v90, v249
v_sub_f32 v26, v26, v249
v_sub_f32 v91, v91, v249
v_sub_f32 v27, v27, v249
v_sub_f32 v92, v92, v249
v_sub_f32 v28, v28, v249
v_sub_f32 v93, v93, v249
v_sub_f32 v29, v29, v249
v_sub_f32 v94, v94, v249
v_sub_f32 v30, v30, v249
v_sub_f32 v95, v95, v249
v_sub_f32 v31, v31, v249
v_sub_f32 v120, v120, v249
v_sub_f32 v56, v56, v249
v_sub_f32 v121, v121, v249
v_sub_f32 v57, v57, v249
v_sub_f32 v122, v122, v249
v_sub_f32 v58, v58, v249
v_sub_f32 v123, v123, v249
v_sub_f32 v59, v59, v249
v_sub_f32 v124, v124, v249
v_sub_f32 v60, v60, v249
v_sub_f32 v125, v125, v249
v_sub_f32 v61, v61, v249
v_sub_f32 v126, v126, v249
v_sub_f32 v62, v62, v249
v_sub_f32 v127, v127, v249
v_sub_f32 v63, v63, v249
s_lshl_b64 exec, 0xffff, 48
v_mul_f32 v236, v236, v250
s_mov_b64 exec, -1
AR v254,a24
v_mul_f32 v254, v254, v250
AW a24,v254
AR v251,a25
v_mul_f32 v251, v251, v250
AW a25,v251
AR v254,a26
v_mul_f32 v254, v254, v250
AW a26,v254
AR v251,a27
v_mul_f32 v251, v251, v250
AW a27,v251
AR v254,a28
v_mul_f32 v254, v254, v250
AW a28,v254
AR v251,a29
v_mul_f32 v251, v251, v250
AW a29,v251
AR v254,a30
v_mul_f32 v254, v254, v250
AW a30,v254
AR v251,a31
v_mul_f32 v251, v251, v250
AW a31,v251
AR v254,a56
v_mul_f32 v254, v254, v250
AW a56,v254
AR v251,a57
v_mul_f32 v251, v251, v250
AW a57,v251
AR v254,a58
v_mul_f32 v254, v254, v250
AW a58,v254
AR v251,a59
v_mul_f32 v251, v251, v250
AW a59,v251
AR v254,a60
v_mul_f32 v254, v254, v250
AW a60,v254
AR v251,a61
v_mul_f32 v251, v251, v250
AW a61,v251
AR v254,a62
v_mul_f32 v254, v254, v250
AW a62,v254
AR v251,a63
v_mul_f32 v251, v251, v250
AW a63,v251
AR v254,a88
v_mul_f32 v254, v254, v250
AW a88,v254
AR v251,a89
v_mul_f32 v251, v251, v250
AW a89,v251
AR v254,a90
v_mul_f32 v254, v254, v250
AW a90,v254
AR v251,a91
v_mul_f32 v251, v251, v250
AW a91,v251
AR v254,a92
v_mul_f32 v254, v254, v250
AW a92,v254
AR v251,a93
v_mul_f32 v251, v251, v250
AW a93,v251
AR v254,a94
v_mul_f32 v254, v254, v250
AW a94,v254
AR v251,a95
v_mul_f32 v251, v251, v250
AW a95,v251
AR v254,a120
v_mul_f32 v254, v254, v250
AW a120,v254
AR v251,a121
v_mul_f32 v251, v251, v250
AW a121,v251
AR v254,a122
v_mul_f32 v254, v254, v250
AW a122,v254
AR v251,a123
v_mul_f32 v251, v251, v250
AW a123,v251
AR v254,a124
v_mul_f32 v254, v254, v250
AW a124,v254
AR v251,a125
v_mul_f32 v251, v251, v250
AW a125,v251
AR v254,a126
v_mul_f32 v254, v254, v250
AW a126,v254
AR v251,a127
v_mul_f32 v251, v251, v250
AW a127,v251
EX v0,v0
EX v1,v1
EX v2,v2
EX v3,v3
EX v4,v4
EX v5,v5
EX v6,v6
EX v7,v7
EX v8,v8
EX v9,v9
EX v10,v10
EX v11,v11
EX v12,v12
EX v13,v13
EX v14,v14
EX v15,v15
EX v16,v16
EX v17,v17
EX v18,v18
EX v19,v19
EX v20,v20
EX v21,v21
EX v22,v22
EX v23,v23
EX v24,v24
EX v25,v25
EX v26,v26
EX v27,v27
EX v28,v28
EX v29,v29
EX v30,v30
EX v31,v31
EX v32,v32
EX v33,v33
EX v34,v34
EX v35,v35
EX v36,v36
EX v37,v37
EX v38,v38
EX v39,v39
EX v40,v40
EX v41,v41
EX v42,v42
EX v43,v43
EX v44,v44
EX v45,v45
EX v46,v46
EX v47,v47
EX v48,v48
EX v49,v49
EX v50,v50
EX v51,v51
EX v52,v52
EX v53,v53
EX v54,v54
EX v55,v55
EX v56,v56
EX v57,v57
EX v58,v58
EX v59,v59
EX v60,v60
EX v61,v61
EX v62,v62
EX v63,v63
CV v0,v0,v1
CV v1,v2,v3
CV v2,v4,v5
CV v3,v6,v7
CV v8,v8,v9
CV v9,v10,v11
CV v10,v12,v13
CV v11,v14,v15
CV v16,v16,v17
CV v17,v18,v19
CV v18,v20,v21
CV v19,v22,v23
)ASM"
R"ASM(
CV v24,v24,v25
CV v25,v26,v27
CV v26,v28,v29
CV v27,v30,v31
CV v4,v32,v33
CV v5,v34,v35
CV v6,v36,v37
CV v7,v38,v39
CV v12,v40,v41
CV v13,v42,v43
CV v14,v44,v45
CV v15,v46,v47
CV v20,v48,v49
CV v21,v50,v51
CV v22,v52,v53
CV v23,v54,v55
CV v28,v56,v57
CV v29,v58,v59
CV v30,v60,v61
CV v31,v62,v63
v_smfmac_f32_16x16x64_bf16 v[236:239], v[144:147], v[0:7], v234
v_smfmac_f32_16x16x64_bf16 v[236:239], v[148:151], v[8:15], v234
v_smfmac_f32_16x16x64_bf16 v[236:239], v[152:155], v[16:23], v234
v_smfmac_f32_16x16x64_bf16 v[236:239], v[156:159], v[24:31], v234
s_nop 9
v_add_f32 v246, v236, v237
s_cmp_eq_u32 s73, 4
s_cbranch_scc1 LBACK4_%=
s_cmp_eq_u32 s73, 6
s_cbranch_scc1 LBACK6_%=
s_cmp_eq_u32 s73, 8
s_cbranch_scc1 LBACK8_%=
s_cmp_eq_u32 s73, 10
s_cbranch_scc1 LBACK10_%=
s_cmp_eq_u32 s73, 12
s_cbranch_scc1 LBACK12_%=
s_cmp_eq_u32 s73, 14
s_cbranch_scc1 LBACK14_%=
s_cmp_eq_u32 s73, 16
s_cbranch_scc1 LBACK16_%=
s_cmp_eq_u32 s73, 18
s_cbranch_scc1 LBACK18_%=
s_cmp_eq_u32 s73, 20
s_cbranch_scc1 LBACK20_%=
s_cmp_eq_u32 s73, 22
s_cbranch_scc1 LBACK22_%=
s_cmp_eq_u32 s73, 24
s_cbranch_scc1 LBACK24_%=
s_cmp_eq_u32 s73, 26
s_cbranch_scc1 LBACK26_%=
s_cmp_eq_u32 s73, 28
s_cbranch_scc1 LBACK28_%=
s_cmp_eq_u32 s73, 30
s_cbranch_scc1 LBACK30_%=
s_endpgm
LSITE32_%=:
s_mov_b32 s73, 32
s_mov_b32 s74, 0
s_mov_b32 s75, 16384
s_branch LRARE022_%=
LSITE34_%=:
s_mov_b32 s73, 34
s_mov_b32 s74, 32768
s_mov_b32 s75, 49152
s_branch LRARE022_%=
LRARE022_%=:
s_nop 15
s_waitcnt vmcnt(0) lgkmcnt(0)
v_add_u32 v249, s74, v224
RD a[192:195],v249 offset:0
RD a[208:211],v249 offset:4096
RD a[224:227],v249 offset:8192
RD a[240:243],v249 offset:12288
v_add_u32 v250, s74, v225
RD a[196:199],v250 offset:0
RD a[212:215],v250 offset:4096
RD a[228:231],v250 offset:8192
RD a[244:247],v250 offset:12288
v_add_u32 v251, s74, v226
RD a[200:203],v251 offset:0
RD a[216:219],v251 offset:4096
RD a[232:235],v251 offset:8192
RD a[248:251],v251 offset:12288
v_add_u32 v254, s74, v227
RD a[204:207],v254 offset:0
RD a[220:223],v254 offset:4096
RD a[236:239],v254 offset:8192
RD a[252:255],v254 offset:12288
s_waitcnt lgkmcnt(0)
MM v[16:19],a[192:195],v[168:171],v[136:139]
MM v[16:19],a[196:199],v[184:187],v[16:19]
MM v[16:19],a[200:203],v[200:203],v[16:19]
MM v[16:19],a[204:207],v[216:219],v[16:19]
MM v[20:23],a[220:223],v[216:219],v[136:139]
MM v[20:23],a[216:219],v[200:203],v[20:23]
MM v[20:23],a[212:215],v[184:187],v[20:23]
MM v[20:23],a[208:211],v[168:171],v[20:23]
MM v[28:31],a[208:211],v[172:175],v[140:143]
MM v[28:31],a[212:215],v[188:191],v[28:31]
MM v[28:31],a[216:219],v[204:207],v[28:31]
MM v[28:31],a[220:223],v[220:223],v[28:31]
MM v[24:27],a[204:207],v[220:223],v[140:143]
MM v[24:27],a[200:203],v[204:207],v[24:27]
MM v[24:27],a[196:199],v[188:191],v[24:27]
MM v[24:27],a[192:195],v[172:175],v[24:27]
MM v[48:51],a[224:227],v[168:171],v[136:139]
MM v[48:51],a[228:231],v[184:187],v[48:51]
MM v[48:51],a[232:235],v[200:203],v[48:51]
MM v[48:51],a[236:239],v[216:219],v[48:51]
MM v[52:55],a[252:255],v[216:219],v[136:139]
MM v[52:55],a[248:251],v[200:203],v[52:55]
MM v[52:55],a[244:247],v[184:187],v[52:55]
MM v[52:55],a[240:243],v[168:171],v[52:55]
MM v[60:63],a[240:243],v[172:175],v[140:143]
MM v[60:63],a[244:247],v[188:191],v[60:63]
MM v[60:63],a[248:251],v[204:207],v[60:63]
MM v[60:63],a[252:255],v[220:223],v[60:63]
MM v[56:59],a[236:239],v[220:223],v[140:143]
MM v[56:59],a[232:235],v[204:207],v[56:59]
MM v[56:59],a[228:231],v[188:191],v[56:59]
MM v[56:59],a[224:227],v[172:175],v[56:59]
s_nop 15
v_add_u32 v249, s75, v224
RD a[192:195],v249 offset:0
RD a[208:211],v249 offset:4096
RD a[224:227],v249 offset:8192
RD a[240:243],v249 offset:12288
v_add_u32 v250, s75, v225
RD a[196:199],v250 offset:0
RD a[212:215],v250 offset:4096
RD a[228:231],v250 offset:8192
RD a[244:247],v250 offset:12288
v_add_u32 v251, s75, v226
RD a[200:203],v251 offset:0
RD a[216:219],v251 offset:4096
RD a[232:235],v251 offset:8192
RD a[248:251],v251 offset:12288
v_add_u32 v254, s75, v227
RD a[204:207],v254 offset:0
RD a[220:223],v254 offset:4096
RD a[236:239],v254 offset:8192
RD a[252:255],v254 offset:12288
s_waitcnt lgkmcnt(0)
v_mov_b32 v236, v247
v_mov_b32 v237, 0
v_max3_f32 v249, v16, v17, v18
v_max3_f32 v251, v48, v49, v50
v_max3_f32 v249, v249, v19, v20
v_max3_f32 v251, v251, v51, v52
v_max3_f32 v249, v249, v21, v22
v_max3_f32 v251, v251, v53, v54
v_max3_f32 v249, v249, v23, v55
v_max_f32 v249, v249, v251
v_log_f32 v250, v247
s_lshl_b64 exec, 0xffff, 32
v_max_f32 v249, v249, v250
s_mov_b64 exec, -1
v_mov_b32 v251, v249
s_nop 1
v_permlane16_swap_b32 v249, v251
v_max_f32 v249, v249, v251
v_mov_b32 v251, v249
s_nop 1
v_permlane32_swap_b32 v249, v251
v_max_f32 v249, v249, v251
v_max_f32 v249, 0, v249
v_sub_f32 v250, 0, v249
EX v250,v250
v_sub_f32 v136, v136, v249
v_sub_f32 v137, v137, v249
v_sub_f32 v138, v138, v249
v_sub_f32 v139, v139, v249
v_sub_f32 v80, v80, v249
v_sub_f32 v16, v16, v249
v_sub_f32 v81, v81, v249
v_sub_f32 v17, v17, v249
v_sub_f32 v82, v82, v249
v_sub_f32 v18, v18, v249
v_sub_f32 v83, v83, v249
v_sub_f32 v19, v19, v249
v_sub_f32 v84, v84, v249
v_sub_f32 v20, v20, v249
v_sub_f32 v85, v85, v249
v_sub_f32 v21, v21, v249
v_sub_f32 v86, v86, v249
v_sub_f32 v22, v22, v249
v_sub_f32 v87, v87, v249
v_sub_f32 v23, v23, v249
v_sub_f32 v112, v112, v249
v_sub_f32 v48, v48, v249
v_sub_f32 v113, v113, v249
v_sub_f32 v49, v49, v249
v_sub_f32 v114, v114, v249
v_sub_f32 v50, v50, v249
v_sub_f32 v115, v115, v249
v_sub_f32 v51, v51, v249
v_sub_f32 v116, v116, v249
v_sub_f32 v52, v52, v249
v_sub_f32 v117, v117, v249
v_sub_f32 v53, v53, v249
v_sub_f32 v118, v118, v249
v_sub_f32 v54, v54, v249
v_sub_f32 v119, v119, v249
v_sub_f32 v55, v55, v249
s_lshl_b64 exec, 0xffff, 32
v_mul_f32 v236, v236, v250
s_mov_b64 exec, -1
AR v254,a16
v_mul_f32 v254, v254, v250
AW a16,v254
AR v251,a17
v_mul_f32 v251, v251, v250
AW a17,v251
AR v254,a18
v_mul_f32 v254, v254, v250
AW a18,v254
AR v251,a19
v_mul_f32 v251, v251, v250
AW a19,v251
AR v254,a20
v_mul_f32 v254, v254, v250
AW a20,v254
AR v251,a21
v_mul_f32 v251, v251, v250
AW a21,v251
AR v254,a22
v_mul_f32 v254, v254, v250
AW a22,v254
AR v251,a23
v_mul_f32 v251, v251, v250
AW a23,v251
AR v254,a48
v_mul_f32 v254, v254, v250
AW a48,v254
AR v251,a49
v_mul_f32 v251, v251, v250
AW a49,v251
AR v254,a50
v_mul_f32 v254, v254, v250
AW a50,v254
AR v251,a51
v_mul_f32 v251, v251, v250
AW a51,v251
AR v254,a52
v_mul_f32 v254, v254, v250
AW a52,v254
AR v251,a53
v_mul_f32 v251, v251, v250
AW a53,v251
AR v254,a54
v_mul_f32 v254, v254, v250
AW a54,v254
AR v251,a55
v_mul_f32 v251, v251, v250
AW a55,v251
AR v254,a80
v_mul_f32 v254, v254, v250
AW a80,v254
AR v251,a81
v_mul_f32 v251, v251, v250
AW a81,v251
AR v254,a82
v_mul_f32 v254, v254, v250
AW a82,v254
AR v251,a83
v_mul_f32 v251, v251, v250
AW a83,v251
AR v254,a84
v_mul_f32 v254, v254, v250
AW a84,v254
AR v251,a85
v_mul_f32 v251, v251, v250
AW a85,v251
AR v254,a86
v_mul_f32 v254, v254, v250
AW a86,v254
AR v251,a87
v_mul_f32 v251, v251, v250
AW a87,v251
AR v254,a112
v_mul_f32 v254, v254, v250
AW a112,v254
AR v251,a113
v_mul_f32 v251, v251, v250
AW a113,v251
AR v254,a114
v_mul_f32 v254, v254, v250
AW a114,v254
AR v251,a115
v_mul_f32 v251, v251, v250
AW a115,v251
AR v254,a116
v_mul_f32 v254, v254, v250
AW a116,v254
AR v251,a117
v_mul_f32 v251, v251, v250
AW a117,v251
AR v254,a118
v_mul_f32 v254, v254, v250
AW a118,v254
AR v251,a119
v_mul_f32 v251, v251, v250
AW a119,v251
v_max3_f32 v249, v24, v25, v26
v_max3_f32 v251, v56, v57, v58
v_max3_f32 v249, v249, v27, v28
v_max3_f32 v251, v251, v59, v60
v_max3_f32 v249, v249, v29, v30
v_max3_f32 v251, v251, v61, v62
v_max3_f32 v249, v249, v31, v63
v_max_f32 v249, v249, v251
v_log_f32 v250, v247
s_lshl_b64 exec, 0xffff, 48
v_max_f32 v249, v249, v250
s_mov_b64 exec, -1
v_mov_b32 v251, v249
s_nop 1
v_permlane16_swap_b32 v249, v251
v_max_f32 v249, v249, v251
v_mov_b32 v251, v249
s_nop 1
v_permlane32_swap_b32 v249, v251
v_max_f32 v249, v249, v251
v_max_f32 v249, 0, v249
v_sub_f32 v250, 0, v249
EX v250,v250
v_sub_f32 v140, v140, v249
v_sub_f32 v141, v141, v249
v_sub_f32 v142, v142, v249
v_sub_f32 v143, v143, v249
v_sub_f32 v88, v88, v249
v_sub_f32 v24, v24, v249
v_sub_f32 v89, v89, v249
v_sub_f32 v25, v25, v249
v_sub_f32 v90, v90, v249
v_sub_f32 v26, v26, v249
v_sub_f32 v91, v91, v249
v_sub_f32 v27, v27, v249
v_sub_f32 v92, v92, v249
v_sub_f32 v28, v28, v249
v_sub_f32 v93, v93, v249
v_sub_f32 v29, v29, v249
v_sub_f32 v94, v94, v249
v_sub_f32 v30, v30, v249
v_sub_f32 v95, v95, v249
v_sub_f32 v31, v31, v249
v_sub_f32 v120, v120, v249
v_sub_f32 v56, v56, v249
v_sub_f32 v121, v121, v249
v_sub_f32 v57, v57, v249
v_sub_f32 v122, v122, v249
v_sub_f32 v58, v58, v249
v_sub_f32 v123, v123, v249
v_sub_f32 v59, v59, v249
v_sub_f32 v124, v124, v249
v_sub_f32 v60, v60, v249
v_sub_f32 v125, v125, v249
v_sub_f32 v61, v61, v249
v_sub_f32 v126, v126, v249
v_sub_f32 v62, v62, v249
v_sub_f32 v127, v127, v249
v_sub_f32 v63, v63, v249
s_lshl_b64 exec, 0xffff, 48
v_mul_f32 v236, v236, v250
s_mov_b64 exec, -1
AR v254,a24
v_mul_f32 v254, v254, v250
AW a24,v254
AR v251,a25
v_mul_f32 v251, v251, v250
AW a25,v251
AR v254,a26
v_mul_f32 v254, v254, v250
AW a26,v254
AR v251,a27
v_mul_f32 v251, v251, v250
AW a27,v251
AR v254,a28
v_mul_f32 v254, v254, v250
AW a28,v254
AR v251,a29
v_mul_f32 v251, v251, v250
AW a29,v251
AR v254,a30
v_mul_f32 v254, v254, v250
AW a30,v254
AR v251,a31
v_mul_f32 v251, v251, v250
AW a31,v251
AR v254,a56
v_mul_f32 v254, v254, v250
AW a56,v254
AR v251,a57
v_mul_f32 v251, v251, v250
AW a57,v251
AR v254,a58
v_mul_f32 v254, v254, v250
AW a58,v254
AR v251,a59
v_mul_f32 v251, v251, v250
AW a59,v251
AR v254,a60
v_mul_f32 v254, v254, v250
AW a60,v254
AR v251,a61
v_mul_f32 v251, v251, v250
AW a61,v251
AR v254,a62
v_mul_f32 v254, v254, v250
AW a62,v254
AR v251,a63
v_mul_f32 v251, v251, v250
AW a63,v251
AR v254,a88
v_mul_f32 v254, v254, v250
AW a88,v254
AR v251,a89
v_mul_f32 v251, v251, v250
AW a89,v251
AR v254,a90
v_mul_f32 v254, v254, v250
AW a90,v254
AR v251,a91
v_mul_f32 v251, v251, v250
AW a91,v251
AR v254,a92
v_mul_f32 v254, v254, v250
AW a92,v254
AR v251,a93
v_mul_f32 v251, v251, v250
AW a93,v251
AR v254,a94
v_mul_f32 v254, v254, v250
AW a94,v254
AR v251,a95
v_mul_f32 v251, v251, v250
AW a95,v251
AR v254,a120
v_mul_f32 v254, v254, v250
AW a120,v254
AR v251,a121
v_mul_f32 v251, v251, v250
AW a121,v251
AR v254,a122
v_mul_f32 v254, v254, v250
AW a122,v254
AR v251,a123
v_mul_f32 v251, v251, v250
AW a123,v251
AR v254,a124
v_mul_f32 v254, v254, v250
AW a124,v254
AR v251,a125
v_mul_f32 v251, v251, v250
AW a125,v251
AR v254,a126
v_mul_f32 v254, v254, v250
AW a126,v254
AR v251,a127
v_mul_f32 v251, v251, v250
AW a127,v251
EX v16,v16
EX v17,v17
EX v18,v18
EX v19,v19
EX v20,v20
EX v21,v21
EX v22,v22
EX v23,v23
EX v24,v24
EX v25,v25
EX v26,v26
EX v27,v27
EX v28,v28
EX v29,v29
EX v30,v30
EX v31,v31
EX v48,v48
EX v49,v49
EX v50,v50
EX v51,v51
EX v52,v52
EX v53,v53
EX v54,v54
EX v55,v55
EX v56,v56
EX v57,v57
EX v58,v58
EX v59,v59
EX v60,v60
EX v61,v61
EX v62,v62
EX v63,v63
CV v16,v16,v17
CV v17,v18,v19
CV v18,v20,v21
CV v19,v22,v23
CV v24,v24,v25
CV v25,v26,v27
CV v26,v28,v29
CV v27,v30,v31
CV v20,v48,v49
CV v21,v50,v51
CV v22,v52,v53
CV v23,v54,v55
CV v28,v56,v57
CV v29,v58,v59
CV v30,v60,v61
CV v31,v62,v63
v_smfmac_f32_16x16x64_bf16 v[236:239], v[152:155], v[16:23], v234
s_nop 0
v_smfmac_f32_16x16x64_bf16 v[236:239], v[156:159], v[24:31], v234
s_nop 9
v_add_f32 v246, v236, v237
s_cmp_eq_u32 s73, 32
s_cbranch_scc1 LBACK32_%=
s_cmp_eq_u32 s73, 34
s_cbranch_scc1 LBACK34_%=
s_endpgm
LSITE33_%=:
s_mov_b32 s73, 33
s_mov_b32 s74, 16384
s_mov_b32 s75, 32768
s_branch LRARE122_%=
LRARE122_%=:
s_nop 15
s_waitcnt vmcnt(0) lgkmcnt(0)
v_add_u32 v249, s74, v224
RD a[192:195],v249 offset:0
RD a[208:211],v249 offset:4096
RD a[224:227],v249 offset:8192
RD a[240:243],v249 offset:12288
v_add_u32 v250, s74, v225
RD a[196:199],v250 offset:0
RD a[212:215],v250 offset:4096
RD a[228:231],v250 offset:8192
RD a[244:247],v250 offset:12288
v_add_u32 v251, s74, v226
RD a[200:203],v251 offset:0
RD a[216:219],v251 offset:4096
)ASM"
R"ASM(
RD a[232:235],v251 offset:8192
RD a[248:251],v251 offset:12288
v_add_u32 v254, s74, v227
RD a[204:207],v254 offset:0
RD a[220:223],v254 offset:4096
RD a[236:239],v254 offset:8192
RD a[252:255],v254 offset:12288
s_waitcnt lgkmcnt(0)
MM v[80:83],a[192:195],v[168:171],v[136:139]
MM v[80:83],a[196:199],v[184:187],v[80:83]
MM v[80:83],a[200:203],v[200:203],v[80:83]
MM v[80:83],a[204:207],v[216:219],v[80:83]
MM v[84:87],a[220:223],v[216:219],v[136:139]
MM v[84:87],a[216:219],v[200:203],v[84:87]
MM v[84:87],a[212:215],v[184:187],v[84:87]
MM v[84:87],a[208:211],v[168:171],v[84:87]
MM v[92:95],a[208:211],v[172:175],v[140:143]
MM v[92:95],a[212:215],v[188:191],v[92:95]
MM v[92:95],a[216:219],v[204:207],v[92:95]
MM v[92:95],a[220:223],v[220:223],v[92:95]
MM v[88:91],a[204:207],v[220:223],v[140:143]
MM v[88:91],a[200:203],v[204:207],v[88:91]
MM v[88:91],a[196:199],v[188:191],v[88:91]
MM v[88:91],a[192:195],v[172:175],v[88:91]
MM v[112:115],a[224:227],v[168:171],v[136:139]
MM v[112:115],a[228:231],v[184:187],v[112:115]
MM v[112:115],a[232:235],v[200:203],v[112:115]
MM v[112:115],a[236:239],v[216:219],v[112:115]
MM v[116:119],a[252:255],v[216:219],v[136:139]
MM v[116:119],a[248:251],v[200:203],v[116:119]
MM v[116:119],a[244:247],v[184:187],v[116:119]
MM v[116:119],a[240:243],v[168:171],v[116:119]
MM v[124:127],a[240:243],v[172:175],v[140:143]
MM v[124:127],a[244:247],v[188:191],v[124:127]
MM v[124:127],a[248:251],v[204:207],v[124:127]
MM v[124:127],a[252:255],v[220:223],v[124:127]
MM v[120:123],a[236:239],v[220:223],v[140:143]
MM v[120:123],a[232:235],v[204:207],v[120:123]
MM v[120:123],a[228:231],v[188:191],v[120:123]
MM v[120:123],a[224:227],v[172:175],v[120:123]
s_nop 15
v_add_u32 v249, s75, v224
RD a[192:195],v249 offset:0
RD a[208:211],v249 offset:4096
RD a[224:227],v249 offset:8192
RD a[240:243],v249 offset:12288
v_add_u32 v250, s75, v225
RD a[196:199],v250 offset:0
RD a[212:215],v250 offset:4096
RD a[228:231],v250 offset:8192
RD a[244:247],v250 offset:12288
v_add_u32 v251, s75, v226
RD a[200:203],v251 offset:0
RD a[216:219],v251 offset:4096
RD a[232:235],v251 offset:8192
RD a[248:251],v251 offset:12288
v_add_u32 v254, s75, v227
RD a[204:207],v254 offset:0
RD a[220:223],v254 offset:4096
RD a[236:239],v254 offset:8192
RD a[252:255],v254 offset:12288
s_waitcnt lgkmcnt(0)
v_mov_b32 v236, v246
v_mov_b32 v237, 0
v_max3_f32 v249, v80, v81, v82
v_max3_f32 v251, v112, v113, v114
v_max3_f32 v249, v249, v83, v84
v_max3_f32 v251, v251, v115, v116
v_max3_f32 v249, v249, v85, v86
v_max3_f32 v251, v251, v117, v118
v_max3_f32 v249, v249, v87, v119
v_max_f32 v249, v249, v251
v_log_f32 v250, v246
s_lshl_b64 exec, 0xffff, 32
v_max_f32 v249, v249, v250
s_mov_b64 exec, -1
v_mov_b32 v251, v249
s_nop 1
v_permlane16_swap_b32 v249, v251
v_max_f32 v249, v249, v251
v_mov_b32 v251, v249
s_nop 1
v_permlane32_swap_b32 v249, v251
v_max_f32 v249, v249, v251
v_max_f32 v249, 0, v249
v_sub_f32 v250, 0, v249
EX v250,v250
v_sub_f32 v136, v136, v249
v_sub_f32 v137, v137, v249
v_sub_f32 v138, v138, v249
v_sub_f32 v139, v139, v249
v_sub_f32 v16, v16, v249
v_sub_f32 v80, v80, v249
v_sub_f32 v17, v17, v249
v_sub_f32 v81, v81, v249
v_sub_f32 v18, v18, v249
v_sub_f32 v82, v82, v249
v_sub_f32 v19, v19, v249
v_sub_f32 v83, v83, v249
v_sub_f32 v20, v20, v249
v_sub_f32 v84, v84, v249
v_sub_f32 v21, v21, v249
v_sub_f32 v85, v85, v249
v_sub_f32 v22, v22, v249
v_sub_f32 v86, v86, v249
v_sub_f32 v23, v23, v249
v_sub_f32 v87, v87, v249
v_sub_f32 v48, v48, v249
v_sub_f32 v112, v112, v249
v_sub_f32 v49, v49, v249
v_sub_f32 v113, v113, v249
v_sub_f32 v50, v50, v249
v_sub_f32 v114, v114, v249
v_sub_f32 v51, v51, v249
v_sub_f32 v115, v115, v249
v_sub_f32 v52, v52, v249
v_sub_f32 v116, v116, v249
v_sub_f32 v53, v53, v249
v_sub_f32 v117, v117, v249
v_sub_f32 v54, v54, v249
v_sub_f32 v118, v118, v249
v_sub_f32 v55, v55, v249
v_sub_f32 v119, v119, v249
s_lshl_b64 exec, 0xffff, 32
v_mul_f32 v236, v236, v250
s_mov_b64 exec, -1
AR v254,a16
v_mul_f32 v254, v254, v250
AW a16,v254
AR v251,a17
v_mul_f32 v251, v251, v250
AW a17,v251
AR v254,a18
v_mul_f32 v254, v254, v250
AW a18,v254
AR v251,a19
v_mul_f32 v251, v251, v250
AW a19,v251
AR v254,a20
v_mul_f32 v254, v254, v250
AW a20,v254
AR v251,a21
v_mul_f32 v251, v251, v250
AW a21,v251
AR v254,a22
v_mul_f32 v254, v254, v250
AW a22,v254
AR v251,a23
v_mul_f32 v251, v251, v250
AW a23,v251
AR v254,a48
v_mul_f32 v254, v254, v250
AW a48,v254
AR v251,a49
v_mul_f32 v251, v251, v250
AW a49,v251
AR v254,a50
v_mul_f32 v254, v254, v250
AW a50,v254
AR v251,a51
v_mul_f32 v251, v251, v250
AW a51,v251
AR v254,a52
v_mul_f32 v254, v254, v250
AW a52,v254
AR v251,a53
v_mul_f32 v251, v251, v250
AW a53,v251
AR v254,a54
v_mul_f32 v254, v254, v250
AW a54,v254
AR v251,a55
v_mul_f32 v251, v251, v250
AW a55,v251
AR v254,a80
v_mul_f32 v254, v254, v250
AW a80,v254
AR v251,a81
v_mul_f32 v251, v251, v250
AW a81,v251
AR v254,a82
v_mul_f32 v254, v254, v250
AW a82,v254
AR v251,a83
v_mul_f32 v251, v251, v250
AW a83,v251
AR v254,a84
v_mul_f32 v254, v254, v250
AW a84,v254
AR v251,a85
v_mul_f32 v251, v251, v250
AW a85,v251
AR v254,a86
v_mul_f32 v254, v254, v250
AW a86,v254
AR v251,a87
v_mul_f32 v251, v251, v250
AW a87,v251
AR v254,a112
v_mul_f32 v254, v254, v250
AW a112,v254
AR v251,a113
v_mul_f32 v251, v251, v250
AW a113,v251
AR v254,a114
v_mul_f32 v254, v254, v250
AW a114,v254
AR v251,a115
v_mul_f32 v251, v251, v250
AW a115,v251
AR v254,a116
v_mul_f32 v254, v254, v250
AW a116,v254
AR v251,a117
v_mul_f32 v251, v251, v250
AW a117,v251
AR v254,a118
v_mul_f32 v254, v254, v250
AW a118,v254
AR v251,a119
v_mul_f32 v251, v251, v250
AW a119,v251
v_max3_f32 v249, v88, v89, v90
v_max3_f32 v251, v120, v121, v122
v_max3_f32 v249, v249, v91, v92
v_max3_f32 v251, v251, v123, v124
v_max3_f32 v249, v249, v93, v94
v_max3_f32 v251, v251, v125, v126
v_max3_f32 v249, v249, v95, v127
v_max_f32 v249, v249, v251
v_log_f32 v250, v246
s_lshl_b64 exec, 0xffff, 48
v_max_f32 v249, v249, v250
s_mov_b64 exec, -1
v_mov_b32 v251, v249
s_nop 1
v_permlane16_swap_b32 v249, v251
v_max_f32 v249, v249, v251
v_mov_b32 v251, v249
s_nop 1
v_permlane32_swap_b32 v249, v251
v_max_f32 v249, v249, v251
v_max_f32 v249, 0, v249
v_sub_f32 v250, 0, v249
EX v250,v250
v_sub_f32 v140, v140, v249
v_sub_f32 v141, v141, v249
v_sub_f32 v142, v142, v249
v_sub_f32 v143, v143, v249
v_sub_f32 v24, v24, v249
v_sub_f32 v88, v88, v249
v_sub_f32 v25, v25, v249
v_sub_f32 v89, v89, v249
v_sub_f32 v26, v26, v249
v_sub_f32 v90, v90, v249
v_sub_f32 v27, v27, v249
v_sub_f32 v91, v91, v249
v_sub_f32 v28, v28, v249
v_sub_f32 v92, v92, v249
v_sub_f32 v29, v29, v249
v_sub_f32 v93, v93, v249
v_sub_f32 v30, v30, v249
v_sub_f32 v94, v94, v249
v_sub_f32 v31, v31, v249
v_sub_f32 v95, v95, v249
v_sub_f32 v56, v56, v249
v_sub_f32 v120, v120, v249
v_sub_f32 v57, v57, v249
v_sub_f32 v121, v121, v249
v_sub_f32 v58, v58, v249
v_sub_f32 v122, v122, v249
v_sub_f32 v59, v59, v249
v_sub_f32 v123, v123, v249
v_sub_f32 v60, v60, v249
v_sub_f32 v124, v124, v249
v_sub_f32 v61, v61, v249
v_sub_f32 v125, v125, v249
v_sub_f32 v62, v62, v249
v_sub_f32 v126, v126, v249
v_sub_f32 v63, v63, v249
v_sub_f32 v127, v127, v249
s_lshl_b64 exec, 0xffff, 48
v_mul_f32 v236, v236, v250
s_mov_b64 exec, -1
AR v254,a24
v_mul_f32 v254, v254, v250
AW a24,v254
AR v251,a25
v_mul_f32 v251, v251, v250
AW a25,v251
AR v254,a26
v_mul_f32 v254, v254, v250
AW a26,v254
AR v251,a27
v_mul_f32 v251, v251, v250
AW a27,v251
AR v254,a28
v_mul_f32 v254, v254, v250
AW a28,v254
AR v251,a29
v_mul_f32 v251, v251, v250
AW a29,v251
AR v254,a30
v_mul_f32 v254, v254, v250
AW a30,v254
AR v251,a31
v_mul_f32 v251, v251, v250
AW a31,v251
AR v254,a56
v_mul_f32 v254, v254, v250
AW a56,v254
AR v251,a57
v_mul_f32 v251, v251, v250
AW a57,v251
AR v254,a58
v_mul_f32 v254, v254, v250
AW a58,v254
AR v251,a59
v_mul_f32 v251, v251, v250
AW a59,v251
AR v254,a60
v_mul_f32 v254, v254, v250
AW a60,v254
AR v251,a61
v_mul_f32 v251, v251, v250
AW a61,v251
AR v254,a62
v_mul_f32 v254, v254, v250
AW a62,v254
AR v251,a63
v_mul_f32 v251, v251, v250
AW a63,v251
AR v254,a88
v_mul_f32 v254, v254, v250
AW a88,v254
AR v251,a89
v_mul_f32 v251, v251, v250
AW a89,v251
AR v254,a90
v_mul_f32 v254, v254, v250
AW a90,v254
AR v251,a91
v_mul_f32 v251, v251, v250
AW a91,v251
AR v254,a92
v_mul_f32 v254, v254, v250
AW a92,v254
AR v251,a93
v_mul_f32 v251, v251, v250
AW a93,v251
AR v254,a94
v_mul_f32 v254, v254, v250
AW a94,v254
AR v251,a95
v_mul_f32 v251, v251, v250
AW a95,v251
AR v254,a120
v_mul_f32 v254, v254, v250
AW a120,v254
AR v251,a121
v_mul_f32 v251, v251, v250
AW a121,v251
AR v254,a122
v_mul_f32 v254, v254, v250
AW a122,v254
AR v251,a123
v_mul_f32 v251, v251, v250
AW a123,v251
AR v254,a124
v_mul_f32 v254, v254, v250
AW a124,v254
AR v251,a125
v_mul_f32 v251, v251, v250
AW a125,v251
AR v254,a126
v_mul_f32 v254, v254, v250
AW a126,v254
AR v251,a127
v_mul_f32 v251, v251, v250
AW a127,v251
EX v80,v80
EX v81,v81
EX v82,v82
EX v83,v83
EX v84,v84
EX v85,v85
EX v86,v86
EX v87,v87
EX v88,v88
EX v89,v89
EX v90,v90
EX v91,v91
EX v92,v92
EX v93,v93
EX v94,v94
EX v95,v95
EX v112,v112
EX v113,v113
EX v114,v114
EX v115,v115
EX v116,v116
EX v117,v117
EX v118,v118
EX v119,v119
EX v120,v120
EX v121,v121
EX v122,v122
EX v123,v123
EX v124,v124
EX v125,v125
EX v126,v126
EX v127,v127
CV v80,v80,v81
CV v81,v82,v83
CV v82,v84,v85
CV v83,v86,v87
CV v88,v88,v89
CV v89,v90,v91
CV v90,v92,v93
CV v91,v94,v95
CV v84,v112,v113
CV v85,v114,v115
CV v86,v116,v117
CV v87,v118,v119
CV v92,v120,v121
CV v93,v122,v123
CV v94,v124,v125
CV v95,v126,v127
v_smfmac_f32_16x16x64_bf16 v[236:239], v[152:155], v[80:87], v234
s_nop 0
v_smfmac_f32_16x16x64_bf16 v[236:239], v[156:159], v[88:95], v234
s_nop 9
v_add_f32 v247, v236, v237
s_cmp_eq_u32 s73, 33
s_cbranch_scc1 LBACK33_%=
s_endpgm
LEND_%=:
)ASM"

    :: [ka] "s"(__builtin_amdgcn_kernarg_segment_ptr()), [slds] "s"(slds), [swave] "s"(w), [sb] "s"(blockIdx.x)
    : "memory", "vcc", "scc", CLOB_V, CLOB_A, CLOB_S);
}

extern "C" void kernel_launch(void* const* d_in, const int* in_sizes, int n_in, void* d_out, int out_size, void* d_ws, size_t ws_size, hipStream_t stream) {
  hipFuncSetAttribute((const void*)ka_fused, hipFuncAttributeMaxDynamicSharedMemorySize, 163840);
  ka_fused<<<256, 256, 163840, stream>>>((const unsigned short*)d_in[0], (const unsigned short*)d_in[1], (const unsigned short*)d_in[2], (const unsigned short*)d_in[3],
                                         (const unsigned short*)d_in[4], (const unsigned short*)d_in[6], (const unsigned short*)d_in[7], (const unsigned short*)d_in[8],
                                         (const unsigned short*)d_in[9], (char*)d_ws, (unsigned short*)d_out
                                         );
}
```

```cpp
#include <hip/hip_runtime.h>
#include <stdint.h>

using f32x4  = __attribute__((ext_vector_type(4))) float;
using u32x4  = __attribute__((ext_vector_type(4))) unsigned;
using u32x2  = __attribute__((ext_vector_type(2))) unsigned;

constexpr int SEQ = 2048, DM = 640, NH = 32, DH = 128, HD = NH * DH, FF = 1728, TOPC = 8;

constexpr unsigned WS_FO  = 0;
constexpr unsigned WS_EP  = 32768;
constexpr unsigned WS_G1  = 36864;
constexpr unsigned WS_G2  = 53248;
constexpr unsigned WS_FH  = 69632;
constexpr unsigned WS_FY  = 73728;
constexpr unsigned WS_FG  = 77824;
constexpr unsigned WS_FR  = 81920;
constexpr unsigned WS_IX  = 86016;
constexpr unsigned WS_XN  = 131072;
constexpr unsigned WS_OF  = WS_XN + SEQ * DM * 2;
constexpr unsigned WS_H   = WS_OF + (18u << 20);
constexpr unsigned WS_Y   = WS_H + 32768;
constexpr unsigned WS_P   = WS_Y + 1048576;
constexpr unsigned WS_SPAN = 64u << 20;
constexpr int DN_FIRST = 76, YP = 18, SPIN = 30000;

#define CLOB_V "v0","v1","v2","v3","v4","v5","v6","v7","v8","v9","v10","v11","v12","v13","v14","v15","v16","v17","v18","v19","v20","v21","v22","v23","v24","v25","v26","v27","v28","v29","v30","v31","v32","v33","v34","v35","v36","v37","v38","v39","v40","v41","v42","v43","v44","v45","v46","v47","v48","v49","v50","v51","v52","v53","v54","v55","v56","v57","v58","v59","v60","v61","v62","v63","v64","v65","v66","v67","v68","v69","v70","v71","v72","v73","v74","v75","v76","v77","v78","v79","v80","v81","v82","v83","v84","v85","v86","v87","v88","v89","v90","v91","v92","v93","v94","v95","v96","v97","v98","v99","v100","v101","v102","v103","v104","v105","v106","v107","v108","v109","v110","v111","v112","v113","v114","v115","v116","v117","v118","v119","v120","v121","v122","v123","v124","v125","v126","v127","v128","v129","v130","v131","v132","v133","v134","v135","v136","v137","v138","v139","v140","v141","v142","v143","v144","v145","v146","v147","v148","v149","v150","v151","v152","v153","v154","v155","v156","v157","v158","v159","v160","v161","v162","v163","v164","v165","v166","v167","v168","v169","v170","v171","v172","v173","v174","v175","v176","v177","v178","v179","v180","v181","v182","v183","v184","v185","v186","v187","v188","v189","v190","v191","v192","v193","v194","v195","v196","v197","v198","v199","v200","v201","v202","v203","v204","v205","v206","v207","v208","v209","v210","v211","v212","v213","v214","v215","v216","v217","v218","v219","v220","v221","v222","v223","v224","v225","v226","v227","v228","v229","v230","v231","v232","v233","v234","v235","v236","v237","v238","v239","v240","v241","v242","v243","v244","v245","v246","v247","v248","v249","v250","v251","v252","v253","v254","v255"
#define CLOB_A "a0","a1","a2","a3","a4","a5","a6","a7","a8","a9","a10","a11","a12","a13","a14","a15","a16","a17","a18","a19","a20","a21","a22","a23","a24","a25","a26","a27","a28","a29","a30","a31","a32","a33","a34","a35","a36","a37","a38","a39","a40","a41","a42","a43","a44","a45","a46","a47","a48","a49","a50","a51","a52","a53","a54","a55","a56","a57","a58","a59","a60","a61","a62","a63","a64","a65","a66","a67","a68","a69","a70","a71","a72","a73","a74","a75","a76","a77","a78","a79","a80","a81","a82","a83","a84","a85","a86","a87","a88","a89","a90","a91","a92","a93","a94","a95","a96","a97","a98","a99","a100","a101","a102","a103","a104","a105","a106","a107","a108","a109","a110","a111","a112","a113","a114","a115","a116","a117","a118","a119","a120","a121","a122","a123","a124","a125","a126","a127","a128","a129","a130","a131","a132","a133","a134","a135","a136","a137","a138","a139","a140","a141","a142","a143","a144","a145","a146","a147","a148","a149","a150","a151","a152","a153","a154","a155","a156","a157","a158","a159","a160","a161","a162","a163","a164","a165","a166","a167","a168","a169","a170","a171","a172","a173","a174","a175","a176","a177","a178","a179","a180","a181","a182","a183","a184","a185","a186","a187","a188","a189","a190","a191","a192","a193","a194","a195","a196","a197","a198","a199","a200","a201","a202","a203","a204","a205","a206","a207","a208","a209","a210","a211","a212","a213","a214","a215","a216","a217","a218","a219","a220","a221","a222","a223","a224","a225","a226","a227","a228","a229","a230","a231","a232","a233","a234","a235","a236","a237","a238","a239","a240","a241","a242","a243","a244","a245","a246","a247","a248","a249","a250","a251","a252","a253","a254","a255"
#define CLOB_S "s8","s9","s10","s11","s12","s13","s14","s15","s16","s17","s18","s19","s20","s21","s22","s23","s24","s25","s26","s27","s28","s29","s30","s31","s32","s33","s34","s35","s36","s37","s38","s39","s40","s41","s42","s43","s44","s45","s46","s47","s48","s49","s50","s51","s52","s53","s54","s55","s56","s57","s58","s59","s60","s61","s62","s63","s64","s65","s66","s67","s68","s69","s70","s71","s72","s73","s74","s75","s76","s77","s78","s79","s80","s81","s82","s83","s84","s85","s86","s87","s88","s89","s90","s91","s92","s93","s94","s95","s96","s97","s98","s99"
__global__ __launch_bounds__(256, 1) __attribute__((aligned(4096))) void ka_fused(const unsigned short* __restrict__ x, const unsigned short* __restrict__ Kc, const unsigned short* __restrict__ Vc,
                                                   const unsigned short* __restrict__ wq, const unsigned short* __restrict__ wo,
                                                   const unsigned short* __restrict__ wr, const unsigned short* __restrict__ wg, const unsigned short* __restrict__ wu,
                                                   const unsigned short* __restrict__ wd,
                                                   char* __restrict__ ws, unsigned short* __restrict__ out
                                                   ) {
  extern __shared__ __attribute__((aligned(16))) char lds[];
  const unsigned w = __builtin_amdgcn_readfirstlane(threadIdx.x >> 6);
  const unsigned slds = __builtin_amdgcn_readfirstlane((unsigned)(size_t)lds);
  asm volatile(
R"ASM(
.macro MM a, b, c, d:vararg
v_mfma_f32_16x16x32_bf16 \a, \b, \c, \d
.endm
.macro TR a, b:vararg
ds_read_b64_tr_b16 \a, \b
.endm
.macro CV a, b, c:vararg
v_cvt_pk_bf16_f32 \a, \b, \c
.endm
.macro AR a, b:vararg
v_accvgpr_read_b32 \a, \b
.endm
.macro AW a, b:vararg
v_accvgpr_write_b32 \a, \b
.endm
.macro EX a, b:vararg
v_exp_f32 \a, \b
.endm
.macro RD a, b:vararg
ds_read_b128 \a, \b
.endm
s_branch LPG0_%=
.p2align 6
LPG0_%=:
s_load_dwordx2 s[16:17], %[ka], 24
s_load_dwordx2 s[32:33], %[ka], 0
s_load_dwordx2 s[20:21], %[ka], 72
s_load_dwordx2 s[8:9], %[ka], 8
s_load_dwordx2 s[12:13], %[ka], 16
s_load_dwordx2 s[24:25], %[ka], 32
s_load_dwordx2 s[44:45], %[ka], 40
s_load_dwordx2 s[28:29], %[ka], 64
s_load_dwordx2 s[40:41], %[ka], 80
s_lshl_b32 s77, %[swave], 2
s_and_b32 s77, s77, 8
s_add_u32 s77, s77, 48
s_load_dwordx2 s[36:37], %[ka], s77
v_mbcnt_lo_u32_b32 v127, -1, 0
v_mbcnt_hi_u32_b32 v127, -1, v127
v_lshlrev_b32 v248, 4, v127
s_lshr_b32 s77, %[sb], 3
s_lshl_b32 s77, s77, 2
s_add_u32 s77, s77, %[swave]
s_lshl_b32 s77, s77, 10
v_add_u32 v118, s77, v248
s_mov_b32 s77, LKEND_ka-LPC_%=-16
v_min_u32 v118, s77, v118
s_getpc_b64 s[98:99]
LPC_%=:
global_load_dwordx4 a[252:255], v118, s[98:99]
s_and_b32 s77, %[sb], 7
s_lshl_b32 s77, s77, 2
s_bfe_u32 s84, %[sb], 131075
s_add_u32 s49, s77, s84
s_lshr_b32 s50, %[sb], 5
s_lshr_b32 s76, %[sb], 8
s_lshl_b32 s76, s76, 7
s_and_b32 s77, %[sb], 127
s_or_b32 s76, s76, s77
s_lshl_b32 s76, s76, 2
s_add_u32 s76, s76, %[swave]
s_lshl_b32 s76, s76, 1
s_bitcmp1_b32 %[sb], 7
s_cselect_b64 s[52:53], -1, 0
s_and_b32 s54, s52, 1
s_mov_b32 s55, s54
s_and_b32 s54, s52, 0x10001
s_mov_b32 s55, s54
v_lshrrev_b32 v180, 5, v127
s_mul_i32 s82, s76, 2560
v_and_b32 v182, 240, v248
v_lshrrev_b32 v181, 4, v127
v_mul_u32_u24 v168, 1280, v181
v_add_u32 v168, v168, v182
s_lshl_b32 s77, %[swave], 12
s_add_u32 s65, s77, %[slds]
s_lshl_b32 s89, %[swave], 17
v_bfe_u32 v210, v127, 2, 3
v_and_b32 v212, 3, v127
v_lshlrev_b32 v213, 4, v212
v_lshl_add_u32 v213, v180, 6, v213
v_lshl_add_u32 v253, v210, 13, v213
v_add_u32 v254, 128, v253
s_mul_i32 s96, %[swave], 4112
s_add_u32 s96, s96, %[slds]
s_lshl_b32 s77, s50, 2
s_add_u32 s77, s77, %[swave]
s_mul_i32 s68, s77, 81920
s_mul_i32 s83, s77, 560128
s_add_u32 s83, s83, 2752512
s_lshl_b32 s93, s77, 9
s_lshl_b32 s94, %[swave], 13
s_add_u32 s94, s94, %[slds]
v_lshrrev_b32 v211, 3, v127
v_mul_u32_u24 v220, 1280, v211
v_lshrrev_b32 v212, 4, v127
v_and_b32 v211, 7, v127
v_xor_b32 v211, v211, v212
v_lshl_add_u32 v220, v211, 4, v220
v_xor_b32 v221, 64, v220
s_bitcmp1_b32 s84, 0
s_mov_b32 s77, 0x77552200
s_cselect_b32 s77, 0x77552200, s77
s_lshl_b32 s91, s50, 2
s_lshr_b32 s77, s77, s91
s_and_b32 s77, s77, 15
s_lshl_b32 s86, s77, 7
s_lshl_b32 s85, s77, 19
s_lshl_b32 s90, s49, 8
s_waitcnt lgkmcnt(0)
s_add_u32 s16, s16, s90
s_addc_u32 s17, s17, 0
s_sub_u32 s18, 5242880, s90
s_mov_b32 s19, 131072
s_mov_b32 s34, 2621440
s_mov_b32 s35, 131072
s_mov_b32 s22, 67108864
s_mov_b32 s23, 131072
s_bitcmp1_b32 %[sb], 7
s_cbranch_scc1 LCA_%=
s_add_u32 s87, s89, s85
s_add_u32 s85, s85, 524288
s_cmp_ge_u32 s85, 5242880
s_cselect_b32 s85, 0, s85
s_add_u32 m0, s96, 65536
s_nop 0
buffer_load_dwordx4 v253, s[16:19], s87 offen offset:0 lds
s_add_u32 m0, s96, 66560
s_nop 0
buffer_load_dwordx4 v254, s[16:19], s87 offen offset:0 lds
s_add_u32 s87, s87, 65536
s_add_u32 m0, s96, 67592
s_nop 0
buffer_load_dwordx4 v253, s[16:19], s87 offen offset:0 lds
s_add_u32 m0, s96, 68616
s_nop 0
buffer_load_dwordx4 v254, s[16:19], s87 offen offset:0 lds
s_add_u32 s88, s68, s86
s_add_u32 s86, s86, 128
s_cmp_ge_u32 s86, 1280
s_cselect_b32 s86, 0, s86
s_add_u32 m0, s94, 0
s_add_u32 s95, s88, 0
buffer_load_dwordx4 v220, s[32:35], s95 offen offset:0 lds
s_add_u32 s95, s88, 9216
buffer_load_dwordx4 v221, s[32:35], s95 offen offset:1024 lds
s_add_u32 s95, s88, 18432
buffer_load_dwordx4 v220, s[32:35], s95 offen offset:2048 lds
s_add_u32 s95, s88, 27648
buffer_load_dwordx4 v221, s[32:35], s95 offen offset:3072 lds
s_add_u32 m0, s94, 4096
s_add_u32 s95, s88, 40960
buffer_load_dwordx4 v220, s[32:35], s95 offen offset:0 lds
s_add_u32 s95, s88, 50176
buffer_load_dwordx4 v221, s[32:35], s95 offen offset:1024 lds
s_add_u32 s95, s88, 59392
buffer_load_dwordx4 v220, s[32:35], s95 offen offset:2048 lds
s_add_u32 s95, s88, 68608
buffer_load_dwordx4 v221, s[32:35], s95 offen offset:3072 lds
s_add_u32 s87, s89, s85
s_add_u32 s85, s85, 524288
s_cmp_ge_u32 s85, 5242880
s_cselect_b32 s85, 0, s85
s_add_u32 m0, s96, 81984
s_nop 0
buffer_load_dwordx4 v253, s[16:19], s87 offen offset:0 lds
s_add_u32 m0, s96, 83008
s_nop 0
buffer_load_dwordx4 v254, s[16:19], s87 offen offset:0 lds
s_add_u32 s87, s87, 65536
s_add_u32 m0, s96, 84040
s_nop 0
buffer_load_dwordx4 v253, s[16:19], s87 offen offset:0 lds
s_add_u32 m0, s96, 85064
s_nop 0
buffer_load_dwordx4 v254, s[16:19], s87 offen offset:0 lds
s_branch LCT_%=
LCA_%=:
buffer_load_dwordx4 v[128:131], v168, s[32:35], s82 offen offset:0
buffer_load_dwordx4 v[132:135], v168, s[32:35], s82 offen offset:256
buffer_load_dwordx4 v[136:139], v168, s[32:35], s82 offen offset:512
buffer_load_dwordx4 v[140:143], v168, s[32:35], s82 offen offset:768
buffer_load_dwordx4 v[144:147], v168, s[32:35], s82 offen offset:1024
global_load_dwordx4 v[100:103], v182, s[44:45] offset:0
global_load_dwordx4 v[104:107], v182, s[44:45] offset:256
global_load_dwordx4 v[108:111], v182, s[44:45] offset:512
global_load_dwordx4 v[112:115], v182, s[44:45] offset:768
global_load_dwordx4 v[116:119], v182, s[44:45] offset:1024
LCT_%=:
v_and_b32 v211, 15, v127
v_bfe_u32 v222, v127, 1, 3
v_xor_b32 v222, v222, v212
v_lshlrev_b32 v222, 4, v222
v_lshl_add_u32 v222, v211, 7, v222
s_lshl_b32 s77, %[sb], 11
s_and_b32 s77, s77, 2048
v_add_u32 v222, s77, v222
v_xor_b32 v223, 64, v222
v_xor_b32 v217, 2048, v222
v_add_u32 v217, s94, v217
v_xor_b32 v218, 2048, v223
v_add_u32 v218, s94, v218
v_add_u32 v222, s94, v222
v_add_u32 v223, s94, v223
v_and_b32 v214, 1, v212
v_lshlrev_b32 v215, 5, v214
v_xor_b32 v215, v215, v213
v_lshl_add_u32 v235, v210, 8, v215
v_add_u32 v235, 128, v235
v_and_b32 v234, 1, v127
v_mul_u32_u24 v234, 43690, v234
v_add_u32 v234, 17476, v234
v_bfe_u32 v215, v127, 2, 2
v_and_b32 v216, 3, v127
v_mul_u32_u24 v252, 2056, v212
v_lshl_add_u32 v252, v215, 6, v252
v_lshl_add_u32 v252, v216, 4, v252
s_xor_b32 s77, %[swave], 0
s_lshl_b32 s77, s77, 9
s_add_u32 s77, s77, %[slds]
s_add_u32 s77, s77, 65536
v_add_u32 v192, s77, v252
v_add_u32 v196, 49344, v192
s_xor_b32 s77, %[swave], 1
s_lshl_b32 s77, s77, 9
s_add_u32 s77, s77, %[slds]
s_add_u32 s77, s77, 65536
v_add_u32 v193, s77, v252
v_add_u32 v197, 49344, v193
s_xor_b32 s77, %[swave], 2
s_lshl_b32 s77, s77, 9
s_add_u32 s77, s77, %[slds]
s_add_u32 s77, s77, 65536
v_add_u32 v194, s77, v252
v_add_u32 v198, 49344, v194
s_xor_b32 s77, %[swave], 3
s_lshl_b32 s77, s77, 9
s_add_u32 s77, s77, %[slds]
s_add_u32 s77, s77, 65536
v_add_u32 v195, s77, v252
v_add_u32 v199, 49344, v195
v_mov_b32 v236, 0x3f803f80
v_mov_b32 v184, 0
v_mov_b32 v237, 0x3f803f80
v_mov_b32 v185, 0
v_mov_b32 v238, 0x3f803f80
v_mov_b32 v188, 0
v_mov_b32 v239, 0x3f803f80
v_mov_b32 v189, 0
s_getpc_b64 s[98:99]
LTA0_%=:
s_lshl_b32 s73, %[swave], 12
s_add_u32 s73, s73, LPG1_%=-LTA0_%=
s_add_u32 s98, s98, s73
s_addc_u32 s99, s99, 0
s_mov_b64 exec, 0
s_setpc_b64 s[98:99]
LTR0_%=:
s_mov_b64 exec, -1
s_nop 0
s_bitcmp1_b32 %[sb], 7
s_cbranch_scc1 LCB_%=
s_add_u32 s88, s68, s86
s_add_u32 s86, s86, 128
s_cmp_ge_u32 s86, 1280
s_cselect_b32 s86, 0, s86
s_add_u32 m0, s94, 32768
s_add_u32 s95, s88, 0
buffer_load_dwordx4 v220, s[32:35], s95 offen offset:0 lds
s_add_u32 s95, s88, 9216
buffer_load_dwordx4 v221, s[32:35], s95 offen offset:1024 lds
s_add_u32 s95, s88, 18432
buffer_load_dwordx4 v220, s[32:35], s95 offen offset:2048 lds
s_add_u32 s95, s88, 27648
buffer_load_dwordx4 v221, s[32:35], s95 offen offset:3072 lds
s_add_u32 m0, s94, 36864
s_add_u32 s95, s88, 40960
buffer_load_dwordx4 v220, s[32:35], s95 offen offset:0 lds
s_add_u32 s95, s88, 50176
buffer_load_dwordx4 v221, s[32:35], s95 offen offset:1024 lds
s_add_u32 s95, s88, 59392
buffer_load_dwordx4 v220, s[32:35], s95 offen offset:2048 lds
s_add_u32 s95, s88, 68608
buffer_load_dwordx4 v221, s[32:35], s95 offen offset:3072 lds
s_branch LCM_%=
LCB_%=:
s_waitcnt vmcnt(5)
s_mov_b32 s61, 0x3acccccd
s_add_u32 s87, s89, s85
s_add_u32 s85, s85, 524288
s_cmp_ge_u32 s85, 5242880
s_cselect_b32 s85, 0, s85
s_add_u32 m0, s96, 65536
s_nop 0
buffer_load_dwordx4 v253, s[16:19], s87 offen offset:0 lds
s_lshl_b32 s60, s76, 1
s_sub_u32 s60, 65535, s60
v_lshlrev_b32 v0, 16, v128
v_and_b32 v1, 0xffff0000, v128
v_lshlrev_b32 v2, 16, v129
v_and_b32 v3, 0xffff0000, v129
v_lshlrev_b32 v4, 16, v130
v_and_b32 v5, 0xffff0000, v130
s_add_u32 m0, s96, 66560
s_nop 0
buffer_load_dwordx4 v254, s[16:19], s87 offen offset:0 lds
v_lshlrev_b32 v6, 16, v131
v_and_b32 v7, 0xffff0000, v131
v_lshlrev_b32 v8, 16, v132
v_and_b32 v9, 0xffff0000, v132
v_lshlrev_b32 v10, 16, v133
v_and_b32 v11, 0xffff0000, v133
v_lshlrev_b32 v12, 16, v134
v_and_b32 v13, 0xffff0000, v134
s_add_u32 s87, s87, 65536
s_add_u32 m0, s96, 67592
s_nop 0
buffer_load_dwordx4 v253, s[16:19], s87 offen offset:0 lds
v_lshlrev_b32 v14, 16, v135
v_and_b32 v15, 0xffff0000, v135
v_lshlrev_b32 v16, 16, v136
v_and_b32 v17, 0xffff0000, v136
v_lshlrev_b32 v18, 16, v137
v_and_b32 v19, 0xffff0000, v137
v_lshlrev_b32 v20, 16, v138
v_and_b32 v21, 0xffff0000, v138
s_add_u32 m0, s96, 68616
s_nop 0
buffer_load_dwordx4 v254, s[16:19], s87 offen offset:0 lds
v_lshlrev_b32 v22, 16, v139
v_and_b32 v23, 0xffff0000, v139
v_lshlrev_b32 v24, 16, v140
v_and_b32 v25, 0xffff0000, v140
v_lshlrev_b32 v26, 16, v141
v_and_b32 v27, 0xffff0000, v141
v_lshlrev_b32 v28, 16, v142
v_and_b32 v29, 0xffff0000, v142
s_add_u32 s88, s68, s86
s_add_u32 s86, s86, 128
s_cmp_ge_u32 s86, 1280
s_cselect_b32 s86, 0, s86
s_add_u32 m0, s94, 0
s_add_u32 s95, s88, 0
buffer_load_dwordx4 v220, s[32:35], s95 offen offset:0 lds
v_lshlrev_b32 v30, 16, v143
v_and_b32 v31, 0xffff0000, v143
v_lshlrev_b32 v32, 16, v144
v_and_b32 v33, 0xffff0000, v144
v_lshlrev_b32 v34, 16, v145
v_and_b32 v35, 0xffff0000, v145
v_lshlrev_b32 v36, 16, v146
v_and_b32 v37, 0xffff0000, v146
s_add_u32 s95, s88, 9216
buffer_load_dwordx4 v221, s[32:35], s95 offen offset:1024 lds
v_lshlrev_b32 v38, 16, v147
v_and_b32 v39, 0xffff0000, v147
v_pk_add_f32 v[40:41], v[0:1], v[2:3]
v_pk_add_f32 v[42:43], v[4:5], v[6:7]
v_pk_add_f32 v[40:41], v[40:41], v[8:9]
v_pk_add_f32 v[42:43], v[42:43], v[10:11]
v_pk_add_f32 v[40:41], v[40:41], v[12:13]
v_pk_add_f32 v[42:43], v[42:43], v[14:15]
s_add_u32 s95, s88, 18432
buffer_load_dwordx4 v220, s[32:35], s95 offen offset:2048 lds
v_pk_add_f32 v[40:41], v[40:41], v[16:17]
v_pk_add_f32 v[42:43], v[42:43], v[18:19]
v_pk_add_f32 v[40:41], v[40:41], v[20:21]
v_pk_add_f32 v[42:43], v[42:43], v[22:23]
v_pk_add_f32 v[40:41], v[40:41], v[24:25]
v_pk_add_f32 v[42:43], v[42:43], v[26:27]
v_pk_add_f32 v[40:41], v[40:41], v[28:29]
v_pk_add_f32 v[42:43], v[42:43], v[30:31]
s_add_u32 s95, s88, 27648
buffer_load_dwordx4 v221, s[32:35], s95 offen offset:3072 lds
v_pk_add_f32 v[40:41], v[40:41], v[32:33]
v_pk_add_f32 v[42:43], v[42:43], v[34:35]
v_pk_add_f32 v[40:41], v[40:41], v[36:37]
v_pk_add_f32 v[42:43], v[42:43], v[38:39]
v_pk_add_f32 v[40:41], v[40:41], v[42:43]
v_add_f32 v48, v40, v41
s_nop 1
v_add_f32_dpp v48, v48, v48 row_ror:8 row_mask:0xf bank_mask:0xf
s_nop 1
v_add_f32_dpp v48, v48, v48 row_ror:4 row_mask:0xf bank_mask:0xf
s_add_u32 m0, s94, 4096
s_add_u32 s95, s88, 40960
buffer_load_dwordx4 v220, s[32:35], s95 offen offset:0 lds
v_add_f32_dpp v48, v48, v48 row_ror:2 row_mask:0xf bank_mask:0xf
s_nop 1
v_add_f32_dpp v48, v48, v48 row_ror:1 row_mask:0xf bank_mask:0xf
v_mul_f32 v48, 0x3acccccd, v48
CV v44,v48,v48
v_and_b32 v44, 0xffff0000, v44
v_sub_f32 v48, v48, v44
v_pk_add_f32 v[0:1], v[0:1], v[44:45] op_sel_hi:[1,0] neg_lo:[0,1] neg_hi:[0,1]
v_pk_add_f32 v[2:3], v[2:3], v[44:45] op_sel_hi:[1,0] neg_lo:[0,1] neg_hi:[0,1]
s_add_u32 s95, s88, 50176
buffer_load_dwordx4 v221, s[32:35], s95 offen offset:1024 lds
v_pk_add_f32 v[4:5], v[4:5], v[44:45] op_sel_hi:[1,0] neg_lo:[0,1] neg_hi:[0,1]
v_pk_add_f32 v[6:7], v[6:7], v[44:45] op_sel_hi:[1,0] neg_lo:[0,1] neg_hi:[0,1]
v_pk_add_f32 v[8:9], v[8:9], v[44:45] op_sel_hi:[1,0] neg_lo:[0,1] neg_hi:[0,1]
v_pk_add_f32 v[10:11], v[10:11], v[44:45] op_sel_hi:[1,0] neg_lo:[0,1] neg_hi:[0,1]
v_pk_add_f32 v[12:13], v[12:13], v[44:45] op_sel_hi:[1,0] neg_lo:[0,1] neg_hi:[0,1]
)ASM"
R"ASM(
v_pk_add_f32 v[14:15], v[14:15], v[44:45] op_sel_hi:[1,0] neg_lo:[0,1] neg_hi:[0,1]
v_pk_add_f32 v[16:17], v[16:17], v[44:45] op_sel_hi:[1,0] neg_lo:[0,1] neg_hi:[0,1]
v_pk_add_f32 v[18:19], v[18:19], v[44:45] op_sel_hi:[1,0] neg_lo:[0,1] neg_hi:[0,1]
s_add_u32 s95, s88, 59392
buffer_load_dwordx4 v220, s[32:35], s95 offen offset:2048 lds
v_pk_add_f32 v[20:21], v[20:21], v[44:45] op_sel_hi:[1,0] neg_lo:[0,1] neg_hi:[0,1]
v_pk_add_f32 v[22:23], v[22:23], v[44:45] op_sel_hi:[1,0] neg_lo:[0,1] neg_hi:[0,1]
v_pk_add_f32 v[24:25], v[24:25], v[44:45] op_sel_hi:[1,0] neg_lo:[0,1] neg_hi:[0,1]
v_pk_add_f32 v[26:27], v[26:27], v[44:45] op_sel_hi:[1,0] neg_lo:[0,1] neg_hi:[0,1]
v_pk_add_f32 v[28:29], v[28:29], v[44:45] op_sel_hi:[1,0] neg_lo:[0,1] neg_hi:[0,1]
v_pk_add_f32 v[30:31], v[30:31], v[44:45] op_sel_hi:[1,0] neg_lo:[0,1] neg_hi:[0,1]
v_pk_add_f32 v[32:33], v[32:33], v[44:45] op_sel_hi:[1,0] neg_lo:[0,1] neg_hi:[0,1]
v_pk_add_f32 v[34:35], v[34:35], v[44:45] op_sel_hi:[1,0] neg_lo:[0,1] neg_hi:[0,1]
s_add_u32 s95, s88, 68608
buffer_load_dwordx4 v221, s[32:35], s95 offen offset:3072 lds
v_pk_add_f32 v[36:37], v[36:37], v[44:45] op_sel_hi:[1,0] neg_lo:[0,1] neg_hi:[0,1]
v_pk_add_f32 v[38:39], v[38:39], v[44:45] op_sel_hi:[1,0] neg_lo:[0,1] neg_hi:[0,1]
v_pk_mul_f32 v[40:41], v[0:1], v[0:1]
v_pk_mul_f32 v[42:43], v[2:3], v[2:3]
v_pk_fma_f32 v[40:41], v[4:5], v[4:5], v[40:41]
v_pk_fma_f32 v[42:43], v[6:7], v[6:7], v[42:43]
v_pk_fma_f32 v[40:41], v[8:9], v[8:9], v[40:41]
v_pk_fma_f32 v[42:43], v[10:11], v[10:11], v[42:43]
s_add_u32 s87, s89, s85
s_add_u32 s85, s85, 524288
s_cmp_ge_u32 s85, 5242880
s_cselect_b32 s85, 0, s85
s_add_u32 m0, s96, 81984
s_nop 0
buffer_load_dwordx4 v253, s[16:19], s87 offen offset:0 lds
v_pk_fma_f32 v[40:41], v[12:13], v[12:13], v[40:41]
v_pk_fma_f32 v[42:43], v[14:15], v[14:15], v[42:43]
v_pk_fma_f32 v[40:41], v[16:17], v[16:17], v[40:41]
v_pk_fma_f32 v[42:43], v[18:19], v[18:19], v[42:43]
v_pk_fma_f32 v[40:41], v[20:21], v[20:21], v[40:41]
v_pk_fma_f32 v[42:43], v[22:23], v[22:23], v[42:43]
v_pk_fma_f32 v[40:41], v[24:25], v[24:25], v[40:41]
v_pk_fma_f32 v[42:43], v[26:27], v[26:27], v[42:43]
s_add_u32 m0, s96, 83008
s_nop 0
buffer_load_dwordx4 v254, s[16:19], s87 offen offset:0 lds
v_pk_fma_f32 v[40:41], v[28:29], v[28:29], v[40:41]
v_pk_fma_f32 v[42:43], v[30:31], v[30:31], v[42:43]
v_pk_fma_f32 v[40:41], v[32:33], v[32:33], v[40:41]
v_pk_fma_f32 v[42:43], v[34:35], v[34:35], v[42:43]
v_pk_fma_f32 v[40:41], v[36:37], v[36:37], v[40:41]
v_pk_fma_f32 v[42:43], v[38:39], v[38:39], v[42:43]
v_mul_f32 v48, v48, v48
v_pk_add_f32 v[40:41], v[40:41], v[42:43]
s_add_u32 s87, s87, 65536
s_add_u32 m0, s96, 84040
s_nop 0
buffer_load_dwordx4 v253, s[16:19], s87 offen offset:0 lds
v_add_f32 v49, v40, v41
CV v128,v0,v1
CV v129,v2,v3
v_add_f32_dpp v49, v49, v49 row_ror:8 row_mask:0xf bank_mask:0xf
CV v130,v4,v5
CV v131,v6,v7
v_add_f32_dpp v49, v49, v49 row_ror:4 row_mask:0xf bank_mask:0xf
CV v132,v8,v9
s_add_u32 m0, s96, 85064
s_nop 0
buffer_load_dwordx4 v254, s[16:19], s87 offen offset:0 lds
CV v133,v10,v11
v_add_f32_dpp v49, v49, v49 row_ror:2 row_mask:0xf bank_mask:0xf
CV v134,v12,v13
CV v135,v14,v15
v_add_f32_dpp v49, v49, v49 row_ror:1 row_mask:0xf bank_mask:0xf
v_fma_f32 v49, v49, s61, -v48
CV v136,v16,v17
v_max_f32 v49, 0, v49
s_add_u32 s88, s68, s86
s_add_u32 s86, s86, 128
s_cmp_ge_u32 s86, 1280
s_cselect_b32 s86, 0, s86
s_add_u32 m0, s94, 32768
s_add_u32 s95, s88, 0
buffer_load_dwordx4 v220, s[32:35], s95 offen offset:0 lds
CV v137,v18,v19
CV v49,v49,v49
CV v138,v20,v21
v_and_b32 v49, 0xffff0000, v49
CV v139,v22,v23
v_add_f32 v49, 0x3727c5ac, v49
CV v140,v24,v25
CV v49,v49,v49
s_add_u32 s95, s88, 9216
buffer_load_dwordx4 v221, s[32:35], s95 offen offset:1024 lds
CV v141,v26,v27
v_and_b32 v49, 0xffff0000, v49
CV v142,v28,v29
v_sqrt_f32 v49, v49
CV v143,v30,v31
CV v178,v44,v49
CV v144,v32,v33
v_and_b32 v49, 0xffff0000, v178
s_add_u32 s95, s88, 18432
buffer_load_dwordx4 v220, s[32:35], s95 offen offset:2048 lds
CV v145,v34,v35
v_rcp_f32 v46, v49
CV v146,v36,v37
CV v147,v38,v39
v_lshlrev_b32 v0, 16, v128
v_and_b32 v1, 0xffff0000, v128
v_lshlrev_b32 v2, 16, v129
v_and_b32 v3, 0xffff0000, v129
s_add_u32 s95, s88, 27648
buffer_load_dwordx4 v221, s[32:35], s95 offen offset:3072 lds
v_lshlrev_b32 v4, 16, v130
v_and_b32 v5, 0xffff0000, v130
v_lshlrev_b32 v6, 16, v131
v_and_b32 v7, 0xffff0000, v131
v_lshlrev_b32 v8, 16, v132
v_and_b32 v9, 0xffff0000, v132
v_lshlrev_b32 v10, 16, v133
v_and_b32 v11, 0xffff0000, v133
s_add_u32 m0, s94, 36864
s_add_u32 s95, s88, 40960
buffer_load_dwordx4 v220, s[32:35], s95 offen offset:0 lds
v_lshlrev_b32 v12, 16, v134
v_and_b32 v13, 0xffff0000, v134
v_lshlrev_b32 v14, 16, v135
v_and_b32 v15, 0xffff0000, v135
v_lshlrev_b32 v16, 16, v136
v_and_b32 v17, 0xffff0000, v136
v_lshlrev_b32 v18, 16, v137
v_and_b32 v19, 0xffff0000, v137
s_add_u32 s95, s88, 50176
buffer_load_dwordx4 v221, s[32:35], s95 offen offset:1024 lds
v_lshlrev_b32 v20, 16, v138
v_and_b32 v21, 0xffff0000, v138
v_lshlrev_b32 v22, 16, v139
v_and_b32 v23, 0xffff0000, v139
v_lshlrev_b32 v24, 16, v140
v_and_b32 v25, 0xffff0000, v140
v_lshlrev_b32 v26, 16, v141
v_and_b32 v27, 0xffff0000, v141
s_add_u32 s95, s88, 59392
buffer_load_dwordx4 v220, s[32:35], s95 offen offset:2048 lds
v_lshlrev_b32 v28, 16, v142
v_and_b32 v29, 0xffff0000, v142
v_lshlrev_b32 v30, 16, v143
v_and_b32 v31, 0xffff0000, v143
v_lshlrev_b32 v32, 16, v144
v_and_b32 v33, 0xffff0000, v144
v_lshlrev_b32 v34, 16, v145
v_and_b32 v35, 0xffff0000, v145
s_add_u32 s95, s88, 68608
buffer_load_dwordx4 v221, s[32:35], s95 offen offset:3072 lds
v_lshlrev_b32 v36, 16, v146
v_and_b32 v37, 0xffff0000, v146
v_lshlrev_b32 v38, 16, v147
v_and_b32 v39, 0xffff0000, v147
v_pk_mul_f32 v[0:1], v[0:1], v[46:47] op_sel_hi:[1,0]
v_pk_mul_f32 v[2:3], v[2:3], v[46:47] op_sel_hi:[1,0]
v_pk_mul_f32 v[4:5], v[4:5], v[46:47] op_sel_hi:[1,0]
v_pk_mul_f32 v[6:7], v[6:7], v[46:47] op_sel_hi:[1,0]
v_pk_mul_f32 v[8:9], v[8:9], v[46:47] op_sel_hi:[1,0]
v_pk_mul_f32 v[10:11], v[10:11], v[46:47] op_sel_hi:[1,0]
v_pk_mul_f32 v[12:13], v[12:13], v[46:47] op_sel_hi:[1,0]
v_pk_mul_f32 v[14:15], v[14:15], v[46:47] op_sel_hi:[1,0]
v_pk_mul_f32 v[16:17], v[16:17], v[46:47] op_sel_hi:[1,0]
v_pk_mul_f32 v[18:19], v[18:19], v[46:47] op_sel_hi:[1,0]
v_pk_mul_f32 v[20:21], v[20:21], v[46:47] op_sel_hi:[1,0]
v_pk_mul_f32 v[22:23], v[22:23], v[46:47] op_sel_hi:[1,0]
v_pk_mul_f32 v[24:25], v[24:25], v[46:47] op_sel_hi:[1,0]
v_pk_mul_f32 v[26:27], v[26:27], v[46:47] op_sel_hi:[1,0]
v_pk_mul_f32 v[28:29], v[28:29], v[46:47] op_sel_hi:[1,0]
v_pk_mul_f32 v[30:31], v[30:31], v[46:47] op_sel_hi:[1,0]
v_pk_mul_f32 v[32:33], v[32:33], v[46:47] op_sel_hi:[1,0]
v_pk_mul_f32 v[34:35], v[34:35], v[46:47] op_sel_hi:[1,0]
v_pk_mul_f32 v[36:37], v[36:37], v[46:47] op_sel_hi:[1,0]
v_pk_mul_f32 v[38:39], v[38:39], v[46:47] op_sel_hi:[1,0]
CV v128,v0,v1
CV v129,v2,v3
CV v130,v4,v5
CV v131,v6,v7
CV v132,v8,v9
CV v133,v10,v11
CV v134,v12,v13
CV v135,v14,v15
CV v136,v16,v17
CV v137,v18,v19
CV v138,v20,v21
CV v139,v22,v23
CV v140,v24,v25
CV v141,v26,v27
CV v142,v28,v29
CV v143,v30,v31
CV v144,v32,v33
CV v145,v34,v35
CV v146,v36,v37
CV v147,v38,v39
s_waitcnt vmcnt(24)
v_dot2_f32_bf16 v40, v128, v100, 0
v_dot2_f32_bf16 v41, v129, v101, 0
v_dot2_f32_bf16 v42, v130, v102, 0
v_dot2_f32_bf16 v43, v131, v103, 0
v_dot2_f32_bf16 v40, v132, v104, v40
v_dot2_f32_bf16 v41, v133, v105, v41
v_dot2_f32_bf16 v42, v134, v106, v42
v_dot2_f32_bf16 v43, v135, v107, v43
v_dot2_f32_bf16 v40, v136, v108, v40
v_dot2_f32_bf16 v41, v137, v109, v41
v_dot2_f32_bf16 v42, v138, v110, v42
v_dot2_f32_bf16 v43, v139, v111, v43
v_dot2_f32_bf16 v40, v140, v112, v40
v_dot2_f32_bf16 v41, v141, v113, v41
v_dot2_f32_bf16 v42, v142, v114, v42
v_dot2_f32_bf16 v43, v143, v115, v43
v_dot2_f32_bf16 v40, v144, v116, v40
v_dot2_f32_bf16 v41, v145, v117, v41
v_dot2_f32_bf16 v42, v146, v118, v42
v_dot2_f32_bf16 v43, v147, v119, v43
s_nop 2
v_pk_add_f32 v[40:41], v[40:41], v[42:43]
v_add_f32 v160, v40, v41
s_nop 1
v_add_f32_dpp v160, v160, v160 row_ror:8 row_mask:0xf bank_mask:0xf
s_nop 1
v_add_f32_dpp v160, v160, v160 row_ror:4 row_mask:0xf bank_mask:0xf
s_nop 1
v_add_f32_dpp v160, v160, v160 row_ror:2 row_mask:0xf bank_mask:0xf
s_nop 1
v_add_f32_dpp v160, v160, v160 row_ror:1 row_mask:0xf bank_mask:0xf
CV v160,v160,v160
v_ashrrev_i32 v50, 31, v160
v_or_b32 v50, 0x80000000, v50
v_xor_b32 v160, v160, v50
v_and_b32 v160, 0xffff0000, v160
v_sub_u32 v50, s60, v181
v_or_b32 v160, v160, v50
LCM_%=:
s_waitcnt vmcnt(12)
LPG1_%=:
s_cbranch_execz LPG5_%=
RD a[128:131],v222 offset:0
RD a[132:135],v223 offset:0
RD a[136:139],v217 offset:0
RD a[140:143],v218 offset:0
RD a[144:147],v222 offset:4096
RD a[148:151],v223 offset:4096
RD a[152:155],v217 offset:4096
RD a[156:159],v218 offset:4096
s_barrier
TR a[0:1],v192 offset:0
TR a[2:3],v192 offset:256
TR a[4:5],v192 offset:8224
TR a[6:7],v192 offset:8480
TR a[8:9],v192 offset:8
TR a[10:11],v192 offset:264
TR a[12:13],v192 offset:8232
TR a[14:15],v192 offset:8488
s_waitcnt lgkmcnt(8)
s_waitcnt lgkmcnt(4)
MM v[0:3],a[0:3],a[128:131],0
s_mov_b32 s66, 32768
TR a[16:17],v193 offset:0
TR a[18:19],v193 offset:256
MM v[0:3],a[4:7],a[132:135],v[0:3]
s_lshl_b32 s90, s76, 1
TR a[20:21],v193 offset:8224
TR a[22:23],v193 offset:8480
MM v[8:11],a[4:7],a[140:143],0
s_and_b32 s77, s90, 15
TR a[24:25],v193 offset:8
TR a[26:27],v193 offset:264
buffer_load_dword v169, off, s[20:23], s66 sc1
MM v[8:11],a[0:3],a[136:139],v[8:11]
s_add_u32 s82, s82, 131072
TR a[28:29],v193 offset:8232
TR a[30:31],v193 offset:8488
MM v[16:19],a[0:3],a[144:147],0
s_add_u32 s87, s89, s85
TR a[32:33],v194 offset:0
TR a[34:35],v194 offset:256
MM v[16:19],a[4:7],a[148:151],v[16:19]
s_add_u32 s85, s85, 524288
TR a[36:37],v194 offset:8224
TR a[38:39],v194 offset:8480
MM v[24:27],a[4:7],a[156:159],0
s_lshl_b32 s77, s77, 1
TR a[40:41],v194 offset:8
TR a[42:43],v194 offset:264
MM v[24:27],a[0:3],a[152:155],v[24:27]
s_waitcnt lgkmcnt(14)
TR a[44:45],v194 offset:8232
TR a[46:47],v194 offset:8488
s_cmp_ge_u32 s85, 5242880
s_cselect_b32 s85, 0, s85
MM v[4:7],a[8:11],a[128:131],0
s_add_u32 m0, s96, 98432
TR a[48:49],v195 offset:0
TR a[50:51],v195 offset:256
MM v[4:7],a[12:15],a[132:135],v[4:7]
s_bfe_u32 s91, s90, 65540
TR a[52:53],v195 offset:8224
TR a[54:55],v195 offset:8480
MM v[12:15],a[12:15],a[140:143],0
s_add_u32 s77, s77, s91
TR a[56:57],v195 offset:8
TR a[58:59],v195 offset:264
buffer_load_dwordx4 v253, s[16:19], s87 offen offset:0 lds
MM v[12:15],a[8:11],a[136:139],v[12:15]
s_add_u32 m0, s96, 99456
TR a[60:61],v195 offset:8232
TR a[62:63],v195 offset:8488
MM v[20:23],a[8:11],a[144:147],0
s_and_b32 s91, s90, 0xffffffe0
buffer_load_dwordx4 v254, s[16:19], s87 offen offset:0 lds
MM v[20:23],a[12:15],a[148:151],v[20:23]
s_add_u32 s87, s87, 65536
s_add_u32 m0, s96, 100488
s_add_u32 s77, s77, s91
MM v[28:31],a[12:15],a[156:159],0
s_lshl_b32 s77, s77, 3
s_add_u32 s97, s77, 36864
s_sub_u32 s79, 65535, s90
MM v[28:31],a[8:11],a[152:155],v[28:31]
s_lshl_b32 s78, s90, 2
s_add_u32 s78, s78, 53248
s_lshl_b32 s80, s76, 2
v_smfmac_f32_16x16x64_bf16 v[184:187], v[236:239], a[0:7], v234
s_add_u32 s80, s80, 81920
v_lshlrev_b32 v232, 11, v180
v_smfmac_f32_16x16x64_bf16 v[188:191], v[236:239], a[8:15], v234
s_waitcnt lgkmcnt(15)
v_lshl_add_u32 v232, v214, 8, v232
MM v[32:35],a[16:19],a[128:131],0
buffer_load_dwordx4 v253, s[16:19], s87 offen offset:0 lds
MM v[32:35],a[20:23],a[132:135],v[32:35]
s_add_u32 m0, s96, 101512
v_lshl_add_u32 v232, v215, 6, v232
v_lshl_add_u32 v232, v216, 3, v232
MM v[40:43],a[20:23],a[140:143],0
s_waitcnt vmcnt(12)
s_barrier
v_lshl_add_u32 v232, v214, 5, v232
MM v[40:43],a[16:19],a[136:139],v[40:43]
TR a[0:1],v192 offset:16448
TR a[2:3],v192 offset:16704
MM v[48:51],a[16:19],a[144:147],0
TR a[4:5],v192 offset:24672
TR a[6:7],v192 offset:24928
buffer_load_dwordx4 v254, s[16:19], s87 offen offset:0 lds
MM v[48:51],a[20:23],a[148:151],v[48:51]
TR a[8:9],v192 offset:16456
TR a[10:11],v192 offset:16712
MM v[56:59],a[20:23],a[156:159],0
s_add_u32 s88, s68, s86
)ASM"
R"ASM(
TR a[12:13],v192 offset:24680
TR a[14:15],v192 offset:24936
MM v[56:59],a[16:19],a[152:155],v[56:59]
s_add_u32 s86, s86, 128
s_cmp_ge_u32 s86, 1280
s_cselect_b32 s86, 0, s86
MM v[36:39],a[24:27],a[128:131],0
s_add_u32 m0, s94, 0
TR a[16:17],v193 offset:16448
TR a[18:19],v193 offset:16704
MM v[36:39],a[28:31],a[132:135],v[36:39]
s_add_u32 s95, s88, 0
TR a[20:21],v193 offset:24672
TR a[22:23],v193 offset:24928
MM v[44:47],a[28:31],a[140:143],0
buffer_load_dwordx4 v220, s[32:35], s95 offen offset:0 lds
MM v[44:47],a[24:27],a[136:139],v[44:47]
s_add_u32 s95, s88, 9216
v_xor_b32 v233, 32, v232
s_add_u32 s77, %[slds], 65536
MM v[52:55],a[24:27],a[144:147],0
buffer_load_dwordx4 v221, s[32:35], s95 offen offset:1024 lds
MM v[52:55],a[28:31],a[148:151],v[52:55]
s_add_u32 s95, s88, 18432
v_add_u32 v232, s77, v232
v_add_u32 v233, s77, v233
MM v[60:63],a[28:31],a[156:159],0
v_lshlrev_b32 v214, 8, v211
v_add_u32 v214, %[slds], v214
MM v[60:63],a[24:27],a[152:155],v[60:63]
s_waitcnt lgkmcnt(15)
v_xor_b32 v215, v211, v212
s_lshl_b32 s77, %[swave], 2
MM v[64:67],a[32:35],a[128:131],0
TR a[24:25],v193 offset:16456
TR a[26:27],v193 offset:16712
MM v[64:67],a[36:39],a[132:135],v[64:67]
TR a[28:29],v193 offset:24680
TR a[30:31],v193 offset:24936
MM v[72:75],a[36:39],a[140:143],0
buffer_load_dwordx4 v220, s[32:35], s95 offen offset:2048 lds
MM v[72:75],a[32:35],a[136:139],v[72:75]
s_add_u32 s95, s88, 27648
v_xor_b32 v213, s77, v215
v_xor_b32 v216, 0, v213
MM v[80:83],a[32:35],a[144:147],0
buffer_load_dwordx4 v221, s[32:35], s95 offen offset:3072 lds
MM v[80:83],a[36:39],a[148:151],v[80:83]
s_waitcnt vmcnt(9)
RD a[160:163],v222 offset:32768
v_lshl_add_u32 v224, v216, 4, v214
MM v[88:91],a[36:39],a[156:159],0
RD a[164:167],v223 offset:32768
v_xor_b32 v216, 4, v213
MM v[88:91],a[32:35],a[152:155],v[88:91]
RD a[168:171],v217 offset:32768
v_lshl_add_u32 v225, v216, 4, v214
MM v[68:71],a[40:43],a[128:131],0
TR a[32:33],v194 offset:16448
TR a[34:35],v194 offset:16704
MM v[68:71],a[44:47],a[132:135],v[68:71]
s_waitcnt vmcnt(8)
TR a[36:37],v194 offset:24672
TR a[38:39],v194 offset:24928
MM v[76:79],a[44:47],a[140:143],0
RD a[172:175],v218 offset:32768
v_readfirstlane_b32 s69, v169
MM v[76:79],a[40:43],a[136:139],v[76:79]
RD a[176:179],v222 offset:36864
v_lshlrev_b32 v173, 4, v181
MM v[84:87],a[40:43],a[144:147],0
s_add_u32 s69, s69, 1
RD a[180:183],v223 offset:36864
v_mov_b32 v179, s69
MM v[84:87],a[44:47],a[148:151],v[84:87]
RD a[184:187],v217 offset:36864
v_xor_b32 v216, 8, v213
MM v[92:95],a[44:47],a[156:159],0
RD a[188:191],v218 offset:36864
v_lshl_add_u32 v226, v216, 4, v214
MM v[92:95],a[40:43],a[152:155],v[92:95]
s_add_u32 m0, s94, 4096
s_add_u32 s95, s88, 40960
s_mov_b64 exec, s[54:55]
buffer_store_dwordx2 v[178:179], v173, s[20:23], s97 offen offset:0 sc1
s_mov_b64 exec, -1
v_xor_b32 v216, 12, v213
MM v[96:99],a[48:51],a[128:131],0
TR a[40:41],v194 offset:16456
TR a[42:43],v194 offset:16712
MM v[96:99],a[52:55],a[132:135],v[96:99]
TR a[44:45],v194 offset:24680
TR a[46:47],v194 offset:24936
buffer_load_dwordx4 v220, s[32:35], s95 offen offset:0 lds
MM v[104:107],a[52:55],a[140:143],0
s_add_u32 s95, s88, 50176
v_lshl_add_u32 v227, v216, 4, v214
v_lshlrev_b32 v214, 8, v212
MM v[104:107],a[48:51],a[136:139],v[104:107]
buffer_load_dwordx4 v221, s[32:35], s95 offen offset:1024 lds
MM v[112:115],a[48:51],a[144:147],0
s_add_u32 s95, s88, 59392
v_xor_b32 v216, 0, v215
v_lshl_add_u32 v228, v216, 4, v214
MM v[112:115],a[52:55],a[148:151],v[112:115]
v_xor_b32 v216, 4, v215
v_lshl_add_u32 v229, v216, 4, v214
MM v[120:123],a[52:55],a[156:159],0
v_xor_b32 v216, 8, v215
v_lshl_add_u32 v230, v216, 4, v214
MM v[120:123],a[48:51],a[152:155],v[120:123]
s_waitcnt lgkmcnt(15)
buffer_load_dwordx4 v220, s[32:35], s95 offen offset:2048 lds
MM v[100:103],a[56:59],a[128:131],0
s_add_u32 s95, s88, 68608
TR a[48:49],v195 offset:16448
TR a[50:51],v195 offset:16704
MM v[100:103],a[60:63],a[132:135],v[100:103]
TR a[52:53],v195 offset:24672
TR a[54:55],v195 offset:24928
MM v[108:111],a[60:63],a[140:143],0
v_xor_b32 v216, 12, v215
v_lshl_add_u32 v231, v216, 4, v214
MM v[108:111],a[56:59],a[136:139],v[108:111]
s_lshl_b32 s90, s49, 19
buffer_load_dwordx4 v221, s[32:35], s95 offen offset:3072 lds
MM v[116:119],a[56:59],a[144:147],0
s_add_u32 s87, s89, s85
s_add_u32 s8, s8, s90
s_addc_u32 s9, s9, 0
MM v[116:119],a[60:63],a[148:151],v[116:119]
s_add_u32 s85, s85, 524288
s_cmp_ge_u32 s85, 5242880
s_cselect_b32 s85, 0, s85
MM v[124:127],a[60:63],a[156:159],0
s_add_u32 m0, s96, 114880
s_mov_b32 s10, 524288
s_mov_b32 s11, 131072
MM v[124:127],a[56:59],a[152:155],v[124:127]
buffer_load_dwordx4 v253, s[16:19], s87 offen offset:0 lds
s_waitcnt vmcnt(10) lgkmcnt(8)
s_barrier
MM v[0:3],a[0:3],a[160:163],v[0:3]
s_add_u32 m0, s96, 115904
TR a[56:57],v195 offset:16456
TR a[58:59],v195 offset:16712
MM v[0:3],a[4:7],a[164:167],v[0:3]
TR a[60:61],v195 offset:24680
TR a[62:63],v195 offset:24936
MM v[8:11],a[4:7],a[172:175],v[8:11]
s_add_u32 s12, s12, s90
s_addc_u32 s13, s13, 0
s_mov_b32 s14, 525312
MM v[8:11],a[0:3],a[168:171],v[8:11]
s_mov_b32 s15, 131072
buffer_load_dwordx4 v254, s[16:19], s87 offen offset:0 lds
MM v[16:19],a[0:3],a[176:179],v[16:19]
s_add_u32 s87, s87, 65536
s_sub_u32 s12, s12, 1024
s_subb_u32 s13, s13, 0
MM v[16:19],a[4:7],a[180:183],v[16:19]
s_add_u32 m0, s96, 116936
s_lshr_b32 s90, s49, 2
s_mul_i32 s90, s90, 655360
MM v[24:27],a[4:7],a[188:191],v[24:27]
s_mul_i32 s77, s84, 320
s_add_u32 s90, s90, s77
MM v[24:27],a[0:3],a[184:187],v[24:27]
buffer_load_dwordx4 v253, s[16:19], s87 offen offset:0 lds
MM v[4:7],a[8:11],a[160:163],v[4:7]
s_add_u32 m0, s96, 117960
s_add_u32 s24, s24, s90
s_addc_u32 s25, s25, 0
MM v[4:7],a[12:15],a[164:167],v[4:7]
s_sub_u32 s26, 5242880, s90
s_mov_b32 s27, 131072
s_sub_u32 s77, %[sb], 76
MM v[12:15],a[12:15],a[172:175],v[12:15]
s_min_u32 s47, s77, %[sb]
s_mul_i32 s77, s47, 205
s_lshr_b32 s46, s77, 11
MM v[12:15],a[8:11],a[168:171],v[12:15]
s_mul_i32 s90, s46, 121600
buffer_load_dwordx4 v254, s[16:19], s87 offen offset:0 lds
MM v[20:23],a[8:11],a[176:179],v[20:23]
s_lshl_b32 s77, s47, 7
s_add_u32 s90, s90, s77
MM v[20:23],a[12:15],a[180:183],v[20:23]
s_add_u32 s28, s28, s90
s_addc_u32 s29, s29, 0
s_sub_u32 s30, 2211840, s90
MM v[28:31],a[12:15],a[188:191],v[28:31]
s_add_u32 s88, s68, s86
s_add_u32 s86, s86, 128
MM v[28:31],a[8:11],a[184:187],v[28:31]
s_mov_b32 s31, 131072
s_cmp_ge_u32 s86, 1280
s_cselect_b32 s86, 0, s86
v_smfmac_f32_16x16x64_bf16 v[184:187], v[236:239], a[0:7], v234
s_add_u32 m0, s94, 32768
s_add_u32 s95, s88, 0
v_smfmac_f32_16x16x64_bf16 v[188:191], v[236:239], a[8:15], v234
s_mov_b32 s38, 2211840
s_mov_b32 s39, 131072
s_mov_b64 exec, s[52:53]
buffer_store_dwordx4 v[128:131], v168, s[20:23], s82 offen offset:0 sc1
s_mov_b64 exec, -1
MM v[32:35],a[16:19],a[160:163],v[32:35]
s_mov_b32 s42, 2621440
TR a[0:1],v192 offset:32896
TR a[2:3],v192 offset:33152
MM v[32:35],a[20:23],a[164:167],v[32:35]
s_mov_b32 s43, 131072
TR a[4:5],v192 offset:41120
TR a[6:7],v192 offset:41376
buffer_load_dwordx4 v220, s[32:35], s95 offen offset:0 lds
MM v[40:43],a[20:23],a[172:175],v[40:43]
s_add_u32 s95, s88, 9216
TR a[8:9],v192 offset:32904
TR a[10:11],v192 offset:33160
MM v[40:43],a[16:19],a[168:171],v[40:43]
s_lshr_b32 s77, %[sb], 6
TR a[12:13],v192 offset:41128
TR a[14:15],v192 offset:41384
MM v[48:51],a[16:19],a[176:179],v[48:51]
s_lshl_b32 s77, s77, 3
s_and_b32 s90, %[sb], 7
s_add_u32 s77, s77, s90
MM v[48:51],a[20:23],a[180:183],v[48:51]
s_sub_u32 s90, s77, 8
buffer_load_dwordx4 v221, s[32:35], s95 offen offset:1024 lds
MM v[56:59],a[20:23],a[188:191],v[56:59]
s_add_u32 s95, s88, 18432
s_cmp_ge_u32 s77, 27
s_cselect_b32 s77, s90, s77
MM v[56:59],a[16:19],a[184:187],v[56:59]
s_lshl_b32 s77, s77, 7
s_lshl_b32 s90, %[sb], 1
s_and_b32 s90, s90, 112
MM v[36:39],a[24:27],a[160:163],v[36:39]
s_add_u32 s48, s77, s90
TR a[16:17],v193 offset:32896
TR a[18:19],v193 offset:33152
MM v[36:39],a[28:31],a[164:167],v[36:39]
s_mov_b32 s71, 0x4e800000
TR a[20:21],v193 offset:41120
TR a[22:23],v193 offset:41376
MM v[44:47],a[28:31],a[172:175],v[44:47]
s_mov_b32 s72, 0x3e0293ee
buffer_load_dwordx4 v220, s[32:35], s95 offen offset:2048 lds
MM v[44:47],a[24:27],a[168:171],v[44:47]
s_add_u32 s95, s88, 27648
s_lshl_b32 s64, %[swave], 12
MM v[52:55],a[24:27],a[176:179],v[52:55]
MM v[52:55],a[28:31],a[180:183],v[52:55]
buffer_load_dwordx4 v221, s[32:35], s95 offen offset:3072 lds
MM v[60:63],a[28:31],a[188:191],v[60:63]
s_add_u32 m0, s94, 36864
s_add_u32 s95, s88, 40960
MM v[60:63],a[24:27],a[184:187],v[60:63]
MM v[64:67],a[32:35],a[160:163],v[64:67]
TR a[24:25],v193 offset:32904
TR a[26:27],v193 offset:33160
MM v[64:67],a[36:39],a[164:167],v[64:67]
TR a[28:29],v193 offset:41128
TR a[30:31],v193 offset:41384
MM v[72:75],a[36:39],a[172:175],v[72:75]
buffer_load_dwordx4 v220, s[32:35], s95 offen offset:0 lds
MM v[72:75],a[32:35],a[168:171],v[72:75]
s_add_u32 s95, s88, 50176
MM v[80:83],a[32:35],a[176:179],v[80:83]
MM v[80:83],a[36:39],a[180:183],v[80:83]
buffer_load_dwordx4 v221, s[32:35], s95 offen offset:1024 lds
MM v[88:91],a[36:39],a[188:191],v[88:91]
s_add_u32 s95, s88, 59392
MM v[88:91],a[32:35],a[184:187],v[88:91]
s_waitcnt lgkmcnt(15)
MM v[68:71],a[40:43],a[160:163],v[68:71]
TR a[32:33],v194 offset:32896
TR a[34:35],v194 offset:33152
MM v[68:71],a[44:47],a[164:167],v[68:71]
TR a[36:37],v194 offset:41120
TR a[38:39],v194 offset:41376
s_mov_b64 exec, s[52:53]
buffer_store_dwordx4 v[132:135], v168, s[20:23], s82 offen offset:256 sc1
s_mov_b64 exec, -1
MM v[76:79],a[44:47],a[172:175],v[76:79]
MM v[76:79],a[40:43],a[168:171],v[76:79]
MM v[84:87],a[40:43],a[176:179],v[84:87]
MM v[84:87],a[44:47],a[180:183],v[84:87]
buffer_load_dwordx4 v220, s[32:35], s95 offen offset:2048 lds
MM v[92:95],a[44:47],a[188:191],v[92:95]
s_add_u32 s95, s88, 68608
MM v[92:95],a[40:43],a[184:187],v[92:95]
MM v[96:99],a[48:51],a[160:163],v[96:99]
TR a[40:41],v194 offset:32904
TR a[42:43],v194 offset:33160
MM v[96:99],a[52:55],a[164:167],v[96:99]
TR a[44:45],v194 offset:41128
TR a[46:47],v194 offset:41384
MM v[104:107],a[52:55],a[172:175],v[104:107]
buffer_load_dwordx4 v221, s[32:35], s95 offen offset:3072 lds
MM v[104:107],a[48:51],a[168:171],v[104:107]
s_add_u32 s87, s89, s85
s_add_u32 s85, s85, 524288
MM v[112:115],a[48:51],a[176:179],v[112:115]
s_cmp_ge_u32 s85, 5242880
s_cselect_b32 s85, 0, s85
s_add_u32 m0, s96, 131328
MM v[112:115],a[52:55],a[180:183],v[112:115]
buffer_load_dwordx4 v253, s[16:19], s87 offen offset:0 lds
MM v[120:123],a[52:55],a[188:191],v[120:123]
s_add_u32 m0, s96, 132352
MM v[120:123],a[48:51],a[184:187],v[120:123]
MM v[100:103],a[56:59],a[160:163],v[100:103]
TR a[48:49],v195 offset:32896
TR a[50:51],v195 offset:33152
MM v[100:103],a[60:63],a[164:167],v[100:103]
TR a[52:53],v195 offset:41120
TR a[54:55],v195 offset:41376
MM v[108:111],a[60:63],a[172:175],v[108:111]
buffer_load_dwordx4 v254, s[16:19], s87 offen offset:0 lds
MM v[108:111],a[56:59],a[168:171],v[108:111]
s_add_u32 s87, s87, 65536
s_add_u32 m0, s96, 133384
MM v[116:119],a[56:59],a[176:179],v[116:119]
MM v[116:119],a[60:63],a[180:183],v[116:119]
buffer_load_dwordx4 v253, s[16:19], s87 offen offset:0 lds
MM v[124:127],a[60:63],a[188:191],v[124:127]
s_add_u32 m0, s96, 134408
MM v[124:127],a[56:59],a[184:187],v[124:127]
buffer_load_dwordx4 v254, s[16:19], s87 offen offset:0 lds
s_waitcnt vmcnt(18)
RD a[192:195],v222 offset:0
RD a[196:199],v223 offset:0
RD a[200:203],v217 offset:0
RD a[204:207],v218 offset:0
RD a[208:211],v222 offset:4096
RD a[212:215],v223 offset:4096
RD a[216:219],v217 offset:4096
RD a[220:223],v218 offset:4096
s_waitcnt vmcnt(14) lgkmcnt(0)
s_barrier
MM v[0:3],a[0:3],a[192:195],v[0:3]
TR a[56:57],v195 offset:32904
TR a[58:59],v195 offset:33160
MM v[0:3],a[4:7],a[196:199],v[0:3]
TR a[60:61],v195 offset:41128
TR a[62:63],v195 offset:41384
s_mov_b64 exec, s[52:53]
buffer_store_dwordx4 v[136:139], v168, s[20:23], s82 offen offset:512 sc1
s_mov_b64 exec, -1
MM v[8:11],a[4:7],a[204:207],v[8:11]
MM v[8:11],a[0:3],a[200:203],v[8:11]
s_add_u32 s88, s68, s86
)ASM"
R"ASM(
s_add_u32 s86, s86, 128
MM v[16:19],a[0:3],a[208:211],v[16:19]
s_cmp_ge_u32 s86, 1280
s_cselect_b32 s86, 0, s86
s_add_u32 m0, s94, 0
MM v[16:19],a[4:7],a[212:215],v[16:19]
s_add_u32 s95, s88, 0
buffer_load_dwordx4 v220, s[32:35], s95 offen offset:0 lds
MM v[24:27],a[4:7],a[220:223],v[24:27]
s_add_u32 s95, s88, 9216
MM v[24:27],a[0:3],a[216:219],v[24:27]
MM v[4:7],a[8:11],a[192:195],v[4:7]
MM v[4:7],a[12:15],a[196:199],v[4:7]
buffer_load_dwordx4 v221, s[32:35], s95 offen offset:1024 lds
MM v[12:15],a[12:15],a[204:207],v[12:15]
s_add_u32 s95, s88, 18432
MM v[12:15],a[8:11],a[200:203],v[12:15]
MM v[20:23],a[8:11],a[208:211],v[20:23]
MM v[20:23],a[12:15],a[212:215],v[20:23]
buffer_load_dwordx4 v220, s[32:35], s95 offen offset:2048 lds
MM v[28:31],a[12:15],a[220:223],v[28:31]
s_add_u32 s95, s88, 27648
MM v[28:31],a[8:11],a[216:219],v[28:31]
v_smfmac_f32_16x16x64_bf16 v[184:187], v[236:239], a[0:7], v234
v_smfmac_f32_16x16x64_bf16 v[188:191], v[236:239], a[8:15], v234
MM v[32:35],a[16:19],a[192:195],v[32:35]
TR a[0:1],v196 offset:0
TR a[2:3],v196 offset:256
MM v[32:35],a[20:23],a[196:199],v[32:35]
TR a[4:5],v196 offset:8224
TR a[6:7],v196 offset:8480
buffer_load_dwordx4 v221, s[32:35], s95 offen offset:3072 lds
MM v[40:43],a[20:23],a[204:207],v[40:43]
s_add_u32 m0, s94, 4096
TR a[8:9],v196 offset:8
TR a[10:11],v196 offset:264
MM v[40:43],a[16:19],a[200:203],v[40:43]
s_add_u32 s95, s88, 40960
TR a[12:13],v196 offset:8232
TR a[14:15],v196 offset:8488
s_mov_b64 exec, s[52:53]
buffer_store_dwordx4 v[140:143], v168, s[20:23], s82 offen offset:768 sc1
s_mov_b64 exec, -1
MM v[48:51],a[16:19],a[208:211],v[48:51]
MM v[48:51],a[20:23],a[212:215],v[48:51]
MM v[56:59],a[20:23],a[220:223],v[56:59]
MM v[56:59],a[16:19],a[216:219],v[56:59]
buffer_load_dwordx4 v220, s[32:35], s95 offen offset:0 lds
MM v[36:39],a[24:27],a[192:195],v[36:39]
s_add_u32 s95, s88, 50176
TR a[16:17],v197 offset:0
TR a[18:19],v197 offset:256
MM v[36:39],a[28:31],a[196:199],v[36:39]
TR a[20:21],v197 offset:8224
TR a[22:23],v197 offset:8480
MM v[44:47],a[28:31],a[204:207],v[44:47]
MM v[44:47],a[24:27],a[200:203],v[44:47]
buffer_load_dwordx4 v221, s[32:35], s95 offen offset:1024 lds
MM v[52:55],a[24:27],a[208:211],v[52:55]
s_add_u32 s95, s88, 59392
MM v[52:55],a[28:31],a[212:215],v[52:55]
MM v[60:63],a[28:31],a[220:223],v[60:63]
MM v[60:63],a[24:27],a[216:219],v[60:63]
buffer_load_dwordx4 v220, s[32:35], s95 offen offset:2048 lds
MM v[64:67],a[32:35],a[192:195],v[64:67]
s_add_u32 s95, s88, 68608
TR a[24:25],v197 offset:8
TR a[26:27],v197 offset:264
MM v[64:67],a[36:39],a[196:199],v[64:67]
TR a[28:29],v197 offset:8232
TR a[30:31],v197 offset:8488
MM v[72:75],a[36:39],a[204:207],v[72:75]
MM v[72:75],a[32:35],a[200:203],v[72:75]
buffer_load_dwordx4 v221, s[32:35], s95 offen offset:3072 lds
MM v[80:83],a[32:35],a[208:211],v[80:83]
s_add_u32 s87, s89, s85
s_add_u32 s85, s85, 524288
MM v[80:83],a[36:39],a[212:215],v[80:83]
s_cmp_ge_u32 s85, 5242880
s_cselect_b32 s85, 0, s85
s_add_u32 m0, s96, 65536
MM v[88:91],a[36:39],a[220:223],v[88:91]
MM v[88:91],a[32:35],a[216:219],v[88:91]
buffer_load_dwordx4 v253, s[16:19], s87 offen offset:0 lds
MM v[68:71],a[40:43],a[192:195],v[68:71]
s_add_u32 m0, s96, 66560
TR a[32:33],v198 offset:0
TR a[34:35],v198 offset:256
MM v[68:71],a[44:47],a[196:199],v[68:71]
TR a[36:37],v198 offset:8224
TR a[38:39],v198 offset:8480
MM v[76:79],a[44:47],a[204:207],v[76:79]
MM v[76:79],a[40:43],a[200:203],v[76:79]
buffer_load_dwordx4 v254, s[16:19], s87 offen offset:0 lds
LPG2_%=:
s_cbranch_execz LPG6_%=
MM v[84:87],a[40:43],a[208:211],v[84:87]
s_add_u32 s87, s87, 65536
s_add_u32 m0, s96, 67592
MM v[84:87],a[44:47],a[212:215],v[84:87]
MM v[92:95],a[44:47],a[220:223],v[92:95]
MM v[92:95],a[40:43],a[216:219],v[92:95]
buffer_load_dwordx4 v253, s[16:19], s87 offen offset:0 lds
MM v[96:99],a[48:51],a[192:195],v[96:99]
s_add_u32 m0, s96, 68616
TR a[40:41],v198 offset:8
TR a[42:43],v198 offset:264
MM v[96:99],a[52:55],a[196:199],v[96:99]
TR a[44:45],v198 offset:8232
TR a[46:47],v198 offset:8488
MM v[104:107],a[52:55],a[204:207],v[104:107]
MM v[104:107],a[48:51],a[200:203],v[104:107]
buffer_load_dwordx4 v254, s[16:19], s87 offen offset:0 lds
MM v[112:115],a[48:51],a[208:211],v[112:115]
s_waitcnt vmcnt(18)
RD a[224:227],v222 offset:32768
MM v[112:115],a[52:55],a[212:215],v[112:115]
RD a[228:231],v223 offset:32768
MM v[120:123],a[52:55],a[220:223],v[120:123]
RD a[232:235],v217 offset:32768
MM v[120:123],a[48:51],a[216:219],v[120:123]
s_waitcnt lgkmcnt(15)
s_mov_b64 exec, s[52:53]
buffer_store_dwordx4 v[144:147], v168, s[20:23], s82 offen offset:1024 sc1
s_mov_b64 exec, -1
MM v[100:103],a[56:59],a[192:195],v[100:103]
TR a[48:49],v199 offset:0
TR a[50:51],v199 offset:256
MM v[100:103],a[60:63],a[196:199],v[100:103]
TR a[52:53],v199 offset:8224
TR a[54:55],v199 offset:8480
RD a[236:239],v218 offset:32768
MM v[108:111],a[60:63],a[204:207],v[108:111]
RD a[240:243],v222 offset:36864
MM v[108:111],a[56:59],a[200:203],v[108:111]
RD a[244:247],v223 offset:36864
MM v[116:119],a[56:59],a[208:211],v[116:119]
RD a[248:251],v217 offset:36864
MM v[116:119],a[60:63],a[212:215],v[116:119]
RD a[252:255],v218 offset:36864
v_lshlrev_b32 v173, 2, v181
MM v[124:127],a[60:63],a[220:223],v[124:127]
s_mov_b64 exec, s[54:55]
buffer_store_dword v160, v173, s[20:23], s78 offen offset:0 sc1
s_mov_b64 exec, -1
MM v[124:127],a[56:59],a[216:219],v[124:127]
v_mov_b32 v170, s69
v_mov_b32 v171, s69
s_waitcnt vmcnt(16) lgkmcnt(0)
s_barrier
MM v[0:3],a[0:3],a[224:227],v[0:3]
TR a[56:57],v199 offset:8
TR a[58:59],v199 offset:264
MM v[0:3],a[4:7],a[228:231],v[0:3]
TR a[60:61],v199 offset:8232
TR a[62:63],v199 offset:8488
MM v[8:11],a[4:7],a[236:239],v[8:11]
s_add_u32 s88, s68, s86
s_add_u32 s86, s86, 128
MM v[8:11],a[0:3],a[232:235],v[8:11]
s_cmp_ge_u32 s86, 1280
s_cselect_b32 s86, 0, s86
s_add_u32 m0, s94, 32768
MM v[16:19],a[0:3],a[240:243],v[16:19]
s_add_u32 s95, s88, 0
buffer_load_dwordx4 v220, s[32:35], s95 offen offset:0 lds
MM v[16:19],a[4:7],a[244:247],v[16:19]
s_add_u32 s95, s88, 9216
MM v[24:27],a[4:7],a[252:255],v[24:27]
buffer_load_dwordx4 v221, s[32:35], s95 offen offset:1024 lds
MM v[24:27],a[0:3],a[248:251],v[24:27]
s_add_u32 s95, s88, 18432
MM v[4:7],a[8:11],a[224:227],v[4:7]
MM v[4:7],a[12:15],a[228:231],v[4:7]
MM v[12:15],a[12:15],a[236:239],v[12:15]
buffer_load_dwordx4 v220, s[32:35], s95 offen offset:2048 lds
MM v[12:15],a[8:11],a[232:235],v[12:15]
s_add_u32 s95, s88, 27648
MM v[20:23],a[8:11],a[240:243],v[20:23]
MM v[20:23],a[12:15],a[244:247],v[20:23]
MM v[28:31],a[12:15],a[252:255],v[28:31]
buffer_load_dwordx4 v221, s[32:35], s95 offen offset:3072 lds
MM v[28:31],a[8:11],a[248:251],v[28:31]
s_add_u32 m0, s94, 36864
s_add_u32 s95, s88, 40960
v_smfmac_f32_16x16x64_bf16 v[184:187], v[236:239], a[0:7], v234
v_smfmac_f32_16x16x64_bf16 v[188:191], v[236:239], a[8:15], v234
MM v[32:35],a[16:19],a[224:227],v[32:35]
TR a[0:1],v196 offset:16448
TR a[2:3],v196 offset:16704
MM v[32:35],a[20:23],a[228:231],v[32:35]
TR a[4:5],v196 offset:24672
TR a[6:7],v196 offset:24928
MM v[40:43],a[20:23],a[236:239],v[40:43]
TR a[8:9],v196 offset:16456
TR a[10:11],v196 offset:16712
buffer_load_dwordx4 v220, s[32:35], s95 offen offset:0 lds
MM v[40:43],a[16:19],a[232:235],v[40:43]
s_add_u32 s95, s88, 50176
TR a[12:13],v196 offset:24680
TR a[14:15],v196 offset:24936
MM v[48:51],a[16:19],a[240:243],v[48:51]
buffer_load_dwordx4 v221, s[32:35], s95 offen offset:1024 lds
MM v[48:51],a[20:23],a[244:247],v[48:51]
s_add_u32 s95, s88, 59392
MM v[56:59],a[20:23],a[252:255],v[56:59]
MM v[56:59],a[16:19],a[248:251],v[56:59]
MM v[36:39],a[24:27],a[224:227],v[36:39]
TR a[16:17],v197 offset:16448
TR a[18:19],v197 offset:16704
MM v[36:39],a[28:31],a[228:231],v[36:39]
TR a[20:21],v197 offset:24672
TR a[22:23],v197 offset:24928
MM v[44:47],a[28:31],a[236:239],v[44:47]
buffer_load_dwordx4 v220, s[32:35], s95 offen offset:2048 lds
MM v[44:47],a[24:27],a[232:235],v[44:47]
s_add_u32 s95, s88, 68608
MM v[52:55],a[24:27],a[240:243],v[52:55]
buffer_load_dwordx4 v221, s[32:35], s95 offen offset:3072 lds
MM v[52:55],a[28:31],a[244:247],v[52:55]
s_add_u32 s87, s89, s85
s_add_u32 s85, s85, 524288
MM v[60:63],a[28:31],a[252:255],v[60:63]
s_cmp_ge_u32 s85, 5242880
s_cselect_b32 s85, 0, s85
s_add_u32 m0, s96, 81984
MM v[60:63],a[24:27],a[248:251],v[60:63]
MM v[64:67],a[32:35],a[224:227],v[64:67]
TR a[24:25],v197 offset:16456
TR a[26:27],v197 offset:16712
MM v[64:67],a[36:39],a[228:231],v[64:67]
TR a[28:29],v197 offset:24680
TR a[30:31],v197 offset:24936
MM v[72:75],a[36:39],a[236:239],v[72:75]
buffer_load_dwordx4 v253, s[16:19], s87 offen offset:0 lds
MM v[72:75],a[32:35],a[232:235],v[72:75]
s_add_u32 m0, s96, 83008
MM v[80:83],a[32:35],a[240:243],v[80:83]
buffer_load_dwordx4 v254, s[16:19], s87 offen offset:0 lds
MM v[80:83],a[36:39],a[244:247],v[80:83]
s_add_u32 s87, s87, 65536
s_add_u32 m0, s96, 84040
MM v[88:91],a[36:39],a[252:255],v[88:91]
MM v[88:91],a[32:35],a[248:251],v[88:91]
MM v[68:71],a[40:43],a[224:227],v[68:71]
TR a[32:33],v198 offset:16448
TR a[34:35],v198 offset:16704
MM v[68:71],a[44:47],a[228:231],v[68:71]
TR a[36:37],v198 offset:24672
TR a[38:39],v198 offset:24928
MM v[76:79],a[44:47],a[236:239],v[76:79]
buffer_load_dwordx4 v253, s[16:19], s87 offen offset:0 lds
MM v[76:79],a[40:43],a[232:235],v[76:79]
s_add_u32 m0, s96, 85064
MM v[84:87],a[40:43],a[240:243],v[84:87]
buffer_load_dwordx4 v254, s[16:19], s87 offen offset:0 lds
MM v[84:87],a[44:47],a[244:247],v[84:87]
s_waitcnt vmcnt(18)
RD a[128:131],v222 offset:0
MM v[92:95],a[44:47],a[252:255],v[92:95]
RD a[132:135],v223 offset:0
MM v[92:95],a[40:43],a[248:251],v[92:95]
RD a[136:139],v217 offset:0
MM v[96:99],a[48:51],a[224:227],v[96:99]
TR a[40:41],v198 offset:16456
TR a[42:43],v198 offset:16712
MM v[96:99],a[52:55],a[228:231],v[96:99]
TR a[44:45],v198 offset:24680
TR a[46:47],v198 offset:24936
MM v[104:107],a[52:55],a[236:239],v[104:107]
RD a[140:143],v218 offset:0
MM v[104:107],a[48:51],a[232:235],v[104:107]
RD a[144:147],v222 offset:4096
MM v[112:115],a[48:51],a[240:243],v[112:115]
RD a[148:151],v223 offset:4096
MM v[112:115],a[52:55],a[244:247],v[112:115]
RD a[152:155],v217 offset:4096
MM v[120:123],a[52:55],a[252:255],v[120:123]
RD a[156:159],v218 offset:4096
MM v[120:123],a[48:51],a[248:251],v[120:123]
s_waitcnt lgkmcnt(15)
MM v[100:103],a[56:59],a[224:227],v[100:103]
TR a[48:49],v199 offset:16448
TR a[50:51],v199 offset:16704
MM v[100:103],a[60:63],a[228:231],v[100:103]
TR a[52:53],v199 offset:24672
TR a[54:55],v199 offset:24928
MM v[108:111],a[60:63],a[236:239],v[108:111]
s_waitcnt lgkmcnt(4)
s_add_u32 s88, s68, s86
s_add_u32 s86, s86, 128
MM v[108:111],a[56:59],a[232:235],v[108:111]
s_cmp_ge_u32 s86, 1280
s_cselect_b32 s86, 0, s86
s_add_u32 m0, s94, 0
MM v[116:119],a[56:59],a[240:243],v[116:119]
s_add_u32 s95, s88, 0
buffer_load_dwordx4 v220, s[32:35], s95 offen offset:0 lds
MM v[116:119],a[60:63],a[244:247],v[116:119]
s_add_u32 s95, s88, 9216
MM v[124:127],a[60:63],a[252:255],v[124:127]
buffer_load_dwordx4 v221, s[32:35], s95 offen offset:1024 lds
MM v[124:127],a[56:59],a[248:251],v[124:127]
s_add_u32 s95, s88, 18432
s_waitcnt vmcnt(16)
s_barrier
MM v[0:3],a[0:3],a[128:131],v[0:3]
TR a[56:57],v199 offset:16456
TR a[58:59],v199 offset:16712
MM v[0:3],a[4:7],a[132:135],v[0:3]
TR a[60:61],v199 offset:24680
TR a[62:63],v199 offset:24936
MM v[8:11],a[4:7],a[140:143],v[8:11]
buffer_load_dwordx4 v220, s[32:35], s95 offen offset:2048 lds
MM v[8:11],a[0:3],a[136:139],v[8:11]
s_add_u32 s95, s88, 27648
MM v[16:19],a[0:3],a[144:147],v[16:19]
MM v[16:19],a[4:7],a[148:151],v[16:19]
MM v[24:27],a[4:7],a[156:159],v[24:27]
buffer_load_dwordx4 v221, s[32:35], s95 offen offset:3072 lds
MM v[24:27],a[0:3],a[152:155],v[24:27]
s_add_u32 m0, s94, 4096
s_add_u32 s95, s88, 40960
MM v[4:7],a[8:11],a[128:131],v[4:7]
MM v[4:7],a[12:15],a[132:135],v[4:7]
MM v[12:15],a[12:15],a[140:143],v[12:15]
buffer_load_dwordx4 v220, s[32:35], s95 offen offset:0 lds
MM v[12:15],a[8:11],a[136:139],v[12:15]
s_add_u32 s95, s88, 50176
MM v[20:23],a[8:11],a[144:147],v[20:23]
)ASM"
R"ASM(
MM v[20:23],a[12:15],a[148:151],v[20:23]
MM v[28:31],a[12:15],a[156:159],v[28:31]
buffer_load_dwordx4 v221, s[32:35], s95 offen offset:1024 lds
MM v[28:31],a[8:11],a[152:155],v[28:31]
s_add_u32 s95, s88, 59392
v_smfmac_f32_16x16x64_bf16 v[184:187], v[236:239], a[0:7], v234
v_smfmac_f32_16x16x64_bf16 v[188:191], v[236:239], a[8:15], v234
MM v[32:35],a[16:19],a[128:131],v[32:35]
TR a[0:1],v192 offset:0
TR a[2:3],v192 offset:256
MM v[32:35],a[20:23],a[132:135],v[32:35]
TR a[4:5],v192 offset:8224
TR a[6:7],v192 offset:8480
MM v[40:43],a[20:23],a[140:143],v[40:43]
TR a[8:9],v192 offset:8
TR a[10:11],v192 offset:264
buffer_load_dwordx4 v220, s[32:35], s95 offen offset:2048 lds
MM v[40:43],a[16:19],a[136:139],v[40:43]
s_add_u32 s95, s88, 68608
TR a[12:13],v192 offset:8232
TR a[14:15],v192 offset:8488
MM v[48:51],a[16:19],a[144:147],v[48:51]
buffer_load_dwordx4 v221, s[32:35], s95 offen offset:3072 lds
MM v[48:51],a[20:23],a[148:151],v[48:51]
s_add_u32 s87, s89, s85
s_add_u32 s85, s85, 524288
MM v[56:59],a[20:23],a[156:159],v[56:59]
s_cmp_ge_u32 s85, 5242880
s_cselect_b32 s85, 0, s85
s_add_u32 m0, s96, 98432
MM v[56:59],a[16:19],a[152:155],v[56:59]
MM v[36:39],a[24:27],a[128:131],v[36:39]
TR a[16:17],v193 offset:0
TR a[18:19],v193 offset:256
MM v[36:39],a[28:31],a[132:135],v[36:39]
TR a[20:21],v193 offset:8224
TR a[22:23],v193 offset:8480
MM v[44:47],a[28:31],a[140:143],v[44:47]
buffer_load_dwordx4 v253, s[16:19], s87 offen offset:0 lds
MM v[44:47],a[24:27],a[136:139],v[44:47]
s_add_u32 m0, s96, 99456
MM v[52:55],a[24:27],a[144:147],v[52:55]
buffer_load_dwordx4 v254, s[16:19], s87 offen offset:0 lds
MM v[52:55],a[28:31],a[148:151],v[52:55]
s_add_u32 s87, s87, 65536
s_add_u32 m0, s96, 100488
MM v[60:63],a[28:31],a[156:159],v[60:63]
MM v[60:63],a[24:27],a[152:155],v[60:63]
MM v[64:67],a[32:35],a[128:131],v[64:67]
TR a[24:25],v193 offset:8
TR a[26:27],v193 offset:264
MM v[64:67],a[36:39],a[132:135],v[64:67]
TR a[28:29],v193 offset:8232
TR a[30:31],v193 offset:8488
MM v[72:75],a[36:39],a[140:143],v[72:75]
buffer_load_dwordx4 v253, s[16:19], s87 offen offset:0 lds
MM v[72:75],a[32:35],a[136:139],v[72:75]
s_add_u32 m0, s96, 101512
MM v[80:83],a[32:35],a[144:147],v[80:83]
buffer_load_dwordx4 v254, s[16:19], s87 offen offset:0 lds
MM v[80:83],a[36:39],a[148:151],v[80:83]
s_waitcnt vmcnt(16)
RD a[160:163],v222 offset:32768
MM v[88:91],a[36:39],a[156:159],v[88:91]
RD a[164:167],v223 offset:32768
MM v[88:91],a[32:35],a[152:155],v[88:91]
RD a[168:171],v217 offset:32768
MM v[68:71],a[40:43],a[128:131],v[68:71]
TR a[32:33],v194 offset:0
TR a[34:35],v194 offset:256
MM v[68:71],a[44:47],a[132:135],v[68:71]
TR a[36:37],v194 offset:8224
TR a[38:39],v194 offset:8480
MM v[76:79],a[44:47],a[140:143],v[76:79]
RD a[172:175],v218 offset:32768
MM v[76:79],a[40:43],a[136:139],v[76:79]
RD a[176:179],v222 offset:36864
MM v[84:87],a[40:43],a[144:147],v[84:87]
RD a[180:183],v223 offset:36864
MM v[84:87],a[44:47],a[148:151],v[84:87]
RD a[184:187],v217 offset:36864
MM v[92:95],a[44:47],a[156:159],v[92:95]
RD a[188:191],v218 offset:36864
MM v[92:95],a[40:43],a[152:155],v[92:95]
s_waitcnt lgkmcnt(15)
MM v[96:99],a[48:51],a[128:131],v[96:99]
TR a[40:41],v194 offset:8
TR a[42:43],v194 offset:264
MM v[96:99],a[52:55],a[132:135],v[96:99]
TR a[44:45],v194 offset:8232
TR a[46:47],v194 offset:8488
MM v[104:107],a[52:55],a[140:143],v[104:107]
s_waitcnt lgkmcnt(4)
s_add_u32 s88, s68, s86
s_add_u32 s86, s86, 128
MM v[104:107],a[48:51],a[136:139],v[104:107]
s_cmp_ge_u32 s86, 1280
s_cselect_b32 s86, 0, s86
s_add_u32 m0, s94, 32768
MM v[112:115],a[48:51],a[144:147],v[112:115]
s_add_u32 s95, s88, 0
buffer_load_dwordx4 v220, s[32:35], s95 offen offset:0 lds
MM v[112:115],a[52:55],a[148:151],v[112:115]
s_add_u32 s95, s88, 9216
MM v[120:123],a[52:55],a[156:159],v[120:123]
buffer_load_dwordx4 v221, s[32:35], s95 offen offset:1024 lds
MM v[120:123],a[48:51],a[152:155],v[120:123]
s_add_u32 s95, s88, 18432
MM v[100:103],a[56:59],a[128:131],v[100:103]
TR a[48:49],v195 offset:0
TR a[50:51],v195 offset:256
MM v[100:103],a[60:63],a[132:135],v[100:103]
TR a[52:53],v195 offset:8224
TR a[54:55],v195 offset:8480
MM v[108:111],a[60:63],a[140:143],v[108:111]
buffer_load_dwordx4 v220, s[32:35], s95 offen offset:2048 lds
MM v[108:111],a[56:59],a[136:139],v[108:111]
s_add_u32 s95, s88, 27648
MM v[116:119],a[56:59],a[144:147],v[116:119]
MM v[116:119],a[60:63],a[148:151],v[116:119]
MM v[124:127],a[60:63],a[156:159],v[124:127]
buffer_load_dwordx4 v221, s[32:35], s95 offen offset:3072 lds
MM v[124:127],a[56:59],a[152:155],v[124:127]
s_add_u32 m0, s94, 36864
s_add_u32 s95, s88, 40960
s_waitcnt vmcnt(16)
s_barrier
MM v[0:3],a[0:3],a[160:163],v[0:3]
TR a[56:57],v195 offset:8
TR a[58:59],v195 offset:264
MM v[0:3],a[4:7],a[164:167],v[0:3]
TR a[60:61],v195 offset:8232
TR a[62:63],v195 offset:8488
MM v[8:11],a[4:7],a[172:175],v[8:11]
buffer_load_dwordx4 v220, s[32:35], s95 offen offset:0 lds
MM v[8:11],a[0:3],a[168:171],v[8:11]
s_add_u32 s95, s88, 50176
MM v[16:19],a[0:3],a[176:179],v[16:19]
MM v[16:19],a[4:7],a[180:183],v[16:19]
MM v[24:27],a[4:7],a[188:191],v[24:27]
buffer_load_dwordx4 v221, s[32:35], s95 offen offset:1024 lds
MM v[24:27],a[0:3],a[184:187],v[24:27]
s_add_u32 s95, s88, 59392
MM v[4:7],a[8:11],a[160:163],v[4:7]
MM v[4:7],a[12:15],a[164:167],v[4:7]
MM v[12:15],a[12:15],a[172:175],v[12:15]
buffer_load_dwordx4 v220, s[32:35], s95 offen offset:2048 lds
MM v[12:15],a[8:11],a[168:171],v[12:15]
s_add_u32 s95, s88, 68608
MM v[20:23],a[8:11],a[176:179],v[20:23]
MM v[20:23],a[12:15],a[180:183],v[20:23]
MM v[28:31],a[12:15],a[188:191],v[28:31]
buffer_load_dwordx4 v221, s[32:35], s95 offen offset:3072 lds
MM v[28:31],a[8:11],a[184:187],v[28:31]
s_add_u32 s87, s89, s85
s_add_u32 s85, s85, 524288
v_smfmac_f32_16x16x64_bf16 v[184:187], v[236:239], a[0:7], v234
s_cmp_ge_u32 s85, 5242880
s_cselect_b32 s85, 0, s85
v_smfmac_f32_16x16x64_bf16 v[188:191], v[236:239], a[8:15], v234
s_add_u32 m0, s96, 114880
MM v[32:35],a[16:19],a[160:163],v[32:35]
TR a[0:1],v192 offset:16448
TR a[2:3],v192 offset:16704
MM v[32:35],a[20:23],a[164:167],v[32:35]
TR a[4:5],v192 offset:24672
TR a[6:7],v192 offset:24928
MM v[40:43],a[20:23],a[172:175],v[40:43]
TR a[8:9],v192 offset:16456
TR a[10:11],v192 offset:16712
buffer_load_dwordx4 v253, s[16:19], s87 offen offset:0 lds
MM v[40:43],a[16:19],a[168:171],v[40:43]
s_add_u32 m0, s96, 115904
TR a[12:13],v192 offset:24680
TR a[14:15],v192 offset:24936
MM v[48:51],a[16:19],a[176:179],v[48:51]
buffer_load_dwordx4 v254, s[16:19], s87 offen offset:0 lds
MM v[48:51],a[20:23],a[180:183],v[48:51]
s_add_u32 s87, s87, 65536
s_add_u32 m0, s96, 116936
MM v[56:59],a[20:23],a[188:191],v[56:59]
MM v[56:59],a[16:19],a[184:187],v[56:59]
MM v[36:39],a[24:27],a[160:163],v[36:39]
TR a[16:17],v193 offset:16448
TR a[18:19],v193 offset:16704
MM v[36:39],a[28:31],a[164:167],v[36:39]
TR a[20:21],v193 offset:24672
TR a[22:23],v193 offset:24928
MM v[44:47],a[28:31],a[172:175],v[44:47]
buffer_load_dwordx4 v253, s[16:19], s87 offen offset:0 lds
MM v[44:47],a[24:27],a[168:171],v[44:47]
s_add_u32 m0, s96, 117960
MM v[52:55],a[24:27],a[176:179],v[52:55]
buffer_load_dwordx4 v254, s[16:19], s87 offen offset:0 lds
MM v[52:55],a[28:31],a[180:183],v[52:55]
s_waitcnt vmcnt(16)
RD a[192:195],v222 offset:0
MM v[60:63],a[28:31],a[188:191],v[60:63]
RD a[196:199],v223 offset:0
MM v[60:63],a[24:27],a[184:187],v[60:63]
RD a[200:203],v217 offset:0
MM v[64:67],a[32:35],a[160:163],v[64:67]
TR a[24:25],v193 offset:16456
TR a[26:27],v193 offset:16712
MM v[64:67],a[36:39],a[164:167],v[64:67]
TR a[28:29],v193 offset:24680
TR a[30:31],v193 offset:24936
MM v[72:75],a[36:39],a[172:175],v[72:75]
RD a[204:207],v218 offset:0
MM v[72:75],a[32:35],a[168:171],v[72:75]
RD a[208:211],v222 offset:4096
MM v[80:83],a[32:35],a[176:179],v[80:83]
RD a[212:215],v223 offset:4096
MM v[80:83],a[36:39],a[180:183],v[80:83]
RD a[216:219],v217 offset:4096
MM v[88:91],a[36:39],a[188:191],v[88:91]
RD a[220:223],v218 offset:4096
MM v[88:91],a[32:35],a[184:187],v[88:91]
s_waitcnt lgkmcnt(15)
MM v[68:71],a[40:43],a[160:163],v[68:71]
TR a[32:33],v194 offset:16448
TR a[34:35],v194 offset:16704
MM v[68:71],a[44:47],a[164:167],v[68:71]
TR a[36:37],v194 offset:24672
TR a[38:39],v194 offset:24928
MM v[76:79],a[44:47],a[172:175],v[76:79]
s_waitcnt lgkmcnt(4)
s_add_u32 s88, s68, s86
s_add_u32 s86, s86, 128
MM v[76:79],a[40:43],a[168:171],v[76:79]
s_cmp_ge_u32 s86, 1280
s_cselect_b32 s86, 0, s86
s_add_u32 m0, s94, 0
MM v[84:87],a[40:43],a[176:179],v[84:87]
s_add_u32 s95, s88, 0
buffer_load_dwordx4 v220, s[32:35], s95 offen offset:0 lds
MM v[84:87],a[44:47],a[180:183],v[84:87]
s_add_u32 s95, s88, 9216
MM v[92:95],a[44:47],a[188:191],v[92:95]
buffer_load_dwordx4 v221, s[32:35], s95 offen offset:1024 lds
MM v[92:95],a[40:43],a[184:187],v[92:95]
s_add_u32 s95, s88, 18432
MM v[96:99],a[48:51],a[160:163],v[96:99]
TR a[40:41],v194 offset:16456
TR a[42:43],v194 offset:16712
MM v[96:99],a[52:55],a[164:167],v[96:99]
TR a[44:45],v194 offset:24680
TR a[46:47],v194 offset:24936
MM v[104:107],a[52:55],a[172:175],v[104:107]
buffer_load_dwordx4 v220, s[32:35], s95 offen offset:2048 lds
MM v[104:107],a[48:51],a[168:171],v[104:107]
s_add_u32 s95, s88, 27648
MM v[112:115],a[48:51],a[176:179],v[112:115]
MM v[112:115],a[52:55],a[180:183],v[112:115]
MM v[120:123],a[52:55],a[188:191],v[120:123]
buffer_load_dwordx4 v221, s[32:35], s95 offen offset:3072 lds
MM v[120:123],a[48:51],a[184:187],v[120:123]
s_add_u32 m0, s94, 4096
s_add_u32 s95, s88, 40960
MM v[100:103],a[56:59],a[160:163],v[100:103]
TR a[48:49],v195 offset:16448
TR a[50:51],v195 offset:16704
MM v[100:103],a[60:63],a[164:167],v[100:103]
TR a[52:53],v195 offset:24672
TR a[54:55],v195 offset:24928
MM v[108:111],a[60:63],a[172:175],v[108:111]
buffer_load_dwordx4 v220, s[32:35], s95 offen offset:0 lds
MM v[108:111],a[56:59],a[168:171],v[108:111]
s_add_u32 s95, s88, 50176
MM v[116:119],a[56:59],a[176:179],v[116:119]
MM v[116:119],a[60:63],a[180:183],v[116:119]
MM v[124:127],a[60:63],a[188:191],v[124:127]
buffer_load_dwordx4 v221, s[32:35], s95 offen offset:1024 lds
MM v[124:127],a[56:59],a[184:187],v[124:127]
s_add_u32 s95, s88, 59392
s_waitcnt vmcnt(18)
s_barrier
MM v[0:3],a[0:3],a[192:195],v[0:3]
TR a[56:57],v195 offset:16456
TR a[58:59],v195 offset:16712
MM v[0:3],a[4:7],a[196:199],v[0:3]
TR a[60:61],v195 offset:24680
TR a[62:63],v195 offset:24936
MM v[8:11],a[4:7],a[204:207],v[8:11]
buffer_load_dwordx4 v220, s[32:35], s95 offen offset:2048 lds
v_and_b32 v208, 240, v248
MM v[8:11],a[0:3],a[200:203],v[8:11]
s_add_u32 s95, s88, 68608
s_add_u32 s93, s93, 36864
MM v[16:19],a[0:3],a[208:211],v[16:19]
MM v[16:19],a[4:7],a[212:215],v[16:19]
MM v[24:27],a[4:7],a[220:223],v[24:27]
buffer_load_dwordx4 v221, s[32:35], s95 offen offset:3072 lds
MM v[24:27],a[0:3],a[216:219],v[24:27]
s_add_u32 s87, s89, s85
s_add_u32 s85, s85, 524288
MM v[4:7],a[8:11],a[192:195],v[4:7]
s_cmp_ge_u32 s85, 5242880
s_cselect_b32 s85, 0, s85
s_add_u32 m0, s96, 131328
MM v[4:7],a[12:15],a[196:199],v[4:7]
s_nop 0
LPG3_%=:
s_cbranch_execz LPG7_%=
MM v[12:15],a[12:15],a[204:207],v[12:15]
buffer_load_dwordx4 v253, s[16:19], s87 offen offset:0 lds
MM v[12:15],a[8:11],a[200:203],v[12:15]
s_add_u32 m0, s96, 132352
MM v[20:23],a[8:11],a[208:211],v[20:23]
MM v[20:23],a[12:15],a[212:215],v[20:23]
MM v[28:31],a[12:15],a[220:223],v[28:31]
buffer_load_dwordx4 v254, s[16:19], s87 offen offset:0 lds
MM v[28:31],a[8:11],a[216:219],v[28:31]
s_add_u32 s87, s87, 65536
s_add_u32 m0, s96, 133384
v_smfmac_f32_16x16x64_bf16 v[184:187], v[236:239], a[0:7], v234
v_smfmac_f32_16x16x64_bf16 v[188:191], v[236:239], a[8:15], v234
MM v[32:35],a[16:19],a[192:195],v[32:35]
TR a[0:1],v192 offset:32896
TR a[2:3],v192 offset:33152
MM v[32:35],a[20:23],a[196:199],v[32:35]
TR a[4:5],v192 offset:41120
TR a[6:7],v192 offset:41376
MM v[40:43],a[20:23],a[204:207],v[40:43]
TR a[8:9],v192 offset:32904
TR a[10:11],v192 offset:33160
buffer_load_dwordx4 v253, s[16:19], s87 offen offset:0 lds
MM v[40:43],a[16:19],a[200:203],v[40:43]
s_add_u32 m0, s96, 134408
TR a[12:13],v192 offset:41128
)ASM"
R"ASM(
TR a[14:15],v192 offset:41384
MM v[48:51],a[16:19],a[208:211],v[48:51]
buffer_load_dwordx4 v254, s[16:19], s87 offen offset:0 lds
MM v[48:51],a[20:23],a[212:215],v[48:51]
s_waitcnt vmcnt(16)
RD a[224:227],v222 offset:32768
MM v[56:59],a[20:23],a[220:223],v[56:59]
RD a[228:231],v223 offset:32768
MM v[56:59],a[16:19],a[216:219],v[56:59]
RD a[232:235],v217 offset:32768
MM v[36:39],a[24:27],a[192:195],v[36:39]
TR a[16:17],v193 offset:32896
TR a[18:19],v193 offset:33152
buffer_load_dwordx4 v[200:203], v208, s[20:23], s93 offen offset:0 sc1
MM v[36:39],a[28:31],a[196:199],v[36:39]
TR a[20:21],v193 offset:41120
TR a[22:23],v193 offset:41376
MM v[44:47],a[28:31],a[204:207],v[44:47]
RD a[236:239],v218 offset:32768
MM v[44:47],a[24:27],a[200:203],v[44:47]
RD a[240:243],v222 offset:36864
MM v[52:55],a[24:27],a[208:211],v[52:55]
buffer_load_dwordx4 v[204:207], v208, s[20:23], s93 offen offset:256 sc1
MM v[52:55],a[28:31],a[212:215],v[52:55]
RD a[244:247],v223 offset:36864
MM v[60:63],a[28:31],a[220:223],v[60:63]
RD a[248:251],v217 offset:36864
MM v[60:63],a[24:27],a[216:219],v[60:63]
s_waitcnt lgkmcnt(15)
RD a[252:255],v218 offset:36864
MM v[64:67],a[32:35],a[192:195],v[64:67]
TR a[24:25],v193 offset:32904
TR a[26:27],v193 offset:33160
MM v[64:67],a[36:39],a[196:199],v[64:67]
TR a[28:29],v193 offset:41128
TR a[30:31],v193 offset:41384
MM v[72:75],a[36:39],a[204:207],v[72:75]
MM v[72:75],a[32:35],a[200:203],v[72:75]
s_waitcnt lgkmcnt(4)
s_add_u32 s88, s68, s86
s_add_u32 s86, s86, 128
MM v[80:83],a[32:35],a[208:211],v[80:83]
s_cmp_ge_u32 s86, 1280
s_cselect_b32 s86, 0, s86
s_add_u32 m0, s94, 32768
MM v[80:83],a[36:39],a[212:215],v[80:83]
s_add_u32 s95, s88, 0
buffer_load_dwordx4 v220, s[32:35], s95 offen offset:0 lds
MM v[88:91],a[36:39],a[220:223],v[88:91]
s_add_u32 s95, s88, 9216
MM v[88:91],a[32:35],a[216:219],v[88:91]
buffer_load_dwordx4 v221, s[32:35], s95 offen offset:1024 lds
MM v[68:71],a[40:43],a[192:195],v[68:71]
s_add_u32 s95, s88, 18432
TR a[32:33],v194 offset:32896
TR a[34:35],v194 offset:33152
MM v[68:71],a[44:47],a[196:199],v[68:71]
TR a[36:37],v194 offset:41120
TR a[38:39],v194 offset:41376
MM v[76:79],a[44:47],a[204:207],v[76:79]
MM v[76:79],a[40:43],a[200:203],v[76:79]
buffer_load_dwordx4 v220, s[32:35], s95 offen offset:2048 lds
MM v[84:87],a[40:43],a[208:211],v[84:87]
s_add_u32 s95, s88, 27648
MM v[84:87],a[44:47],a[212:215],v[84:87]
MM v[92:95],a[44:47],a[220:223],v[92:95]
MM v[92:95],a[40:43],a[216:219],v[92:95]
buffer_load_dwordx4 v221, s[32:35], s95 offen offset:3072 lds
MM v[96:99],a[48:51],a[192:195],v[96:99]
s_waitcnt vmcnt(10)
TR a[40:41],v194 offset:32904
TR a[42:43],v194 offset:33160
MM v[96:99],a[52:55],a[196:199],v[96:99]
TR a[44:45],v194 offset:41128
TR a[46:47],v194 offset:41384
MM v[104:107],a[52:55],a[204:207],v[104:107]
RD a[128:131],v222 offset:0
MM v[104:107],a[48:51],a[200:203],v[104:107]
RD a[132:135],v223 offset:0
MM v[112:115],a[48:51],a[208:211],v[112:115]
RD a[136:139],v217 offset:0
MM v[112:115],a[52:55],a[212:215],v[112:115]
RD a[140:143],v218 offset:0
MM v[120:123],a[52:55],a[220:223],v[120:123]
RD a[144:147],v222 offset:4096
MM v[120:123],a[48:51],a[216:219],v[120:123]
RD a[148:151],v223 offset:4096
MM v[100:103],a[56:59],a[192:195],v[100:103]
TR a[48:49],v195 offset:32896
TR a[50:51],v195 offset:33152
MM v[100:103],a[60:63],a[196:199],v[100:103]
TR a[52:53],v195 offset:41120
TR a[54:55],v195 offset:41376
MM v[108:111],a[60:63],a[204:207],v[108:111]
RD a[152:155],v217 offset:4096
MM v[108:111],a[56:59],a[200:203],v[108:111]
RD a[156:159],v218 offset:4096
MM v[116:119],a[56:59],a[208:211],v[116:119]
s_add_u32 m0, s94, 36864
s_add_u32 s95, s88, 40960
MM v[116:119],a[60:63],a[212:215],v[116:119]
buffer_load_dwordx4 v220, s[32:35], s95 offen offset:0 lds
MM v[124:127],a[60:63],a[220:223],v[124:127]
s_add_u32 s95, s88, 50176
MM v[124:127],a[56:59],a[216:219],v[124:127]
buffer_load_dwordx4 v221, s[32:35], s95 offen offset:1024 lds
s_add_u32 s95, s88, 59392
buffer_load_dwordx4 v220, s[32:35], s95 offen offset:2048 lds
s_add_u32 s95, s88, 68608
buffer_load_dwordx4 v221, s[32:35], s95 offen offset:3072 lds
s_waitcnt lgkmcnt(0)
s_waitcnt vmcnt(10)
s_barrier
MM v[0:3],a[0:3],a[224:227],v[0:3]
s_add_u32 s77, s64, 0
TR a[56:57],v195 offset:32904
TR a[58:59],v195 offset:33160
MM v[0:3],a[4:7],a[228:231],v[0:3]
s_add_u32 m0, s65, 0
TR a[60:61],v195 offset:41128
TR a[62:63],v195 offset:41384
MM v[8:11],a[4:7],a[236:239],v[8:11]
s_waitcnt vmcnt(8)
TR a[64:65],v196 offset:0
TR a[66:67],v196 offset:256
MM v[8:11],a[0:3],a[232:235],v[8:11]
s_lshl_b32 s76, %[swave], 7
v_lshrrev_b32 v253, 8, v248
v_lshlrev_b32 v253, 5, v253
MM v[16:19],a[0:3],a[240:243],v[16:19]
s_add_u32 s76, s76, %[slds]
TR a[68:69],v196 offset:8224
TR a[70:71],v196 offset:8480
v_xor_b32 v201, s69, v201
MM v[16:19],a[4:7],a[244:247],v[16:19]
buffer_load_dwordx4 v228, s[8:11], s77 offen offset:0 lds
s_mov_b64 exec, s[54:55]
buffer_store_dwordx2 v[170:171], off, s[20:23], s80 sc1
s_mov_b64 exec, -1
v_add_u32 v253, s76, v253
v_add_u32 v253, 151552, v253
MM v[24:27],a[4:7],a[252:255],v[24:27]
TR a[72:73],v196 offset:16448
TR a[74:75],v196 offset:16704
MM v[24:27],a[0:3],a[248:251],v[24:27]
TR a[76:77],v196 offset:24672
TR a[78:79],v196 offset:24928
v_xor_b32 v203, s69, v203
MM v[4:7],a[8:11],a[224:227],v[4:7]
TR a[80:81],v196 offset:8
TR a[82:83],v196 offset:264
MM v[4:7],a[12:15],a[228:231],v[4:7]
TR a[84:85],v196 offset:8232
TR a[86:87],v196 offset:8488
buffer_load_dwordx4 v229, s[8:11], s77 offen offset:1024 lds
MM v[12:15],a[12:15],a[236:239],v[12:15]
TR a[88:89],v196 offset:16456
TR a[90:91],v196 offset:16712
v_xor_b32 v205, s69, v205
MM v[12:15],a[8:11],a[232:235],v[12:15]
TR a[92:93],v196 offset:24680
TR a[94:95],v196 offset:24936
MM v[20:23],a[8:11],a[240:243],v[20:23]
TR a[96:97],v197 offset:0
TR a[98:99],v197 offset:256
MM v[20:23],a[12:15],a[244:247],v[20:23]
TR a[100:101],v197 offset:8224
TR a[102:103],v197 offset:8480
buffer_load_dwordx4 v230, s[8:11], s77 offen offset:2048 lds
v_xor_b32 v207, s69, v207
MM v[28:31],a[12:15],a[252:255],v[28:31]
TR a[104:105],v197 offset:16448
TR a[106:107],v197 offset:16704
MM v[28:31],a[8:11],a[248:251],v[28:31]
TR a[108:109],v197 offset:24672
TR a[110:111],v197 offset:24928
v_smfmac_f32_16x16x64_bf16 v[184:187], v[236:239], a[0:7], v234
TR a[112:113],v197 offset:8
v_or3_b32 v209, v201, v203, v205
v_smfmac_f32_16x16x64_bf16 v[188:191], v[236:239], a[8:15], v234
TR a[114:115],v197 offset:264
buffer_load_dwordx4 v231, s[8:11], s77 offen offset:3072 lds
MM v[32:35],a[16:19],a[224:227],v[32:35]
s_add_u32 s77, s64, 16384
TR a[116:117],v197 offset:8232
TR a[118:119],v197 offset:8488
MM v[32:35],a[20:23],a[228:231],v[32:35]
s_add_u32 m0, s65, 16384
TR a[120:121],v197 offset:16456
TR a[122:123],v197 offset:16712
v_or_b32 v209, v209, v207
MM v[40:43],a[20:23],a[236:239],v[40:43]
TR a[124:125],v197 offset:24680
TR a[126:127],v197 offset:24936
MM v[40:43],a[16:19],a[232:235],v[40:43]
TR a[0:1],v198 offset:0
TR a[2:3],v198 offset:256
MM v[48:51],a[16:19],a[240:243],v[48:51]
TR a[4:5],v198 offset:8224
TR a[6:7],v198 offset:8480
v_cmp_ne_u32 vcc, 0, v209
s_cbranch_vccnz LRG1o_%=
LRG1ob_%=:
MM v[48:51],a[20:23],a[244:247],v[48:51]
TR a[8:9],v198 offset:16448
TR a[10:11],v198 offset:16704
buffer_load_dwordx4 v228, s[8:11], s77 offen offset:0 lds
MM v[56:59],a[20:23],a[252:255],v[56:59]
TR a[12:13],v198 offset:24672
TR a[14:15],v198 offset:24928
MM v[56:59],a[16:19],a[248:251],v[56:59]
s_bitcmp1_b32 %[sb], 0
s_cselect_b64 exec, -1, 0
v_swap_b32 v200, v202
v_swap_b32 v204, v206
s_mov_b64 exec, -1
MM v[36:39],a[24:27],a[224:227],v[36:39]
v_lshlrev_b32 v240, 16, v200
v_and_b32 v200, 0xffff0000, v200
MM v[36:39],a[28:31],a[228:231],v[36:39]
v_rcp_f32 v246, v200
MM v[44:47],a[28:31],a[236:239],v[44:47]
v_xor_b32 v240, 0x80000000, v240
v_mul_f32 v246, s72, v246
MM v[44:47],a[24:27],a[232:235],v[44:47]
v_lshlrev_b32 v241, 16, v202
v_and_b32 v202, 0xffff0000, v202
MM v[52:55],a[24:27],a[240:243],v[52:55]
v_rcp_f32 v247, v202
MM v[52:55],a[28:31],a[244:247],v[52:55]
buffer_load_dwordx4 v229, s[8:11], s77 offen offset:1024 lds
MM v[60:63],a[28:31],a[252:255],v[60:63]
v_xor_b32 v241, 0x80000000, v241
v_mul_f32 v247, s72, v247
MM v[60:63],a[24:27],a[248:251],v[60:63]
v_lshlrev_b32 v242, 16, v204
v_and_b32 v204, 0xffff0000, v204
MM v[64:67],a[32:35],a[224:227],v[64:67]
TR a[16:17],v198 offset:8
TR a[18:19],v198 offset:264
MM v[64:67],a[36:39],a[228:231],v[64:67]
TR a[20:21],v198 offset:8232
TR a[22:23],v198 offset:8488
v_rcp_f32 v244, v204
MM v[72:75],a[36:39],a[236:239],v[72:75]
TR a[24:25],v198 offset:16456
TR a[26:27],v198 offset:16712
MM v[72:75],a[32:35],a[232:235],v[72:75]
TR a[28:29],v198 offset:24680
TR a[30:31],v198 offset:24936
MM v[80:83],a[32:35],a[240:243],v[80:83]
v_xor_b32 v242, 0x80000000, v242
v_mul_f32 v244, s72, v244
MM v[80:83],a[36:39],a[244:247],v[80:83]
v_lshlrev_b32 v243, 16, v206
v_and_b32 v206, 0xffff0000, v206
MM v[88:91],a[36:39],a[252:255],v[88:91]
v_rcp_f32 v245, v206
MM v[88:91],a[32:35],a[248:251],v[88:91]
s_waitcnt lgkmcnt(15)
buffer_load_dwordx4 v230, s[8:11], s77 offen offset:2048 lds
v_smfmac_f32_16x16x64_bf16 v[184:187], v[236:239], a[64:71], v234
v_xor_b32 v243, 0x80000000, v243
v_smfmac_f32_16x16x64_bf16 v[184:187], v[236:239], a[72:79], v234
v_mul_f32 v245, s72, v245
v_smfmac_f32_16x16x64_bf16 v[188:191], v[236:239], a[80:87], v234
v_smfmac_f32_16x16x64_bf16 v[188:191], v[236:239], a[88:95], v234
MM v[68:71],a[40:43],a[224:227],v[68:71]
v_bfe_u32 v183, v248, 6, 2
v_lshlrev_b32 v183, 5, v183
MM v[68:71],a[44:47],a[228:231],v[68:71]
v_bfe_u32 v182, v248, 4, 2
v_lshl_add_u32 v183, v182, 2, v183
MM v[76:79],a[44:47],a[236:239],v[76:79]
v_add_u32 v183, s76, v183
v_add_u32 v183, 151552, v183
MM v[76:79],a[40:43],a[232:235],v[76:79]
MM v[84:87],a[40:43],a[240:243],v[84:87]
MM v[84:87],a[44:47],a[244:247],v[84:87]
buffer_load_dwordx4 v231, s[8:11], s77 offen offset:3072 lds
MM v[92:95],a[44:47],a[252:255],v[92:95]
s_waitcnt vmcnt(9)
RD a[160:163],v222 offset:32768
MM v[92:95],a[40:43],a[248:251],v[92:95]
v_add_f32 v184, v184, v185
v_add_f32 v188, v188, v189
MM v[96:99],a[48:51],a[224:227],v[96:99]
TR a[32:33],v199 offset:0
TR a[34:35],v199 offset:256
MM v[96:99],a[52:55],a[228:231],v[96:99]
RD a[164:167],v223 offset:32768
ds_write_b32 v183, v184 offset:0
ds_write_b32 v183, v188 offset:16
MM v[104:107],a[52:55],a[236:239],v[104:107]
TR a[36:37],v199 offset:8224
TR a[38:39],v199 offset:8480
MM v[104:107],a[48:51],a[232:235],v[104:107]
TR a[40:41],v199 offset:16448
TR a[42:43],v199 offset:16704
MM v[112:115],a[48:51],a[240:243],v[112:115]
TR a[44:45],v199 offset:24672
TR a[46:47],v199 offset:24928
RD a[168:171],v217 offset:32768
MM v[112:115],a[52:55],a[244:247],v[112:115]
RD a[172:175],v218 offset:32768
MM v[120:123],a[52:55],a[252:255],v[120:123]
RD a[176:179],v222 offset:36864
MM v[120:123],a[48:51],a[248:251],v[120:123]
RD a[180:183],v223 offset:36864
MM v[100:103],a[56:59],a[224:227],v[100:103]
RD a[184:187],v217 offset:36864
MM v[100:103],a[60:63],a[228:231],v[100:103]
RD a[188:191],v218 offset:36864
MM v[108:111],a[60:63],a[236:239],v[108:111]
s_add_u32 s77, s64, 0
s_add_u32 s81, s77, 896
s_add_u32 m0, s65, 65536
MM v[108:111],a[56:59],a[232:235],v[108:111]
buffer_load_dwordx4 v235, s[12:15], s81 offen offset:0 lds
MM v[116:119],a[56:59],a[240:243],v[116:119]
MM v[116:119],a[60:63],a[244:247],v[116:119]
MM v[124:127],a[60:63],a[252:255],v[124:127]
MM v[124:127],a[56:59],a[248:251],v[124:127]
s_waitcnt lgkmcnt(0)
s_barrier
MM v[0:3],a[64:67],a[128:131],v[0:3]
s_mov_b32 s56, 0x000f000f
TR a[48:49],v199 offset:8
TR a[50:51],v199 offset:264
MM v[0:3],a[68:71],a[132:135],v[0:3]
s_mov_b32 s57, s56
TR a[52:53],v199 offset:8232
TR a[54:55],v199 offset:8488
MM v[0:3],a[72:75],a[160:163],v[0:3]
s_lshl_b64 s[58:59], s[56:57], 4
TR a[56:57],v199 offset:16456
TR a[58:59],v199 offset:16712
MM v[0:3],a[76:79],a[164:167],v[0:3]
TR a[60:61],v199 offset:24680
TR a[62:63],v199 offset:24936
buffer_load_dwordx4 v235, s[12:15], s77 offen offset:1024 lds
MM v[8:11],a[76:79],a[172:175],v[8:11]
)ASM"
R"ASM(
v_xor_b32 v151, 0, v253
v_xor_b32 v143, 0, v253
MM v[8:11],a[72:75],a[168:171],v[8:11]
RD v[144:147],v151 offset:0
RD v[148:151],v151 offset:16
MM v[8:11],a[68:71],a[140:143],v[8:11]
RD v[136:139],v143 offset:0
RD v[140:143],v143 offset:16
MM v[8:11],a[64:67],a[136:139],v[8:11]
MM v[4:7],a[80:83],a[128:131],v[4:7]
MM v[4:7],a[84:87],a[132:135],v[4:7]
s_waitcnt lgkmcnt(2)
MM v[4:7],a[88:91],a[160:163],v[4:7]
s_waitcnt lgkmcnt(0)
MM v[4:7],a[92:95],a[164:167],v[4:7]
buffer_load_dwordx4 v235, s[12:15], s81 offen offset:2048 lds
MM v[12:15],a[92:95],a[172:175],v[12:15]
MM v[12:15],a[88:91],a[168:171],v[12:15]
MM v[12:15],a[84:87],a[140:143],v[12:15]
MM v[12:15],a[80:83],a[136:139],v[12:15]
MM v[32:35],a[96:99],a[128:131],v[32:35]
MM v[32:35],a[100:103],a[132:135],v[32:35]
MM v[32:35],a[104:107],a[160:163],v[32:35]
MM v[32:35],a[108:111],a[164:167],v[32:35]
v_pk_fma_f32 v[0:1], v[240:241], v[144:145], v[0:1] op_sel:[0,0,0] op_sel_hi:[0,1,1]
v_pk_fma_f32 v[2:3], v[240:241], v[146:147], v[2:3] op_sel:[0,0,0] op_sel_hi:[0,1,1]
v_pk_fma_f32 v[4:5], v[240:241], v[148:149], v[4:5] op_sel:[0,0,0] op_sel_hi:[0,1,1]
v_pk_fma_f32 v[6:7], v[240:241], v[150:151], v[6:7] op_sel:[0,0,0] op_sel_hi:[0,1,1]
v_pk_mul_f32 v[0:1], v[246:247], v[0:1] op_sel:[0,0] op_sel_hi:[0,1]
v_pk_mul_f32 v[2:3], v[246:247], v[2:3] op_sel:[0,0] op_sel_hi:[0,1]
v_pk_mul_f32 v[4:5], v[246:247], v[4:5] op_sel:[0,0] op_sel_hi:[0,1]
v_pk_mul_f32 v[6:7], v[246:247], v[6:7] op_sel:[0,0] op_sel_hi:[0,1]
MM v[40:43],a[108:111],a[172:175],v[40:43]
CV v160,v0,v1
CV v161,v2,v3
MM v[40:43],a[104:107],a[168:171],v[40:43]
buffer_load_dwordx4 v235, s[12:15], s77 offen offset:3072 lds
MM v[40:43],a[100:103],a[140:143],v[40:43]
s_add_u32 s77, s64, 32768
s_add_u32 m0, s65, 32768
CV v162,v4,v5
MM v[40:43],a[96:99],a[136:139],v[40:43]
CV v163,v6,v7
MM v[36:39],a[112:115],a[128:131],v[36:39]
v_pk_fma_f32 v[8:9], v[240:241], v[144:145], v[8:9] op_sel:[1,0,0] op_sel_hi:[1,1,1]
v_pk_fma_f32 v[10:11], v[240:241], v[146:147], v[10:11] op_sel:[1,0,0] op_sel_hi:[1,1,1]
v_pk_fma_f32 v[12:13], v[240:241], v[148:149], v[12:13] op_sel:[1,0,0] op_sel_hi:[1,1,1]
v_pk_fma_f32 v[14:15], v[240:241], v[150:151], v[14:15] op_sel:[1,0,0] op_sel_hi:[1,1,1]
v_pk_mul_f32 v[8:9], v[246:247], v[8:9] op_sel:[1,0] op_sel_hi:[1,1]
v_pk_mul_f32 v[10:11], v[246:247], v[10:11] op_sel:[1,0] op_sel_hi:[1,1]
v_pk_mul_f32 v[12:13], v[246:247], v[12:13] op_sel:[1,0] op_sel_hi:[1,1]
v_pk_mul_f32 v[14:15], v[246:247], v[14:15] op_sel:[1,0] op_sel_hi:[1,1]
MM v[36:39],a[116:119],a[132:135],v[36:39]
CV v164,v8,v9
CV v165,v10,v11
MM v[36:39],a[120:123],a[160:163],v[36:39]
CV v166,v12,v13
CV v167,v14,v15
MM v[36:39],a[124:127],a[164:167],v[36:39]
v_xor_b32 v159, 128, v253
MM v[44:47],a[124:127],a[172:175],v[44:47]
RD v[152:155],v159 offset:0
RD v[156:159],v159 offset:16
MM v[44:47],a[120:123],a[168:171],v[44:47]
MM v[44:47],a[116:119],a[140:143],v[44:47]
MM v[44:47],a[112:115],a[136:139],v[44:47]
buffer_load_dwordx4 v228, s[8:11], s77 offen offset:0 lds
MM v[64:67],a[0:3],a[128:131],v[64:67]
s_waitcnt lgkmcnt(0)
MM v[64:67],a[4:7],a[132:135],v[64:67]
MM v[64:67],a[8:11],a[160:163],v[64:67]
MM v[64:67],a[12:15],a[164:167],v[64:67]
v_pk_fma_f32 v[32:33], v[240:241], v[152:153], v[32:33] op_sel:[0,0,0] op_sel_hi:[0,1,1]
v_pk_fma_f32 v[34:35], v[240:241], v[154:155], v[34:35] op_sel:[0,0,0] op_sel_hi:[0,1,1]
v_pk_fma_f32 v[36:37], v[240:241], v[156:157], v[36:37] op_sel:[0,0,0] op_sel_hi:[0,1,1]
v_pk_fma_f32 v[38:39], v[240:241], v[158:159], v[38:39] op_sel:[0,0,0] op_sel_hi:[0,1,1]
v_pk_mul_f32 v[32:33], v[246:247], v[32:33] op_sel:[0,0] op_sel_hi:[0,1]
v_pk_mul_f32 v[34:35], v[246:247], v[34:35] op_sel:[0,0] op_sel_hi:[0,1]
v_pk_mul_f32 v[36:37], v[246:247], v[36:37] op_sel:[0,0] op_sel_hi:[0,1]
v_pk_mul_f32 v[38:39], v[246:247], v[38:39] op_sel:[0,0] op_sel_hi:[0,1]
MM v[72:75],a[12:15],a[172:175],v[72:75]
CV v176,v32,v33
CV v177,v34,v35
MM v[72:75],a[8:11],a[168:171],v[72:75]
CV v178,v36,v37
CV v179,v38,v39
MM v[72:75],a[4:7],a[140:143],v[72:75]
v_pk_fma_f32 v[40:41], v[240:241], v[152:153], v[40:41] op_sel:[1,0,0] op_sel_hi:[1,1,1]
v_pk_fma_f32 v[42:43], v[240:241], v[154:155], v[42:43] op_sel:[1,0,0] op_sel_hi:[1,1,1]
v_pk_fma_f32 v[44:45], v[240:241], v[156:157], v[44:45] op_sel:[1,0,0] op_sel_hi:[1,1,1]
v_pk_fma_f32 v[46:47], v[240:241], v[158:159], v[46:47] op_sel:[1,0,0] op_sel_hi:[1,1,1]
v_pk_mul_f32 v[40:41], v[246:247], v[40:41] op_sel:[1,0] op_sel_hi:[1,1]
v_pk_mul_f32 v[42:43], v[246:247], v[42:43] op_sel:[1,0] op_sel_hi:[1,1]
v_pk_mul_f32 v[44:45], v[246:247], v[44:45] op_sel:[1,0] op_sel_hi:[1,1]
v_pk_mul_f32 v[46:47], v[246:247], v[46:47] op_sel:[1,0] op_sel_hi:[1,1]
MM v[72:75],a[0:3],a[136:139],v[72:75]
CV v180,v40,v41
CV v181,v42,v43
MM v[68:71],a[16:19],a[128:131],v[68:71]
CV v182,v44,v45
CV v183,v46,v47
MM v[68:71],a[20:23],a[132:135],v[68:71]
buffer_load_dwordx4 v229, s[8:11], s77 offen offset:1024 lds
MM v[68:71],a[24:27],a[160:163],v[68:71]
v_xor_b32 v151, 256, v253
MM v[68:71],a[28:31],a[164:167],v[68:71]
RD v[144:147],v151 offset:0
RD v[148:151],v151 offset:16
MM v[76:79],a[28:31],a[172:175],v[76:79]
MM v[76:79],a[24:27],a[168:171],v[76:79]
MM v[76:79],a[20:23],a[140:143],v[76:79]
MM v[76:79],a[16:19],a[136:139],v[76:79]
s_waitcnt lgkmcnt(0)
MM v[96:99],a[32:35],a[128:131],v[96:99]
MM v[96:99],a[36:39],a[132:135],v[96:99]
MM v[96:99],a[40:43],a[160:163],v[96:99]
MM v[96:99],a[44:47],a[164:167],v[96:99]
buffer_load_dwordx4 v230, s[8:11], s77 offen offset:2048 lds
MM v[104:107],a[44:47],a[172:175],v[104:107]
v_pk_fma_f32 v[64:65], v[240:241], v[144:145], v[64:65] op_sel:[0,0,0] op_sel_hi:[0,1,1]
v_pk_fma_f32 v[66:67], v[240:241], v[146:147], v[66:67] op_sel:[0,0,0] op_sel_hi:[0,1,1]
v_pk_fma_f32 v[68:69], v[240:241], v[148:149], v[68:69] op_sel:[0,0,0] op_sel_hi:[0,1,1]
v_pk_fma_f32 v[70:71], v[240:241], v[150:151], v[70:71] op_sel:[0,0,0] op_sel_hi:[0,1,1]
v_pk_mul_f32 v[64:65], v[246:247], v[64:65] op_sel:[0,0] op_sel_hi:[0,1]
v_pk_mul_f32 v[66:67], v[246:247], v[66:67] op_sel:[0,0] op_sel_hi:[0,1]
v_pk_mul_f32 v[68:69], v[246:247], v[68:69] op_sel:[0,0] op_sel_hi:[0,1]
v_pk_mul_f32 v[70:71], v[246:247], v[70:71] op_sel:[0,0] op_sel_hi:[0,1]
MM v[104:107],a[40:43],a[168:171],v[104:107]
CV v192,v64,v65
CV v193,v66,v67
MM v[104:107],a[36:39],a[140:143],v[104:107]
CV v194,v68,v69
CV v195,v70,v71
MM v[104:107],a[32:35],a[136:139],v[104:107]
v_pk_fma_f32 v[72:73], v[240:241], v[144:145], v[72:73] op_sel:[1,0,0] op_sel_hi:[1,1,1]
v_pk_fma_f32 v[74:75], v[240:241], v[146:147], v[74:75] op_sel:[1,0,0] op_sel_hi:[1,1,1]
v_pk_fma_f32 v[76:77], v[240:241], v[148:149], v[76:77] op_sel:[1,0,0] op_sel_hi:[1,1,1]
v_pk_fma_f32 v[78:79], v[240:241], v[150:151], v[78:79] op_sel:[1,0,0] op_sel_hi:[1,1,1]
v_pk_mul_f32 v[72:73], v[246:247], v[72:73] op_sel:[1,0] op_sel_hi:[1,1]
v_pk_mul_f32 v[74:75], v[246:247], v[74:75] op_sel:[1,0] op_sel_hi:[1,1]
v_pk_mul_f32 v[76:77], v[246:247], v[76:77] op_sel:[1,0] op_sel_hi:[1,1]
v_pk_mul_f32 v[78:79], v[246:247], v[78:79] op_sel:[1,0] op_sel_hi:[1,1]
MM v[100:103],a[48:51],a[128:131],v[100:103]
CV v196,v72,v73
CV v197,v74,v75
MM v[100:103],a[52:55],a[132:135],v[100:103]
CV v198,v76,v77
CV v199,v78,v79
MM v[100:103],a[56:59],a[160:163],v[100:103]
v_xor_b32 v159, 384, v253
MM v[100:103],a[60:63],a[164:167],v[100:103]
RD v[152:155],v159 offset:0
RD v[156:159],v159 offset:16
MM v[108:111],a[60:63],a[172:175],v[108:111]
MM v[108:111],a[56:59],a[168:171],v[108:111]
buffer_load_dwordx4 v231, s[8:11], s77 offen offset:3072 lds
MM v[108:111],a[52:55],a[140:143],v[108:111]
s_add_u32 s77, s64, 16384
s_add_u32 s81, s77, 896
s_add_u32 m0, s65, 81920
MM v[108:111],a[48:51],a[136:139],v[108:111]
s_waitcnt lgkmcnt(0)
MM v[16:19],a[64:67],a[144:147],v[16:19]
MM v[16:19],a[68:71],a[148:151],v[16:19]
MM v[16:19],a[72:75],a[176:179],v[16:19]
MM v[16:19],a[76:79],a[180:183],v[16:19]
v_pk_fma_f32 v[96:97], v[240:241], v[152:153], v[96:97] op_sel:[0,0,0] op_sel_hi:[0,1,1]
v_pk_fma_f32 v[98:99], v[240:241], v[154:155], v[98:99] op_sel:[0,0,0] op_sel_hi:[0,1,1]
v_pk_fma_f32 v[100:101], v[240:241], v[156:157], v[100:101] op_sel:[0,0,0] op_sel_hi:[0,1,1]
v_pk_fma_f32 v[102:103], v[240:241], v[158:159], v[102:103] op_sel:[0,0,0] op_sel_hi:[0,1,1]
v_pk_mul_f32 v[96:97], v[246:247], v[96:97] op_sel:[0,0] op_sel_hi:[0,1]
v_pk_mul_f32 v[98:99], v[246:247], v[98:99] op_sel:[0,0] op_sel_hi:[0,1]
v_pk_mul_f32 v[100:101], v[246:247], v[100:101] op_sel:[0,0] op_sel_hi:[0,1]
v_pk_mul_f32 v[102:103], v[246:247], v[102:103] op_sel:[0,0] op_sel_hi:[0,1]
MM v[24:27],a[76:79],a[188:191],v[24:27]
s_nop 0
LPG4_%=:
s_cbranch_execz LPG8_%=
CV v208,v96,v97
CV v209,v98,v99
MM v[24:27],a[72:75],a[184:187],v[24:27]
CV v210,v100,v101
CV v211,v102,v103
MM v[24:27],a[68:71],a[156:159],v[24:27]
s_waitcnt vmcnt(12)
s_barrier
RD a[192:195],v224 offset:0
MM v[24:27],a[64:67],a[152:155],v[24:27]
buffer_load_dwordx4 v235, s[12:15], s81 offen offset:0 lds
MM v[20:23],a[80:83],a[144:147],v[20:23]
RD a[196:199],v225 offset:0
RD a[200:203],v226 offset:0
MM v[20:23],a[84:87],a[148:151],v[20:23]
RD a[204:207],v227 offset:0
RD a[208:211],v224 offset:4096
MM v[20:23],a[88:91],a[176:179],v[20:23]
RD a[212:215],v225 offset:4096
RD a[216:219],v226 offset:4096
MM v[20:23],a[92:95],a[180:183],v[20:23]
RD a[220:223],v227 offset:4096
RD a[224:227],v224 offset:8192
MM v[28:31],a[92:95],a[188:191],v[28:31]
RD a[228:231],v225 offset:8192
RD a[232:235],v226 offset:8192
MM v[28:31],a[88:91],a[184:187],v[28:31]
RD a[236:239],v227 offset:8192
RD a[240:243],v224 offset:12288
MM v[28:31],a[84:87],a[156:159],v[28:31]
RD a[244:247],v225 offset:12288
RD a[248:251],v226 offset:12288
MM v[28:31],a[80:83],a[152:155],v[28:31]
RD a[252:255],v227 offset:12288
MM v[48:51],a[96:99],a[144:147],v[48:51]
v_pk_fma_f32 v[104:105], v[240:241], v[152:153], v[104:105] op_sel:[1,0,0] op_sel_hi:[1,1,1]
v_pk_fma_f32 v[106:107], v[240:241], v[154:155], v[106:107] op_sel:[1,0,0] op_sel_hi:[1,1,1]
v_pk_fma_f32 v[108:109], v[240:241], v[156:157], v[108:109] op_sel:[1,0,0] op_sel_hi:[1,1,1]
v_pk_fma_f32 v[110:111], v[240:241], v[158:159], v[110:111] op_sel:[1,0,0] op_sel_hi:[1,1,1]
v_pk_mul_f32 v[104:105], v[246:247], v[104:105] op_sel:[1,0] op_sel_hi:[1,1]
v_pk_mul_f32 v[106:107], v[246:247], v[106:107] op_sel:[1,0] op_sel_hi:[1,1]
v_pk_mul_f32 v[108:109], v[246:247], v[108:109] op_sel:[1,0] op_sel_hi:[1,1]
v_pk_mul_f32 v[110:111], v[246:247], v[110:111] op_sel:[1,0] op_sel_hi:[1,1]
MM v[48:51],a[100:103],a[148:151],v[48:51]
buffer_load_dwordx4 v235, s[12:15], s77 offen offset:1024 lds
MM v[48:51],a[104:107],a[176:179],v[48:51]
CV v212,v104,v105
CV v213,v106,v107
MM v[48:51],a[108:111],a[180:183],v[48:51]
CV v214,v108,v109
CV v215,v110,v111
MM v[56:59],a[108:111],a[188:191],v[56:59]
v_mov_b32 v247, 0
v_mov_b32 v236, 0
MM v[56:59],a[104:107],a[184:187],v[56:59]
v_mov_b32 v237, 0
v_cndmask_b32 v144, 0, v238, s[56:57]
MM v[56:59],a[100:103],a[156:159],v[56:59]
v_cndmask_b32 v145, 0, v238, s[56:57]
v_cndmask_b32 v146, 0, v238, s[56:57]
MM v[56:59],a[96:99],a[152:155],v[56:59]
v_cndmask_b32 v147, 0, v238, s[56:57]
v_cndmask_b32 v148, 0, v238, s[58:59]
MM v[52:55],a[112:115],a[144:147],v[52:55]
v_cndmask_b32 v149, 0, v238, s[58:59]
v_cndmask_b32 v150, 0, v238, s[58:59]
MM v[52:55],a[116:119],a[148:151],v[52:55]
v_pk_fma_f32 v[16:17], v[242:243], v[136:137], v[16:17] op_sel:[0,0,0] op_sel_hi:[0,1,1]
v_pk_fma_f32 v[18:19], v[242:243], v[138:139], v[18:19] op_sel:[0,0,0] op_sel_hi:[0,1,1]
v_pk_fma_f32 v[20:21], v[242:243], v[140:141], v[20:21] op_sel:[0,0,0] op_sel_hi:[0,1,1]
v_pk_fma_f32 v[22:23], v[242:243], v[142:143], v[22:23] op_sel:[0,0,0] op_sel_hi:[0,1,1]
v_pk_mul_f32 v[16:17], v[244:245], v[16:17] op_sel:[0,0] op_sel_hi:[0,1]
v_pk_mul_f32 v[18:19], v[244:245], v[18:19] op_sel:[0,0] op_sel_hi:[0,1]
v_pk_mul_f32 v[20:21], v[244:245], v[20:21] op_sel:[0,0] op_sel_hi:[0,1]
v_pk_mul_f32 v[22:23], v[244:245], v[22:23] op_sel:[0,0] op_sel_hi:[0,1]
MM v[52:55],a[120:123],a[176:179],v[52:55]
v_cndmask_b32 v151, 0, v238, s[58:59]
CV v168,v16,v17
)ASM"
R"ASM(
MM v[52:55],a[124:127],a[180:183],v[52:55]
buffer_load_dwordx4 v235, s[12:15], s81 offen offset:2048 lds
s_waitcnt lgkmcnt(0)
MM v[0:3],a[192:195],v[160:163],0
CV v169,v18,v19
CV v170,v20,v21
MM v[0:3],a[196:199],v[176:179],v[0:3]
CV v171,v22,v23
MM v[0:3],a[200:203],v[192:195],v[0:3]
v_pk_fma_f32 v[24:25], v[242:243], v[136:137], v[24:25] op_sel:[1,0,0] op_sel_hi:[1,1,1]
v_pk_fma_f32 v[26:27], v[242:243], v[138:139], v[26:27] op_sel:[1,0,0] op_sel_hi:[1,1,1]
v_pk_fma_f32 v[28:29], v[242:243], v[140:141], v[28:29] op_sel:[1,0,0] op_sel_hi:[1,1,1]
v_pk_fma_f32 v[30:31], v[242:243], v[142:143], v[30:31] op_sel:[1,0,0] op_sel_hi:[1,1,1]
v_pk_mul_f32 v[24:25], v[244:245], v[24:25] op_sel:[1,0] op_sel_hi:[1,1]
v_pk_mul_f32 v[26:27], v[244:245], v[26:27] op_sel:[1,0] op_sel_hi:[1,1]
v_pk_mul_f32 v[28:29], v[244:245], v[28:29] op_sel:[1,0] op_sel_hi:[1,1]
v_pk_mul_f32 v[30:31], v[244:245], v[30:31] op_sel:[1,0] op_sel_hi:[1,1]
MM v[0:3],a[204:207],v[208:211],v[0:3]
CV v172,v24,v25
CV v173,v26,v27
MM v[8:11],a[192:195],v[164:167],0
CV v174,v28,v29
CV v175,v30,v31
MM v[8:11],a[196:199],v[180:183],v[8:11]
v_xor_b32 v143, 128, v253
MM v[8:11],a[200:203],v[196:199],v[8:11]
RD v[136:139],v143 offset:0
RD v[140:143],v143 offset:16
MM v[8:11],a[204:207],v[212:215],v[8:11]
MM v[4:7],a[208:211],v[160:163],0
v_max3_f32 v152, v0, v1, v2
v_max_f32 v152, v152, v3
MM v[4:7],a[212:215],v[176:179],v[4:7]
buffer_load_dwordx4 v235, s[12:15], s77 offen offset:3072 lds
MM v[4:7],a[216:219],v[192:195],v[4:7]
s_add_u32 s77, s64, 32768
s_add_u32 s81, s77, 896
s_add_u32 m0, s65, 98304
v_mov_b32 v153, v152
MM v[4:7],a[220:223],v[208:211],v[4:7]
s_waitcnt lgkmcnt(0)
v_permlane16_swap_b32 v152, v153
v_max_f32 v152, v152, v153
MM v[12:15],a[208:211],v[164:167],0
s_waitcnt vmcnt(8)
s_barrier
RD a[192:195],v224 offset:16384
v_mov_b32 v153, v152
MM v[12:15],a[212:215],v[180:183],v[12:15]
RD a[196:199],v225 offset:16384
RD a[200:203],v226 offset:16384
v_permlane32_swap_b32 v152, v153
v_max_f32 v152, v152, v153
v_sub_f32 v128, 0, v152
MM v[12:15],a[216:219],v[196:199],v[12:15]
RD a[204:207],v227 offset:16384
v_sub_f32 v129, 0, v152
v_sub_f32 v130, 0, v152
MM v[12:15],a[220:223],v[212:215],v[12:15]
v_sub_f32 v131, 0, v152
v_max3_f32 v154, v8, v9, v10
MM v[32:35],a[224:227],v[160:163],0
RD a[208:211],v224 offset:20480
RD a[212:215],v225 offset:20480
v_max_f32 v154, v154, v11
v_mov_b32 v155, v154
MM v[32:35],a[228:231],v[176:179],v[32:35]
RD a[216:219],v226 offset:20480
RD a[220:223],v227 offset:20480
v_permlane16_swap_b32 v154, v155
v_max_f32 v154, v154, v155
MM v[32:35],a[232:235],v[192:195],v[32:35]
v_mov_b32 v155, v154
v_sub_f32 v0, v0, v152
MM v[32:35],a[236:239],v[208:211],v[32:35]
buffer_load_dwordx4 v235, s[12:15], s81 offen offset:0 lds
v_permlane32_swap_b32 v154, v155
v_max_f32 v154, v154, v155
v_sub_f32 v132, 0, v154
MM v[40:43],a[224:227],v[164:167],0
v_sub_f32 v133, 0, v154
v_sub_f32 v134, 0, v154
v_sub_f32 v135, 0, v154
v_sub_f32 v1, v1, v152
EX v0,v0
MM v[40:43],a[228:231],v[180:183],v[40:43]
EX v1,v1
MM v[40:43],a[232:235],v[196:199],v[40:43]
v_sub_f32 v2, v2, v152
v_sub_f32 v3, v3, v152
MM v[40:43],a[236:239],v[212:215],v[40:43]
EX v2,v2
MM v[36:39],a[240:243],v[160:163],v[128:131]
RD a[224:227],v224 offset:24576
RD a[228:231],v225 offset:24576
EX v3,v3
MM v[36:39],a[244:247],v[176:179],v[36:39]
RD a[232:235],v226 offset:24576
RD a[236:239],v227 offset:24576
CV v0,v0,v1
v_sub_f32 v4, v4, v152
MM v[36:39],a[248:251],v[192:195],v[36:39]
v_sub_f32 v5, v5, v152
EX v4,v4
MM v[36:39],a[252:255],v[208:211],v[36:39]
EX v5,v5
MM v[44:47],a[240:243],v[164:167],v[132:135]
CV v1,v2,v3
v_sub_f32 v6, v6, v152
MM v[44:47],a[244:247],v[180:183],v[44:47]
buffer_load_dwordx4 v235, s[12:15], s77 offen offset:1024 lds
v_sub_f32 v7, v7, v152
MM v[44:47],a[248:251],v[196:199],v[44:47]
EX v6,v6
EX v7,v7
MM v[44:47],a[252:255],v[212:215],v[44:47]
CV v2,v4,v5
v_sub_f32 v8, v8, v154
MM v[60:63],a[124:127],a[188:191],v[60:63]
RD a[240:243],v224 offset:28672
RD a[244:247],v225 offset:28672
v_sub_f32 v9, v9, v154
MM v[60:63],a[120:123],a[184:187],v[60:63]
RD a[248:251],v226 offset:28672
RD a[252:255],v227 offset:28672
EX v8,v8
MM v[60:63],a[116:119],a[156:159],v[60:63]
EX v9,v9
MM v[60:63],a[112:115],a[152:155],v[60:63]
CV v3,v6,v7
v_sub_f32 v10, v10, v154
MM v[80:83],a[0:3],a[144:147],v[80:83]
v_sub_f32 v11, v11, v154
EX v10,v10
MM v[80:83],a[4:7],a[148:151],v[80:83]
EX v11,v11
MM v[80:83],a[8:11],a[176:179],v[80:83]
CV v8,v8,v9
v_sub_f32 v12, v12, v154
MM v[80:83],a[12:15],a[180:183],v[80:83]
buffer_load_dwordx4 v235, s[12:15], s81 offen offset:2048 lds
v_sub_f32 v13, v13, v154
EX v12,v12
MM v[88:91],a[12:15],a[188:191],v[88:91]
EX v13,v13
MM v[88:91],a[8:11],a[184:187],v[88:91]
v_pk_fma_f32 v[48:49], v[242:243], v[136:137], v[48:49] op_sel:[0,0,0] op_sel_hi:[0,1,1]
v_pk_fma_f32 v[50:51], v[242:243], v[138:139], v[50:51] op_sel:[0,0,0] op_sel_hi:[0,1,1]
v_pk_fma_f32 v[52:53], v[242:243], v[140:141], v[52:53] op_sel:[0,0,0] op_sel_hi:[0,1,1]
v_pk_fma_f32 v[54:55], v[242:243], v[142:143], v[54:55] op_sel:[0,0,0] op_sel_hi:[0,1,1]
v_pk_mul_f32 v[48:49], v[244:245], v[48:49] op_sel:[0,0] op_sel_hi:[0,1]
v_pk_mul_f32 v[50:51], v[244:245], v[50:51] op_sel:[0,0] op_sel_hi:[0,1]
v_pk_mul_f32 v[52:53], v[244:245], v[52:53] op_sel:[0,0] op_sel_hi:[0,1]
v_pk_mul_f32 v[54:55], v[244:245], v[54:55] op_sel:[0,0] op_sel_hi:[0,1]
CV v9,v10,v11
MM v[88:91],a[4:7],a[156:159],v[88:91]
v_sub_f32 v14, v14, v154
v_sub_f32 v15, v15, v154
EX v14,v14
MM v[88:91],a[0:3],a[152:155],v[88:91]
EX v15,v15
MM v[84:87],a[16:19],a[144:147],v[84:87]
CV v10,v12,v13
v_sub_f32 v32, v32, v152
MM v[84:87],a[20:23],a[148:151],v[84:87]
v_sub_f32 v33, v33, v152
CV v184,v48,v49
MM v[84:87],a[24:27],a[176:179],v[84:87]
EX v32,v32
MM v[84:87],a[28:31],a[180:183],v[84:87]
EX v33,v33
MM v[92:95],a[28:31],a[188:191],v[92:95]
CV v11,v14,v15
v_sub_f32 v34, v34, v152
v_sub_f32 v35, v35, v152
MM v[92:95],a[24:27],a[184:187],v[92:95]
buffer_load_dwordx4 v235, s[12:15], s77 offen offset:3072 lds
EX v34,v34
MM v[92:95],a[20:23],a[156:159],v[92:95]
s_add_u32 s77, s64, 49152
s_add_u32 m0, s65, 49152
CV v185,v50,v51
EX v35,v35
MM v[92:95],a[16:19],a[152:155],v[92:95]
buffer_load_dwordx4 v228, s[8:11], s77 offen offset:0 lds
CV v4,v32,v33
EX v36,v36
MM v[112:115],a[32:35],a[144:147],v[112:115]
EX v37,v37
MM v[112:115],a[36:39],a[148:151],v[112:115]
CV v5,v34,v35
CV v186,v52,v53
MM v[112:115],a[40:43],a[176:179],v[112:115]
EX v38,v38
EX v39,v39
MM v[112:115],a[44:47],a[180:183],v[112:115]
buffer_load_dwordx4 v229, s[8:11], s77 offen offset:1024 lds
CV v6,v36,v37
MM v[120:123],a[44:47],a[188:191],v[120:123]
v_sub_f32 v40, v40, v154
v_sub_f32 v41, v41, v154
MM v[120:123],a[40:43],a[184:187],v[120:123]
EX v40,v40
MM v[120:123],a[36:39],a[156:159],v[120:123]
EX v41,v41
MM v[120:123],a[32:35],a[152:155],v[120:123]
buffer_load_dwordx4 v230, s[8:11], s77 offen offset:2048 lds
CV v7,v38,v39
v_sub_f32 v42, v42, v154
MM v[116:119],a[48:51],a[144:147],v[116:119]
v_sub_f32 v43, v43, v154
CV v187,v54,v55
EX v42,v42
MM v[116:119],a[52:55],a[148:151],v[116:119]
EX v43,v43
MM v[116:119],a[56:59],a[176:179],v[116:119]
v_pk_fma_f32 v[56:57], v[242:243], v[136:137], v[56:57] op_sel:[1,0,0] op_sel_hi:[1,1,1]
v_pk_fma_f32 v[58:59], v[242:243], v[138:139], v[58:59] op_sel:[1,0,0] op_sel_hi:[1,1,1]
v_pk_fma_f32 v[60:61], v[242:243], v[140:141], v[60:61] op_sel:[1,0,0] op_sel_hi:[1,1,1]
v_pk_fma_f32 v[62:63], v[242:243], v[142:143], v[62:63] op_sel:[1,0,0] op_sel_hi:[1,1,1]
v_pk_mul_f32 v[56:57], v[244:245], v[56:57] op_sel:[1,0] op_sel_hi:[1,1]
v_pk_mul_f32 v[58:59], v[244:245], v[58:59] op_sel:[1,0] op_sel_hi:[1,1]
v_pk_mul_f32 v[60:61], v[244:245], v[60:61] op_sel:[1,0] op_sel_hi:[1,1]
v_pk_mul_f32 v[62:63], v[244:245], v[62:63] op_sel:[1,0] op_sel_hi:[1,1]
CV v12,v40,v41
EX v44,v44
MM v[116:119],a[60:63],a[180:183],v[116:119]
buffer_load_dwordx4 v231, s[8:11], s77 offen offset:3072 lds
EX v45,v45
MM v[124:127],a[60:63],a[188:191],v[124:127]
s_add_u32 s77, s64, 49152
s_add_u32 s81, s77, 896
s_add_u32 m0, s65, 114688
CV v13,v42,v43
MM v[124:127],a[56:59],a[184:187],v[124:127]
EX v46,v46
MM v[124:127],a[52:55],a[156:159],v[124:127]
EX v47,v47
CV v14,v44,v45
CV v15,v46,v47
MM v[124:127],a[48:51],a[152:155],v[124:127]
buffer_load_dwordx4 v235, s[12:15], s81 offen offset:0 lds
v_smfmac_f32_16x16x64_bf16 v[236:239], v[144:147], v[0:7], v234
CV v188,v56,v57
v_smfmac_f32_16x16x64_bf16 v[236:239], v[148:151], v[8:15], v234
s_waitcnt lgkmcnt(0)
CV v189,v58,v59
buffer_load_dwordx4 v235, s[12:15], s77 offen offset:1024 lds
buffer_load_dwordx4 v235, s[12:15], s81 offen offset:2048 lds
buffer_load_dwordx4 v235, s[12:15], s77 offen offset:3072 lds
s_add_u32 s64, s64, 49152
MM v[64:67],a[192:195],v[160:163],v[128:131]
s_add_u32 s64, s64, 16384
TR a[128:129],v232 offset:0
CV v190,v60,v61
MM v[64:67],a[196:199],v[176:179],v[64:67]
s_add_u32 s81, s64, 896
TR a[130:131],v232 offset:4096
CV v191,v62,v63
MM v[64:67],a[200:203],v[192:195],v[64:67]
TR a[132:133],v232 offset:8192
TR a[134:135],v232 offset:12288
MM v[64:67],a[204:207],v[208:211],v[64:67]
v_add_f32 v246, v236, v237
v_xor_b32 v143, 256, v253
MM v[68:71],a[220:223],v[208:211],v[128:131]
TR a[136:137],v233 offset:0
v_cmp_nge_f32 vcc, s71, v246
MM v[68:71],a[216:219],v[192:195],v[68:71]
s_cbranch_vccnz LSITE0_%=
LBACK0_%=:
TR a[138:139],v233 offset:4096
RD v[136:139],v143 offset:0
MM v[68:71],a[212:215],v[176:179],v[68:71]
TR a[140:141],v233 offset:8192
RD v[140:143],v143 offset:16
MM v[68:71],a[208:211],v[160:163],v[68:71]
TR a[142:143],v233 offset:12288
TR a[144:145],v232 offset:512
MM v[76:79],a[208:211],v[164:167],v[132:135]
TR a[146:147],v232 offset:4608
TR a[148:149],v232 offset:8704
MM v[76:79],a[212:215],v[180:183],v[76:79]
TR a[150:151],v232 offset:12800
TR a[152:153],v233 offset:512
MM v[76:79],a[216:219],v[196:199],v[76:79]
s_waitcnt lgkmcnt(6)
v_pk_fma_f32 v[80:81], v[242:243], v[136:137], v[80:81] op_sel:[0,0,0] op_sel_hi:[0,1,1]
v_pk_fma_f32 v[82:83], v[242:243], v[138:139], v[82:83] op_sel:[0,0,0] op_sel_hi:[0,1,1]
v_pk_fma_f32 v[84:85], v[242:243], v[140:141], v[84:85] op_sel:[0,0,0] op_sel_hi:[0,1,1]
v_pk_fma_f32 v[86:87], v[242:243], v[142:143], v[86:87] op_sel:[0,0,0] op_sel_hi:[0,1,1]
v_pk_mul_f32 v[80:81], v[244:245], v[80:81] op_sel:[0,0] op_sel_hi:[0,1]
v_pk_mul_f32 v[82:83], v[244:245], v[82:83] op_sel:[0,0] op_sel_hi:[0,1]
v_pk_mul_f32 v[84:85], v[244:245], v[84:85] op_sel:[0,0] op_sel_hi:[0,1]
v_pk_mul_f32 v[86:87], v[244:245], v[86:87] op_sel:[0,0] op_sel_hi:[0,1]
MM v[76:79],a[220:223],v[212:215],v[76:79]
EX v64,v64
MM v[72:75],a[204:207],v[212:215],v[132:135]
EX v65,v65
MM v[72:75],a[200:203],v[196:199],v[72:75]
TR a[154:155],v233 offset:4608
TR a[156:157],v233 offset:8704
MM v[72:75],a[196:199],v[180:183],v[72:75]
TR a[158:159],v233 offset:12800
TR a[160:161],v232 offset:1024
MM v[72:75],a[192:195],v[164:167],v[72:75]
EX v66,v66
MM v[96:99],a[224:227],v[160:163],v[128:131]
EX v67,v67
MM v[96:99],a[228:231],v[176:179],v[96:99]
TR a[162:163],v232 offset:5120
TR a[164:165],v232 offset:9216
MM v[96:99],a[232:235],v[192:195],v[96:99]
TR a[166:167],v232 offset:13312
CV v200,v80,v81
EX v68,v68
MM v[96:99],a[236:239],v[208:211],v[96:99]
TR a[168:169],v233 offset:1024
CV v201,v82,v83
MM v[100:103],a[252:255],v[208:211],v[128:131]
TR a[170:171],v233 offset:5120
EX v69,v69
MM v[100:103],a[248:251],v[192:195],v[100:103]
TR a[172:173],v233 offset:9216
TR a[174:175],v233 offset:13312
CV v202,v84,v85
MM v[100:103],a[244:247],v[176:179],v[100:103]
TR a[176:177],v232 offset:1536
EX v70,v70
CV v203,v86,v87
MM v[100:103],a[240:243],v[160:163],v[100:103]
TR a[178:179],v232 offset:5632
v_pk_fma_f32 v[88:89], v[242:243], v[136:137], v[88:89] op_sel:[1,0,0] op_sel_hi:[1,1,1]
v_pk_fma_f32 v[90:91], v[242:243], v[138:139], v[90:91] op_sel:[1,0,0] op_sel_hi:[1,1,1]
v_pk_fma_f32 v[92:93], v[242:243], v[140:141], v[92:93] op_sel:[1,0,0] op_sel_hi:[1,1,1]
v_pk_fma_f32 v[94:95], v[242:243], v[142:143], v[94:95] op_sel:[1,0,0] op_sel_hi:[1,1,1]
v_pk_mul_f32 v[88:89], v[244:245], v[88:89] op_sel:[1,0] op_sel_hi:[1,1]
)ASM"
R"ASM(
v_pk_mul_f32 v[90:91], v[244:245], v[90:91] op_sel:[1,0] op_sel_hi:[1,1]
v_pk_mul_f32 v[92:93], v[244:245], v[92:93] op_sel:[1,0] op_sel_hi:[1,1]
v_pk_mul_f32 v[94:95], v[244:245], v[94:95] op_sel:[1,0] op_sel_hi:[1,1]
EX v71,v71
MM v[108:111],a[240:243],v[164:167],v[132:135]
TR a[180:181],v232 offset:9728
CV v64,v64,v65
MM v[108:111],a[244:247],v[180:183],v[108:111]
TR a[182:183],v232 offset:13824
TR a[184:185],v233 offset:1536
EX v72,v72
MM v[108:111],a[248:251],v[196:199],v[108:111]
TR a[186:187],v233 offset:5632
TR a[188:189],v233 offset:9728
TR a[190:191],v233 offset:13824
EX v73,v73
MM v[108:111],a[252:255],v[212:215],v[108:111]
CV v65,v66,v67
CV v66,v68,v69
MM v[104:107],a[236:239],v[212:215],v[132:135]
EX v74,v74
MM v[104:107],a[232:235],v[196:199],v[104:107]
EX v75,v75
MM v[104:107],a[228:231],v[180:183],v[104:107]
CV v67,v70,v71
CV v204,v88,v89
MM v[104:107],a[224:227],v[164:167],v[104:107]
s_waitcnt vmcnt(12) lgkmcnt(0)
s_barrier
CV v205,v90,v91
EX v76,v76
MM a[0:3],a[128:131],v[0:3],0
s_add_u32 m0, s65, 0
EX v77,v77
MM a[0:3],a[132:135],v[4:7],a[0:3]
CV v206,v92,v93
CV v72,v72,v73
MM a[8:11],a[132:135],v[12:15],0
CV v207,v94,v95
v_xor_b32 v143, 384, v253
MM a[8:11],a[128:131],v[8:11],a[8:11]
EX v78,v78
MM a[12:15],a[136:139],v[8:11],0
EX v79,v79
MM a[12:15],a[140:143],v[12:15],a[12:15]
RD v[136:139],v143 offset:0
RD v[140:143],v143 offset:16
MM a[4:7],a[140:143],v[4:7],0
EX v96,v96
MM a[4:7],a[136:139],v[0:3],a[4:7]
RD a[192:195],v224 offset:32768
CV v73,v74,v75
MM a[32:35],a[144:147],v[0:3],0
EX v97,v97
MM a[32:35],a[148:151],v[4:7],a[32:35]
s_waitcnt lgkmcnt(1)
buffer_load_dwordx4 v228, s[8:11], s64 offen offset:0 lds
v_pk_fma_f32 v[112:113], v[242:243], v[136:137], v[112:113] op_sel:[0,0,0] op_sel_hi:[0,1,1]
v_pk_fma_f32 v[114:115], v[242:243], v[138:139], v[114:115] op_sel:[0,0,0] op_sel_hi:[0,1,1]
v_pk_fma_f32 v[116:117], v[242:243], v[140:141], v[116:117] op_sel:[0,0,0] op_sel_hi:[0,1,1]
v_pk_fma_f32 v[118:119], v[242:243], v[142:143], v[118:119] op_sel:[0,0,0] op_sel_hi:[0,1,1]
v_pk_mul_f32 v[112:113], v[244:245], v[112:113] op_sel:[0,0] op_sel_hi:[0,1]
v_pk_mul_f32 v[114:115], v[244:245], v[114:115] op_sel:[0,0] op_sel_hi:[0,1]
v_pk_mul_f32 v[116:117], v[244:245], v[116:117] op_sel:[0,0] op_sel_hi:[0,1]
v_pk_mul_f32 v[118:119], v[244:245], v[118:119] op_sel:[0,0] op_sel_hi:[0,1]
EX v98,v98
MM a[40:43],a[148:151],v[12:15],0
RD a[196:199],v225 offset:32768
CV v74,v76,v77
MM a[40:43],a[144:147],v[8:11],a[40:43]
RD a[200:203],v226 offset:32768
EX v99,v99
MM a[44:47],a[152:155],v[8:11],0
buffer_load_dwordx4 v229, s[8:11], s64 offen offset:1024 lds
MM a[44:47],a[156:159],v[12:15],a[44:47]
RD a[204:207],v227 offset:32768
EX v100,v100
MM a[36:39],a[156:159],v[4:7],0
EX v101,v101
CV v75,v78,v79
MM a[36:39],a[152:155],v[0:3],a[36:39]
RD a[208:211],v224 offset:36864
CV v216,v112,v113
MM a[64:67],a[160:163],v[0:3],0
EX v102,v102
MM a[64:67],a[164:167],v[4:7],a[64:67]
buffer_load_dwordx4 v230, s[8:11], s64 offen offset:2048 lds
RD a[212:215],v225 offset:36864
CV v217,v114,v115
MM a[72:75],a[164:167],v[12:15],0
EX v103,v103
CV v68,v96,v97
CV v218,v116,v117
MM a[72:75],a[160:163],v[8:11],a[72:75]
RD a[216:219],v226 offset:36864
CV v219,v118,v119
EX v104,v104
MM a[76:79],a[168:171],v[8:11],0
buffer_load_dwordx4 v231, s[8:11], s64 offen offset:3072 lds
RD a[220:223],v227 offset:36864
MM a[76:79],a[172:175],v[12:15],a[76:79]
s_add_u32 m0, s65, 65536
v_pk_fma_f32 v[120:121], v[242:243], v[136:137], v[120:121] op_sel:[1,0,0] op_sel_hi:[1,1,1]
v_pk_fma_f32 v[122:123], v[242:243], v[138:139], v[122:123] op_sel:[1,0,0] op_sel_hi:[1,1,1]
v_pk_fma_f32 v[124:125], v[242:243], v[140:141], v[124:125] op_sel:[1,0,0] op_sel_hi:[1,1,1]
v_pk_fma_f32 v[126:127], v[242:243], v[142:143], v[126:127] op_sel:[1,0,0] op_sel_hi:[1,1,1]
v_pk_mul_f32 v[120:121], v[244:245], v[120:121] op_sel:[1,0] op_sel_hi:[1,1]
v_pk_mul_f32 v[122:123], v[244:245], v[122:123] op_sel:[1,0] op_sel_hi:[1,1]
v_pk_mul_f32 v[124:125], v[244:245], v[124:125] op_sel:[1,0] op_sel_hi:[1,1]
v_pk_mul_f32 v[126:127], v[244:245], v[126:127] op_sel:[1,0] op_sel_hi:[1,1]
EX v105,v105
CV v69,v98,v99
MM a[68:71],a[172:175],v[4:7],0
RD a[224:227],v224 offset:40960
CV v70,v100,v101
MM a[68:71],a[168:171],v[0:3],a[68:71]
EX v106,v106
MM a[96:99],a[176:179],v[0:3],0
RD a[228:231],v225 offset:40960
buffer_load_dwordx4 v235, s[12:15], s81 offen offset:0 lds
EX v107,v107
MM a[96:99],a[180:183],v[4:7],a[96:99]
RD a[232:235],v226 offset:40960
CV v71,v102,v103
MM a[104:107],a[180:183],v[12:15],0
EX v108,v108
MM a[104:107],a[176:179],v[8:11],a[104:107]
EX v109,v109
MM a[108:111],a[184:187],v[8:11],0
RD a[236:239],v227 offset:40960
RD a[240:243],v224 offset:45056
buffer_load_dwordx4 v235, s[12:15], s64 offen offset:1024 lds
MM a[108:111],a[188:191],v[12:15],a[108:111]
EX v110,v110
CV v220,v120,v121
MM a[100:103],a[188:191],v[4:7],0
RD a[244:247],v225 offset:45056
RD a[248:251],v226 offset:45056
RD a[252:255],v227 offset:45056
CV v221,v122,v123
EX v111,v111
CV v222,v124,v125
CV v223,v126,v127
MM a[100:103],a[184:187],v[0:3],a[100:103]
buffer_load_dwordx4 v235, s[12:15], s81 offen offset:2048 lds
CV v76,v104,v105
v_smfmac_f32_16x16x64_bf16 v[236:239], v[144:147], v[64:71], v234
CV v77,v106,v107
CV v78,v108,v109
CV v79,v110,v111
s_nop 1
v_smfmac_f32_16x16x64_bf16 v[236:239], v[148:151], v[72:79], v234
s_waitcnt lgkmcnt(0)
buffer_load_dwordx4 v235, s[12:15], s64 offen offset:3072 lds
MM v[0:3],a[192:195],v[160:163],v[128:131]
s_add_u32 s64, s64, 16384
TR a[128:129],v232 offset:16384
TR a[130:131],v232 offset:20480
MM v[0:3],a[196:199],v[176:179],v[0:3]
s_add_u32 s81, s64, 896
TR a[132:133],v232 offset:24576
TR a[134:135],v232 offset:28672
s_nop 0
LPG5_%=:
s_cbranch_execz LPG9_%=
MM v[0:3],a[200:203],v[192:195],v[0:3]
TR a[136:137],v233 offset:16384
TR a[138:139],v233 offset:20480
MM v[0:3],a[204:207],v[208:211],v[0:3]
TR a[140:141],v233 offset:24576
v_add_f32 v247, v236, v237
MM v[4:7],a[220:223],v[208:211],v[128:131]
TR a[142:143],v233 offset:28672
v_cmp_nge_f32 vcc, s71, v247
MM v[4:7],a[216:219],v[192:195],v[4:7]
s_cbranch_vccnz LSITE1_%=
LBACK1_%=:
TR a[144:145],v232 offset:16896
TR a[146:147],v232 offset:20992
MM v[4:7],a[212:215],v[176:179],v[4:7]
s_cmp_lg_u32 %[sb], 0
s_cbranch_scc1 LKA_%=
s_lshl_b32 s90, %[swave], 10
s_add_u32 s90, s90, 81920
buffer_load_dwordx4 v[48:51], v248, s[20:23], s90 offen offset:0 sc1
LKA_%=:
MM v[4:7],a[208:211],v[160:163],v[4:7]
TR a[148:149],v232 offset:25088
TR a[150:151],v232 offset:29184
MM v[12:15],a[208:211],v[164:167],v[132:135]
EX v0,v0
MM v[12:15],a[212:215],v[180:183],v[12:15]
EX v1,v1
MM v[12:15],a[216:219],v[196:199],v[12:15]
TR a[152:153],v233 offset:16896
CV v0,v0,v1
MM v[12:15],a[220:223],v[212:215],v[12:15]
EX v2,v2
MM v[8:11],a[204:207],v[212:215],v[132:135]
TR a[154:155],v233 offset:20992
TR a[156:157],v233 offset:25088
MM v[8:11],a[200:203],v[196:199],v[8:11]
EX v3,v3
MM v[8:11],a[196:199],v[180:183],v[8:11]
TR a[158:159],v233 offset:29184
CV v1,v2,v3
MM v[8:11],a[192:195],v[164:167],v[8:11]
EX v4,v4
MM v[32:35],a[224:227],v[160:163],v[128:131]
TR a[160:161],v232 offset:17408
TR a[162:163],v232 offset:21504
MM v[32:35],a[228:231],v[176:179],v[32:35]
EX v5,v5
MM v[32:35],a[232:235],v[192:195],v[32:35]
TR a[164:165],v232 offset:25600
CV v2,v4,v5
MM v[32:35],a[236:239],v[208:211],v[32:35]
TR a[166:167],v232 offset:29696
TR a[168:169],v233 offset:17408
MM v[36:39],a[252:255],v[208:211],v[128:131]
EX v6,v6
MM v[36:39],a[248:251],v[192:195],v[36:39]
EX v7,v7
MM v[36:39],a[244:247],v[176:179],v[36:39]
TR a[170:171],v233 offset:21504
CV v3,v6,v7
MM v[36:39],a[240:243],v[160:163],v[36:39]
TR a[172:173],v233 offset:25600
TR a[174:175],v233 offset:29696
MM v[44:47],a[240:243],v[164:167],v[132:135]
EX v8,v8
MM v[44:47],a[244:247],v[180:183],v[44:47]
TR a[176:177],v232 offset:17920
TR a[178:179],v232 offset:22016
MM v[44:47],a[248:251],v[196:199],v[44:47]
EX v9,v9
MM v[44:47],a[252:255],v[212:215],v[44:47]
TR a[180:181],v232 offset:26112
CV v8,v8,v9
MM v[40:43],a[236:239],v[212:215],v[132:135]
EX v10,v10
MM v[40:43],a[232:235],v[196:199],v[40:43]
TR a[182:183],v232 offset:30208
TR a[184:185],v233 offset:17920
MM v[40:43],a[228:231],v[180:183],v[40:43]
EX v11,v11
MM v[40:43],a[224:227],v[164:167],v[40:43]
TR a[186:187],v233 offset:22016
CV v9,v10,v11
MM v[16:19],a[192:195],v[168:171],0
EX v12,v12
MM v[16:19],a[196:199],v[184:187],v[16:19]
TR a[188:189],v233 offset:26112
TR a[190:191],v233 offset:30208
MM v[16:19],a[200:203],v[200:203],v[16:19]
EX v13,v13
MM v[16:19],a[204:207],v[216:219],v[16:19]
EX v14,v14
MM v[24:27],a[192:195],v[172:175],0
EX v15,v15
MM v[24:27],a[196:199],v[188:191],v[24:27]
EX v32,v32
MM v[24:27],a[200:203],v[204:207],v[24:27]
EX v33,v33
MM v[24:27],a[204:207],v[220:223],v[24:27]
s_waitcnt vmcnt(12) lgkmcnt(0)
s_barrier
CV v10,v12,v13
MM a[0:3],a[128:131],v[64:67],a[0:3]
s_add_u32 m0, s65, 16384
RD a[192:195],v224 offset:49152
CV v11,v14,v15
MM a[0:3],a[132:135],v[68:71],a[0:3]
CV v4,v32,v33
v_max3_f32 v154, v16, v17, v18
MM a[8:11],a[132:135],v[76:79],a[8:11]
buffer_load_dwordx4 v228, s[8:11], s64 offen offset:0 lds
MM a[8:11],a[128:131],v[72:75],a[8:11]
RD a[196:199],v225 offset:49152
v_max_f32 v154, v154, v19
MM a[12:15],a[136:139],v[72:75],a[12:15]
EX v34,v34
MM a[12:15],a[140:143],v[76:79],a[12:15]
RD a[200:203],v226 offset:49152
v_mov_b32 v155, v154
MM a[4:7],a[140:143],v[68:71],a[4:7]
s_nop 0
v_permlane16_swap_b32 v154, v155
v_max_f32 v154, v154, v155
MM a[4:7],a[136:139],v[64:67],a[4:7]
EX v35,v35
MM a[32:35],a[144:147],v[64:67],a[32:35]
RD a[204:207],v227 offset:49152
v_mov_b32 v155, v154
MM a[32:35],a[148:151],v[68:71],a[32:35]
s_nop 0
v_permlane32_swap_b32 v154, v155
v_max_f32 v154, v154, v155
MM a[40:43],a[148:151],v[76:79],a[40:43]
EX v36,v36
MM a[40:43],a[144:147],v[72:75],a[40:43]
v_sub_f32 v136, 0, v154
v_sub_f32 v137, 0, v154
MM a[44:47],a[152:155],v[72:75],a[44:47]
EX v37,v37
MM a[44:47],a[156:159],v[76:79],a[44:47]
buffer_load_dwordx4 v229, s[8:11], s64 offen offset:1024 lds
v_sub_f32 v138, 0, v154
v_sub_f32 v139, 0, v154
MM a[36:39],a[156:159],v[68:71],a[36:39]
EX v38,v38
MM a[36:39],a[152:155],v[64:67],a[36:39]
RD a[208:211],v224 offset:53248
v_mov_b32_dpp v152, v144 row_ror:8 row_mask:0xf bank_mask:0xf
v_mov_b32_dpp v153, v145 row_ror:8 row_mask:0xf bank_mask:0xf
MM a[64:67],a[160:163],v[64:67],a[64:67]
EX v39,v39
v_mov_b32_dpp v154, v146 row_ror:8 row_mask:0xf bank_mask:0xf
MM a[64:67],a[164:167],v[68:71],a[64:67]
RD a[212:215],v225 offset:53248
buffer_load_dwordx4 v230, s[8:11], s64 offen offset:2048 lds
v_mov_b32_dpp v155, v147 row_ror:8 row_mask:0xf bank_mask:0xf
MM a[72:75],a[164:167],v[76:79],a[72:75]
RD a[216:219],v226 offset:53248
v_max3_f32 v158, v24, v25, v26
EX v40,v40
MM a[72:75],a[160:163],v[72:75],a[72:75]
v_max_f32 v158, v158, v27
v_mov_b32 v159, v158
CV v5,v34,v35
MM a[76:79],a[168:171],v[72:75],a[76:79]
buffer_load_dwordx4 v231, s[8:11], s64 offen offset:3072 lds
RD a[220:223],v227 offset:53248
EX v41,v41
v_permlane16_swap_b32 v158, v159
MM a[76:79],a[172:175],v[76:79],a[76:79]
s_add_u32 m0, s65, 81920
v_max_f32 v158, v158, v159
CV v6,v36,v37
MM a[68:71],a[172:175],v[68:71],a[68:71]
RD a[224:227],v224 offset:57344
EX v42,v42
v_mov_b32 v159, v158
MM a[68:71],a[168:171],v[64:67],a[68:71]
s_nop 0
v_permlane32_swap_b32 v158, v159
v_max_f32 v158, v158, v159
EX v43,v43
MM a[96:99],a[176:179],v[64:67],a[96:99]
RD a[228:231],v225 offset:57344
buffer_load_dwordx4 v235, s[12:15], s81 offen offset:0 lds
v_sub_f32 v140, 0, v158
MM a[96:99],a[180:183],v[68:71],a[96:99]
v_sub_f32 v141, 0, v158
CV v7,v38,v39
EX v44,v44
MM a[104:107],a[180:183],v[76:79],a[104:107]
RD a[232:235],v226 offset:57344
v_sub_f32 v142, 0, v158
MM a[104:107],a[176:179],v[72:75],a[104:107]
v_sub_f32 v143, 0, v158
v_mov_b32_dpp v156, v148 row_ror:8 row_mask:0xf bank_mask:0xf
EX v45,v45
MM a[108:111],a[184:187],v[72:75],a[108:111]
RD a[236:239],v227 offset:57344
buffer_load_dwordx4 v235, s[12:15], s64 offen offset:1024 lds
v_mov_b32_dpp v157, v149 row_ror:8 row_mask:0xf bank_mask:0xf
v_mov_b32_dpp v158, v150 row_ror:8 row_mask:0xf bank_mask:0xf
)ASM"
R"ASM(
v_mov_b32_dpp v159, v151 row_ror:8 row_mask:0xf bank_mask:0xf
MM a[108:111],a[188:191],v[76:79],a[108:111]
EX v46,v46
MM a[100:103],a[188:191],v[68:71],a[100:103]
RD a[240:243],v224 offset:61440
RD a[244:247],v225 offset:61440
RD a[248:251],v226 offset:61440
RD a[252:255],v227 offset:61440
EX v47,v47
MM a[100:103],a[184:187],v[64:67],a[100:103]
buffer_load_dwordx4 v235, s[12:15], s81 offen offset:2048 lds
CV v12,v40,v41
v_smfmac_f32_16x16x64_bf16 v[236:239], v[144:147], v[0:7], v234
CV v13,v42,v43
CV v14,v44,v45
CV v15,v46,v47
s_nop 1
v_smfmac_f32_16x16x64_bf16 v[236:239], v[148:151], v[8:15], v234
s_waitcnt lgkmcnt(0)
buffer_load_dwordx4 v235, s[12:15], s64 offen offset:3072 lds
MM v[64:67],a[192:195],v[160:163],v[128:131]
s_add_u32 s64, s64, 16384
TR a[128:129],v232 offset:32768
s_add_u32 s81, s64, 896
MM v[64:67],a[196:199],v[176:179],v[64:67]
TR a[130:131],v232 offset:36864
TR a[132:133],v232 offset:40960
MM v[64:67],a[200:203],v[192:195],v[64:67]
TR a[134:135],v232 offset:45056
TR a[136:137],v233 offset:32768
MM v[64:67],a[204:207],v[208:211],v[64:67]
TR a[138:139],v233 offset:36864
v_add_f32 v246, v236, v237
MM v[68:71],a[220:223],v[208:211],v[128:131]
TR a[140:141],v233 offset:40960
v_cmp_nge_f32 vcc, s71, v246
MM v[68:71],a[216:219],v[192:195],v[68:71]
s_cbranch_vccnz LSITE2_%=
LBACK2_%=:
TR a[142:143],v233 offset:45056
TR a[144:145],v232 offset:33280
MM v[68:71],a[212:215],v[176:179],v[68:71]
TR a[146:147],v232 offset:37376
TR a[148:149],v232 offset:41472
MM v[68:71],a[208:211],v[160:163],v[68:71]
TR a[150:151],v232 offset:45568
TR a[152:153],v233 offset:33280
MM v[76:79],a[208:211],v[164:167],v[132:135]
EX v64,v64
MM v[76:79],a[212:215],v[180:183],v[76:79]
EX v65,v65
MM v[76:79],a[216:219],v[196:199],v[76:79]
TR a[154:155],v233 offset:37376
CV v64,v64,v65
MM v[76:79],a[220:223],v[212:215],v[76:79]
EX v66,v66
MM v[72:75],a[204:207],v[212:215],v[132:135]
EX v67,v67
MM v[72:75],a[200:203],v[196:199],v[72:75]
TR a[156:157],v233 offset:41472
CV v65,v66,v67
MM v[72:75],a[196:199],v[180:183],v[72:75]
EX v68,v68
MM v[72:75],a[192:195],v[164:167],v[72:75]
EX v69,v69
MM v[80:83],a[192:195],v[168:171],v[136:139]
TR a[158:159],v233 offset:45568
CV v66,v68,v69
MM v[80:83],a[196:199],v[184:187],v[80:83]
EX v70,v70
MM v[80:83],a[200:203],v[200:203],v[80:83]
EX v71,v71
MM v[80:83],a[204:207],v[216:219],v[80:83]
TR a[160:161],v232 offset:33792
CV v67,v70,v71
MM v[84:87],a[220:223],v[216:219],v[136:139]
s_cmp_lg_u32 %[sb], 0
s_cbranch_scc1 LKB_%=
s_waitcnt vmcnt(8)
v_xor_b32 v48, s69, v48
v_xor_b32 v49, s69, v49
v_xor_b32 v50, s69, v50
v_xor_b32 v51, s69, v51
v_or3_b32 v52, v48, v49, v50
v_or_b32 v52, v52, v51
v_cmp_ne_u32 vcc, 0, v52
s_cbranch_vccnz LRFR_%=
LRFRb_%=:
s_lshl_b32 s90, %[swave], 11
s_add_u32 s78, s90, %[slds]
s_add_u32 s78, s78, 155648
s_add_u32 s90, s90, 53248
s_mov_b32 m0, s78
s_nop 0
buffer_load_dwordx4 v248, s[20:23], s90 offen offset:0 sc1 lds
buffer_load_dwordx4 v248, s[20:23], s90 offen offset:1024 sc1 lds
LKB_%=:
MM v[84:87],a[216:219],v[200:203],v[84:87]
EX v72,v72
MM v[84:87],a[212:215],v[184:187],v[84:87]
EX v73,v73
MM v[84:87],a[208:211],v[168:171],v[84:87]
EX v74,v74
MM v[92:95],a[208:211],v[172:175],v[140:143]
EX v75,v75
MM v[92:95],a[212:215],v[188:191],v[92:95]
CV v72,v72,v73
CV v73,v74,v75
MM v[92:95],a[216:219],v[204:207],v[92:95]
EX v76,v76
MM v[92:95],a[220:223],v[220:223],v[92:95]
EX v77,v77
MM v[88:91],a[204:207],v[220:223],v[140:143]
EX v78,v78
MM v[88:91],a[200:203],v[204:207],v[88:91]
EX v79,v79
MM v[88:91],a[196:199],v[188:191],v[88:91]
CV v74,v76,v77
CV v75,v78,v79
MM v[88:91],a[192:195],v[172:175],v[88:91]
EX v80,v80
MM v[96:99],a[224:227],v[160:163],v[128:131]
EX v81,v81
MM v[96:99],a[228:231],v[176:179],v[96:99]
TR a[162:163],v232 offset:37888
TR a[164:165],v232 offset:41984
MM v[96:99],a[232:235],v[192:195],v[96:99]
EX v82,v82
MM v[96:99],a[236:239],v[208:211],v[96:99]
EX v83,v83
MM v[100:103],a[252:255],v[208:211],v[128:131]
TR a[166:167],v232 offset:46080
CV v80,v80,v81
MM v[100:103],a[248:251],v[192:195],v[100:103]
EX v84,v84
MM v[100:103],a[244:247],v[176:179],v[100:103]
EX v85,v85
MM v[100:103],a[240:243],v[160:163],v[100:103]
TR a[168:169],v233 offset:33792
CV v81,v82,v83
MM v[108:111],a[240:243],v[164:167],v[132:135]
EX v86,v86
MM v[108:111],a[244:247],v[180:183],v[108:111]
EX v87,v87
MM v[108:111],a[248:251],v[196:199],v[108:111]
TR a[170:171],v233 offset:37888
CV v82,v84,v85
MM v[108:111],a[252:255],v[212:215],v[108:111]
EX v88,v88
MM v[104:107],a[236:239],v[212:215],v[132:135]
EX v89,v89
MM v[104:107],a[232:235],v[196:199],v[104:107]
TR a[172:173],v233 offset:41984
TR a[174:175],v233 offset:46080
MM v[104:107],a[228:231],v[180:183],v[104:107]
EX v90,v90
MM v[104:107],a[224:227],v[164:167],v[104:107]
EX v91,v91
MM v[112:115],a[224:227],v[168:171],v[136:139]
TR a[176:177],v232 offset:34304
CV v83,v86,v87
MM v[112:115],a[228:231],v[184:187],v[112:115]
EX v92,v92
MM v[112:115],a[232:235],v[200:203],v[112:115]
EX v93,v93
MM v[112:115],a[236:239],v[216:219],v[112:115]
TR a[178:179],v232 offset:38400
CV v88,v88,v89
MM v[116:119],a[252:255],v[216:219],v[136:139]
EX v94,v94
MM v[116:119],a[248:251],v[200:203],v[116:119]
EX v95,v95
MM v[116:119],a[244:247],v[184:187],v[116:119]
TR a[180:181],v232 offset:42496
TR a[182:183],v232 offset:46592
MM v[116:119],a[240:243],v[168:171],v[116:119]
EX v96,v96
MM v[124:127],a[240:243],v[172:175],v[140:143]
EX v97,v97
CV v89,v90,v91
MM v[124:127],a[244:247],v[188:191],v[124:127]
EX v98,v98
MM v[124:127],a[248:251],v[204:207],v[124:127]
TR a[184:185],v233 offset:34304
TR a[186:187],v233 offset:38400
TR a[188:189],v233 offset:42496
TR a[190:191],v233 offset:46592
MM v[124:127],a[252:255],v[220:223],v[124:127]
EX v99,v99
CV v90,v92,v93
MM v[120:123],a[236:239],v[220:223],v[140:143]
EX v100,v100
MM v[120:123],a[232:235],v[204:207],v[120:123]
EX v101,v101
CV v91,v94,v95
MM v[120:123],a[228:231],v[188:191],v[120:123]
EX v102,v102
MM v[120:123],a[224:227],v[172:175],v[120:123]
s_waitcnt vmcnt(12) lgkmcnt(0)
s_barrier
CV v68,v96,v97
MM a[0:3],a[128:131],v[0:3],a[0:3]
s_add_u32 m0, s65, 32768
EX v103,v103
MM a[0:3],a[132:135],v[4:7],a[0:3]
EX v104,v104
MM a[8:11],a[132:135],v[12:15],a[8:11]
CV v69,v98,v99
CV v70,v100,v101
MM a[8:11],a[128:131],v[8:11],a[8:11]
EX v105,v105
MM a[12:15],a[136:139],v[8:11],a[12:15]
EX v106,v106
MM a[12:15],a[140:143],v[12:15],a[12:15]
EX v107,v107
MM a[4:7],a[140:143],v[4:7],a[4:7]
EX v108,v108
MM a[4:7],a[136:139],v[0:3],a[4:7]
RD a[192:195],v224 offset:0
CV v71,v102,v103
MM a[32:35],a[144:147],v[0:3],a[32:35]
EX v109,v109
MM a[32:35],a[148:151],v[4:7],a[32:35]
buffer_load_dwordx4 v228, s[8:11], s64 offen offset:0 lds
MM a[40:43],a[148:151],v[12:15],a[40:43]
RD a[196:199],v225 offset:0
EX v110,v110
MM a[40:43],a[144:147],v[8:11],a[40:43]
EX v111,v111
CV v76,v104,v105
MM a[44:47],a[152:155],v[8:11],a[44:47]
RD a[200:203],v226 offset:0
EX v112,v112
MM a[44:47],a[156:159],v[12:15],a[44:47]
buffer_load_dwordx4 v229, s[8:11], s64 offen offset:1024 lds
MM a[36:39],a[156:159],v[4:7],a[36:39]
RD a[204:207],v227 offset:0
EX v113,v113
CV v77,v106,v107
MM a[36:39],a[152:155],v[0:3],a[36:39]
EX v114,v114
MM a[64:67],a[160:163],v[0:3],a[64:67]
RD a[208:211],v224 offset:4096
EX v115,v115
CV v78,v108,v109
MM a[64:67],a[164:167],v[4:7],a[64:67]
buffer_load_dwordx4 v230, s[8:11], s64 offen offset:2048 lds
MM a[72:75],a[164:167],v[12:15],a[72:75]
RD a[212:215],v225 offset:4096
EX v116,v116
MM a[72:75],a[160:163],v[8:11],a[72:75]
EX v117,v117
CV v79,v110,v111
MM a[76:79],a[168:171],v[8:11],a[76:79]
RD a[216:219],v226 offset:4096
CV v84,v112,v113
EX v118,v118
MM a[76:79],a[172:175],v[12:15],a[76:79]
buffer_load_dwordx4 v231, s[8:11], s64 offen offset:3072 lds
MM a[68:71],a[172:175],v[4:7],a[68:71]
s_add_u32 m0, s65, 98304
RD a[220:223],v227 offset:4096
EX v119,v119
MM a[68:71],a[168:171],v[0:3],a[68:71]
EX v120,v120
MM a[96:99],a[176:179],v[0:3],a[96:99]
RD a[224:227],v224 offset:8192
CV v85,v114,v115
EX v121,v121
MM a[96:99],a[180:183],v[4:7],a[96:99]
RD a[228:231],v225 offset:8192
CV v86,v116,v117
MM a[104:107],a[180:183],v[12:15],a[104:107]
buffer_load_dwordx4 v235, s[12:15], s81 offen offset:0 lds
EX v122,v122
MM a[104:107],a[176:179],v[8:11],a[104:107]
EX v123,v123
MM a[108:111],a[184:187],v[8:11],a[108:111]
RD a[232:235],v226 offset:8192
CV v87,v118,v119
MM a[108:111],a[188:191],v[12:15],a[108:111]
EX v124,v124
MM a[100:103],a[188:191],v[4:7],a[100:103]
RD a[236:239],v227 offset:8192
buffer_load_dwordx4 v235, s[12:15], s64 offen offset:1024 lds
EX v125,v125
MM a[100:103],a[184:187],v[0:3],a[100:103]
EX v126,v126
v_smfmac_f32_16x16x64_bf16 v[236:239], v[144:147], v[64:71], v234
RD a[240:243],v224 offset:12288
RD a[244:247],v225 offset:12288
RD a[248:251],v226 offset:12288
RD a[252:255],v227 offset:12288
EX v127,v127
v_smfmac_f32_16x16x64_bf16 v[236:239], v[148:151], v[72:79], v234
CV v92,v120,v121
v_smfmac_f32_16x16x64_bf16 v[236:239], v[152:155], v[80:87], v234
buffer_load_dwordx4 v235, s[12:15], s81 offen offset:2048 lds
CV v93,v122,v123
CV v94,v124,v125
CV v95,v126,v127
s_nop 1
v_smfmac_f32_16x16x64_bf16 v[236:239], v[156:159], v[88:95], v234
s_waitcnt lgkmcnt(0)
buffer_load_dwordx4 v235, s[12:15], s64 offen offset:3072 lds
MM v[0:3],a[192:195],v[160:163],v[128:131]
s_add_u32 s64, s64, 16384
TR a[128:129],v232 offset:49152
s_add_u32 s81, s64, 896
MM v[0:3],a[196:199],v[176:179],v[0:3]
TR a[130:131],v232 offset:53248
TR a[132:133],v232 offset:57344
MM v[0:3],a[200:203],v[192:195],v[0:3]
TR a[134:135],v232 offset:61440
TR a[136:137],v233 offset:49152
MM v[0:3],a[204:207],v[208:211],v[0:3]
TR a[138:139],v233 offset:53248
v_add_f32 v247, v236, v237
MM v[4:7],a[220:223],v[208:211],v[128:131]
TR a[140:141],v233 offset:57344
v_cmp_nge_f32 vcc, s71, v247
MM v[4:7],a[216:219],v[192:195],v[4:7]
s_cbranch_vccnz LSITE3_%=
LBACK3_%=:
TR a[142:143],v233 offset:61440
TR a[144:145],v232 offset:49664
MM v[4:7],a[212:215],v[176:179],v[4:7]
v_lshrrev_b32 v252, 6, v248
v_mul_u32_u24 v252, 3456, v252
MM v[4:7],a[208:211],v[160:163],v[4:7]
v_and_b32 v251, 48, v248
v_add_u32 v252, v252, v251
MM v[12:15],a[208:211],v[164:167],v[132:135]
s_and_b32 s90, %[swave], 1
EX v0,v0
MM v[12:15],a[212:215],v[180:183],v[12:15]
s_mul_i32 s90, s90, 276480
EX v1,v1
MM v[12:15],a[216:219],v[196:199],v[12:15]
s_bfe_u32 s91, s48, 131077
TR a[146:147],v232 offset:53760
CV v0,v0,v1
MM v[12:15],a[220:223],v[212:215],v[12:15]
s_mul_i32 s91, s91, 552960
EX v2,v2
MM v[8:11],a[204:207],v[212:215],v[132:135]
s_add_u32 s90, s90, s91
EX v3,v3
MM v[8:11],a[200:203],v[196:199],v[8:11]
s_and_b32 s91, s48, 0xffffff80
TR a[148:149],v232 offset:57856
CV v1,v2,v3
MM v[8:11],a[196:199],v[180:183],v[8:11]
s_add_u32 s90, s90, s91
EX v4,v4
MM v[8:11],a[192:195],v[164:167],v[8:11]
s_and_b32 s91, s48, 16
TR a[150:151],v232 offset:61952
TR a[152:153],v233 offset:49664
MM v[16:19],a[192:195],v[168:171],v[136:139]
s_lshl_b32 s91, s91, 2
EX v5,v5
MM v[16:19],a[196:199],v[184:187],v[16:19]
s_add_u32 s66, s90, s91
TR a[154:155],v233 offset:53760
CV v2,v4,v5
MM v[16:19],a[200:203],v[200:203],v[16:19]
s_mul_i32 s91, %[swave], 5120
EX v6,v6
MM v[16:19],a[204:207],v[216:219],v[16:19]
s_add_u32 s91, s91, %[slds]
EX v7,v7
MM v[20:23],a[220:223],v[216:219],v[136:139]
s_add_u32 s67, s91, 131072
TR a[156:157],v233 offset:57856
CV v3,v6,v7
MM v[20:23],a[216:219],v[200:203],v[20:23]
s_add_u32 s85, s66, 221184
EX v8,v8
MM v[20:23],a[212:215],v[184:187],v[20:23]
s_add_u32 s86, s67, 4096
TR a[158:159],v233 offset:61952
TR a[160:161],v232 offset:50176
MM v[20:23],a[208:211],v[168:171],v[20:23]
EX v9,v9
MM v[28:31],a[208:211],v[172:175],v[140:143]
TR a[162:163],v232 offset:54272
CV v8,v8,v9
MM v[28:31],a[212:215],v[188:191],v[28:31]
EX v10,v10
MM v[28:31],a[216:219],v[204:207],v[28:31]
EX v11,v11
MM v[28:31],a[220:223],v[220:223],v[28:31]
TR a[164:165],v232 offset:58368
CV v9,v10,v11
MM v[24:27],a[204:207],v[220:223],v[140:143]
EX v12,v12
MM v[24:27],a[200:203],v[204:207],v[24:27]
TR a[166:167],v232 offset:62464
TR a[168:169],v233 offset:50176
MM v[24:27],a[196:199],v[188:191],v[24:27]
)ASM"
R"ASM(
EX v13,v13
MM v[24:27],a[192:195],v[172:175],v[24:27]
TR a[170:171],v233 offset:54272
LPG6_%=:
s_cbranch_execz LPG10_%=
CV v10,v12,v13
MM v[32:35],a[224:227],v[160:163],v[128:131]
EX v14,v14
MM v[32:35],a[228:231],v[176:179],v[32:35]
EX v15,v15
MM v[32:35],a[232:235],v[192:195],v[32:35]
TR a[172:173],v233 offset:58368
CV v11,v14,v15
MM v[32:35],a[236:239],v[208:211],v[32:35]
TR a[174:175],v233 offset:62464
TR a[176:177],v232 offset:50688
MM v[36:39],a[252:255],v[208:211],v[128:131]
EX v16,v16
MM v[36:39],a[248:251],v[192:195],v[36:39]
EX v17,v17
MM v[36:39],a[244:247],v[176:179],v[36:39]
TR a[178:179],v232 offset:54784
CV v16,v16,v17
MM v[36:39],a[240:243],v[160:163],v[36:39]
EX v18,v18
MM v[44:47],a[240:243],v[164:167],v[132:135]
TR a[180:181],v232 offset:58880
TR a[182:183],v232 offset:62976
MM v[44:47],a[244:247],v[180:183],v[44:47]
EX v19,v19
MM v[44:47],a[248:251],v[196:199],v[44:47]
TR a[184:185],v233 offset:50688
CV v17,v18,v19
MM v[44:47],a[252:255],v[212:215],v[44:47]
EX v20,v20
MM v[40:43],a[236:239],v[212:215],v[132:135]
EX v21,v21
MM v[40:43],a[232:235],v[196:199],v[40:43]
TR a[186:187],v233 offset:54784
CV v18,v20,v21
MM v[40:43],a[228:231],v[180:183],v[40:43]
EX v22,v22
MM v[40:43],a[224:227],v[164:167],v[40:43]
TR a[188:189],v233 offset:58880
TR a[190:191],v233 offset:62976
MM v[48:51],a[224:227],v[168:171],v[136:139]
EX v23,v23
MM v[48:51],a[228:231],v[184:187],v[48:51]
CV v19,v22,v23
MM v[48:51],a[232:235],v[200:203],v[48:51]
EX v24,v24
MM v[48:51],a[236:239],v[216:219],v[48:51]
EX v25,v25
MM v[52:55],a[252:255],v[216:219],v[136:139]
EX v26,v26
MM v[52:55],a[248:251],v[200:203],v[52:55]
EX v27,v27
MM v[52:55],a[244:247],v[184:187],v[52:55]
EX v28,v28
MM v[52:55],a[240:243],v[168:171],v[52:55]
EX v29,v29
MM v[60:63],a[240:243],v[172:175],v[140:143]
EX v30,v30
MM v[60:63],a[244:247],v[188:191],v[60:63]
EX v31,v31
MM v[60:63],a[248:251],v[204:207],v[60:63]
EX v32,v32
MM v[60:63],a[252:255],v[220:223],v[60:63]
EX v33,v33
MM v[56:59],a[236:239],v[220:223],v[140:143]
EX v34,v34
MM v[56:59],a[232:235],v[204:207],v[56:59]
EX v35,v35
MM v[56:59],a[228:231],v[188:191],v[56:59]
EX v36,v36
MM v[56:59],a[224:227],v[172:175],v[56:59]
s_waitcnt vmcnt(8) lgkmcnt(0)
s_barrier
MM a[0:3],a[128:131],v[64:67],a[0:3]
s_add_u32 m0, s65, 49152
CV v24,v24,v25
CV v25,v26,v27
MM a[0:3],a[132:135],v[68:71],a[0:3]
RD a[192:195],v224 offset:16384
CV v26,v28,v29
MM a[8:11],a[132:135],v[76:79],a[8:11]
RD a[196:199],v225 offset:16384
CV v27,v30,v31
MM a[8:11],a[128:131],v[72:75],a[8:11]
buffer_load_dwordx4 v228, s[8:11], s64 offen offset:0 lds
MM a[16:19],a[128:131],v[80:83],0
RD a[200:203],v226 offset:16384
CV v4,v32,v33
MM a[16:19],a[132:135],v[84:87],a[16:19]
RD a[204:207],v227 offset:16384
CV v5,v34,v35
MM a[24:27],a[132:135],v[92:95],0
buffer_load_dwordx4 v229, s[8:11], s64 offen offset:1024 lds
MM a[24:27],a[128:131],v[88:91],a[24:27]
RD a[208:211],v224 offset:20480
RD a[212:215],v225 offset:20480
MM a[28:31],a[136:139],v[88:91],0
buffer_load_dwordx4 v230, s[8:11], s64 offen offset:2048 lds
MM a[28:31],a[140:143],v[92:95],a[28:31]
RD a[216:219],v226 offset:20480
RD a[220:223],v227 offset:20480
MM a[20:23],a[140:143],v[84:87],0
EX v37,v37
MM a[20:23],a[136:139],v[80:83],a[20:23]
RD a[224:227],v224 offset:24576
CV v6,v36,v37
MM a[12:15],a[136:139],v[72:75],a[12:15]
EX v38,v38
MM a[12:15],a[140:143],v[76:79],a[12:15]
buffer_load_dwordx4 v231, s[8:11], s64 offen offset:3072 lds
MM a[4:7],a[140:143],v[68:71],a[4:7]
s_add_u32 m0, s65, 114688
EX v39,v39
MM a[4:7],a[136:139],v[64:67],a[4:7]
RD a[228:231],v225 offset:24576
CV v7,v38,v39
MM a[32:35],a[144:147],v[64:67],a[32:35]
EX v40,v40
MM a[32:35],a[148:151],v[68:71],a[32:35]
EX v41,v41
MM a[40:43],a[148:151],v[76:79],a[40:43]
RD a[232:235],v226 offset:24576
CV v12,v40,v41
MM a[40:43],a[144:147],v[72:75],a[40:43]
EX v42,v42
MM a[48:51],a[144:147],v[80:83],0
buffer_load_dwordx4 v235, s[12:15], s81 offen offset:0 lds
MM a[48:51],a[148:151],v[84:87],a[48:51]
EX v43,v43
MM a[56:59],a[148:151],v[92:95],0
RD a[236:239],v227 offset:24576
CV v13,v42,v43
MM a[56:59],a[144:147],v[88:91],a[56:59]
EX v44,v44
MM a[60:63],a[152:155],v[88:91],0
EX v45,v45
MM a[60:63],a[156:159],v[92:95],a[60:63]
RD a[240:243],v224 offset:28672
CV v14,v44,v45
MM a[52:55],a[156:159],v[84:87],0
EX v46,v46
MM a[52:55],a[152:155],v[80:83],a[52:55]
buffer_load_dwordx4 v235, s[12:15], s64 offen offset:1024 lds
MM a[44:47],a[152:155],v[72:75],a[44:47]
EX v47,v47
MM a[44:47],a[156:159],v[76:79],a[44:47]
RD a[244:247],v225 offset:28672
CV v15,v46,v47
MM a[36:39],a[156:159],v[68:71],a[36:39]
EX v48,v48
MM a[36:39],a[152:155],v[64:67],a[36:39]
EX v49,v49
MM a[64:67],a[160:163],v[64:67],a[64:67]
RD a[248:251],v226 offset:28672
CV v20,v48,v49
MM a[64:67],a[164:167],v[68:71],a[64:67]
EX v50,v50
MM a[72:75],a[164:167],v[76:79],a[72:75]
buffer_load_dwordx4 v235, s[12:15], s81 offen offset:2048 lds
MM a[72:75],a[160:163],v[72:75],a[72:75]
EX v51,v51
MM a[80:83],a[160:163],v[80:83],0
RD a[252:255],v227 offset:28672
CV v21,v50,v51
MM a[80:83],a[164:167],v[84:87],a[80:83]
EX v52,v52
MM a[88:91],a[164:167],v[92:95],0
EX v53,v53
MM a[88:91],a[160:163],v[88:91],a[88:91]
buffer_load_dwordx4 v235, s[12:15], s64 offen offset:3072 lds
MM a[92:95],a[168:171],v[88:91],0
EX v54,v54
MM a[92:95],a[172:175],v[92:95],a[92:95]
EX v55,v55
MM a[84:87],a[172:175],v[84:87],0
EX v56,v56
MM a[84:87],a[168:171],v[80:83],a[84:87]
EX v57,v57
MM a[76:79],a[168:171],v[72:75],a[76:79]
EX v58,v58
MM a[76:79],a[172:175],v[76:79],a[76:79]
EX v59,v59
MM a[68:71],a[172:175],v[68:71],a[68:71]
EX v60,v60
MM a[68:71],a[168:171],v[64:67],a[68:71]
EX v61,v61
MM a[96:99],a[176:179],v[64:67],a[96:99]
EX v62,v62
MM a[96:99],a[180:183],v[68:71],a[96:99]
EX v63,v63
MM a[104:107],a[180:183],v[76:79],a[104:107]
CV v22,v52,v53
CV v23,v54,v55
MM a[104:107],a[176:179],v[72:75],a[104:107]
CV v28,v56,v57
CV v29,v58,v59
MM a[112:115],a[176:179],v[80:83],0
CV v30,v60,v61
CV v31,v62,v63
MM a[112:115],a[180:183],v[84:87],a[112:115]
MM a[120:123],a[180:183],v[92:95],0
MM a[120:123],a[176:179],v[88:91],a[120:123]
MM a[124:127],a[184:187],v[88:91],0
MM a[124:127],a[188:191],v[92:95],a[124:127]
MM a[116:119],a[188:191],v[84:87],0
MM a[116:119],a[184:187],v[80:83],a[116:119]
MM a[108:111],a[184:187],v[72:75],a[108:111]
MM a[108:111],a[188:191],v[76:79],a[108:111]
MM a[100:103],a[188:191],v[68:71],a[100:103]
MM a[100:103],a[184:187],v[64:67],a[100:103]
v_smfmac_f32_16x16x64_bf16 v[236:239], v[144:147], v[0:7], v234
v_smfmac_f32_16x16x64_bf16 v[236:239], v[148:151], v[8:15], v234
v_smfmac_f32_16x16x64_bf16 v[236:239], v[152:155], v[16:23], v234
v_smfmac_f32_16x16x64_bf16 v[236:239], v[156:159], v[24:31], v234
s_waitcnt lgkmcnt(0)
s_cmp_lg_u32 %[sb], 0
s_cbranch_scc1 LXS_%=
s_getpc_b64 s[98:99]
LTA1_%=:
s_lshl_b32 s73, %[swave], 12
s_add_u32 s73, s73, LPG26_%=-LTA1_%=
s_ashr_i32 s74, s73, 31
s_add_u32 s98, s98, s73
s_addc_u32 s99, s99, s74
s_mov_b64 exec, 0
s_setpc_b64 s[98:99]
LTR1_%=:
s_mov_b64 exec, -1
s_branch LXP_%=
LXS_%=:
s_mov_b32 s70, 2
LLA_%=:
MM v[64:67],a[192:195],v[160:163],v[128:131]
s_add_u32 s64, s64, 16384
TR a[128:129],v232 offset:0
s_add_u32 s81, s64, 896
MM v[64:67],a[196:199],v[176:179],v[64:67]
TR a[130:131],v232 offset:4096
TR a[132:133],v232 offset:8192
MM v[64:67],a[200:203],v[192:195],v[64:67]
TR a[134:135],v232 offset:12288
TR a[136:137],v233 offset:0
MM v[64:67],a[204:207],v[208:211],v[64:67]
TR a[138:139],v233 offset:4096
v_add_f32 v246, v236, v237
MM v[68:71],a[220:223],v[208:211],v[128:131]
TR a[140:141],v233 offset:8192
v_cmp_nge_f32 vcc, s71, v246
MM v[68:71],a[216:219],v[192:195],v[68:71]
s_cbranch_vccnz LSITE8_%=
LBACK8_%=:
TR a[142:143],v233 offset:12288
TR a[144:145],v232 offset:512
MM v[68:71],a[212:215],v[176:179],v[68:71]
s_mov_b32 m0, s67
TR a[146:147],v232 offset:4608
TR a[148:149],v232 offset:8704
MM v[68:71],a[208:211],v[160:163],v[68:71]
buffer_load_dwordx4 v252, s[36:39], s66 offen offset:0 lds
MM v[76:79],a[208:211],v[164:167],v[132:135]
s_add_u32 s66, s66, 55296
EX v64,v64
MM v[76:79],a[212:215],v[180:183],v[76:79]
s_min_u32 s66, s66, s85
EX v65,v65
MM v[76:79],a[216:219],v[196:199],v[76:79]
s_add_u32 s67, s67, 1024
TR a[150:151],v232 offset:12800
CV v64,v64,v65
MM v[76:79],a[220:223],v[212:215],v[76:79]
s_min_u32 s67, s67, s86
EX v66,v66
MM v[72:75],a[204:207],v[212:215],v[132:135]
EX v67,v67
MM v[72:75],a[200:203],v[196:199],v[72:75]
TR a[152:153],v233 offset:512
CV v65,v66,v67
MM v[72:75],a[196:199],v[180:183],v[72:75]
EX v68,v68
MM v[72:75],a[192:195],v[164:167],v[72:75]
EX v69,v69
MM v[80:83],a[192:195],v[168:171],v[136:139]
TR a[154:155],v233 offset:4608
CV v66,v68,v69
MM v[80:83],a[196:199],v[184:187],v[80:83]
EX v70,v70
MM v[80:83],a[200:203],v[200:203],v[80:83]
EX v71,v71
MM v[80:83],a[204:207],v[216:219],v[80:83]
TR a[156:157],v233 offset:8704
CV v67,v70,v71
MM v[84:87],a[220:223],v[216:219],v[136:139]
EX v72,v72
MM v[84:87],a[216:219],v[200:203],v[84:87]
TR a[158:159],v233 offset:12800
TR a[160:161],v232 offset:1024
MM v[84:87],a[212:215],v[184:187],v[84:87]
EX v73,v73
MM v[84:87],a[208:211],v[168:171],v[84:87]
TR a[162:163],v232 offset:5120
CV v72,v72,v73
MM v[92:95],a[208:211],v[172:175],v[140:143]
EX v74,v74
MM v[92:95],a[212:215],v[188:191],v[92:95]
TR a[164:165],v232 offset:9216
TR a[166:167],v232 offset:13312
MM v[92:95],a[216:219],v[204:207],v[92:95]
EX v75,v75
MM v[92:95],a[220:223],v[220:223],v[92:95]
TR a[168:169],v233 offset:1024
CV v73,v74,v75
MM v[88:91],a[204:207],v[220:223],v[140:143]
EX v76,v76
MM v[88:91],a[200:203],v[204:207],v[88:91]
EX v77,v77
MM v[88:91],a[196:199],v[188:191],v[88:91]
TR a[170:171],v233 offset:5120
CV v74,v76,v77
MM v[88:91],a[192:195],v[172:175],v[88:91]
EX v78,v78
MM v[96:99],a[224:227],v[160:163],v[128:131]
TR a[172:173],v233 offset:9216
TR a[174:175],v233 offset:13312
MM v[96:99],a[228:231],v[176:179],v[96:99]
EX v79,v79
MM v[96:99],a[232:235],v[192:195],v[96:99]
TR a[176:177],v232 offset:1536
CV v75,v78,v79
MM v[96:99],a[236:239],v[208:211],v[96:99]
EX v80,v80
MM v[100:103],a[252:255],v[208:211],v[128:131]
EX v81,v81
MM v[100:103],a[248:251],v[192:195],v[100:103]
TR a[178:179],v232 offset:5632
CV v80,v80,v81
MM v[100:103],a[244:247],v[176:179],v[100:103]
EX v82,v82
MM v[100:103],a[240:243],v[160:163],v[100:103]
TR a[180:181],v232 offset:9728
TR a[182:183],v232 offset:13824
MM v[108:111],a[240:243],v[164:167],v[132:135]
EX v83,v83
MM v[108:111],a[244:247],v[180:183],v[108:111]
TR a[184:185],v233 offset:1536
CV v81,v82,v83
MM v[108:111],a[248:251],v[196:199],v[108:111]
EX v84,v84
MM v[108:111],a[252:255],v[212:215],v[108:111]
EX v85,v85
MM v[104:107],a[236:239],v[212:215],v[132:135]
TR a[186:187],v233 offset:5632
CV v82,v84,v85
MM v[104:107],a[232:235],v[196:199],v[104:107]
EX v86,v86
MM v[104:107],a[228:231],v[180:183],v[104:107]
TR a[188:189],v233 offset:9728
TR a[190:191],v233 offset:13824
MM v[104:107],a[224:227],v[164:167],v[104:107]
EX v87,v87
MM v[112:115],a[224:227],v[168:171],v[136:139]
CV v83,v86,v87
MM v[112:115],a[228:231],v[184:187],v[112:115]
EX v88,v88
MM v[112:115],a[232:235],v[200:203],v[112:115]
EX v89,v89
MM v[112:115],a[236:239],v[216:219],v[112:115]
EX v90,v90
MM v[116:119],a[252:255],v[216:219],v[136:139]
EX v91,v91
MM v[116:119],a[248:251],v[200:203],v[116:119]
EX v92,v92
MM v[116:119],a[244:247],v[184:187],v[116:119]
EX v93,v93
MM v[116:119],a[240:243],v[168:171],v[116:119]
EX v94,v94
MM v[124:127],a[240:243],v[172:175],v[140:143]
EX v95,v95
MM v[124:127],a[244:247],v[188:191],v[124:127]
EX v96,v96
MM v[124:127],a[248:251],v[204:207],v[124:127]
EX v97,v97
MM v[124:127],a[252:255],v[220:223],v[124:127]
EX v98,v98
MM v[120:123],a[236:239],v[220:223],v[140:143]
EX v99,v99
MM v[120:123],a[232:235],v[204:207],v[120:123]
EX v100,v100
MM v[120:123],a[228:231],v[188:191],v[120:123]
EX v101,v101
MM v[120:123],a[224:227],v[172:175],v[120:123]
s_waitcnt vmcnt(13) lgkmcnt(0)
s_barrier
MM a[0:3],a[128:131],v[0:3],a[0:3]
s_add_u32 m0, s65, 0
CV v88,v88,v89
CV v89,v90,v91
MM a[0:3],a[132:135],v[4:7],a[0:3]
RD a[192:195],v224 offset:32768
CV v90,v92,v93
)ASM"
R"ASM(
MM a[8:11],a[132:135],v[12:15],a[8:11]
RD a[196:199],v225 offset:32768
CV v91,v94,v95
MM a[8:11],a[128:131],v[8:11],a[8:11]
buffer_load_dwordx4 v228, s[8:11], s64 offen offset:0 lds
MM a[16:19],a[128:131],v[16:19],a[16:19]
RD a[200:203],v226 offset:32768
CV v68,v96,v97
MM a[16:19],a[132:135],v[20:23],a[16:19]
RD a[204:207],v227 offset:32768
CV v69,v98,v99
MM a[24:27],a[132:135],v[28:31],a[24:27]
buffer_load_dwordx4 v229, s[8:11], s64 offen offset:1024 lds
MM a[24:27],a[128:131],v[24:27],a[24:27]
RD a[208:211],v224 offset:36864
CV v70,v100,v101
MM a[28:31],a[136:139],v[24:27],a[28:31]
RD a[212:215],v225 offset:36864
RD a[216:219],v226 offset:36864
MM a[28:31],a[140:143],v[28:31],a[28:31]
buffer_load_dwordx4 v230, s[8:11], s64 offen offset:2048 lds
MM a[20:23],a[140:143],v[20:23],a[20:23]
RD a[220:223],v227 offset:36864
RD a[224:227],v224 offset:40960
MM a[20:23],a[136:139],v[16:19],a[20:23]
EX v102,v102
MM a[12:15],a[136:139],v[8:11],a[12:15]
buffer_load_dwordx4 v231, s[8:11], s64 offen offset:3072 lds
MM a[12:15],a[140:143],v[12:15],a[12:15]
s_add_u32 m0, s65, 65536
EX v103,v103
MM a[4:7],a[140:143],v[4:7],a[4:7]
RD a[228:231],v225 offset:40960
CV v71,v102,v103
MM a[4:7],a[136:139],v[0:3],a[4:7]
EX v104,v104
MM a[32:35],a[144:147],v[0:3],a[32:35]
EX v105,v105
MM a[32:35],a[148:151],v[4:7],a[32:35]
RD a[232:235],v226 offset:40960
CV v76,v104,v105
MM a[40:43],a[148:151],v[12:15],a[40:43]
EX v106,v106
MM a[40:43],a[144:147],v[8:11],a[40:43]
buffer_load_dwordx4 v235, s[12:15], s81 offen offset:0 lds
MM a[48:51],a[144:147],v[16:19],a[48:51]
EX v107,v107
MM a[48:51],a[148:151],v[20:23],a[48:51]
RD a[236:239],v227 offset:40960
CV v77,v106,v107
MM a[56:59],a[148:151],v[28:31],a[56:59]
EX v108,v108
MM a[56:59],a[144:147],v[24:27],a[56:59]
EX v109,v109
MM a[60:63],a[152:155],v[24:27],a[60:63]
RD a[240:243],v224 offset:45056
CV v78,v108,v109
MM a[60:63],a[156:159],v[28:31],a[60:63]
buffer_load_dwordx4 v235, s[12:15], s64 offen offset:1024 lds
MM a[52:55],a[156:159],v[20:23],a[52:55]
EX v110,v110
MM a[52:55],a[152:155],v[16:19],a[52:55]
EX v111,v111
MM a[44:47],a[152:155],v[8:11],a[44:47]
RD a[244:247],v225 offset:45056
CV v79,v110,v111
MM a[44:47],a[156:159],v[12:15],a[44:47]
EX v112,v112
MM a[36:39],a[156:159],v[4:7],a[36:39]
EX v113,v113
MM a[36:39],a[152:155],v[0:3],a[36:39]
RD a[248:251],v226 offset:45056
CV v84,v112,v113
MM a[64:67],a[160:163],v[0:3],a[64:67]
buffer_load_dwordx4 v235, s[12:15], s81 offen offset:2048 lds
MM a[64:67],a[164:167],v[4:7],a[64:67]
EX v114,v114
MM a[72:75],a[164:167],v[12:15],a[72:75]
EX v115,v115
MM a[72:75],a[160:163],v[8:11],a[72:75]
RD a[252:255],v227 offset:45056
CV v85,v114,v115
MM a[80:83],a[160:163],v[16:19],a[80:83]
EX v116,v116
MM a[80:83],a[164:167],v[20:23],a[80:83]
buffer_load_dwordx4 v235, s[12:15], s64 offen offset:3072 lds
MM a[88:91],a[164:167],v[28:31],a[88:91]
EX v117,v117
MM a[88:91],a[160:163],v[24:27],a[88:91]
EX v118,v118
MM a[92:95],a[168:171],v[24:27],a[92:95]
EX v119,v119
MM a[92:95],a[172:175],v[28:31],a[92:95]
EX v120,v120
MM a[84:87],a[172:175],v[20:23],a[84:87]
EX v121,v121
MM a[84:87],a[168:171],v[16:19],a[84:87]
EX v122,v122
MM a[76:79],a[168:171],v[8:11],a[76:79]
EX v123,v123
MM a[76:79],a[172:175],v[12:15],a[76:79]
EX v124,v124
MM a[68:71],a[172:175],v[4:7],a[68:71]
EX v125,v125
MM a[68:71],a[168:171],v[0:3],a[68:71]
EX v126,v126
MM a[96:99],a[176:179],v[0:3],a[96:99]
EX v127,v127
MM a[96:99],a[180:183],v[4:7],a[96:99]
CV v86,v116,v117
CV v87,v118,v119
MM a[104:107],a[180:183],v[12:15],a[104:107]
CV v92,v120,v121
CV v93,v122,v123
MM a[104:107],a[176:179],v[8:11],a[104:107]
CV v94,v124,v125
CV v95,v126,v127
MM a[112:115],a[176:179],v[16:19],a[112:115]
MM a[112:115],a[180:183],v[20:23],a[112:115]
MM a[120:123],a[180:183],v[28:31],a[120:123]
MM a[120:123],a[176:179],v[24:27],a[120:123]
MM a[124:127],a[184:187],v[24:27],a[124:127]
MM a[124:127],a[188:191],v[28:31],a[124:127]
MM a[116:119],a[188:191],v[20:23],a[116:119]
MM a[116:119],a[184:187],v[16:19],a[116:119]
MM a[108:111],a[184:187],v[8:11],a[108:111]
MM a[108:111],a[188:191],v[12:15],a[108:111]
MM a[100:103],a[188:191],v[4:7],a[100:103]
MM a[100:103],a[184:187],v[0:3],a[100:103]
v_smfmac_f32_16x16x64_bf16 v[236:239], v[144:147], v[64:71], v234
v_smfmac_f32_16x16x64_bf16 v[236:239], v[148:151], v[72:79], v234
v_smfmac_f32_16x16x64_bf16 v[236:239], v[152:155], v[80:87], v234
v_smfmac_f32_16x16x64_bf16 v[236:239], v[156:159], v[88:95], v234
s_waitcnt lgkmcnt(0)
MM v[0:3],a[192:195],v[160:163],v[128:131]
s_add_u32 s64, s64, 16384
TR a[128:129],v232 offset:16384
s_add_u32 s81, s64, 896
MM v[0:3],a[196:199],v[176:179],v[0:3]
TR a[130:131],v232 offset:20480
TR a[132:133],v232 offset:24576
MM v[0:3],a[200:203],v[192:195],v[0:3]
TR a[134:135],v232 offset:28672
TR a[136:137],v233 offset:16384
MM v[0:3],a[204:207],v[208:211],v[0:3]
TR a[138:139],v233 offset:20480
v_add_f32 v247, v236, v237
MM v[4:7],a[220:223],v[208:211],v[128:131]
TR a[140:141],v233 offset:24576
v_cmp_nge_f32 vcc, s71, v247
MM v[4:7],a[216:219],v[192:195],v[4:7]
s_cbranch_vccnz LSITE9_%=
LBACK9_%=:
TR a[142:143],v233 offset:28672
TR a[144:145],v232 offset:16896
MM v[4:7],a[212:215],v[176:179],v[4:7]
s_mov_b32 m0, s67
TR a[146:147],v232 offset:20992
TR a[148:149],v232 offset:25088
MM v[4:7],a[208:211],v[160:163],v[4:7]
buffer_load_dwordx4 v252, s[36:39], s66 offen offset:0 lds
MM v[12:15],a[208:211],v[164:167],v[132:135]
s_add_u32 s66, s66, 55296
EX v0,v0
MM v[12:15],a[212:215],v[180:183],v[12:15]
s_min_u32 s66, s66, s85
EX v1,v1
MM v[12:15],a[216:219],v[196:199],v[12:15]
s_add_u32 s67, s67, 1024
TR a[150:151],v232 offset:29184
CV v0,v0,v1
MM v[12:15],a[220:223],v[212:215],v[12:15]
s_min_u32 s67, s67, s86
EX v2,v2
MM v[8:11],a[204:207],v[212:215],v[132:135]
EX v3,v3
MM v[8:11],a[200:203],v[196:199],v[8:11]
TR a[152:153],v233 offset:16896
CV v1,v2,v3
MM v[8:11],a[196:199],v[180:183],v[8:11]
EX v4,v4
MM v[8:11],a[192:195],v[164:167],v[8:11]
EX v5,v5
MM v[16:19],a[192:195],v[168:171],v[136:139]
TR a[154:155],v233 offset:20992
CV v2,v4,v5
MM v[16:19],a[196:199],v[184:187],v[16:19]
EX v6,v6
MM v[16:19],a[200:203],v[200:203],v[16:19]
EX v7,v7
MM v[16:19],a[204:207],v[216:219],v[16:19]
TR a[156:157],v233 offset:25088
CV v3,v6,v7
MM v[20:23],a[220:223],v[216:219],v[136:139]
EX v8,v8
s_nop 0
LPG7_%=:
s_cbranch_execz LPG11_%=
MM v[20:23],a[216:219],v[200:203],v[20:23]
TR a[158:159],v233 offset:29184
TR a[160:161],v232 offset:17408
MM v[20:23],a[212:215],v[184:187],v[20:23]
EX v9,v9
MM v[20:23],a[208:211],v[168:171],v[20:23]
TR a[162:163],v232 offset:21504
CV v8,v8,v9
MM v[28:31],a[208:211],v[172:175],v[140:143]
EX v10,v10
MM v[28:31],a[212:215],v[188:191],v[28:31]
TR a[164:165],v232 offset:25600
TR a[166:167],v232 offset:29696
MM v[28:31],a[216:219],v[204:207],v[28:31]
EX v11,v11
MM v[28:31],a[220:223],v[220:223],v[28:31]
TR a[168:169],v233 offset:17408
CV v9,v10,v11
MM v[24:27],a[204:207],v[220:223],v[140:143]
EX v12,v12
MM v[24:27],a[200:203],v[204:207],v[24:27]
EX v13,v13
MM v[24:27],a[196:199],v[188:191],v[24:27]
TR a[170:171],v233 offset:21504
CV v10,v12,v13
MM v[24:27],a[192:195],v[172:175],v[24:27]
EX v14,v14
MM v[32:35],a[224:227],v[160:163],v[128:131]
TR a[172:173],v233 offset:25600
TR a[174:175],v233 offset:29696
MM v[32:35],a[228:231],v[176:179],v[32:35]
EX v15,v15
MM v[32:35],a[232:235],v[192:195],v[32:35]
TR a[176:177],v232 offset:17920
CV v11,v14,v15
MM v[32:35],a[236:239],v[208:211],v[32:35]
EX v16,v16
MM v[36:39],a[252:255],v[208:211],v[128:131]
EX v17,v17
MM v[36:39],a[248:251],v[192:195],v[36:39]
TR a[178:179],v232 offset:22016
CV v16,v16,v17
MM v[36:39],a[244:247],v[176:179],v[36:39]
EX v18,v18
MM v[36:39],a[240:243],v[160:163],v[36:39]
TR a[180:181],v232 offset:26112
TR a[182:183],v232 offset:30208
MM v[44:47],a[240:243],v[164:167],v[132:135]
EX v19,v19
MM v[44:47],a[244:247],v[180:183],v[44:47]
TR a[184:185],v233 offset:17920
CV v17,v18,v19
MM v[44:47],a[248:251],v[196:199],v[44:47]
EX v20,v20
MM v[44:47],a[252:255],v[212:215],v[44:47]
EX v21,v21
MM v[40:43],a[236:239],v[212:215],v[132:135]
TR a[186:187],v233 offset:22016
CV v18,v20,v21
MM v[40:43],a[232:235],v[196:199],v[40:43]
EX v22,v22
MM v[40:43],a[228:231],v[180:183],v[40:43]
TR a[188:189],v233 offset:26112
TR a[190:191],v233 offset:30208
MM v[40:43],a[224:227],v[164:167],v[40:43]
EX v23,v23
MM v[48:51],a[224:227],v[168:171],v[136:139]
CV v19,v22,v23
MM v[48:51],a[228:231],v[184:187],v[48:51]
EX v24,v24
MM v[48:51],a[232:235],v[200:203],v[48:51]
EX v25,v25
MM v[48:51],a[236:239],v[216:219],v[48:51]
EX v26,v26
MM v[52:55],a[252:255],v[216:219],v[136:139]
EX v27,v27
MM v[52:55],a[248:251],v[200:203],v[52:55]
EX v28,v28
MM v[52:55],a[244:247],v[184:187],v[52:55]
EX v29,v29
MM v[52:55],a[240:243],v[168:171],v[52:55]
EX v30,v30
MM v[60:63],a[240:243],v[172:175],v[140:143]
EX v31,v31
MM v[60:63],a[244:247],v[188:191],v[60:63]
EX v32,v32
MM v[60:63],a[248:251],v[204:207],v[60:63]
EX v33,v33
MM v[60:63],a[252:255],v[220:223],v[60:63]
EX v34,v34
MM v[56:59],a[236:239],v[220:223],v[140:143]
EX v35,v35
MM v[56:59],a[232:235],v[204:207],v[56:59]
EX v36,v36
MM v[56:59],a[228:231],v[188:191],v[56:59]
EX v37,v37
MM v[56:59],a[224:227],v[172:175],v[56:59]
s_waitcnt vmcnt(13) lgkmcnt(0)
s_barrier
MM a[0:3],a[128:131],v[64:67],a[0:3]
s_add_u32 m0, s65, 16384
CV v24,v24,v25
CV v25,v26,v27
MM a[0:3],a[132:135],v[68:71],a[0:3]
RD a[192:195],v224 offset:49152
CV v26,v28,v29
MM a[8:11],a[132:135],v[76:79],a[8:11]
RD a[196:199],v225 offset:49152
CV v27,v30,v31
MM a[8:11],a[128:131],v[72:75],a[8:11]
buffer_load_dwordx4 v228, s[8:11], s64 offen offset:0 lds
MM a[16:19],a[128:131],v[80:83],a[16:19]
RD a[200:203],v226 offset:49152
CV v4,v32,v33
MM a[16:19],a[132:135],v[84:87],a[16:19]
RD a[204:207],v227 offset:49152
CV v5,v34,v35
MM a[24:27],a[132:135],v[92:95],a[24:27]
buffer_load_dwordx4 v229, s[8:11], s64 offen offset:1024 lds
MM a[24:27],a[128:131],v[88:91],a[24:27]
RD a[208:211],v224 offset:53248
CV v6,v36,v37
MM a[28:31],a[136:139],v[88:91],a[28:31]
RD a[212:215],v225 offset:53248
RD a[216:219],v226 offset:53248
MM a[28:31],a[140:143],v[92:95],a[28:31]
buffer_load_dwordx4 v230, s[8:11], s64 offen offset:2048 lds
MM a[20:23],a[140:143],v[84:87],a[20:23]
RD a[220:223],v227 offset:53248
RD a[224:227],v224 offset:57344
MM a[20:23],a[136:139],v[80:83],a[20:23]
EX v38,v38
MM a[12:15],a[136:139],v[72:75],a[12:15]
buffer_load_dwordx4 v231, s[8:11], s64 offen offset:3072 lds
MM a[12:15],a[140:143],v[76:79],a[12:15]
s_add_u32 m0, s65, 81920
EX v39,v39
MM a[4:7],a[140:143],v[68:71],a[4:7]
RD a[228:231],v225 offset:57344
CV v7,v38,v39
MM a[4:7],a[136:139],v[64:67],a[4:7]
EX v40,v40
MM a[32:35],a[144:147],v[64:67],a[32:35]
EX v41,v41
MM a[32:35],a[148:151],v[68:71],a[32:35]
RD a[232:235],v226 offset:57344
CV v12,v40,v41
MM a[40:43],a[148:151],v[76:79],a[40:43]
EX v42,v42
MM a[40:43],a[144:147],v[72:75],a[40:43]
buffer_load_dwordx4 v235, s[12:15], s81 offen offset:0 lds
MM a[48:51],a[144:147],v[80:83],a[48:51]
EX v43,v43
MM a[48:51],a[148:151],v[84:87],a[48:51]
RD a[236:239],v227 offset:57344
CV v13,v42,v43
MM a[56:59],a[148:151],v[92:95],a[56:59]
EX v44,v44
MM a[56:59],a[144:147],v[88:91],a[56:59]
EX v45,v45
MM a[60:63],a[152:155],v[88:91],a[60:63]
RD a[240:243],v224 offset:61440
CV v14,v44,v45
MM a[60:63],a[156:159],v[92:95],a[60:63]
buffer_load_dwordx4 v235, s[12:15], s64 offen offset:1024 lds
MM a[52:55],a[156:159],v[84:87],a[52:55]
EX v46,v46
MM a[52:55],a[152:155],v[80:83],a[52:55]
EX v47,v47
MM a[44:47],a[152:155],v[72:75],a[44:47]
RD a[244:247],v225 offset:61440
CV v15,v46,v47
MM a[44:47],a[156:159],v[76:79],a[44:47]
EX v48,v48
MM a[36:39],a[156:159],v[68:71],a[36:39]
EX v49,v49
MM a[36:39],a[152:155],v[64:67],a[36:39]
RD a[248:251],v226 offset:61440
CV v20,v48,v49
MM a[64:67],a[160:163],v[64:67],a[64:67]
buffer_load_dwordx4 v235, s[12:15], s81 offen offset:2048 lds
MM a[64:67],a[164:167],v[68:71],a[64:67]
EX v50,v50
MM a[72:75],a[164:167],v[76:79],a[72:75]
EX v51,v51
MM a[72:75],a[160:163],v[72:75],a[72:75]
RD a[252:255],v227 offset:61440
CV v21,v50,v51
MM a[80:83],a[160:163],v[80:83],a[80:83]
)ASM"
R"ASM(
EX v52,v52
MM a[80:83],a[164:167],v[84:87],a[80:83]
buffer_load_dwordx4 v235, s[12:15], s64 offen offset:3072 lds
MM a[88:91],a[164:167],v[92:95],a[88:91]
EX v53,v53
MM a[88:91],a[160:163],v[88:91],a[88:91]
EX v54,v54
MM a[92:95],a[168:171],v[88:91],a[92:95]
EX v55,v55
MM a[92:95],a[172:175],v[92:95],a[92:95]
EX v56,v56
MM a[84:87],a[172:175],v[84:87],a[84:87]
EX v57,v57
MM a[84:87],a[168:171],v[80:83],a[84:87]
EX v58,v58
MM a[76:79],a[168:171],v[72:75],a[76:79]
EX v59,v59
MM a[76:79],a[172:175],v[76:79],a[76:79]
EX v60,v60
MM a[68:71],a[172:175],v[68:71],a[68:71]
EX v61,v61
MM a[68:71],a[168:171],v[64:67],a[68:71]
EX v62,v62
MM a[96:99],a[176:179],v[64:67],a[96:99]
EX v63,v63
MM a[96:99],a[180:183],v[68:71],a[96:99]
CV v22,v52,v53
CV v23,v54,v55
MM a[104:107],a[180:183],v[76:79],a[104:107]
CV v28,v56,v57
CV v29,v58,v59
MM a[104:107],a[176:179],v[72:75],a[104:107]
CV v30,v60,v61
CV v31,v62,v63
MM a[112:115],a[176:179],v[80:83],a[112:115]
MM a[112:115],a[180:183],v[84:87],a[112:115]
MM a[120:123],a[180:183],v[92:95],a[120:123]
MM a[120:123],a[176:179],v[88:91],a[120:123]
MM a[124:127],a[184:187],v[88:91],a[124:127]
MM a[124:127],a[188:191],v[92:95],a[124:127]
MM a[116:119],a[188:191],v[84:87],a[116:119]
MM a[116:119],a[184:187],v[80:83],a[116:119]
MM a[108:111],a[184:187],v[72:75],a[108:111]
MM a[108:111],a[188:191],v[76:79],a[108:111]
MM a[100:103],a[188:191],v[68:71],a[100:103]
MM a[100:103],a[184:187],v[64:67],a[100:103]
v_smfmac_f32_16x16x64_bf16 v[236:239], v[144:147], v[0:7], v234
v_smfmac_f32_16x16x64_bf16 v[236:239], v[148:151], v[8:15], v234
v_smfmac_f32_16x16x64_bf16 v[236:239], v[152:155], v[16:23], v234
v_smfmac_f32_16x16x64_bf16 v[236:239], v[156:159], v[24:31], v234
s_waitcnt lgkmcnt(0)
MM v[64:67],a[192:195],v[160:163],v[128:131]
s_add_u32 s64, s64, 16384
TR a[128:129],v232 offset:32768
s_add_u32 s81, s64, 896
MM v[64:67],a[196:199],v[176:179],v[64:67]
TR a[130:131],v232 offset:36864
TR a[132:133],v232 offset:40960
MM v[64:67],a[200:203],v[192:195],v[64:67]
TR a[134:135],v232 offset:45056
TR a[136:137],v233 offset:32768
MM v[64:67],a[204:207],v[208:211],v[64:67]
TR a[138:139],v233 offset:36864
v_add_f32 v246, v236, v237
MM v[68:71],a[220:223],v[208:211],v[128:131]
TR a[140:141],v233 offset:40960
v_cmp_nge_f32 vcc, s71, v246
MM v[68:71],a[216:219],v[192:195],v[68:71]
s_cbranch_vccnz LSITE10_%=
LBACK10_%=:
TR a[142:143],v233 offset:45056
TR a[144:145],v232 offset:33280
MM v[68:71],a[212:215],v[176:179],v[68:71]
s_mov_b32 m0, s67
TR a[146:147],v232 offset:37376
TR a[148:149],v232 offset:41472
MM v[68:71],a[208:211],v[160:163],v[68:71]
buffer_load_dwordx4 v252, s[36:39], s66 offen offset:0 lds
MM v[76:79],a[208:211],v[164:167],v[132:135]
s_add_u32 s66, s66, 55296
EX v64,v64
MM v[76:79],a[212:215],v[180:183],v[76:79]
s_min_u32 s66, s66, s85
EX v65,v65
MM v[76:79],a[216:219],v[196:199],v[76:79]
s_add_u32 s67, s67, 1024
TR a[150:151],v232 offset:45568
CV v64,v64,v65
MM v[76:79],a[220:223],v[212:215],v[76:79]
s_min_u32 s67, s67, s86
EX v66,v66
MM v[72:75],a[204:207],v[212:215],v[132:135]
EX v67,v67
MM v[72:75],a[200:203],v[196:199],v[72:75]
TR a[152:153],v233 offset:33280
CV v65,v66,v67
MM v[72:75],a[196:199],v[180:183],v[72:75]
EX v68,v68
MM v[72:75],a[192:195],v[164:167],v[72:75]
EX v69,v69
MM v[80:83],a[192:195],v[168:171],v[136:139]
TR a[154:155],v233 offset:37376
CV v66,v68,v69
MM v[80:83],a[196:199],v[184:187],v[80:83]
EX v70,v70
MM v[80:83],a[200:203],v[200:203],v[80:83]
EX v71,v71
MM v[80:83],a[204:207],v[216:219],v[80:83]
TR a[156:157],v233 offset:41472
CV v67,v70,v71
MM v[84:87],a[220:223],v[216:219],v[136:139]
EX v72,v72
MM v[84:87],a[216:219],v[200:203],v[84:87]
TR a[158:159],v233 offset:45568
TR a[160:161],v232 offset:33792
MM v[84:87],a[212:215],v[184:187],v[84:87]
EX v73,v73
MM v[84:87],a[208:211],v[168:171],v[84:87]
TR a[162:163],v232 offset:37888
CV v72,v72,v73
MM v[92:95],a[208:211],v[172:175],v[140:143]
EX v74,v74
MM v[92:95],a[212:215],v[188:191],v[92:95]
TR a[164:165],v232 offset:41984
TR a[166:167],v232 offset:46080
MM v[92:95],a[216:219],v[204:207],v[92:95]
EX v75,v75
MM v[92:95],a[220:223],v[220:223],v[92:95]
TR a[168:169],v233 offset:33792
CV v73,v74,v75
MM v[88:91],a[204:207],v[220:223],v[140:143]
EX v76,v76
MM v[88:91],a[200:203],v[204:207],v[88:91]
EX v77,v77
MM v[88:91],a[196:199],v[188:191],v[88:91]
TR a[170:171],v233 offset:37888
CV v74,v76,v77
MM v[88:91],a[192:195],v[172:175],v[88:91]
EX v78,v78
MM v[96:99],a[224:227],v[160:163],v[128:131]
TR a[172:173],v233 offset:41984
TR a[174:175],v233 offset:46080
MM v[96:99],a[228:231],v[176:179],v[96:99]
EX v79,v79
MM v[96:99],a[232:235],v[192:195],v[96:99]
TR a[176:177],v232 offset:34304
CV v75,v78,v79
MM v[96:99],a[236:239],v[208:211],v[96:99]
EX v80,v80
MM v[100:103],a[252:255],v[208:211],v[128:131]
EX v81,v81
MM v[100:103],a[248:251],v[192:195],v[100:103]
TR a[178:179],v232 offset:38400
CV v80,v80,v81
MM v[100:103],a[244:247],v[176:179],v[100:103]
EX v82,v82
MM v[100:103],a[240:243],v[160:163],v[100:103]
TR a[180:181],v232 offset:42496
TR a[182:183],v232 offset:46592
MM v[108:111],a[240:243],v[164:167],v[132:135]
EX v83,v83
MM v[108:111],a[244:247],v[180:183],v[108:111]
TR a[184:185],v233 offset:34304
CV v81,v82,v83
MM v[108:111],a[248:251],v[196:199],v[108:111]
EX v84,v84
MM v[108:111],a[252:255],v[212:215],v[108:111]
EX v85,v85
MM v[104:107],a[236:239],v[212:215],v[132:135]
TR a[186:187],v233 offset:38400
CV v82,v84,v85
MM v[104:107],a[232:235],v[196:199],v[104:107]
EX v86,v86
MM v[104:107],a[228:231],v[180:183],v[104:107]
TR a[188:189],v233 offset:42496
TR a[190:191],v233 offset:46592
MM v[104:107],a[224:227],v[164:167],v[104:107]
EX v87,v87
MM v[112:115],a[224:227],v[168:171],v[136:139]
CV v83,v86,v87
MM v[112:115],a[228:231],v[184:187],v[112:115]
EX v88,v88
MM v[112:115],a[232:235],v[200:203],v[112:115]
EX v89,v89
MM v[112:115],a[236:239],v[216:219],v[112:115]
EX v90,v90
MM v[116:119],a[252:255],v[216:219],v[136:139]
EX v91,v91
MM v[116:119],a[248:251],v[200:203],v[116:119]
EX v92,v92
MM v[116:119],a[244:247],v[184:187],v[116:119]
EX v93,v93
MM v[116:119],a[240:243],v[168:171],v[116:119]
EX v94,v94
MM v[124:127],a[240:243],v[172:175],v[140:143]
EX v95,v95
MM v[124:127],a[244:247],v[188:191],v[124:127]
EX v96,v96
MM v[124:127],a[248:251],v[204:207],v[124:127]
EX v97,v97
MM v[124:127],a[252:255],v[220:223],v[124:127]
EX v98,v98
MM v[120:123],a[236:239],v[220:223],v[140:143]
EX v99,v99
MM v[120:123],a[232:235],v[204:207],v[120:123]
EX v100,v100
MM v[120:123],a[228:231],v[188:191],v[120:123]
EX v101,v101
MM v[120:123],a[224:227],v[172:175],v[120:123]
s_waitcnt vmcnt(13) lgkmcnt(0)
s_barrier
MM a[0:3],a[128:131],v[0:3],a[0:3]
s_add_u32 m0, s65, 32768
CV v88,v88,v89
CV v89,v90,v91
MM a[0:3],a[132:135],v[4:7],a[0:3]
RD a[192:195],v224 offset:0
CV v90,v92,v93
MM a[8:11],a[132:135],v[12:15],a[8:11]
RD a[196:199],v225 offset:0
CV v91,v94,v95
MM a[8:11],a[128:131],v[8:11],a[8:11]
buffer_load_dwordx4 v228, s[8:11], s64 offen offset:0 lds
MM a[16:19],a[128:131],v[16:19],a[16:19]
RD a[200:203],v226 offset:0
CV v68,v96,v97
MM a[16:19],a[132:135],v[20:23],a[16:19]
RD a[204:207],v227 offset:0
CV v69,v98,v99
MM a[24:27],a[132:135],v[28:31],a[24:27]
buffer_load_dwordx4 v229, s[8:11], s64 offen offset:1024 lds
MM a[24:27],a[128:131],v[24:27],a[24:27]
RD a[208:211],v224 offset:4096
CV v70,v100,v101
MM a[28:31],a[136:139],v[24:27],a[28:31]
RD a[212:215],v225 offset:4096
RD a[216:219],v226 offset:4096
MM a[28:31],a[140:143],v[28:31],a[28:31]
buffer_load_dwordx4 v230, s[8:11], s64 offen offset:2048 lds
MM a[20:23],a[140:143],v[20:23],a[20:23]
RD a[220:223],v227 offset:4096
RD a[224:227],v224 offset:8192
MM a[20:23],a[136:139],v[16:19],a[20:23]
EX v102,v102
MM a[12:15],a[136:139],v[8:11],a[12:15]
buffer_load_dwordx4 v231, s[8:11], s64 offen offset:3072 lds
MM a[12:15],a[140:143],v[12:15],a[12:15]
s_add_u32 m0, s65, 98304
EX v103,v103
MM a[4:7],a[140:143],v[4:7],a[4:7]
RD a[228:231],v225 offset:8192
CV v71,v102,v103
MM a[4:7],a[136:139],v[0:3],a[4:7]
EX v104,v104
MM a[32:35],a[144:147],v[0:3],a[32:35]
EX v105,v105
MM a[32:35],a[148:151],v[4:7],a[32:35]
RD a[232:235],v226 offset:8192
CV v76,v104,v105
MM a[40:43],a[148:151],v[12:15],a[40:43]
EX v106,v106
MM a[40:43],a[144:147],v[8:11],a[40:43]
buffer_load_dwordx4 v235, s[12:15], s81 offen offset:0 lds
MM a[48:51],a[144:147],v[16:19],a[48:51]
EX v107,v107
MM a[48:51],a[148:151],v[20:23],a[48:51]
RD a[236:239],v227 offset:8192
CV v77,v106,v107
MM a[56:59],a[148:151],v[28:31],a[56:59]
EX v108,v108
MM a[56:59],a[144:147],v[24:27],a[56:59]
EX v109,v109
MM a[60:63],a[152:155],v[24:27],a[60:63]
RD a[240:243],v224 offset:12288
CV v78,v108,v109
MM a[60:63],a[156:159],v[28:31],a[60:63]
buffer_load_dwordx4 v235, s[12:15], s64 offen offset:1024 lds
MM a[52:55],a[156:159],v[20:23],a[52:55]
EX v110,v110
MM a[52:55],a[152:155],v[16:19],a[52:55]
EX v111,v111
MM a[44:47],a[152:155],v[8:11],a[44:47]
RD a[244:247],v225 offset:12288
CV v79,v110,v111
MM a[44:47],a[156:159],v[12:15],a[44:47]
EX v112,v112
MM a[36:39],a[156:159],v[4:7],a[36:39]
EX v113,v113
MM a[36:39],a[152:155],v[0:3],a[36:39]
RD a[248:251],v226 offset:12288
CV v84,v112,v113
MM a[64:67],a[160:163],v[0:3],a[64:67]
buffer_load_dwordx4 v235, s[12:15], s81 offen offset:2048 lds
MM a[64:67],a[164:167],v[4:7],a[64:67]
EX v114,v114
MM a[72:75],a[164:167],v[12:15],a[72:75]
EX v115,v115
MM a[72:75],a[160:163],v[8:11],a[72:75]
RD a[252:255],v227 offset:12288
CV v85,v114,v115
MM a[80:83],a[160:163],v[16:19],a[80:83]
EX v116,v116
MM a[80:83],a[164:167],v[20:23],a[80:83]
buffer_load_dwordx4 v235, s[12:15], s64 offen offset:3072 lds
MM a[88:91],a[164:167],v[28:31],a[88:91]
EX v117,v117
MM a[88:91],a[160:163],v[24:27],a[88:91]
EX v118,v118
MM a[92:95],a[168:171],v[24:27],a[92:95]
EX v119,v119
MM a[92:95],a[172:175],v[28:31],a[92:95]
EX v120,v120
MM a[84:87],a[172:175],v[20:23],a[84:87]
EX v121,v121
MM a[84:87],a[168:171],v[16:19],a[84:87]
EX v122,v122
MM a[76:79],a[168:171],v[8:11],a[76:79]
EX v123,v123
MM a[76:79],a[172:175],v[12:15],a[76:79]
EX v124,v124
MM a[68:71],a[172:175],v[4:7],a[68:71]
EX v125,v125
MM a[68:71],a[168:171],v[0:3],a[68:71]
EX v126,v126
MM a[96:99],a[176:179],v[0:3],a[96:99]
EX v127,v127
MM a[96:99],a[180:183],v[4:7],a[96:99]
CV v86,v116,v117
CV v87,v118,v119
MM a[104:107],a[180:183],v[12:15],a[104:107]
CV v92,v120,v121
CV v93,v122,v123
MM a[104:107],a[176:179],v[8:11],a[104:107]
CV v94,v124,v125
CV v95,v126,v127
MM a[112:115],a[176:179],v[16:19],a[112:115]
MM a[112:115],a[180:183],v[20:23],a[112:115]
MM a[120:123],a[180:183],v[28:31],a[120:123]
MM a[120:123],a[176:179],v[24:27],a[120:123]
MM a[124:127],a[184:187],v[24:27],a[124:127]
MM a[124:127],a[188:191],v[28:31],a[124:127]
MM a[116:119],a[188:191],v[20:23],a[116:119]
MM a[116:119],a[184:187],v[16:19],a[116:119]
MM a[108:111],a[184:187],v[8:11],a[108:111]
MM a[108:111],a[188:191],v[12:15],a[108:111]
MM a[100:103],a[188:191],v[4:7],a[100:103]
MM a[100:103],a[184:187],v[0:3],a[100:103]
v_smfmac_f32_16x16x64_bf16 v[236:239], v[144:147], v[64:71], v234
v_smfmac_f32_16x16x64_bf16 v[236:239], v[148:151], v[72:79], v234
v_smfmac_f32_16x16x64_bf16 v[236:239], v[152:155], v[80:87], v234
v_smfmac_f32_16x16x64_bf16 v[236:239], v[156:159], v[88:95], v234
s_waitcnt lgkmcnt(0)
MM v[0:3],a[192:195],v[160:163],v[128:131]
s_add_u32 s64, s64, 16384
TR a[128:129],v232 offset:49152
s_add_u32 s81, s64, 896
MM v[0:3],a[196:199],v[176:179],v[0:3]
TR a[130:131],v232 offset:53248
TR a[132:133],v232 offset:57344
MM v[0:3],a[200:203],v[192:195],v[0:3]
TR a[134:135],v232 offset:61440
TR a[136:137],v233 offset:49152
MM v[0:3],a[204:207],v[208:211],v[0:3]
TR a[138:139],v233 offset:53248
v_add_f32 v247, v236, v237
MM v[4:7],a[220:223],v[208:211],v[128:131]
TR a[140:141],v233 offset:57344
v_cmp_nge_f32 vcc, s71, v247
MM v[4:7],a[216:219],v[192:195],v[4:7]
s_cbranch_vccnz LSITE11_%=
LBACK11_%=:
TR a[142:143],v233 offset:61440
TR a[144:145],v232 offset:49664
MM v[4:7],a[212:215],v[176:179],v[4:7]
s_mov_b32 m0, s67
TR a[146:147],v232 offset:53760
TR a[148:149],v232 offset:57856
MM v[4:7],a[208:211],v[160:163],v[4:7]
)ASM"
R"ASM(
buffer_load_dwordx4 v252, s[36:39], s66 offen offset:0 lds
MM v[12:15],a[208:211],v[164:167],v[132:135]
s_add_u32 s66, s66, 55296
EX v0,v0
MM v[12:15],a[212:215],v[180:183],v[12:15]
s_min_u32 s66, s66, s85
EX v1,v1
MM v[12:15],a[216:219],v[196:199],v[12:15]
s_add_u32 s67, s67, 1024
TR a[150:151],v232 offset:61952
CV v0,v0,v1
MM v[12:15],a[220:223],v[212:215],v[12:15]
s_min_u32 s67, s67, s86
EX v2,v2
MM v[8:11],a[204:207],v[212:215],v[132:135]
EX v3,v3
MM v[8:11],a[200:203],v[196:199],v[8:11]
LPG8_%=:
s_cbranch_execz LPG12_%=
TR a[152:153],v233 offset:49664
CV v1,v2,v3
MM v[8:11],a[196:199],v[180:183],v[8:11]
EX v4,v4
MM v[8:11],a[192:195],v[164:167],v[8:11]
EX v5,v5
MM v[16:19],a[192:195],v[168:171],v[136:139]
TR a[154:155],v233 offset:53760
CV v2,v4,v5
MM v[16:19],a[196:199],v[184:187],v[16:19]
EX v6,v6
MM v[16:19],a[200:203],v[200:203],v[16:19]
EX v7,v7
MM v[16:19],a[204:207],v[216:219],v[16:19]
TR a[156:157],v233 offset:57856
CV v3,v6,v7
MM v[20:23],a[220:223],v[216:219],v[136:139]
EX v8,v8
MM v[20:23],a[216:219],v[200:203],v[20:23]
TR a[158:159],v233 offset:61952
TR a[160:161],v232 offset:50176
MM v[20:23],a[212:215],v[184:187],v[20:23]
EX v9,v9
MM v[20:23],a[208:211],v[168:171],v[20:23]
TR a[162:163],v232 offset:54272
CV v8,v8,v9
MM v[28:31],a[208:211],v[172:175],v[140:143]
EX v10,v10
MM v[28:31],a[212:215],v[188:191],v[28:31]
TR a[164:165],v232 offset:58368
TR a[166:167],v232 offset:62464
MM v[28:31],a[216:219],v[204:207],v[28:31]
EX v11,v11
MM v[28:31],a[220:223],v[220:223],v[28:31]
TR a[168:169],v233 offset:50176
CV v9,v10,v11
MM v[24:27],a[204:207],v[220:223],v[140:143]
EX v12,v12
MM v[24:27],a[200:203],v[204:207],v[24:27]
EX v13,v13
MM v[24:27],a[196:199],v[188:191],v[24:27]
TR a[170:171],v233 offset:54272
CV v10,v12,v13
MM v[24:27],a[192:195],v[172:175],v[24:27]
EX v14,v14
MM v[32:35],a[224:227],v[160:163],v[128:131]
TR a[172:173],v233 offset:58368
TR a[174:175],v233 offset:62464
MM v[32:35],a[228:231],v[176:179],v[32:35]
EX v15,v15
MM v[32:35],a[232:235],v[192:195],v[32:35]
TR a[176:177],v232 offset:50688
CV v11,v14,v15
MM v[32:35],a[236:239],v[208:211],v[32:35]
EX v16,v16
MM v[36:39],a[252:255],v[208:211],v[128:131]
EX v17,v17
MM v[36:39],a[248:251],v[192:195],v[36:39]
TR a[178:179],v232 offset:54784
CV v16,v16,v17
MM v[36:39],a[244:247],v[176:179],v[36:39]
EX v18,v18
MM v[36:39],a[240:243],v[160:163],v[36:39]
TR a[180:181],v232 offset:58880
TR a[182:183],v232 offset:62976
MM v[44:47],a[240:243],v[164:167],v[132:135]
EX v19,v19
MM v[44:47],a[244:247],v[180:183],v[44:47]
TR a[184:185],v233 offset:50688
CV v17,v18,v19
MM v[44:47],a[248:251],v[196:199],v[44:47]
EX v20,v20
MM v[44:47],a[252:255],v[212:215],v[44:47]
EX v21,v21
MM v[40:43],a[236:239],v[212:215],v[132:135]
TR a[186:187],v233 offset:54784
CV v18,v20,v21
MM v[40:43],a[232:235],v[196:199],v[40:43]
EX v22,v22
MM v[40:43],a[228:231],v[180:183],v[40:43]
TR a[188:189],v233 offset:58880
TR a[190:191],v233 offset:62976
MM v[40:43],a[224:227],v[164:167],v[40:43]
EX v23,v23
MM v[48:51],a[224:227],v[168:171],v[136:139]
CV v19,v22,v23
MM v[48:51],a[228:231],v[184:187],v[48:51]
EX v24,v24
MM v[48:51],a[232:235],v[200:203],v[48:51]
EX v25,v25
MM v[48:51],a[236:239],v[216:219],v[48:51]
EX v26,v26
MM v[52:55],a[252:255],v[216:219],v[136:139]
EX v27,v27
MM v[52:55],a[248:251],v[200:203],v[52:55]
EX v28,v28
MM v[52:55],a[244:247],v[184:187],v[52:55]
EX v29,v29
MM v[52:55],a[240:243],v[168:171],v[52:55]
EX v30,v30
MM v[60:63],a[240:243],v[172:175],v[140:143]
EX v31,v31
MM v[60:63],a[244:247],v[188:191],v[60:63]
EX v32,v32
MM v[60:63],a[248:251],v[204:207],v[60:63]
EX v33,v33
MM v[60:63],a[252:255],v[220:223],v[60:63]
EX v34,v34
MM v[56:59],a[236:239],v[220:223],v[140:143]
EX v35,v35
MM v[56:59],a[232:235],v[204:207],v[56:59]
EX v36,v36
MM v[56:59],a[228:231],v[188:191],v[56:59]
EX v37,v37
MM v[56:59],a[224:227],v[172:175],v[56:59]
s_waitcnt vmcnt(13) lgkmcnt(0)
s_barrier
MM a[0:3],a[128:131],v[64:67],a[0:3]
s_add_u32 m0, s65, 49152
CV v24,v24,v25
CV v25,v26,v27
MM a[0:3],a[132:135],v[68:71],a[0:3]
RD a[192:195],v224 offset:16384
CV v26,v28,v29
MM a[8:11],a[132:135],v[76:79],a[8:11]
RD a[196:199],v225 offset:16384
CV v27,v30,v31
MM a[8:11],a[128:131],v[72:75],a[8:11]
buffer_load_dwordx4 v228, s[8:11], s64 offen offset:0 lds
MM a[16:19],a[128:131],v[80:83],a[16:19]
RD a[200:203],v226 offset:16384
CV v4,v32,v33
MM a[16:19],a[132:135],v[84:87],a[16:19]
RD a[204:207],v227 offset:16384
CV v5,v34,v35
MM a[24:27],a[132:135],v[92:95],a[24:27]
buffer_load_dwordx4 v229, s[8:11], s64 offen offset:1024 lds
MM a[24:27],a[128:131],v[88:91],a[24:27]
RD a[208:211],v224 offset:20480
CV v6,v36,v37
MM a[28:31],a[136:139],v[88:91],a[28:31]
RD a[212:215],v225 offset:20480
RD a[216:219],v226 offset:20480
MM a[28:31],a[140:143],v[92:95],a[28:31]
buffer_load_dwordx4 v230, s[8:11], s64 offen offset:2048 lds
MM a[20:23],a[140:143],v[84:87],a[20:23]
RD a[220:223],v227 offset:20480
RD a[224:227],v224 offset:24576
MM a[20:23],a[136:139],v[80:83],a[20:23]
EX v38,v38
MM a[12:15],a[136:139],v[72:75],a[12:15]
buffer_load_dwordx4 v231, s[8:11], s64 offen offset:3072 lds
MM a[12:15],a[140:143],v[76:79],a[12:15]
s_add_u32 m0, s65, 114688
EX v39,v39
MM a[4:7],a[140:143],v[68:71],a[4:7]
RD a[228:231],v225 offset:24576
CV v7,v38,v39
MM a[4:7],a[136:139],v[64:67],a[4:7]
EX v40,v40
MM a[32:35],a[144:147],v[64:67],a[32:35]
EX v41,v41
MM a[32:35],a[148:151],v[68:71],a[32:35]
RD a[232:235],v226 offset:24576
CV v12,v40,v41
MM a[40:43],a[148:151],v[76:79],a[40:43]
EX v42,v42
MM a[40:43],a[144:147],v[72:75],a[40:43]
buffer_load_dwordx4 v235, s[12:15], s81 offen offset:0 lds
MM a[48:51],a[144:147],v[80:83],a[48:51]
EX v43,v43
MM a[48:51],a[148:151],v[84:87],a[48:51]
RD a[236:239],v227 offset:24576
CV v13,v42,v43
MM a[56:59],a[148:151],v[92:95],a[56:59]
EX v44,v44
MM a[56:59],a[144:147],v[88:91],a[56:59]
EX v45,v45
MM a[60:63],a[152:155],v[88:91],a[60:63]
RD a[240:243],v224 offset:28672
CV v14,v44,v45
MM a[60:63],a[156:159],v[92:95],a[60:63]
buffer_load_dwordx4 v235, s[12:15], s64 offen offset:1024 lds
MM a[52:55],a[156:159],v[84:87],a[52:55]
EX v46,v46
MM a[52:55],a[152:155],v[80:83],a[52:55]
EX v47,v47
MM a[44:47],a[152:155],v[72:75],a[44:47]
RD a[244:247],v225 offset:28672
CV v15,v46,v47
MM a[44:47],a[156:159],v[76:79],a[44:47]
EX v48,v48
MM a[36:39],a[156:159],v[68:71],a[36:39]
EX v49,v49
MM a[36:39],a[152:155],v[64:67],a[36:39]
RD a[248:251],v226 offset:28672
CV v20,v48,v49
MM a[64:67],a[160:163],v[64:67],a[64:67]
buffer_load_dwordx4 v235, s[12:15], s81 offen offset:2048 lds
MM a[64:67],a[164:167],v[68:71],a[64:67]
EX v50,v50
MM a[72:75],a[164:167],v[76:79],a[72:75]
EX v51,v51
MM a[72:75],a[160:163],v[72:75],a[72:75]
RD a[252:255],v227 offset:28672
CV v21,v50,v51
MM a[80:83],a[160:163],v[80:83],a[80:83]
EX v52,v52
MM a[80:83],a[164:167],v[84:87],a[80:83]
buffer_load_dwordx4 v235, s[12:15], s64 offen offset:3072 lds
MM a[88:91],a[164:167],v[92:95],a[88:91]
EX v53,v53
MM a[88:91],a[160:163],v[88:91],a[88:91]
EX v54,v54
MM a[92:95],a[168:171],v[88:91],a[92:95]
EX v55,v55
MM a[92:95],a[172:175],v[92:95],a[92:95]
EX v56,v56
MM a[84:87],a[172:175],v[84:87],a[84:87]
EX v57,v57
MM a[84:87],a[168:171],v[80:83],a[84:87]
EX v58,v58
MM a[76:79],a[168:171],v[72:75],a[76:79]
EX v59,v59
MM a[76:79],a[172:175],v[76:79],a[76:79]
EX v60,v60
MM a[68:71],a[172:175],v[68:71],a[68:71]
EX v61,v61
MM a[68:71],a[168:171],v[64:67],a[68:71]
EX v62,v62
MM a[96:99],a[176:179],v[64:67],a[96:99]
EX v63,v63
MM a[96:99],a[180:183],v[68:71],a[96:99]
CV v22,v52,v53
CV v23,v54,v55
MM a[104:107],a[180:183],v[76:79],a[104:107]
CV v28,v56,v57
CV v29,v58,v59
MM a[104:107],a[176:179],v[72:75],a[104:107]
CV v30,v60,v61
CV v31,v62,v63
MM a[112:115],a[176:179],v[80:83],a[112:115]
MM a[112:115],a[180:183],v[84:87],a[112:115]
MM a[120:123],a[180:183],v[92:95],a[120:123]
MM a[120:123],a[176:179],v[88:91],a[120:123]
MM a[124:127],a[184:187],v[88:91],a[124:127]
MM a[124:127],a[188:191],v[92:95],a[124:127]
MM a[116:119],a[188:191],v[84:87],a[116:119]
MM a[116:119],a[184:187],v[80:83],a[116:119]
MM a[108:111],a[184:187],v[72:75],a[108:111]
MM a[108:111],a[188:191],v[76:79],a[108:111]
MM a[100:103],a[188:191],v[68:71],a[100:103]
MM a[100:103],a[184:187],v[64:67],a[100:103]
v_smfmac_f32_16x16x64_bf16 v[236:239], v[144:147], v[0:7], v234
v_smfmac_f32_16x16x64_bf16 v[236:239], v[148:151], v[8:15], v234
v_smfmac_f32_16x16x64_bf16 v[236:239], v[152:155], v[16:23], v234
v_smfmac_f32_16x16x64_bf16 v[236:239], v[156:159], v[24:31], v234
s_waitcnt lgkmcnt(0)
s_sub_u32 s70, s70, 1
s_cmp_lg_u32 s70, 0
s_cbranch_scc1 LLA_%=
MM v[64:67],a[192:195],v[160:163],v[128:131]
s_add_u32 s64, s64, 16384
TR a[128:129],v232 offset:0
s_add_u32 s81, s64, 896
MM v[64:67],a[196:199],v[176:179],v[64:67]
TR a[130:131],v232 offset:4096
TR a[132:133],v232 offset:8192
MM v[64:67],a[200:203],v[192:195],v[64:67]
TR a[134:135],v232 offset:12288
TR a[136:137],v233 offset:0
MM v[64:67],a[204:207],v[208:211],v[64:67]
TR a[138:139],v233 offset:4096
v_add_f32 v246, v236, v237
MM v[68:71],a[220:223],v[208:211],v[128:131]
TR a[140:141],v233 offset:8192
v_cmp_nge_f32 vcc, s71, v246
MM v[68:71],a[216:219],v[192:195],v[68:71]
s_cbranch_vccnz LSITE12_%=
LBACK12_%=:
TR a[142:143],v233 offset:12288
TR a[144:145],v232 offset:512
MM v[68:71],a[212:215],v[176:179],v[68:71]
v_lshrrev_b32 v52, 1, v248
v_and_b32 v52, 56, v52
MM v[68:71],a[208:211],v[160:163],v[68:71]
s_mov_b32 s90, 86016
buffer_load_dwordx2 v[48:49], v52, s[20:23], s90 offen offset:0 sc1
MM v[76:79],a[208:211],v[164:167],v[132:135]
EX v64,v64
MM v[76:79],a[212:215],v[180:183],v[76:79]
EX v65,v65
MM v[76:79],a[216:219],v[196:199],v[76:79]
TR a[146:147],v232 offset:4608
CV v64,v64,v65
MM v[76:79],a[220:223],v[212:215],v[76:79]
EX v66,v66
MM v[72:75],a[204:207],v[212:215],v[132:135]
EX v67,v67
MM v[72:75],a[200:203],v[196:199],v[72:75]
TR a[148:149],v232 offset:8704
CV v65,v66,v67
MM v[72:75],a[196:199],v[180:183],v[72:75]
EX v68,v68
MM v[72:75],a[192:195],v[164:167],v[72:75]
TR a[150:151],v232 offset:12800
TR a[152:153],v233 offset:512
MM v[80:83],a[192:195],v[168:171],v[136:139]
EX v69,v69
MM v[80:83],a[196:199],v[184:187],v[80:83]
TR a[154:155],v233 offset:4608
CV v66,v68,v69
MM v[80:83],a[200:203],v[200:203],v[80:83]
EX v70,v70
MM v[80:83],a[204:207],v[216:219],v[80:83]
EX v71,v71
MM v[84:87],a[220:223],v[216:219],v[136:139]
TR a[156:157],v233 offset:8704
CV v67,v70,v71
MM v[84:87],a[216:219],v[200:203],v[84:87]
EX v72,v72
MM v[84:87],a[212:215],v[184:187],v[84:87]
TR a[158:159],v233 offset:12800
TR a[160:161],v232 offset:1024
MM v[84:87],a[208:211],v[168:171],v[84:87]
EX v73,v73
MM v[92:95],a[208:211],v[172:175],v[140:143]
TR a[162:163],v232 offset:5120
CV v72,v72,v73
MM v[92:95],a[212:215],v[188:191],v[92:95]
EX v74,v74
MM v[92:95],a[216:219],v[204:207],v[92:95]
EX v75,v75
MM v[92:95],a[220:223],v[220:223],v[92:95]
TR a[164:165],v232 offset:9216
CV v73,v74,v75
MM v[88:91],a[204:207],v[220:223],v[140:143]
EX v76,v76
MM v[88:91],a[200:203],v[204:207],v[88:91]
TR a[166:167],v232 offset:13312
TR a[168:169],v233 offset:1024
MM v[88:91],a[196:199],v[188:191],v[88:91]
EX v77,v77
MM v[88:91],a[192:195],v[172:175],v[88:91]
TR a[170:171],v233 offset:5120
CV v74,v76,v77
MM v[96:99],a[224:227],v[160:163],v[128:131]
EX v78,v78
MM v[96:99],a[228:231],v[176:179],v[96:99]
EX v79,v79
MM v[96:99],a[232:235],v[192:195],v[96:99]
TR a[172:173],v233 offset:9216
CV v75,v78,v79
MM v[96:99],a[236:239],v[208:211],v[96:99]
TR a[174:175],v233 offset:13312
TR a[176:177],v232 offset:1536
MM v[100:103],a[252:255],v[208:211],v[128:131]
EX v80,v80
MM v[100:103],a[248:251],v[192:195],v[100:103]
EX v81,v81
MM v[100:103],a[244:247],v[176:179],v[100:103]
TR a[178:179],v232 offset:5632
CV v80,v80,v81
MM v[100:103],a[240:243],v[160:163],v[100:103]
EX v82,v82
MM v[108:111],a[240:243],v[164:167],v[132:135]
TR a[180:181],v232 offset:9728
TR a[182:183],v232 offset:13824
MM v[108:111],a[244:247],v[180:183],v[108:111]
EX v83,v83
MM v[108:111],a[248:251],v[196:199],v[108:111]
TR a[184:185],v233 offset:1536
CV v81,v82,v83
MM v[108:111],a[252:255],v[212:215],v[108:111]
)ASM"
R"ASM(
EX v84,v84
MM v[104:107],a[236:239],v[212:215],v[132:135]
EX v85,v85
MM v[104:107],a[232:235],v[196:199],v[104:107]
TR a[186:187],v233 offset:5632
CV v82,v84,v85
MM v[104:107],a[228:231],v[180:183],v[104:107]
EX v86,v86
MM v[104:107],a[224:227],v[164:167],v[104:107]
TR a[188:189],v233 offset:9728
TR a[190:191],v233 offset:13824
MM v[112:115],a[224:227],v[168:171],v[136:139]
EX v87,v87
MM v[112:115],a[228:231],v[184:187],v[112:115]
CV v83,v86,v87
MM v[112:115],a[232:235],v[200:203],v[112:115]
EX v88,v88
MM v[112:115],a[236:239],v[216:219],v[112:115]
EX v89,v89
MM v[116:119],a[252:255],v[216:219],v[136:139]
EX v90,v90
MM v[116:119],a[248:251],v[200:203],v[116:119]
EX v91,v91
MM v[116:119],a[244:247],v[184:187],v[116:119]
EX v92,v92
MM v[116:119],a[240:243],v[168:171],v[116:119]
EX v93,v93
MM v[124:127],a[240:243],v[172:175],v[140:143]
EX v94,v94
MM v[124:127],a[244:247],v[188:191],v[124:127]
EX v95,v95
MM v[124:127],a[248:251],v[204:207],v[124:127]
EX v96,v96
MM v[124:127],a[252:255],v[220:223],v[124:127]
EX v97,v97
MM v[120:123],a[236:239],v[220:223],v[140:143]
EX v98,v98
MM v[120:123],a[232:235],v[204:207],v[120:123]
EX v99,v99
MM v[120:123],a[228:231],v[188:191],v[120:123]
EX v100,v100
MM v[120:123],a[224:227],v[172:175],v[120:123]
s_waitcnt vmcnt(14) lgkmcnt(0)
s_barrier
MM a[0:3],a[128:131],v[0:3],a[0:3]
s_waitcnt vmcnt(0)
v_cmp_ne_u32 s[76:77], s69, v49
CV v88,v88,v89
MM a[0:3],a[132:135],v[4:7],a[0:3]
s_add_u32 m0, s65, 0
CV v89,v90,v91
CV v90,v92,v93
MM a[8:11],a[132:135],v[12:15],a[8:11]
s_cmp_lg_u64 s[76:77], 0
s_cbranch_scc1 LRIX_%=
LRIXb_%=:
v_readlane_b32 s52, v48, 0
MM a[8:11],a[128:131],v[8:11],a[8:11]
v_readlane_b32 s53, v48, 1
v_readlane_b32 s54, v48, 2
MM a[16:19],a[128:131],v[16:19],a[16:19]
v_readlane_b32 s55, v48, 3
v_readlane_b32 s56, v48, 4
MM a[16:19],a[132:135],v[20:23],a[16:19]
v_readlane_b32 s57, v48, 5
v_readlane_b32 s58, v48, 6
MM a[24:27],a[132:135],v[28:31],a[24:27]
RD a[192:195],v224 offset:32768
v_readlane_b32 s59, v48, 7
MM a[24:27],a[128:131],v[24:27],a[24:27]
s_lshl_b32 s90, %[swave], 1
RD a[196:199],v225 offset:32768
CV v91,v94,v95
MM a[28:31],a[136:139],v[24:27],a[28:31]
s_add_u32 s91, s90, 1
v_readlane_b32 s66, v48, s90
v_readlane_b32 s67, v48, s91
MM a[28:31],a[140:143],v[28:31],a[28:31]
s_mul_i32 s91, %[swave], 640
buffer_load_dwordx4 v228, s[8:11], s64 offen offset:0 lds
MM a[20:23],a[140:143],v[20:23],a[20:23]
s_add_u32 s91, s91, %[slds]
RD a[200:203],v226 offset:32768
CV v68,v96,v97
MM a[20:23],a[136:139],v[16:19],a[20:23]
s_bfe_u32 s90, s48, 131077
CV v69,v98,v99
MM a[12:15],a[136:139],v[8:11],a[12:15]
s_mul_i32 s90, s90, 320
buffer_load_dwordx4 v229, s[8:11], s64 offen offset:1024 lds
MM a[12:15],a[140:143],v[12:15],a[12:15]
s_add_u32 s90, s90, 131072
EX v101,v101
MM a[4:7],a[140:143],v[4:7],a[4:7]
s_mul_i32 s66, s66, 1280
RD a[204:207],v227 offset:32768
CV v70,v100,v101
MM a[4:7],a[136:139],v[0:3],a[4:7]
s_add_u32 s66, s66, s90
s_add_u32 m0, s91, 151552
s_mov_b64 exec, 0xfffff
buffer_load_dwordx4 v248, s[20:23], s66 offen offset:0 sc1 lds
s_mov_b64 exec, -1
MM a[32:35],a[144:147],v[0:3],a[32:35]
s_mul_i32 s67, s67, 1280
EX v102,v102
MM a[32:35],a[148:151],v[4:7],a[32:35]
s_add_u32 s67, s67, s90
s_add_u32 m0, s91, 151872
s_mov_b64 exec, 0xfffff
buffer_load_dwordx4 v248, s[20:23], s67 offen offset:0 sc1 lds
s_mov_b64 exec, -1
MM a[40:43],a[148:151],v[12:15],a[40:43]
s_add_u32 m0, s65, 0
EX v103,v103
MM a[40:43],a[144:147],v[8:11],a[40:43]
RD a[208:211],v224 offset:36864
CV v71,v102,v103
MM a[48:51],a[144:147],v[16:19],a[48:51]
EX v104,v104
MM a[48:51],a[148:151],v[20:23],a[48:51]
EX v105,v105
MM a[56:59],a[148:151],v[28:31],a[56:59]
buffer_load_dwordx4 v230, s[8:11], s64 offen offset:2048 lds
MM a[56:59],a[144:147],v[24:27],a[56:59]
EX v106,v106
MM a[60:63],a[152:155],v[24:27],a[60:63]
RD a[212:215],v225 offset:36864
CV v76,v104,v105
MM a[60:63],a[156:159],v[28:31],a[60:63]
EX v107,v107
MM a[52:55],a[156:159],v[20:23],a[52:55]
RD a[216:219],v226 offset:36864
CV v77,v106,v107
MM a[52:55],a[152:155],v[16:19],a[52:55]
EX v108,v108
MM a[44:47],a[152:155],v[8:11],a[44:47]
EX v109,v109
MM a[44:47],a[156:159],v[12:15],a[44:47]
buffer_load_dwordx4 v231, s[8:11], s64 offen offset:3072 lds
MM a[36:39],a[156:159],v[4:7],a[36:39]
s_add_u32 m0, s65, 65536
RD a[220:223],v227 offset:36864
CV v78,v108,v109
MM a[36:39],a[152:155],v[0:3],a[36:39]
EX v110,v110
MM a[64:67],a[160:163],v[0:3],a[64:67]
EX v111,v111
MM a[64:67],a[164:167],v[4:7],a[64:67]
RD a[224:227],v224 offset:40960
CV v79,v110,v111
MM a[72:75],a[164:167],v[12:15],a[72:75]
EX v112,v112
MM a[72:75],a[160:163],v[8:11],a[72:75]
buffer_load_dwordx4 v235, s[12:15], s81 offen offset:0 lds
MM a[80:83],a[160:163],v[16:19],a[80:83]
EX v113,v113
MM a[80:83],a[164:167],v[20:23],a[80:83]
RD a[228:231],v225 offset:40960
CV v84,v112,v113
MM a[88:91],a[164:167],v[28:31],a[88:91]
EX v114,v114
MM a[88:91],a[160:163],v[24:27],a[88:91]
EX v115,v115
MM a[92:95],a[168:171],v[24:27],a[92:95]
RD a[232:235],v226 offset:40960
CV v85,v114,v115
MM a[92:95],a[172:175],v[28:31],a[92:95]
EX v116,v116
MM a[84:87],a[172:175],v[20:23],a[84:87]
buffer_load_dwordx4 v235, s[12:15], s64 offen offset:1024 lds
MM a[84:87],a[168:171],v[16:19],a[84:87]
EX v117,v117
MM a[76:79],a[168:171],v[8:11],a[76:79]
RD a[236:239],v227 offset:40960
CV v86,v116,v117
MM a[76:79],a[172:175],v[12:15],a[76:79]
EX v118,v118
MM a[68:71],a[172:175],v[4:7],a[68:71]
EX v119,v119
MM a[68:71],a[168:171],v[0:3],a[68:71]
RD a[240:243],v224 offset:45056
CV v87,v118,v119
MM a[96:99],a[176:179],v[0:3],a[96:99]
EX v120,v120
MM a[96:99],a[180:183],v[4:7],a[96:99]
buffer_load_dwordx4 v235, s[12:15], s81 offen offset:2048 lds
MM a[104:107],a[180:183],v[12:15],a[104:107]
EX v121,v121
MM a[104:107],a[176:179],v[8:11],a[104:107]
RD a[244:247],v225 offset:45056
CV v92,v120,v121
MM a[112:115],a[176:179],v[16:19],a[112:115]
EX v122,v122
MM a[112:115],a[180:183],v[20:23],a[112:115]
RD a[248:251],v226 offset:45056
RD a[252:255],v227 offset:45056
MM a[120:123],a[180:183],v[28:31],a[120:123]
EX v123,v123
MM a[120:123],a[176:179],v[24:27],a[120:123]
EX v124,v124
MM a[124:127],a[184:187],v[24:27],a[124:127]
buffer_load_dwordx4 v235, s[12:15], s64 offen offset:3072 lds
MM a[124:127],a[188:191],v[28:31],a[124:127]
EX v125,v125
MM a[116:119],a[188:191],v[20:23],a[116:119]
EX v126,v126
MM a[116:119],a[184:187],v[16:19],a[116:119]
EX v127,v127
MM a[108:111],a[184:187],v[8:11],a[108:111]
CV v93,v122,v123
CV v94,v124,v125
MM a[108:111],a[188:191],v[12:15],a[108:111]
CV v95,v126,v127
LPG9_%=:
s_cbranch_execz LTR0_%=
MM a[100:103],a[188:191],v[4:7],a[100:103]
MM a[100:103],a[184:187],v[0:3],a[100:103]
v_smfmac_f32_16x16x64_bf16 v[236:239], v[144:147], v[64:71], v234
v_smfmac_f32_16x16x64_bf16 v[236:239], v[148:151], v[72:79], v234
v_smfmac_f32_16x16x64_bf16 v[236:239], v[152:155], v[80:87], v234
v_smfmac_f32_16x16x64_bf16 v[236:239], v[156:159], v[88:95], v234
s_waitcnt lgkmcnt(0)
MM v[0:3],a[192:195],v[160:163],v[128:131]
s_add_u32 s64, s64, 16384
TR a[128:129],v232 offset:16384
s_add_u32 s81, s64, 896
MM v[0:3],a[196:199],v[176:179],v[0:3]
TR a[130:131],v232 offset:20480
TR a[132:133],v232 offset:24576
MM v[0:3],a[200:203],v[192:195],v[0:3]
TR a[134:135],v232 offset:28672
TR a[136:137],v233 offset:16384
MM v[0:3],a[204:207],v[208:211],v[0:3]
TR a[138:139],v233 offset:20480
v_add_f32 v247, v236, v237
MM v[4:7],a[220:223],v[208:211],v[128:131]
TR a[140:141],v233 offset:24576
v_cmp_nge_f32 vcc, s71, v247
MM v[4:7],a[216:219],v[192:195],v[4:7]
s_cbranch_vccnz LSITE13_%=
LBACK13_%=:
TR a[142:143],v233 offset:28672
TR a[144:145],v232 offset:16896
MM v[4:7],a[212:215],v[176:179],v[4:7]
v_lshrrev_b32 v127, 8, v248
v_lshlrev_b32 v124, 9, v127
MM v[4:7],a[208:211],v[160:163],v[4:7]
v_and_b32 v126, 192, v248
v_add_u32 v124, v124, v126
MM v[12:15],a[208:211],v[164:167],v[132:135]
v_bfe_u32 v126, v248, 4, 2
v_lshl_add_u32 v124, v126, 3, v124
MM v[12:15],a[212:215],v[180:183],v[12:15]
s_lshr_b32 s76, %[swave], 1
EX v0,v0
MM v[12:15],a[216:219],v[196:199],v[12:15]
s_mul_i32 s76, s76, 10240
EX v1,v1
MM v[12:15],a[220:223],v[212:215],v[12:15]
s_and_b32 s77, %[swave], 1
TR a[146:147],v232 offset:20992
CV v0,v0,v1
MM v[8:11],a[204:207],v[212:215],v[132:135]
s_lshl_b32 s77, s77, 5
EX v2,v2
MM v[8:11],a[200:203],v[196:199],v[8:11]
s_add_u32 s76, s76, s77
EX v3,v3
MM v[8:11],a[196:199],v[180:183],v[8:11]
s_add_u32 s76, s76, %[slds]
TR a[148:149],v232 offset:25088
CV v1,v2,v3
MM v[8:11],a[192:195],v[164:167],v[8:11]
s_add_u32 s76, s76, 131072
v_add_u32 v124, s76, v124
v_bfe_u32 v126, v248, 4, 3
MM v[16:19],a[192:195],v[168:171],v[136:139]
v_mul_u32_u24 v125, 320, v126
v_lshl_add_u32 v125, v127, 4, v125
MM v[16:19],a[196:199],v[184:187],v[16:19]
s_add_u32 s76, %[slds], 151552
v_add_u32 v125, s76, v125
v_lshl_add_u32 v126, v127, 3, v126
MM v[16:19],a[200:203],v[200:203],v[16:19]
TR a[150:151],v232 offset:29184
v_lshlrev_b32 v126, 4, v126
MM v[16:19],a[204:207],v[216:219],v[16:19]
s_lshr_b32 s76, s48, 7
EX v4,v4
MM v[20:23],a[220:223],v[216:219],v[136:139]
s_lshl_b32 s76, s76, 5
EX v5,v5
MM v[20:23],a[216:219],v[200:203],v[20:23]
s_and_b32 s77, s48, 16
EX v6,v6
MM v[20:23],a[212:215],v[184:187],v[20:23]
s_add_u32 s76, s76, s77
EX v7,v7
MM v[20:23],a[208:211],v[168:171],v[20:23]
s_bfe_u32 s77, s48, 131077
TR a[152:153],v233 offset:16896
CV v2,v4,v5
MM v[28:31],a[208:211],v[172:175],v[140:143]
s_lshl_b32 s77, s77, 2
EX v8,v8
MM v[28:31],a[212:215],v[188:191],v[28:31]
s_add_u32 s76, s76, s77
TR a[154:155],v233 offset:20992
CV v3,v6,v7
MM v[28:31],a[216:219],v[204:207],v[28:31]
s_add_u32 s76, s76, %[swave]
EX v9,v9
MM v[28:31],a[220:223],v[220:223],v[28:31]
s_lshl_b32 s76, s76, 9
TR a[156:157],v233 offset:25088
TR a[158:159],v233 offset:29184
MM v[24:27],a[204:207],v[220:223],v[140:143]
s_add_u32 s76, s76, 22183936
EX v10,v10
MM v[24:27],a[200:203],v[204:207],v[24:27]
TR a[160:161],v232 offset:17408
CV v8,v8,v9
MM v[24:27],a[196:199],v[188:191],v[24:27]
EX v11,v11
MM v[24:27],a[192:195],v[172:175],v[24:27]
EX v12,v12
MM v[32:35],a[224:227],v[160:163],v[128:131]
TR a[162:163],v232 offset:21504
CV v9,v10,v11
MM v[32:35],a[228:231],v[176:179],v[32:35]
EX v13,v13
MM v[32:35],a[232:235],v[192:195],v[32:35]
TR a[164:165],v232 offset:25600
CV v10,v12,v13
MM v[32:35],a[236:239],v[208:211],v[32:35]
EX v14,v14
MM v[36:39],a[252:255],v[208:211],v[128:131]
TR a[166:167],v232 offset:29696
TR a[168:169],v233 offset:17408
MM v[36:39],a[248:251],v[192:195],v[36:39]
EX v15,v15
MM v[36:39],a[244:247],v[176:179],v[36:39]
TR a[170:171],v233 offset:21504
CV v11,v14,v15
MM v[36:39],a[240:243],v[160:163],v[36:39]
EX v16,v16
MM v[44:47],a[240:243],v[164:167],v[132:135]
TR a[172:173],v233 offset:25600
TR a[174:175],v233 offset:29696
MM v[44:47],a[244:247],v[180:183],v[44:47]
EX v17,v17
MM v[44:47],a[248:251],v[196:199],v[44:47]
EX v18,v18
MM v[44:47],a[252:255],v[212:215],v[44:47]
TR a[176:177],v232 offset:17920
CV v16,v16,v17
MM v[40:43],a[236:239],v[212:215],v[132:135]
EX v19,v19
MM v[40:43],a[232:235],v[196:199],v[40:43]
TR a[178:179],v232 offset:22016
CV v17,v18,v19
MM v[40:43],a[228:231],v[180:183],v[40:43]
EX v20,v20
MM v[40:43],a[224:227],v[164:167],v[40:43]
TR a[180:181],v232 offset:26112
TR a[182:183],v232 offset:30208
MM v[48:51],a[224:227],v[168:171],v[136:139]
EX v21,v21
MM v[48:51],a[228:231],v[184:187],v[48:51]
TR a[184:185],v233 offset:17920
CV v18,v20,v21
MM v[48:51],a[232:235],v[200:203],v[48:51]
EX v22,v22
MM v[48:51],a[236:239],v[216:219],v[48:51]
EX v23,v23
MM v[52:55],a[252:255],v[216:219],v[136:139]
TR a[186:187],v233 offset:22016
CV v19,v22,v23
MM v[52:55],a[248:251],v[200:203],v[52:55]
EX v24,v24
MM v[52:55],a[244:247],v[184:187],v[52:55]
TR a[188:189],v233 offset:26112
TR a[190:191],v233 offset:30208
MM v[52:55],a[240:243],v[168:171],v[52:55]
EX v25,v25
MM v[60:63],a[240:243],v[172:175],v[140:143]
EX v26,v26
MM v[60:63],a[244:247],v[188:191],v[60:63]
EX v27,v27
MM v[60:63],a[248:251],v[204:207],v[60:63]
EX v28,v28
MM v[60:63],a[252:255],v[220:223],v[60:63]
EX v29,v29
MM v[56:59],a[236:239],v[220:223],v[140:143]
)ASM"
R"ASM(
EX v30,v30
MM v[56:59],a[232:235],v[204:207],v[56:59]
EX v31,v31
MM v[56:59],a[228:231],v[188:191],v[56:59]
EX v32,v32
MM v[56:59],a[224:227],v[172:175],v[56:59]
s_waitcnt vmcnt(6) lgkmcnt(0)
s_barrier
MM a[0:3],a[128:131],v[64:67],a[0:3]
s_add_u32 m0, s65, 16384
CV v24,v24,v25
CV v25,v26,v27
MM a[0:3],a[132:135],v[68:71],a[0:3]
RD a[192:195],v224 offset:49152
CV v26,v28,v29
MM a[8:11],a[132:135],v[76:79],a[8:11]
RD a[196:199],v225 offset:49152
CV v27,v30,v31
MM a[8:11],a[128:131],v[72:75],a[8:11]
buffer_load_dwordx4 v228, s[8:11], s64 offen offset:0 lds
MM a[16:19],a[128:131],v[80:83],a[16:19]
RD a[200:203],v226 offset:49152
RD a[204:207],v227 offset:49152
MM a[16:19],a[132:135],v[84:87],a[16:19]
EX v33,v33
MM a[24:27],a[132:135],v[92:95],a[24:27]
RD a[208:211],v224 offset:53248
CV v4,v32,v33
MM a[24:27],a[128:131],v[88:91],a[24:27]
EX v34,v34
MM a[28:31],a[136:139],v[88:91],a[28:31]
buffer_load_dwordx4 v229, s[8:11], s64 offen offset:1024 lds
MM a[28:31],a[140:143],v[92:95],a[28:31]
EX v35,v35
MM a[20:23],a[140:143],v[84:87],a[20:23]
RD a[212:215],v225 offset:53248
CV v5,v34,v35
MM a[20:23],a[136:139],v[80:83],a[20:23]
EX v36,v36
MM a[12:15],a[136:139],v[72:75],a[12:15]
TR v[96:97],v124 offset:0
TR v[98:99],v124 offset:256
MM a[12:15],a[140:143],v[76:79],a[12:15]
RD v[108:111],v125 offset:0
TR v[100:101],v124 offset:2048
MM a[4:7],a[140:143],v[68:71],a[4:7]
TR v[102:103],v124 offset:2304
RD v[112:115],v125 offset:64
MM a[4:7],a[136:139],v[64:67],a[4:7]
TR v[104:105],v124 offset:4096
TR v[106:107],v124 offset:4352
MM a[32:35],a[144:147],v[64:67],a[32:35]
RD v[116:119],v125 offset:128
RD a[216:219],v226 offset:53248
MM a[32:35],a[148:151],v[68:71],a[32:35]
EX v37,v37
MM a[40:43],a[148:151],v[76:79],a[40:43]
EX v38,v38
MM a[40:43],a[144:147],v[72:75],a[40:43]
EX v39,v39
MM a[48:51],a[144:147],v[80:83],a[48:51]
CV v6,v36,v37
CV v7,v38,v39
MM a[48:51],a[148:151],v[84:87],a[48:51]
EX v40,v40
MM a[56:59],a[148:151],v[92:95],a[56:59]
EX v41,v41
MM a[56:59],a[144:147],v[88:91],a[56:59]
buffer_load_dwordx4 v230, s[8:11], s64 offen offset:2048 lds
MM a[60:63],a[152:155],v[88:91],a[60:63]
s_waitcnt lgkmcnt(1)
EX v42,v42
MM a[60:63],a[156:159],v[92:95],a[60:63]
EX v43,v43
MM v[120:123],v[96:99],v[108:111],0
MM v[120:123],v[100:103],v[112:115],v[120:123]
MM v[120:123],v[104:107],v[116:119],v[120:123]
MM a[52:55],a[156:159],v[84:87],a[52:55]
TR v[96:97],v124 offset:6144
TR v[98:99],v124 offset:6400
MM a[52:55],a[152:155],v[80:83],a[52:55]
RD v[108:111],v125 offset:192
TR v[100:101],v124 offset:8192
MM a[44:47],a[152:155],v[72:75],a[44:47]
TR v[102:103],v124 offset:8448
RD v[112:115],v125 offset:256
MM a[44:47],a[156:159],v[76:79],a[44:47]
EX v44,v44
MM a[36:39],a[156:159],v[68:71],a[36:39]
EX v45,v45
MM a[36:39],a[152:155],v[64:67],a[36:39]
CV v12,v40,v41
CV v13,v42,v43
MM a[64:67],a[160:163],v[64:67],a[64:67]
EX v46,v46
MM a[64:67],a[164:167],v[68:71],a[64:67]
EX v47,v47
MM a[72:75],a[164:167],v[76:79],a[72:75]
buffer_load_dwordx4 v231, s[8:11], s64 offen offset:3072 lds
MM a[72:75],a[160:163],v[72:75],a[72:75]
s_add_u32 m0, s65, 81920
RD a[220:223],v227 offset:53248
CV v14,v44,v45
MM a[80:83],a[160:163],v[80:83],a[80:83]
s_waitcnt lgkmcnt(1)
EX v48,v48
MM a[80:83],a[164:167],v[84:87],a[80:83]
EX v49,v49
MM v[120:123],v[96:99],v[108:111],v[120:123]
MM v[120:123],v[100:103],v[112:115],v[120:123]
MM a[88:91],a[164:167],v[92:95],a[88:91]
RD a[224:227],v224 offset:57344
CV v15,v46,v47
MM a[88:91],a[160:163],v[88:91],a[88:91]
EX v50,v50
MM a[92:95],a[168:171],v[88:91],a[92:95]
EX v51,v51
MM a[92:95],a[172:175],v[92:95],a[92:95]
buffer_load_dwordx4 v235, s[12:15], s81 offen offset:0 lds
MM a[84:87],a[172:175],v[84:87],a[84:87]
RD a[228:231],v225 offset:57344
CV v20,v48,v49
MM a[84:87],a[168:171],v[80:83],a[84:87]
EX v52,v52
MM a[76:79],a[168:171],v[72:75],a[76:79]
s_mov_b32 exec_lo, 0x00ff00ff
s_mov_b32 exec_hi, 0x00ff00ff
buffer_store_dwordx4 v[120:123], v126, s[20:23], s76 offen offset:0 sc1
s_mov_b64 exec, -1
s_nop 0
MM a[76:79],a[172:175],v[76:79],a[76:79]
s_lshl_b32 s76, s50, 2
EX v53,v53
MM a[68:71],a[172:175],v[68:71],a[68:71]
s_add_u32 s76, s76, s84
RD a[232:235],v226 offset:57344
CV v21,v50,v51
MM a[68:71],a[168:171],v[64:67],a[68:71]
s_lshl_b32 s76, s76, 3
EX v54,v54
MM a[96:99],a[176:179],v[64:67],a[96:99]
s_lshr_b32 s77, s49, 2
EX v55,v55
MM a[96:99],a[180:183],v[68:71],a[96:99]
s_add_u32 s76, s76, s77
RD a[236:239],v227 offset:57344
CV v22,v52,v53
MM a[104:107],a[180:183],v[76:79],a[104:107]
s_mul_i32 s95, s76, 82944
EX v56,v56
MM a[104:107],a[176:179],v[72:75],a[104:107]
s_mul_i32 s76, %[swave], 20480
buffer_load_dwordx4 v235, s[12:15], s64 offen offset:1024 lds
MM a[112:115],a[176:179],v[80:83],a[112:115]
s_add_u32 s95, s95, s76
EX v57,v57
MM a[112:115],a[180:183],v[84:87],a[112:115]
s_add_u32 s95, s95, 22708224
v_mov_b32 v240, -1
v_mov_b32 v241, -1
MM a[120:123],a[180:183],v[92:95],a[120:123]
v_mov_b32 v242, -1
v_mov_b32 v243, -1
MM a[120:123],a[176:179],v[88:91],a[120:123]
EX v58,v58
MM a[124:127],a[184:187],v[88:91],a[124:127]
RD a[240:243],v224 offset:61440
CV v23,v54,v55
MM a[124:127],a[188:191],v[92:95],a[124:127]
EX v59,v59
MM a[116:119],a[188:191],v[84:87],a[116:119]
EX v60,v60
MM a[116:119],a[184:187],v[80:83],a[116:119]
RD a[244:247],v225 offset:61440
CV v28,v56,v57
MM a[108:111],a[184:187],v[72:75],a[108:111]
EX v61,v61
MM a[108:111],a[188:191],v[76:79],a[108:111]
buffer_load_dwordx4 v235, s[12:15], s81 offen offset:2048 lds
MM a[100:103],a[188:191],v[68:71],a[100:103]
EX v62,v62
MM a[100:103],a[184:187],v[64:67],a[100:103]
RD a[248:251],v226 offset:61440
CV v29,v58,v59
v_smfmac_f32_16x16x64_bf16 v[236:239], v[144:147], v[0:7], v234
RD a[252:255],v227 offset:61440
EX v63,v63
v_smfmac_f32_16x16x64_bf16 v[236:239], v[148:151], v[8:15], v234
CV v30,v60,v61
v_smfmac_f32_16x16x64_bf16 v[236:239], v[152:155], v[16:23], v234
CV v31,v62,v63
s_nop 1
v_smfmac_f32_16x16x64_bf16 v[236:239], v[156:159], v[24:31], v234
s_waitcnt lgkmcnt(0)
buffer_load_dwordx4 v235, s[12:15], s64 offen offset:3072 lds
MM v[64:67],a[192:195],v[160:163],v[128:131]
s_add_u32 s64, s64, 16384
TR a[128:129],v232 offset:32768
s_add_u32 s81, s64, 896
MM v[64:67],a[196:199],v[176:179],v[64:67]
TR a[130:131],v232 offset:36864
TR a[132:133],v232 offset:40960
MM v[64:67],a[200:203],v[192:195],v[64:67]
TR a[134:135],v232 offset:45056
TR a[136:137],v233 offset:32768
MM v[64:67],a[204:207],v[208:211],v[64:67]
TR a[138:139],v233 offset:36864
v_add_f32 v246, v236, v237
MM v[68:71],a[220:223],v[208:211],v[128:131]
TR a[140:141],v233 offset:40960
v_cmp_nge_f32 vcc, s71, v246
MM v[68:71],a[216:219],v[192:195],v[68:71]
s_cbranch_vccnz LSITE14_%=
LBACK14_%=:
TR a[142:143],v233 offset:45056
TR a[144:145],v232 offset:33280
MM v[68:71],a[212:215],v[176:179],v[68:71]
s_lshr_b32 s77, %[sb], 3
TR a[146:147],v232 offset:37376
TR a[148:149],v232 offset:41472
MM v[68:71],a[208:211],v[160:163],v[68:71]
s_lshl_b32 s77, s77, 2
TR a[150:151],v232 offset:45568
TR a[152:153],v233 offset:33280
MM v[76:79],a[208:211],v[164:167],v[132:135]
s_add_u32 s77, s77, %[swave]
EX v64,v64
MM v[76:79],a[212:215],v[180:183],v[76:79]
s_lshl_b32 s77, s77, 9
EX v65,v65
MM v[76:79],a[216:219],v[196:199],v[76:79]
s_add_u32 s77, s77, 0
v_lshrrev_b32 v253, 1, v248
v_add_u32 v253, s77, v253
MM v[76:79],a[220:223],v[212:215],v[76:79]
s_mov_b32 s77, LKEND_ka-LPD0_%=-16
v_min_u32 v253, s77, v253
CV v64,v64,v65
MM v[72:75],a[204:207],v[212:215],v[132:135]
s_getpc_b64 s[98:99]
LPD0_%=:
global_load_dword v253, v253, s[98:99]
MM v[72:75],a[200:203],v[196:199],v[72:75]
EX v66,v66
MM v[72:75],a[196:199],v[180:183],v[72:75]
EX v67,v67
MM v[72:75],a[192:195],v[164:167],v[72:75]
TR a[154:155],v233 offset:37376
CV v65,v66,v67
MM v[80:83],a[192:195],v[168:171],v[136:139]
EX v68,v68
MM v[80:83],a[196:199],v[184:187],v[80:83]
EX v69,v69
MM v[80:83],a[200:203],v[200:203],v[80:83]
TR a[156:157],v233 offset:41472
CV v66,v68,v69
MM v[80:83],a[204:207],v[216:219],v[80:83]
EX v70,v70
MM v[84:87],a[220:223],v[216:219],v[136:139]
EX v71,v71
MM v[84:87],a[216:219],v[200:203],v[84:87]
TR a[158:159],v233 offset:45568
CV v67,v70,v71
MM v[84:87],a[212:215],v[184:187],v[84:87]
EX v72,v72
MM v[84:87],a[208:211],v[168:171],v[84:87]
EX v73,v73
MM v[92:95],a[208:211],v[172:175],v[140:143]
TR a[160:161],v232 offset:33792
CV v72,v72,v73
MM v[92:95],a[212:215],v[188:191],v[92:95]
EX v74,v74
MM v[92:95],a[216:219],v[204:207],v[92:95]
TR a[162:163],v232 offset:37888
TR a[164:165],v232 offset:41984
MM v[92:95],a[220:223],v[220:223],v[92:95]
EX v75,v75
MM v[88:91],a[204:207],v[220:223],v[140:143]
TR a[166:167],v232 offset:46080
CV v73,v74,v75
MM v[88:91],a[200:203],v[204:207],v[88:91]
EX v76,v76
MM v[88:91],a[196:199],v[188:191],v[88:91]
EX v77,v77
MM v[88:91],a[192:195],v[172:175],v[88:91]
TR a[168:169],v233 offset:33792
CV v74,v76,v77
MM v[96:99],a[224:227],v[160:163],v[128:131]
EX v78,v78
MM v[96:99],a[228:231],v[176:179],v[96:99]
TR a[170:171],v233 offset:37888
TR a[172:173],v233 offset:41984
MM v[96:99],a[232:235],v[192:195],v[96:99]
EX v79,v79
MM v[96:99],a[236:239],v[208:211],v[96:99]
TR a[174:175],v233 offset:46080
CV v75,v78,v79
MM v[100:103],a[252:255],v[208:211],v[128:131]
EX v80,v80
MM v[100:103],a[248:251],v[192:195],v[100:103]
EX v81,v81
MM v[100:103],a[244:247],v[176:179],v[100:103]
TR a[176:177],v232 offset:34304
CV v80,v80,v81
MM v[100:103],a[240:243],v[160:163],v[100:103]
EX v82,v82
MM v[108:111],a[240:243],v[164:167],v[132:135]
s_add_u32 s67, s95, 10240
buffer_store_dwordx4 v[240:243], v248, s[20:23], s67 offen offset:0 sc1
MM v[108:111],a[244:247],v[180:183],v[108:111]
TR a[178:179],v232 offset:38400
TR a[180:181],v232 offset:42496
MM v[108:111],a[248:251],v[196:199],v[108:111]
EX v83,v83
MM v[108:111],a[252:255],v[212:215],v[108:111]
TR a[182:183],v232 offset:46592
CV v81,v82,v83
MM v[104:107],a[236:239],v[212:215],v[132:135]
EX v84,v84
MM v[104:107],a[232:235],v[196:199],v[104:107]
EX v85,v85
MM v[104:107],a[228:231],v[180:183],v[104:107]
TR a[184:185],v233 offset:34304
CV v82,v84,v85
MM v[104:107],a[224:227],v[164:167],v[104:107]
EX v86,v86
MM v[112:115],a[224:227],v[168:171],v[136:139]
TR a[186:187],v233 offset:38400
TR a[188:189],v233 offset:42496
MM v[112:115],a[228:231],v[184:187],v[112:115]
EX v87,v87
MM v[112:115],a[232:235],v[200:203],v[112:115]
TR a[190:191],v233 offset:46592
CV v83,v86,v87
MM v[112:115],a[236:239],v[216:219],v[112:115]
EX v88,v88
MM v[116:119],a[252:255],v[216:219],v[136:139]
EX v89,v89
MM v[116:119],a[248:251],v[200:203],v[116:119]
EX v90,v90
MM v[116:119],a[244:247],v[184:187],v[116:119]
EX v91,v91
MM v[116:119],a[240:243],v[168:171],v[116:119]
EX v92,v92
MM v[124:127],a[240:243],v[172:175],v[140:143]
EX v93,v93
MM v[124:127],a[244:247],v[188:191],v[124:127]
EX v94,v94
MM v[124:127],a[248:251],v[204:207],v[124:127]
EX v95,v95
MM v[124:127],a[252:255],v[220:223],v[124:127]
EX v96,v96
MM v[120:123],a[236:239],v[220:223],v[140:143]
EX v97,v97
MM v[120:123],a[232:235],v[204:207],v[120:123]
EX v98,v98
MM v[120:123],a[228:231],v[188:191],v[120:123]
EX v99,v99
MM v[120:123],a[224:227],v[172:175],v[120:123]
s_waitcnt vmcnt(15) lgkmcnt(0)
s_barrier
MM a[0:3],a[128:131],v[0:3],a[0:3]
s_add_u32 m0, s65, 32768
CV v88,v88,v89
CV v89,v90,v91
MM a[0:3],a[132:135],v[4:7],a[0:3]
RD a[192:195],v224 offset:0
CV v90,v92,v93
MM a[8:11],a[132:135],v[12:15],a[8:11]
RD a[196:199],v225 offset:0
CV v91,v94,v95
MM a[8:11],a[128:131],v[8:11],a[8:11]
buffer_load_dwordx4 v228, s[8:11], s64 offen offset:0 lds
MM a[16:19],a[128:131],v[16:19],a[16:19]
RD a[200:203],v226 offset:0
CV v68,v96,v97
MM a[16:19],a[132:135],v[20:23],a[16:19]
RD a[204:207],v227 offset:0
CV v69,v98,v99
MM a[24:27],a[132:135],v[28:31],a[24:27]
buffer_load_dwordx4 v229, s[8:11], s64 offen offset:1024 lds
MM a[24:27],a[128:131],v[24:27],a[24:27]
RD a[208:211],v224 offset:4096
RD a[212:215],v225 offset:4096
MM a[28:31],a[136:139],v[24:27],a[28:31]
buffer_load_dwordx4 v230, s[8:11], s64 offen offset:2048 lds
MM a[28:31],a[140:143],v[28:31],a[28:31]
EX v100,v100
MM a[20:23],a[140:143],v[20:23],a[20:23]
EX v101,v101
)ASM"
R"ASM(
MM a[20:23],a[136:139],v[16:19],a[20:23]
RD a[216:219],v226 offset:4096
CV v70,v100,v101
MM a[12:15],a[136:139],v[8:11],a[12:15]
EX v102,v102
MM a[12:15],a[140:143],v[12:15],a[12:15]
RD a[220:223],v227 offset:4096
RD a[224:227],v224 offset:8192
MM a[4:7],a[140:143],v[4:7],a[4:7]
EX v103,v103
MM a[4:7],a[136:139],v[0:3],a[4:7]
RD a[228:231],v225 offset:8192
CV v71,v102,v103
MM a[32:35],a[144:147],v[0:3],a[32:35]
EX v104,v104
MM a[32:35],a[148:151],v[4:7],a[32:35]
buffer_load_dwordx4 v231, s[8:11], s64 offen offset:3072 lds
MM a[40:43],a[148:151],v[12:15],a[40:43]
s_add_u32 m0, s65, 98304
EX v105,v105
MM a[40:43],a[144:147],v[8:11],a[40:43]
RD a[232:235],v226 offset:8192
CV v76,v104,v105
MM a[48:51],a[144:147],v[16:19],a[48:51]
EX v106,v106
MM a[48:51],a[148:151],v[20:23],a[48:51]
EX v107,v107
LPG10_%=:
s_cbranch_execz LTR0_%=
MM a[56:59],a[148:151],v[28:31],a[56:59]
RD a[236:239],v227 offset:8192
CV v77,v106,v107
MM a[56:59],a[144:147],v[24:27],a[56:59]
EX v108,v108
MM a[60:63],a[152:155],v[24:27],a[60:63]
buffer_load_dwordx4 v235, s[12:15], s81 offen offset:0 lds
MM a[60:63],a[156:159],v[28:31],a[60:63]
EX v109,v109
MM a[52:55],a[156:159],v[20:23],a[52:55]
RD a[240:243],v224 offset:12288
CV v78,v108,v109
MM a[52:55],a[152:155],v[16:19],a[52:55]
EX v110,v110
MM a[44:47],a[152:155],v[8:11],a[44:47]
EX v111,v111
MM a[44:47],a[156:159],v[12:15],a[44:47]
RD a[244:247],v225 offset:12288
CV v79,v110,v111
MM a[36:39],a[156:159],v[4:7],a[36:39]
buffer_load_dwordx4 v235, s[12:15], s64 offen offset:1024 lds
MM a[36:39],a[152:155],v[0:3],a[36:39]
EX v112,v112
MM a[64:67],a[160:163],v[0:3],a[64:67]
EX v113,v113
MM a[64:67],a[164:167],v[4:7],a[64:67]
RD a[248:251],v226 offset:12288
CV v84,v112,v113
MM a[72:75],a[164:167],v[12:15],a[72:75]
EX v114,v114
MM a[72:75],a[160:163],v[8:11],a[72:75]
EX v115,v115
MM a[80:83],a[160:163],v[16:19],a[80:83]
RD a[252:255],v227 offset:12288
CV v85,v114,v115
MM a[80:83],a[164:167],v[20:23],a[80:83]
buffer_load_dwordx4 v235, s[12:15], s81 offen offset:2048 lds
MM a[88:91],a[164:167],v[28:31],a[88:91]
EX v116,v116
MM a[88:91],a[160:163],v[24:27],a[88:91]
EX v117,v117
MM a[92:95],a[168:171],v[24:27],a[92:95]
EX v118,v118
MM a[92:95],a[172:175],v[28:31],a[92:95]
buffer_load_dwordx4 v235, s[12:15], s64 offen offset:3072 lds
MM a[84:87],a[172:175],v[20:23],a[84:87]
EX v119,v119
MM a[84:87],a[168:171],v[16:19],a[84:87]
EX v120,v120
MM a[76:79],a[168:171],v[8:11],a[76:79]
EX v121,v121
MM a[76:79],a[172:175],v[12:15],a[76:79]
EX v122,v122
MM a[68:71],a[172:175],v[4:7],a[68:71]
EX v123,v123
MM a[68:71],a[168:171],v[0:3],a[68:71]
EX v124,v124
MM a[96:99],a[176:179],v[0:3],a[96:99]
EX v125,v125
MM a[96:99],a[180:183],v[4:7],a[96:99]
EX v126,v126
MM a[104:107],a[180:183],v[12:15],a[104:107]
EX v127,v127
MM a[104:107],a[176:179],v[8:11],a[104:107]
CV v86,v116,v117
CV v87,v118,v119
MM a[112:115],a[176:179],v[16:19],a[112:115]
CV v92,v120,v121
CV v93,v122,v123
MM a[112:115],a[180:183],v[20:23],a[112:115]
CV v94,v124,v125
CV v95,v126,v127
MM a[120:123],a[180:183],v[28:31],a[120:123]
MM a[120:123],a[176:179],v[24:27],a[120:123]
MM a[124:127],a[184:187],v[24:27],a[124:127]
MM a[124:127],a[188:191],v[28:31],a[124:127]
MM a[116:119],a[188:191],v[20:23],a[116:119]
MM a[116:119],a[184:187],v[16:19],a[116:119]
MM a[108:111],a[184:187],v[8:11],a[108:111]
MM a[108:111],a[188:191],v[12:15],a[108:111]
MM a[100:103],a[188:191],v[4:7],a[100:103]
MM a[100:103],a[184:187],v[0:3],a[100:103]
v_smfmac_f32_16x16x64_bf16 v[236:239], v[144:147], v[64:71], v234
v_smfmac_f32_16x16x64_bf16 v[236:239], v[148:151], v[72:79], v234
v_smfmac_f32_16x16x64_bf16 v[236:239], v[152:155], v[80:87], v234
v_smfmac_f32_16x16x64_bf16 v[236:239], v[156:159], v[88:95], v234
s_waitcnt lgkmcnt(0)
MM v[0:3],a[192:195],v[160:163],v[128:131]
s_add_u32 s64, s64, 16384
TR a[128:129],v232 offset:49152
s_add_u32 s81, s64, 896
MM v[0:3],a[196:199],v[176:179],v[0:3]
TR a[130:131],v232 offset:53248
TR a[132:133],v232 offset:57344
MM v[0:3],a[200:203],v[192:195],v[0:3]
TR a[134:135],v232 offset:61440
TR a[136:137],v233 offset:49152
MM v[0:3],a[204:207],v[208:211],v[0:3]
TR a[138:139],v233 offset:53248
v_add_f32 v247, v236, v237
MM v[4:7],a[220:223],v[208:211],v[128:131]
TR a[140:141],v233 offset:57344
v_cmp_nge_f32 vcc, s71, v247
MM v[4:7],a[216:219],v[192:195],v[4:7]
s_cbranch_vccnz LSITE15_%=
LBACK15_%=:
TR a[142:143],v233 offset:61440
TR a[144:145],v232 offset:49664
MM v[4:7],a[212:215],v[176:179],v[4:7]
s_add_u32 s67, s95, 11264
buffer_store_dwordx4 v[240:243], v248, s[20:23], s67 offen offset:0 sc1
MM v[4:7],a[208:211],v[160:163],v[4:7]
TR a[146:147],v232 offset:53760
TR a[148:149],v232 offset:57856
MM v[12:15],a[208:211],v[164:167],v[132:135]
EX v0,v0
MM v[12:15],a[212:215],v[180:183],v[12:15]
EX v1,v1
MM v[12:15],a[216:219],v[196:199],v[12:15]
TR a[150:151],v232 offset:61952
CV v0,v0,v1
MM v[12:15],a[220:223],v[212:215],v[12:15]
EX v2,v2
MM v[8:11],a[204:207],v[212:215],v[132:135]
EX v3,v3
MM v[8:11],a[200:203],v[196:199],v[8:11]
TR a[152:153],v233 offset:49664
CV v1,v2,v3
MM v[8:11],a[196:199],v[180:183],v[8:11]
EX v4,v4
MM v[8:11],a[192:195],v[164:167],v[8:11]
EX v5,v5
MM v[16:19],a[192:195],v[168:171],v[136:139]
TR a[154:155],v233 offset:53760
CV v2,v4,v5
MM v[16:19],a[196:199],v[184:187],v[16:19]
EX v6,v6
MM v[16:19],a[200:203],v[200:203],v[16:19]
EX v7,v7
MM v[16:19],a[204:207],v[216:219],v[16:19]
TR a[156:157],v233 offset:57856
CV v3,v6,v7
MM v[20:23],a[220:223],v[216:219],v[136:139]
EX v8,v8
MM v[20:23],a[216:219],v[200:203],v[20:23]
TR a[158:159],v233 offset:61952
TR a[160:161],v232 offset:50176
MM v[20:23],a[212:215],v[184:187],v[20:23]
EX v9,v9
MM v[20:23],a[208:211],v[168:171],v[20:23]
TR a[162:163],v232 offset:54272
CV v8,v8,v9
MM v[28:31],a[208:211],v[172:175],v[140:143]
EX v10,v10
MM v[28:31],a[212:215],v[188:191],v[28:31]
TR a[164:165],v232 offset:58368
TR a[166:167],v232 offset:62464
MM v[28:31],a[216:219],v[204:207],v[28:31]
EX v11,v11
MM v[28:31],a[220:223],v[220:223],v[28:31]
TR a[168:169],v233 offset:50176
CV v9,v10,v11
MM v[24:27],a[204:207],v[220:223],v[140:143]
EX v12,v12
MM v[24:27],a[200:203],v[204:207],v[24:27]
EX v13,v13
MM v[24:27],a[196:199],v[188:191],v[24:27]
TR a[170:171],v233 offset:54272
CV v10,v12,v13
MM v[24:27],a[192:195],v[172:175],v[24:27]
EX v14,v14
MM v[32:35],a[224:227],v[160:163],v[128:131]
TR a[172:173],v233 offset:58368
TR a[174:175],v233 offset:62464
MM v[32:35],a[228:231],v[176:179],v[32:35]
EX v15,v15
MM v[32:35],a[232:235],v[192:195],v[32:35]
TR a[176:177],v232 offset:50688
CV v11,v14,v15
MM v[32:35],a[236:239],v[208:211],v[32:35]
EX v16,v16
MM v[36:39],a[252:255],v[208:211],v[128:131]
EX v17,v17
MM v[36:39],a[248:251],v[192:195],v[36:39]
TR a[178:179],v232 offset:54784
CV v16,v16,v17
MM v[36:39],a[244:247],v[176:179],v[36:39]
EX v18,v18
MM v[36:39],a[240:243],v[160:163],v[36:39]
TR a[180:181],v232 offset:58880
TR a[182:183],v232 offset:62976
MM v[44:47],a[240:243],v[164:167],v[132:135]
s_add_u32 s67, s95, 12288
buffer_store_dwordx4 v[240:243], v248, s[20:23], s67 offen offset:0 sc1
MM v[44:47],a[244:247],v[180:183],v[44:47]
EX v19,v19
MM v[44:47],a[248:251],v[196:199],v[44:47]
TR a[184:185],v233 offset:50688
CV v17,v18,v19
MM v[44:47],a[252:255],v[212:215],v[44:47]
EX v20,v20
MM v[40:43],a[236:239],v[212:215],v[132:135]
EX v21,v21
MM v[40:43],a[232:235],v[196:199],v[40:43]
TR a[186:187],v233 offset:54784
CV v18,v20,v21
MM v[40:43],a[228:231],v[180:183],v[40:43]
EX v22,v22
MM v[40:43],a[224:227],v[164:167],v[40:43]
TR a[188:189],v233 offset:58880
TR a[190:191],v233 offset:62976
MM v[48:51],a[224:227],v[168:171],v[136:139]
EX v23,v23
MM v[48:51],a[228:231],v[184:187],v[48:51]
CV v19,v22,v23
MM v[48:51],a[232:235],v[200:203],v[48:51]
EX v24,v24
MM v[48:51],a[236:239],v[216:219],v[48:51]
EX v25,v25
MM v[52:55],a[252:255],v[216:219],v[136:139]
EX v26,v26
MM v[52:55],a[248:251],v[200:203],v[52:55]
EX v27,v27
MM v[52:55],a[244:247],v[184:187],v[52:55]
EX v28,v28
MM v[52:55],a[240:243],v[168:171],v[52:55]
EX v29,v29
MM v[60:63],a[240:243],v[172:175],v[140:143]
EX v30,v30
MM v[60:63],a[244:247],v[188:191],v[60:63]
EX v31,v31
MM v[60:63],a[248:251],v[204:207],v[60:63]
EX v32,v32
MM v[60:63],a[252:255],v[220:223],v[60:63]
EX v33,v33
MM v[56:59],a[236:239],v[220:223],v[140:143]
EX v34,v34
MM v[56:59],a[232:235],v[204:207],v[56:59]
EX v35,v35
MM v[56:59],a[228:231],v[188:191],v[56:59]
EX v36,v36
MM v[56:59],a[224:227],v[172:175],v[56:59]
s_waitcnt vmcnt(17) lgkmcnt(0)
s_barrier
MM a[0:3],a[128:131],v[64:67],a[0:3]
s_lshr_b32 s91, s48, 7
CV v24,v24,v25
CV v25,v26,v27
MM a[0:3],a[132:135],v[68:71],a[0:3]
s_lshl_b32 s91, s91, 5
RD a[192:195],v224 offset:16384
CV v26,v28,v29
MM a[8:11],a[132:135],v[76:79],a[8:11]
s_and_b32 s77, s48, 16
s_add_u32 m0, s65, 49152
CV v27,v30,v31
MM a[8:11],a[128:131],v[72:75],a[8:11]
s_add_u32 s91, s91, s77
buffer_load_dwordx4 v228, s[8:11], s64 offen offset:0 lds
MM a[16:19],a[128:131],v[80:83],a[16:19]
s_bfe_u32 s77, s48, 131077
RD a[196:199],v225 offset:16384
CV v4,v32,v33
MM a[16:19],a[132:135],v[84:87],a[16:19]
s_lshl_b32 s77, s77, 2
RD a[200:203],v226 offset:16384
CV v5,v34,v35
MM a[24:27],a[132:135],v[92:95],a[24:27]
s_add_u32 s91, s91, s77
RD a[204:207],v227 offset:16384
MM a[24:27],a[128:131],v[88:91],a[24:27]
s_add_u32 s91, s91, %[swave]
buffer_load_dwordx4 v229, s[8:11], s64 offen offset:1024 lds
MM a[28:31],a[136:139],v[88:91],a[28:31]
s_lshl_b32 s91, s91, 2
RD a[208:211],v224 offset:20480
MM a[28:31],a[140:143],v[92:95],a[28:31]
s_add_u32 s91, s91, 77824
RD a[212:215],v225 offset:20480
RD a[216:219],v226 offset:20480
MM a[20:23],a[140:143],v[84:87],a[20:23]
s_waitcnt vmcnt(17)
v_mov_b32 v250, s69
s_mov_b64 exec, 1
buffer_store_dword v250, off, s[20:23], s91 offset:0 sc1
s_mov_b64 exec, -1
MM a[20:23],a[136:139],v[80:83],a[20:23]
EX v37,v37
MM a[12:15],a[136:139],v[72:75],a[12:15]
RD a[220:223],v227 offset:20480
CV v6,v36,v37
MM a[12:15],a[140:143],v[76:79],a[12:15]
buffer_load_dwordx4 v230, s[8:11], s64 offen offset:2048 lds
MM a[4:7],a[140:143],v[68:71],a[4:7]
EX v38,v38
MM a[4:7],a[136:139],v[64:67],a[4:7]
EX v39,v39
MM a[32:35],a[144:147],v[64:67],a[32:35]
RD a[224:227],v224 offset:24576
CV v7,v38,v39
MM a[32:35],a[148:151],v[68:71],a[32:35]
EX v40,v40
MM a[40:43],a[148:151],v[76:79],a[40:43]
EX v41,v41
MM a[40:43],a[144:147],v[72:75],a[40:43]
RD a[228:231],v225 offset:24576
CV v12,v40,v41
MM a[48:51],a[144:147],v[80:83],a[48:51]
buffer_load_dwordx4 v231, s[8:11], s64 offen offset:3072 lds
MM a[48:51],a[148:151],v[84:87],a[48:51]
s_add_u32 m0, s65, 114688
EX v42,v42
MM a[56:59],a[148:151],v[92:95],a[56:59]
EX v43,v43
MM a[56:59],a[144:147],v[88:91],a[56:59]
RD a[232:235],v226 offset:24576
CV v13,v42,v43
MM a[60:63],a[152:155],v[88:91],a[60:63]
EX v44,v44
MM a[60:63],a[156:159],v[92:95],a[60:63]
buffer_load_dwordx4 v235, s[12:15], s81 offen offset:0 lds
MM a[52:55],a[156:159],v[84:87],a[52:55]
EX v45,v45
MM a[52:55],a[152:155],v[80:83],a[52:55]
RD a[236:239],v227 offset:24576
CV v14,v44,v45
MM a[44:47],a[152:155],v[72:75],a[44:47]
EX v46,v46
MM a[44:47],a[156:159],v[76:79],a[44:47]
EX v47,v47
MM a[36:39],a[156:159],v[68:71],a[36:39]
RD a[240:243],v224 offset:28672
CV v15,v46,v47
MM a[36:39],a[152:155],v[64:67],a[36:39]
EX v48,v48
MM a[64:67],a[160:163],v[64:67],a[64:67]
buffer_load_dwordx4 v235, s[12:15], s64 offen offset:1024 lds
MM a[64:67],a[164:167],v[68:71],a[64:67]
EX v49,v49
MM a[72:75],a[164:167],v[76:79],a[72:75]
RD a[244:247],v225 offset:28672
CV v20,v48,v49
MM a[72:75],a[160:163],v[72:75],a[72:75]
EX v50,v50
MM a[80:83],a[160:163],v[80:83],a[80:83]
EX v51,v51
MM a[80:83],a[164:167],v[84:87],a[80:83]
RD a[248:251],v226 offset:28672
CV v21,v50,v51
MM a[88:91],a[164:167],v[92:95],a[88:91]
EX v52,v52
MM a[88:91],a[160:163],v[88:91],a[88:91]
buffer_load_dwordx4 v235, s[12:15], s81 offen offset:2048 lds
MM a[92:95],a[168:171],v[88:91],a[92:95]
EX v53,v53
MM a[92:95],a[172:175],v[92:95],a[92:95]
RD a[252:255],v227 offset:28672
CV v22,v52,v53
MM a[84:87],a[172:175],v[84:87],a[84:87]
EX v54,v54
MM a[84:87],a[168:171],v[80:83],a[84:87]
)ASM"
R"ASM(
EX v55,v55
MM a[76:79],a[168:171],v[72:75],a[76:79]
CV v23,v54,v55
MM a[76:79],a[172:175],v[76:79],a[76:79]
EX v56,v56
MM a[68:71],a[172:175],v[68:71],a[68:71]
buffer_load_dwordx4 v235, s[12:15], s64 offen offset:3072 lds
MM a[68:71],a[168:171],v[64:67],a[68:71]
EX v57,v57
MM a[96:99],a[176:179],v[64:67],a[96:99]
EX v58,v58
MM a[96:99],a[180:183],v[68:71],a[96:99]
EX v59,v59
MM a[104:107],a[180:183],v[76:79],a[104:107]
EX v60,v60
MM a[104:107],a[176:179],v[72:75],a[104:107]
EX v61,v61
MM a[112:115],a[176:179],v[80:83],a[112:115]
EX v62,v62
MM a[112:115],a[180:183],v[84:87],a[112:115]
EX v63,v63
MM a[120:123],a[180:183],v[92:95],a[120:123]
CV v28,v56,v57
CV v29,v58,v59
MM a[120:123],a[176:179],v[88:91],a[120:123]
CV v30,v60,v61
CV v31,v62,v63
MM a[124:127],a[184:187],v[88:91],a[124:127]
MM a[124:127],a[188:191],v[92:95],a[124:127]
MM a[116:119],a[188:191],v[84:87],a[116:119]
MM a[116:119],a[184:187],v[80:83],a[116:119]
MM a[108:111],a[184:187],v[72:75],a[108:111]
MM a[108:111],a[188:191],v[76:79],a[108:111]
MM a[100:103],a[188:191],v[68:71],a[100:103]
MM a[100:103],a[184:187],v[64:67],a[100:103]
v_smfmac_f32_16x16x64_bf16 v[236:239], v[144:147], v[0:7], v234
v_smfmac_f32_16x16x64_bf16 v[236:239], v[148:151], v[8:15], v234
v_smfmac_f32_16x16x64_bf16 v[236:239], v[152:155], v[16:23], v234
v_smfmac_f32_16x16x64_bf16 v[236:239], v[156:159], v[24:31], v234
s_waitcnt lgkmcnt(0)
MM v[64:67],a[192:195],v[160:163],v[128:131]
s_add_u32 s64, s64, 16384
TR a[128:129],v232 offset:0
s_add_u32 s81, s64, 896
MM v[64:67],a[196:199],v[176:179],v[64:67]
TR a[130:131],v232 offset:4096
TR a[132:133],v232 offset:8192
MM v[64:67],a[200:203],v[192:195],v[64:67]
TR a[134:135],v232 offset:12288
TR a[136:137],v233 offset:0
MM v[64:67],a[204:207],v[208:211],v[64:67]
TR a[138:139],v233 offset:4096
v_add_f32 v246, v236, v237
MM v[68:71],a[220:223],v[208:211],v[128:131]
TR a[140:141],v233 offset:8192
v_cmp_nge_f32 vcc, s71, v246
MM v[68:71],a[216:219],v[192:195],v[68:71]
s_cbranch_vccnz LSITE16_%=
LBACK16_%=:
TR a[142:143],v233 offset:12288
TR a[144:145],v232 offset:512
MM v[68:71],a[212:215],v[176:179],v[68:71]
s_add_u32 s67, s95, 13312
buffer_store_dwordx4 v[240:243], v248, s[20:23], s67 offen offset:0 sc1
MM v[68:71],a[208:211],v[160:163],v[68:71]
TR a[146:147],v232 offset:4608
TR a[148:149],v232 offset:8704
MM v[76:79],a[208:211],v[164:167],v[132:135]
EX v64,v64
MM v[76:79],a[212:215],v[180:183],v[76:79]
EX v65,v65
MM v[76:79],a[216:219],v[196:199],v[76:79]
TR a[150:151],v232 offset:12800
CV v64,v64,v65
MM v[76:79],a[220:223],v[212:215],v[76:79]
EX v66,v66
MM v[72:75],a[204:207],v[212:215],v[132:135]
EX v67,v67
MM v[72:75],a[200:203],v[196:199],v[72:75]
TR a[152:153],v233 offset:512
CV v65,v66,v67
MM v[72:75],a[196:199],v[180:183],v[72:75]
EX v68,v68
MM v[72:75],a[192:195],v[164:167],v[72:75]
EX v69,v69
MM v[80:83],a[192:195],v[168:171],v[136:139]
TR a[154:155],v233 offset:4608
CV v66,v68,v69
MM v[80:83],a[196:199],v[184:187],v[80:83]
EX v70,v70
MM v[80:83],a[200:203],v[200:203],v[80:83]
EX v71,v71
MM v[80:83],a[204:207],v[216:219],v[80:83]
TR a[156:157],v233 offset:8704
CV v67,v70,v71
MM v[84:87],a[220:223],v[216:219],v[136:139]
EX v72,v72
MM v[84:87],a[216:219],v[200:203],v[84:87]
TR a[158:159],v233 offset:12800
TR a[160:161],v232 offset:1024
MM v[84:87],a[212:215],v[184:187],v[84:87]
EX v73,v73
MM v[84:87],a[208:211],v[168:171],v[84:87]
TR a[162:163],v232 offset:5120
CV v72,v72,v73
MM v[92:95],a[208:211],v[172:175],v[140:143]
s_and_b32 s44, s50, 7
EX v74,v74
MM v[92:95],a[212:215],v[188:191],v[92:95]
s_lshl_b32 s44, s44, 2
TR a[164:165],v232 offset:9216
TR a[166:167],v232 offset:13312
MM v[92:95],a[216:219],v[204:207],v[92:95]
s_add_u32 s44, s44, s84
EX v75,v75
MM v[92:95],a[220:223],v[220:223],v[92:95]
s_lshl_b32 s44, s44, 2
TR a[168:169],v233 offset:1024
CV v73,v74,v75
MM v[88:91],a[204:207],v[220:223],v[140:143]
s_add_u32 s44, s44, %[swave]
EX v76,v76
MM v[88:91],a[200:203],v[204:207],v[88:91]
s_mul_i32 s44, s44, 5120
EX v77,v77
MM v[88:91],a[196:199],v[188:191],v[88:91]
s_mul_i32 s45, s84, 320
TR a[170:171],v233 offset:5120
CV v74,v76,v77
MM v[88:91],a[192:195],v[172:175],v[88:91]
s_sub_u32 s44, s44, s45
EX v78,v78
MM v[96:99],a[224:227],v[160:163],v[128:131]
s_lshl_b32 s45, %[swave], 10
TR a[172:173],v233 offset:9216
TR a[174:175],v233 offset:13312
MM v[96:99],a[228:231],v[176:179],v[96:99]
s_add_u32 s45, s45, %[slds]
s_add_u32 m0, s45, 147456
s_add_u32 s44, s44, 0
buffer_load_dwordx4 v248, s[24:27], s44 offen offset:0 lds
MM v[96:99],a[232:235],v[192:195],v[96:99]
EX v79,v79
MM v[96:99],a[236:239],v[208:211],v[96:99]
TR a[176:177],v232 offset:1536
CV v75,v78,v79
MM v[100:103],a[252:255],v[208:211],v[128:131]
EX v80,v80
MM v[100:103],a[248:251],v[192:195],v[100:103]
EX v81,v81
MM v[100:103],a[244:247],v[176:179],v[100:103]
TR a[178:179],v232 offset:5632
CV v80,v80,v81
MM v[100:103],a[240:243],v[160:163],v[100:103]
EX v82,v82
MM v[108:111],a[240:243],v[164:167],v[132:135]
s_add_u32 s67, s95, 14336
buffer_store_dwordx4 v[240:243], v248, s[20:23], s67 offen offset:0 sc1
MM v[108:111],a[244:247],v[180:183],v[108:111]
EX v83,v83
MM v[108:111],a[248:251],v[196:199],v[108:111]
TR a[180:181],v232 offset:9728
CV v81,v82,v83
MM v[108:111],a[252:255],v[212:215],v[108:111]
EX v84,v84
MM v[104:107],a[236:239],v[212:215],v[132:135]
TR a[182:183],v232 offset:13824
TR a[184:185],v233 offset:1536
MM v[104:107],a[232:235],v[196:199],v[104:107]
EX v85,v85
MM v[104:107],a[228:231],v[180:183],v[104:107]
TR a[186:187],v233 offset:5632
CV v82,v84,v85
MM v[104:107],a[224:227],v[164:167],v[104:107]
EX v86,v86
MM v[112:115],a[224:227],v[168:171],v[136:139]
TR a[188:189],v233 offset:9728
TR a[190:191],v233 offset:13824
MM v[112:115],a[228:231],v[184:187],v[112:115]
EX v87,v87
MM v[112:115],a[232:235],v[200:203],v[112:115]
CV v83,v86,v87
MM v[112:115],a[236:239],v[216:219],v[112:115]
EX v88,v88
MM v[116:119],a[252:255],v[216:219],v[136:139]
EX v89,v89
MM v[116:119],a[248:251],v[200:203],v[116:119]
EX v90,v90
MM v[116:119],a[244:247],v[184:187],v[116:119]
EX v91,v91
MM v[116:119],a[240:243],v[168:171],v[116:119]
EX v92,v92
MM v[124:127],a[240:243],v[172:175],v[140:143]
EX v93,v93
MM v[124:127],a[244:247],v[188:191],v[124:127]
EX v94,v94
MM v[124:127],a[248:251],v[204:207],v[124:127]
EX v95,v95
MM v[124:127],a[252:255],v[220:223],v[124:127]
EX v96,v96
MM v[120:123],a[236:239],v[220:223],v[140:143]
EX v97,v97
MM v[120:123],a[232:235],v[204:207],v[120:123]
EX v98,v98
MM v[120:123],a[228:231],v[188:191],v[120:123]
EX v99,v99
MM v[120:123],a[224:227],v[172:175],v[120:123]
s_waitcnt vmcnt(18) lgkmcnt(0)
s_barrier
MM a[0:3],a[128:131],v[0:3],a[0:3]
s_add_u32 m0, s65, 0
CV v88,v88,v89
CV v89,v90,v91
MM a[0:3],a[132:135],v[4:7],a[0:3]
RD a[192:195],v224 offset:32768
CV v90,v92,v93
MM a[8:11],a[132:135],v[12:15],a[8:11]
RD a[196:199],v225 offset:32768
CV v91,v94,v95
MM a[8:11],a[128:131],v[8:11],a[8:11]
buffer_load_dwordx4 v228, s[8:11], s64 offen offset:0 lds
MM a[16:19],a[128:131],v[16:19],a[16:19]
RD a[200:203],v226 offset:32768
CV v68,v96,v97
MM a[16:19],a[132:135],v[20:23],a[16:19]
RD a[204:207],v227 offset:32768
CV v69,v98,v99
MM a[24:27],a[132:135],v[28:31],a[24:27]
buffer_load_dwordx4 v229, s[8:11], s64 offen offset:1024 lds
s_nop 0
LPG11_%=:
s_cbranch_execz LTR0_%=
MM a[24:27],a[128:131],v[24:27],a[24:27]
RD a[208:211],v224 offset:36864
RD a[212:215],v225 offset:36864
MM a[28:31],a[136:139],v[24:27],a[28:31]
buffer_load_dwordx4 v230, s[8:11], s64 offen offset:2048 lds
MM a[28:31],a[140:143],v[28:31],a[28:31]
EX v100,v100
MM a[20:23],a[140:143],v[20:23],a[20:23]
EX v101,v101
MM a[20:23],a[136:139],v[16:19],a[20:23]
RD a[216:219],v226 offset:36864
CV v70,v100,v101
MM a[12:15],a[136:139],v[8:11],a[12:15]
EX v102,v102
MM a[12:15],a[140:143],v[12:15],a[12:15]
RD a[220:223],v227 offset:36864
RD a[224:227],v224 offset:40960
MM a[4:7],a[140:143],v[4:7],a[4:7]
EX v103,v103
MM a[4:7],a[136:139],v[0:3],a[4:7]
RD a[228:231],v225 offset:40960
CV v71,v102,v103
MM a[32:35],a[144:147],v[0:3],a[32:35]
EX v104,v104
MM a[32:35],a[148:151],v[4:7],a[32:35]
buffer_load_dwordx4 v231, s[8:11], s64 offen offset:3072 lds
MM a[40:43],a[148:151],v[12:15],a[40:43]
s_add_u32 m0, s65, 65536
EX v105,v105
MM a[40:43],a[144:147],v[8:11],a[40:43]
RD a[232:235],v226 offset:40960
CV v76,v104,v105
MM a[48:51],a[144:147],v[16:19],a[48:51]
EX v106,v106
MM a[48:51],a[148:151],v[20:23],a[48:51]
EX v107,v107
MM a[56:59],a[148:151],v[28:31],a[56:59]
RD a[236:239],v227 offset:40960
CV v77,v106,v107
MM a[56:59],a[144:147],v[24:27],a[56:59]
EX v108,v108
MM a[60:63],a[152:155],v[24:27],a[60:63]
buffer_load_dwordx4 v235, s[12:15], s81 offen offset:0 lds
MM a[60:63],a[156:159],v[28:31],a[60:63]
EX v109,v109
MM a[52:55],a[156:159],v[20:23],a[52:55]
RD a[240:243],v224 offset:45056
CV v78,v108,v109
MM a[52:55],a[152:155],v[16:19],a[52:55]
EX v110,v110
MM a[44:47],a[152:155],v[8:11],a[44:47]
EX v111,v111
MM a[44:47],a[156:159],v[12:15],a[44:47]
RD a[244:247],v225 offset:45056
CV v79,v110,v111
MM a[36:39],a[156:159],v[4:7],a[36:39]
buffer_load_dwordx4 v235, s[12:15], s64 offen offset:1024 lds
MM a[36:39],a[152:155],v[0:3],a[36:39]
EX v112,v112
MM a[64:67],a[160:163],v[0:3],a[64:67]
EX v113,v113
MM a[64:67],a[164:167],v[4:7],a[64:67]
RD a[248:251],v226 offset:45056
CV v84,v112,v113
MM a[72:75],a[164:167],v[12:15],a[72:75]
EX v114,v114
MM a[72:75],a[160:163],v[8:11],a[72:75]
EX v115,v115
MM a[80:83],a[160:163],v[16:19],a[80:83]
RD a[252:255],v227 offset:45056
CV v85,v114,v115
MM a[80:83],a[164:167],v[20:23],a[80:83]
buffer_load_dwordx4 v235, s[12:15], s81 offen offset:2048 lds
MM a[88:91],a[164:167],v[28:31],a[88:91]
EX v116,v116
MM a[88:91],a[160:163],v[24:27],a[88:91]
EX v117,v117
MM a[92:95],a[168:171],v[24:27],a[92:95]
EX v118,v118
MM a[92:95],a[172:175],v[28:31],a[92:95]
buffer_load_dwordx4 v235, s[12:15], s64 offen offset:3072 lds
MM a[84:87],a[172:175],v[20:23],a[84:87]
EX v119,v119
MM a[84:87],a[168:171],v[16:19],a[84:87]
EX v120,v120
MM a[76:79],a[168:171],v[8:11],a[76:79]
EX v121,v121
MM a[76:79],a[172:175],v[12:15],a[76:79]
EX v122,v122
MM a[68:71],a[172:175],v[4:7],a[68:71]
EX v123,v123
MM a[68:71],a[168:171],v[0:3],a[68:71]
EX v124,v124
MM a[96:99],a[176:179],v[0:3],a[96:99]
EX v125,v125
MM a[96:99],a[180:183],v[4:7],a[96:99]
EX v126,v126
MM a[104:107],a[180:183],v[12:15],a[104:107]
EX v127,v127
MM a[104:107],a[176:179],v[8:11],a[104:107]
CV v86,v116,v117
CV v87,v118,v119
MM a[112:115],a[176:179],v[16:19],a[112:115]
CV v92,v120,v121
CV v93,v122,v123
MM a[112:115],a[180:183],v[20:23],a[112:115]
CV v94,v124,v125
CV v95,v126,v127
MM a[120:123],a[180:183],v[28:31],a[120:123]
MM a[120:123],a[176:179],v[24:27],a[120:123]
MM a[124:127],a[184:187],v[24:27],a[124:127]
MM a[124:127],a[188:191],v[28:31],a[124:127]
MM a[116:119],a[188:191],v[20:23],a[116:119]
MM a[116:119],a[184:187],v[16:19],a[116:119]
MM a[108:111],a[184:187],v[8:11],a[108:111]
MM a[108:111],a[188:191],v[12:15],a[108:111]
MM a[100:103],a[188:191],v[4:7],a[100:103]
MM a[100:103],a[184:187],v[0:3],a[100:103]
v_smfmac_f32_16x16x64_bf16 v[236:239], v[144:147], v[64:71], v234
v_smfmac_f32_16x16x64_bf16 v[236:239], v[148:151], v[72:79], v234
v_smfmac_f32_16x16x64_bf16 v[236:239], v[152:155], v[80:87], v234
v_smfmac_f32_16x16x64_bf16 v[236:239], v[156:159], v[88:95], v234
s_waitcnt lgkmcnt(0)
MM v[0:3],a[192:195],v[160:163],v[128:131]
s_add_u32 s64, s64, 16384
TR a[128:129],v232 offset:16384
s_add_u32 s81, s64, 896
MM v[0:3],a[196:199],v[176:179],v[0:3]
TR a[130:131],v232 offset:20480
TR a[132:133],v232 offset:24576
MM v[0:3],a[200:203],v[192:195],v[0:3]
TR a[134:135],v232 offset:28672
TR a[136:137],v233 offset:16384
MM v[0:3],a[204:207],v[208:211],v[0:3]
TR a[138:139],v233 offset:20480
v_add_f32 v247, v236, v237
MM v[4:7],a[220:223],v[208:211],v[128:131]
TR a[140:141],v233 offset:24576
v_cmp_nge_f32 vcc, s71, v247
MM v[4:7],a[216:219],v[192:195],v[4:7]
s_cbranch_vccnz LSITE17_%=
LBACK17_%=:
TR a[142:143],v233 offset:28672
TR a[144:145],v232 offset:16896
MM v[4:7],a[212:215],v[176:179],v[4:7]
TR a[146:147],v232 offset:20992
)ASM"
R"ASM(
TR a[148:149],v232 offset:25088
MM v[4:7],a[208:211],v[160:163],v[4:7]
TR a[150:151],v232 offset:29184
TR a[152:153],v233 offset:16896
MM v[12:15],a[208:211],v[164:167],v[132:135]
EX v0,v0
MM v[12:15],a[212:215],v[180:183],v[12:15]
EX v1,v1
MM v[12:15],a[216:219],v[196:199],v[12:15]
TR a[154:155],v233 offset:20992
CV v0,v0,v1
MM v[12:15],a[220:223],v[212:215],v[12:15]
EX v2,v2
MM v[8:11],a[204:207],v[212:215],v[132:135]
EX v3,v3
MM v[8:11],a[200:203],v[196:199],v[8:11]
TR a[156:157],v233 offset:25088
CV v1,v2,v3
MM v[8:11],a[196:199],v[180:183],v[8:11]
EX v4,v4
MM v[8:11],a[192:195],v[164:167],v[8:11]
EX v5,v5
MM v[16:19],a[192:195],v[168:171],v[136:139]
TR a[158:159],v233 offset:29184
CV v2,v4,v5
MM v[16:19],a[196:199],v[184:187],v[16:19]
EX v6,v6
MM v[16:19],a[200:203],v[200:203],v[16:19]
EX v7,v7
MM v[16:19],a[204:207],v[216:219],v[16:19]
TR a[160:161],v232 offset:17408
CV v3,v6,v7
MM v[20:23],a[220:223],v[216:219],v[136:139]
EX v8,v8
MM v[20:23],a[216:219],v[200:203],v[20:23]
EX v9,v9
MM v[20:23],a[212:215],v[184:187],v[20:23]
TR a[162:163],v232 offset:21504
CV v8,v8,v9
MM v[20:23],a[208:211],v[168:171],v[20:23]
EX v10,v10
MM v[28:31],a[208:211],v[172:175],v[140:143]
s_add_u32 m0, s45, 147456
s_add_u32 s44, s44, 1024
buffer_load_dwordx4 v248, s[24:27], s44 offen offset:0 lds
MM v[28:31],a[212:215],v[188:191],v[28:31]
TR a[164:165],v232 offset:25600
TR a[166:167],v232 offset:29696
MM v[28:31],a[216:219],v[204:207],v[28:31]
EX v11,v11
MM v[28:31],a[220:223],v[220:223],v[28:31]
TR a[168:169],v233 offset:17408
CV v9,v10,v11
MM v[24:27],a[204:207],v[220:223],v[140:143]
EX v12,v12
MM v[24:27],a[200:203],v[204:207],v[24:27]
EX v13,v13
MM v[24:27],a[196:199],v[188:191],v[24:27]
TR a[170:171],v233 offset:21504
CV v10,v12,v13
MM v[24:27],a[192:195],v[172:175],v[24:27]
EX v14,v14
MM v[32:35],a[224:227],v[160:163],v[128:131]
TR a[172:173],v233 offset:25600
TR a[174:175],v233 offset:29696
MM v[32:35],a[228:231],v[176:179],v[32:35]
EX v15,v15
MM v[32:35],a[232:235],v[192:195],v[32:35]
TR a[176:177],v232 offset:17920
CV v11,v14,v15
MM v[32:35],a[236:239],v[208:211],v[32:35]
EX v16,v16
MM v[36:39],a[252:255],v[208:211],v[128:131]
EX v17,v17
MM v[36:39],a[248:251],v[192:195],v[36:39]
TR a[178:179],v232 offset:22016
CV v16,v16,v17
MM v[36:39],a[244:247],v[176:179],v[36:39]
EX v18,v18
MM v[36:39],a[240:243],v[160:163],v[36:39]
TR a[180:181],v232 offset:26112
TR a[182:183],v232 offset:30208
MM v[44:47],a[240:243],v[164:167],v[132:135]
EX v19,v19
MM v[44:47],a[244:247],v[180:183],v[44:47]
TR a[184:185],v233 offset:17920
CV v17,v18,v19
MM v[44:47],a[248:251],v[196:199],v[44:47]
EX v20,v20
MM v[44:47],a[252:255],v[212:215],v[44:47]
EX v21,v21
MM v[40:43],a[236:239],v[212:215],v[132:135]
TR a[186:187],v233 offset:22016
CV v18,v20,v21
MM v[40:43],a[232:235],v[196:199],v[40:43]
EX v22,v22
MM v[40:43],a[228:231],v[180:183],v[40:43]
TR a[188:189],v233 offset:26112
TR a[190:191],v233 offset:30208
MM v[40:43],a[224:227],v[164:167],v[40:43]
EX v23,v23
MM v[48:51],a[224:227],v[168:171],v[136:139]
CV v19,v22,v23
MM v[48:51],a[228:231],v[184:187],v[48:51]
EX v24,v24
MM v[48:51],a[232:235],v[200:203],v[48:51]
EX v25,v25
MM v[48:51],a[236:239],v[216:219],v[48:51]
EX v26,v26
MM v[52:55],a[252:255],v[216:219],v[136:139]
EX v27,v27
MM v[52:55],a[248:251],v[200:203],v[52:55]
EX v28,v28
MM v[52:55],a[244:247],v[184:187],v[52:55]
EX v29,v29
MM v[52:55],a[240:243],v[168:171],v[52:55]
EX v30,v30
MM v[60:63],a[240:243],v[172:175],v[140:143]
EX v31,v31
MM v[60:63],a[244:247],v[188:191],v[60:63]
EX v32,v32
MM v[60:63],a[248:251],v[204:207],v[60:63]
EX v33,v33
MM v[60:63],a[252:255],v[220:223],v[60:63]
EX v34,v34
MM v[56:59],a[236:239],v[220:223],v[140:143]
EX v35,v35
MM v[56:59],a[232:235],v[204:207],v[56:59]
EX v36,v36
MM v[56:59],a[228:231],v[188:191],v[56:59]
EX v37,v37
MM v[56:59],a[224:227],v[172:175],v[56:59]
s_waitcnt vmcnt(16) lgkmcnt(0)
s_barrier
MM a[0:3],a[128:131],v[64:67],a[0:3]
s_lshl_b32 s60, s50, 2
CV v24,v24,v25
CV v25,v26,v27
MM a[0:3],a[132:135],v[68:71],a[0:3]
s_add_u32 s60, s60, %[swave]
RD a[192:195],v224 offset:49152
CV v26,v28,v29
MM a[8:11],a[132:135],v[76:79],a[8:11]
s_lshl_b32 s60, s60, 2
s_add_u32 m0, s65, 16384
CV v27,v30,v31
MM a[8:11],a[128:131],v[72:75],a[8:11]
s_bfe_u32 s62, s49, 65538
buffer_load_dwordx4 v228, s[8:11], s64 offen offset:0 lds
MM a[16:19],a[128:131],v[80:83],a[16:19]
s_add_u32 s93, s60, s62
RD a[196:199],v225 offset:49152
CV v4,v32,v33
MM a[16:19],a[132:135],v[84:87],a[16:19]
s_lshl_b32 s93, s93, 2
RD a[200:203],v226 offset:49152
CV v5,v34,v35
MM a[24:27],a[132:135],v[92:95],a[24:27]
s_lshr_b32 s61, s49, 3
RD a[204:207],v227 offset:49152
MM a[24:27],a[128:131],v[88:91],a[24:27]
s_add_u32 s93, s93, s61
buffer_load_dwordx4 v229, s[8:11], s64 offen offset:1024 lds
MM a[28:31],a[136:139],v[88:91],a[28:31]
s_mul_i32 s18, s93, 5120
RD a[208:211],v224 offset:53248
CV v6,v36,v37
MM a[28:31],a[140:143],v[92:95],a[28:31]
s_mul_i32 s60, s84, 320
RD a[212:215],v225 offset:53248
MM a[20:23],a[140:143],v[84:87],a[20:23]
s_add_u32 s18, s18, s60
buffer_load_dwordx4 v230, s[8:11], s64 offen offset:2048 lds
MM a[20:23],a[136:139],v[80:83],a[20:23]
s_and_b32 s60, s84, 1
RD a[216:219],v226 offset:53248
MM a[12:15],a[136:139],v[72:75],a[12:15]
s_xor_b32 s60, s60, 1
EX v38,v38
MM a[12:15],a[140:143],v[76:79],a[12:15]
s_lshl_b32 s60, s60, 8
RD a[220:223],v227 offset:53248
RD a[224:227],v224 offset:57344
MM a[4:7],a[140:143],v[68:71],a[4:7]
s_add_u32 s63, s18, s60
EX v39,v39
MM a[4:7],a[136:139],v[64:67],a[4:7]
s_and_b32 s60, s84, 1
RD a[228:231],v225 offset:57344
CV v7,v38,v39
MM a[32:35],a[144:147],v[64:67],a[32:35]
s_lshl_b32 s60, s60, 6
EX v40,v40
MM a[32:35],a[148:151],v[68:71],a[32:35]
s_add_u32 s18, s18, s60
buffer_load_dwordx4 v231, s[8:11], s64 offen offset:3072 lds
MM a[40:43],a[148:151],v[76:79],a[40:43]
s_lshl_b32 s60, s50, 2
EX v41,v41
MM a[40:43],a[144:147],v[72:75],a[40:43]
s_add_u32 s60, s60, s84
s_add_u32 m0, s65, 81920
CV v12,v40,v41
MM a[48:51],a[144:147],v[80:83],a[48:51]
s_mul_i32 s16, s60, 663552
EX v42,v42
MM a[48:51],a[148:151],v[84:87],a[48:51]
s_lshl_b32 s62, s62, 2
EX v43,v43
MM a[56:59],a[148:151],v[92:95],a[56:59]
s_add_u32 s62, s62, s61
RD a[232:235],v226 offset:57344
CV v13,v42,v43
MM a[56:59],a[144:147],v[88:91],a[56:59]
s_lshl_b32 s62, s62, 8
EX v44,v44
MM a[60:63],a[152:155],v[88:91],a[60:63]
s_add_u32 s16, s16, s62
buffer_load_dwordx4 v235, s[12:15], s81 offen offset:0 lds
MM a[60:63],a[156:159],v[92:95],a[60:63]
s_mul_i32 s60, %[swave], 20480
EX v45,v45
MM a[52:55],a[156:159],v[84:87],a[52:55]
s_add_u32 s16, s16, s60
RD a[236:239],v227 offset:57344
CV v14,v44,v45
MM a[52:55],a[152:155],v[80:83],a[52:55]
s_lshr_b32 s61, s49, 4
EX v46,v46
MM a[44:47],a[152:155],v[72:75],a[44:47]
s_mul_i32 s61, s61, 331776
EX v47,v47
MM a[44:47],a[156:159],v[76:79],a[44:47]
s_add_u32 s16, s16, s61
CV v15,v46,v47
MM a[36:39],a[156:159],v[68:71],a[36:39]
s_add_u32 s16, s16, 22708224
buffer_load_dwordx4 v235, s[12:15], s64 offen offset:1024 lds
MM a[36:39],a[152:155],v[64:67],a[36:39]
s_mov_b32 s17, 31
EX v48,v48
MM a[64:67],a[160:163],v[64:67],a[64:67]
s_mov_b32 s72, 0
EX v49,v49
MM a[64:67],a[164:167],v[68:71],a[64:67]
s_lshl_b32 s60, s50, 2
RD a[240:243],v224 offset:61440
CV v20,v48,v49
MM a[72:75],a[164:167],v[76:79],a[72:75]
s_add_u32 s60, s60, s84
EX v50,v50
MM a[72:75],a[160:163],v[72:75],a[72:75]
s_lshl_b32 s60, s60, 2
RD a[244:247],v225 offset:61440
MM a[80:83],a[160:163],v[80:83],a[80:83]
s_add_u32 s60, s60, %[swave]
EX v51,v51
MM a[80:83],a[164:167],v[84:87],a[80:83]
s_lshl_b32 s60, s60, 5
RD a[248:251],v226 offset:61440
CV v21,v50,v51
MM a[88:91],a[164:167],v[92:95],a[88:91]
s_and_b32 s61, s49, 28
EX v52,v52
MM a[88:91],a[160:163],v[88:91],a[88:91]
s_add_u32 s19, s60, s61
buffer_load_dwordx4 v235, s[12:15], s81 offen offset:2048 lds
MM a[92:95],a[168:171],v[88:91],a[92:95]
s_lshl_b32 s19, s19, 2
EX v53,v53
MM a[92:95],a[172:175],v[92:95],a[92:95]
s_add_u32 s19, s19, 16384
RD a[252:255],v227 offset:61440
CV v22,v52,v53
MM a[84:87],a[172:175],v[84:87],a[84:87]
s_and_b32 s68, s19, 0xffffffc0
EX v54,v54
MM a[84:87],a[168:171],v[80:83],a[84:87]
s_mov_b32 s82, 0
EX v55,v55
MM a[76:79],a[168:171],v[72:75],a[76:79]
s_lshr_b32 s61, s52, 2
CV v23,v54,v55
MM a[76:79],a[172:175],v[76:79],a[76:79]
s_sub_u32 s61, s61, s93
s_cmp_eq_u32 s61, 0
s_cselect_b32 s60, 1, 0
MM a[68:71],a[172:175],v[68:71],a[68:71]
s_or_b32 s82, s82, s60
s_cmp_eq_u32 s61, 8
s_cselect_b32 s60, 256, 0
MM a[68:71],a[168:171],v[64:67],a[68:71]
s_or_b32 s82, s82, s60
EX v56,v56
MM a[96:99],a[176:179],v[64:67],a[96:99]
s_lshr_b32 s61, s53, 2
EX v57,v57
MM a[96:99],a[180:183],v[68:71],a[96:99]
s_sub_u32 s61, s61, s93
s_cmp_eq_u32 s61, 0
s_cselect_b32 s60, 2, 0
MM a[104:107],a[180:183],v[76:79],a[104:107]
s_or_b32 s82, s82, s60
s_cmp_eq_u32 s61, 8
s_cselect_b32 s60, 512, 0
MM a[104:107],a[176:179],v[72:75],a[104:107]
s_or_b32 s82, s82, s60
EX v58,v58
MM a[112:115],a[176:179],v[80:83],a[112:115]
s_lshr_b32 s61, s54, 2
EX v59,v59
MM a[112:115],a[180:183],v[84:87],a[112:115]
s_sub_u32 s61, s61, s93
s_cmp_eq_u32 s61, 0
s_cselect_b32 s60, 4, 0
MM a[120:123],a[180:183],v[92:95],a[120:123]
s_or_b32 s82, s82, s60
s_cmp_eq_u32 s61, 8
s_cselect_b32 s60, 1024, 0
MM a[120:123],a[176:179],v[88:91],a[120:123]
s_or_b32 s82, s82, s60
EX v60,v60
MM a[124:127],a[184:187],v[88:91],a[124:127]
s_lshr_b32 s61, s55, 2
buffer_load_dwordx4 v235, s[12:15], s64 offen offset:3072 lds
MM a[124:127],a[188:191],v[92:95],a[124:127]
s_sub_u32 s61, s61, s93
s_cmp_eq_u32 s61, 0
s_cselect_b32 s60, 8, 0
MM a[116:119],a[188:191],v[84:87],a[116:119]
s_or_b32 s82, s82, s60
s_cmp_eq_u32 s61, 8
s_cselect_b32 s60, 2048, 0
MM a[116:119],a[184:187],v[80:83],a[116:119]
s_or_b32 s82, s82, s60
EX v61,v61
MM a[108:111],a[184:187],v[72:75],a[108:111]
s_lshr_b32 s61, s56, 2
EX v62,v62
MM a[108:111],a[188:191],v[76:79],a[108:111]
s_sub_u32 s61, s61, s93
s_cmp_eq_u32 s61, 0
s_cselect_b32 s60, 16, 0
MM a[100:103],a[188:191],v[68:71],a[100:103]
s_or_b32 s82, s82, s60
s_cmp_eq_u32 s61, 8
s_cselect_b32 s60, 4096, 0
MM a[100:103],a[184:187],v[64:67],a[100:103]
s_or_b32 s82, s82, s60
EX v63,v63
v_smfmac_f32_16x16x64_bf16 v[236:239], v[144:147], v[0:7], v234
s_lshr_b32 s61, s57, 2
CV v28,v56,v57
v_smfmac_f32_16x16x64_bf16 v[236:239], v[148:151], v[8:15], v234
s_sub_u32 s61, s61, s93
CV v29,v58,v59
v_smfmac_f32_16x16x64_bf16 v[236:239], v[152:155], v[16:23], v234
CV v30,v60,v61
CV v31,v62,v63
s_nop 1
v_smfmac_f32_16x16x64_bf16 v[236:239], v[156:159], v[24:31], v234
s_waitcnt lgkmcnt(0)
s_cmp_eq_u32 s61, 0
s_cselect_b32 s60, 32, 0
MM v[64:67],a[192:195],v[160:163],v[128:131]
s_or_b32 s82, s82, s60
s_cmp_eq_u32 s61, 8
s_cselect_b32 s60, 8192, 0
MM v[64:67],a[196:199],v[176:179],v[64:67]
s_or_b32 s82, s82, s60
TR a[128:129],v232 offset:32768
TR a[130:131],v232 offset:36864
MM v[64:67],a[200:203],v[192:195],v[64:67]
s_lshr_b32 s61, s58, 2
s_add_u32 s64, s64, 16384
s_add_u32 s81, s64, 896
MM v[64:67],a[204:207],v[208:211],v[64:67]
s_sub_u32 s61, s61, s93
TR a[132:133],v232 offset:40960
v_add_f32 v246, v236, v237
MM v[68:71],a[220:223],v[208:211],v[128:131]
s_cmp_eq_u32 s61, 0
s_cselect_b32 s60, 64, 0
v_cmp_nge_f32 vcc, s71, v246
MM v[68:71],a[216:219],v[192:195],v[68:71]
s_cbranch_vccnz LSITE18_%=
LBACK18_%=:
TR a[134:135],v232 offset:45056
TR a[136:137],v233 offset:32768
MM v[68:71],a[212:215],v[176:179],v[68:71]
v_bfe_u32 v253, v248, 4, 4
v_lshlrev_b32 v253, 2, v253
MM v[68:71],a[208:211],v[160:163],v[68:71]
s_and_b32 s76, s48, 0xffffffc0
TR a[138:139],v233 offset:36864
TR a[140:141],v233 offset:40960
MM v[76:79],a[208:211],v[164:167],v[132:135]
s_add_u32 s76, s76, 77824
buffer_load_dword v252, v253, s[20:23], s76 offen offset:0 sc1
MM v[76:79],a[212:215],v[180:183],v[76:79]
s_or_b32 s82, s82, s60
s_cmp_eq_u32 s61, 8
s_cselect_b32 s60, 16384, 0
MM v[76:79],a[216:219],v[196:199],v[76:79]
s_or_b32 s82, s82, s60
EX v64,v64
MM v[76:79],a[220:223],v[212:215],v[76:79]
s_lshr_b32 s61, s59, 2
EX v65,v65
MM v[72:75],a[204:207],v[212:215],v[132:135]
s_sub_u32 s61, s61, s93
s_cmp_eq_u32 s61, 0
)ASM"
R"ASM(
s_cselect_b32 s60, 128, 0
MM v[72:75],a[200:203],v[196:199],v[72:75]
s_or_b32 s82, s82, s60
s_cmp_eq_u32 s61, 8
s_cselect_b32 s60, 32768, 0
MM v[72:75],a[196:199],v[180:183],v[72:75]
s_or_b32 s82, s82, s60
s_and_b32 s60, s82, 255
s_cselect_b32 s51, 0, 31
MM v[72:75],a[192:195],v[164:167],v[72:75]
EX v66,v66
MM v[80:83],a[192:195],v[168:171],v[136:139]
EX v67,v67
MM v[80:83],a[196:199],v[184:187],v[80:83]
TR a[142:143],v233 offset:45056
TR a[144:145],v232 offset:33280
MM v[80:83],a[200:203],v[200:203],v[80:83]
EX v68,v68
MM v[80:83],a[204:207],v[216:219],v[80:83]
TR a[146:147],v232 offset:37376
CV v64,v64,v65
MM v[84:87],a[220:223],v[216:219],v[136:139]
EX v69,v69
MM v[84:87],a[216:219],v[200:203],v[84:87]
TR a[148:149],v232 offset:41472
CV v65,v66,v67
MM v[84:87],a[212:215],v[184:187],v[84:87]
EX v70,v70
MM v[84:87],a[208:211],v[168:171],v[84:87]
EX v71,v71
MM v[92:95],a[208:211],v[172:175],v[140:143]
TR a[150:151],v232 offset:45568
TR a[152:153],v233 offset:33280
MM v[92:95],a[212:215],v[188:191],v[92:95]
EX v72,v72
MM v[92:95],a[216:219],v[204:207],v[92:95]
TR a[154:155],v233 offset:37376
CV v66,v68,v69
MM v[92:95],a[220:223],v[220:223],v[92:95]
EX v73,v73
MM v[88:91],a[204:207],v[220:223],v[140:143]
TR a[156:157],v233 offset:41472
CV v67,v70,v71
MM v[88:91],a[200:203],v[204:207],v[88:91]
EX v74,v74
MM v[88:91],a[196:199],v[188:191],v[88:91]
TR a[158:159],v233 offset:45568
CV v72,v72,v73
MM v[88:91],a[192:195],v[172:175],v[88:91]
EX v75,v75
MM v[96:99],a[224:227],v[160:163],v[128:131]
TR a[160:161],v232 offset:33792
TR a[162:163],v232 offset:37888
MM v[96:99],a[228:231],v[176:179],v[96:99]
EX v76,v76
MM v[96:99],a[232:235],v[192:195],v[96:99]
EX v77,v77
MM v[96:99],a[236:239],v[208:211],v[96:99]
TR a[164:165],v232 offset:41984
CV v73,v74,v75
LPG12_%=:
s_cbranch_execz LTR0_%=
MM v[100:103],a[252:255],v[208:211],v[128:131]
EX v78,v78
MM v[100:103],a[248:251],v[192:195],v[100:103]
TR a[166:167],v232 offset:46080
CV v74,v76,v77
MM v[100:103],a[244:247],v[176:179],v[100:103]
EX v79,v79
MM v[100:103],a[240:243],v[160:163],v[100:103]
TR a[168:169],v233 offset:33792
TR a[170:171],v233 offset:37888
MM v[108:111],a[240:243],v[164:167],v[132:135]
EX v80,v80
MM v[108:111],a[244:247],v[180:183],v[108:111]
EX v81,v81
MM v[108:111],a[248:251],v[196:199],v[108:111]
TR a[172:173],v233 offset:41984
CV v75,v78,v79
MM v[108:111],a[252:255],v[212:215],v[108:111]
EX v82,v82
MM v[104:107],a[236:239],v[212:215],v[132:135]
TR a[174:175],v233 offset:46080
CV v80,v80,v81
MM v[104:107],a[232:235],v[196:199],v[104:107]
EX v83,v83
MM v[104:107],a[228:231],v[180:183],v[104:107]
TR a[176:177],v232 offset:34304
TR a[178:179],v232 offset:38400
MM v[104:107],a[224:227],v[164:167],v[104:107]
EX v84,v84
MM v[112:115],a[224:227],v[168:171],v[136:139]
EX v85,v85
MM v[112:115],a[228:231],v[184:187],v[112:115]
TR a[180:181],v232 offset:42496
CV v81,v82,v83
MM v[112:115],a[232:235],v[200:203],v[112:115]
EX v86,v86
MM v[112:115],a[236:239],v[216:219],v[112:115]
TR a[182:183],v232 offset:46592
CV v82,v84,v85
MM v[116:119],a[252:255],v[216:219],v[136:139]
EX v87,v87
MM v[116:119],a[248:251],v[200:203],v[116:119]
TR a[184:185],v233 offset:34304
TR a[186:187],v233 offset:38400
MM v[116:119],a[244:247],v[184:187],v[116:119]
EX v88,v88
MM v[116:119],a[240:243],v[168:171],v[116:119]
TR a[188:189],v233 offset:42496
CV v83,v86,v87
MM v[124:127],a[240:243],v[172:175],v[140:143]
EX v89,v89
MM v[124:127],a[244:247],v[188:191],v[124:127]
TR a[190:191],v233 offset:46592
CV v88,v88,v89
MM v[124:127],a[248:251],v[204:207],v[124:127]
EX v90,v90
MM v[124:127],a[252:255],v[220:223],v[124:127]
EX v91,v91
MM v[120:123],a[236:239],v[220:223],v[140:143]
EX v92,v92
MM v[120:123],a[232:235],v[204:207],v[120:123]
EX v93,v93
MM v[120:123],a[228:231],v[188:191],v[120:123]
s_getpc_b64 s[98:99]
LTA2_%=:
s_lshl_b32 s73, %[swave], 12
s_add_u32 s73, s73, LPG13_%=-LTA2_%=
s_add_u32 s98, s98, s73
s_addc_u32 s99, s99, 0
s_mov_b64 exec, 0
s_setpc_b64 s[98:99]
LTR2_%=:
s_mov_b64 exec, -1
s_nop 0
CV v89,v90,v91
MM v[120:123],a[224:227],v[172:175],v[120:123]
s_waitcnt vmcnt(14) lgkmcnt(0)
s_barrier
MM a[0:3],a[128:131],v[0:3],a[0:3]
s_waitcnt vmcnt(0)
v_cmp_ne_u32 s[90:91], s69, v252
CV v90,v92,v93
MM a[0:3],a[132:135],v[4:7],a[0:3]
s_add_u32 m0, s65, 32768
EX v94,v94
MM a[8:11],a[132:135],v[12:15],a[8:11]
s_cmp_lg_u64 s[90:91], 0
s_cbranch_scc1 LRFG_%=
LRFGb_%=:
v_lshrrev_b32 v253, 8, v248
MM a[8:11],a[128:131],v[8:11],a[8:11]
v_lshlrev_b32 v253, 10, v253
v_and_b32 v252, 240, v248
MM a[16:19],a[128:131],v[16:19],a[16:19]
RD a[192:195],v224 offset:0
v_add_u32 v253, v253, v252
MM a[16:19],a[132:135],v[20:23],a[16:19]
s_lshr_b32 s76, s48, 6
EX v95,v95
MM a[24:27],a[132:135],v[28:31],a[24:27]
s_lshl_b32 s76, s76, 13
EX v96,v96
MM a[24:27],a[128:131],v[24:27],a[24:27]
s_bfe_u32 s90, s48, 131076
EX v97,v97
MM a[28:31],a[136:139],v[24:27],a[28:31]
s_lshl_b32 s90, s90, 8
RD a[196:199],v225 offset:0
CV v91,v94,v95
MM a[28:31],a[140:143],v[28:31],a[28:31]
s_add_u32 s76, s76, s90
EX v98,v98
MM a[20:23],a[140:143],v[20:23],a[20:23]
s_add_u32 s76, s76, 22183936
buffer_load_dwordx4 v228, s[8:11], s64 offen offset:0 lds
MM a[20:23],a[136:139],v[16:19],a[20:23]
s_lshl_b32 s90, %[swave], 11
EX v99,v99
MM a[12:15],a[136:139],v[8:11],a[12:15]
s_add_u32 s90, s90, %[slds]
s_add_u32 m0, s90, 131072
s_nop 0
buffer_load_dwordx4 v253, s[20:23], s76 offen offset:0 sc1 lds
MM a[12:15],a[140:143],v[12:15],a[12:15]
s_add_u32 m0, s65, 32768
EX v100,v100
MM a[4:7],a[140:143],v[4:7],a[4:7]
s_add_u32 m0, s90, 132096
s_add_u32 s76, s76, 4096
buffer_load_dwordx4 v253, s[20:23], s76 offen offset:0 sc1 lds
MM a[4:7],a[136:139],v[0:3],a[4:7]
EX v101,v101
MM a[32:35],a[144:147],v[0:3],a[32:35]
RD a[200:203],v226 offset:0
CV v68,v96,v97
MM a[32:35],a[148:151],v[4:7],a[32:35]
EX v102,v102
MM a[40:43],a[148:151],v[12:15],a[40:43]
RD a[204:207],v227 offset:0
CV v69,v98,v99
MM a[40:43],a[144:147],v[8:11],a[40:43]
EX v103,v103
MM a[48:51],a[144:147],v[16:19],a[48:51]
s_add_u32 m0, s65, 32768
s_nop 0
buffer_load_dwordx4 v229, s[8:11], s64 offen offset:1024 lds
MM a[48:51],a[148:151],v[20:23],a[48:51]
EX v104,v104
MM a[56:59],a[148:151],v[28:31],a[56:59]
CV v70,v100,v101
CV v71,v102,v103
MM a[56:59],a[144:147],v[24:27],a[56:59]
EX v105,v105
MM a[60:63],a[152:155],v[24:27],a[60:63]
EX v106,v106
MM a[60:63],a[156:159],v[28:31],a[60:63]
RD a[208:211],v224 offset:4096
RD a[212:215],v225 offset:4096
MM a[52:55],a[156:159],v[20:23],a[52:55]
EX v107,v107
MM a[52:55],a[152:155],v[16:19],a[52:55]
buffer_load_dwordx4 v230, s[8:11], s64 offen offset:2048 lds
MM a[44:47],a[152:155],v[8:11],a[44:47]
EX v108,v108
MM a[44:47],a[156:159],v[12:15],a[44:47]
CV v76,v104,v105
CV v77,v106,v107
MM a[36:39],a[156:159],v[4:7],a[36:39]
EX v109,v109
MM a[36:39],a[152:155],v[0:3],a[36:39]
RD a[216:219],v226 offset:4096
CV v78,v108,v109
MM a[64:67],a[160:163],v[0:3],a[64:67]
EX v110,v110
MM a[64:67],a[164:167],v[4:7],a[64:67]
EX v111,v111
MM a[72:75],a[164:167],v[12:15],a[72:75]
buffer_load_dwordx4 v231, s[8:11], s64 offen offset:3072 lds
MM a[72:75],a[160:163],v[8:11],a[72:75]
s_add_u32 m0, s65, 98304
EX v112,v112
MM a[80:83],a[160:163],v[16:19],a[80:83]
RD a[220:223],v227 offset:4096
CV v79,v110,v111
MM a[80:83],a[164:167],v[20:23],a[80:83]
EX v113,v113
MM a[88:91],a[164:167],v[28:31],a[88:91]
RD a[224:227],v224 offset:8192
CV v84,v112,v113
MM a[88:91],a[160:163],v[24:27],a[88:91]
EX v114,v114
MM a[92:95],a[168:171],v[24:27],a[92:95]
EX v115,v115
MM a[92:95],a[172:175],v[28:31],a[92:95]
buffer_load_dwordx4 v235, s[12:15], s81 offen offset:0 lds
MM a[84:87],a[172:175],v[20:23],a[84:87]
RD a[228:231],v225 offset:8192
CV v85,v114,v115
MM a[84:87],a[168:171],v[16:19],a[84:87]
EX v116,v116
MM a[76:79],a[168:171],v[8:11],a[76:79]
EX v117,v117
MM a[76:79],a[172:175],v[12:15],a[76:79]
RD a[232:235],v226 offset:8192
CV v86,v116,v117
MM a[68:71],a[172:175],v[4:7],a[68:71]
EX v118,v118
MM a[68:71],a[168:171],v[0:3],a[68:71]
buffer_load_dwordx4 v235, s[12:15], s64 offen offset:1024 lds
MM a[96:99],a[176:179],v[0:3],a[96:99]
EX v119,v119
MM a[96:99],a[180:183],v[4:7],a[96:99]
RD a[236:239],v227 offset:8192
CV v87,v118,v119
MM a[104:107],a[180:183],v[12:15],a[104:107]
EX v120,v120
MM a[104:107],a[176:179],v[8:11],a[104:107]
EX v121,v121
MM a[112:115],a[176:179],v[16:19],a[112:115]
RD a[240:243],v224 offset:12288
CV v92,v120,v121
MM a[112:115],a[180:183],v[20:23],a[112:115]
EX v122,v122
MM a[120:123],a[180:183],v[28:31],a[120:123]
buffer_load_dwordx4 v235, s[12:15], s81 offen offset:2048 lds
MM a[120:123],a[176:179],v[24:27],a[120:123]
EX v123,v123
MM a[124:127],a[184:187],v[24:27],a[124:127]
RD a[244:247],v225 offset:12288
CV v93,v122,v123
MM a[124:127],a[188:191],v[28:31],a[124:127]
EX v124,v124
MM a[116:119],a[188:191],v[20:23],a[116:119]
EX v125,v125
MM a[116:119],a[184:187],v[16:19],a[116:119]
RD a[248:251],v226 offset:12288
CV v94,v124,v125
MM a[108:111],a[184:187],v[8:11],a[108:111]
EX v126,v126
MM a[108:111],a[188:191],v[12:15],a[108:111]
buffer_load_dwordx4 v235, s[12:15], s64 offen offset:3072 lds
MM a[100:103],a[188:191],v[4:7],a[100:103]
EX v127,v127
MM a[100:103],a[184:187],v[0:3],a[100:103]
RD a[252:255],v227 offset:12288
CV v95,v126,v127
v_smfmac_f32_16x16x64_bf16 v[236:239], v[144:147], v[64:71], v234
v_smfmac_f32_16x16x64_bf16 v[236:239], v[148:151], v[72:79], v234
v_smfmac_f32_16x16x64_bf16 v[236:239], v[152:155], v[80:87], v234
v_smfmac_f32_16x16x64_bf16 v[236:239], v[156:159], v[88:95], v234
s_waitcnt lgkmcnt(0)
MM v[0:3],a[192:195],v[160:163],v[128:131]
s_add_u32 s64, s64, 16384
TR a[128:129],v232 offset:49152
s_add_u32 s81, s64, 896
MM v[0:3],a[196:199],v[176:179],v[0:3]
TR a[130:131],v232 offset:53248
TR a[132:133],v232 offset:57344
MM v[0:3],a[200:203],v[192:195],v[0:3]
TR a[134:135],v232 offset:61440
TR a[136:137],v233 offset:49152
MM v[0:3],a[204:207],v[208:211],v[0:3]
TR a[138:139],v233 offset:53248
v_add_f32 v247, v236, v237
MM v[4:7],a[220:223],v[208:211],v[128:131]
TR a[140:141],v233 offset:57344
v_cmp_nge_f32 vcc, s71, v247
MM v[4:7],a[216:219],v[192:195],v[4:7]
s_cbranch_vccnz LSITE19_%=
LBACK19_%=:
TR a[142:143],v233 offset:61440
TR a[144:145],v232 offset:49664
MM v[4:7],a[212:215],v[176:179],v[4:7]
v_lshrrev_b32 v253, 7, v248
v_mul_u32_u24 v253, 1280, v253
MM v[4:7],a[208:211],v[160:163],v[4:7]
v_and_b32 v252, 112, v248
v_add_u32 v253, v253, v252
MM v[12:15],a[208:211],v[164:167],v[132:135]
s_mul_i32 s90, %[swave], 30720
EX v0,v0
MM v[12:15],a[212:215],v[180:183],v[12:15]
s_mul_i32 s91, %[swave], 3072
EX v1,v1
MM v[12:15],a[216:219],v[196:199],v[12:15]
s_add_u32 s91, s91, %[slds]
s_add_u32 m0, s91, 151552
s_add_u32 s77, s90, 0
buffer_load_dwordx4 v253, s[28:31], s77 offen offset:0 lds
MM v[12:15],a[220:223],v[212:215],v[12:15]
TR a[146:147],v232 offset:53760
CV v0,v0,v1
MM v[8:11],a[204:207],v[212:215],v[132:135]
EX v2,v2
MM v[8:11],a[200:203],v[196:199],v[8:11]
s_add_u32 m0, s91, 152576
s_add_u32 s77, s90, 10240
buffer_load_dwordx4 v253, s[28:31], s77 offen offset:0 lds
MM v[8:11],a[196:199],v[180:183],v[8:11]
EX v3,v3
MM v[8:11],a[192:195],v[164:167],v[8:11]
TR a[148:149],v232 offset:57856
CV v1,v2,v3
MM v[16:19],a[192:195],v[168:171],v[136:139]
s_add_u32 m0, s91, 153600
s_add_u32 s77, s90, 20480
buffer_load_dwordx4 v253, s[28:31], s77 offen offset:0 lds
MM v[16:19],a[196:199],v[184:187],v[16:19]
EX v4,v4
MM v[16:19],a[200:203],v[200:203],v[16:19]
EX v5,v5
MM v[16:19],a[204:207],v[216:219],v[16:19]
TR a[150:151],v232 offset:61952
CV v2,v4,v5
MM v[20:23],a[220:223],v[216:219],v[136:139]
EX v6,v6
MM v[20:23],a[216:219],v[200:203],v[20:23]
TR a[152:153],v233 offset:49664
TR a[154:155],v233 offset:53760
MM v[20:23],a[212:215],v[184:187],v[20:23]
EX v7,v7
MM v[20:23],a[208:211],v[168:171],v[20:23]
TR a[156:157],v233 offset:57856
CV v3,v6,v7
MM v[28:31],a[208:211],v[172:175],v[140:143]
EX v8,v8
MM v[28:31],a[212:215],v[188:191],v[28:31]
EX v9,v9
MM v[28:31],a[216:219],v[204:207],v[28:31]
TR a[158:159],v233 offset:61952
CV v8,v8,v9
MM v[28:31],a[220:223],v[220:223],v[28:31]
EX v10,v10
MM v[24:27],a[204:207],v[220:223],v[140:143]
)ASM"
R"ASM(
TR a[160:161],v232 offset:50176
TR a[162:163],v232 offset:54272
MM v[24:27],a[200:203],v[204:207],v[24:27]
EX v11,v11
MM v[24:27],a[196:199],v[188:191],v[24:27]
TR a[164:165],v232 offset:58368
CV v9,v10,v11
MM v[24:27],a[192:195],v[172:175],v[24:27]
EX v12,v12
MM v[32:35],a[224:227],v[160:163],v[128:131]
EX v13,v13
MM v[32:35],a[228:231],v[176:179],v[32:35]
TR a[166:167],v232 offset:62464
CV v10,v12,v13
MM v[32:35],a[232:235],v[192:195],v[32:35]
EX v14,v14
MM v[32:35],a[236:239],v[208:211],v[32:35]
TR a[168:169],v233 offset:50176
TR a[170:171],v233 offset:54272
MM v[36:39],a[252:255],v[208:211],v[128:131]
EX v15,v15
MM v[36:39],a[248:251],v[192:195],v[36:39]
TR a[172:173],v233 offset:58368
CV v11,v14,v15
MM v[36:39],a[244:247],v[176:179],v[36:39]
EX v16,v16
MM v[36:39],a[240:243],v[160:163],v[36:39]
EX v17,v17
MM v[44:47],a[240:243],v[164:167],v[132:135]
TR a[174:175],v233 offset:62464
CV v16,v16,v17
MM v[44:47],a[244:247],v[180:183],v[44:47]
EX v18,v18
MM v[44:47],a[248:251],v[196:199],v[44:47]
TR a[176:177],v232 offset:50688
TR a[178:179],v232 offset:54784
MM v[44:47],a[252:255],v[212:215],v[44:47]
EX v19,v19
MM v[40:43],a[236:239],v[212:215],v[132:135]
TR a[180:181],v232 offset:58880
CV v17,v18,v19
MM v[40:43],a[232:235],v[196:199],v[40:43]
EX v20,v20
MM v[40:43],a[228:231],v[180:183],v[40:43]
TR a[182:183],v232 offset:62976
TR a[184:185],v233 offset:50688
MM v[40:43],a[224:227],v[164:167],v[40:43]
EX v21,v21
MM v[48:51],a[224:227],v[168:171],v[136:139]
TR a[186:187],v233 offset:54784
CV v18,v20,v21
MM v[48:51],a[228:231],v[184:187],v[48:51]
EX v22,v22
MM v[48:51],a[232:235],v[200:203],v[48:51]
EX v23,v23
MM v[48:51],a[236:239],v[216:219],v[48:51]
TR a[188:189],v233 offset:58880
CV v19,v22,v23
MM v[52:55],a[252:255],v[216:219],v[136:139]
EX v24,v24
MM v[52:55],a[248:251],v[200:203],v[52:55]
EX v25,v25
MM v[52:55],a[244:247],v[184:187],v[52:55]
TR a[190:191],v233 offset:62976
CV v24,v24,v25
MM v[52:55],a[240:243],v[168:171],v[52:55]
EX v26,v26
MM v[60:63],a[240:243],v[172:175],v[140:143]
EX v27,v27
MM v[60:63],a[244:247],v[188:191],v[60:63]
EX v28,v28
MM v[60:63],a[248:251],v[204:207],v[60:63]
EX v29,v29
MM v[60:63],a[252:255],v[220:223],v[60:63]
EX v30,v30
MM v[56:59],a[236:239],v[220:223],v[140:143]
EX v31,v31
MM v[56:59],a[232:235],v[204:207],v[56:59]
EX v32,v32
MM v[56:59],a[228:231],v[188:191],v[56:59]
EX v33,v33
MM v[56:59],a[224:227],v[172:175],v[56:59]
s_waitcnt lgkmcnt(0)
s_barrier
MM a[0:3],a[128:131],v[64:67],a[0:3]
s_add_u32 m0, s65, 49152
CV v25,v26,v27
CV v26,v28,v29
MM a[0:3],a[132:135],v[68:71],a[0:3]
RD a[192:195],v224 offset:16384
CV v27,v30,v31
MM a[8:11],a[132:135],v[76:79],a[8:11]
buffer_load_dwordx4 v228, s[8:11], s64 offen offset:0 lds
MM a[8:11],a[128:131],v[72:75],a[8:11]
RD a[196:199],v225 offset:16384
CV v4,v32,v33
MM a[16:19],a[128:131],v[80:83],a[16:19]
RD a[200:203],v226 offset:16384
RD a[204:207],v227 offset:16384
MM a[16:19],a[132:135],v[84:87],a[16:19]
buffer_load_dwordx4 v229, s[8:11], s64 offen offset:1024 lds
MM a[24:27],a[132:135],v[92:95],a[24:27]
EX v34,v34
MM a[24:27],a[128:131],v[88:91],a[24:27]
RD a[208:211],v224 offset:20480
RD a[212:215],v225 offset:20480
MM a[28:31],a[136:139],v[88:91],a[28:31]
EX v35,v35
MM a[28:31],a[140:143],v[92:95],a[28:31]
RD a[216:219],v226 offset:20480
CV v5,v34,v35
MM a[20:23],a[140:143],v[84:87],a[20:23]
EX v36,v36
MM a[20:23],a[136:139],v[80:83],a[20:23]
EX v37,v37
MM a[12:15],a[136:139],v[72:75],a[12:15]
RD a[220:223],v227 offset:20480
CV v6,v36,v37
MM a[12:15],a[140:143],v[76:79],a[12:15]
buffer_load_dwordx4 v230, s[8:11], s64 offen offset:2048 lds
MM a[4:7],a[140:143],v[68:71],a[4:7]
EX v38,v38
MM a[4:7],a[136:139],v[64:67],a[4:7]
EX v39,v39
MM a[32:35],a[144:147],v[64:67],a[32:35]
RD a[224:227],v224 offset:24576
CV v7,v38,v39
MM a[32:35],a[148:151],v[68:71],a[32:35]
EX v40,v40
MM a[40:43],a[148:151],v[76:79],a[40:43]
EX v41,v41
MM a[40:43],a[144:147],v[72:75],a[40:43]
RD a[228:231],v225 offset:24576
CV v12,v40,v41
MM a[48:51],a[144:147],v[80:83],a[48:51]
buffer_load_dwordx4 v231, s[8:11], s64 offen offset:3072 lds
MM a[48:51],a[148:151],v[84:87],a[48:51]
s_add_u32 m0, s65, 114688
EX v42,v42
MM a[56:59],a[148:151],v[92:95],a[56:59]
EX v43,v43
MM a[56:59],a[144:147],v[88:91],a[56:59]
RD a[232:235],v226 offset:24576
CV v13,v42,v43
MM a[60:63],a[152:155],v[88:91],a[60:63]
EX v44,v44
MM a[60:63],a[156:159],v[92:95],a[60:63]
buffer_load_dwordx4 v235, s[12:15], s81 offen offset:0 lds
MM a[52:55],a[156:159],v[84:87],a[52:55]
EX v45,v45
MM a[52:55],a[152:155],v[80:83],a[52:55]
RD a[236:239],v227 offset:24576
CV v14,v44,v45
MM a[44:47],a[152:155],v[72:75],a[44:47]
EX v46,v46
MM a[44:47],a[156:159],v[76:79],a[44:47]
EX v47,v47
MM a[36:39],a[156:159],v[68:71],a[36:39]
RD a[240:243],v224 offset:28672
CV v15,v46,v47
MM a[36:39],a[152:155],v[64:67],a[36:39]
EX v48,v48
MM a[64:67],a[160:163],v[64:67],a[64:67]
buffer_load_dwordx4 v235, s[12:15], s64 offen offset:1024 lds
MM a[64:67],a[164:167],v[68:71],a[64:67]
EX v49,v49
MM a[72:75],a[164:167],v[76:79],a[72:75]
RD a[244:247],v225 offset:28672
CV v20,v48,v49
MM a[72:75],a[160:163],v[72:75],a[72:75]
EX v50,v50
MM a[80:83],a[160:163],v[80:83],a[80:83]
EX v51,v51
MM a[80:83],a[164:167],v[84:87],a[80:83]
RD a[248:251],v226 offset:28672
CV v21,v50,v51
MM a[88:91],a[164:167],v[92:95],a[88:91]
EX v52,v52
MM a[88:91],a[160:163],v[88:91],a[88:91]
buffer_load_dwordx4 v235, s[12:15], s81 offen offset:2048 lds
MM a[92:95],a[168:171],v[88:91],a[92:95]
EX v53,v53
MM a[92:95],a[172:175],v[92:95],a[92:95]
RD a[252:255],v227 offset:28672
CV v22,v52,v53
MM a[84:87],a[172:175],v[84:87],a[84:87]
EX v54,v54
MM a[84:87],a[168:171],v[80:83],a[84:87]
EX v55,v55
MM a[76:79],a[168:171],v[72:75],a[76:79]
CV v23,v54,v55
MM a[76:79],a[172:175],v[76:79],a[76:79]
EX v56,v56
MM a[68:71],a[172:175],v[68:71],a[68:71]
buffer_load_dwordx4 v235, s[12:15], s64 offen offset:3072 lds
MM a[68:71],a[168:171],v[64:67],a[68:71]
EX v57,v57
MM a[96:99],a[176:179],v[64:67],a[96:99]
EX v58,v58
MM a[96:99],a[180:183],v[68:71],a[96:99]
EX v59,v59
MM a[104:107],a[180:183],v[76:79],a[104:107]
EX v60,v60
MM a[104:107],a[176:179],v[72:75],a[104:107]
EX v61,v61
MM a[112:115],a[176:179],v[80:83],a[112:115]
EX v62,v62
MM a[112:115],a[180:183],v[84:87],a[112:115]
EX v63,v63
MM a[120:123],a[180:183],v[92:95],a[120:123]
CV v28,v56,v57
CV v29,v58,v59
MM a[120:123],a[176:179],v[88:91],a[120:123]
CV v30,v60,v61
CV v31,v62,v63
MM a[124:127],a[184:187],v[88:91],a[124:127]
MM a[124:127],a[188:191],v[92:95],a[124:127]
MM a[116:119],a[188:191],v[84:87],a[116:119]
MM a[116:119],a[184:187],v[80:83],a[116:119]
MM a[108:111],a[184:187],v[72:75],a[108:111]
MM a[108:111],a[188:191],v[76:79],a[108:111]
MM a[100:103],a[188:191],v[68:71],a[100:103]
MM a[100:103],a[184:187],v[64:67],a[100:103]
v_smfmac_f32_16x16x64_bf16 v[236:239], v[144:147], v[0:7], v234
v_smfmac_f32_16x16x64_bf16 v[236:239], v[148:151], v[8:15], v234
v_smfmac_f32_16x16x64_bf16 v[236:239], v[152:155], v[16:23], v234
v_smfmac_f32_16x16x64_bf16 v[236:239], v[156:159], v[24:31], v234
s_waitcnt lgkmcnt(0)
MM v[64:67],a[192:195],v[160:163],v[128:131]
s_add_u32 s64, s64, 16384
TR a[128:129],v232 offset:0
s_add_u32 s81, s64, 896
MM v[64:67],a[196:199],v[176:179],v[64:67]
TR a[130:131],v232 offset:4096
TR a[132:133],v232 offset:8192
MM v[64:67],a[200:203],v[192:195],v[64:67]
TR a[134:135],v232 offset:12288
TR a[136:137],v233 offset:0
MM v[64:67],a[204:207],v[208:211],v[64:67]
TR a[138:139],v233 offset:4096
v_add_f32 v246, v236, v237
MM v[68:71],a[220:223],v[208:211],v[128:131]
TR a[140:141],v233 offset:8192
v_cmp_nge_f32 vcc, s71, v246
MM v[68:71],a[216:219],v[192:195],v[68:71]
s_cbranch_vccnz LSITE20_%=
LBACK20_%=:
TR a[142:143],v233 offset:12288
TR a[144:145],v232 offset:512
MM v[68:71],a[212:215],v[176:179],v[68:71]
v_lshrrev_b32 v43, 7, v248
v_bfe_u32 v42, v248, 6, 1
MM v[68:71],a[208:211],v[160:163],v[68:71]
v_lshl_add_u32 v40, v42, 3, v43
v_lshlrev_b32 v40, 4, v40
MM v[76:79],a[208:211],v[164:167],v[132:135]
v_bfe_u32 v42, v248, 4, 2
v_lshl_add_u32 v40, v42, 2, v40
MM v[76:79],a[212:215],v[180:183],v[76:79]
s_lshl_b32 s76, %[swave], 11
LPG13_%=:
s_cbranch_execz LTR2_%=
EX v64,v64
MM v[76:79],a[216:219],v[196:199],v[76:79]
s_add_u32 s76, s76, %[slds]
EX v65,v65
MM v[76:79],a[220:223],v[212:215],v[76:79]
s_add_u32 s76, s76, 131072
v_add_u32 v40, s76, v40
CV v64,v64,v65
MM v[72:75],a[204:207],v[212:215],v[132:135]
s_waitcnt vmcnt(18)
ds_read_b32 v32, v40 offset:0
ds_read_b32 v36, v40 offset:256
MM v[72:75],a[200:203],v[196:199],v[72:75]
ds_read_b32 v33, v40 offset:512
ds_read_b32 v37, v40 offset:768
MM v[72:75],a[196:199],v[180:183],v[72:75]
ds_read_b32 v34, v40 offset:1024
ds_read_b32 v38, v40 offset:1280
MM v[72:75],a[192:195],v[164:167],v[72:75]
ds_read_b32 v35, v40 offset:1536
ds_read_b32 v39, v40 offset:1792
MM v[80:83],a[192:195],v[168:171],v[136:139]
v_mul_u32_u24 v41, 3456, v43
v_bfe_u32 v42, v248, 4, 3
MM v[80:83],a[196:199],v[184:187],v[80:83]
TR a[146:147],v232 offset:4608
v_lshl_add_u32 v41, v42, 1, v41
MM v[80:83],a[200:203],v[200:203],v[80:83]
s_add_u32 s76, s48, 21626880
EX v66,v66
MM v[80:83],a[204:207],v[216:219],v[80:83]
s_lshl_b32 s77, %[swave], 4
EX v67,v67
MM v[84:87],a[220:223],v[216:219],v[136:139]
EX v68,v68
MM v[84:87],a[216:219],v[200:203],v[84:87]
EX v69,v69
MM v[84:87],a[212:215],v[184:187],v[84:87]
EX v70,v70
MM v[84:87],a[208:211],v[168:171],v[84:87]
TR a[148:149],v232 offset:8704
CV v65,v66,v67
MM v[92:95],a[208:211],v[172:175],v[140:143]
EX v71,v71
MM v[92:95],a[212:215],v[188:191],v[92:95]
TR a[150:151],v232 offset:12800
TR a[152:153],v233 offset:512
MM v[92:95],a[216:219],v[204:207],v[92:95]
EX v72,v72
MM v[92:95],a[220:223],v[220:223],v[92:95]
s_waitcnt lgkmcnt(4)
v_add_f32 v32, v32, v33
v_add_f32 v36, v36, v37
MM v[88:91],a[204:207],v[220:223],v[140:143]
v_add_f32 v34, v34, v35
v_add_f32 v38, v38, v39
MM v[88:91],a[200:203],v[204:207],v[88:91]
v_add_f32 v32, v32, v34
v_add_f32 v36, v36, v38
EX v73,v73
MM v[88:91],a[196:199],v[188:191],v[88:91]
TR a[154:155],v233 offset:4608
CV v42,v32,v36
v_lshlrev_b32 v32, 16, v42
MM v[88:91],a[192:195],v[172:175],v[88:91]
v_and_b32 v36, 0xffff0000, v42
v_mul_f32 v42, 0xbfb8aa3b, v32
EX v74,v74
MM v[96:99],a[224:227],v[160:163],v[128:131]
TR a[156:157],v233 offset:8704
EX v42,v42
EX v75,v75
CV v66,v68,v69
MM v[96:99],a[228:231],v[176:179],v[96:99]
TR a[158:159],v233 offset:12800
v_add_f32 v42, 1.0, v42
MM v[96:99],a[232:235],v[192:195],v[96:99]
v_rcp_f32 v42, v42
EX v76,v76
MM v[96:99],a[236:239],v[208:211],v[96:99]
v_mul_f32 v42, v32, v42
CV v42,v42,v42
MM v[100:103],a[252:255],v[208:211],v[128:131]
TR a[160:161],v232 offset:1024
v_lshlrev_b32 v42, 16, v42
v_mul_f32 v32, v42, v36
EX v77,v77
CV v67,v70,v71
MM v[100:103],a[248:251],v[192:195],v[100:103]
TR a[162:163],v232 offset:5120
CV v43,v32,v32
MM v[100:103],a[244:247],v[176:179],v[100:103]
s_lshl_b64 exec, 0xffff, s77
buffer_store_short v43, v41, s[20:23], s76 offen offset:0 sc1
s_mov_b64 exec, -1
s_nop 0
EX v78,v78
MM v[100:103],a[240:243],v[160:163],v[100:103]
TR a[164:165],v232 offset:9216
CV v72,v72,v73
MM v[108:111],a[240:243],v[164:167],v[132:135]
EX v79,v79
MM v[108:111],a[244:247],v[180:183],v[108:111]
TR a[166:167],v232 offset:13312
CV v73,v74,v75
MM v[108:111],a[248:251],v[196:199],v[108:111]
EX v80,v80
MM v[108:111],a[252:255],v[212:215],v[108:111]
TR a[168:169],v233 offset:1024
TR a[170:171],v233 offset:5120
EX v81,v81
MM v[104:107],a[236:239],v[212:215],v[132:135]
TR a[172:173],v233 offset:9216
CV v74,v76,v77
MM v[104:107],a[232:235],v[196:199],v[104:107]
EX v82,v82
MM v[104:107],a[228:231],v[180:183],v[104:107]
EX v83,v83
MM v[104:107],a[224:227],v[164:167],v[104:107]
TR a[174:175],v233 offset:13312
CV v75,v78,v79
MM v[112:115],a[224:227],v[168:171],v[136:139]
EX v84,v84
MM v[112:115],a[228:231],v[184:187],v[112:115]
TR a[176:177],v232 offset:1536
TR a[178:179],v232 offset:5632
MM v[112:115],a[232:235],v[200:203],v[112:115]
EX v85,v85
MM v[112:115],a[236:239],v[216:219],v[112:115]
TR a[180:181],v232 offset:9728
CV v80,v80,v81
MM v[116:119],a[252:255],v[216:219],v[136:139]
)ASM"
R"ASM(
EX v86,v86
MM v[116:119],a[248:251],v[200:203],v[116:119]
TR a[182:183],v232 offset:13824
CV v81,v82,v83
MM v[116:119],a[244:247],v[184:187],v[116:119]
EX v87,v87
MM v[116:119],a[240:243],v[168:171],v[116:119]
TR a[184:185],v233 offset:1536
CV v82,v84,v85
MM v[124:127],a[240:243],v[172:175],v[140:143]
EX v88,v88
MM v[124:127],a[244:247],v[188:191],v[124:127]
TR a[186:187],v233 offset:5632
TR a[188:189],v233 offset:9728
MM v[124:127],a[248:251],v[204:207],v[124:127]
TR a[190:191],v233 offset:13824
CV v83,v86,v87
MM v[124:127],a[252:255],v[220:223],v[124:127]
EX v89,v89
MM v[120:123],a[236:239],v[220:223],v[140:143]
EX v90,v90
MM v[120:123],a[232:235],v[204:207],v[120:123]
EX v91,v91
MM v[120:123],a[228:231],v[188:191],v[120:123]
EX v92,v92
MM v[120:123],a[224:227],v[172:175],v[120:123]
s_waitcnt vmcnt(16) lgkmcnt(0)
s_barrier
MM a[0:3],a[128:131],v[0:3],a[0:3]
s_add_u32 m0, s65, 0
CV v88,v88,v89
CV v89,v90,v91
MM a[0:3],a[132:135],v[4:7],a[0:3]
EX v93,v93
MM a[8:11],a[132:135],v[12:15],a[8:11]
EX v94,v94
MM a[8:11],a[128:131],v[8:11],a[8:11]
EX v95,v95
MM a[16:19],a[128:131],v[16:19],a[16:19]
RD a[192:195],v224 offset:32768
CV v90,v92,v93
MM a[16:19],a[132:135],v[20:23],a[16:19]
EX v96,v96
MM a[24:27],a[132:135],v[28:31],a[24:27]
RD a[196:199],v225 offset:32768
CV v91,v94,v95
MM a[24:27],a[128:131],v[24:27],a[24:27]
EX v97,v97
MM a[28:31],a[136:139],v[24:27],a[28:31]
buffer_load_dwordx4 v228, s[8:11], s64 offen offset:0 lds
MM a[28:31],a[140:143],v[28:31],a[28:31]
EX v98,v98
MM a[20:23],a[140:143],v[20:23],a[20:23]
EX v99,v99
MM a[20:23],a[136:139],v[16:19],a[20:23]
RD a[200:203],v226 offset:32768
CV v68,v96,v97
MM a[12:15],a[136:139],v[8:11],a[12:15]
EX v100,v100
MM a[12:15],a[140:143],v[12:15],a[12:15]
RD a[204:207],v227 offset:32768
CV v69,v98,v99
MM a[4:7],a[140:143],v[4:7],a[4:7]
EX v101,v101
MM a[4:7],a[136:139],v[0:3],a[4:7]
buffer_load_dwordx4 v229, s[8:11], s64 offen offset:1024 lds
MM a[32:35],a[144:147],v[0:3],a[32:35]
EX v102,v102
MM a[32:35],a[148:151],v[4:7],a[32:35]
RD a[208:211],v224 offset:36864
CV v70,v100,v101
MM a[40:43],a[148:151],v[12:15],a[40:43]
EX v103,v103
MM a[40:43],a[144:147],v[8:11],a[40:43]
RD a[212:215],v225 offset:36864
CV v71,v102,v103
MM a[48:51],a[144:147],v[16:19],a[48:51]
EX v104,v104
MM a[48:51],a[148:151],v[20:23],a[48:51]
EX v105,v105
MM a[56:59],a[148:151],v[28:31],a[56:59]
buffer_load_dwordx4 v230, s[8:11], s64 offen offset:2048 lds
MM a[56:59],a[144:147],v[24:27],a[56:59]
EX v106,v106
MM a[60:63],a[152:155],v[24:27],a[60:63]
RD a[216:219],v226 offset:36864
CV v76,v104,v105
MM a[60:63],a[156:159],v[28:31],a[60:63]
EX v107,v107
MM a[52:55],a[156:159],v[20:23],a[52:55]
RD a[220:223],v227 offset:36864
CV v77,v106,v107
MM a[52:55],a[152:155],v[16:19],a[52:55]
EX v108,v108
MM a[44:47],a[152:155],v[8:11],a[44:47]
EX v109,v109
MM a[44:47],a[156:159],v[12:15],a[44:47]
buffer_load_dwordx4 v231, s[8:11], s64 offen offset:3072 lds
MM a[36:39],a[156:159],v[4:7],a[36:39]
s_add_u32 m0, s65, 65536
RD a[224:227],v224 offset:40960
CV v78,v108,v109
MM a[36:39],a[152:155],v[0:3],a[36:39]
EX v110,v110
MM a[64:67],a[160:163],v[0:3],a[64:67]
EX v111,v111
MM a[64:67],a[164:167],v[4:7],a[64:67]
RD a[228:231],v225 offset:40960
CV v79,v110,v111
MM a[72:75],a[164:167],v[12:15],a[72:75]
EX v112,v112
MM a[72:75],a[160:163],v[8:11],a[72:75]
buffer_load_dwordx4 v235, s[12:15], s81 offen offset:0 lds
MM a[80:83],a[160:163],v[16:19],a[80:83]
EX v113,v113
MM a[80:83],a[164:167],v[20:23],a[80:83]
RD a[232:235],v226 offset:40960
CV v84,v112,v113
MM a[88:91],a[164:167],v[28:31],a[88:91]
EX v114,v114
MM a[88:91],a[160:163],v[24:27],a[88:91]
EX v115,v115
MM a[92:95],a[168:171],v[24:27],a[92:95]
RD a[236:239],v227 offset:40960
CV v85,v114,v115
MM a[92:95],a[172:175],v[28:31],a[92:95]
EX v116,v116
MM a[84:87],a[172:175],v[20:23],a[84:87]
buffer_load_dwordx4 v235, s[12:15], s64 offen offset:1024 lds
MM a[84:87],a[168:171],v[16:19],a[84:87]
EX v117,v117
MM a[76:79],a[168:171],v[8:11],a[76:79]
RD a[240:243],v224 offset:45056
CV v86,v116,v117
MM a[76:79],a[172:175],v[12:15],a[76:79]
EX v118,v118
MM a[68:71],a[172:175],v[4:7],a[68:71]
EX v119,v119
MM a[68:71],a[168:171],v[0:3],a[68:71]
RD a[244:247],v225 offset:45056
CV v87,v118,v119
MM a[96:99],a[176:179],v[0:3],a[96:99]
EX v120,v120
MM a[96:99],a[180:183],v[4:7],a[96:99]
buffer_load_dwordx4 v235, s[12:15], s81 offen offset:2048 lds
MM a[104:107],a[180:183],v[12:15],a[104:107]
EX v121,v121
MM a[104:107],a[176:179],v[8:11],a[104:107]
RD a[248:251],v226 offset:45056
CV v92,v120,v121
MM a[112:115],a[176:179],v[16:19],a[112:115]
EX v122,v122
MM a[112:115],a[180:183],v[20:23],a[112:115]
EX v123,v123
MM a[120:123],a[180:183],v[28:31],a[120:123]
RD a[252:255],v227 offset:45056
CV v93,v122,v123
MM a[120:123],a[176:179],v[24:27],a[120:123]
EX v124,v124
MM a[124:127],a[184:187],v[24:27],a[124:127]
buffer_load_dwordx4 v235, s[12:15], s64 offen offset:3072 lds
MM a[124:127],a[188:191],v[28:31],a[124:127]
EX v125,v125
MM a[116:119],a[188:191],v[20:23],a[116:119]
EX v126,v126
MM a[116:119],a[184:187],v[16:19],a[116:119]
EX v127,v127
MM a[108:111],a[184:187],v[8:11],a[108:111]
CV v94,v124,v125
CV v95,v126,v127
MM a[108:111],a[188:191],v[12:15],a[108:111]
MM a[100:103],a[188:191],v[4:7],a[100:103]
MM a[100:103],a[184:187],v[0:3],a[100:103]
v_smfmac_f32_16x16x64_bf16 v[236:239], v[144:147], v[64:71], v234
v_smfmac_f32_16x16x64_bf16 v[236:239], v[148:151], v[72:79], v234
v_smfmac_f32_16x16x64_bf16 v[236:239], v[152:155], v[80:87], v234
v_smfmac_f32_16x16x64_bf16 v[236:239], v[156:159], v[88:95], v234
s_waitcnt lgkmcnt(0)
MM v[0:3],a[192:195],v[160:163],v[128:131]
s_add_u32 s64, s64, 16384
TR a[128:129],v232 offset:16384
s_add_u32 s81, s64, 896
MM v[0:3],a[196:199],v[176:179],v[0:3]
TR a[130:131],v232 offset:20480
TR a[132:133],v232 offset:24576
MM v[0:3],a[200:203],v[192:195],v[0:3]
TR a[134:135],v232 offset:28672
TR a[136:137],v233 offset:16384
MM v[0:3],a[204:207],v[208:211],v[0:3]
TR a[138:139],v233 offset:20480
v_add_f32 v247, v236, v237
MM v[4:7],a[220:223],v[208:211],v[128:131]
TR a[140:141],v233 offset:24576
v_cmp_nge_f32 vcc, s71, v247
MM v[4:7],a[216:219],v[192:195],v[4:7]
s_cbranch_vccnz LSITE21_%=
LBACK21_%=:
TR a[142:143],v233 offset:28672
TR a[144:145],v232 offset:16896
MM v[4:7],a[212:215],v[176:179],v[4:7]
s_lshr_b32 s77, %[sb], 3
TR a[146:147],v232 offset:20992
TR a[148:149],v232 offset:25088
MM v[4:7],a[208:211],v[160:163],v[4:7]
s_lshl_b32 s77, s77, 2
TR a[150:151],v232 offset:29184
TR a[152:153],v233 offset:16896
MM v[12:15],a[208:211],v[164:167],v[132:135]
s_add_u32 s77, s77, %[swave]
EX v0,v0
MM v[12:15],a[212:215],v[180:183],v[12:15]
s_lshl_b32 s77, s77, 9
EX v1,v1
MM v[12:15],a[216:219],v[196:199],v[12:15]
s_add_u32 s77, s77, 0
v_lshrrev_b32 v253, 1, v248
v_add_u32 v253, s77, v253
MM v[12:15],a[220:223],v[212:215],v[12:15]
s_mov_b32 s77, LKEND_ka-LPD1_%=-16
v_min_u32 v253, s77, v253
CV v0,v0,v1
MM v[8:11],a[204:207],v[212:215],v[132:135]
s_getpc_b64 s[98:99]
LPD1_%=:
global_load_dword v253, v253, s[98:99]
MM v[8:11],a[200:203],v[196:199],v[8:11]
EX v2,v2
MM v[8:11],a[196:199],v[180:183],v[8:11]
EX v3,v3
MM v[8:11],a[192:195],v[164:167],v[8:11]
TR a[154:155],v233 offset:20992
CV v1,v2,v3
MM v[16:19],a[192:195],v[168:171],v[136:139]
EX v4,v4
MM v[16:19],a[196:199],v[184:187],v[16:19]
EX v5,v5
MM v[16:19],a[200:203],v[200:203],v[16:19]
TR a[156:157],v233 offset:25088
CV v2,v4,v5
MM v[16:19],a[204:207],v[216:219],v[16:19]
EX v6,v6
MM v[20:23],a[220:223],v[216:219],v[136:139]
EX v7,v7
MM v[20:23],a[216:219],v[200:203],v[20:23]
TR a[158:159],v233 offset:29184
CV v3,v6,v7
MM v[20:23],a[212:215],v[184:187],v[20:23]
EX v8,v8
MM v[20:23],a[208:211],v[168:171],v[20:23]
EX v9,v9
MM v[28:31],a[208:211],v[172:175],v[140:143]
s_add_u32 m0, s45, 147456
s_add_u32 s44, s44, 1024
buffer_load_dwordx4 v248, s[24:27], s44 offen offset:0 lds
MM v[28:31],a[212:215],v[188:191],v[28:31]
TR a[160:161],v232 offset:17408
CV v8,v8,v9
MM v[28:31],a[216:219],v[204:207],v[28:31]
EX v10,v10
MM v[28:31],a[220:223],v[220:223],v[28:31]
EX v11,v11
MM v[24:27],a[204:207],v[220:223],v[140:143]
TR a[162:163],v232 offset:21504
CV v9,v10,v11
MM v[24:27],a[200:203],v[204:207],v[24:27]
TR a[164:165],v232 offset:25600
TR a[166:167],v232 offset:29696
MM v[24:27],a[196:199],v[188:191],v[24:27]
EX v12,v12
MM v[24:27],a[192:195],v[172:175],v[24:27]
EX v13,v13
MM v[32:35],a[224:227],v[160:163],v[128:131]
TR a[168:169],v233 offset:17408
CV v10,v12,v13
MM v[32:35],a[228:231],v[176:179],v[32:35]
EX v14,v14
MM v[32:35],a[232:235],v[192:195],v[32:35]
TR a[170:171],v233 offset:21504
TR a[172:173],v233 offset:25600
MM v[32:35],a[236:239],v[208:211],v[32:35]
EX v15,v15
MM v[36:39],a[252:255],v[208:211],v[128:131]
TR a[174:175],v233 offset:29696
CV v11,v14,v15
MM v[36:39],a[248:251],v[192:195],v[36:39]
EX v16,v16
MM v[36:39],a[244:247],v[176:179],v[36:39]
EX v17,v17
MM v[36:39],a[240:243],v[160:163],v[36:39]
TR a[176:177],v232 offset:17920
CV v16,v16,v17
MM v[44:47],a[240:243],v[164:167],v[132:135]
s_add_u32 s67, s95, 15360
buffer_store_dwordx4 v[240:243], v248, s[20:23], s67 offen offset:0 sc1
MM v[44:47],a[244:247],v[180:183],v[44:47]
EX v18,v18
MM v[44:47],a[248:251],v[196:199],v[44:47]
TR a[178:179],v232 offset:22016
TR a[180:181],v232 offset:26112
MM v[44:47],a[252:255],v[212:215],v[44:47]
EX v19,v19
MM v[40:43],a[236:239],v[212:215],v[132:135]
TR a[182:183],v232 offset:30208
CV v17,v18,v19
MM v[40:43],a[232:235],v[196:199],v[40:43]
EX v20,v20
MM v[40:43],a[228:231],v[180:183],v[40:43]
EX v21,v21
MM v[40:43],a[224:227],v[164:167],v[40:43]
TR a[184:185],v233 offset:17920
CV v18,v20,v21
MM v[48:51],a[224:227],v[168:171],v[136:139]
EX v22,v22
MM v[48:51],a[228:231],v[184:187],v[48:51]
TR a[186:187],v233 offset:22016
TR a[188:189],v233 offset:26112
MM v[48:51],a[232:235],v[200:203],v[48:51]
EX v23,v23
MM v[48:51],a[236:239],v[216:219],v[48:51]
TR a[190:191],v233 offset:30208
CV v19,v22,v23
MM v[52:55],a[252:255],v[216:219],v[136:139]
EX v24,v24
MM v[52:55],a[248:251],v[200:203],v[52:55]
EX v25,v25
MM v[52:55],a[244:247],v[184:187],v[52:55]
EX v26,v26
MM v[52:55],a[240:243],v[168:171],v[52:55]
EX v27,v27
MM v[60:63],a[240:243],v[172:175],v[140:143]
EX v28,v28
MM v[60:63],a[244:247],v[188:191],v[60:63]
EX v29,v29
MM v[60:63],a[248:251],v[204:207],v[60:63]
EX v30,v30
MM v[60:63],a[252:255],v[220:223],v[60:63]
EX v31,v31
MM v[56:59],a[236:239],v[220:223],v[140:143]
EX v32,v32
MM v[56:59],a[232:235],v[204:207],v[56:59]
EX v33,v33
MM v[56:59],a[228:231],v[188:191],v[56:59]
EX v34,v34
MM v[56:59],a[224:227],v[172:175],v[56:59]
s_waitcnt vmcnt(16) lgkmcnt(0)
s_barrier
MM a[0:3],a[128:131],v[64:67],a[0:3]
s_add_u32 m0, s65, 16384
CV v24,v24,v25
CV v25,v26,v27
MM a[0:3],a[132:135],v[68:71],a[0:3]
RD a[192:195],v224 offset:49152
CV v26,v28,v29
MM a[8:11],a[132:135],v[76:79],a[8:11]
RD a[196:199],v225 offset:49152
CV v27,v30,v31
MM a[8:11],a[128:131],v[72:75],a[8:11]
buffer_load_dwordx4 v228, s[8:11], s64 offen offset:0 lds
MM a[16:19],a[128:131],v[80:83],a[16:19]
RD a[200:203],v226 offset:49152
CV v4,v32,v33
MM a[16:19],a[132:135],v[84:87],a[16:19]
buffer_load_dwordx4 v229, s[8:11], s64 offen offset:1024 lds
MM a[24:27],a[132:135],v[92:95],a[24:27]
RD a[204:207],v227 offset:49152
RD a[208:211],v224 offset:53248
MM a[24:27],a[128:131],v[88:91],a[24:27]
EX v35,v35
MM a[28:31],a[136:139],v[88:91],a[28:31]
RD a[212:215],v225 offset:53248
CV v5,v34,v35
MM a[28:31],a[140:143],v[92:95],a[28:31]
EX v36,v36
MM a[20:23],a[140:143],v[84:87],a[20:23]
buffer_load_dwordx4 v230, s[8:11], s64 offen offset:2048 lds
MM a[20:23],a[136:139],v[80:83],a[20:23]
EX v37,v37
MM a[12:15],a[136:139],v[72:75],a[12:15]
RD a[216:219],v226 offset:53248
CV v6,v36,v37
MM a[12:15],a[140:143],v[76:79],a[12:15]
EX v38,v38
MM a[4:7],a[140:143],v[68:71],a[4:7]
EX v39,v39
MM a[4:7],a[136:139],v[64:67],a[4:7]
RD a[220:223],v227 offset:53248
CV v7,v38,v39
MM a[32:35],a[144:147],v[64:67],a[32:35]
EX v40,v40
MM a[32:35],a[148:151],v[68:71],a[32:35]
buffer_load_dwordx4 v231, s[8:11], s64 offen offset:3072 lds
MM a[40:43],a[148:151],v[76:79],a[40:43]
)ASM"
R"ASM(
s_add_u32 m0, s65, 81920
EX v41,v41
MM a[40:43],a[144:147],v[72:75],a[40:43]
RD a[224:227],v224 offset:57344
CV v12,v40,v41
MM a[48:51],a[144:147],v[80:83],a[48:51]
EX v42,v42
MM a[48:51],a[148:151],v[84:87],a[48:51]
EX v43,v43
MM a[56:59],a[148:151],v[92:95],a[56:59]
RD a[228:231],v225 offset:57344
CV v13,v42,v43
MM a[56:59],a[144:147],v[88:91],a[56:59]
EX v44,v44
MM a[60:63],a[152:155],v[88:91],a[60:63]
buffer_load_dwordx4 v235, s[12:15], s81 offen offset:0 lds
MM a[60:63],a[156:159],v[92:95],a[60:63]
EX v45,v45
MM a[52:55],a[156:159],v[84:87],a[52:55]
RD a[232:235],v226 offset:57344
CV v14,v44,v45
MM a[52:55],a[152:155],v[80:83],a[52:55]
EX v46,v46
MM a[44:47],a[152:155],v[72:75],a[44:47]
EX v47,v47
MM a[44:47],a[156:159],v[76:79],a[44:47]
RD a[236:239],v227 offset:57344
CV v15,v46,v47
MM a[36:39],a[156:159],v[68:71],a[36:39]
buffer_load_dwordx4 v235, s[12:15], s64 offen offset:1024 lds
MM a[36:39],a[152:155],v[64:67],a[36:39]
EX v48,v48
MM a[64:67],a[160:163],v[64:67],a[64:67]
EX v49,v49
MM a[64:67],a[164:167],v[68:71],a[64:67]
RD a[240:243],v224 offset:61440
CV v20,v48,v49
MM a[72:75],a[164:167],v[76:79],a[72:75]
EX v50,v50
MM a[72:75],a[160:163],v[72:75],a[72:75]
RD a[244:247],v225 offset:61440
RD a[248:251],v226 offset:61440
MM a[80:83],a[160:163],v[80:83],a[80:83]
EX v51,v51
MM a[80:83],a[164:167],v[84:87],a[80:83]
RD a[252:255],v227 offset:61440
CV v21,v50,v51
MM a[88:91],a[164:167],v[92:95],a[88:91]
EX v52,v52
MM a[88:91],a[160:163],v[88:91],a[88:91]
buffer_load_dwordx4 v235, s[12:15], s81 offen offset:2048 lds
MM a[92:95],a[168:171],v[88:91],a[92:95]
EX v53,v53
MM a[92:95],a[172:175],v[92:95],a[92:95]
EX v54,v54
MM a[84:87],a[172:175],v[84:87],a[84:87]
buffer_load_dwordx4 v235, s[12:15], s64 offen offset:3072 lds
MM a[84:87],a[168:171],v[80:83],a[84:87]
EX v55,v55
MM a[76:79],a[168:171],v[72:75],a[76:79]
EX v56,v56
MM a[76:79],a[172:175],v[76:79],a[76:79]
EX v57,v57
MM a[68:71],a[172:175],v[68:71],a[68:71]
EX v58,v58
MM a[68:71],a[168:171],v[64:67],a[68:71]
EX v59,v59
MM a[96:99],a[176:179],v[64:67],a[96:99]
EX v60,v60
MM a[96:99],a[180:183],v[68:71],a[96:99]
EX v61,v61
s_nop 0
LPG14_%=:
s_cbranch_execz LTR2_%=
MM a[104:107],a[180:183],v[76:79],a[104:107]
EX v62,v62
MM a[104:107],a[176:179],v[72:75],a[104:107]
EX v63,v63
MM a[112:115],a[176:179],v[80:83],a[112:115]
CV v22,v52,v53
CV v23,v54,v55
MM a[112:115],a[180:183],v[84:87],a[112:115]
CV v28,v56,v57
CV v29,v58,v59
MM a[120:123],a[180:183],v[92:95],a[120:123]
CV v30,v60,v61
CV v31,v62,v63
MM a[120:123],a[176:179],v[88:91],a[120:123]
MM a[124:127],a[184:187],v[88:91],a[124:127]
MM a[124:127],a[188:191],v[92:95],a[124:127]
MM a[116:119],a[188:191],v[84:87],a[116:119]
MM a[116:119],a[184:187],v[80:83],a[116:119]
MM a[108:111],a[184:187],v[72:75],a[108:111]
MM a[108:111],a[188:191],v[76:79],a[108:111]
MM a[100:103],a[188:191],v[68:71],a[100:103]
MM a[100:103],a[184:187],v[64:67],a[100:103]
v_smfmac_f32_16x16x64_bf16 v[236:239], v[144:147], v[0:7], v234
v_smfmac_f32_16x16x64_bf16 v[236:239], v[148:151], v[8:15], v234
v_smfmac_f32_16x16x64_bf16 v[236:239], v[152:155], v[16:23], v234
v_smfmac_f32_16x16x64_bf16 v[236:239], v[156:159], v[24:31], v234
s_waitcnt lgkmcnt(0)
MM v[64:67],a[192:195],v[160:163],v[128:131]
s_add_u32 s64, s64, 16384
TR a[128:129],v232 offset:32768
s_add_u32 s81, s64, 896
MM v[64:67],a[196:199],v[176:179],v[64:67]
TR a[130:131],v232 offset:36864
TR a[132:133],v232 offset:40960
MM v[64:67],a[200:203],v[192:195],v[64:67]
TR a[134:135],v232 offset:45056
TR a[136:137],v233 offset:32768
MM v[64:67],a[204:207],v[208:211],v[64:67]
TR a[138:139],v233 offset:36864
v_add_f32 v246, v236, v237
MM v[68:71],a[220:223],v[208:211],v[128:131]
TR a[140:141],v233 offset:40960
v_cmp_nge_f32 vcc, s71, v246
MM v[68:71],a[216:219],v[192:195],v[68:71]
s_cbranch_vccnz LSITE22_%=
LBACK22_%=:
TR a[142:143],v233 offset:45056
TR a[144:145],v232 offset:33280
MM v[68:71],a[212:215],v[176:179],v[68:71]
s_add_u32 s67, s95, 16384
buffer_store_dwordx4 v[240:243], v248, s[20:23], s67 offen offset:0 sc1
MM v[68:71],a[208:211],v[160:163],v[68:71]
TR a[146:147],v232 offset:37376
TR a[148:149],v232 offset:41472
MM v[76:79],a[208:211],v[164:167],v[132:135]
EX v64,v64
MM v[76:79],a[212:215],v[180:183],v[76:79]
EX v65,v65
MM v[76:79],a[216:219],v[196:199],v[76:79]
TR a[150:151],v232 offset:45568
CV v64,v64,v65
MM v[76:79],a[220:223],v[212:215],v[76:79]
EX v66,v66
MM v[72:75],a[204:207],v[212:215],v[132:135]
EX v67,v67
MM v[72:75],a[200:203],v[196:199],v[72:75]
TR a[152:153],v233 offset:33280
CV v65,v66,v67
MM v[72:75],a[196:199],v[180:183],v[72:75]
EX v68,v68
MM v[72:75],a[192:195],v[164:167],v[72:75]
EX v69,v69
MM v[80:83],a[192:195],v[168:171],v[136:139]
TR a[154:155],v233 offset:37376
CV v66,v68,v69
MM v[80:83],a[196:199],v[184:187],v[80:83]
EX v70,v70
MM v[80:83],a[200:203],v[200:203],v[80:83]
EX v71,v71
MM v[80:83],a[204:207],v[216:219],v[80:83]
TR a[156:157],v233 offset:41472
CV v67,v70,v71
MM v[84:87],a[220:223],v[216:219],v[136:139]
EX v72,v72
MM v[84:87],a[216:219],v[200:203],v[84:87]
TR a[158:159],v233 offset:45568
TR a[160:161],v232 offset:33792
MM v[84:87],a[212:215],v[184:187],v[84:87]
EX v73,v73
MM v[84:87],a[208:211],v[168:171],v[84:87]
TR a[162:163],v232 offset:37888
CV v72,v72,v73
MM v[92:95],a[208:211],v[172:175],v[140:143]
EX v74,v74
MM v[92:95],a[212:215],v[188:191],v[92:95]
TR a[164:165],v232 offset:41984
TR a[166:167],v232 offset:46080
MM v[92:95],a[216:219],v[204:207],v[92:95]
EX v75,v75
MM v[92:95],a[220:223],v[220:223],v[92:95]
TR a[168:169],v233 offset:33792
CV v73,v74,v75
MM v[88:91],a[204:207],v[220:223],v[140:143]
EX v76,v76
MM v[88:91],a[200:203],v[204:207],v[88:91]
EX v77,v77
MM v[88:91],a[196:199],v[188:191],v[88:91]
TR a[170:171],v233 offset:37888
CV v74,v76,v77
MM v[88:91],a[192:195],v[172:175],v[88:91]
EX v78,v78
MM v[96:99],a[224:227],v[160:163],v[128:131]
TR a[172:173],v233 offset:41984
TR a[174:175],v233 offset:46080
MM v[96:99],a[228:231],v[176:179],v[96:99]
EX v79,v79
MM v[96:99],a[232:235],v[192:195],v[96:99]
TR a[176:177],v232 offset:34304
CV v75,v78,v79
MM v[96:99],a[236:239],v[208:211],v[96:99]
EX v80,v80
MM v[100:103],a[252:255],v[208:211],v[128:131]
EX v81,v81
MM v[100:103],a[248:251],v[192:195],v[100:103]
TR a[178:179],v232 offset:38400
CV v80,v80,v81
MM v[100:103],a[244:247],v[176:179],v[100:103]
EX v82,v82
MM v[100:103],a[240:243],v[160:163],v[100:103]
TR a[180:181],v232 offset:42496
TR a[182:183],v232 offset:46592
MM v[108:111],a[240:243],v[164:167],v[132:135]
s_add_u32 s67, s95, 17408
buffer_store_dwordx4 v[240:243], v248, s[20:23], s67 offen offset:0 sc1
MM v[108:111],a[244:247],v[180:183],v[108:111]
EX v83,v83
MM v[108:111],a[248:251],v[196:199],v[108:111]
TR a[184:185],v233 offset:34304
CV v81,v82,v83
MM v[108:111],a[252:255],v[212:215],v[108:111]
EX v84,v84
MM v[104:107],a[236:239],v[212:215],v[132:135]
EX v85,v85
MM v[104:107],a[232:235],v[196:199],v[104:107]
TR a[186:187],v233 offset:38400
CV v82,v84,v85
MM v[104:107],a[228:231],v[180:183],v[104:107]
EX v86,v86
MM v[104:107],a[224:227],v[164:167],v[104:107]
TR a[188:189],v233 offset:42496
TR a[190:191],v233 offset:46592
MM v[112:115],a[224:227],v[168:171],v[136:139]
EX v87,v87
MM v[112:115],a[228:231],v[184:187],v[112:115]
CV v83,v86,v87
MM v[112:115],a[232:235],v[200:203],v[112:115]
EX v88,v88
MM v[112:115],a[236:239],v[216:219],v[112:115]
EX v89,v89
MM v[116:119],a[252:255],v[216:219],v[136:139]
EX v90,v90
MM v[116:119],a[248:251],v[200:203],v[116:119]
EX v91,v91
MM v[116:119],a[244:247],v[184:187],v[116:119]
EX v92,v92
MM v[116:119],a[240:243],v[168:171],v[116:119]
EX v93,v93
MM v[124:127],a[240:243],v[172:175],v[140:143]
EX v94,v94
MM v[124:127],a[244:247],v[188:191],v[124:127]
EX v95,v95
MM v[124:127],a[248:251],v[204:207],v[124:127]
EX v96,v96
MM v[124:127],a[252:255],v[220:223],v[124:127]
EX v97,v97
MM v[120:123],a[236:239],v[220:223],v[140:143]
EX v98,v98
MM v[120:123],a[232:235],v[204:207],v[120:123]
EX v99,v99
MM v[120:123],a[228:231],v[188:191],v[120:123]
EX v100,v100
MM v[120:123],a[224:227],v[172:175],v[120:123]
s_waitcnt vmcnt(17) lgkmcnt(0)
s_barrier
MM a[0:3],a[128:131],v[0:3],a[0:3]
s_lshl_b32 s91, %[swave], 2
CV v88,v88,v89
CV v89,v90,v91
MM a[0:3],a[132:135],v[4:7],a[0:3]
s_add_u32 s91, s91, s48
RD a[192:195],v224 offset:0
CV v90,v92,v93
MM a[8:11],a[132:135],v[12:15],a[8:11]
s_add_u32 s91, s91, 69632
s_add_u32 m0, s65, 32768
CV v91,v94,v95
MM a[8:11],a[128:131],v[8:11],a[8:11]
v_mov_b32 v250, s69
s_mov_b64 exec, 1
buffer_store_dword v250, off, s[20:23], s91 offset:0 sc1
s_mov_b64 exec, -1
MM a[16:19],a[128:131],v[16:19],a[16:19]
buffer_load_dwordx4 v228, s[8:11], s64 offen offset:0 lds
MM a[16:19],a[132:135],v[20:23],a[16:19]
RD a[196:199],v225 offset:0
CV v68,v96,v97
MM a[24:27],a[132:135],v[28:31],a[24:27]
RD a[200:203],v226 offset:0
CV v69,v98,v99
MM a[24:27],a[128:131],v[24:27],a[24:27]
buffer_load_dwordx4 v229, s[8:11], s64 offen offset:1024 lds
MM a[28:31],a[136:139],v[24:27],a[28:31]
RD a[204:207],v227 offset:0
RD a[208:211],v224 offset:4096
MM a[28:31],a[140:143],v[28:31],a[28:31]
RD a[212:215],v225 offset:4096
RD a[216:219],v226 offset:4096
MM a[20:23],a[140:143],v[20:23],a[20:23]
buffer_load_dwordx4 v230, s[8:11], s64 offen offset:2048 lds
MM a[20:23],a[136:139],v[16:19],a[20:23]
EX v101,v101
MM a[12:15],a[136:139],v[8:11],a[12:15]
RD a[220:223],v227 offset:4096
CV v70,v100,v101
MM a[12:15],a[140:143],v[12:15],a[12:15]
EX v102,v102
MM a[4:7],a[140:143],v[4:7],a[4:7]
EX v103,v103
MM a[4:7],a[136:139],v[0:3],a[4:7]
RD a[224:227],v224 offset:8192
CV v71,v102,v103
MM a[32:35],a[144:147],v[0:3],a[32:35]
EX v104,v104
MM a[32:35],a[148:151],v[4:7],a[32:35]
buffer_load_dwordx4 v231, s[8:11], s64 offen offset:3072 lds
MM a[40:43],a[148:151],v[12:15],a[40:43]
s_add_u32 m0, s65, 98304
EX v105,v105
MM a[40:43],a[144:147],v[8:11],a[40:43]
RD a[228:231],v225 offset:8192
CV v76,v104,v105
MM a[48:51],a[144:147],v[16:19],a[48:51]
EX v106,v106
MM a[48:51],a[148:151],v[20:23],a[48:51]
EX v107,v107
MM a[56:59],a[148:151],v[28:31],a[56:59]
RD a[232:235],v226 offset:8192
CV v77,v106,v107
MM a[56:59],a[144:147],v[24:27],a[56:59]
EX v108,v108
MM a[60:63],a[152:155],v[24:27],a[60:63]
buffer_load_dwordx4 v235, s[12:15], s81 offen offset:0 lds
MM a[60:63],a[156:159],v[28:31],a[60:63]
EX v109,v109
MM a[52:55],a[156:159],v[20:23],a[52:55]
RD a[236:239],v227 offset:8192
CV v78,v108,v109
MM a[52:55],a[152:155],v[16:19],a[52:55]
EX v110,v110
MM a[44:47],a[152:155],v[8:11],a[44:47]
EX v111,v111
MM a[44:47],a[156:159],v[12:15],a[44:47]
RD a[240:243],v224 offset:12288
CV v79,v110,v111
MM a[36:39],a[156:159],v[4:7],a[36:39]
buffer_load_dwordx4 v235, s[12:15], s64 offen offset:1024 lds
MM a[36:39],a[152:155],v[0:3],a[36:39]
EX v112,v112
MM a[64:67],a[160:163],v[0:3],a[64:67]
EX v113,v113
MM a[64:67],a[164:167],v[4:7],a[64:67]
RD a[244:247],v225 offset:12288
CV v84,v112,v113
MM a[72:75],a[164:167],v[12:15],a[72:75]
EX v114,v114
MM a[72:75],a[160:163],v[8:11],a[72:75]
EX v115,v115
MM a[80:83],a[160:163],v[16:19],a[80:83]
RD a[248:251],v226 offset:12288
CV v85,v114,v115
MM a[80:83],a[164:167],v[20:23],a[80:83]
buffer_load_dwordx4 v235, s[12:15], s81 offen offset:2048 lds
MM a[88:91],a[164:167],v[28:31],a[88:91]
EX v116,v116
MM a[88:91],a[160:163],v[24:27],a[88:91]
EX v117,v117
MM a[92:95],a[168:171],v[24:27],a[92:95]
RD a[252:255],v227 offset:12288
CV v86,v116,v117
MM a[92:95],a[172:175],v[28:31],a[92:95]
EX v118,v118
MM a[84:87],a[172:175],v[20:23],a[84:87]
buffer_load_dwordx4 v235, s[12:15], s64 offen offset:3072 lds
MM a[84:87],a[168:171],v[16:19],a[84:87]
EX v119,v119
MM a[76:79],a[168:171],v[8:11],a[76:79]
CV v87,v118,v119
MM a[76:79],a[172:175],v[12:15],a[76:79]
EX v120,v120
MM a[68:71],a[172:175],v[4:7],a[68:71]
EX v121,v121
MM a[68:71],a[168:171],v[0:3],a[68:71]
EX v122,v122
MM a[96:99],a[176:179],v[0:3],a[96:99]
EX v123,v123
MM a[96:99],a[180:183],v[4:7],a[96:99]
EX v124,v124
MM a[104:107],a[180:183],v[12:15],a[104:107]
EX v125,v125
MM a[104:107],a[176:179],v[8:11],a[104:107]
EX v126,v126
MM a[112:115],a[176:179],v[16:19],a[112:115]
)ASM"
R"ASM(
EX v127,v127
MM a[112:115],a[180:183],v[20:23],a[112:115]
CV v92,v120,v121
CV v93,v122,v123
MM a[120:123],a[180:183],v[28:31],a[120:123]
CV v94,v124,v125
CV v95,v126,v127
MM a[120:123],a[176:179],v[24:27],a[120:123]
MM a[124:127],a[184:187],v[24:27],a[124:127]
MM a[124:127],a[188:191],v[28:31],a[124:127]
MM a[116:119],a[188:191],v[20:23],a[116:119]
MM a[116:119],a[184:187],v[16:19],a[116:119]
MM a[108:111],a[184:187],v[8:11],a[108:111]
MM a[108:111],a[188:191],v[12:15],a[108:111]
MM a[100:103],a[188:191],v[4:7],a[100:103]
MM a[100:103],a[184:187],v[0:3],a[100:103]
v_smfmac_f32_16x16x64_bf16 v[236:239], v[144:147], v[64:71], v234
v_smfmac_f32_16x16x64_bf16 v[236:239], v[148:151], v[72:79], v234
v_smfmac_f32_16x16x64_bf16 v[236:239], v[152:155], v[80:87], v234
v_smfmac_f32_16x16x64_bf16 v[236:239], v[156:159], v[88:95], v234
s_waitcnt lgkmcnt(0)
MM v[0:3],a[192:195],v[160:163],v[128:131]
s_add_u32 s64, s64, 16384
TR a[128:129],v232 offset:49152
s_add_u32 s81, s64, 896
MM v[0:3],a[196:199],v[176:179],v[0:3]
TR a[130:131],v232 offset:53248
TR a[132:133],v232 offset:57344
MM v[0:3],a[200:203],v[192:195],v[0:3]
TR a[134:135],v232 offset:61440
TR a[136:137],v233 offset:49152
MM v[0:3],a[204:207],v[208:211],v[0:3]
TR a[138:139],v233 offset:53248
v_add_f32 v247, v236, v237
MM v[4:7],a[220:223],v[208:211],v[128:131]
TR a[140:141],v233 offset:57344
v_cmp_nge_f32 vcc, s71, v247
MM v[4:7],a[216:219],v[192:195],v[4:7]
s_cbranch_vccnz LSITE23_%=
LBACK23_%=:
TR a[142:143],v233 offset:61440
TR a[144:145],v232 offset:49664
MM v[4:7],a[212:215],v[176:179],v[4:7]
s_add_u32 s67, s95, 18432
buffer_store_dwordx4 v[240:243], v248, s[20:23], s67 offen offset:0 sc1
MM v[4:7],a[208:211],v[160:163],v[4:7]
TR a[146:147],v232 offset:53760
TR a[148:149],v232 offset:57856
MM v[12:15],a[208:211],v[164:167],v[132:135]
EX v0,v0
MM v[12:15],a[212:215],v[180:183],v[12:15]
EX v1,v1
MM v[12:15],a[216:219],v[196:199],v[12:15]
TR a[150:151],v232 offset:61952
CV v0,v0,v1
MM v[12:15],a[220:223],v[212:215],v[12:15]
EX v2,v2
MM v[8:11],a[204:207],v[212:215],v[132:135]
EX v3,v3
MM v[8:11],a[200:203],v[196:199],v[8:11]
TR a[152:153],v233 offset:49664
CV v1,v2,v3
MM v[8:11],a[196:199],v[180:183],v[8:11]
EX v4,v4
MM v[8:11],a[192:195],v[164:167],v[8:11]
EX v5,v5
MM v[16:19],a[192:195],v[168:171],v[136:139]
TR a[154:155],v233 offset:53760
CV v2,v4,v5
MM v[16:19],a[196:199],v[184:187],v[16:19]
EX v6,v6
MM v[16:19],a[200:203],v[200:203],v[16:19]
EX v7,v7
MM v[16:19],a[204:207],v[216:219],v[16:19]
TR a[156:157],v233 offset:57856
CV v3,v6,v7
MM v[20:23],a[220:223],v[216:219],v[136:139]
EX v8,v8
MM v[20:23],a[216:219],v[200:203],v[20:23]
TR a[158:159],v233 offset:61952
TR a[160:161],v232 offset:50176
MM v[20:23],a[212:215],v[184:187],v[20:23]
EX v9,v9
MM v[20:23],a[208:211],v[168:171],v[20:23]
TR a[162:163],v232 offset:54272
CV v8,v8,v9
MM v[28:31],a[208:211],v[172:175],v[140:143]
s_add_u32 m0, s45, 147456
s_add_u32 s44, s44, 1024
buffer_load_dwordx4 v248, s[24:27], s44 offen offset:0 lds
MM v[28:31],a[212:215],v[188:191],v[28:31]
EX v10,v10
MM v[28:31],a[216:219],v[204:207],v[28:31]
EX v11,v11
MM v[28:31],a[220:223],v[220:223],v[28:31]
TR a[164:165],v232 offset:58368
CV v9,v10,v11
MM v[24:27],a[204:207],v[220:223],v[140:143]
EX v12,v12
MM v[24:27],a[200:203],v[204:207],v[24:27]
TR a[166:167],v232 offset:62464
TR a[168:169],v233 offset:50176
MM v[24:27],a[196:199],v[188:191],v[24:27]
EX v13,v13
MM v[24:27],a[192:195],v[172:175],v[24:27]
TR a[170:171],v233 offset:54272
CV v10,v12,v13
MM v[32:35],a[224:227],v[160:163],v[128:131]
EX v14,v14
MM v[32:35],a[228:231],v[176:179],v[32:35]
EX v15,v15
MM v[32:35],a[232:235],v[192:195],v[32:35]
TR a[172:173],v233 offset:58368
CV v11,v14,v15
MM v[32:35],a[236:239],v[208:211],v[32:35]
TR a[174:175],v233 offset:62464
TR a[176:177],v232 offset:50688
MM v[36:39],a[252:255],v[208:211],v[128:131]
EX v16,v16
MM v[36:39],a[248:251],v[192:195],v[36:39]
EX v17,v17
MM v[36:39],a[244:247],v[176:179],v[36:39]
TR a[178:179],v232 offset:54784
CV v16,v16,v17
MM v[36:39],a[240:243],v[160:163],v[36:39]
EX v18,v18
MM v[44:47],a[240:243],v[164:167],v[132:135]
s_add_u32 s67, s95, 19456
buffer_store_dwordx4 v[240:243], v248, s[20:23], s67 offen offset:0 sc1
MM v[44:47],a[244:247],v[180:183],v[44:47]
EX v19,v19
MM v[44:47],a[248:251],v[196:199],v[44:47]
TR a[180:181],v232 offset:58880
CV v17,v18,v19
MM v[44:47],a[252:255],v[212:215],v[44:47]
EX v20,v20
MM v[40:43],a[236:239],v[212:215],v[132:135]
TR a[182:183],v232 offset:62976
TR a[184:185],v233 offset:50688
MM v[40:43],a[232:235],v[196:199],v[40:43]
EX v21,v21
MM v[40:43],a[228:231],v[180:183],v[40:43]
TR a[186:187],v233 offset:54784
CV v18,v20,v21
MM v[40:43],a[224:227],v[164:167],v[40:43]
EX v22,v22
MM v[48:51],a[224:227],v[168:171],v[136:139]
TR a[188:189],v233 offset:58880
TR a[190:191],v233 offset:62976
MM v[48:51],a[228:231],v[184:187],v[48:51]
EX v23,v23
MM v[48:51],a[232:235],v[200:203],v[48:51]
CV v19,v22,v23
MM v[48:51],a[236:239],v[216:219],v[48:51]
EX v24,v24
MM v[52:55],a[252:255],v[216:219],v[136:139]
EX v25,v25
MM v[52:55],a[248:251],v[200:203],v[52:55]
EX v26,v26
MM v[52:55],a[244:247],v[184:187],v[52:55]
EX v27,v27
MM v[52:55],a[240:243],v[168:171],v[52:55]
EX v28,v28
MM v[60:63],a[240:243],v[172:175],v[140:143]
EX v29,v29
MM v[60:63],a[244:247],v[188:191],v[60:63]
EX v30,v30
MM v[60:63],a[248:251],v[204:207],v[60:63]
EX v31,v31
MM v[60:63],a[252:255],v[220:223],v[60:63]
EX v32,v32
MM v[56:59],a[236:239],v[220:223],v[140:143]
EX v33,v33
MM v[56:59],a[232:235],v[204:207],v[56:59]
EX v34,v34
MM v[56:59],a[228:231],v[188:191],v[56:59]
EX v35,v35
MM v[56:59],a[224:227],v[172:175],v[56:59]
s_waitcnt vmcnt(18) lgkmcnt(0)
s_barrier
MM a[0:3],a[128:131],v[64:67],a[0:3]
s_add_u32 m0, s65, 49152
CV v24,v24,v25
CV v25,v26,v27
MM a[0:3],a[132:135],v[68:71],a[0:3]
RD a[192:195],v224 offset:16384
CV v26,v28,v29
MM a[8:11],a[132:135],v[76:79],a[8:11]
RD a[196:199],v225 offset:16384
CV v27,v30,v31
MM a[8:11],a[128:131],v[72:75],a[8:11]
buffer_load_dwordx4 v228, s[8:11], s64 offen offset:0 lds
MM a[16:19],a[128:131],v[80:83],a[16:19]
RD a[200:203],v226 offset:16384
CV v4,v32,v33
MM a[16:19],a[132:135],v[84:87],a[16:19]
RD a[204:207],v227 offset:16384
CV v5,v34,v35
MM a[24:27],a[132:135],v[92:95],a[24:27]
buffer_load_dwordx4 v229, s[8:11], s64 offen offset:1024 lds
MM a[24:27],a[128:131],v[88:91],a[24:27]
RD a[208:211],v224 offset:20480
RD a[212:215],v225 offset:20480
MM a[28:31],a[136:139],v[88:91],a[28:31]
buffer_load_dwordx4 v230, s[8:11], s64 offen offset:2048 lds
MM a[28:31],a[140:143],v[92:95],a[28:31]
EX v36,v36
MM a[20:23],a[140:143],v[84:87],a[20:23]
EX v37,v37
MM a[20:23],a[136:139],v[80:83],a[20:23]
RD a[216:219],v226 offset:20480
CV v6,v36,v37
MM a[12:15],a[136:139],v[72:75],a[12:15]
EX v38,v38
MM a[12:15],a[140:143],v[76:79],a[12:15]
RD a[220:223],v227 offset:20480
RD a[224:227],v224 offset:24576
MM a[4:7],a[140:143],v[68:71],a[4:7]
EX v39,v39
MM a[4:7],a[136:139],v[64:67],a[4:7]
RD a[228:231],v225 offset:24576
CV v7,v38,v39
MM a[32:35],a[144:147],v[64:67],a[32:35]
EX v40,v40
MM a[32:35],a[148:151],v[68:71],a[32:35]
buffer_load_dwordx4 v231, s[8:11], s64 offen offset:3072 lds
MM a[40:43],a[148:151],v[76:79],a[40:43]
s_add_u32 m0, s65, 114688
EX v41,v41
MM a[40:43],a[144:147],v[72:75],a[40:43]
RD a[232:235],v226 offset:24576
CV v12,v40,v41
MM a[48:51],a[144:147],v[80:83],a[48:51]
EX v42,v42
MM a[48:51],a[148:151],v[84:87],a[48:51]
EX v43,v43
MM a[56:59],a[148:151],v[92:95],a[56:59]
RD a[236:239],v227 offset:24576
CV v13,v42,v43
MM a[56:59],a[144:147],v[88:91],a[56:59]
EX v44,v44
MM a[60:63],a[152:155],v[88:91],a[60:63]
buffer_load_dwordx4 v235, s[12:15], s81 offen offset:0 lds
MM a[60:63],a[156:159],v[92:95],a[60:63]
EX v45,v45
MM a[52:55],a[156:159],v[84:87],a[52:55]
RD a[240:243],v224 offset:28672
CV v14,v44,v45
MM a[52:55],a[152:155],v[80:83],a[52:55]
EX v46,v46
MM a[44:47],a[152:155],v[72:75],a[44:47]
EX v47,v47
MM a[44:47],a[156:159],v[76:79],a[44:47]
RD a[244:247],v225 offset:28672
CV v15,v46,v47
MM a[36:39],a[156:159],v[68:71],a[36:39]
buffer_load_dwordx4 v235, s[12:15], s64 offen offset:1024 lds
MM a[36:39],a[152:155],v[64:67],a[36:39]
EX v48,v48
MM a[64:67],a[160:163],v[64:67],a[64:67]
EX v49,v49
MM a[64:67],a[164:167],v[68:71],a[64:67]
RD a[248:251],v226 offset:28672
CV v20,v48,v49
MM a[72:75],a[164:167],v[76:79],a[72:75]
EX v50,v50
MM a[72:75],a[160:163],v[72:75],a[72:75]
EX v51,v51
MM a[80:83],a[160:163],v[80:83],a[80:83]
LPG15_%=:
s_cbranch_execz LTR2_%=
RD a[252:255],v227 offset:28672
CV v21,v50,v51
MM a[80:83],a[164:167],v[84:87],a[80:83]
buffer_load_dwordx4 v235, s[12:15], s81 offen offset:2048 lds
MM a[88:91],a[164:167],v[92:95],a[88:91]
EX v52,v52
MM a[88:91],a[160:163],v[88:91],a[88:91]
EX v53,v53
MM a[92:95],a[168:171],v[88:91],a[92:95]
EX v54,v54
MM a[92:95],a[172:175],v[92:95],a[92:95]
buffer_load_dwordx4 v235, s[12:15], s64 offen offset:3072 lds
MM a[84:87],a[172:175],v[84:87],a[84:87]
EX v55,v55
MM a[84:87],a[168:171],v[80:83],a[84:87]
EX v56,v56
MM a[76:79],a[168:171],v[72:75],a[76:79]
EX v57,v57
MM a[76:79],a[172:175],v[76:79],a[76:79]
EX v58,v58
MM a[68:71],a[172:175],v[68:71],a[68:71]
EX v59,v59
MM a[68:71],a[168:171],v[64:67],a[68:71]
EX v60,v60
MM a[96:99],a[176:179],v[64:67],a[96:99]
EX v61,v61
MM a[96:99],a[180:183],v[68:71],a[96:99]
EX v62,v62
MM a[104:107],a[180:183],v[76:79],a[104:107]
EX v63,v63
MM a[104:107],a[176:179],v[72:75],a[104:107]
CV v22,v52,v53
CV v23,v54,v55
MM a[112:115],a[176:179],v[80:83],a[112:115]
CV v28,v56,v57
CV v29,v58,v59
MM a[112:115],a[180:183],v[84:87],a[112:115]
CV v30,v60,v61
CV v31,v62,v63
MM a[120:123],a[180:183],v[92:95],a[120:123]
MM a[120:123],a[176:179],v[88:91],a[120:123]
MM a[124:127],a[184:187],v[88:91],a[124:127]
MM a[124:127],a[188:191],v[92:95],a[124:127]
MM a[116:119],a[188:191],v[84:87],a[116:119]
MM a[116:119],a[184:187],v[80:83],a[116:119]
MM a[108:111],a[184:187],v[72:75],a[108:111]
MM a[108:111],a[188:191],v[76:79],a[108:111]
MM a[100:103],a[188:191],v[68:71],a[100:103]
MM a[100:103],a[184:187],v[64:67],a[100:103]
v_smfmac_f32_16x16x64_bf16 v[236:239], v[144:147], v[0:7], v234
v_smfmac_f32_16x16x64_bf16 v[236:239], v[148:151], v[8:15], v234
v_smfmac_f32_16x16x64_bf16 v[236:239], v[152:155], v[16:23], v234
v_smfmac_f32_16x16x64_bf16 v[236:239], v[156:159], v[24:31], v234
s_waitcnt lgkmcnt(0)
MM v[64:67],a[192:195],v[160:163],v[128:131]
s_add_u32 s64, s64, 16384
TR a[128:129],v232 offset:0
s_add_u32 s81, s64, 896
MM v[64:67],a[196:199],v[176:179],v[64:67]
TR a[130:131],v232 offset:4096
TR a[132:133],v232 offset:8192
MM v[64:67],a[200:203],v[192:195],v[64:67]
TR a[134:135],v232 offset:12288
TR a[136:137],v233 offset:0
MM v[64:67],a[204:207],v[208:211],v[64:67]
TR a[138:139],v233 offset:4096
v_add_f32 v246, v236, v237
MM v[68:71],a[220:223],v[208:211],v[128:131]
TR a[140:141],v233 offset:8192
v_cmp_nge_f32 vcc, s71, v246
MM v[68:71],a[216:219],v[192:195],v[68:71]
s_cbranch_vccnz LSITE24_%=
LBACK24_%=:
TR a[142:143],v233 offset:12288
TR a[144:145],v232 offset:512
MM v[68:71],a[212:215],v[176:179],v[68:71]
v_bfe_u32 v242, v248, 6, 3
v_mul_u32_u24 v242, 1280, v242
MM v[68:71],a[208:211],v[160:163],v[68:71]
v_and_b32 v240, 48, v248
v_bfe_u32 v243, v248, 8, 1
MM v[76:79],a[208:211],v[164:167],v[132:135]
v_lshlrev_b32 v243, 5, v243
v_xor_b32 v240, v240, v243
MM v[76:79],a[212:215],v[180:183],v[76:79]
v_add_u32 v240, v240, v242
v_lshrrev_b32 v243, 9, v248
MM v[76:79],a[216:219],v[196:199],v[76:79]
v_mul_u32_u24 v242, 10240, v243
v_add_u32 v241, v240, v242
MM v[76:79],a[220:223],v[212:215],v[76:79]
TR a[146:147],v232 offset:4608
v_lshl_add_u32 v240, v243, 6, v240
MM v[72:75],a[204:207],v[212:215],v[132:135]
s_and_b32 s66, s84, 1
EX v64,v64
MM v[72:75],a[200:203],v[196:199],v[72:75]
s_lshl_b32 s94, s66, 6
TR a[148:149],v232 offset:8704
v_add_u32 v240, s94, v240
MM v[72:75],a[196:199],v[180:183],v[72:75]
s_xor_b32 s66, s66, 1
EX v65,v65
MM v[72:75],a[192:195],v[164:167],v[72:75]
s_lshl_b32 s66, s66, 8
v_add_u32 v241, s66, v241
CV v64,v64,v65
MM v[80:83],a[192:195],v[168:171],v[136:139]
)ASM"
R"ASM(
s_mul_i32 s89, %[swave], 20480
EX v66,v66
MM v[80:83],a[196:199],v[184:187],v[80:83]
s_lshl_b32 s94, %[swave], 10
EX v67,v67
MM v[80:83],a[200:203],v[200:203],v[80:83]
s_add_u32 s94, s94, %[slds]
EX v68,v68
MM v[80:83],a[204:207],v[216:219],v[80:83]
s_xor_b32 s66, s84, 0
EX v69,v69
MM v[84:87],a[220:223],v[216:219],v[136:139]
s_mul_i32 s66, s66, 163840
CV v65,v66,v67
CV v66,v68,v69
MM v[84:87],a[216:219],v[200:203],v[84:87]
s_add_u32 s85, s66, s89
EX v70,v70
MM v[84:87],a[212:215],v[184:187],v[84:87]
s_xor_b32 s66, s84, 1
TR a[150:151],v232 offset:12800
TR a[152:153],v233 offset:512
MM v[84:87],a[208:211],v[168:171],v[84:87]
s_mul_i32 s66, s66, 163840
EX v71,v71
MM v[92:95],a[208:211],v[172:175],v[140:143]
s_add_u32 s86, s66, s89
s_add_u32 m0, s45, 147456
s_add_u32 s44, s44, 1024
buffer_load_dwordx4 v248, s[24:27], s44 offen offset:0 lds
MM v[92:95],a[212:215],v[188:191],v[92:95]
s_xor_b32 s66, s84, 2
EX v72,v72
MM v[92:95],a[216:219],v[204:207],v[92:95]
s_mul_i32 s66, s66, 163840
TR a[154:155],v233 offset:4608
CV v67,v70,v71
MM v[92:95],a[220:223],v[220:223],v[92:95]
s_add_u32 s87, s66, s89
EX v73,v73
MM v[88:91],a[204:207],v[220:223],v[140:143]
s_xor_b32 s66, s84, 3
TR a[156:157],v233 offset:8704
TR a[158:159],v233 offset:12800
MM v[88:91],a[200:203],v[204:207],v[88:91]
s_mul_i32 s66, s66, 163840
EX v74,v74
MM v[88:91],a[196:199],v[188:191],v[88:91]
s_add_u32 s88, s66, s89
EX v75,v75
MM v[88:91],a[192:195],v[172:175],v[88:91]
s_lshl_b32 s89, %[swave], 11
TR a[160:161],v232 offset:1024
CV v72,v72,v73
MM v[96:99],a[224:227],v[160:163],v[128:131]
s_add_u32 s89, s89, %[slds]
EX v76,v76
MM v[96:99],a[228:231],v[176:179],v[96:99]
s_lshr_b32 s96, s49, 2
TR a[162:163],v232 offset:5120
CV v73,v74,v75
MM v[96:99],a[232:235],v[192:195],v[96:99]
s_lshl_b32 s97, s50, 3
EX v77,v77
MM v[96:99],a[236:239],v[208:211],v[96:99]
s_add_u32 s96, s96, s97
TR a[164:165],v232 offset:9216
TR a[166:167],v232 offset:13312
MM v[100:103],a[252:255],v[208:211],v[128:131]
s_lshl_b32 s96, s96, 2
EX v78,v78
MM v[100:103],a[248:251],v[192:195],v[100:103]
s_add_u32 s96, s96, %[swave]
EX v79,v79
MM v[100:103],a[244:247],v[176:179],v[100:103]
s_lshl_b32 s96, s96, 6
TR a[168:169],v233 offset:1024
CV v74,v76,v77
MM v[100:103],a[240:243],v[160:163],v[100:103]
s_add_u32 s96, s96, 0
buffer_load_dword v242, off, s[20:23], s96 sc1
MM v[108:111],a[240:243],v[164:167],v[132:135]
buffer_load_dword v243, off, s[20:23], s19 sc1
MM v[108:111],a[244:247],v[180:183],v[108:111]
EX v80,v80
MM v[108:111],a[248:251],v[196:199],v[108:111]
EX v81,v81
MM v[108:111],a[252:255],v[212:215],v[108:111]
TR a[170:171],v233 offset:5120
TR a[172:173],v233 offset:9216
MM v[104:107],a[236:239],v[212:215],v[132:135]
EX v82,v82
MM v[104:107],a[232:235],v[196:199],v[104:107]
TR a[174:175],v233 offset:13312
CV v75,v78,v79
MM v[104:107],a[228:231],v[180:183],v[104:107]
EX v83,v83
MM v[104:107],a[224:227],v[164:167],v[104:107]
TR a[176:177],v232 offset:1536
CV v80,v80,v81
MM v[112:115],a[224:227],v[168:171],v[136:139]
EX v84,v84
MM v[112:115],a[228:231],v[184:187],v[112:115]
TR a[178:179],v232 offset:5632
TR a[180:181],v232 offset:9728
MM v[112:115],a[232:235],v[200:203],v[112:115]
EX v85,v85
MM v[112:115],a[236:239],v[216:219],v[112:115]
EX v86,v86
MM v[116:119],a[252:255],v[216:219],v[136:139]
TR a[182:183],v232 offset:13824
CV v81,v82,v83
MM v[116:119],a[248:251],v[200:203],v[116:119]
EX v87,v87
MM v[116:119],a[244:247],v[184:187],v[116:119]
TR a[184:185],v233 offset:1536
CV v82,v84,v85
MM v[116:119],a[240:243],v[168:171],v[116:119]
EX v88,v88
MM v[124:127],a[240:243],v[172:175],v[140:143]
TR a[186:187],v233 offset:5632
CV v83,v86,v87
MM v[124:127],a[244:247],v[188:191],v[124:127]
EX v89,v89
MM v[124:127],a[248:251],v[204:207],v[124:127]
TR a[188:189],v233 offset:9728
TR a[190:191],v233 offset:13824
MM v[124:127],a[252:255],v[220:223],v[124:127]
EX v90,v90
MM v[120:123],a[236:239],v[220:223],v[140:143]
EX v91,v91
MM v[120:123],a[232:235],v[204:207],v[120:123]
EX v92,v92
MM v[120:123],a[228:231],v[188:191],v[120:123]
EX v93,v93
MM v[120:123],a[224:227],v[172:175],v[120:123]
s_waitcnt vmcnt(18) lgkmcnt(0)
s_barrier
MM a[0:3],a[128:131],v[0:3],a[0:3]
s_add_u32 m0, s65, 0
CV v88,v88,v89
CV v89,v90,v91
MM a[0:3],a[132:135],v[4:7],a[0:3]
EX v94,v94
MM a[8:11],a[132:135],v[12:15],a[8:11]
RD a[192:195],v224 offset:32768
CV v90,v92,v93
MM a[8:11],a[128:131],v[8:11],a[8:11]
EX v95,v95
MM a[16:19],a[128:131],v[16:19],a[16:19]
RD a[196:199],v225 offset:32768
CV v91,v94,v95
MM a[16:19],a[132:135],v[20:23],a[16:19]
EX v96,v96
MM a[24:27],a[132:135],v[28:31],a[24:27]
buffer_load_dwordx4 v228, s[8:11], s64 offen offset:0 lds
MM a[24:27],a[128:131],v[24:27],a[24:27]
EX v97,v97
MM a[28:31],a[136:139],v[24:27],a[28:31]
EX v98,v98
MM a[28:31],a[140:143],v[28:31],a[28:31]
RD a[200:203],v226 offset:32768
CV v68,v96,v97
MM a[20:23],a[140:143],v[20:23],a[20:23]
EX v99,v99
MM a[20:23],a[136:139],v[16:19],a[20:23]
RD a[204:207],v227 offset:32768
CV v69,v98,v99
MM a[12:15],a[136:139],v[8:11],a[12:15]
EX v100,v100
MM a[12:15],a[140:143],v[12:15],a[12:15]
buffer_load_dwordx4 v229, s[8:11], s64 offen offset:1024 lds
MM a[4:7],a[140:143],v[4:7],a[4:7]
EX v101,v101
MM a[4:7],a[136:139],v[0:3],a[4:7]
RD a[208:211],v224 offset:36864
CV v70,v100,v101
MM a[32:35],a[144:147],v[0:3],a[32:35]
EX v102,v102
MM a[32:35],a[148:151],v[4:7],a[32:35]
EX v103,v103
MM a[40:43],a[148:151],v[12:15],a[40:43]
RD a[212:215],v225 offset:36864
CV v71,v102,v103
MM a[40:43],a[144:147],v[8:11],a[40:43]
EX v104,v104
MM a[48:51],a[144:147],v[16:19],a[48:51]
buffer_load_dwordx4 v230, s[8:11], s64 offen offset:2048 lds
MM a[48:51],a[148:151],v[20:23],a[48:51]
EX v105,v105
MM a[56:59],a[148:151],v[28:31],a[56:59]
RD a[216:219],v226 offset:36864
CV v76,v104,v105
MM a[56:59],a[144:147],v[24:27],a[56:59]
EX v106,v106
MM a[60:63],a[152:155],v[24:27],a[60:63]
EX v107,v107
MM a[60:63],a[156:159],v[28:31],a[60:63]
RD a[220:223],v227 offset:36864
CV v77,v106,v107
MM a[52:55],a[156:159],v[20:23],a[52:55]
EX v108,v108
MM a[52:55],a[152:155],v[16:19],a[52:55]
buffer_load_dwordx4 v231, s[8:11], s64 offen offset:3072 lds
MM a[44:47],a[152:155],v[8:11],a[44:47]
s_add_u32 m0, s65, 65536
EX v109,v109
MM a[44:47],a[156:159],v[12:15],a[44:47]
RD a[224:227],v224 offset:40960
CV v78,v108,v109
MM a[36:39],a[156:159],v[4:7],a[36:39]
EX v110,v110
MM a[36:39],a[152:155],v[0:3],a[36:39]
EX v111,v111
MM a[64:67],a[160:163],v[0:3],a[64:67]
RD a[228:231],v225 offset:40960
CV v79,v110,v111
MM a[64:67],a[164:167],v[4:7],a[64:67]
EX v112,v112
MM a[72:75],a[164:167],v[12:15],a[72:75]
buffer_load_dwordx4 v235, s[12:15], s81 offen offset:0 lds
MM a[72:75],a[160:163],v[8:11],a[72:75]
EX v113,v113
MM a[80:83],a[160:163],v[16:19],a[80:83]
RD a[232:235],v226 offset:40960
CV v84,v112,v113
MM a[80:83],a[164:167],v[20:23],a[80:83]
EX v114,v114
MM a[88:91],a[164:167],v[28:31],a[88:91]
EX v115,v115
MM a[88:91],a[160:163],v[24:27],a[88:91]
RD a[236:239],v227 offset:40960
CV v85,v114,v115
MM a[92:95],a[168:171],v[24:27],a[92:95]
EX v116,v116
MM a[92:95],a[172:175],v[28:31],a[92:95]
buffer_load_dwordx4 v235, s[12:15], s64 offen offset:1024 lds
MM a[84:87],a[172:175],v[20:23],a[84:87]
EX v117,v117
MM a[84:87],a[168:171],v[16:19],a[84:87]
RD a[240:243],v224 offset:45056
CV v86,v116,v117
MM a[76:79],a[168:171],v[8:11],a[76:79]
EX v118,v118
MM a[76:79],a[172:175],v[12:15],a[76:79]
EX v119,v119
MM a[68:71],a[172:175],v[4:7],a[68:71]
RD a[244:247],v225 offset:45056
CV v87,v118,v119
MM a[68:71],a[168:171],v[0:3],a[68:71]
buffer_load_dwordx4 v235, s[12:15], s81 offen offset:2048 lds
MM a[96:99],a[176:179],v[0:3],a[96:99]
EX v120,v120
MM a[96:99],a[180:183],v[4:7],a[96:99]
EX v121,v121
MM a[104:107],a[180:183],v[12:15],a[104:107]
RD a[248:251],v226 offset:45056
CV v92,v120,v121
MM a[104:107],a[176:179],v[8:11],a[104:107]
EX v122,v122
MM a[112:115],a[176:179],v[16:19],a[112:115]
EX v123,v123
MM a[112:115],a[180:183],v[20:23],a[112:115]
RD a[252:255],v227 offset:45056
CV v93,v122,v123
MM a[120:123],a[180:183],v[28:31],a[120:123]
buffer_load_dwordx4 v235, s[12:15], s64 offen offset:3072 lds
MM a[120:123],a[176:179],v[24:27],a[120:123]
EX v124,v124
MM a[124:127],a[184:187],v[24:27],a[124:127]
EX v125,v125
MM a[124:127],a[188:191],v[28:31],a[124:127]
EX v126,v126
MM a[116:119],a[188:191],v[20:23],a[116:119]
EX v127,v127
MM a[116:119],a[184:187],v[16:19],a[116:119]
CV v94,v124,v125
CV v95,v126,v127
MM a[108:111],a[184:187],v[8:11],a[108:111]
MM a[108:111],a[188:191],v[12:15],a[108:111]
MM a[100:103],a[188:191],v[4:7],a[100:103]
MM a[100:103],a[184:187],v[0:3],a[100:103]
v_smfmac_f32_16x16x64_bf16 v[236:239], v[144:147], v[64:71], v234
v_smfmac_f32_16x16x64_bf16 v[236:239], v[148:151], v[72:79], v234
v_smfmac_f32_16x16x64_bf16 v[236:239], v[152:155], v[80:87], v234
v_smfmac_f32_16x16x64_bf16 v[236:239], v[156:159], v[88:95], v234
s_waitcnt lgkmcnt(0)
MM v[0:3],a[192:195],v[160:163],v[128:131]
s_add_u32 s64, s64, 16384
TR a[128:129],v232 offset:16384
s_add_u32 s81, s64, 896
MM v[0:3],a[196:199],v[176:179],v[0:3]
TR a[130:131],v232 offset:20480
TR a[132:133],v232 offset:24576
MM v[0:3],a[200:203],v[192:195],v[0:3]
TR a[134:135],v232 offset:28672
TR a[136:137],v233 offset:16384
MM v[0:3],a[204:207],v[208:211],v[0:3]
TR a[138:139],v233 offset:20480
v_add_f32 v247, v236, v237
MM v[4:7],a[220:223],v[208:211],v[128:131]
TR a[140:141],v233 offset:24576
v_cmp_nge_f32 vcc, s71, v247
MM v[4:7],a[216:219],v[192:195],v[4:7]
s_cbranch_vccnz LSITE25_%=
LBACK25_%=:
TR a[142:143],v233 offset:28672
TR a[144:145],v232 offset:16896
MM v[4:7],a[212:215],v[176:179],v[4:7]
TR a[146:147],v232 offset:20992
TR a[148:149],v232 offset:25088
MM v[4:7],a[208:211],v[160:163],v[4:7]
s_mul_i32 s76, s46, 192
TR a[150:151],v232 offset:29184
TR a[152:153],v233 offset:16896
MM v[12:15],a[208:211],v[164:167],v[132:135]
s_add_u32 s76, s76, 69632
v_lshrrev_b32 v253, 2, v248
v_min_u32 v253, 188, v253
MM v[12:15],a[212:215],v[180:183],v[12:15]
buffer_load_dword v252, v253, s[20:23], s76 offen offset:0 sc1
MM v[12:15],a[216:219],v[196:199],v[12:15]
EX v0,v0
MM v[12:15],a[220:223],v[212:215],v[12:15]
EX v1,v1
MM v[8:11],a[204:207],v[212:215],v[132:135]
EX v2,v2
MM v[8:11],a[200:203],v[196:199],v[8:11]
TR a[154:155],v233 offset:20992
CV v0,v0,v1
MM v[8:11],a[196:199],v[180:183],v[8:11]
EX v3,v3
MM v[8:11],a[192:195],v[164:167],v[8:11]
TR a[156:157],v233 offset:25088
CV v1,v2,v3
MM v[16:19],a[192:195],v[168:171],v[136:139]
EX v4,v4
MM v[16:19],a[196:199],v[184:187],v[16:19]
EX v5,v5
MM v[16:19],a[200:203],v[200:203],v[16:19]
TR a[158:159],v233 offset:29184
CV v2,v4,v5
MM v[16:19],a[204:207],v[216:219],v[16:19]
EX v6,v6
MM v[20:23],a[220:223],v[216:219],v[136:139]
EX v7,v7
MM v[20:23],a[216:219],v[200:203],v[20:23]
TR a[160:161],v232 offset:17408
CV v3,v6,v7
MM v[20:23],a[212:215],v[184:187],v[20:23]
EX v8,v8
MM v[20:23],a[208:211],v[168:171],v[20:23]
EX v9,v9
MM v[28:31],a[208:211],v[172:175],v[140:143]
TR a[162:163],v232 offset:21504
CV v8,v8,v9
MM v[28:31],a[212:215],v[188:191],v[28:31]
EX v10,v10
MM v[28:31],a[216:219],v[204:207],v[28:31]
EX v11,v11
MM v[28:31],a[220:223],v[220:223],v[28:31]
TR a[164:165],v232 offset:25600
CV v9,v10,v11
MM v[24:27],a[204:207],v[220:223],v[140:143]
EX v12,v12
MM v[24:27],a[200:203],v[204:207],v[24:27]
TR a[166:167],v232 offset:29696
TR a[168:169],v233 offset:17408
MM v[24:27],a[196:199],v[188:191],v[24:27]
EX v13,v13
MM v[24:27],a[192:195],v[172:175],v[24:27]
TR a[170:171],v233 offset:21504
CV v10,v12,v13
MM v[32:35],a[224:227],v[160:163],v[128:131]
EX v14,v14
MM v[32:35],a[228:231],v[176:179],v[32:35]
EX v15,v15
MM v[32:35],a[232:235],v[192:195],v[32:35]
TR a[172:173],v233 offset:25600
CV v11,v14,v15
MM v[32:35],a[236:239],v[208:211],v[32:35]
TR a[174:175],v233 offset:29696
TR a[176:177],v232 offset:17920
MM v[36:39],a[252:255],v[208:211],v[128:131]
EX v16,v16
MM v[36:39],a[248:251],v[192:195],v[36:39]
EX v17,v17
MM v[36:39],a[244:247],v[176:179],v[36:39]
TR a[178:179],v232 offset:22016
CV v16,v16,v17
MM v[36:39],a[240:243],v[160:163],v[36:39]
EX v18,v18
MM v[44:47],a[240:243],v[164:167],v[132:135]
)ASM"
R"ASM(
TR a[180:181],v232 offset:26112
TR a[182:183],v232 offset:30208
MM v[44:47],a[244:247],v[180:183],v[44:47]
EX v19,v19
MM v[44:47],a[248:251],v[196:199],v[44:47]
TR a[184:185],v233 offset:17920
CV v17,v18,v19
MM v[44:47],a[252:255],v[212:215],v[44:47]
EX v20,v20
MM v[40:43],a[236:239],v[212:215],v[132:135]
EX v21,v21
MM v[40:43],a[232:235],v[196:199],v[40:43]
TR a[186:187],v233 offset:22016
CV v18,v20,v21
MM v[40:43],a[228:231],v[180:183],v[40:43]
EX v22,v22
MM v[40:43],a[224:227],v[164:167],v[40:43]
TR a[188:189],v233 offset:26112
TR a[190:191],v233 offset:30208
MM v[48:51],a[224:227],v[168:171],v[136:139]
EX v23,v23
MM v[48:51],a[228:231],v[184:187],v[48:51]
CV v19,v22,v23
MM v[48:51],a[232:235],v[200:203],v[48:51]
EX v24,v24
MM v[48:51],a[236:239],v[216:219],v[48:51]
EX v25,v25
MM v[52:55],a[252:255],v[216:219],v[136:139]
EX v26,v26
MM v[52:55],a[248:251],v[200:203],v[52:55]
EX v27,v27
MM v[52:55],a[244:247],v[184:187],v[52:55]
EX v28,v28
MM v[52:55],a[240:243],v[168:171],v[52:55]
EX v29,v29
MM v[60:63],a[240:243],v[172:175],v[140:143]
EX v30,v30
MM v[60:63],a[244:247],v[188:191],v[60:63]
EX v31,v31
MM v[60:63],a[248:251],v[204:207],v[60:63]
EX v32,v32
MM v[60:63],a[252:255],v[220:223],v[60:63]
EX v33,v33
MM v[56:59],a[236:239],v[220:223],v[140:143]
EX v34,v34
MM v[56:59],a[232:235],v[204:207],v[56:59]
EX v35,v35
MM v[56:59],a[228:231],v[188:191],v[56:59]
EX v36,v36
MM v[56:59],a[224:227],v[172:175],v[56:59]
s_waitcnt vmcnt(16) lgkmcnt(0)
s_barrier
MM a[0:3],a[128:131],v[64:67],a[0:3]
s_waitcnt vmcnt(0)
v_cmp_ne_u32 s[90:91], s69, v252
CV v24,v24,v25
MM a[0:3],a[132:135],v[68:71],a[0:3]
s_add_u32 m0, s65, 16384
CV v25,v26,v27
CV v26,v28,v29
MM a[8:11],a[132:135],v[76:79],a[8:11]
s_cmp_lg_u64 s[90:91], 0
s_cbranch_scc1 LRFH_%=
LRFHb_%=:
v_lshrrev_b32 v253, 7, v248
MM a[8:11],a[128:131],v[72:75],a[8:11]
v_mul_u32_u24 v253, 3328, v253
v_add_u32 v253, v253, v248
MM a[16:19],a[128:131],v[80:83],a[16:19]
v_lshrrev_b32 v252, 6, v248
v_mul_u32_u24 v252, 3392, v252
MM a[16:19],a[132:135],v[84:87],a[16:19]
RD a[192:195],v224 offset:49152
v_add_u32 v252, v252, v248
MM a[24:27],a[132:135],v[92:95],a[24:27]
s_add_u32 s76, s76, 21557248
RD a[196:199],v225 offset:49152
CV v27,v30,v31
MM a[24:27],a[128:131],v[88:91],a[24:27]
s_lshl_b32 s90, %[swave], 11
buffer_load_dwordx4 v228, s[8:11], s64 offen offset:0 lds
MM a[28:31],a[136:139],v[88:91],a[28:31]
s_add_u32 s90, s90, %[slds]
s_add_u32 m0, s90, 139264
s_nop 0
buffer_load_dwordx4 v253, s[20:23], s76 offen offset:0 sc1 lds
MM a[28:31],a[140:143],v[92:95],a[28:31]
s_add_u32 m0, s65, 16384
RD a[200:203],v226 offset:49152
CV v4,v32,v33
MM a[20:23],a[140:143],v[84:87],a[20:23]
s_add_u32 s76, s76, 128
RD a[204:207],v227 offset:49152
CV v5,v34,v35
MM a[20:23],a[136:139],v[80:83],a[20:23]
s_add_u32 m0, s90, 140288
s_mov_b32 exec_hi, 0
buffer_load_dwordx4 v252, s[20:23], s76 offen offset:0 sc1 lds
LPG16_%=:
s_cbranch_execz LTR2_%=
s_mov_b64 exec, -1
MM a[12:15],a[136:139],v[72:75],a[12:15]
EX v37,v37
MM a[12:15],a[140:143],v[76:79],a[12:15]
RD a[208:211],v224 offset:53248
CV v6,v36,v37
MM a[4:7],a[140:143],v[68:71],a[4:7]
s_add_u32 m0, s65, 16384
s_nop 0
buffer_load_dwordx4 v229, s[8:11], s64 offen offset:1024 lds
MM a[4:7],a[136:139],v[64:67],a[4:7]
EX v38,v38
MM a[32:35],a[144:147],v[64:67],a[32:35]
EX v39,v39
MM a[32:35],a[148:151],v[68:71],a[32:35]
RD a[212:215],v225 offset:53248
CV v7,v38,v39
MM a[40:43],a[148:151],v[76:79],a[40:43]
buffer_load_dwordx4 v230, s[8:11], s64 offen offset:2048 lds
MM a[40:43],a[144:147],v[72:75],a[40:43]
EX v40,v40
MM a[48:51],a[144:147],v[80:83],a[48:51]
EX v41,v41
MM a[48:51],a[148:151],v[84:87],a[48:51]
RD a[216:219],v226 offset:53248
CV v12,v40,v41
MM a[56:59],a[148:151],v[92:95],a[56:59]
EX v42,v42
MM a[56:59],a[144:147],v[88:91],a[56:59]
EX v43,v43
MM a[60:63],a[152:155],v[88:91],a[60:63]
RD a[220:223],v227 offset:53248
CV v13,v42,v43
MM a[60:63],a[156:159],v[92:95],a[60:63]
buffer_load_dwordx4 v231, s[8:11], s64 offen offset:3072 lds
MM a[52:55],a[156:159],v[84:87],a[52:55]
s_add_u32 m0, s65, 81920
EX v44,v44
MM a[52:55],a[152:155],v[80:83],a[52:55]
EX v45,v45
MM a[44:47],a[152:155],v[72:75],a[44:47]
RD a[224:227],v224 offset:57344
CV v14,v44,v45
MM a[44:47],a[156:159],v[76:79],a[44:47]
EX v46,v46
MM a[36:39],a[156:159],v[68:71],a[36:39]
buffer_load_dwordx4 v235, s[12:15], s81 offen offset:0 lds
MM a[36:39],a[152:155],v[64:67],a[36:39]
EX v47,v47
MM a[64:67],a[160:163],v[64:67],a[64:67]
RD a[228:231],v225 offset:57344
CV v15,v46,v47
MM a[64:67],a[164:167],v[68:71],a[64:67]
EX v48,v48
MM a[72:75],a[164:167],v[76:79],a[72:75]
EX v49,v49
MM a[72:75],a[160:163],v[72:75],a[72:75]
RD a[232:235],v226 offset:57344
CV v20,v48,v49
MM a[80:83],a[160:163],v[80:83],a[80:83]
EX v50,v50
MM a[80:83],a[164:167],v[84:87],a[80:83]
RD a[236:239],v227 offset:57344
RD a[240:243],v224 offset:61440
MM a[88:91],a[164:167],v[92:95],a[88:91]
EX v51,v51
MM a[88:91],a[160:163],v[88:91],a[88:91]
RD a[244:247],v225 offset:61440
CV v21,v50,v51
MM a[92:95],a[168:171],v[88:91],a[92:95]
EX v52,v52
MM a[92:95],a[172:175],v[92:95],a[92:95]
buffer_load_dwordx4 v235, s[12:15], s64 offen offset:1024 lds
MM a[84:87],a[172:175],v[84:87],a[84:87]
EX v53,v53
MM a[84:87],a[168:171],v[80:83],a[84:87]
RD a[248:251],v226 offset:61440
CV v22,v52,v53
MM a[76:79],a[168:171],v[72:75],a[76:79]
EX v54,v54
MM a[76:79],a[172:175],v[76:79],a[76:79]
EX v55,v55
MM a[68:71],a[172:175],v[68:71],a[68:71]
RD a[252:255],v227 offset:61440
CV v23,v54,v55
MM a[68:71],a[168:171],v[64:67],a[68:71]
buffer_load_dwordx4 v235, s[12:15], s81 offen offset:2048 lds
MM a[96:99],a[176:179],v[64:67],a[96:99]
EX v56,v56
MM a[96:99],a[180:183],v[68:71],a[96:99]
EX v57,v57
MM a[104:107],a[180:183],v[76:79],a[104:107]
EX v58,v58
MM a[104:107],a[176:179],v[72:75],a[104:107]
buffer_load_dwordx4 v235, s[12:15], s64 offen offset:3072 lds
MM a[112:115],a[176:179],v[80:83],a[112:115]
EX v59,v59
MM a[112:115],a[180:183],v[84:87],a[112:115]
EX v60,v60
MM a[120:123],a[180:183],v[92:95],a[120:123]
EX v61,v61
MM a[120:123],a[176:179],v[88:91],a[120:123]
EX v62,v62
MM a[124:127],a[184:187],v[88:91],a[124:127]
EX v63,v63
MM a[124:127],a[188:191],v[92:95],a[124:127]
CV v28,v56,v57
CV v29,v58,v59
MM a[116:119],a[188:191],v[84:87],a[116:119]
CV v30,v60,v61
CV v31,v62,v63
MM a[116:119],a[184:187],v[80:83],a[116:119]
MM a[108:111],a[184:187],v[72:75],a[108:111]
MM a[108:111],a[188:191],v[76:79],a[108:111]
MM a[100:103],a[188:191],v[68:71],a[100:103]
MM a[100:103],a[184:187],v[64:67],a[100:103]
v_smfmac_f32_16x16x64_bf16 v[236:239], v[144:147], v[0:7], v234
v_smfmac_f32_16x16x64_bf16 v[236:239], v[148:151], v[8:15], v234
v_smfmac_f32_16x16x64_bf16 v[236:239], v[152:155], v[16:23], v234
v_smfmac_f32_16x16x64_bf16 v[236:239], v[156:159], v[24:31], v234
s_waitcnt lgkmcnt(0)
MM v[64:67],a[192:195],v[160:163],v[128:131]
s_add_u32 s64, s64, 16384
TR a[128:129],v232 offset:32768
s_add_u32 s81, s64, 896
MM v[64:67],a[196:199],v[176:179],v[64:67]
TR a[130:131],v232 offset:36864
TR a[132:133],v232 offset:40960
MM v[64:67],a[200:203],v[192:195],v[64:67]
TR a[134:135],v232 offset:45056
TR a[136:137],v233 offset:32768
MM v[64:67],a[204:207],v[208:211],v[64:67]
TR a[138:139],v233 offset:36864
v_add_f32 v246, v236, v237
MM v[68:71],a[220:223],v[208:211],v[128:131]
TR a[140:141],v233 offset:40960
v_cmp_nge_f32 vcc, s71, v246
MM v[68:71],a[216:219],v[192:195],v[68:71]
s_cbranch_vccnz LSITE26_%=
LBACK26_%=:
TR a[142:143],v233 offset:45056
TR a[144:145],v232 offset:33280
MM v[68:71],a[212:215],v[176:179],v[68:71]
s_add_u32 m0, s94, 131072
s_add_u32 s66, s85, 0
buffer_load_dwordx4 v241, s[24:27], s66 offen offset:0 lds
MM v[68:71],a[208:211],v[160:163],v[68:71]
TR a[146:147],v232 offset:37376
TR a[148:149],v232 offset:41472
MM v[76:79],a[208:211],v[164:167],v[132:135]
EX v64,v64
MM v[76:79],a[212:215],v[180:183],v[76:79]
EX v65,v65
MM v[76:79],a[216:219],v[196:199],v[76:79]
TR a[150:151],v232 offset:45568
CV v64,v64,v65
MM v[76:79],a[220:223],v[212:215],v[76:79]
EX v66,v66
MM v[72:75],a[204:207],v[212:215],v[132:135]
EX v67,v67
MM v[72:75],a[200:203],v[196:199],v[72:75]
TR a[152:153],v233 offset:33280
CV v65,v66,v67
MM v[72:75],a[196:199],v[180:183],v[72:75]
s_add_u32 m0, s94, 135168
s_add_u32 s66, s85, 81920
buffer_load_dwordx4 v241, s[24:27], s66 offen offset:0 lds
MM v[72:75],a[192:195],v[164:167],v[72:75]
EX v68,v68
MM v[80:83],a[192:195],v[168:171],v[136:139]
EX v69,v69
MM v[80:83],a[196:199],v[184:187],v[80:83]
TR a[154:155],v233 offset:37376
CV v66,v68,v69
MM v[80:83],a[200:203],v[200:203],v[80:83]
EX v70,v70
MM v[80:83],a[204:207],v[216:219],v[80:83]
EX v71,v71
MM v[84:87],a[220:223],v[216:219],v[136:139]
TR a[156:157],v233 offset:41472
CV v67,v70,v71
MM v[84:87],a[216:219],v[200:203],v[84:87]
EX v72,v72
MM v[84:87],a[212:215],v[184:187],v[84:87]
s_add_u32 m0, s94, 147456
s_add_u32 s66, s87, 0
buffer_load_dwordx4 v241, s[24:27], s66 offen offset:0 lds
MM v[84:87],a[208:211],v[168:171],v[84:87]
TR a[158:159],v233 offset:45568
TR a[160:161],v232 offset:33792
MM v[92:95],a[208:211],v[172:175],v[140:143]
EX v73,v73
MM v[92:95],a[212:215],v[188:191],v[92:95]
TR a[162:163],v232 offset:37888
CV v72,v72,v73
MM v[92:95],a[216:219],v[204:207],v[92:95]
EX v74,v74
MM v[92:95],a[220:223],v[220:223],v[92:95]
EX v75,v75
MM v[88:91],a[204:207],v[220:223],v[140:143]
TR a[164:165],v232 offset:41984
CV v73,v74,v75
MM v[88:91],a[200:203],v[204:207],v[88:91]
EX v76,v76
MM v[88:91],a[196:199],v[188:191],v[88:91]
TR a[166:167],v232 offset:46080
TR a[168:169],v233 offset:33792
MM v[88:91],a[192:195],v[172:175],v[88:91]
EX v77,v77
MM v[96:99],a[224:227],v[160:163],v[128:131]
TR a[170:171],v233 offset:37888
CV v74,v76,v77
MM v[96:99],a[228:231],v[176:179],v[96:99]
EX v78,v78
MM v[96:99],a[232:235],v[192:195],v[96:99]
EX v79,v79
MM v[96:99],a[236:239],v[208:211],v[96:99]
TR a[172:173],v233 offset:41984
CV v75,v78,v79
MM v[100:103],a[252:255],v[208:211],v[128:131]
EX v80,v80
MM v[100:103],a[248:251],v[192:195],v[100:103]
TR a[174:175],v233 offset:46080
TR a[176:177],v232 offset:34304
MM v[100:103],a[244:247],v[176:179],v[100:103]
EX v81,v81
MM v[100:103],a[240:243],v[160:163],v[100:103]
TR a[178:179],v232 offset:38400
CV v80,v80,v81
MM v[108:111],a[240:243],v[164:167],v[132:135]
EX v82,v82
MM v[108:111],a[244:247],v[180:183],v[108:111]
EX v83,v83
MM v[108:111],a[248:251],v[196:199],v[108:111]
TR a[180:181],v232 offset:42496
CV v81,v82,v83
MM v[108:111],a[252:255],v[212:215],v[108:111]
EX v84,v84
MM v[104:107],a[236:239],v[212:215],v[132:135]
TR a[182:183],v232 offset:46592
TR a[184:185],v233 offset:34304
MM v[104:107],a[232:235],v[196:199],v[104:107]
EX v85,v85
MM v[104:107],a[228:231],v[180:183],v[104:107]
TR a[186:187],v233 offset:38400
CV v82,v84,v85
MM v[104:107],a[224:227],v[164:167],v[104:107]
EX v86,v86
MM v[112:115],a[224:227],v[168:171],v[136:139]
TR a[188:189],v233 offset:42496
TR a[190:191],v233 offset:46592
MM v[112:115],a[228:231],v[184:187],v[112:115]
EX v87,v87
MM v[112:115],a[232:235],v[200:203],v[112:115]
CV v83,v86,v87
MM v[112:115],a[236:239],v[216:219],v[112:115]
EX v88,v88
MM v[116:119],a[252:255],v[216:219],v[136:139]
EX v89,v89
MM v[116:119],a[248:251],v[200:203],v[116:119]
EX v90,v90
MM v[116:119],a[244:247],v[184:187],v[116:119]
EX v91,v91
MM v[116:119],a[240:243],v[168:171],v[116:119]
EX v92,v92
MM v[124:127],a[240:243],v[172:175],v[140:143]
EX v93,v93
MM v[124:127],a[244:247],v[188:191],v[124:127]
EX v94,v94
MM v[124:127],a[248:251],v[204:207],v[124:127]
EX v95,v95
MM v[124:127],a[252:255],v[220:223],v[124:127]
EX v96,v96
MM v[120:123],a[236:239],v[220:223],v[140:143]
EX v97,v97
MM v[120:123],a[232:235],v[204:207],v[120:123]
EX v98,v98
MM v[120:123],a[228:231],v[188:191],v[120:123]
s_getpc_b64 s[98:99]
LTA3_%=:
s_lshl_b32 s73, %[swave], 12
s_add_u32 s73, s73, LPG17_%=-LTA3_%=
s_add_u32 s98, s98, s73
s_addc_u32 s99, s99, 0
s_mov_b64 exec, 0
s_setpc_b64 s[98:99]
LTR3_%=:
s_mov_b64 exec, -1
s_nop 0
CV v88,v88,v89
MM v[120:123],a[224:227],v[172:175],v[120:123]
s_waitcnt lgkmcnt(0)
s_barrier
MM a[0:3],a[128:131],v[0:3],a[0:3]
)ASM"
R"ASM(
s_add_u32 m0, s65, 32768
CV v89,v90,v91
CV v90,v92,v93
MM a[0:3],a[132:135],v[4:7],a[0:3]
RD a[192:195],v224 offset:0
CV v91,v94,v95
MM a[8:11],a[132:135],v[12:15],a[8:11]
buffer_load_dwordx4 v228, s[8:11], s64 offen offset:0 lds
MM a[8:11],a[128:131],v[8:11],a[8:11]
RD a[196:199],v225 offset:0
CV v68,v96,v97
MM a[16:19],a[128:131],v[16:19],a[16:19]
RD a[200:203],v226 offset:0
RD a[204:207],v227 offset:0
MM a[16:19],a[132:135],v[20:23],a[16:19]
buffer_load_dwordx4 v229, s[8:11], s64 offen offset:1024 lds
MM a[24:27],a[132:135],v[28:31],a[24:27]
RD a[208:211],v224 offset:4096
RD a[212:215],v225 offset:4096
MM a[24:27],a[128:131],v[24:27],a[24:27]
EX v99,v99
MM a[28:31],a[136:139],v[24:27],a[28:31]
RD a[216:219],v226 offset:4096
CV v69,v98,v99
MM a[28:31],a[140:143],v[28:31],a[28:31]
EX v100,v100
MM a[20:23],a[140:143],v[20:23],a[20:23]
buffer_load_dwordx4 v230, s[8:11], s64 offen offset:2048 lds
MM a[20:23],a[136:139],v[16:19],a[20:23]
EX v101,v101
MM a[12:15],a[136:139],v[8:11],a[12:15]
RD a[220:223],v227 offset:4096
CV v70,v100,v101
MM a[12:15],a[140:143],v[12:15],a[12:15]
EX v102,v102
MM a[4:7],a[140:143],v[4:7],a[4:7]
EX v103,v103
MM a[4:7],a[136:139],v[0:3],a[4:7]
RD a[224:227],v224 offset:8192
CV v71,v102,v103
MM a[32:35],a[144:147],v[0:3],a[32:35]
EX v104,v104
MM a[32:35],a[148:151],v[4:7],a[32:35]
buffer_load_dwordx4 v231, s[8:11], s64 offen offset:3072 lds
MM a[40:43],a[148:151],v[12:15],a[40:43]
s_add_u32 m0, s65, 98304
EX v105,v105
MM a[40:43],a[144:147],v[8:11],a[40:43]
RD a[228:231],v225 offset:8192
CV v76,v104,v105
MM a[48:51],a[144:147],v[16:19],a[48:51]
EX v106,v106
MM a[48:51],a[148:151],v[20:23],a[48:51]
EX v107,v107
MM a[56:59],a[148:151],v[28:31],a[56:59]
RD a[232:235],v226 offset:8192
CV v77,v106,v107
MM a[56:59],a[144:147],v[24:27],a[56:59]
EX v108,v108
MM a[60:63],a[152:155],v[24:27],a[60:63]
buffer_load_dwordx4 v235, s[12:15], s81 offen offset:0 lds
MM a[60:63],a[156:159],v[28:31],a[60:63]
EX v109,v109
MM a[52:55],a[156:159],v[20:23],a[52:55]
RD a[236:239],v227 offset:8192
CV v78,v108,v109
MM a[52:55],a[152:155],v[16:19],a[52:55]
EX v110,v110
MM a[44:47],a[152:155],v[8:11],a[44:47]
EX v111,v111
MM a[44:47],a[156:159],v[12:15],a[44:47]
RD a[240:243],v224 offset:12288
CV v79,v110,v111
MM a[36:39],a[156:159],v[4:7],a[36:39]
buffer_load_dwordx4 v235, s[12:15], s64 offen offset:1024 lds
MM a[36:39],a[152:155],v[0:3],a[36:39]
EX v112,v112
MM a[64:67],a[160:163],v[0:3],a[64:67]
EX v113,v113
MM a[64:67],a[164:167],v[4:7],a[64:67]
RD a[244:247],v225 offset:12288
CV v84,v112,v113
MM a[72:75],a[164:167],v[12:15],a[72:75]
EX v114,v114
MM a[72:75],a[160:163],v[8:11],a[72:75]
EX v115,v115
MM a[80:83],a[160:163],v[16:19],a[80:83]
RD a[248:251],v226 offset:12288
CV v85,v114,v115
MM a[80:83],a[164:167],v[20:23],a[80:83]
buffer_load_dwordx4 v235, s[12:15], s81 offen offset:2048 lds
MM a[88:91],a[164:167],v[28:31],a[88:91]
EX v116,v116
MM a[88:91],a[160:163],v[24:27],a[88:91]
EX v117,v117
MM a[92:95],a[168:171],v[24:27],a[92:95]
RD a[252:255],v227 offset:12288
CV v86,v116,v117
MM a[92:95],a[172:175],v[28:31],a[92:95]
EX v118,v118
MM a[84:87],a[172:175],v[20:23],a[84:87]
buffer_load_dwordx4 v235, s[12:15], s64 offen offset:3072 lds
MM a[84:87],a[168:171],v[16:19],a[84:87]
EX v119,v119
MM a[76:79],a[168:171],v[8:11],a[76:79]
CV v87,v118,v119
MM a[76:79],a[172:175],v[12:15],a[76:79]
EX v120,v120
MM a[68:71],a[172:175],v[4:7],a[68:71]
EX v121,v121
MM a[68:71],a[168:171],v[0:3],a[68:71]
EX v122,v122
MM a[96:99],a[176:179],v[0:3],a[96:99]
EX v123,v123
MM a[96:99],a[180:183],v[4:7],a[96:99]
EX v124,v124
MM a[104:107],a[180:183],v[12:15],a[104:107]
EX v125,v125
MM a[104:107],a[176:179],v[8:11],a[104:107]
EX v126,v126
MM a[112:115],a[176:179],v[16:19],a[112:115]
EX v127,v127
MM a[112:115],a[180:183],v[20:23],a[112:115]
CV v92,v120,v121
CV v93,v122,v123
MM a[120:123],a[180:183],v[28:31],a[120:123]
CV v94,v124,v125
CV v95,v126,v127
MM a[120:123],a[176:179],v[24:27],a[120:123]
MM a[124:127],a[184:187],v[24:27],a[124:127]
MM a[124:127],a[188:191],v[28:31],a[124:127]
MM a[116:119],a[188:191],v[20:23],a[116:119]
MM a[116:119],a[184:187],v[16:19],a[116:119]
MM a[108:111],a[184:187],v[8:11],a[108:111]
MM a[108:111],a[188:191],v[12:15],a[108:111]
MM a[100:103],a[188:191],v[4:7],a[100:103]
MM a[100:103],a[184:187],v[0:3],a[100:103]
v_smfmac_f32_16x16x64_bf16 v[236:239], v[144:147], v[64:71], v234
v_smfmac_f32_16x16x64_bf16 v[236:239], v[148:151], v[72:79], v234
v_smfmac_f32_16x16x64_bf16 v[236:239], v[152:155], v[80:87], v234
v_smfmac_f32_16x16x64_bf16 v[236:239], v[156:159], v[88:95], v234
s_waitcnt lgkmcnt(0)
MM v[0:3],a[192:195],v[160:163],v[128:131]
s_add_u32 s64, s64, 16384
TR a[128:129],v232 offset:49152
s_add_u32 s81, s64, 896
MM v[0:3],a[196:199],v[176:179],v[0:3]
TR a[130:131],v232 offset:53248
TR a[132:133],v232 offset:57344
MM v[0:3],a[200:203],v[192:195],v[0:3]
TR a[134:135],v232 offset:61440
TR a[136:137],v233 offset:49152
MM v[0:3],a[204:207],v[208:211],v[0:3]
TR a[138:139],v233 offset:53248
v_add_f32 v247, v236, v237
MM v[4:7],a[220:223],v[208:211],v[128:131]
TR a[140:141],v233 offset:57344
v_cmp_nge_f32 vcc, s71, v247
MM v[4:7],a[216:219],v[192:195],v[4:7]
s_cbranch_vccnz LSITE27_%=
LBACK27_%=:
TR a[142:143],v233 offset:61440
TR a[144:145],v232 offset:49664
MM v[4:7],a[212:215],v[176:179],v[4:7]
v_lshrrev_b32 v125, 8, v248
v_lshlrev_b32 v124, 10, v125
MM v[4:7],a[208:211],v[160:163],v[4:7]
v_bfe_u32 v126, v248, 6, 2
v_lshl_add_u32 v124, v126, 7, v124
MM v[12:15],a[208:211],v[164:167],v[132:135]
v_bfe_u32 v126, v248, 4, 2
v_lshl_add_u32 v124, v126, 3, v124
MM v[12:15],a[212:215],v[180:183],v[12:15]
s_lshl_b32 s91, %[swave], 5
EX v0,v0
MM v[12:15],a[216:219],v[196:199],v[12:15]
s_add_u32 s91, s91, %[slds]
EX v1,v1
MM v[12:15],a[220:223],v[212:215],v[12:15]
s_add_u32 s91, s91, 151552
v_add_u32 v124, s91, v124
v_and_b32 v126, 112, v248
MM v[8:11],a[204:207],v[212:215],v[132:135]
s_lshl_b32 s91, %[swave], 11
EX v2,v2
MM v[8:11],a[200:203],v[196:199],v[8:11]
s_add_u32 s91, s91, %[slds]
TR a[146:147],v232 offset:53760
CV v0,v0,v1
MM v[8:11],a[196:199],v[180:183],v[8:11]
s_add_u32 s91, s91, 139264
v_lshl_add_u32 v127, v125, 4, s91
v_lshl_add_u32 v127, v126, 2, v127
MM v[8:11],a[192:195],v[164:167],v[8:11]
TR a[148:149],v232 offset:57856
v_lshl_add_u32 v126, v126, 2, v127
MM v[16:19],a[192:195],v[168:171],v[136:139]
s_waitcnt vmcnt(18)
TR v[96:97],v124 offset:0
TR v[98:99],v124 offset:512
MM v[16:19],a[196:199],v[184:187],v[16:19]
RD v[108:111],v126 offset:0
TR v[100:101],v124 offset:4096
MM v[16:19],a[200:203],v[200:203],v[16:19]
TR v[102:103],v124 offset:4608
RD v[112:115],v126 offset:64
MM v[16:19],a[204:207],v[216:219],v[16:19]
TR v[104:105],v124 offset:8192
TR v[106:107],v124 offset:8704
MM v[20:23],a[220:223],v[216:219],v[136:139]
RD v[116:119],v127 offset:1024
v_bfe_u32 v127, v248, 4, 3
EX v3,v3
MM v[20:23],a[216:219],v[200:203],v[20:23]
v_mul_u32_u24 v127, 2560, v127
v_lshl_add_u32 v127, v125, 4, v127
EX v4,v4
MM v[20:23],a[212:215],v[184:187],v[20:23]
s_lshl_b32 s90, %[swave], 6
EX v5,v5
MM v[20:23],a[208:211],v[168:171],v[20:23]
s_lshl_b32 s91, s47, 8
EX v6,v6
MM v[28:31],a[208:211],v[172:175],v[140:143]
s_add_u32 s90, s90, s91
EX v7,v7
MM v[28:31],a[212:215],v[188:191],v[28:31]
s_mul_i32 s91, s46, 17920
TR a[150:151],v232 offset:61952
CV v1,v2,v3
MM v[28:31],a[216:219],v[204:207],v[28:31]
s_add_u32 s90, s90, s91
EX v8,v8
MM v[28:31],a[220:223],v[220:223],v[28:31]
s_add_u32 s90, s90, 21659648
TR a[152:153],v233 offset:49664
CV v2,v4,v5
MM v[24:27],a[204:207],v[220:223],v[140:143]
EX v9,v9
MM v[24:27],a[200:203],v[204:207],v[24:27]
TR a[154:155],v233 offset:53760
TR a[156:157],v233 offset:57856
MM v[24:27],a[196:199],v[188:191],v[24:27]
EX v10,v10
MM v[24:27],a[192:195],v[172:175],v[24:27]
EX v11,v11
MM v[32:35],a[224:227],v[160:163],v[128:131]
TR a[158:159],v233 offset:61952
CV v3,v6,v7
MM v[32:35],a[228:231],v[176:179],v[32:35]
EX v12,v12
MM v[32:35],a[232:235],v[192:195],v[32:35]
TR a[160:161],v232 offset:50176
CV v8,v8,v9
MM v[32:35],a[236:239],v[208:211],v[32:35]
s_waitcnt lgkmcnt(6)
EX v13,v13
MM v[36:39],a[252:255],v[208:211],v[128:131]
TR a[162:163],v232 offset:54272
TR a[164:165],v232 offset:58368
MM v[120:123],v[96:99],v[108:111],0
MM v[120:123],v[100:103],v[112:115],v[120:123]
MM v[120:123],v[104:107],v[116:119],v[120:123]
MM v[36:39],a[248:251],v[192:195],v[36:39]
EX v14,v14
MM v[36:39],a[244:247],v[176:179],v[36:39]
EX v15,v15
MM v[36:39],a[240:243],v[160:163],v[36:39]
TR a[166:167],v232 offset:62464
CV v9,v10,v11
MM v[44:47],a[240:243],v[164:167],v[132:135]
TR a[168:169],v233 offset:50176
CV v10,v12,v13
MM v[44:47],a[244:247],v[180:183],v[44:47]
EX v16,v16
MM v[44:47],a[248:251],v[196:199],v[44:47]
EX v17,v17
MM v[44:47],a[252:255],v[212:215],v[44:47]
s_mov_b32 exec_lo, 0x00ff00ff
s_mov_b32 exec_hi, 0x00ff00ff
buffer_store_dwordx4 v[120:123], v127, s[20:23], s90 offen offset:0 sc1
s_mov_b64 exec, -1
s_nop 0
MM v[40:43],a[236:239],v[212:215],v[132:135]
TR a[170:171],v233 offset:54272
TR a[172:173],v233 offset:58368
MM v[40:43],a[232:235],v[196:199],v[40:43]
EX v18,v18
MM v[40:43],a[228:231],v[180:183],v[40:43]
EX v19,v19
MM v[40:43],a[224:227],v[164:167],v[40:43]
TR a[174:175],v233 offset:62464
CV v11,v14,v15
MM v[48:51],a[224:227],v[168:171],v[136:139]
EX v20,v20
MM v[48:51],a[228:231],v[184:187],v[48:51]
LPG17_%=:
s_cbranch_execz LPG21_%=
TR a[176:177],v232 offset:50688
TR a[178:179],v232 offset:54784
MM v[48:51],a[232:235],v[200:203],v[48:51]
EX v21,v21
MM v[48:51],a[236:239],v[216:219],v[48:51]
TR a[180:181],v232 offset:58880
CV v16,v16,v17
MM v[52:55],a[252:255],v[216:219],v[136:139]
EX v22,v22
MM v[52:55],a[248:251],v[200:203],v[52:55]
TR a[182:183],v232 offset:62976
CV v17,v18,v19
MM v[52:55],a[244:247],v[184:187],v[52:55]
EX v23,v23
MM v[52:55],a[240:243],v[168:171],v[52:55]
TR a[184:185],v233 offset:50688
CV v18,v20,v21
MM v[60:63],a[240:243],v[172:175],v[140:143]
EX v24,v24
MM v[60:63],a[244:247],v[188:191],v[60:63]
TR a[186:187],v233 offset:54784
TR a[188:189],v233 offset:58880
MM v[60:63],a[248:251],v[204:207],v[60:63]
TR a[190:191],v233 offset:62976
CV v19,v22,v23
MM v[60:63],a[252:255],v[220:223],v[60:63]
EX v25,v25
MM v[56:59],a[236:239],v[220:223],v[140:143]
EX v26,v26
MM v[56:59],a[232:235],v[204:207],v[56:59]
EX v27,v27
MM v[56:59],a[228:231],v[188:191],v[56:59]
EX v28,v28
MM v[56:59],a[224:227],v[172:175],v[56:59]
s_waitcnt vmcnt(16) lgkmcnt(0)
s_barrier
MM a[0:3],a[128:131],v[64:67],a[0:3]
s_add_u32 m0, s65, 49152
CV v24,v24,v25
CV v25,v26,v27
MM a[0:3],a[132:135],v[68:71],a[0:3]
EX v29,v29
MM a[8:11],a[132:135],v[76:79],a[8:11]
EX v30,v30
MM a[8:11],a[128:131],v[72:75],a[8:11]
EX v31,v31
MM a[16:19],a[128:131],v[80:83],a[16:19]
RD a[192:195],v224 offset:16384
CV v26,v28,v29
MM a[16:19],a[132:135],v[84:87],a[16:19]
EX v32,v32
MM a[24:27],a[132:135],v[92:95],a[24:27]
RD a[196:199],v225 offset:16384
CV v27,v30,v31
MM a[24:27],a[128:131],v[88:91],a[24:27]
EX v33,v33
MM a[28:31],a[136:139],v[88:91],a[28:31]
buffer_load_dwordx4 v228, s[8:11], s64 offen offset:0 lds
MM a[28:31],a[140:143],v[92:95],a[28:31]
EX v34,v34
MM a[20:23],a[140:143],v[84:87],a[20:23]
EX v35,v35
MM a[20:23],a[136:139],v[80:83],a[20:23]
RD a[200:203],v226 offset:16384
CV v4,v32,v33
MM a[12:15],a[136:139],v[72:75],a[12:15]
EX v36,v36
MM a[12:15],a[140:143],v[76:79],a[12:15]
RD a[204:207],v227 offset:16384
CV v5,v34,v35
MM a[4:7],a[140:143],v[68:71],a[4:7]
EX v37,v37
MM a[4:7],a[136:139],v[64:67],a[4:7]
buffer_load_dwordx4 v229, s[8:11], s64 offen offset:1024 lds
MM a[32:35],a[144:147],v[64:67],a[32:35]
EX v38,v38
MM a[32:35],a[148:151],v[68:71],a[32:35]
RD a[208:211],v224 offset:20480
CV v6,v36,v37
MM a[40:43],a[148:151],v[76:79],a[40:43]
EX v39,v39
MM a[40:43],a[144:147],v[72:75],a[40:43]
RD a[212:215],v225 offset:20480
CV v7,v38,v39
MM a[48:51],a[144:147],v[80:83],a[48:51]
EX v40,v40
MM a[48:51],a[148:151],v[84:87],a[48:51]
EX v41,v41
MM a[56:59],a[148:151],v[92:95],a[56:59]
buffer_load_dwordx4 v230, s[8:11], s64 offen offset:2048 lds
MM a[56:59],a[144:147],v[88:91],a[56:59]
)ASM"
R"ASM(
EX v42,v42
MM a[60:63],a[152:155],v[88:91],a[60:63]
RD a[216:219],v226 offset:20480
CV v12,v40,v41
MM a[60:63],a[156:159],v[92:95],a[60:63]
EX v43,v43
MM a[52:55],a[156:159],v[84:87],a[52:55]
RD a[220:223],v227 offset:20480
CV v13,v42,v43
MM a[52:55],a[152:155],v[80:83],a[52:55]
EX v44,v44
MM a[44:47],a[152:155],v[72:75],a[44:47]
EX v45,v45
MM a[44:47],a[156:159],v[76:79],a[44:47]
buffer_load_dwordx4 v231, s[8:11], s64 offen offset:3072 lds
MM a[36:39],a[156:159],v[68:71],a[36:39]
s_add_u32 m0, s65, 114688
RD a[224:227],v224 offset:24576
CV v14,v44,v45
MM a[36:39],a[152:155],v[64:67],a[36:39]
EX v46,v46
MM a[64:67],a[160:163],v[64:67],a[64:67]
EX v47,v47
MM a[64:67],a[164:167],v[68:71],a[64:67]
RD a[228:231],v225 offset:24576
CV v15,v46,v47
MM a[72:75],a[164:167],v[76:79],a[72:75]
EX v48,v48
MM a[72:75],a[160:163],v[72:75],a[72:75]
buffer_load_dwordx4 v235, s[12:15], s81 offen offset:0 lds
MM a[80:83],a[160:163],v[80:83],a[80:83]
EX v49,v49
MM a[80:83],a[164:167],v[84:87],a[80:83]
RD a[232:235],v226 offset:24576
CV v20,v48,v49
MM a[88:91],a[164:167],v[92:95],a[88:91]
EX v50,v50
MM a[88:91],a[160:163],v[88:91],a[88:91]
EX v51,v51
MM a[92:95],a[168:171],v[88:91],a[92:95]
RD a[236:239],v227 offset:24576
CV v21,v50,v51
MM a[92:95],a[172:175],v[92:95],a[92:95]
EX v52,v52
MM a[84:87],a[172:175],v[84:87],a[84:87]
buffer_load_dwordx4 v235, s[12:15], s64 offen offset:1024 lds
MM a[84:87],a[168:171],v[80:83],a[84:87]
EX v53,v53
MM a[76:79],a[168:171],v[72:75],a[76:79]
RD a[240:243],v224 offset:28672
CV v22,v52,v53
MM a[76:79],a[172:175],v[76:79],a[76:79]
EX v54,v54
MM a[68:71],a[172:175],v[68:71],a[68:71]
EX v55,v55
MM a[68:71],a[168:171],v[64:67],a[68:71]
RD a[244:247],v225 offset:28672
CV v23,v54,v55
MM a[96:99],a[176:179],v[64:67],a[96:99]
EX v56,v56
MM a[96:99],a[180:183],v[68:71],a[96:99]
buffer_load_dwordx4 v235, s[12:15], s81 offen offset:2048 lds
MM a[104:107],a[180:183],v[76:79],a[104:107]
EX v57,v57
MM a[104:107],a[176:179],v[72:75],a[104:107]
RD a[248:251],v226 offset:28672
CV v28,v56,v57
MM a[112:115],a[176:179],v[80:83],a[112:115]
EX v58,v58
MM a[112:115],a[180:183],v[84:87],a[112:115]
EX v59,v59
MM a[120:123],a[180:183],v[92:95],a[120:123]
RD a[252:255],v227 offset:28672
CV v29,v58,v59
MM a[120:123],a[176:179],v[88:91],a[120:123]
EX v60,v60
MM a[124:127],a[184:187],v[88:91],a[124:127]
buffer_load_dwordx4 v235, s[12:15], s64 offen offset:3072 lds
MM a[124:127],a[188:191],v[92:95],a[124:127]
EX v61,v61
MM a[116:119],a[188:191],v[84:87],a[116:119]
EX v62,v62
MM a[116:119],a[184:187],v[80:83],a[116:119]
EX v63,v63
MM a[108:111],a[184:187],v[72:75],a[108:111]
CV v30,v60,v61
CV v31,v62,v63
MM a[108:111],a[188:191],v[76:79],a[108:111]
MM a[100:103],a[188:191],v[68:71],a[100:103]
MM a[100:103],a[184:187],v[64:67],a[100:103]
v_smfmac_f32_16x16x64_bf16 v[236:239], v[144:147], v[0:7], v234
v_smfmac_f32_16x16x64_bf16 v[236:239], v[148:151], v[8:15], v234
v_smfmac_f32_16x16x64_bf16 v[236:239], v[152:155], v[16:23], v234
v_smfmac_f32_16x16x64_bf16 v[236:239], v[156:159], v[24:31], v234
s_waitcnt lgkmcnt(0)
MM v[64:67],a[192:195],v[160:163],v[128:131]
s_sub_u32 s64, s64, 507904
TR a[128:129],v232 offset:0
s_add_u32 s81, s64, 896
MM v[64:67],a[196:199],v[176:179],v[64:67]
TR a[130:131],v232 offset:4096
TR a[132:133],v232 offset:8192
MM v[64:67],a[200:203],v[192:195],v[64:67]
TR a[134:135],v232 offset:12288
TR a[136:137],v233 offset:0
MM v[64:67],a[204:207],v[208:211],v[64:67]
TR a[138:139],v233 offset:4096
v_add_f32 v246, v236, v237
MM v[68:71],a[220:223],v[208:211],v[128:131]
TR a[140:141],v233 offset:8192
v_cmp_nge_f32 vcc, s71, v246
MM v[68:71],a[216:219],v[192:195],v[68:71]
s_cbranch_vccnz LSITE28_%=
LBACK28_%=:
TR a[142:143],v233 offset:12288
TR a[144:145],v232 offset:512
MM v[68:71],a[212:215],v[176:179],v[68:71]
TR a[146:147],v232 offset:4608
TR a[148:149],v232 offset:8704
MM v[68:71],a[208:211],v[160:163],v[68:71]
TR a[150:151],v232 offset:12800
TR a[152:153],v233 offset:512
MM v[76:79],a[208:211],v[164:167],v[132:135]
EX v64,v64
MM v[76:79],a[212:215],v[180:183],v[76:79]
EX v65,v65
MM v[76:79],a[216:219],v[196:199],v[76:79]
TR a[154:155],v233 offset:4608
CV v64,v64,v65
MM v[76:79],a[220:223],v[212:215],v[76:79]
EX v66,v66
MM v[72:75],a[204:207],v[212:215],v[132:135]
EX v67,v67
MM v[72:75],a[200:203],v[196:199],v[72:75]
TR a[156:157],v233 offset:8704
CV v65,v66,v67
MM v[72:75],a[196:199],v[180:183],v[72:75]
EX v68,v68
MM v[72:75],a[192:195],v[164:167],v[72:75]
EX v69,v69
MM v[80:83],a[192:195],v[168:171],v[136:139]
TR a[158:159],v233 offset:12800
CV v66,v68,v69
MM v[80:83],a[196:199],v[184:187],v[80:83]
EX v70,v70
MM v[80:83],a[200:203],v[200:203],v[80:83]
EX v71,v71
MM v[80:83],a[204:207],v[216:219],v[80:83]
TR a[160:161],v232 offset:1024
CV v67,v70,v71
MM v[84:87],a[220:223],v[216:219],v[136:139]
EX v72,v72
MM v[84:87],a[216:219],v[200:203],v[84:87]
EX v73,v73
MM v[84:87],a[212:215],v[184:187],v[84:87]
TR a[162:163],v232 offset:5120
CV v72,v72,v73
MM v[84:87],a[208:211],v[168:171],v[84:87]
EX v74,v74
MM v[92:95],a[208:211],v[172:175],v[140:143]
TR a[164:165],v232 offset:9216
TR a[166:167],v232 offset:13312
MM v[92:95],a[212:215],v[188:191],v[92:95]
EX v75,v75
MM v[92:95],a[216:219],v[204:207],v[92:95]
TR a[168:169],v233 offset:1024
CV v73,v74,v75
MM v[92:95],a[220:223],v[220:223],v[92:95]
EX v76,v76
MM v[88:91],a[204:207],v[220:223],v[140:143]
EX v77,v77
MM v[88:91],a[200:203],v[204:207],v[88:91]
TR a[170:171],v233 offset:5120
CV v74,v76,v77
MM v[88:91],a[196:199],v[188:191],v[88:91]
EX v78,v78
MM v[88:91],a[192:195],v[172:175],v[88:91]
TR a[172:173],v233 offset:9216
TR a[174:175],v233 offset:13312
MM v[96:99],a[224:227],v[160:163],v[128:131]
EX v79,v79
MM v[96:99],a[228:231],v[176:179],v[96:99]
TR a[176:177],v232 offset:1536
CV v75,v78,v79
MM v[96:99],a[232:235],v[192:195],v[96:99]
EX v80,v80
MM v[96:99],a[236:239],v[208:211],v[96:99]
TR a[178:179],v232 offset:5632
TR a[180:181],v232 offset:9728
MM v[100:103],a[252:255],v[208:211],v[128:131]
EX v81,v81
MM v[100:103],a[248:251],v[192:195],v[100:103]
TR a[182:183],v232 offset:13824
CV v80,v80,v81
MM v[100:103],a[244:247],v[176:179],v[100:103]
EX v82,v82
MM v[100:103],a[240:243],v[160:163],v[100:103]
EX v83,v83
MM v[108:111],a[240:243],v[164:167],v[132:135]
TR a[184:185],v233 offset:1536
CV v81,v82,v83
MM v[108:111],a[244:247],v[180:183],v[108:111]
EX v84,v84
MM v[108:111],a[248:251],v[196:199],v[108:111]
TR a[186:187],v233 offset:5632
TR a[188:189],v233 offset:9728
MM v[108:111],a[252:255],v[212:215],v[108:111]
EX v85,v85
MM v[104:107],a[236:239],v[212:215],v[132:135]
TR a[190:191],v233 offset:13824
CV v82,v84,v85
MM v[104:107],a[232:235],v[196:199],v[104:107]
EX v86,v86
MM v[104:107],a[228:231],v[180:183],v[104:107]
EX v87,v87
MM v[104:107],a[224:227],v[164:167],v[104:107]
CV v83,v86,v87
MM v[112:115],a[224:227],v[168:171],v[136:139]
EX v88,v88
MM v[112:115],a[228:231],v[184:187],v[112:115]
EX v89,v89
MM v[112:115],a[232:235],v[200:203],v[112:115]
EX v90,v90
MM v[112:115],a[236:239],v[216:219],v[112:115]
EX v91,v91
MM v[116:119],a[252:255],v[216:219],v[136:139]
EX v92,v92
MM v[116:119],a[248:251],v[200:203],v[116:119]
EX v93,v93
MM v[116:119],a[244:247],v[184:187],v[116:119]
EX v94,v94
MM v[116:119],a[240:243],v[168:171],v[116:119]
EX v95,v95
MM v[124:127],a[240:243],v[172:175],v[140:143]
EX v96,v96
MM v[124:127],a[244:247],v[188:191],v[124:127]
EX v97,v97
MM v[124:127],a[248:251],v[204:207],v[124:127]
EX v98,v98
MM v[124:127],a[252:255],v[220:223],v[124:127]
EX v99,v99
MM v[120:123],a[236:239],v[220:223],v[140:143]
EX v100,v100
MM v[120:123],a[232:235],v[204:207],v[120:123]
EX v101,v101
MM v[120:123],a[228:231],v[188:191],v[120:123]
EX v102,v102
MM v[120:123],a[224:227],v[172:175],v[120:123]
s_waitcnt vmcnt(13) lgkmcnt(0)
s_barrier
MM a[0:3],a[128:131],v[0:3],a[0:3]
s_add_u32 m0, s65, 0
s_add_u32 m0, s94, 139264
s_add_u32 s66, s86, 0
buffer_load_dwordx4 v241, s[24:27], s66 offen offset:0 lds
MM a[0:3],a[132:135],v[4:7],a[0:3]
CV v88,v88,v89
CV v89,v90,v91
MM a[8:11],a[132:135],v[12:15],a[8:11]
RD a[192:195],v224 offset:32768
CV v90,v92,v93
MM a[8:11],a[128:131],v[8:11],a[8:11]
RD a[196:199],v225 offset:32768
CV v91,v94,v95
MM a[16:19],a[128:131],v[16:19],a[16:19]
s_add_u32 m0, s65, 0
s_nop 0
buffer_load_dwordx4 v228, s[8:11], s64 offen offset:0 lds
MM a[16:19],a[132:135],v[20:23],a[16:19]
RD a[200:203],v226 offset:32768
CV v68,v96,v97
MM a[24:27],a[132:135],v[28:31],a[24:27]
RD a[204:207],v227 offset:32768
CV v69,v98,v99
MM a[24:27],a[128:131],v[24:27],a[24:27]
buffer_load_dwordx4 v229, s[8:11], s64 offen offset:1024 lds
MM a[28:31],a[136:139],v[24:27],a[28:31]
s_add_u32 m0, s94, 143360
s_add_u32 s66, s86, 81920
buffer_load_dwordx4 v241, s[24:27], s66 offen offset:0 lds
MM a[28:31],a[140:143],v[28:31],a[28:31]
RD a[208:211],v224 offset:36864
CV v70,v100,v101
MM a[20:23],a[140:143],v[20:23],a[20:23]
RD a[212:215],v225 offset:36864
RD a[216:219],v226 offset:36864
MM a[20:23],a[136:139],v[16:19],a[20:23]
s_add_u32 m0, s65, 0
s_nop 0
buffer_load_dwordx4 v230, s[8:11], s64 offen offset:2048 lds
MM a[12:15],a[136:139],v[8:11],a[12:15]
RD a[220:223],v227 offset:36864
RD a[224:227],v224 offset:40960
MM a[12:15],a[140:143],v[12:15],a[12:15]
EX v103,v103
MM a[4:7],a[140:143],v[4:7],a[4:7]
RD a[228:231],v225 offset:40960
CV v71,v102,v103
MM a[4:7],a[136:139],v[0:3],a[4:7]
buffer_load_dwordx4 v231, s[8:11], s64 offen offset:3072 lds
MM a[32:35],a[144:147],v[0:3],a[32:35]
s_add_u32 m0, s65, 65536
EX v104,v104
MM a[32:35],a[148:151],v[4:7],a[32:35]
EX v105,v105
MM a[40:43],a[148:151],v[12:15],a[40:43]
RD a[232:235],v226 offset:40960
CV v76,v104,v105
MM a[40:43],a[144:147],v[8:11],a[40:43]
EX v106,v106
MM a[48:51],a[144:147],v[16:19],a[48:51]
buffer_load_dwordx4 v235, s[12:15], s81 offen offset:0 lds
MM a[48:51],a[148:151],v[20:23],a[48:51]
EX v107,v107
MM a[56:59],a[148:151],v[28:31],a[56:59]
RD a[236:239],v227 offset:40960
CV v77,v106,v107
MM a[56:59],a[144:147],v[24:27],a[56:59]
EX v108,v108
MM a[60:63],a[152:155],v[24:27],a[60:63]
EX v109,v109
MM a[60:63],a[156:159],v[28:31],a[60:63]
RD a[240:243],v224 offset:45056
CV v78,v108,v109
MM a[52:55],a[156:159],v[20:23],a[52:55]
EX v110,v110
MM a[52:55],a[152:155],v[16:19],a[52:55]
buffer_load_dwordx4 v235, s[12:15], s64 offen offset:1024 lds
MM a[44:47],a[152:155],v[8:11],a[44:47]
EX v111,v111
MM a[44:47],a[156:159],v[12:15],a[44:47]
RD a[244:247],v225 offset:45056
CV v79,v110,v111
MM a[36:39],a[156:159],v[4:7],a[36:39]
EX v112,v112
MM a[36:39],a[152:155],v[0:3],a[36:39]
EX v113,v113
MM a[64:67],a[160:163],v[0:3],a[64:67]
RD a[248:251],v226 offset:45056
CV v84,v112,v113
MM a[64:67],a[164:167],v[4:7],a[64:67]
EX v114,v114
MM a[72:75],a[164:167],v[12:15],a[72:75]
buffer_load_dwordx4 v235, s[12:15], s81 offen offset:2048 lds
MM a[72:75],a[160:163],v[8:11],a[72:75]
EX v115,v115
MM a[80:83],a[160:163],v[16:19],a[80:83]
RD a[252:255],v227 offset:45056
CV v85,v114,v115
MM a[80:83],a[164:167],v[20:23],a[80:83]
EX v116,v116
MM a[88:91],a[164:167],v[28:31],a[88:91]
EX v117,v117
MM a[88:91],a[160:163],v[24:27],a[88:91]
buffer_load_dwordx4 v235, s[12:15], s64 offen offset:3072 lds
MM a[92:95],a[168:171],v[24:27],a[92:95]
EX v118,v118
MM a[92:95],a[172:175],v[28:31],a[92:95]
EX v119,v119
MM a[84:87],a[172:175],v[20:23],a[84:87]
EX v120,v120
MM a[84:87],a[168:171],v[16:19],a[84:87]
EX v121,v121
MM a[76:79],a[168:171],v[8:11],a[76:79]
EX v122,v122
MM a[76:79],a[172:175],v[12:15],a[76:79]
EX v123,v123
MM a[68:71],a[172:175],v[4:7],a[68:71]
EX v124,v124
MM a[68:71],a[168:171],v[0:3],a[68:71]
EX v125,v125
MM a[96:99],a[176:179],v[0:3],a[96:99]
EX v126,v126
MM a[96:99],a[180:183],v[4:7],a[96:99]
EX v127,v127
MM a[104:107],a[180:183],v[12:15],a[104:107]
CV v86,v116,v117
CV v87,v118,v119
MM a[104:107],a[176:179],v[8:11],a[104:107]
CV v92,v120,v121
CV v93,v122,v123
MM a[112:115],a[176:179],v[16:19],a[112:115]
CV v94,v124,v125
CV v95,v126,v127
MM a[112:115],a[180:183],v[20:23],a[112:115]
MM a[120:123],a[180:183],v[28:31],a[120:123]
MM a[120:123],a[176:179],v[24:27],a[120:123]
MM a[124:127],a[184:187],v[24:27],a[124:127]
)ASM"
R"ASM(
MM a[124:127],a[188:191],v[28:31],a[124:127]
MM a[116:119],a[188:191],v[20:23],a[116:119]
MM a[116:119],a[184:187],v[16:19],a[116:119]
MM a[108:111],a[184:187],v[8:11],a[108:111]
MM a[108:111],a[188:191],v[12:15],a[108:111]
MM a[100:103],a[188:191],v[4:7],a[100:103]
MM a[100:103],a[184:187],v[0:3],a[100:103]
v_smfmac_f32_16x16x64_bf16 v[236:239], v[144:147], v[64:71], v234
v_smfmac_f32_16x16x64_bf16 v[236:239], v[148:151], v[72:79], v234
v_smfmac_f32_16x16x64_bf16 v[236:239], v[152:155], v[80:87], v234
v_smfmac_f32_16x16x64_bf16 v[236:239], v[156:159], v[88:95], v234
s_waitcnt lgkmcnt(0)
MM v[0:3],a[192:195],v[160:163],v[128:131]
s_add_u32 s64, s64, 16384
TR a[128:129],v232 offset:16384
s_add_u32 s81, s64, 896
MM v[0:3],a[196:199],v[176:179],v[0:3]
TR a[130:131],v232 offset:20480
TR a[132:133],v232 offset:24576
MM v[0:3],a[200:203],v[192:195],v[0:3]
TR a[134:135],v232 offset:28672
TR a[136:137],v233 offset:16384
MM v[0:3],a[204:207],v[208:211],v[0:3]
TR a[138:139],v233 offset:20480
v_add_f32 v247, v236, v237
MM v[4:7],a[220:223],v[208:211],v[128:131]
TR a[140:141],v233 offset:24576
v_cmp_nge_f32 vcc, s71, v247
MM v[4:7],a[216:219],v[192:195],v[4:7]
s_cbranch_vccnz LSITE29_%=
LBACK29_%=:
TR a[142:143],v233 offset:28672
TR a[144:145],v232 offset:16896
MM v[4:7],a[212:215],v[176:179],v[4:7]
s_add_u32 m0, s94, 151552
s_add_u32 s66, s87, 81920
buffer_load_dwordx4 v241, s[24:27], s66 offen offset:0 lds
MM v[4:7],a[208:211],v[160:163],v[4:7]
v_bfe_u32 v96, v248, 6, 2
v_mul_u32_u24 v96, 1280, v96
MM v[12:15],a[208:211],v[164:167],v[132:135]
v_bfe_u32 v97, v248, 8, 1
v_mul_u32_u24 v97, 40960, v97
MM v[12:15],a[212:215],v[180:183],v[12:15]
v_add_u32 v96, v96, v97
v_and_b32 v97, 48, v248
MM v[12:15],a[216:219],v[196:199],v[12:15]
v_mul_u32_u24 v97, 6, v97
v_add_u32 v96, v96, v97
MM v[12:15],a[220:223],v[212:215],v[12:15]
s_min_u32 s60, s18, s63
EX v0,v0
MM v[8:11],a[204:207],v[212:215],v[132:135]
s_add_u32 s61, s60, 131072
s_mov_b32 exec_hi, 0
buffer_load_dword v100, v96, s[32:35], s60 offen offset:0
s_mov_b64 exec, -1
MM v[8:11],a[200:203],v[196:199],v[8:11]
EX v1,v1
MM v[8:11],a[196:199],v[180:183],v[8:11]
s_add_u32 m0, s94, 155648
s_add_u32 s66, s88, 0
buffer_load_dwordx4 v241, s[24:27], s66 offen offset:0 lds
MM v[8:11],a[192:195],v[164:167],v[8:11]
s_mov_b32 exec_hi, 0
buffer_load_dword v101, v96, s[20:23], s61 offen offset:0 sc1
s_mov_b64 exec, -1
MM v[16:19],a[192:195],v[168:171],v[136:139]
EX v2,v2
MM v[16:19],a[196:199],v[184:187],v[16:19]
EX v3,v3
MM v[16:19],a[200:203],v[200:203],v[16:19]
EX v4,v4
MM v[16:19],a[204:207],v[216:219],v[16:19]
TR a[146:147],v232 offset:20992
CV v0,v0,v1
MM v[20:23],a[220:223],v[216:219],v[136:139]
EX v5,v5
MM v[20:23],a[216:219],v[200:203],v[20:23]
TR a[148:149],v232 offset:25088
CV v1,v2,v3
MM v[20:23],a[212:215],v[184:187],v[20:23]
s_add_u32 m0, s94, 159744
s_add_u32 s66, s88, 81920
buffer_load_dwordx4 v241, s[24:27], s66 offen offset:0 lds
MM v[20:23],a[208:211],v[168:171],v[20:23]
EX v6,v6
MM v[28:31],a[208:211],v[172:175],v[140:143]
EX v7,v7
MM v[28:31],a[212:215],v[188:191],v[28:31]
TR a[150:151],v232 offset:29184
TR a[152:153],v233 offset:16896
MM v[28:31],a[216:219],v[204:207],v[28:31]
EX v8,v8
MM v[28:31],a[220:223],v[220:223],v[28:31]
EX v9,v9
MM v[24:27],a[204:207],v[220:223],v[140:143]
TR a[154:155],v233 offset:20992
CV v2,v4,v5
MM v[24:27],a[200:203],v[204:207],v[24:27]
EX v10,v10
MM v[24:27],a[196:199],v[188:191],v[24:27]
TR a[156:157],v233 offset:25088
CV v3,v6,v7
MM v[24:27],a[192:195],v[172:175],v[24:27]
EX v11,v11
MM v[32:35],a[224:227],v[160:163],v[128:131]
TR a[158:159],v233 offset:29184
LPG18_%=:
s_cbranch_execz LPG22_%=
TR a[160:161],v232 offset:17408
MM v[32:35],a[228:231],v[176:179],v[32:35]
EX v12,v12
MM v[32:35],a[232:235],v[192:195],v[32:35]
TR a[162:163],v232 offset:21504
CV v8,v8,v9
MM v[32:35],a[236:239],v[208:211],v[32:35]
EX v13,v13
MM v[36:39],a[252:255],v[208:211],v[128:131]
TR a[164:165],v232 offset:25600
CV v9,v10,v11
MM v[36:39],a[248:251],v[192:195],v[36:39]
EX v14,v14
MM v[36:39],a[244:247],v[176:179],v[36:39]
EX v15,v15
MM v[36:39],a[240:243],v[160:163],v[36:39]
TR a[166:167],v232 offset:29696
TR a[168:169],v233 offset:17408
MM v[44:47],a[240:243],v[164:167],v[132:135]
EX v16,v16
MM v[44:47],a[244:247],v[180:183],v[44:47]
TR a[170:171],v233 offset:21504
CV v10,v12,v13
MM v[44:47],a[248:251],v[196:199],v[44:47]
EX v17,v17
MM v[44:47],a[252:255],v[212:215],v[44:47]
TR a[172:173],v233 offset:25600
CV v11,v14,v15
MM v[40:43],a[236:239],v[212:215],v[132:135]
EX v18,v18
MM v[40:43],a[232:235],v[196:199],v[40:43]
TR a[174:175],v233 offset:29696
TR a[176:177],v232 offset:17920
MM v[40:43],a[228:231],v[180:183],v[40:43]
EX v19,v19
MM v[40:43],a[224:227],v[164:167],v[40:43]
EX v20,v20
MM v[48:51],a[224:227],v[168:171],v[136:139]
TR a[178:179],v232 offset:22016
CV v16,v16,v17
MM v[48:51],a[228:231],v[184:187],v[48:51]
EX v21,v21
MM v[48:51],a[232:235],v[200:203],v[48:51]
TR a[180:181],v232 offset:26112
CV v17,v18,v19
MM v[48:51],a[236:239],v[216:219],v[48:51]
EX v22,v22
MM v[52:55],a[252:255],v[216:219],v[136:139]
TR a[182:183],v232 offset:30208
CV v18,v20,v21
MM v[52:55],a[248:251],v[200:203],v[52:55]
EX v23,v23
MM v[52:55],a[244:247],v[184:187],v[52:55]
TR a[184:185],v233 offset:17920
TR a[186:187],v233 offset:22016
MM v[52:55],a[240:243],v[168:171],v[52:55]
EX v24,v24
MM v[60:63],a[240:243],v[172:175],v[140:143]
EX v25,v25
MM v[60:63],a[244:247],v[188:191],v[60:63]
TR a[188:189],v233 offset:26112
CV v19,v22,v23
MM v[60:63],a[248:251],v[204:207],v[60:63]
TR a[190:191],v233 offset:30208
CV v24,v24,v25
MM v[60:63],a[252:255],v[220:223],v[60:63]
EX v26,v26
MM v[56:59],a[236:239],v[220:223],v[140:143]
EX v27,v27
MM v[56:59],a[232:235],v[204:207],v[56:59]
EX v28,v28
MM v[56:59],a[228:231],v[188:191],v[56:59]
EX v29,v29
MM v[56:59],a[224:227],v[172:175],v[56:59]
s_waitcnt vmcnt(19) lgkmcnt(0)
s_barrier
MM a[0:3],a[128:131],v[64:67],a[0:3]
CV v25,v26,v27
CV v26,v28,v29
MM a[0:3],a[132:135],v[68:71],a[0:3]
s_lshl_b32 s91, s47, 2
EX v30,v30
MM a[8:11],a[132:135],v[76:79],a[8:11]
s_add_u32 s91, s91, %[swave]
EX v31,v31
MM a[8:11],a[128:131],v[72:75],a[8:11]
s_lshl_b32 s91, s91, 2
RD a[192:195],v224 offset:49152
CV v27,v30,v31
MM a[16:19],a[128:131],v[80:83],a[16:19]
s_add_u32 s91, s91, 73728
s_add_u32 m0, s65, 16384
RD a[196:199],v225 offset:49152
MM a[16:19],a[132:135],v[84:87],a[16:19]
v_mov_b32 v250, s69
s_mov_b64 exec, 1
buffer_store_dword v250, off, s[20:23], s91 offset:0 sc1
s_mov_b64 exec, -1
MM a[24:27],a[132:135],v[92:95],a[24:27]
s_lshr_b32 s77, %[sb], 3
EX v32,v32
MM a[24:27],a[128:131],v[88:91],a[24:27]
s_lshl_b32 s77, s77, 2
EX v33,v33
MM a[28:31],a[136:139],v[88:91],a[28:31]
s_add_u32 s77, s77, %[swave]
buffer_load_dwordx4 v228, s[8:11], s64 offen offset:0 lds
MM a[28:31],a[140:143],v[92:95],a[28:31]
s_lshl_b32 s77, s77, 9
EX v34,v34
MM a[20:23],a[140:143],v[84:87],a[20:23]
s_add_u32 s77, s77, 0
v_lshrrev_b32 v253, 1, v248
v_add_u32 v253, s77, v253
MM a[20:23],a[136:139],v[80:83],a[20:23]
s_mov_b32 s77, LKEND_ka-LPD2_%=-16
v_min_u32 v253, s77, v253
CV v4,v32,v33
MM a[12:15],a[136:139],v[72:75],a[12:15]
s_getpc_b64 s[98:99]
LPD2_%=:
global_load_dword v253, v253, s[98:99]
MM a[12:15],a[140:143],v[76:79],a[12:15]
EX v35,v35
MM a[4:7],a[140:143],v[68:71],a[4:7]
EX v36,v36
MM a[4:7],a[136:139],v[64:67],a[4:7]
EX v37,v37
MM a[32:35],a[144:147],v[64:67],a[32:35]
RD a[200:203],v226 offset:49152
CV v5,v34,v35
MM a[32:35],a[148:151],v[68:71],a[32:35]
EX v38,v38
MM a[40:43],a[148:151],v[76:79],a[40:43]
buffer_load_dwordx4 v229, s[8:11], s64 offen offset:1024 lds
MM a[40:43],a[144:147],v[72:75],a[40:43]
EX v39,v39
MM a[48:51],a[144:147],v[80:83],a[48:51]
RD a[204:207],v227 offset:49152
CV v6,v36,v37
MM a[48:51],a[148:151],v[84:87],a[48:51]
EX v40,v40
MM a[56:59],a[148:151],v[92:95],a[56:59]
EX v41,v41
MM a[56:59],a[144:147],v[88:91],a[56:59]
RD a[208:211],v224 offset:53248
CV v7,v38,v39
MM a[60:63],a[152:155],v[88:91],a[60:63]
EX v42,v42
MM a[60:63],a[156:159],v[92:95],a[60:63]
buffer_load_dwordx4 v230, s[8:11], s64 offen offset:2048 lds
MM a[52:55],a[156:159],v[84:87],a[52:55]
EX v43,v43
MM a[52:55],a[152:155],v[80:83],a[52:55]
RD a[212:215],v225 offset:53248
CV v12,v40,v41
MM a[44:47],a[152:155],v[72:75],a[44:47]
EX v44,v44
MM a[44:47],a[156:159],v[76:79],a[44:47]
EX v45,v45
MM a[36:39],a[156:159],v[68:71],a[36:39]
RD a[216:219],v226 offset:53248
CV v13,v42,v43
MM a[36:39],a[152:155],v[64:67],a[36:39]
EX v46,v46
MM a[64:67],a[160:163],v[64:67],a[64:67]
buffer_load_dwordx4 v231, s[8:11], s64 offen offset:3072 lds
MM a[64:67],a[164:167],v[68:71],a[64:67]
s_add_u32 m0, s65, 81920
EX v47,v47
MM a[72:75],a[164:167],v[76:79],a[72:75]
RD a[220:223],v227 offset:53248
CV v14,v44,v45
MM a[72:75],a[160:163],v[72:75],a[72:75]
EX v48,v48
MM a[80:83],a[160:163],v[80:83],a[80:83]
RD a[224:227],v224 offset:57344
CV v15,v46,v47
MM a[80:83],a[164:167],v[84:87],a[80:83]
EX v49,v49
MM a[88:91],a[164:167],v[92:95],a[88:91]
EX v50,v50
MM a[88:91],a[160:163],v[88:91],a[88:91]
buffer_load_dwordx4 v235, s[12:15], s81 offen offset:0 lds
MM a[92:95],a[168:171],v[88:91],a[92:95]
EX v51,v51
MM a[92:95],a[172:175],v[92:95],a[92:95]
RD a[228:231],v225 offset:57344
CV v20,v48,v49
MM a[84:87],a[172:175],v[84:87],a[84:87]
EX v52,v52
MM a[84:87],a[168:171],v[80:83],a[84:87]
RD a[232:235],v226 offset:57344
CV v21,v50,v51
MM a[76:79],a[168:171],v[72:75],a[76:79]
EX v53,v53
MM a[76:79],a[172:175],v[76:79],a[76:79]
RD a[236:239],v227 offset:57344
CV v22,v52,v53
MM a[68:71],a[172:175],v[68:71],a[68:71]
EX v54,v54
MM a[68:71],a[168:171],v[64:67],a[68:71]
buffer_load_dwordx4 v235, s[12:15], s64 offen offset:1024 lds
MM a[96:99],a[176:179],v[64:67],a[96:99]
EX v55,v55
MM a[96:99],a[180:183],v[68:71],a[96:99]
EX v56,v56
MM a[104:107],a[180:183],v[76:79],a[104:107]
RD a[240:243],v224 offset:61440
CV v23,v54,v55
MM a[104:107],a[176:179],v[72:75],a[104:107]
EX v57,v57
MM a[112:115],a[176:179],v[80:83],a[112:115]
RD a[244:247],v225 offset:61440
CV v28,v56,v57
MM a[112:115],a[180:183],v[84:87],a[112:115]
EX v58,v58
MM a[120:123],a[180:183],v[92:95],a[120:123]
s_waitcnt vmcnt(9)
buffer_load_dwordx4 v235, s[12:15], s81 offen offset:2048 lds
MM a[120:123],a[176:179],v[88:91],a[120:123]
EX v59,v59
MM a[124:127],a[184:187],v[88:91],a[124:127]
RD a[248:251],v226 offset:61440
CV v29,v58,v59
MM a[124:127],a[188:191],v[92:95],a[124:127]
EX v60,v60
MM a[116:119],a[188:191],v[84:87],a[116:119]
EX v61,v61
MM a[116:119],a[184:187],v[80:83],a[116:119]
RD a[252:255],v227 offset:61440
CV v30,v60,v61
MM a[108:111],a[184:187],v[72:75],a[108:111]
EX v62,v62
MM a[108:111],a[188:191],v[76:79],a[108:111]
buffer_load_dwordx4 v235, s[12:15], s64 offen offset:3072 lds
MM a[100:103],a[188:191],v[68:71],a[100:103]
EX v63,v63
MM a[100:103],a[184:187],v[64:67],a[100:103]
CV v31,v62,v63
v_smfmac_f32_16x16x64_bf16 v[236:239], v[144:147], v[0:7], v234
v_smfmac_f32_16x16x64_bf16 v[236:239], v[148:151], v[8:15], v234
v_smfmac_f32_16x16x64_bf16 v[236:239], v[152:155], v[16:23], v234
v_smfmac_f32_16x16x64_bf16 v[236:239], v[156:159], v[24:31], v234
s_waitcnt lgkmcnt(0)
MM v[64:67],a[192:195],v[160:163],v[128:131]
s_add_u32 s64, s64, 16384
TR a[128:129],v232 offset:32768
s_add_u32 s81, s64, 896
MM v[64:67],a[196:199],v[176:179],v[64:67]
TR a[130:131],v232 offset:36864
TR a[132:133],v232 offset:40960
MM v[64:67],a[200:203],v[192:195],v[64:67]
TR a[134:135],v232 offset:45056
TR a[136:137],v233 offset:32768
MM v[64:67],a[204:207],v[208:211],v[64:67]
TR a[138:139],v233 offset:36864
v_add_f32 v246, v236, v237
MM v[68:71],a[220:223],v[208:211],v[128:131]
TR a[140:141],v233 offset:40960
v_cmp_nge_f32 vcc, s71, v246
MM v[68:71],a[216:219],v[192:195],v[68:71]
s_cbranch_vccnz LSITE30_%=
LBACK30_%=:
TR a[142:143],v233 offset:45056
TR a[144:145],v232 offset:33280
MM v[68:71],a[212:215],v[176:179],v[68:71]
TR a[146:147],v232 offset:37376
TR a[148:149],v232 offset:41472
MM v[68:71],a[208:211],v[160:163],v[68:71]
TR a[150:151],v232 offset:45568
TR a[152:153],v233 offset:33280
MM v[76:79],a[208:211],v[164:167],v[132:135]
)ASM"
R"ASM(
EX v64,v64
MM v[76:79],a[212:215],v[180:183],v[76:79]
EX v65,v65
MM v[76:79],a[216:219],v[196:199],v[76:79]
TR a[154:155],v233 offset:37376
CV v64,v64,v65
MM v[76:79],a[220:223],v[212:215],v[76:79]
EX v66,v66
MM v[72:75],a[204:207],v[212:215],v[132:135]
EX v67,v67
MM v[72:75],a[200:203],v[196:199],v[72:75]
TR a[156:157],v233 offset:41472
CV v65,v66,v67
MM v[72:75],a[196:199],v[180:183],v[72:75]
EX v68,v68
MM v[72:75],a[192:195],v[164:167],v[72:75]
EX v69,v69
MM v[80:83],a[192:195],v[168:171],v[136:139]
TR a[158:159],v233 offset:45568
CV v66,v68,v69
MM v[80:83],a[196:199],v[184:187],v[80:83]
EX v70,v70
MM v[80:83],a[200:203],v[200:203],v[80:83]
EX v71,v71
MM v[80:83],a[204:207],v[216:219],v[80:83]
TR a[160:161],v232 offset:33792
CV v67,v70,v71
MM v[84:87],a[220:223],v[216:219],v[136:139]
EX v72,v72
MM v[84:87],a[216:219],v[200:203],v[84:87]
EX v73,v73
MM v[84:87],a[212:215],v[184:187],v[84:87]
TR a[162:163],v232 offset:37888
CV v72,v72,v73
MM v[84:87],a[208:211],v[168:171],v[84:87]
EX v74,v74
MM v[92:95],a[208:211],v[172:175],v[140:143]
TR a[164:165],v232 offset:41984
TR a[166:167],v232 offset:46080
MM v[92:95],a[212:215],v[188:191],v[92:95]
EX v75,v75
MM v[92:95],a[216:219],v[204:207],v[92:95]
TR a[168:169],v233 offset:33792
CV v73,v74,v75
MM v[92:95],a[220:223],v[220:223],v[92:95]
EX v76,v76
MM v[88:91],a[204:207],v[220:223],v[140:143]
EX v77,v77
MM v[88:91],a[200:203],v[204:207],v[88:91]
TR a[170:171],v233 offset:37888
CV v74,v76,v77
MM v[88:91],a[196:199],v[188:191],v[88:91]
EX v78,v78
MM v[88:91],a[192:195],v[172:175],v[88:91]
TR a[172:173],v233 offset:41984
TR a[174:175],v233 offset:46080
MM v[96:99],a[224:227],v[160:163],v[128:131]
EX v79,v79
MM v[96:99],a[228:231],v[176:179],v[96:99]
TR a[176:177],v232 offset:34304
CV v75,v78,v79
MM v[96:99],a[232:235],v[192:195],v[96:99]
EX v80,v80
MM v[96:99],a[236:239],v[208:211],v[96:99]
TR a[178:179],v232 offset:38400
TR a[180:181],v232 offset:42496
MM v[100:103],a[252:255],v[208:211],v[128:131]
EX v81,v81
MM v[100:103],a[248:251],v[192:195],v[100:103]
TR a[182:183],v232 offset:46592
CV v80,v80,v81
MM v[100:103],a[244:247],v[176:179],v[100:103]
EX v82,v82
MM v[100:103],a[240:243],v[160:163],v[100:103]
EX v83,v83
MM v[108:111],a[240:243],v[164:167],v[132:135]
TR a[184:185],v233 offset:34304
CV v81,v82,v83
MM v[108:111],a[244:247],v[180:183],v[108:111]
EX v84,v84
MM v[108:111],a[248:251],v[196:199],v[108:111]
TR a[186:187],v233 offset:38400
TR a[188:189],v233 offset:42496
MM v[108:111],a[252:255],v[212:215],v[108:111]
EX v85,v85
MM v[104:107],a[236:239],v[212:215],v[132:135]
TR a[190:191],v233 offset:46592
CV v82,v84,v85
MM v[104:107],a[232:235],v[196:199],v[104:107]
EX v86,v86
MM v[104:107],a[228:231],v[180:183],v[104:107]
EX v87,v87
MM v[104:107],a[224:227],v[164:167],v[104:107]
CV v83,v86,v87
MM v[112:115],a[224:227],v[168:171],v[136:139]
EX v88,v88
MM v[112:115],a[228:231],v[184:187],v[112:115]
EX v89,v89
MM v[112:115],a[232:235],v[200:203],v[112:115]
EX v90,v90
MM v[112:115],a[236:239],v[216:219],v[112:115]
EX v91,v91
MM v[116:119],a[252:255],v[216:219],v[136:139]
EX v92,v92
MM v[116:119],a[248:251],v[200:203],v[116:119]
EX v93,v93
MM v[116:119],a[244:247],v[184:187],v[116:119]
EX v94,v94
MM v[116:119],a[240:243],v[168:171],v[116:119]
EX v95,v95
MM v[124:127],a[240:243],v[172:175],v[140:143]
EX v96,v96
MM v[124:127],a[244:247],v[188:191],v[124:127]
EX v97,v97
MM v[124:127],a[248:251],v[204:207],v[124:127]
EX v98,v98
MM v[124:127],a[252:255],v[220:223],v[124:127]
EX v99,v99
MM v[120:123],a[236:239],v[220:223],v[140:143]
EX v100,v100
MM v[120:123],a[232:235],v[204:207],v[120:123]
EX v101,v101
MM v[120:123],a[228:231],v[188:191],v[120:123]
EX v102,v102
MM v[120:123],a[224:227],v[172:175],v[120:123]
s_waitcnt lgkmcnt(0)
s_barrier
MM a[0:3],a[128:131],v[0:3],a[0:3]
s_add_u32 m0, s65, 32768
CV v88,v88,v89
CV v89,v90,v91
MM a[0:3],a[132:135],v[4:7],a[0:3]
RD a[192:195],v224 offset:0
CV v90,v92,v93
MM a[8:11],a[132:135],v[12:15],a[8:11]
RD a[196:199],v225 offset:0
CV v91,v94,v95
MM a[8:11],a[128:131],v[8:11],a[8:11]
buffer_load_dwordx4 v228, s[8:11], s64 offen offset:0 lds
MM a[16:19],a[128:131],v[16:19],a[16:19]
RD a[200:203],v226 offset:0
CV v68,v96,v97
MM a[16:19],a[132:135],v[20:23],a[16:19]
RD a[204:207],v227 offset:0
CV v69,v98,v99
MM a[24:27],a[132:135],v[28:31],a[24:27]
buffer_load_dwordx4 v229, s[8:11], s64 offen offset:1024 lds
MM a[24:27],a[128:131],v[24:27],a[24:27]
RD a[208:211],v224 offset:4096
CV v70,v100,v101
MM a[28:31],a[136:139],v[24:27],a[28:31]
RD a[212:215],v225 offset:4096
RD a[216:219],v226 offset:4096
MM a[28:31],a[140:143],v[28:31],a[28:31]
buffer_load_dwordx4 v230, s[8:11], s64 offen offset:2048 lds
MM a[20:23],a[140:143],v[20:23],a[20:23]
RD a[220:223],v227 offset:4096
RD a[224:227],v224 offset:8192
MM a[20:23],a[136:139],v[16:19],a[20:23]
buffer_load_dwordx4 v231, s[8:11], s64 offen offset:3072 lds
MM a[12:15],a[136:139],v[8:11],a[12:15]
s_add_u32 m0, s65, 98304
EX v103,v103
MM a[12:15],a[140:143],v[12:15],a[12:15]
RD a[228:231],v225 offset:8192
CV v71,v102,v103
MM a[4:7],a[140:143],v[4:7],a[4:7]
EX v104,v104
MM a[4:7],a[136:139],v[0:3],a[4:7]
buffer_load_dwordx4 v235, s[12:15], s81 offen offset:0 lds
MM a[32:35],a[144:147],v[0:3],a[32:35]
EX v105,v105
MM a[32:35],a[148:151],v[4:7],a[32:35]
RD a[232:235],v226 offset:8192
CV v76,v104,v105
MM a[40:43],a[148:151],v[12:15],a[40:43]
EX v106,v106
MM a[40:43],a[144:147],v[8:11],a[40:43]
EX v107,v107
MM a[48:51],a[144:147],v[16:19],a[48:51]
RD a[236:239],v227 offset:8192
CV v77,v106,v107
MM a[48:51],a[148:151],v[20:23],a[48:51]
EX v108,v108
MM a[56:59],a[148:151],v[28:31],a[56:59]
buffer_load_dwordx4 v235, s[12:15], s64 offen offset:1024 lds
MM a[56:59],a[144:147],v[24:27],a[56:59]
EX v109,v109
MM a[60:63],a[152:155],v[24:27],a[60:63]
RD a[240:243],v224 offset:12288
CV v78,v108,v109
MM a[60:63],a[156:159],v[28:31],a[60:63]
EX v110,v110
MM a[52:55],a[156:159],v[20:23],a[52:55]
EX v111,v111
MM a[52:55],a[152:155],v[16:19],a[52:55]
RD a[244:247],v225 offset:12288
CV v79,v110,v111
MM a[44:47],a[152:155],v[8:11],a[44:47]
EX v112,v112
MM a[44:47],a[156:159],v[12:15],a[44:47]
buffer_load_dwordx4 v235, s[12:15], s81 offen offset:2048 lds
MM a[36:39],a[156:159],v[4:7],a[36:39]
EX v113,v113
MM a[36:39],a[152:155],v[0:3],a[36:39]
RD a[248:251],v226 offset:12288
CV v84,v112,v113
MM a[64:67],a[160:163],v[0:3],a[64:67]
EX v114,v114
MM a[64:67],a[164:167],v[4:7],a[64:67]
EX v115,v115
MM a[72:75],a[164:167],v[12:15],a[72:75]
RD a[252:255],v227 offset:12288
CV v85,v114,v115
MM a[72:75],a[160:163],v[8:11],a[72:75]
EX v116,v116
MM a[80:83],a[160:163],v[16:19],a[80:83]
buffer_load_dwordx4 v235, s[12:15], s64 offen offset:3072 lds
MM a[80:83],a[164:167],v[20:23],a[80:83]
EX v117,v117
MM a[88:91],a[164:167],v[28:31],a[88:91]
EX v118,v118
MM a[88:91],a[160:163],v[24:27],a[88:91]
EX v119,v119
MM a[92:95],a[168:171],v[24:27],a[92:95]
EX v120,v120
MM a[92:95],a[172:175],v[28:31],a[92:95]
EX v121,v121
MM a[84:87],a[172:175],v[20:23],a[84:87]
EX v122,v122
MM a[84:87],a[168:171],v[16:19],a[84:87]
EX v123,v123
MM a[76:79],a[168:171],v[8:11],a[76:79]
EX v124,v124
MM a[76:79],a[172:175],v[12:15],a[76:79]
EX v125,v125
MM a[68:71],a[172:175],v[4:7],a[68:71]
EX v126,v126
MM a[68:71],a[168:171],v[0:3],a[68:71]
EX v127,v127
MM a[96:99],a[176:179],v[0:3],a[96:99]
CV v86,v116,v117
CV v87,v118,v119
MM a[96:99],a[180:183],v[4:7],a[96:99]
CV v92,v120,v121
CV v93,v122,v123
MM a[104:107],a[180:183],v[12:15],a[104:107]
CV v94,v124,v125
CV v95,v126,v127
MM a[104:107],a[176:179],v[8:11],a[104:107]
MM a[112:115],a[176:179],v[16:19],a[112:115]
MM a[112:115],a[180:183],v[20:23],a[112:115]
MM a[120:123],a[180:183],v[28:31],a[120:123]
MM a[120:123],a[176:179],v[24:27],a[120:123]
MM a[124:127],a[184:187],v[24:27],a[124:127]
MM a[124:127],a[188:191],v[28:31],a[124:127]
MM a[116:119],a[188:191],v[20:23],a[116:119]
MM a[116:119],a[184:187],v[16:19],a[116:119]
MM a[108:111],a[184:187],v[8:11],a[108:111]
MM a[108:111],a[188:191],v[12:15],a[108:111]
MM a[100:103],a[188:191],v[4:7],a[100:103]
MM a[100:103],a[184:187],v[0:3],a[100:103]
v_smfmac_f32_16x16x64_bf16 v[236:239], v[144:147], v[64:71], v234
v_smfmac_f32_16x16x64_bf16 v[236:239], v[148:151], v[72:79], v234
v_smfmac_f32_16x16x64_bf16 v[236:239], v[152:155], v[80:87], v234
v_smfmac_f32_16x16x64_bf16 v[236:239], v[156:159], v[88:95], v234
s_waitcnt lgkmcnt(0)
MM v[16:19],a[192:195],v[168:171],v[136:139]
TR a[128:129],v232 offset:49152
TR a[130:131],v232 offset:53248
MM v[16:19],a[196:199],v[184:187],v[16:19]
TR a[132:133],v232 offset:57344
TR a[134:135],v232 offset:61440
MM v[16:19],a[200:203],v[200:203],v[16:19]
TR a[136:137],v233 offset:49152
TR a[138:139],v233 offset:53248
MM v[16:19],a[204:207],v[216:219],v[16:19]
TR a[140:141],v233 offset:57344
v_add_f32 v247, v236, v237
MM v[20:23],a[220:223],v[216:219],v[136:139]
TR a[142:143],v233 offset:61440
v_cmp_nge_f32 vcc, s71, v247
MM v[20:23],a[216:219],v[200:203],v[20:23]
s_cbranch_vccnz LSITE31_%=
LBACK31_%=:
TR a[144:145],v232 offset:49664
TR a[146:147],v232 offset:53760
MM v[20:23],a[212:215],v[184:187],v[20:23]
v_rcp_f32 v145, v247
MM v[20:23],a[208:211],v[168:171],v[20:23]
TR a[148:149],v232 offset:57856
TR a[150:151],v232 offset:61952
MM v[28:31],a[208:211],v[172:175],v[140:143]
EX v16,v16
MM v[28:31],a[212:215],v[188:191],v[28:31]
TR a[152:153],v233 offset:49664
TR a[154:155],v233 offset:53760
MM v[28:31],a[216:219],v[204:207],v[28:31]
EX v17,v17
MM v[28:31],a[220:223],v[220:223],v[28:31]
TR a[156:157],v233 offset:57856
CV v16,v16,v17
MM v[24:27],a[204:207],v[220:223],v[140:143]
TR a[158:159],v233 offset:61952
TR a[160:161],v232 offset:50176
MM v[24:27],a[200:203],v[204:207],v[24:27]
TR a[162:163],v232 offset:54272
TR a[164:165],v232 offset:58368
MM v[24:27],a[196:199],v[188:191],v[24:27]
EX v18,v18
MM v[24:27],a[192:195],v[172:175],v[24:27]
TR a[166:167],v232 offset:62464
TR a[168:169],v233 offset:50176
MM v[48:51],a[224:227],v[168:171],v[136:139]
EX v19,v19
MM v[48:51],a[228:231],v[184:187],v[48:51]
LPG19_%=:
s_cbranch_execz LPG23_%=
TR a[170:171],v233 offset:54272
CV v17,v18,v19
MM v[48:51],a[232:235],v[200:203],v[48:51]
TR a[172:173],v233 offset:58368
TR a[174:175],v233 offset:62464
MM v[48:51],a[236:239],v[216:219],v[48:51]
TR a[176:177],v232 offset:50688
TR a[178:179],v232 offset:54784
MM v[52:55],a[252:255],v[216:219],v[136:139]
EX v20,v20
MM v[52:55],a[248:251],v[200:203],v[52:55]
TR a[180:181],v232 offset:58880
TR a[182:183],v232 offset:62976
MM v[52:55],a[244:247],v[184:187],v[52:55]
EX v21,v21
MM v[52:55],a[240:243],v[168:171],v[52:55]
TR a[184:185],v233 offset:50688
TR a[186:187],v233 offset:54784
MM v[60:63],a[240:243],v[172:175],v[140:143]
TR a[188:189],v233 offset:58880
CV v18,v20,v21
MM v[60:63],a[244:247],v[188:191],v[60:63]
TR a[190:191],v233 offset:62976
v_mov_b32 v144, v145
MM v[60:63],a[248:251],v[204:207],v[60:63]
EX v22,v22
MM v[60:63],a[252:255],v[220:223],v[60:63]
EX v23,v23
MM v[56:59],a[236:239],v[220:223],v[140:143]
CV v19,v22,v23
v_permlane32_swap_b32 v145, v144
MM v[56:59],a[232:235],v[204:207],v[56:59]
EX v24,v24
MM v[56:59],a[228:231],v[188:191],v[56:59]
EX v25,v25
MM v[56:59],a[224:227],v[172:175],v[56:59]
s_waitcnt vmcnt(12) lgkmcnt(0)
s_barrier
CV v24,v24,v25
MM a[0:3],a[128:131],v[64:67],a[0:3]
s_add_u32 m0, s89, 49152
RD a[192:195],v224 offset:16384
RD a[196:199],v225 offset:16384
MM a[0:3],a[132:135],v[68:71],a[0:3]
s_add_u32 s66, s85, 0
buffer_load_dwordx4 v240, s[24:27], s66 offen offset:0 lds
MM a[8:11],a[132:135],v[76:79],a[8:11]
s_add_u32 m0, s89, 50176
EX v26,v26
MM a[8:11],a[128:131],v[72:75],a[8:11]
s_add_u32 s66, s85, 10240
RD a[200:203],v226 offset:16384
RD a[204:207],v227 offset:16384
MM a[12:15],a[136:139],v[72:75],a[12:15]
EX v27,v27
MM a[12:15],a[140:143],v[76:79],a[12:15]
RD a[208:211],v224 offset:20480
CV v25,v26,v27
MM a[4:7],a[140:143],v[68:71],a[4:7]
buffer_load_dwordx4 v240, s[24:27], s66 offen offset:0 lds
MM a[4:7],a[136:139],v[64:67],a[4:7]
s_add_u32 m0, s89, 57344
EX v28,v28
MM a[32:35],a[144:147],v[64:67],a[32:35]
s_add_u32 s66, s85, 81920
EX v29,v29
)ASM"
R"ASM(
MM a[32:35],a[148:151],v[68:71],a[32:35]
CV v26,v28,v29
v_mov_b32 v149, v145
MM a[40:43],a[148:151],v[76:79],a[40:43]
RD a[212:215],v225 offset:20480
v_permlane16_swap_b32 v145, v149
MM a[40:43],a[144:147],v[72:75],a[40:43]
s_mul_i32 s78, s49, 17408
s_add_u32 s78, s83, s78
s_add_u32 s78, s78, 0
MM a[44:47],a[152:155],v[72:75],a[44:47]
s_add_u32 s79, s78, 4096
AR v0,a0
AR v1,a1
MM a[44:47],a[156:159],v[76:79],a[44:47]
AR v2,a2
AR v3,a3
MM a[36:39],a[156:159],v[68:71],a[36:39]
AR v4,a4
AR v5,a5
MM a[36:39],a[152:155],v[64:67],a[36:39]
AR v6,a6
AR v7,a7
MM a[64:67],a[160:163],v[64:67],a[64:67]
EX v30,v30
MM a[64:67],a[164:167],v[68:71],a[64:67]
v_mul_f32 v0, v0, v145
v_mul_f32 v1, v1, v145
MM a[72:75],a[164:167],v[76:79],a[72:75]
v_mul_f32 v2, v2, v145
v_mul_f32 v3, v3, v145
MM a[72:75],a[160:163],v[72:75],a[72:75]
v_mul_f32 v4, v4, v145
v_mul_f32 v5, v5, v145
MM a[76:79],a[168:171],v[72:75],a[76:79]
v_mul_f32 v6, v6, v145
v_mul_f32 v7, v7, v145
MM a[76:79],a[172:175],v[76:79],a[76:79]
EX v31,v31
MM a[68:71],a[172:175],v[68:71],a[68:71]
CV v160,v0,v1
CV v161,v2,v3
MM a[68:71],a[168:171],v[64:67],a[68:71]
CV v162,v4,v5
CV v163,v6,v7
MM a[96:99],a[176:179],v[64:67],a[96:99]
buffer_store_dwordx4 v[160:163], v248, s[20:23], s78 offen offset:0 sc1
EX v48,v48
MM a[96:99],a[180:183],v[68:71],a[96:99]
AR v8,a8
AR v9,a9
MM a[104:107],a[180:183],v[76:79],a[104:107]
AR v10,a10
AR v11,a11
MM a[104:107],a[176:179],v[72:75],a[104:107]
AR v12,a12
AR v13,a13
EX v49,v49
MM a[108:111],a[184:187],v[72:75],a[108:111]
AR v14,a14
AR v15,a15
MM a[108:111],a[188:191],v[76:79],a[108:111]
v_mul_f32 v8, v8, v149
v_mul_f32 v9, v9, v149
EX v50,v50
MM a[100:103],a[188:191],v[68:71],a[100:103]
v_mul_f32 v10, v10, v149
v_mul_f32 v11, v11, v149
MM a[100:103],a[184:187],v[64:67],a[100:103]
buffer_load_dwordx4 v240, s[24:27], s66 offen offset:0 lds
v_mul_f32 v12, v12, v149
v_mul_f32 v13, v13, v149
MM a[16:19],a[128:131],v[80:83],a[16:19]
s_add_u32 m0, s89, 58368
v_mul_f32 v14, v14, v149
v_mul_f32 v15, v15, v149
EX v51,v51
MM a[16:19],a[132:135],v[84:87],a[16:19]
s_add_u32 s66, s85, 92160
CV v164,v8,v9
CV v27,v30,v31
MM a[24:27],a[132:135],v[92:95],a[24:27]
CV v165,v10,v11
CV v166,v12,v13
MM a[24:27],a[128:131],v[88:91],a[24:27]
RD a[216:219],v226 offset:20480
CV v167,v14,v15
EX v52,v52
MM a[28:31],a[136:139],v[88:91],a[28:31]
buffer_store_dwordx4 v[164:167], v248, s[20:23], s79 offen offset:0 sc1
AR v32,a32
MM a[28:31],a[140:143],v[92:95],a[28:31]
AR v33,a33
AR v34,a34
EX v53,v53
MM a[20:23],a[140:143],v[84:87],a[20:23]
AR v35,a35
AR v36,a36
MM a[20:23],a[136:139],v[80:83],a[20:23]
buffer_load_dwordx4 v240, s[24:27], s66 offen offset:0 lds
AR v37,a37
AR v38,a38
MM a[48:51],a[144:147],v[80:83],a[48:51]
s_add_u32 m0, s89, 114688
AR v39,a39
v_mul_f32 v32, v32, v145
EX v54,v54
MM a[48:51],a[148:151],v[84:87],a[48:51]
s_add_u32 s66, s86, 0
v_mul_f32 v33, v33, v145
CV v20,v48,v49
MM a[56:59],a[148:151],v[92:95],a[56:59]
RD a[220:223],v227 offset:20480
v_mul_f32 v34, v34, v145
v_mul_f32 v35, v35, v145
EX v55,v55
MM a[56:59],a[144:147],v[88:91],a[56:59]
v_mul_f32 v36, v36, v145
v_mul_f32 v37, v37, v145
MM a[60:63],a[152:155],v[88:91],a[60:63]
v_mul_f32 v38, v38, v145
v_mul_f32 v39, v39, v145
MM a[60:63],a[156:159],v[92:95],a[60:63]
CV v176,v32,v33
CV v177,v34,v35
EX v56,v56
MM a[52:55],a[156:159],v[84:87],a[52:55]
RD a[224:227],v224 offset:24576
buffer_load_dwordx4 v240, s[24:27], s66 offen offset:0 lds
CV v178,v36,v37
CV v179,v38,v39
CV v21,v50,v51
MM a[52:55],a[152:155],v[80:83],a[52:55]
s_add_u32 m0, s89, 115712
buffer_store_dwordx4 v[176:179], v248, s[20:23], s78 offen offset:1024 sc1
MM a[80:83],a[160:163],v[80:83],a[80:83]
s_add_u32 s66, s86, 10240
AR v40,a40
AR v41,a41
EX v57,v57
MM a[80:83],a[164:167],v[84:87],a[80:83]
AR v42,a42
AR v43,a43
MM a[88:91],a[164:167],v[92:95],a[88:91]
RD a[228:231],v225 offset:24576
AR v44,a44
AR v45,a45
MM a[88:91],a[160:163],v[88:91],a[88:91]
AR v46,a46
CV v22,v52,v53
EX v58,v58
MM a[92:95],a[168:171],v[88:91],a[92:95]
AR v47,a47
v_mul_f32 v40, v40, v149
MM a[92:95],a[172:175],v[92:95],a[92:95]
v_mul_f32 v41, v41, v149
v_mul_f32 v42, v42, v149
EX v59,v59
MM a[84:87],a[172:175],v[84:87],a[84:87]
RD a[232:235],v226 offset:24576
buffer_load_dwordx4 v240, s[24:27], s66 offen offset:0 lds
v_mul_f32 v43, v43, v149
v_mul_f32 v44, v44, v149
MM a[84:87],a[168:171],v[80:83],a[84:87]
s_add_u32 m0, s89, 122880
v_mul_f32 v45, v45, v149
v_mul_f32 v46, v46, v149
MM a[112:115],a[176:179],v[80:83],a[112:115]
s_add_u32 s66, s86, 81920
v_mul_f32 v47, v47, v149
CV v180,v40,v41
EX v60,v60
CV v23,v54,v55
MM a[112:115],a[180:183],v[84:87],a[112:115]
CV v181,v42,v43
CV v182,v44,v45
MM a[120:123],a[180:183],v[92:95],a[120:123]
RD a[236:239],v227 offset:24576
CV v183,v46,v47
EX v61,v61
MM a[120:123],a[176:179],v[88:91],a[120:123]
buffer_store_dwordx4 v[180:183], v248, s[20:23], s79 offen offset:1024 sc1
AR v0,a64
MM a[124:127],a[184:187],v[88:91],a[124:127]
AR v1,a65
AR v2,a66
MM a[124:127],a[188:191],v[92:95],a[124:127]
AR v3,a67
AR v4,a68
EX v62,v62
MM a[116:119],a[188:191],v[84:87],a[116:119]
RD a[240:243],v224 offset:28672
RD a[244:247],v225 offset:28672
RD a[248:251],v226 offset:28672
RD a[252:255],v227 offset:28672
buffer_load_dwordx4 v240, s[24:27], s66 offen offset:0 lds
EX v63,v63
AR v5,a69
CV v28,v56,v57
MM a[116:119],a[184:187],v[80:83],a[116:119]
s_add_u32 m0, s89, 123904
AR v6,a70
AR v7,a71
v_smfmac_f32_16x16x64_bf16 v[236:239], v[152:155], v[16:23], v234
s_add_u32 s66, s86, 92160
buffer_load_dwordx4 v240, s[24:27], s66 offen offset:0 lds
CV v29,v58,v59
CV v30,v60,v61
CV v31,v62,v63
v_mul_f32 v0, v0, v145
v_mul_f32 v1, v1, v145
v_smfmac_f32_16x16x64_bf16 v[236:239], v[156:159], v[24:31], v234
s_waitcnt lgkmcnt(0)
v_mul_f32 v2, v2, v145
v_mul_f32 v3, v3, v145
MM v[80:83],a[192:195],v[168:171],v[136:139]
v_mul_f32 v4, v4, v145
v_mul_f32 v5, v5, v145
MM v[80:83],a[196:199],v[184:187],v[80:83]
v_mul_f32 v6, v6, v145
v_mul_f32 v7, v7, v145
MM v[80:83],a[200:203],v[200:203],v[80:83]
CV v192,v0,v1
CV v193,v2,v3
MM v[80:83],a[204:207],v[216:219],v[80:83]
TR a[128:129],v232 offset:0
v_add_f32 v246, v236, v237
CV v194,v4,v5
CV v195,v6,v7
MM v[84:87],a[220:223],v[216:219],v[136:139]
TR a[130:131],v232 offset:4096
buffer_store_dwordx4 v[192:195], v248, s[20:23], s78 offen offset:2048 sc1
TR a[132:133],v232 offset:8192
v_cmp_nge_f32 vcc, s71, v246
MM v[84:87],a[216:219],v[200:203],v[84:87]
s_cbranch_vccnz LSITE32_%=
LBACK32_%=:
TR a[134:135],v232 offset:12288
AR v8,a72
AR v9,a73
MM v[84:87],a[212:215],v[184:187],v[84:87]
TR a[136:137],v233 offset:0
AR v10,a74
AR v11,a75
MM v[84:87],a[208:211],v[168:171],v[84:87]
TR a[138:139],v233 offset:4096
AR v12,a76
MM v[92:95],a[208:211],v[172:175],v[140:143]
TR a[140:141],v233 offset:8192
TR a[142:143],v233 offset:12288
AR v13,a77
AR v14,a78
MM v[92:95],a[212:215],v[188:191],v[92:95]
TR a[144:145],v232 offset:512
AR v15,a79
v_mul_f32 v8, v8, v149
MM v[92:95],a[216:219],v[204:207],v[92:95]
TR a[146:147],v232 offset:4608
v_mul_f32 v9, v9, v149
v_mul_f32 v10, v10, v149
MM v[92:95],a[220:223],v[220:223],v[92:95]
TR a[148:149],v232 offset:8704
v_mul_f32 v11, v11, v149
v_mul_f32 v12, v12, v149
MM v[88:91],a[204:207],v[220:223],v[140:143]
TR a[150:151],v232 offset:12800
v_mul_f32 v13, v13, v149
EX v80,v80
v_mul_f32 v14, v14, v149
MM v[88:91],a[200:203],v[204:207],v[88:91]
TR a[152:153],v233 offset:512
TR a[154:155],v233 offset:4608
v_mul_f32 v15, v15, v149
CV v196,v8,v9
EX v81,v81
MM v[88:91],a[196:199],v[188:191],v[88:91]
TR a[156:157],v233 offset:8704
CV v197,v10,v11
CV v198,v12,v13
MM v[88:91],a[192:195],v[172:175],v[88:91]
TR a[158:159],v233 offset:12800
CV v199,v14,v15
EX v82,v82
MM v[112:115],a[224:227],v[168:171],v[136:139]
buffer_store_dwordx4 v[196:199], v248, s[20:23], s79 offen offset:2048 sc1
TR a[160:161],v232 offset:1024
AR v32,a96
EX v83,v83
MM v[112:115],a[228:231],v[184:187],v[112:115]
TR a[162:163],v232 offset:5120
TR a[164:165],v232 offset:9216
AR v33,a97
AR v34,a98
MM v[112:115],a[232:235],v[200:203],v[112:115]
TR a[166:167],v232 offset:13312
AR v35,a99
EX v84,v84
MM v[112:115],a[236:239],v[216:219],v[112:115]
TR a[168:169],v233 offset:1024
AR v36,a100
AR v37,a101
MM v[116:119],a[252:255],v[216:219],v[136:139]
TR a[170:171],v233 offset:5120
AR v38,a102
EX v85,v85
AR v39,a103
MM v[116:119],a[248:251],v[200:203],v[116:119]
TR a[172:173],v233 offset:9216
TR a[174:175],v233 offset:13312
v_mul_f32 v32, v32, v145
v_mul_f32 v33, v33, v145
MM v[116:119],a[244:247],v[184:187],v[116:119]
TR a[176:177],v232 offset:1536
v_mul_f32 v34, v34, v145
v_mul_f32 v35, v35, v145
EX v86,v86
MM v[116:119],a[240:243],v[168:171],v[116:119]
TR a[178:179],v232 offset:5632
v_mul_f32 v36, v36, v145
v_mul_f32 v37, v37, v145
EX v87,v87
MM v[124:127],a[240:243],v[172:175],v[140:143]
TR a[180:181],v232 offset:9728
v_mul_f32 v38, v38, v145
v_mul_f32 v39, v39, v145
CV v80,v80,v81
MM v[124:127],a[244:247],v[188:191],v[124:127]
TR a[182:183],v232 offset:13824
CV v208,v32,v33
EX v88,v88
CV v209,v34,v35
MM v[124:127],a[248:251],v[204:207],v[124:127]
TR a[184:185],v233 offset:1536
TR a[186:187],v233 offset:5632
TR a[188:189],v233 offset:9728
TR a[190:191],v233 offset:13824
CV v210,v36,v37
CV v211,v38,v39
EX v89,v89
MM v[124:127],a[252:255],v[220:223],v[124:127]
buffer_store_dwordx4 v[208:211], v248, s[20:23], s78 offen offset:3072 sc1
CV v81,v82,v83
MM v[120:123],a[236:239],v[220:223],v[140:143]
AR v40,a104
AR v41,a105
EX v90,v90
MM v[120:123],a[232:235],v[204:207],v[120:123]
AR v42,a106
AR v43,a107
MM v[120:123],a[228:231],v[188:191],v[120:123]
EX v91,v91
AR v44,a108
CV v82,v84,v85
MM v[120:123],a[224:227],v[172:175],v[120:123]
s_waitcnt vmcnt(19) lgkmcnt(0)
s_barrier
AR v45,a109
AR v46,a110
EX v92,v92
MM a[16:19],a[128:131],v[16:19],a[16:19]
s_add_u32 m0, s89, 0
AR v47,a111
v_mul_f32 v40, v40, v149
MM a[16:19],a[132:135],v[20:23],a[16:19]
s_add_u32 s66, s87, 0
v_mul_f32 v41, v41, v149
v_mul_f32 v42, v42, v149
EX v93,v93
CV v83,v86,v87
MM a[24:27],a[132:135],v[28:31],a[24:27]
v_mul_f32 v43, v43, v149
v_mul_f32 v44, v44, v149
MM a[24:27],a[128:131],v[24:27],a[24:27]
v_mul_f32 v45, v45, v149
v_mul_f32 v46, v46, v149
EX v94,v94
MM a[28:31],a[136:139],v[24:27],a[28:31]
v_mul_f32 v47, v47, v149
CV v212,v40,v41
EX v95,v95
MM a[28:31],a[140:143],v[28:31],a[28:31]
CV v213,v42,v43
CV v88,v88,v89
CV v214,v44,v45
MM a[20:23],a[140:143],v[20:23],a[20:23]
CV v215,v46,v47
CV v89,v90,v91
EX v112,v112
MM a[20:23],a[136:139],v[16:19],a[20:23]
buffer_store_dwordx4 v[212:215], v248, s[20:23], s79 offen offset:3072 sc1
MM a[48:51],a[144:147],v[16:19],a[48:51]
buffer_load_dwordx4 v240, s[24:27], s66 offen offset:0 lds
RD a[192:195],v224 offset:32768
EX v113,v113
MM a[48:51],a[148:151],v[20:23],a[48:51]
s_add_u32 m0, s89, 1024
EX v114,v114
MM a[56:59],a[148:151],v[28:31],a[56:59]
s_add_u32 s66, s87, 10240
RD a[196:199],v225 offset:32768
CV v90,v92,v93
MM a[56:59],a[144:147],v[24:27],a[56:59]
buffer_load_dwordx4 v240, s[24:27], s66 offen offset:0 lds
RD a[200:203],v226 offset:32768
EX v115,v115
MM a[60:63],a[152:155],v[24:27],a[60:63]
s_add_u32 m0, s89, 8192
EX v116,v116
MM a[60:63],a[156:159],v[28:31],a[60:63]
s_add_u32 s66, s87, 81920
RD a[204:207],v227 offset:32768
CV v91,v94,v95
MM a[52:55],a[156:159],v[20:23],a[52:55]
EX v117,v117
MM a[52:55],a[152:155],v[16:19],a[52:55]
RD a[208:211],v224 offset:36864
RD a[212:215],v225 offset:36864
buffer_load_dwordx4 v240, s[24:27], s66 offen offset:0 lds
MM a[80:83],a[160:163],v[16:19],a[80:83]
s_add_u32 m0, s89, 9216
EX v118,v118
MM a[80:83],a[164:167],v[20:23],a[80:83]
s_add_u32 s66, s87, 92160
RD a[216:219],v226 offset:36864
CV v84,v112,v113
MM a[88:91],a[164:167],v[28:31],a[88:91]
EX v119,v119
MM a[88:91],a[160:163],v[24:27],a[88:91]
buffer_load_dwordx4 v240, s[24:27], s66 offen offset:0 lds
EX v120,v120
MM a[92:95],a[168:171],v[24:27],a[92:95]
s_add_u32 m0, s89, 65536
RD a[220:223],v227 offset:36864
CV v85,v114,v115
MM a[92:95],a[172:175],v[28:31],a[92:95]
s_add_u32 s66, s88, 0
EX v121,v121
MM a[84:87],a[172:175],v[20:23],a[84:87]
RD a[224:227],v224 offset:40960
CV v86,v116,v117
)ASM"
R"ASM(
MM a[84:87],a[168:171],v[16:19],a[84:87]
buffer_load_dwordx4 v240, s[24:27], s66 offen offset:0 lds
EX v122,v122
MM a[112:115],a[176:179],v[16:19],a[112:115]
s_add_u32 m0, s89, 66560
RD a[228:231],v225 offset:40960
EX v123,v123
MM a[112:115],a[180:183],v[20:23],a[112:115]
s_add_u32 s66, s88, 10240
RD a[232:235],v226 offset:40960
CV v87,v118,v119
MM a[120:123],a[180:183],v[28:31],a[120:123]
EX v124,v124
MM a[120:123],a[176:179],v[24:27],a[120:123]
buffer_load_dwordx4 v240, s[24:27], s66 offen offset:0 lds
MM a[124:127],a[184:187],v[24:27],a[124:127]
s_add_u32 m0, s89, 73728
RD a[236:239],v227 offset:40960
EX v125,v125
MM a[124:127],a[188:191],v[28:31],a[124:127]
s_add_u32 s66, s88, 81920
EX v126,v126
MM a[116:119],a[188:191],v[20:23],a[116:119]
RD a[240:243],v224 offset:45056
RD a[244:247],v225 offset:45056
RD a[248:251],v226 offset:45056
RD a[252:255],v227 offset:45056
EX v127,v127
MM a[116:119],a[184:187],v[16:19],a[116:119]
buffer_load_dwordx4 v240, s[24:27], s66 offen offset:0 lds
CV v92,v120,v121
v_smfmac_f32_16x16x64_bf16 v[236:239], v[152:155], v[80:87], v234
s_add_u32 m0, s89, 74752
CV v93,v122,v123
CV v94,v124,v125
CV v95,v126,v127
s_nop 1
v_smfmac_f32_16x16x64_bf16 v[236:239], v[156:159], v[88:95], v234
s_waitcnt lgkmcnt(0)
s_add_u32 s66, s88, 92160
buffer_load_dwordx4 v240, s[24:27], s66 offen offset:0 lds
v_lshrrev_b32 v230, 9, v248
v_lshlrev_b32 v230, 9, v230
v_sub_u32 v244, v232, v230
v_sub_u32 v235, v233, v230
v_sub_u32 v244, v244, v230
v_sub_u32 v235, v235, v230
v_sub_u32 v228, v244, v230
v_sub_u32 v229, v235, v230
v_add_u32 v228, 65536, v228
v_add_u32 v229, 65536, v229
v_subrev_u32 v230, 65536, v244
v_subrev_u32 v231, 65536, v235
MM v[16:19],a[192:195],v[168:171],v[136:139]
TR a[128:129],v232 offset:16384
TR a[130:131],v232 offset:20480
MM v[16:19],a[196:199],v[184:187],v[16:19]
TR a[132:133],v232 offset:24576
TR a[134:135],v232 offset:28672
MM v[16:19],a[200:203],v[200:203],v[16:19]
TR a[136:137],v233 offset:16384
TR a[138:139],v233 offset:20480
MM v[16:19],a[204:207],v[216:219],v[16:19]
TR a[140:141],v233 offset:24576
v_add_f32 v247, v236, v237
MM v[20:23],a[220:223],v[216:219],v[136:139]
TR a[142:143],v233 offset:28672
v_cmp_nge_f32 vcc, s71, v247
MM v[20:23],a[216:219],v[200:203],v[20:23]
s_cbranch_vccnz LSITE33_%=
LBACK33_%=:
TR a[144:145],v232 offset:16896
TR a[146:147],v232 offset:20992
MM v[20:23],a[212:215],v[184:187],v[20:23]
TR a[148:149],v232 offset:25088
TR a[150:151],v232 offset:29184
MM v[20:23],a[208:211],v[168:171],v[20:23]
TR a[152:153],v233 offset:16896
TR a[154:155],v233 offset:20992
MM v[28:31],a[208:211],v[172:175],v[140:143]
EX v16,v16
MM v[28:31],a[212:215],v[188:191],v[28:31]
EX v17,v17
MM v[28:31],a[216:219],v[204:207],v[28:31]
s_lshl_b32 s97, s84, 2
s_add_u32 s97, s97, s96
s_add_u32 s97, s97, 0
MM v[28:31],a[220:223],v[220:223],v[28:31]
LPG20_%=:
s_cbranch_execz LPG24_%=
s_waitcnt vmcnt(8)
v_mov_b32 v242, s69
s_mov_b64 exec, 1
buffer_store_dword v242, off, s[20:23], s97 offset:0 sc1
s_mov_b64 exec, -1
MM v[24:27],a[204:207],v[220:223],v[140:143]
TR a[156:157],v233 offset:25088
TR a[158:159],v233 offset:29184
MM v[24:27],a[200:203],v[204:207],v[24:27]
EX v18,v18
MM v[24:27],a[196:199],v[188:191],v[24:27]
TR a[160:161],v232 offset:17408
TR a[162:163],v232 offset:21504
MM v[24:27],a[192:195],v[172:175],v[24:27]
EX v19,v19
MM v[48:51],a[224:227],v[168:171],v[136:139]
TR a[164:165],v232 offset:25600
TR a[166:167],v232 offset:29696
MM v[48:51],a[228:231],v[184:187],v[48:51]
EX v20,v20
MM v[48:51],a[232:235],v[200:203],v[48:51]
TR a[168:169],v233 offset:17408
TR a[170:171],v233 offset:21504
MM v[48:51],a[236:239],v[216:219],v[48:51]
EX v21,v21
MM v[52:55],a[252:255],v[216:219],v[136:139]
TR a[172:173],v233 offset:25600
CV v16,v16,v17
MM v[52:55],a[248:251],v[200:203],v[52:55]
TR a[174:175],v233 offset:29696
TR a[176:177],v232 offset:17920
MM v[52:55],a[244:247],v[184:187],v[52:55]
EX v22,v22
MM v[52:55],a[240:243],v[168:171],v[52:55]
TR a[178:179],v232 offset:22016
TR a[180:181],v232 offset:26112
EX v23,v23
MM v[60:63],a[240:243],v[172:175],v[140:143]
TR a[182:183],v232 offset:30208
CV v17,v18,v19
MM v[60:63],a[244:247],v[188:191],v[60:63]
EX v24,v24
MM v[60:63],a[248:251],v[204:207],v[60:63]
TR a[184:185],v233 offset:17920
TR a[186:187],v233 offset:22016
TR a[188:189],v233 offset:26112
TR a[190:191],v233 offset:30208
EX v25,v25
MM v[60:63],a[252:255],v[220:223],v[60:63]
EX v26,v26
MM v[56:59],a[236:239],v[220:223],v[140:143]
EX v27,v27
MM v[56:59],a[232:235],v[204:207],v[56:59]
EX v28,v28
MM v[56:59],a[228:231],v[188:191],v[56:59]
EX v29,v29
CV v18,v20,v21
MM v[56:59],a[224:227],v[172:175],v[56:59]
s_waitcnt lgkmcnt(0)
s_barrier
CV v19,v22,v23
MM a[16:19],a[128:131],v[80:83],a[16:19]
s_add_u32 m0, s89, 16384
EX v30,v30
MM a[16:19],a[132:135],v[84:87],a[16:19]
s_add_u32 s66, s85, 128
EX v31,v31
MM a[24:27],a[132:135],v[92:95],a[24:27]
buffer_load_dwordx4 v240, s[24:27], s66 offen offset:0 lds
MM a[24:27],a[128:131],v[88:91],a[24:27]
s_add_u32 m0, s89, 17408
EX v48,v48
MM a[28:31],a[136:139],v[88:91],a[28:31]
s_add_u32 s66, s85, 10368
CV v24,v24,v25
CV v25,v26,v27
MM a[28:31],a[140:143],v[92:95],a[28:31]
CV v26,v28,v29
CV v27,v30,v31
MM a[20:23],a[140:143],v[84:87],a[20:23]
EX v49,v49
MM a[20:23],a[136:139],v[80:83],a[20:23]
EX v50,v50
MM a[48:51],a[144:147],v[80:83],a[48:51]
buffer_load_dwordx4 v240, s[24:27], s66 offen offset:0 lds
MM a[48:51],a[148:151],v[84:87],a[48:51]
s_add_u32 m0, s89, 24576
EX v51,v51
MM a[56:59],a[148:151],v[92:95],a[56:59]
s_add_u32 s66, s85, 82048
TR a[128:129],v232 offset:32768
CV v20,v48,v49
MM a[56:59],a[144:147],v[88:91],a[56:59]
EX v52,v52
MM a[60:63],a[152:155],v[88:91],a[60:63]
TR a[130:131],v232 offset:36864
TR a[132:133],v232 offset:40960
MM a[60:63],a[156:159],v[92:95],a[60:63]
EX v53,v53
MM a[52:55],a[156:159],v[84:87],a[52:55]
TR a[134:135],v232 offset:45056
TR a[136:137],v233 offset:32768
MM a[52:55],a[152:155],v[80:83],a[52:55]
buffer_load_dwordx4 v240, s[24:27], s66 offen offset:0 lds
MM a[80:83],a[160:163],v[80:83],a[80:83]
s_add_u32 m0, s89, 25600
TR a[138:139],v233 offset:36864
TR a[140:141],v233 offset:40960
EX v54,v54
MM a[80:83],a[164:167],v[84:87],a[80:83]
s_add_u32 s66, s85, 92288
TR a[142:143],v233 offset:45056
TR a[144:145],v232 offset:33280
MM a[88:91],a[164:167],v[92:95],a[88:91]
EX v55,v55
MM a[88:91],a[160:163],v[88:91],a[88:91]
TR a[146:147],v232 offset:37376
TR a[148:149],v232 offset:41472
buffer_load_dwordx4 v240, s[24:27], s66 offen offset:0 lds
EX v56,v56
MM a[92:95],a[168:171],v[88:91],a[92:95]
s_add_u32 m0, s89, 81920
TR a[150:151],v232 offset:45568
CV v21,v50,v51
MM a[92:95],a[172:175],v[92:95],a[92:95]
s_add_u32 s66, s86, 128
TR a[152:153],v233 offset:33280
EX v57,v57
MM a[84:87],a[172:175],v[84:87],a[84:87]
TR a[154:155],v233 offset:37376
TR a[156:157],v233 offset:41472
MM a[84:87],a[168:171],v[80:83],a[84:87]
TR a[158:159],v233 offset:45568
buffer_load_dwordx4 v240, s[24:27], s66 offen offset:0 lds
EX v58,v58
MM a[112:115],a[176:179],v[80:83],a[112:115]
s_add_u32 m0, s89, 82944
TR a[160:161],v232 offset:33792
CV v22,v52,v53
EX v59,v59
MM a[112:115],a[180:183],v[84:87],a[112:115]
s_add_u32 s66, s86, 10368
TR a[162:163],v232 offset:37888
TR a[164:165],v232 offset:41984
MM a[120:123],a[180:183],v[92:95],a[120:123]
TR a[166:167],v232 offset:46080
CV v23,v54,v55
EX v60,v60
MM a[120:123],a[176:179],v[88:91],a[120:123]
TR a[168:169],v233 offset:33792
TR a[170:171],v233 offset:37888
buffer_load_dwordx4 v240, s[24:27], s66 offen offset:0 lds
MM a[124:127],a[184:187],v[88:91],a[124:127]
s_add_u32 m0, s89, 90112
TR a[172:173],v233 offset:41984
v_lshrrev_b32 v243, 2, v248
v_min_u32 v243, 12, v243
EX v61,v61
MM a[124:127],a[188:191],v[92:95],a[124:127]
s_add_u32 s66, s86, 82048
buffer_load_dword v242, v243, s[20:23], s96 offen offset:0 sc1
TR a[174:175],v233 offset:46080
EX v62,v62
MM a[116:119],a[188:191],v[84:87],a[116:119]
TR a[176:177],v232 offset:34304
TR a[178:179],v232 offset:38400
EX v63,v63
MM a[116:119],a[184:187],v[80:83],a[116:119]
TR a[180:181],v232 offset:42496
buffer_load_dwordx4 v240, s[24:27], s66 offen offset:0 lds
CV v28,v56,v57
v_smfmac_f32_16x16x64_bf16 v[236:239], v[152:155], v[16:23], v234
s_add_u32 m0, s89, 91136
TR a[182:183],v232 offset:46592
CV v29,v58,v59
CV v30,v60,v61
CV v31,v62,v63
s_nop 1
v_smfmac_f32_16x16x64_bf16 v[236:239], v[156:159], v[24:31], v234
TR a[184:185],v233 offset:34304
TR a[186:187],v233 offset:38400
TR a[188:189],v233 offset:42496
TR a[190:191],v233 offset:46592
s_waitcnt lgkmcnt(0)
s_add_u32 s66, s86, 92288
buffer_load_dwordx4 v240, s[24:27], s66 offen offset:0 lds
s_nop 6
v_add_f32 v246, v236, v237
v_cmp_nge_f32 vcc, s71, v246
s_cbranch_vccnz LSITE34_%=
LBACK34_%=:
s_waitcnt lgkmcnt(0)
MM a[16:19],a[128:131],v[16:19],a[16:19]
s_waitcnt vmcnt(10)
s_barrier
MM a[16:19],a[132:135],v[20:23],a[16:19]
v_rcp_f32 v152, v246
MM a[24:27],a[132:135],v[28:31],a[24:27]
v_mov_b32 v153, v152
MM a[24:27],a[128:131],v[24:27],a[24:27]
s_nop 0
v_permlane32_swap_b32 v152, v153
MM a[28:31],a[136:139],v[24:27],a[28:31]
v_mov_b32 v157, v153
MM a[28:31],a[140:143],v[28:31],a[28:31]
s_nop 0
v_permlane16_swap_b32 v153, v157
MM a[20:23],a[140:143],v[20:23],a[20:23]
s_mul_i32 s78, s49, 17408
s_add_u32 s78, s83, s78
s_add_u32 s78, s78, 8192
MM a[20:23],a[136:139],v[16:19],a[20:23]
s_add_u32 s79, s78, 4096
MM a[48:51],a[144:147],v[16:19],a[48:51]
TR a[128:129],v230 offset:49152
TR a[130:131],v230 offset:51200
MM a[48:51],a[148:151],v[20:23],a[48:51]
MM a[56:59],a[148:151],v[28:31],a[56:59]
TR a[132:133],v230 offset:53248
TR a[134:135],v230 offset:55296
MM a[56:59],a[144:147],v[24:27],a[56:59]
AR v80,a16
AR v81,a17
MM a[60:63],a[152:155],v[24:27],a[60:63]
TR a[136:137],v230 offset:57344
TR a[138:139],v230 offset:59392
MM a[60:63],a[156:159],v[28:31],a[60:63]
AR v82,a18
AR v83,a19
MM a[52:55],a[156:159],v[20:23],a[52:55]
TR a[140:141],v230 offset:61440
TR a[142:143],v230 offset:63488
MM a[52:55],a[152:155],v[16:19],a[52:55]
AR v84,a20
AR v85,a21
MM a[80:83],a[160:163],v[16:19],a[80:83]
TR a[144:145],v231 offset:49152
TR a[146:147],v231 offset:51200
MM a[80:83],a[164:167],v[20:23],a[80:83]
AR v86,a22
AR v87,a23
MM a[88:91],a[164:167],v[28:31],a[88:91]
TR a[148:149],v231 offset:53248
TR a[150:151],v231 offset:55296
MM a[88:91],a[160:163],v[24:27],a[88:91]
s_waitcnt vmcnt(2)
v_cmp_ne_u32 vcc, s69, v242
s_cbranch_vccnz LRPO0_%=
LRPO0b_%=:
v_mul_f32 v80, v80, v153
MM a[92:95],a[168:171],v[24:27],a[92:95]
s_xor_b32 s92, s49, 1
TR a[152:153],v231 offset:57344
TR a[154:155],v231 offset:59392
v_mul_f32 v81, v81, v153
v_mul_f32 v82, v82, v153
MM a[92:95],a[172:175],v[28:31],a[92:95]
s_mul_i32 s92, s92, 17408
s_add_u32 s92, s83, s92
v_mul_f32 v83, v83, v153
MM a[84:87],a[172:175],v[20:23],a[84:87]
TR a[156:157],v231 offset:61440
TR a[158:159],v231 offset:63488
v_mul_f32 v84, v84, v153
v_mul_f32 v85, v85, v153
MM a[84:87],a[168:171],v[16:19],a[84:87]
buffer_load_dwordx4 a[0:3], v248, s[20:23], s92 offen offset:0 sc1
v_mul_f32 v86, v86, v153
v_mul_f32 v87, v87, v153
MM a[112:115],a[176:179],v[16:19],a[112:115]
TR a[160:161],v230 offset:49664
TR a[162:163],v230 offset:51712
CV v168,v80,v81
MM a[112:115],a[180:183],v[20:23],a[112:115]
buffer_load_dwordx4 a[32:35], v248, s[20:23], s92 offen offset:1024 sc1
CV v169,v82,v83
CV v170,v84,v85
MM a[120:123],a[180:183],v[28:31],a[120:123]
TR a[164:165],v230 offset:53760
TR a[166:167],v230 offset:55808
CV v171,v86,v87
MM a[120:123],a[176:179],v[24:27],a[120:123]
buffer_load_dwordx4 a[64:67], v248, s[20:23], s92 offen offset:2048 sc1
MM a[124:127],a[184:187],v[24:27],a[124:127]
TR a[168:169],v230 offset:57856
TR a[170:171],v230 offset:59904
MM a[124:127],a[188:191],v[28:31],a[124:127]
MM a[116:119],a[188:191],v[20:23],a[116:119]
buffer_store_dwordx4 v[168:171], v248, s[20:23], s78 offen offset:0 sc1
TR a[172:173],v230 offset:61952
TR a[174:175],v230 offset:64000
AR v88,a24
AR v89,a25
AR v90,a26
AR v91,a27
AR v92,a28
MM a[116:119],a[184:187],v[16:19],a[116:119]
AR v93,a29
AR v94,a30
s_waitcnt lgkmcnt(15)
MM v[0:3],a[128:131],v[160:163],0
TR a[176:177],v231 offset:49664
)ASM"
R"ASM(
TR a[178:179],v231 offset:51712
AR v95,a31
MM v[0:3],a[132:135],v[176:179],v[0:3]
v_bfe_u32 v249, v248, 6, 2
v_lshlrev_b32 v249, 8, v249
v_mul_f32 v88, v88, v157
v_mul_f32 v89, v89, v157
MM v[0:3],a[136:139],v[192:195],v[0:3]
TR a[180:181],v231 offset:53760
TR a[182:183],v231 offset:55808
v_mul_f32 v90, v90, v157
v_mul_f32 v91, v91, v157
MM v[0:3],a[140:143],v[208:211],v[0:3]
buffer_load_dwordx4 a[96:99], v248, s[20:23], s92 offen offset:3072 sc1
v_lshrrev_b32 v250, 8, v248
v_lshl_add_u32 v249, v250, 6, v249
v_mul_f32 v92, v92, v157
MM v[8:11],a[140:143],v[212:215],0
s_add_u32 s92, s92, 4096
TR a[184:185],v231 offset:57856
TR a[186:187],v231 offset:59904
v_mul_f32 v93, v93, v157
v_mul_f32 v94, v94, v157
MM v[8:11],a[136:139],v[196:199],v[8:11]
v_and_b32 v251, 48, v248
v_add_u32 v249, v249, v251
v_mul_f32 v95, v95, v157
CV v172,v88,v89
MM v[8:11],a[132:135],v[180:183],v[8:11]
s_lshr_b32 s45, s49, 3
buffer_load_dwordx4 a[4:7], v248, s[20:23], s92 offen offset:0 sc1
TR a[188:189],v231 offset:61952
TR a[190:191],v231 offset:64000
CV v173,v90,v91
MM v[8:11],a[128:131],v[164:167],v[8:11]
s_waitcnt lgkmcnt(15)
v_bfe_u32 v245, v248, 6, 2
v_xor_b32 v251, s45, v245
CV v174,v92,v93
CV v175,v94,v95
MM v[4:7],a[144:147],v[160:163],0
buffer_store_dwordx4 v[172:175], v248, s[20:23], s79 offen offset:0 sc1
TR a[128:129],v228 offset:0
TR a[130:131],v228 offset:1024
MM v[4:7],a[148:151],v[176:179],v[4:7]
v_add_u32 v251, -1, v251
v_and_b32 v251, 0x40000000, v251
AR v112,a48
MM v[4:7],a[152:155],v[192:195],v[4:7]
TR a[132:133],v228 offset:2048
TR a[134:135],v228 offset:3072
AR v113,a49
AR v114,a50
MM v[4:7],a[156:159],v[208:211],v[4:7]
v_lshlrev_b32 v245, 6, v245
v_lshl_add_u32 v245, s45, 4, v245
AR v115,a51
MM v[12:15],a[156:159],v[212:215],0
TR a[136:137],v228 offset:4096
TR a[138:139],v228 offset:5120
AR v116,a52
AR v117,a53
MM v[12:15],a[152:155],v[196:199],v[12:15]
v_mov_b32 v234, v249
v_add_u32 v249, v249, v251
AR v118,a54
AR v119,a55
MM v[12:15],a[148:151],v[180:183],v[12:15]
buffer_load_dwordx4 a[36:39], v248, s[20:23], s92 offen offset:1024 sc1
TR a[140:141],v228 offset:6144
TR a[142:143],v228 offset:7168
v_mul_f32 v112, v112, v153
MM v[12:15],a[144:147],v[164:167],v[12:15]
s_waitcnt lgkmcnt(15)
v_bfe_u32 v251, v248, 4, 2
v_lshl_add_u32 v245, v251, 2, v245
v_mul_f32 v113, v113, v153
v_mul_f32 v114, v114, v153
MM v[32:35],a[160:163],v[160:163],0
s_lshl_b32 s47, %[sb], 10
TR a[144:145],v229 offset:0
TR a[146:147],v229 offset:1024
v_mul_f32 v115, v115, v153
v_mul_f32 v116, v116, v153
MM v[32:35],a[164:167],v[176:179],v[32:35]
s_and_b32 s47, s47, 1024
s_add_u32 s46, s95, s47
s_sub_u32 s47, s95, s47
v_mul_f32 v117, v117, v153
MM v[32:35],a[168:171],v[192:195],v[32:35]
TR a[148:149],v229 offset:2048
TR a[150:151],v229 offset:3072
v_mul_f32 v118, v118, v153
v_mul_f32 v119, v119, v153
MM v[32:35],a[172:175],v[208:211],v[32:35]
s_bitcmp1_b32 s49, 4
s_cselect_b32 s44, -1, 1
s_mul_i32 s44, s44, 331776
buffer_load_dwordx4 a[68:71], v248, s[20:23], s92 offen offset:2048 sc1
CV v184,v112,v113
MM v[40:43],a[172:175],v[212:215],0
s_add_u32 s44, s44, s16
TR a[152:153],v229 offset:4096
TR a[154:155],v229 offset:5120
CV v185,v114,v115
CV v186,v116,v117
MM v[40:43],a[168:171],v[196:199],v[40:43]
s_xor_b32 s45, s68, 64
buffer_load_dwordx4 a[100:103], v248, s[20:23], s92 offen offset:3072 sc1
CV v187,v118,v119
MM v[40:43],a[164:167],v[180:183],v[40:43]
s_bfe_u32 s80, s49, 131074
TR a[156:157],v229 offset:6144
TR a[158:159],v229 offset:7168
v_xor_b32 v250, s80, v250
v_mul_u32_u24 v250, 82944, v250
v_and_b32 v252, 240, v248
v_add_u32 v250, v250, v252
v_lshrrev_b32 v251, 8, v248
v_mul_u32_u24 v251, 1280, v251
v_bfe_u32 v252, v248, 4, 3
v_lshl_add_u32 v251, v252, 3, v251
v_bfe_u32 v252, v248, 7, 1
v_mul_u32_u24 v254, 40960, v252
v_add_u32 v254, v254, v251
v_lshl_add_u32 v251, v252, 6, v251
v_lshrrev_b32 v253, 8, v248
MM v[40:43],a[160:163],v[164:167],v[40:43]
s_waitcnt lgkmcnt(15)
v_and_b32 v252, 240, v248
v_lshl_add_u32 v232, v253, 2, v252
s_waitcnt vmcnt(10)
s_barrier
MM v[36:39],a[176:179],v[160:163],0
TR a[160:161],v230 offset:16384
TR a[162:163],v230 offset:18432
MM v[36:39],a[180:183],v[176:179],v[36:39]
buffer_store_dwordx4 v[184:187], v248, s[20:23], s78 offen offset:1024 sc1
AR v120,a56
AR v121,a57
AR v122,a58
AR v123,a59
AR v124,a60
v_xor_b32 v253, s80, v253
MM v[36:39],a[184:187],v[192:195],v[36:39]
TR a[164:165],v230 offset:20480
TR a[166:167],v230 offset:22528
AR v125,a61
AR v126,a62
MM v[36:39],a[188:191],v[208:211],v[36:39]
AR v127,a63
v_lshlrev_b32 v253, 4, v253
v_mul_f32 v120, v120, v157
MM v[44:47],a[188:191],v[212:215],0
TR a[168:169],v230 offset:24576
TR a[170:171],v230 offset:26624
v_mul_f32 v121, v121, v157
v_mov_b32 v233, s69
MM v[44:47],a[184:187],v[196:199],v[44:47]
s_add_u32 s80, s18, 131072
v_mul_f32 v122, v122, v157
v_mul_f32 v123, v123, v157
MM v[44:47],a[180:183],v[180:183],v[44:47]
buffer_load_dwordx2 v[224:225], v251, s[20:23], s80 offen offset:0 sc1
TR a[172:173],v230 offset:28672
TR a[174:175],v230 offset:30720
v_mul_f32 v124, v124, v157
MM v[44:47],a[176:179],v[164:167],v[44:47]
s_waitcnt lgkmcnt(15)
v_mul_f32 v125, v125, v157
v_mul_f32 v126, v126, v157
MM v[128:131],a[128:131],v[160:163],0
s_add_u32 s80, s18, 131200
TR a[176:177],v231 offset:16384
TR a[178:179],v231 offset:18432
v_mul_f32 v127, v127, v157
CV v188,v120,v121
MM v[128:131],a[132:135],v[176:179],v[128:131]
buffer_load_dwordx2 v[226:227], v251, s[20:23], s80 offen offset:0 sc1
CV v189,v122,v123
CV v190,v124,v125
MM v[128:131],a[136:139],v[192:195],v[128:131]
TR a[180:181],v231 offset:20480
TR a[182:183],v231 offset:22528
CV v191,v126,v127
MM v[128:131],a[140:143],v[208:211],v[128:131]
s_add_u32 s80, s18, 172032
buffer_load_dwordx2 v[236:237], v251, s[20:23], s80 offen offset:0 sc1
MM v[136:139],a[140:143],v[212:215],0
TR a[184:185],v231 offset:24576
TR a[186:187],v231 offset:26624
MM v[136:139],a[136:139],v[196:199],v[136:139]
s_add_u32 s80, s18, 172160
buffer_load_dwordx2 v[238:239], v251, s[20:23], s80 offen offset:0 sc1
MM v[136:139],a[132:135],v[180:183],v[136:139]
buffer_store_dwordx4 v[188:191], v248, s[20:23], s79 offen offset:1024 sc1
TR a[188:189],v231 offset:28672
TR a[190:191],v231 offset:30720
AR v80,a80
AR v81,a81
AR v82,a82
AR v83,a83
AR v84,a84
MM v[136:139],a[128:131],v[164:167],v[136:139]
s_waitcnt lgkmcnt(15)
AR v85,a85
AR v86,a86
MM v[132:135],a[144:147],v[160:163],0
s_add_u32 s80, s63, 131072
buffer_load_dwordx2 v[246:247], v254, s[20:23], s80 offen offset:0 sc1
TR a[128:129],v230 offset:16896
TR a[130:131],v230 offset:18944
AR v87,a87
MM v[132:135],a[148:151],v[176:179],v[132:135]
v_mul_f32 v80, v80, v153
v_mul_f32 v81, v81, v153
MM v[132:135],a[152:155],v[192:195],v[132:135]
TR a[132:133],v230 offset:20992
TR a[134:135],v230 offset:23040
v_mul_f32 v82, v82, v153
MM v[132:135],a[156:159],v[208:211],v[132:135]
v_mul_f32 v83, v83, v153
v_mul_f32 v84, v84, v153
MM v[140:143],a[156:159],v[212:215],0
TR a[136:137],v230 offset:25088
TR a[138:139],v230 offset:27136
v_mul_f32 v85, v85, v153
MM v[140:143],a[152:155],v[196:199],v[140:143]
v_mul_f32 v86, v86, v153
v_mul_f32 v87, v87, v153
MM v[140:143],a[148:151],v[180:183],v[140:143]
TR a[140:141],v230 offset:29184
TR a[142:143],v230 offset:31232
CV v200,v80,v81
CV v201,v82,v83
MM v[140:143],a[144:147],v[164:167],v[140:143]
s_waitcnt lgkmcnt(15)
CV v202,v84,v85
CV v203,v86,v87
MM v[64:67],a[160:163],v[160:163],0
TR a[144:145],v231 offset:16896
TR a[146:147],v231 offset:18944
MM v[64:67],a[164:167],v[176:179],v[64:67]
MM v[64:67],a[168:171],v[192:195],v[64:67]
TR a[148:149],v231 offset:20992
TR a[150:151],v231 offset:23040
MM v[64:67],a[172:175],v[208:211],v[64:67]
buffer_store_dwordx4 v[200:203], v248, s[20:23], s78 offen offset:2048 sc1
AR v88,a88
AR v89,a89
AR v90,a90
AR v91,a91
MM v[72:75],a[172:175],v[212:215],0
TR a[152:153],v231 offset:25088
TR a[154:155],v231 offset:27136
AR v92,a92
MM v[72:75],a[168:171],v[196:199],v[72:75]
AR v93,a93
AR v94,a94
MM v[72:75],a[164:167],v[180:183],v[72:75]
TR a[156:157],v231 offset:29184
TR a[158:159],v231 offset:31232
AR v95,a95
v_mul_f32 v88, v88, v157
MM v[72:75],a[160:163],v[164:167],v[72:75]
s_waitcnt lgkmcnt(15)
v_mul_f32 v89, v89, v157
v_mul_f32 v90, v90, v157
MM v[68:71],a[176:179],v[160:163],0
TR a[160:161],v244 offset:49152
TR a[162:163],v244 offset:51200
v_mul_f32 v91, v91, v157
MM v[68:71],a[180:183],v[176:179],v[68:71]
v_mul_f32 v92, v92, v157
v_mul_f32 v93, v93, v157
MM v[68:71],a[184:187],v[192:195],v[68:71]
TR a[164:165],v244 offset:53248
TR a[166:167],v244 offset:55296
v_mul_f32 v94, v94, v157
MM v[68:71],a[188:191],v[208:211],v[68:71]
v_mul_f32 v95, v95, v157
CV v204,v88,v89
MM v[76:79],a[188:191],v[212:215],0
s_nop 0
LPG21_%=:
s_cbranch_execz LTR3_%=
TR a[168:169],v244 offset:57344
TR a[170:171],v244 offset:59392
CV v205,v90,v91
CV v206,v92,v93
MM v[76:79],a[184:187],v[196:199],v[76:79]
CV v207,v94,v95
MM v[76:79],a[180:183],v[180:183],v[76:79]
TR a[172:173],v244 offset:61440
TR a[174:175],v244 offset:63488
MM v[76:79],a[176:179],v[164:167],v[76:79]
s_waitcnt lgkmcnt(15)
MM v[96:99],a[128:131],v[160:163],0
s_xor_b32 s92, s49, 2
buffer_store_dwordx4 v[204:207], v248, s[20:23], s79 offen offset:2048 sc1
TR a[176:177],v235 offset:49152
TR a[178:179],v235 offset:51200
AR v112,a112
AR v113,a113
AR v114,a114
MM v[96:99],a[132:135],v[176:179],v[96:99]
s_mul_i32 s92, s92, 17408
s_add_u32 s92, s83, s92
AR v115,a115
AR v116,a116
MM v[96:99],a[136:139],v[192:195],v[96:99]
TR a[180:181],v235 offset:53248
TR a[182:183],v235 offset:55296
AR v117,a117
MM v[96:99],a[140:143],v[208:211],v[96:99]
buffer_load_dwordx4 a[8:11], v248, s[20:23], s92 offen offset:0 sc1
AR v118,a118
AR v119,a119
MM v[104:107],a[140:143],v[212:215],0
TR a[184:185],v235 offset:57344
TR a[186:187],v235 offset:59392
v_mul_f32 v112, v112, v153
v_mul_f32 v113, v113, v153
MM v[104:107],a[136:139],v[196:199],v[104:107]
buffer_load_dwordx4 a[40:43], v248, s[20:23], s92 offen offset:1024 sc1
v_mul_f32 v114, v114, v153
MM v[104:107],a[132:135],v[180:183],v[104:107]
TR a[188:189],v235 offset:61440
TR a[190:191],v235 offset:63488
v_mul_f32 v115, v115, v153
v_mul_f32 v116, v116, v153
MM v[104:107],a[128:131],v[164:167],v[104:107]
s_waitcnt lgkmcnt(15)
buffer_load_dwordx4 a[72:75], v248, s[20:23], s92 offen offset:2048 sc1
v_mul_f32 v117, v117, v153
v_mul_f32 v118, v118, v153
MM v[100:103],a[144:147],v[160:163],0
TR a[128:129],v244 offset:49664
TR a[130:131],v244 offset:51712
v_mul_f32 v119, v119, v153
MM v[100:103],a[148:151],v[176:179],v[100:103]
CV v216,v112,v113
CV v217,v114,v115
MM v[100:103],a[152:155],v[192:195],v[100:103]
TR a[132:133],v244 offset:53760
TR a[134:135],v244 offset:55808
CV v218,v116,v117
MM v[100:103],a[156:159],v[208:211],v[100:103]
buffer_load_dwordx4 a[104:107], v248, s[20:23], s92 offen offset:3072 sc1
CV v219,v118,v119
MM v[108:111],a[156:159],v[212:215],0
s_add_u32 s92, s92, 4096
TR a[136:137],v244 offset:57856
TR a[138:139],v244 offset:59904
MM v[108:111],a[152:155],v[196:199],v[108:111]
buffer_load_dwordx4 a[12:15], v248, s[20:23], s92 offen offset:0 sc1
MM v[108:111],a[148:151],v[180:183],v[108:111]
TR a[140:141],v244 offset:61952
TR a[142:143],v244 offset:64000
MM v[108:111],a[144:147],v[164:167],v[108:111]
s_waitcnt lgkmcnt(15)
s_waitcnt vmcnt(19)
MM v[0:3],a[160:163],a[0:3],v[0:3]
buffer_store_dwordx4 v[216:219], v248, s[20:23], s78 offen offset:3072 sc1
TR a[144:145],v235 offset:49664
TR a[146:147],v235 offset:51712
AR v120,a120
AR v121,a121
AR v122,a122
AR v123,a123
AR v124,a124
AR v125,a125
AR v126,a126
MM v[0:3],a[164:167],a[32:35],v[0:3]
AR v127,a127
v_mul_f32 v120, v120, v157
MM v[0:3],a[168:171],a[64:67],v[0:3]
TR a[148:149],v235 offset:53760
TR a[150:151],v235 offset:55808
v_mul_f32 v121, v121, v157
v_mul_f32 v122, v122, v157
MM v[0:3],a[172:175],a[96:99],v[0:3]
s_waitcnt vmcnt(15)
buffer_load_dwordx4 a[44:47], v248, s[20:23], s92 offen offset:1024 sc1
v_mul_f32 v123, v123, v157
MM v[8:11],a[172:175],a[100:103],v[8:11]
TR a[152:153],v235 offset:57856
TR a[154:155],v235 offset:59904
v_mul_f32 v124, v124, v157
)ASM"
R"ASM(
v_mul_f32 v125, v125, v157
MM v[8:11],a[168:171],a[68:71],v[8:11]
v_mul_f32 v126, v126, v157
v_mul_f32 v127, v127, v157
MM v[8:11],a[164:167],a[36:39],v[8:11]
TR a[156:157],v235 offset:61952
TR a[158:159],v235 offset:64000
CV v220,v120,v121
MM v[8:11],a[160:163],a[4:7],v[8:11]
s_waitcnt lgkmcnt(15)
buffer_load_dwordx4 a[76:79], v248, s[20:23], s92 offen offset:2048 sc1
CV v221,v122,v123
CV v222,v124,v125
MM v[4:7],a[176:179],a[0:3],v[4:7]
TR a[160:161],v228 offset:8192
TR a[162:163],v228 offset:9216
CV v223,v126,v127
MM v[4:7],a[180:183],a[32:35],v[4:7]
buffer_load_dwordx4 a[108:111], v248, s[20:23], s92 offen offset:3072 sc1
MM v[4:7],a[184:187],a[64:67],v[4:7]
TR a[164:165],v228 offset:10240
TR a[166:167],v228 offset:11264
MM v[4:7],a[188:191],a[96:99],v[4:7]
MM v[12:15],a[188:191],a[100:103],v[12:15]
buffer_store_dwordx4 v[220:223], v248, s[20:23], s79 offen offset:3072 sc1
TR a[168:169],v228 offset:12288
TR a[170:171],v228 offset:13312
MM v[12:15],a[184:187],a[68:71],v[12:15]
MM v[12:15],a[180:183],a[36:39],v[12:15]
TR a[172:173],v228 offset:14336
TR a[174:175],v228 offset:15360
MM v[12:15],a[176:179],a[4:7],v[12:15]
s_waitcnt lgkmcnt(15)
MM v[32:35],a[128:131],a[0:3],v[32:35]
TR a[176:177],v229 offset:8192
TR a[178:179],v229 offset:9216
MM v[32:35],a[132:135],a[32:35],v[32:35]
MM v[32:35],a[136:139],a[64:67],v[32:35]
TR a[180:181],v229 offset:10240
TR a[182:183],v229 offset:11264
MM v[32:35],a[140:143],a[96:99],v[32:35]
MM v[40:43],a[140:143],a[100:103],v[40:43]
TR a[184:185],v229 offset:12288
TR a[186:187],v229 offset:13312
MM v[40:43],a[136:139],a[68:71],v[40:43]
MM v[40:43],a[132:135],a[36:39],v[40:43]
TR a[188:189],v229 offset:14336
TR a[190:191],v229 offset:15360
MM v[40:43],a[128:131],a[4:7],v[40:43]
s_waitcnt lgkmcnt(15)
MM v[36:39],a[144:147],a[0:3],v[36:39]
s_waitcnt vmcnt(12)
TR a[128:129],v230 offset:0
TR a[130:131],v230 offset:2048
MM v[36:39],a[148:151],a[32:35],v[36:39]
v_lshlrev_b32 v112, 16, v224
v_and_b32 v113, 0xffff0000, v224
MM v[36:39],a[152:155],a[64:67],v[36:39]
TR a[132:133],v230 offset:4096
TR a[134:135],v230 offset:6144
MM v[36:39],a[156:159],a[96:99],v[36:39]
v_lshlrev_b32 v114, 16, v225
v_and_b32 v115, 0xffff0000, v225
MM v[44:47],a[156:159],a[100:103],v[44:47]
TR a[136:137],v230 offset:8192
TR a[138:139],v230 offset:10240
MM v[44:47],a[152:155],a[68:71],v[44:47]
v_mul_f32 v116, 0xbfb8aa3b, v112
v_mul_f32 v117, 0xbfb8aa3b, v113
MM v[44:47],a[148:151],a[36:39],v[44:47]
TR a[140:141],v230 offset:12288
TR a[142:143],v230 offset:14336
MM v[44:47],a[144:147],a[4:7],v[44:47]
s_waitcnt lgkmcnt(15)
v_mul_f32 v118, 0xbfb8aa3b, v114
v_mul_f32 v119, 0xbfb8aa3b, v115
MM v[128:131],a[160:163],a[0:3],v[128:131]
TR a[144:145],v231 offset:0
TR a[146:147],v231 offset:2048
MM v[128:131],a[164:167],a[32:35],v[128:131]
EX v116,v116
MM v[128:131],a[168:171],a[64:67],v[128:131]
TR a[148:149],v231 offset:4096
TR a[150:151],v231 offset:6144
MM v[128:131],a[172:175],a[96:99],v[128:131]
EX v117,v117
MM v[136:139],a[172:175],a[100:103],v[136:139]
TR a[152:153],v231 offset:8192
TR a[154:155],v231 offset:10240
MM v[136:139],a[168:171],a[68:71],v[136:139]
EX v118,v118
MM v[136:139],a[164:167],a[36:39],v[136:139]
TR a[156:157],v231 offset:12288
TR a[158:159],v231 offset:14336
MM v[136:139],a[160:163],a[4:7],v[136:139]
s_waitcnt lgkmcnt(15)
EX v119,v119
MM v[132:135],a[176:179],a[0:3],v[132:135]
s_xor_b32 s92, s49, 3
TR a[160:161],v230 offset:512
TR a[162:163],v230 offset:2560
MM v[132:135],a[180:183],a[32:35],v[132:135]
s_mul_i32 s92, s92, 17408
s_add_u32 s92, s83, s92
v_add_f32 v116, 1.0, v116
MM v[132:135],a[184:187],a[64:67],v[132:135]
TR a[164:165],v230 offset:4608
TR a[166:167],v230 offset:6656
MM v[132:135],a[188:191],a[96:99],v[132:135]
buffer_load_dwordx4 a[192:195], v248, s[20:23], s92 offen offset:0 sc1
MM v[140:143],a[188:191],a[100:103],v[140:143]
TR a[168:169],v230 offset:8704
TR a[170:171],v230 offset:10752
MM v[140:143],a[184:187],a[68:71],v[140:143]
buffer_load_dwordx4 a[208:211], v248, s[20:23], s92 offen offset:1024 sc1
MM v[140:143],a[180:183],a[36:39],v[140:143]
TR a[172:173],v230 offset:12800
TR a[174:175],v230 offset:14848
MM v[140:143],a[176:179],a[4:7],v[140:143]
s_waitcnt lgkmcnt(15)
buffer_load_dwordx4 a[224:227], v248, s[20:23], s92 offen offset:2048 sc1
s_waitcnt vmcnt(9)
MM v[0:3],a[128:131],a[8:11],v[0:3]
TR a[176:177],v231 offset:512
TR a[178:179],v231 offset:2560
MM v[0:3],a[132:135],a[40:43],v[0:3]
v_add_f32 v117, 1.0, v117
v_add_f32 v118, 1.0, v118
MM v[0:3],a[136:139],a[72:75],v[0:3]
TR a[180:181],v231 offset:4608
TR a[182:183],v231 offset:6656
MM v[0:3],a[140:143],a[104:107],v[0:3]
s_waitcnt vmcnt(4)
buffer_load_dwordx4 a[240:243], v248, s[20:23], s92 offen offset:3072 sc1
MM v[8:11],a[140:143],a[108:111],v[8:11]
s_add_u32 s92, s92, 4096
TR a[184:185],v231 offset:8704
TR a[186:187],v231 offset:10752
MM v[8:11],a[136:139],a[76:79],v[8:11]
buffer_load_dwordx4 a[196:199], v248, s[20:23], s92 offen offset:0 sc1
MM v[8:11],a[132:135],a[44:47],v[8:11]
TR a[188:189],v231 offset:12800
TR a[190:191],v231 offset:14848
MM v[8:11],a[128:131],a[12:15],v[8:11]
s_waitcnt lgkmcnt(15)
v_add_f32 v119, 1.0, v119
MM v[4:7],a[144:147],a[8:11],v[4:7]
TR a[128:129],v228 offset:16384
TR a[130:131],v228 offset:17408
MM v[4:7],a[148:151],a[40:43],v[4:7]
buffer_load_dwordx4 a[212:215], v248, s[20:23], s92 offen offset:1024 sc1
MM v[4:7],a[152:155],a[72:75],v[4:7]
TR a[132:133],v228 offset:18432
TR a[134:135],v228 offset:19456
MM v[4:7],a[156:159],a[104:107],v[4:7]
buffer_load_dwordx4 a[228:231], v248, s[20:23], s92 offen offset:2048 sc1
MM v[12:15],a[156:159],a[108:111],v[12:15]
TR a[136:137],v228 offset:20480
TR a[138:139],v228 offset:21504
MM v[12:15],a[152:155],a[76:79],v[12:15]
v_rcp_f32 v116, v116
MM v[12:15],a[148:151],a[44:47],v[12:15]
TR a[140:141],v228 offset:22528
TR a[142:143],v228 offset:23552
MM v[12:15],a[144:147],a[12:15],v[12:15]
s_waitcnt lgkmcnt(15)
buffer_load_dwordx4 a[244:247], v248, s[20:23], s92 offen offset:3072 sc1
MM v[32:35],a[160:163],a[8:11],v[32:35]
TR a[144:145],v229 offset:16384
TR a[146:147],v229 offset:17408
MM v[32:35],a[164:167],a[40:43],v[32:35]
s_add_u32 m0, s89, 32768
v_rcp_f32 v117, v117
MM v[32:35],a[168:171],a[72:75],v[32:35]
s_add_u32 s66, s87, 128
TR a[148:149],v229 offset:18432
TR a[150:151],v229 offset:19456
MM v[32:35],a[172:175],a[104:107],v[32:35]
buffer_load_dwordx4 v240, s[24:27], s66 offen offset:0 lds
MM v[40:43],a[172:175],a[108:111],v[40:43]
s_add_u32 m0, s89, 33792
TR a[152:153],v229 offset:20480
TR a[154:155],v229 offset:21504
MM v[40:43],a[168:171],a[76:79],v[40:43]
s_add_u32 s66, s87, 10368
buffer_load_dwordx4 v240, s[24:27], s66 offen offset:0 lds
MM v[40:43],a[164:167],a[44:47],v[40:43]
s_add_u32 m0, s89, 40960
TR a[156:157],v229 offset:22528
TR a[158:159],v229 offset:23552
MM v[40:43],a[160:163],a[12:15],v[40:43]
s_waitcnt lgkmcnt(15)
v_rcp_f32 v118, v118
MM v[36:39],a[176:179],a[8:11],v[36:39]
s_add_u32 s66, s87, 82048
TR a[160:161],v244 offset:0
TR a[162:163],v244 offset:2048
MM v[36:39],a[180:183],a[40:43],v[36:39]
buffer_load_dwordx4 v240, s[24:27], s66 offen offset:0 lds
MM v[36:39],a[184:187],a[72:75],v[36:39]
s_add_u32 m0, s89, 41984
TR a[164:165],v244 offset:4096
TR a[166:167],v244 offset:6144
MM v[36:39],a[188:191],a[104:107],v[36:39]
s_add_u32 s66, s87, 92288
buffer_load_dwordx4 v240, s[24:27], s66 offen offset:0 lds
MM v[44:47],a[188:191],a[108:111],v[44:47]
s_add_u32 m0, s89, 98304
TR a[168:169],v244 offset:8192
TR a[170:171],v244 offset:10240
MM v[44:47],a[184:187],a[76:79],v[44:47]
s_add_u32 s66, s88, 128
buffer_load_dwordx4 v240, s[24:27], s66 offen offset:0 lds
MM v[44:47],a[180:183],a[44:47],v[44:47]
s_add_u32 m0, s89, 99328
TR a[172:173],v244 offset:12288
TR a[174:175],v244 offset:14336
MM v[44:47],a[176:179],a[12:15],v[44:47]
s_waitcnt lgkmcnt(15)
v_rcp_f32 v119, v119
MM v[128:131],a[128:131],a[8:11],v[128:131]
s_add_u32 s66, s88, 10368
TR a[176:177],v235 offset:0
TR a[178:179],v235 offset:2048
MM v[128:131],a[132:135],a[40:43],v[128:131]
buffer_load_dwordx4 v240, s[24:27], s66 offen offset:0 lds
MM v[128:131],a[136:139],a[72:75],v[128:131]
s_add_u32 m0, s89, 106496
TR a[180:181],v235 offset:4096
TR a[182:183],v235 offset:6144
MM v[128:131],a[140:143],a[104:107],v[128:131]
s_add_u32 s66, s88, 82048
buffer_load_dwordx4 v240, s[24:27], s66 offen offset:0 lds
MM v[136:139],a[140:143],a[108:111],v[136:139]
s_add_u32 m0, s89, 107520
TR a[184:185],v235 offset:8192
TR a[186:187],v235 offset:10240
MM v[136:139],a[136:139],a[76:79],v[136:139]
s_add_u32 s66, s88, 92288
v_mul_f32 v116, v112, v116
v_mul_f32 v117, v113, v117
MM v[136:139],a[132:135],a[44:47],v[136:139]
TR a[188:189],v235 offset:12288
TR a[190:191],v235 offset:14336
MM v[136:139],a[128:131],a[12:15],v[136:139]
s_waitcnt lgkmcnt(15)
buffer_load_dwordx4 v240, s[24:27], s66 offen offset:0 lds
MM v[132:135],a[144:147],a[8:11],v[132:135]
s_lshl_b32 s97, s84, 2
TR a[128:129],v244 offset:512
TR a[130:131],v244 offset:2560
MM v[132:135],a[148:151],a[40:43],v[132:135]
s_add_u32 s97, s97, s96
s_add_u32 s97, s97, 16
s_waitcnt vmcnt(16)
MM v[132:135],a[152:155],a[72:75],v[132:135]
TR a[132:133],v244 offset:4608
TR a[134:135],v244 offset:6656
MM v[132:135],a[156:159],a[104:107],v[132:135]
v_mov_b32 v242, s69
s_mov_b64 exec, 1
buffer_store_dword v242, off, s[20:23], s97 offset:0 sc1
s_mov_b64 exec, -1
MM v[140:143],a[156:159],a[108:111],v[140:143]
TR a[136:137],v244 offset:8704
TR a[138:139],v244 offset:10752
MM v[140:143],a[152:155],a[76:79],v[140:143]
v_mul_f32 v118, v114, v118
v_mul_f32 v119, v115, v119
MM v[140:143],a[148:151],a[44:47],v[140:143]
TR a[140:141],v244 offset:12800
TR a[142:143],v244 offset:14848
MM v[140:143],a[144:147],a[12:15],v[140:143]
s_waitcnt lgkmcnt(15)
s_waitcnt vmcnt(13)
MM v[0:3],a[160:163],a[192:195],v[0:3]
TR a[144:145],v235 offset:512
TR a[146:147],v235 offset:2560
MM v[0:3],a[164:167],a[208:211],v[0:3]
MM v[0:3],a[168:171],a[224:227],v[0:3]
TR a[148:149],v235 offset:4608
TR a[150:151],v235 offset:6656
MM v[0:3],a[172:175],a[240:243],v[0:3]
s_waitcnt vmcnt(9)
MM v[8:11],a[172:175],a[244:247],v[8:11]
TR a[152:153],v235 offset:8704
TR a[154:155],v235 offset:10752
MM v[8:11],a[168:171],a[228:231],v[8:11]
MM v[8:11],a[164:167],a[212:215],v[8:11]
TR a[156:157],v235 offset:12800
TR a[158:159],v235 offset:14848
MM v[8:11],a[160:163],a[196:199],v[8:11]
s_waitcnt lgkmcnt(15)
MM v[4:7],a[176:179],a[192:195],v[4:7]
TR a[160:161],v228 offset:24576
TR a[162:163],v228 offset:25600
MM v[4:7],a[180:183],a[208:211],v[4:7]
MM v[4:7],a[184:187],a[224:227],v[4:7]
TR a[164:165],v228 offset:26624
TR a[166:167],v228 offset:27648
MM v[4:7],a[188:191],a[240:243],v[4:7]
MM v[12:15],a[188:191],a[244:247],v[12:15]
TR a[168:169],v228 offset:28672
TR a[170:171],v228 offset:29696
MM v[12:15],a[184:187],a[228:231],v[12:15]
MM v[12:15],a[180:183],a[212:215],v[12:15]
TR a[172:173],v228 offset:30720
TR a[174:175],v228 offset:31744
MM v[12:15],a[176:179],a[196:199],v[12:15]
s_waitcnt lgkmcnt(15)
MM v[32:35],a[128:131],a[192:195],v[32:35]
TR a[176:177],v229 offset:24576
TR a[178:179],v229 offset:25600
MM v[32:35],a[132:135],a[208:211],v[32:35]
MM v[32:35],a[136:139],a[224:227],v[32:35]
TR a[180:181],v229 offset:26624
TR a[182:183],v229 offset:27648
MM v[32:35],a[140:143],a[240:243],v[32:35]
s_add_u32 s67, s46, 0
CV v0,v0,v1
CV v1,v2,v3
MM v[40:43],a[140:143],a[244:247],v[40:43]
TR a[184:185],v229 offset:28672
TR a[186:187],v229 offset:29696
MM v[40:43],a[136:139],a[228:231],v[40:43]
CV v2,v4,v5
CV v3,v6,v7
MM v[40:43],a[132:135],a[212:215],v[40:43]
TR a[188:189],v229 offset:30720
TR a[190:191],v229 offset:31744
MM v[40:43],a[128:131],a[196:199],v[40:43]
s_waitcnt lgkmcnt(15)
buffer_store_dwordx4 v[0:3], v249, s[20:23], s67 offen offset:0 sc1
MM v[36:39],a[144:147],a[192:195],v[36:39]
s_add_u32 s67, s47, 0
TR a[128:129],v244 offset:16384
TR a[130:131],v244 offset:18432
MM v[36:39],a[148:151],a[208:211],v[36:39]
CV v8,v8,v9
CV v9,v10,v11
MM v[36:39],a[152:155],a[224:227],v[36:39]
TR a[132:133],v244 offset:20480
TR a[134:135],v244 offset:22528
MM v[36:39],a[156:159],a[240:243],v[36:39]
)ASM"
R"ASM(
CV v10,v12,v13
CV v11,v14,v15
MM v[44:47],a[156:159],a[244:247],v[44:47]
TR a[136:137],v244 offset:24576
TR a[138:139],v244 offset:26624
MM v[44:47],a[152:155],a[228:231],v[44:47]
buffer_store_dwordx4 v[8:11], v234, s[20:23], s67 offen offset:1024 sc1
MM v[44:47],a[148:151],a[212:215],v[44:47]
TR a[140:141],v244 offset:28672
TR a[142:143],v244 offset:30720
MM v[44:47],a[144:147],a[196:199],v[44:47]
s_waitcnt lgkmcnt(15)
ds_bpermute_b32 v160, v245, v0
ds_bpermute_b32 v161, v245, v1
MM v[128:131],a[160:163],a[192:195],v[128:131]
TR a[144:145],v235 offset:16384
TR a[146:147],v235 offset:18432
MM v[128:131],a[164:167],a[208:211],v[128:131]
ds_bpermute_b32 v162, v245, v2
ds_bpermute_b32 v163, v245, v3
MM v[128:131],a[168:171],a[224:227],v[128:131]
TR a[148:149],v235 offset:20480
TR a[150:151],v235 offset:22528
MM v[128:131],a[172:175],a[240:243],v[128:131]
CV v0,v116,v117
CV v1,v118,v119
MM v[136:139],a[172:175],a[244:247],v[136:139]
s_add_u32 s67, s46, 0
TR a[152:153],v235 offset:24576
TR a[154:155],v235 offset:26624
MM v[136:139],a[168:171],a[228:231],v[136:139]
CV v32,v32,v33
CV v33,v34,v35
MM v[136:139],a[164:167],a[212:215],v[136:139]
TR a[156:157],v235 offset:28672
TR a[158:159],v235 offset:30720
MM v[136:139],a[160:163],a[196:199],v[136:139]
s_waitcnt lgkmcnt(15)
CV v34,v36,v37
CV v35,v38,v39
s_waitcnt vmcnt(3)
s_barrier
MM v[132:135],a[176:179],a[192:195],v[132:135]
TR a[160:161],v230 offset:32768
TR a[162:163],v230 offset:34816
MM v[132:135],a[180:183],a[208:211],v[132:135]
buffer_store_dwordx4 v[32:35], v249, s[20:23], s67 offen offset:2048 sc1
MM v[132:135],a[184:187],a[224:227],v[132:135]
s_add_u32 s67, s47, 0
TR a[164:165],v230 offset:36864
TR a[166:167],v230 offset:38912
v_lshrrev_b32 v243, 2, v248
MM v[132:135],a[188:191],a[240:243],v[132:135]
CV v40,v40,v41
CV v41,v42,v43
MM v[140:143],a[188:191],a[244:247],v[140:143]
TR a[168:169],v230 offset:40960
TR a[170:171],v230 offset:43008
MM v[140:143],a[184:187],a[228:231],v[140:143]
CV v42,v44,v45
CV v43,v46,v47
v_min_u32 v243, 12, v243
MM v[140:143],a[180:183],a[212:215],v[140:143]
TR a[172:173],v230 offset:45056
TR a[174:175],v230 offset:47104
MM v[140:143],a[176:179],a[196:199],v[140:143]
s_waitcnt lgkmcnt(15)
buffer_store_dwordx4 v[40:43], v234, s[20:23], s67 offen offset:3072 sc1
MM v[64:67],a[128:131],a[0:3],v[64:67]
buffer_load_dword v242, v243, s[20:23], s96 offen offset:16 sc1
TR a[176:177],v231 offset:32768
TR a[178:179],v231 offset:34816
MM v[64:67],a[132:135],a[32:35],v[64:67]
ds_bpermute_b32 v176, v245, v32
ds_bpermute_b32 v177, v245, v33
MM v[64:67],a[136:139],a[64:67],v[64:67]
TR a[180:181],v231 offset:36864
TR a[182:183],v231 offset:38912
MM v[64:67],a[140:143],a[96:99],v[64:67]
ds_bpermute_b32 v178, v245, v34
ds_bpermute_b32 v179, v245, v35
MM v[72:75],a[140:143],a[100:103],v[72:75]
TR a[184:185],v231 offset:40960
TR a[186:187],v231 offset:43008
MM v[72:75],a[136:139],a[68:71],v[72:75]
v_lshlrev_b32 v112, 16, v226
v_and_b32 v113, 0xffff0000, v226
MM v[72:75],a[132:135],a[36:39],v[72:75]
s_add_u32 s67, s46, 8192
TR a[188:189],v231 offset:45056
TR a[190:191],v231 offset:47104
MM v[72:75],a[128:131],a[4:7],v[72:75]
s_waitcnt lgkmcnt(15)
CV v128,v128,v129
CV v129,v130,v131
MM v[68:71],a[144:147],a[0:3],v[68:71]
TR a[128:129],v244 offset:32768
TR a[130:131],v244 offset:34816
MM v[68:71],a[148:151],a[32:35],v[68:71]
CV v130,v132,v133
CV v131,v134,v135
MM v[68:71],a[152:155],a[64:67],v[68:71]
TR a[132:133],v244 offset:36864
TR a[134:135],v244 offset:38912
MM v[68:71],a[156:159],a[96:99],v[68:71]
buffer_store_dwordx4 v[128:131], v249, s[20:23], s67 offen offset:0 sc1
LPG22_%=:
s_cbranch_execz LTR3_%=
MM v[76:79],a[156:159],a[100:103],v[76:79]
s_add_u32 s67, s47, 8192
TR a[136:137],v244 offset:40960
TR a[138:139],v244 offset:43008
MM v[76:79],a[152:155],a[68:71],v[76:79]
CV v136,v136,v137
CV v137,v138,v139
MM v[76:79],a[148:151],a[36:39],v[76:79]
TR a[140:141],v244 offset:45056
TR a[142:143],v244 offset:47104
MM v[76:79],a[144:147],a[4:7],v[76:79]
s_waitcnt lgkmcnt(15)
CV v138,v140,v141
CV v139,v142,v143
MM v[64:67],a[160:163],a[8:11],v[64:67]
TR a[144:145],v235 offset:32768
TR a[146:147],v235 offset:34816
MM v[64:67],a[164:167],a[40:43],v[64:67]
buffer_store_dwordx4 v[136:139], v234, s[20:23], s67 offen offset:1024 sc1
MM v[64:67],a[168:171],a[72:75],v[64:67]
TR a[148:149],v235 offset:36864
TR a[150:151],v235 offset:38912
MM v[64:67],a[172:175],a[104:107],v[64:67]
ds_bpermute_b32 v164, v245, v128
ds_bpermute_b32 v165, v245, v129
MM v[72:75],a[172:175],a[108:111],v[72:75]
TR a[152:153],v235 offset:40960
TR a[154:155],v235 offset:43008
MM v[72:75],a[168:171],a[76:79],v[72:75]
ds_bpermute_b32 v166, v245, v130
ds_bpermute_b32 v167, v245, v131
MM v[72:75],a[164:167],a[44:47],v[72:75]
TR a[156:157],v235 offset:45056
TR a[158:159],v235 offset:47104
MM v[72:75],a[160:163],a[12:15],v[72:75]
s_waitcnt lgkmcnt(15)
v_lshlrev_b32 v114, 16, v227
v_and_b32 v115, 0xffff0000, v227
MM v[68:71],a[176:179],a[8:11],v[68:71]
TR a[160:161],v244 offset:16896
TR a[162:163],v244 offset:18944
MM v[68:71],a[180:183],a[40:43],v[68:71]
v_mul_f32 v116, 0xbfb8aa3b, v112
v_mul_f32 v117, 0xbfb8aa3b, v113
MM v[68:71],a[184:187],a[72:75],v[68:71]
TR a[164:165],v244 offset:20992
TR a[166:167],v244 offset:23040
MM v[68:71],a[188:191],a[104:107],v[68:71]
v_mul_f32 v118, 0xbfb8aa3b, v114
v_mul_f32 v119, 0xbfb8aa3b, v115
MM v[76:79],a[188:191],a[108:111],v[76:79]
TR a[168:169],v244 offset:25088
TR a[170:171],v244 offset:27136
MM v[76:79],a[184:187],a[76:79],v[76:79]
EX v116,v116
MM v[76:79],a[180:183],a[44:47],v[76:79]
TR a[172:173],v244 offset:29184
TR a[174:175],v244 offset:31232
MM v[76:79],a[176:179],a[12:15],v[76:79]
s_waitcnt lgkmcnt(15)
EX v117,v117
MM v[64:67],a[128:131],a[192:195],v[64:67]
TR a[176:177],v235 offset:16896
TR a[178:179],v235 offset:18944
MM v[64:67],a[132:135],a[208:211],v[64:67]
EX v118,v118
MM v[64:67],a[136:139],a[224:227],v[64:67]
TR a[180:181],v235 offset:20992
TR a[182:183],v235 offset:23040
MM v[64:67],a[140:143],a[240:243],v[64:67]
EX v119,v119
MM v[72:75],a[140:143],a[244:247],v[72:75]
TR a[184:185],v235 offset:25088
TR a[186:187],v235 offset:27136
MM v[72:75],a[136:139],a[228:231],v[72:75]
v_add_f32 v116, 1.0, v116
v_add_f32 v117, 1.0, v117
MM v[72:75],a[132:135],a[212:215],v[72:75]
TR a[188:189],v235 offset:29184
TR a[190:191],v235 offset:31232
MM v[72:75],a[128:131],a[196:199],v[72:75]
s_waitcnt lgkmcnt(15)
v_add_f32 v118, 1.0, v118
v_add_f32 v119, 1.0, v119
MM v[68:71],a[144:147],a[192:195],v[68:71]
TR a[128:129],v230 offset:33280
TR a[130:131],v230 offset:35328
MM v[68:71],a[148:151],a[208:211],v[68:71]
v_rcp_f32 v116, v116
MM v[68:71],a[152:155],a[224:227],v[68:71]
TR a[132:133],v230 offset:37376
TR a[134:135],v230 offset:39424
MM v[68:71],a[156:159],a[240:243],v[68:71]
v_rcp_f32 v117, v117
MM v[76:79],a[156:159],a[244:247],v[76:79]
TR a[136:137],v230 offset:41472
TR a[138:139],v230 offset:43520
MM v[76:79],a[152:155],a[228:231],v[76:79]
v_rcp_f32 v118, v118
MM v[76:79],a[148:151],a[212:215],v[76:79]
TR a[140:141],v230 offset:45568
TR a[142:143],v230 offset:47616
MM v[76:79],a[144:147],a[196:199],v[76:79]
s_waitcnt lgkmcnt(15)
v_rcp_f32 v119, v119
MM v[96:99],a[160:163],a[0:3],v[96:99]
s_waitcnt vmcnt(2)
TR a[144:145],v231 offset:33280
TR a[146:147],v231 offset:35328
MM v[96:99],a[164:167],a[32:35],v[96:99]
v_cmp_ne_u32 vcc, s69, v242
s_cbranch_vccnz LRPO1_%=
LRPO1b_%=:
MM v[96:99],a[168:171],a[64:67],v[96:99]
s_xor_b32 s92, s49, 1
TR a[148:149],v231 offset:37376
TR a[150:151],v231 offset:39424
MM v[96:99],a[172:175],a[96:99],v[96:99]
s_add_u32 s67, s46, 4096
CV v64,v64,v65
CV v65,v66,v67
MM v[104:107],a[172:175],a[100:103],v[104:107]
s_mul_i32 s92, s92, 17408
TR a[152:153],v231 offset:41472
TR a[154:155],v231 offset:43520
MM v[104:107],a[168:171],a[68:71],v[104:107]
s_add_u32 s92, s83, s92
CV v66,v68,v69
CV v67,v70,v71
MM v[104:107],a[164:167],a[36:39],v[104:107]
s_add_u32 s92, s92, 8192
TR a[156:157],v231 offset:45568
TR a[158:159],v231 offset:47616
MM v[104:107],a[160:163],a[4:7],v[104:107]
s_waitcnt lgkmcnt(15)
buffer_store_dwordx4 v[64:67], v249, s[20:23], s67 offen offset:0 sc1
MM v[100:103],a[176:179],a[0:3],v[100:103]
s_add_u32 s67, s47, 4096
TR a[160:161],v244 offset:33280
TR a[162:163],v244 offset:35328
MM v[100:103],a[180:183],a[32:35],v[100:103]
s_cmp_eq_u32 s82, 0
s_cbranch_scc1 LY0_%=
v_lshrrev_b32 v135, 4, v248
v_min_u32 v135, 59, v135
v_mul_u32_u24 v132, 205, v135
v_lshrrev_b32 v132, 11, v132
v_mul_u32_u24 v132, 30, v132
v_add_lshl_u32 v135, v135, v132, 2
s_mul_i32 s78, s84, 40
s_add_u32 s78, s78, 73728
buffer_load_dword v132, v135, s[20:23], s78 offen offset:0 sc1
buffer_load_dword v133, v135, s[20:23], s78 offen offset:960 sc1
buffer_load_dword v134, v135, s[20:23], s78 offen offset:1920 sc1
LY0_%=:
buffer_load_dwordx4 a[16:19], v248, s[20:23], s92 offen offset:0 sc1
CV v72,v72,v73
CV v73,v74,v75
MM v[100:103],a[184:187],a[64:67],v[100:103]
TR a[164:165],v244 offset:37376
TR a[166:167],v244 offset:39424
MM v[100:103],a[188:191],a[96:99],v[100:103]
CV v74,v76,v77
CV v75,v78,v79
MM v[108:111],a[188:191],a[100:103],v[108:111]
TR a[168:169],v244 offset:41472
TR a[170:171],v244 offset:43520
MM v[108:111],a[184:187],a[68:71],v[108:111]
buffer_store_dwordx4 v[72:75], v234, s[20:23], s67 offen offset:1024 sc1
MM v[108:111],a[180:183],a[36:39],v[108:111]
buffer_load_dwordx4 a[48:51], v248, s[20:23], s92 offen offset:1024 sc1
TR a[172:173],v244 offset:45568
TR a[174:175],v244 offset:47616
MM v[108:111],a[176:179],a[4:7],v[108:111]
s_waitcnt lgkmcnt(15)
ds_bpermute_b32 v192, v245, v64
ds_bpermute_b32 v193, v245, v65
MM v[96:99],a[128:131],a[8:11],v[96:99]
TR a[176:177],v235 offset:33280
TR a[178:179],v235 offset:35328
MM v[96:99],a[132:135],a[40:43],v[96:99]
ds_bpermute_b32 v194, v245, v66
ds_bpermute_b32 v195, v245, v67
MM v[96:99],a[136:139],a[72:75],v[96:99]
TR a[180:181],v235 offset:37376
TR a[182:183],v235 offset:39424
MM v[96:99],a[140:143],a[104:107],v[96:99]
buffer_load_dwordx4 a[80:83], v248, s[20:23], s92 offen offset:2048 sc1
MM v[104:107],a[140:143],a[108:111],v[104:107]
TR a[184:185],v235 offset:41472
TR a[186:187],v235 offset:43520
MM v[104:107],a[136:139],a[76:79],v[104:107]
buffer_load_dwordx4 a[112:115], v248, s[20:23], s92 offen offset:3072 sc1
MM v[104:107],a[132:135],a[44:47],v[104:107]
s_add_u32 s92, s92, 4096
TR a[188:189],v235 offset:45568
TR a[190:191],v235 offset:47616
MM v[104:107],a[128:131],a[12:15],v[104:107]
s_waitcnt lgkmcnt(15)
buffer_load_dwordx4 a[20:23], v248, s[20:23], s92 offen offset:0 sc1
MM v[100:103],a[144:147],a[8:11],v[100:103]
TR a[128:129],v230 offset:49152
TR a[130:131],v230 offset:51200
MM v[100:103],a[148:151],a[40:43],v[100:103]
v_mov_b32 v252, s69
s_mov_b64 exec, 1
buffer_store_dword v252, off, s[20:23], s19 offset:0 sc1
s_mov_b64 exec, -1
MM v[100:103],a[152:155],a[72:75],v[100:103]
TR a[132:133],v230 offset:53248
TR a[134:135],v230 offset:55296
MM v[100:103],a[156:159],a[104:107],v[100:103]
buffer_load_dwordx4 a[52:55], v248, s[20:23], s92 offen offset:1024 sc1
MM v[108:111],a[156:159],a[108:111],v[108:111]
TR a[136:137],v230 offset:57344
TR a[138:139],v230 offset:59392
MM v[108:111],a[152:155],a[76:79],v[108:111]
buffer_load_dwordx4 a[84:87], v248, s[20:23], s92 offen offset:2048 sc1
MM v[108:111],a[148:151],a[44:47],v[108:111]
TR a[140:141],v230 offset:61440
TR a[142:143],v230 offset:63488
MM v[108:111],a[144:147],a[12:15],v[108:111]
s_waitcnt lgkmcnt(15)
v_mul_f32 v116, v112, v116
v_mul_f32 v117, v113, v117
MM v[96:99],a[160:163],a[192:195],v[96:99]
TR a[144:145],v231 offset:49152
TR a[146:147],v231 offset:51200
MM v[96:99],a[164:167],a[208:211],v[96:99]
buffer_load_dwordx4 a[116:119], v248, s[20:23], s92 offen offset:3072 sc1
MM v[96:99],a[168:171],a[224:227],v[96:99]
TR a[148:149],v231 offset:53248
TR a[150:151],v231 offset:55296
MM v[96:99],a[172:175],a[240:243],v[96:99]
v_mul_f32 v118, v114, v118
v_mul_f32 v119, v115, v119
MM v[104:107],a[172:175],a[244:247],v[104:107]
TR a[152:153],v231 offset:57344
TR a[154:155],v231 offset:59392
MM v[104:107],a[168:171],a[228:231],v[104:107]
)ASM"
R"ASM(
CV v2,v116,v117
CV v3,v118,v119
MM v[104:107],a[164:167],a[212:215],v[104:107]
TR a[156:157],v231 offset:61440
TR a[158:159],v231 offset:63488
MM v[104:107],a[160:163],a[196:199],v[104:107]
s_waitcnt lgkmcnt(15)
v_lshlrev_b32 v10, 16, v236
v_and_b32 v11, 0xffff0000, v236
MM v[100:103],a[176:179],a[192:195],v[100:103]
TR a[160:161],v230 offset:49664
TR a[162:163],v230 offset:51712
MM v[100:103],a[180:183],a[208:211],v[100:103]
v_lshlrev_b32 v12, 16, v237
v_and_b32 v13, 0xffff0000, v237
MM v[100:103],a[184:187],a[224:227],v[100:103]
TR a[164:165],v230 offset:53760
TR a[166:167],v230 offset:55808
MM v[100:103],a[188:191],a[240:243],v[100:103]
v_mul_f32 v14, 0xbfb8aa3b, v10
v_mul_f32 v15, 0xbfb8aa3b, v11
MM v[108:111],a[188:191],a[244:247],v[108:111]
TR a[168:169],v230 offset:57856
TR a[170:171],v230 offset:59904
MM v[108:111],a[184:187],a[228:231],v[108:111]
v_mul_f32 v42, 0xbfb8aa3b, v12
v_mul_f32 v43, 0xbfb8aa3b, v13
MM v[108:111],a[180:183],a[212:215],v[108:111]
TR a[172:173],v230 offset:61952
TR a[174:175],v230 offset:64000
MM v[108:111],a[176:179],a[196:199],v[108:111]
s_waitcnt lgkmcnt(15)
EX v14,v14
MM v[16:19],a[128:131],v[168:171],0
TR a[176:177],v231 offset:49664
TR a[178:179],v231 offset:51712
MM v[16:19],a[132:135],v[184:187],v[16:19]
AW a3,-1
AW a7,-1
MM v[16:19],a[136:139],v[200:203],v[16:19]
TR a[180:181],v231 offset:53760
TR a[182:183],v231 offset:55808
MM v[16:19],a[140:143],v[216:219],v[16:19]
s_add_u32 s67, s46, 4096
CV v96,v96,v97
CV v97,v98,v99
AW a11,-1
MM v[24:27],a[140:143],v[220:223],0
TR a[184:185],v231 offset:57856
TR a[186:187],v231 offset:59904
MM v[24:27],a[136:139],v[204:207],v[24:27]
CV v98,v100,v101
CV v99,v102,v103
MM v[24:27],a[132:135],v[188:191],v[24:27]
TR a[188:189],v231 offset:61952
TR a[190:191],v231 offset:64000
AW a15,-1
MM v[24:27],a[128:131],v[172:175],v[24:27]
s_waitcnt lgkmcnt(15)
buffer_store_dwordx4 v[96:99], v249, s[20:23], s67 offen offset:2048 sc1
MM v[20:23],a[144:147],v[168:171],0
s_add_u32 s67, s47, 4096
TR a[128:129],v230 offset:16384
TR a[130:131],v230 offset:18432
MM v[20:23],a[148:151],v[184:187],v[20:23]
CV v104,v104,v105
CV v105,v106,v107
AW a35,-1
MM v[20:23],a[152:155],v[200:203],v[20:23]
TR a[132:133],v230 offset:20480
TR a[134:135],v230 offset:22528
MM v[20:23],a[156:159],v[216:219],v[20:23]
CV v106,v108,v109
CV v107,v110,v111
s_mov_b64 exec, 0xffff
AW a0,v160
AW a1,v161
AW a2,v162
AW a3,v163
s_mov_b64 exec, -1
MM v[28:31],a[156:159],v[220:223],0
TR a[136:137],v230 offset:24576
TR a[138:139],v230 offset:26624
AW a39,-1
MM v[28:31],a[152:155],v[204:207],v[28:31]
buffer_store_dwordx4 v[104:107], v234, s[20:23], s67 offen offset:3072 sc1
MM v[28:31],a[148:151],v[188:191],v[28:31]
TR a[140:141],v230 offset:28672
TR a[142:143],v230 offset:30720
s_mov_b64 exec, 0xffff
AW a8,v176
AW a9,v177
AW a10,v178
AW a11,v179
s_mov_b64 exec, -1
MM v[28:31],a[144:147],v[172:175],v[28:31]
s_waitcnt lgkmcnt(15)
ds_bpermute_b32 v208, v245, v96
ds_bpermute_b32 v209, v245, v97
AW a43,-1
MM v[48:51],a[160:163],v[168:171],0
TR a[144:145],v231 offset:16384
TR a[146:147],v231 offset:18432
MM v[48:51],a[164:167],v[184:187],v[48:51]
ds_bpermute_b32 v210, v245, v98
ds_bpermute_b32 v211, v245, v99
MM v[48:51],a[168:171],v[200:203],v[48:51]
TR a[148:149],v231 offset:20480
TR a[150:151],v231 offset:22528
AW a47,-1
MM v[48:51],a[172:175],v[216:219],v[48:51]
buffer_load_dword v240, v253, s[20:23], s68 offen offset:0 sc1
buffer_load_dword v140, v253, s[20:23], s45 offen offset:0 sc1
MM v[56:59],a[172:175],v[220:223],0
TR a[152:153],v231 offset:24576
TR a[154:155],v231 offset:26624
MM v[56:59],a[168:171],v[204:207],v[56:59]
AW a71,-1
AW a79,-1
MM v[56:59],a[164:167],v[188:191],v[56:59]
TR a[156:157],v231 offset:28672
TR a[158:159],v231 offset:30720
MM v[56:59],a[160:163],v[172:175],v[56:59]
s_waitcnt lgkmcnt(15)
AW a103,-1
AW a111,-1
MM v[52:55],a[176:179],v[168:171],0
TR a[160:161],v230 offset:16896
TR a[162:163],v230 offset:18944
MM v[52:55],a[180:183],v[184:187],v[52:55]
AW a195,-1
AW a199,-1
MM v[52:55],a[184:187],v[200:203],v[52:55]
TR a[164:165],v230 offset:20992
TR a[166:167],v230 offset:23040
MM v[52:55],a[188:191],v[216:219],v[52:55]
AW a215,-1
MM v[60:63],a[188:191],v[220:223],0
TR a[168:169],v230 offset:25088
TR a[170:171],v230 offset:27136
MM v[60:63],a[184:187],v[204:207],v[60:63]
s_mov_b64 exec, 0xffff
AW a32,v192
AW a33,v193
AW a34,v194
AW a35,v195
s_mov_b64 exec, -1
MM v[60:63],a[180:183],v[188:191],v[60:63]
s_waitcnt lgkmcnt(12)
TR a[172:173],v230 offset:29184
TR a[174:175],v230 offset:31232
MM v[60:63],a[176:179],v[172:175],v[60:63]
s_mov_b64 exec, 0xffff
AW a40,v208
AW a41,v209
AW a42,v210
AW a43,v211
s_mov_b64 exec, -1
MM v[80:83],a[128:131],v[168:171],0
TR a[176:177],v231 offset:16896
TR a[178:179],v231 offset:18944
MM v[80:83],a[132:135],v[184:187],v[80:83]
s_mov_b64 exec, 0xffff
AW a192,v164
AW a193,v165
AW a194,v166
AW a195,v167
s_mov_b64 exec, -1
MM v[80:83],a[136:139],v[200:203],v[80:83]
TR a[180:181],v231 offset:20992
TR a[182:183],v231 offset:23040
MM v[80:83],a[140:143],v[216:219],v[80:83]
v_lshrrev_b32 v104, 8, v248
v_min_u32 v104, 1, v104
MM v[88:91],a[140:143],v[220:223],0
TR a[184:185],v231 offset:25088
TR a[186:187],v231 offset:27136
MM v[88:91],a[136:139],v[204:207],v[88:91]
EX v15,v15
v_sub_u32 v104, 0, v104
MM v[88:91],a[132:135],v[188:191],v[88:91]
TR a[188:189],v231 offset:29184
TR a[190:191],v231 offset:31232
MM v[88:91],a[128:131],v[172:175],v[88:91]
s_waitcnt lgkmcnt(15)
EX v42,v42
MM v[84:87],a[144:147],v[168:171],0
TR a[128:129],v228 offset:0
TR a[130:131],v228 offset:1024
MM v[84:87],a[148:151],v[184:187],v[84:87]
EX v43,v43
MM v[84:87],a[152:155],v[200:203],v[84:87]
TR a[132:133],v228 offset:2048
TR a[134:135],v228 offset:3072
MM v[84:87],a[156:159],v[216:219],v[84:87]
v_add_f32 v14, 1.0, v14
v_add_f32 v15, 1.0, v15
MM v[92:95],a[156:159],v[220:223],0
TR a[136:137],v228 offset:4096
TR a[138:139],v228 offset:5120
MM v[92:95],a[152:155],v[204:207],v[92:95]
v_add_f32 v42, 1.0, v42
v_add_f32 v43, 1.0, v43
MM v[92:95],a[148:151],v[188:191],v[92:95]
s_xor_b32 s92, s49, 2
TR a[140:141],v228 offset:6144
TR a[142:143],v228 offset:7168
MM v[92:95],a[144:147],v[172:175],v[92:95]
s_waitcnt lgkmcnt(15)
s_mul_i32 s92, s92, 17408
s_add_u32 s92, s83, s92
MM v[112:115],a[160:163],v[168:171],0
s_add_u32 s92, s92, 8192
TR a[144:145],v229 offset:0
TR a[146:147],v229 offset:1024
MM v[112:115],a[164:167],v[184:187],v[112:115]
buffer_load_dwordx4 a[24:27], v248, s[20:23], s92 offen offset:0 sc1
MM v[112:115],a[168:171],v[200:203],v[112:115]
TR a[148:149],v229 offset:2048
TR a[150:151],v229 offset:3072
MM v[112:115],a[172:175],v[216:219],v[112:115]
buffer_load_dwordx4 a[56:59], v248, s[20:23], s92 offen offset:1024 sc1
MM v[120:123],a[172:175],v[220:223],0
TR a[152:153],v229 offset:4096
TR a[154:155],v229 offset:5120
MM v[120:123],a[168:171],v[204:207],v[120:123]
buffer_load_dwordx4 a[88:91], v248, s[20:23], s92 offen offset:2048 sc1
MM v[120:123],a[164:167],v[188:191],v[120:123]
TR a[156:157],v229 offset:6144
TR a[158:159],v229 offset:7168
MM v[120:123],a[160:163],v[172:175],v[120:123]
s_waitcnt lgkmcnt(15)
v_rcp_f32 v14, v14
MM v[116:119],a[176:179],v[168:171],0
TR a[160:161],v244 offset:49152
TR a[162:163],v244 offset:51200
MM v[116:119],a[180:183],v[184:187],v[116:119]
buffer_load_dwordx4 a[120:123], v248, s[20:23], s92 offen offset:3072 sc1
MM v[116:119],a[184:187],v[200:203],v[116:119]
s_add_u32 s92, s92, 4096
TR a[164:165],v244 offset:53248
TR a[166:167],v244 offset:55296
MM v[116:119],a[188:191],v[216:219],v[116:119]
buffer_load_dwordx4 a[28:31], v248, s[20:23], s92 offen offset:0 sc1
MM v[124:127],a[188:191],v[220:223],0
TR a[168:169],v244 offset:57344
TR a[170:171],v244 offset:59392
MM v[124:127],a[184:187],v[204:207],v[124:127]
v_rcp_f32 v15, v15
MM v[124:127],a[180:183],v[188:191],v[124:127]
TR a[172:173],v244 offset:61440
TR a[174:175],v244 offset:63488
MM v[124:127],a[176:179],v[172:175],v[124:127]
s_waitcnt lgkmcnt(15)
buffer_load_dwordx4 a[60:63], v248, s[20:23], s92 offen offset:1024 sc1
MM v[144:147],a[128:131],v[168:171],0
TR a[176:177],v235 offset:49152
TR a[178:179],v235 offset:51200
MM v[144:147],a[132:135],v[184:187],v[144:147]
buffer_load_dwordx4 a[92:95], v248, s[20:23], s92 offen offset:2048 sc1
MM v[144:147],a[136:139],v[200:203],v[144:147]
TR a[180:181],v235 offset:53248
TR a[182:183],v235 offset:55296
MM v[144:147],a[140:143],v[216:219],v[144:147]
v_rcp_f32 v42, v42
MM v[152:155],a[140:143],v[220:223],0
TR a[184:185],v235 offset:57344
TR a[186:187],v235 offset:59392
MM v[152:155],a[136:139],v[204:207],v[152:155]
buffer_load_dwordx4 a[124:127], v248, s[20:23], s92 offen offset:3072 sc1
MM v[152:155],a[132:135],v[188:191],v[152:155]
s_xor_b32 s92, s49, 3
TR a[188:189],v235 offset:61440
TR a[190:191],v235 offset:63488
MM v[152:155],a[128:131],v[172:175],v[152:155]
s_waitcnt lgkmcnt(15)
s_mul_i32 s92, s92, 17408
s_add_u32 s92, s83, s92
MM v[148:151],a[144:147],v[168:171],0
s_add_u32 s92, s92, 8192
TR a[128:129],v244 offset:49664
TR a[130:131],v244 offset:51712
MM v[148:151],a[148:151],v[184:187],v[148:151]
buffer_load_dwordx4 a[200:203], v248, s[20:23], s92 offen offset:0 sc1
MM v[148:151],a[152:155],v[200:203],v[148:151]
TR a[132:133],v244 offset:53760
TR a[134:135],v244 offset:55808
MM v[148:151],a[156:159],v[216:219],v[148:151]
buffer_load_dwordx4 a[216:219], v248, s[20:23], s92 offen offset:1024 sc1
MM v[156:159],a[156:159],v[220:223],0
TR a[136:137],v244 offset:57856
TR a[138:139],v244 offset:59904
MM v[156:159],a[152:155],v[204:207],v[156:159]
buffer_load_dwordx4 a[232:235], v248, s[20:23], s92 offen offset:2048 sc1
MM v[156:159],a[148:151],v[188:191],v[156:159]
TR a[140:141],v244 offset:61952
TR a[142:143],v244 offset:64000
MM v[156:159],a[144:147],v[172:175],v[156:159]
s_waitcnt lgkmcnt(15)
s_waitcnt vmcnt(11)
v_cmp_eq_u32 s[36:37], s69, v240
MM v[16:19],a[160:163],a[16:19],v[16:19]
TR a[144:145],v235 offset:49664
TR a[146:147],v235 offset:51712
MM v[16:19],a[164:167],a[48:51],v[16:19]
buffer_load_dwordx4 a[248:251], v248, s[20:23], s92 offen offset:3072 sc1
v_cmp_eq_u32 s[38:39], s69, v140
MM v[16:19],a[168:171],a[80:83],v[16:19]
s_add_u32 s92, s92, 4096
TR a[148:149],v235 offset:53760
TR a[150:151],v235 offset:55808
MM v[16:19],a[172:175],a[112:115],v[16:19]
s_nop 0
s_nop 0
LPG23_%=:
s_cbranch_execz LTR3_%=
buffer_load_dwordx4 a[204:207], v248, s[20:23], s92 offen offset:0 sc1
MM v[24:27],a[172:175],a[116:119],v[24:27]
s_and_b32 s36, s36, 0xffff0000
TR a[152:153],v235 offset:57856
TR a[154:155],v235 offset:59904
MM v[24:27],a[168:171],a[84:87],v[24:27]
v_rcp_f32 v43, v43
MM v[24:27],a[164:167],a[52:55],v[24:27]
TR a[156:157],v235 offset:61952
TR a[158:159],v235 offset:64000
MM v[24:27],a[160:163],a[20:23],v[24:27]
s_waitcnt lgkmcnt(15)
buffer_load_dwordx4 a[220:223], v248, s[20:23], s92 offen offset:1024 sc1
MM v[20:23],a[176:179],a[16:19],v[20:23]
TR a[160:161],v244 offset:16384
TR a[162:163],v244 offset:18432
MM v[20:23],a[180:183],a[48:51],v[20:23]
buffer_load_dwordx4 a[236:239], v248, s[20:23], s92 offen offset:2048 sc1
MM v[20:23],a[184:187],a[80:83],v[20:23]
v_mov_b32 v252, s69
s_mov_b64 exec, 1
buffer_store_dword v252, off, s[20:23], s19 offset:4 sc1
s_mov_b64 exec, -1
TR a[164:165],v244 offset:20480
TR a[166:167],v244 offset:22528
MM v[20:23],a[188:191],a[112:115],v[20:23]
v_mul_f32 v14, v10, v14
v_mul_f32 v15, v11, v15
MM v[28:31],a[188:191],a[116:119],v[28:31]
TR a[168:169],v244 offset:24576
TR a[170:171],v244 offset:26624
MM v[28:31],a[184:187],a[84:87],v[28:31]
buffer_load_dwordx4 a[252:255], v248, s[20:23], s92 offen offset:3072 sc1
MM v[28:31],a[180:183],a[52:55],v[28:31]
TR a[172:173],v244 offset:28672
TR a[174:175],v244 offset:30720
MM v[28:31],a[176:179],a[20:23],v[28:31]
s_waitcnt lgkmcnt(15)
s_add_u32 s66, s16, 0
v_mul_f32 v42, v12, v42
MM v[48:51],a[128:131],a[16:19],v[48:51]
TR a[176:177],v235 offset:16384
TR a[178:179],v235 offset:18432
MM v[48:51],a[132:135],a[48:51],v[48:51]
s_mov_b64 exec, s[36:37]
buffer_load_dwordx4 a[0:3], v250, s[20:23], s66 offen offset:0 sc1
s_mov_b64 exec, -1
)ASM"
R"ASM(
MM v[48:51],a[136:139],a[80:83],v[48:51]
s_add_u32 s66, s44, 0
TR a[180:181],v235 offset:20480
TR a[182:183],v235 offset:22528
MM v[48:51],a[140:143],a[112:115],v[48:51]
s_mov_b64 exec, s[38:39]
buffer_load_dwordx4 a[4:7], v250, s[20:23], s66 offen offset:0 sc1
s_mov_b64 exec, -1
MM v[56:59],a[140:143],a[116:119],v[56:59]
s_add_u32 s66, s16, 2048
TR a[184:185],v235 offset:24576
TR a[186:187],v235 offset:26624
MM v[56:59],a[136:139],a[84:87],v[56:59]
s_mov_b64 exec, s[36:37]
buffer_load_dwordx4 a[8:11], v250, s[20:23], s66 offen offset:0 sc1
s_mov_b64 exec, -1
MM v[56:59],a[132:135],a[52:55],v[56:59]
s_add_u32 s66, s44, 2048
TR a[188:189],v235 offset:28672
TR a[190:191],v235 offset:30720
MM v[56:59],a[128:131],a[20:23],v[56:59]
s_waitcnt lgkmcnt(15)
v_mul_f32 v43, v13, v43
CV v4,v14,v15
MM v[52:55],a[144:147],a[16:19],v[52:55]
TR a[128:129],v244 offset:16896
TR a[130:131],v244 offset:18944
MM v[52:55],a[148:151],a[48:51],v[52:55]
s_mov_b64 exec, s[38:39]
buffer_load_dwordx4 a[12:15], v250, s[20:23], s66 offen offset:0 sc1
s_mov_b64 exec, -1
MM v[52:55],a[152:155],a[80:83],v[52:55]
TR a[132:133],v244 offset:20992
TR a[134:135],v244 offset:23040
MM v[52:55],a[156:159],a[112:115],v[52:55]
CV v5,v42,v43
v_lshlrev_b32 v10, 16, v238
MM v[60:63],a[156:159],a[116:119],v[60:63]
TR a[136:137],v244 offset:25088
TR a[138:139],v244 offset:27136
MM v[60:63],a[152:155],a[84:87],v[60:63]
v_and_b32 v11, 0xffff0000, v238
v_lshlrev_b32 v12, 16, v239
MM v[60:63],a[148:151],a[52:55],v[60:63]
TR a[140:141],v244 offset:29184
TR a[142:143],v244 offset:31232
MM v[60:63],a[144:147],a[20:23],v[60:63]
s_waitcnt lgkmcnt(15)
v_and_b32 v13, 0xffff0000, v239
v_mul_f32 v14, 0xbfb8aa3b, v10
MM v[80:83],a[160:163],a[16:19],v[80:83]
TR a[144:145],v235 offset:16896
TR a[146:147],v235 offset:18944
MM v[80:83],a[164:167],a[48:51],v[80:83]
v_mul_f32 v15, 0xbfb8aa3b, v11
v_mul_f32 v42, 0xbfb8aa3b, v12
MM v[80:83],a[168:171],a[80:83],v[80:83]
TR a[148:149],v235 offset:20992
TR a[150:151],v235 offset:23040
MM v[80:83],a[172:175],a[112:115],v[80:83]
v_mul_f32 v43, 0xbfb8aa3b, v13
MM v[88:91],a[172:175],a[116:119],v[88:91]
TR a[152:153],v235 offset:25088
TR a[154:155],v235 offset:27136
MM v[88:91],a[168:171],a[84:87],v[88:91]
EX v14,v14
MM v[88:91],a[164:167],a[52:55],v[88:91]
TR a[156:157],v235 offset:29184
TR a[158:159],v235 offset:31232
MM v[88:91],a[160:163],a[20:23],v[88:91]
s_waitcnt lgkmcnt(15)
EX v15,v15
MM v[84:87],a[176:179],a[16:19],v[84:87]
TR a[160:161],v228 offset:8192
TR a[162:163],v228 offset:9216
MM v[84:87],a[180:183],a[48:51],v[84:87]
EX v42,v42
MM v[84:87],a[184:187],a[80:83],v[84:87]
TR a[164:165],v228 offset:10240
TR a[166:167],v228 offset:11264
MM v[84:87],a[188:191],a[112:115],v[84:87]
EX v43,v43
MM v[92:95],a[188:191],a[116:119],v[92:95]
TR a[168:169],v228 offset:12288
TR a[170:171],v228 offset:13312
MM v[92:95],a[184:187],a[84:87],v[92:95]
v_add_f32 v14, 1.0, v14
v_add_f32 v15, 1.0, v15
MM v[92:95],a[180:183],a[52:55],v[92:95]
TR a[172:173],v228 offset:14336
TR a[174:175],v228 offset:15360
MM v[92:95],a[176:179],a[20:23],v[92:95]
s_waitcnt lgkmcnt(15)
v_add_f32 v42, 1.0, v42
v_add_f32 v43, 1.0, v43
MM v[112:115],a[128:131],a[16:19],v[112:115]
TR a[176:177],v229 offset:8192
TR a[178:179],v229 offset:9216
MM v[112:115],a[132:135],a[48:51],v[112:115]
v_rcp_f32 v14, v14
MM v[112:115],a[136:139],a[80:83],v[112:115]
TR a[180:181],v229 offset:10240
TR a[182:183],v229 offset:11264
MM v[112:115],a[140:143],a[112:115],v[112:115]
buffer_load_dword v241, v253, s[20:23], s68 offen offset:4 sc1
buffer_load_dword v141, v253, s[20:23], s45 offen offset:4 sc1
MM v[120:123],a[140:143],a[116:119],v[120:123]
TR a[184:185],v229 offset:12288
TR a[186:187],v229 offset:13312
MM v[120:123],a[136:139],a[84:87],v[120:123]
v_rcp_f32 v15, v15
MM v[120:123],a[132:135],a[52:55],v[120:123]
TR a[188:189],v229 offset:14336
TR a[190:191],v229 offset:15360
MM v[120:123],a[128:131],a[20:23],v[120:123]
s_waitcnt lgkmcnt(15)
v_rcp_f32 v42, v42
MM v[116:119],a[144:147],a[16:19],v[116:119]
TR a[128:129],v230 offset:0
TR a[130:131],v230 offset:2048
MM v[116:119],a[148:151],a[48:51],v[116:119]
v_rcp_f32 v43, v43
MM v[116:119],a[152:155],a[80:83],v[116:119]
TR a[132:133],v230 offset:4096
TR a[134:135],v230 offset:6144
MM v[116:119],a[156:159],a[112:115],v[116:119]
v_mul_f32 v14, v10, v14
v_mul_f32 v15, v11, v15
MM v[124:127],a[156:159],a[116:119],v[124:127]
TR a[136:137],v230 offset:8192
TR a[138:139],v230 offset:10240
MM v[124:127],a[152:155],a[84:87],v[124:127]
v_mul_f32 v42, v12, v42
v_mul_f32 v43, v13, v43
MM v[124:127],a[148:151],a[52:55],v[124:127]
TR a[140:141],v230 offset:12288
TR a[142:143],v230 offset:14336
MM v[124:127],a[144:147],a[20:23],v[124:127]
s_waitcnt lgkmcnt(15)
CV v6,v14,v15
CV v7,v42,v43
MM v[144:147],a[160:163],a[16:19],v[144:147]
s_add_u32 s80, s18, 0
TR a[144:145],v244 offset:0
TR a[146:147],v244 offset:2048
MM v[144:147],a[164:167],a[48:51],v[144:147]
buffer_load_dwordx2 v[32:33], v251, s[32:35], s80 offen offset:0
MM v[144:147],a[168:171],a[80:83],v[144:147]
s_add_u32 s80, s18, 128
TR a[148:149],v244 offset:4096
TR a[150:151],v244 offset:6144
MM v[144:147],a[172:175],a[112:115],v[144:147]
buffer_load_dwordx2 v[34:35], v251, s[32:35], s80 offen offset:0
MM v[152:155],a[172:175],a[116:119],v[152:155]
s_add_u32 s80, s18, 40960
TR a[152:153],v244 offset:8192
TR a[154:155],v244 offset:10240
MM v[152:155],a[168:171],a[84:87],v[152:155]
buffer_load_dwordx2 v[36:37], v251, s[32:35], s80 offen offset:0
MM v[152:155],a[164:167],a[52:55],v[152:155]
s_add_u32 s80, s18, 41088
TR a[156:157],v244 offset:12288
TR a[158:159],v244 offset:14336
MM v[152:155],a[160:163],a[20:23],v[152:155]
s_waitcnt lgkmcnt(15)
buffer_load_dwordx2 v[38:39], v251, s[32:35], s80 offen offset:0
MM v[148:151],a[176:179],a[16:19],v[148:151]
s_add_u32 s80, s63, 0
TR a[160:161],v231 offset:0
TR a[162:163],v231 offset:2048
MM v[148:151],a[180:183],a[48:51],v[148:151]
buffer_load_dwordx2 v[40:41], v254, s[32:35], s80 offen offset:0
MM v[148:151],a[184:187],a[80:83],v[148:151]
TR a[164:165],v231 offset:4096
TR a[166:167],v231 offset:6144
MM v[148:151],a[188:191],a[112:115],v[148:151]
v_mov_b32 v77, 0x3f80
v_and_b32 v76, 16, v248
MM v[156:159],a[188:191],a[116:119],v[156:159]
TR a[168:169],v231 offset:8192
TR a[170:171],v231 offset:10240
MM v[156:159],a[184:187],a[84:87],v[156:159]
v_lshlrev_b32 v76, v76, v77
s_mov_b32 s28, 0x30003
MM v[156:159],a[180:183],a[52:55],v[156:159]
s_mov_b32 s29, s28
TR a[172:173],v231 offset:12288
TR a[174:175],v231 offset:14336
MM v[156:159],a[176:179],a[20:23],v[156:159]
s_waitcnt lgkmcnt(15)
v_cndmask_b32 v64, 0, v76, s[28:29]
s_lshl_b64 s[28:29], s[28:29], 2
s_waitcnt vmcnt(24)
MM v[16:19],a[128:131],a[24:27],v[16:19]
TR a[176:177],v235 offset:0
TR a[178:179],v235 offset:2048
MM v[16:19],a[132:135],a[56:59],v[16:19]
v_cndmask_b32 v65, 0, v76, s[28:29]
s_lshl_b64 s[28:29], s[28:29], 2
MM v[16:19],a[136:139],a[88:91],v[16:19]
TR a[180:181],v235 offset:4096
TR a[182:183],v235 offset:6144
MM v[16:19],a[140:143],a[120:123],v[16:19]
s_waitcnt vmcnt(20)
v_cndmask_b32 v66, 0, v76, s[28:29]
s_lshl_b64 s[28:29], s[28:29], 2
MM v[24:27],a[140:143],a[124:127],v[24:27]
TR a[184:185],v235 offset:8192
TR a[186:187],v235 offset:10240
MM v[24:27],a[136:139],a[92:95],v[24:27]
v_cndmask_b32 v67, 0, v76, s[28:29]
s_lshl_b64 s[28:29], s[28:29], 2
MM v[24:27],a[132:135],a[60:63],v[24:27]
TR a[188:189],v235 offset:12288
TR a[190:191],v235 offset:14336
MM v[24:27],a[128:131],a[28:31],v[24:27]
s_waitcnt lgkmcnt(15)
v_cndmask_b32 v68, 0, v76, s[28:29]
s_lshl_b64 s[28:29], s[28:29], 2
s_waitcnt vmcnt(16)
MM v[16:19],a[144:147],a[200:203],v[16:19]
TR a[128:129],v230 offset:512
TR a[130:131],v230 offset:2560
MM v[16:19],a[148:151],a[216:219],v[16:19]
v_cndmask_b32 v69, 0, v76, s[28:29]
s_lshl_b64 s[28:29], s[28:29], 2
MM v[16:19],a[152:155],a[232:235],v[16:19]
TR a[132:133],v230 offset:4608
TR a[134:135],v230 offset:6656
MM v[16:19],a[156:159],a[248:251],v[16:19]
s_waitcnt vmcnt(11)
v_cndmask_b32 v70, 0, v76, s[28:29]
s_lshl_b64 s[28:29], s[28:29], 2
MM v[24:27],a[156:159],a[252:255],v[24:27]
TR a[136:137],v230 offset:8704
TR a[138:139],v230 offset:10752
MM v[24:27],a[152:155],a[236:239],v[24:27]
v_cndmask_b32 v71, 0, v76, s[28:29]
MM v[24:27],a[148:151],a[220:223],v[24:27]
TR a[140:141],v230 offset:12800
TR a[142:143],v230 offset:14848
MM v[24:27],a[144:147],a[204:207],v[24:27]
s_waitcnt lgkmcnt(15)
MM v[20:23],a[160:163],a[24:27],v[20:23]
TR a[144:145],v244 offset:512
TR a[146:147],v244 offset:2560
MM v[20:23],a[164:167],a[56:59],v[20:23]
MM v[20:23],a[168:171],a[88:91],v[20:23]
TR a[148:149],v244 offset:4608
TR a[150:151],v244 offset:6656
MM v[20:23],a[172:175],a[120:123],v[20:23]
MM v[28:31],a[172:175],a[124:127],v[28:31]
TR a[152:153],v244 offset:8704
TR a[154:155],v244 offset:10752
MM v[28:31],a[168:171],a[92:95],v[28:31]
MM v[28:31],a[164:167],a[60:63],v[28:31]
TR a[156:157],v244 offset:12800
TR a[158:159],v244 offset:14848
MM v[28:31],a[160:163],a[28:31],v[28:31]
s_waitcnt lgkmcnt(15)
MM v[28:31],a[188:191],a[252:255],v[28:31]
TR a[160:161],v231 offset:512
TR a[162:163],v231 offset:2560
MM v[28:31],a[184:187],a[236:239],v[28:31]
MM v[28:31],a[180:183],a[220:223],v[28:31]
TR a[164:165],v231 offset:4608
TR a[166:167],v231 offset:6656
MM v[28:31],a[176:179],a[204:207],v[28:31]
MM v[20:23],a[176:179],a[200:203],v[20:23]
TR a[168:169],v231 offset:8704
TR a[170:171],v231 offset:10752
MM v[20:23],a[180:183],a[216:219],v[20:23]
MM v[20:23],a[184:187],a[232:235],v[20:23]
TR a[172:173],v231 offset:12800
TR a[174:175],v231 offset:14848
MM v[20:23],a[188:191],a[248:251],v[20:23]
s_waitcnt lgkmcnt(15)
s_waitcnt vmcnt(5)
v_cmp_eq_u32 s[8:9], s69, v241
MM v[48:51],a[128:131],a[24:27],v[48:51]
TR a[176:177],v235 offset:512
TR a[178:179],v235 offset:2560
MM v[48:51],a[132:135],a[56:59],v[48:51]
v_cmp_eq_u32 s[10:11], s69, v141
MM v[48:51],a[136:139],a[88:91],v[48:51]
TR a[180:181],v235 offset:4608
TR a[182:183],v235 offset:6656
MM v[48:51],a[140:143],a[120:123],v[48:51]
s_add_u32 s67, s46, 8192
CV v16,v16,v17
CV v17,v18,v19
MM v[56:59],a[140:143],a[124:127],v[56:59]
s_nop 0
TR a[184:185],v235 offset:8704
TR a[186:187],v235 offset:10752
MM v[56:59],a[136:139],a[92:95],v[56:59]
s_and_b32 s8, s8, 0xffff0000
CV v18,v20,v21
CV v19,v22,v23
MM v[56:59],a[132:135],a[60:63],v[56:59]
s_add_u32 s66, s16, 4096
TR a[188:189],v235 offset:12800
TR a[190:191],v235 offset:14848
MM v[56:59],a[128:131],a[28:31],v[56:59]
s_waitcnt lgkmcnt(15)
buffer_store_dwordx4 v[16:19], v249, s[20:23], s67 offen offset:2048 sc1
MM v[56:59],a[156:159],a[252:255],v[56:59]
s_add_u32 s67, s47, 8192
TR a[128:129],v230 offset:32768
TR a[130:131],v230 offset:34816
MM v[56:59],a[152:155],a[236:239],v[56:59]
s_mov_b64 exec, s[8:9]
buffer_load_dwordx4 a[32:35], v250, s[20:23], s66 offen offset:0 sc1
s_mov_b64 exec, -1
CV v24,v24,v25
CV v25,v26,v27
MM v[56:59],a[148:151],a[220:223],v[56:59]
s_add_u32 s66, s44, 4096
TR a[132:133],v230 offset:36864
TR a[134:135],v230 offset:38912
MM v[56:59],a[144:147],a[204:207],v[56:59]
CV v26,v28,v29
CV v27,v30,v31
MM v[48:51],a[144:147],a[200:203],v[48:51]
TR a[136:137],v230 offset:40960
TR a[138:139],v230 offset:43008
MM v[48:51],a[148:151],a[216:219],v[48:51]
buffer_store_dwordx4 v[24:27], v234, s[20:23], s67 offen offset:3072 sc1
MM v[48:51],a[152:155],a[232:235],v[48:51]
TR a[140:141],v230 offset:45056
TR a[142:143],v230 offset:47104
MM v[48:51],a[156:159],a[248:251],v[48:51]
s_waitcnt lgkmcnt(15)
ds_bpermute_b32 v16, v245, v16
ds_bpermute_b32 v17, v245, v17
MM v[52:55],a[160:163],a[24:27],v[52:55]
TR a[144:145],v244 offset:32768
TR a[146:147],v244 offset:34816
MM v[52:55],a[164:167],a[56:59],v[52:55]
s_mov_b64 exec, s[10:11]
buffer_load_dwordx4 a[36:39], v250, s[20:23], s66 offen offset:0 sc1
s_mov_b64 exec, -1
ds_bpermute_b32 v18, v245, v18
MM v[52:55],a[168:171],a[88:91],v[52:55]
s_add_u32 s66, s16, 6144
TR a[148:149],v244 offset:36864
TR a[150:151],v244 offset:38912
MM v[52:55],a[172:175],a[120:123],v[52:55]
ds_bpermute_b32 v19, v245, v19
MM v[60:63],a[172:175],a[124:127],v[60:63]
)ASM"
R"ASM(
TR a[152:153],v244 offset:40960
TR a[154:155],v244 offset:43008
MM v[60:63],a[168:171],a[92:95],v[60:63]
MM v[60:63],a[164:167],a[60:63],v[60:63]
TR a[156:157],v244 offset:45056
TR a[158:159],v244 offset:47104
MM v[60:63],a[160:163],a[28:31],v[60:63]
s_waitcnt lgkmcnt(15)
s_mov_b64 exec, s[8:9]
buffer_load_dwordx4 a[40:43], v250, s[20:23], s66 offen offset:0 sc1
s_mov_b64 exec, -1
MM v[60:63],a[188:191],a[252:255],v[60:63]
s_add_u32 s66, s44, 6144
TR a[160:161],v231 offset:32768
TR a[162:163],v231 offset:34816
MM v[60:63],a[184:187],a[236:239],v[60:63]
s_waitcnt lgkmcnt(6)
v_or_b32 v19, v104, v19
MM v[60:63],a[180:183],a[220:223],v[60:63]
TR a[164:165],v231 offset:36864
TR a[166:167],v231 offset:38912
MM v[60:63],a[176:179],a[204:207],v[60:63]
MM v[52:55],a[176:179],a[200:203],v[52:55]
TR a[168:169],v231 offset:40960
TR a[170:171],v231 offset:43008
MM v[52:55],a[180:183],a[216:219],v[52:55]
s_mov_b64 exec, s[10:11]
buffer_load_dwordx4 a[44:47], v250, s[20:23], s66 offen offset:0 sc1
s_mov_b64 exec, -1
MM v[52:55],a[184:187],a[232:235],v[52:55]
s_add_u32 s66, s16, 8192
TR a[172:173],v231 offset:45056
TR a[174:175],v231 offset:47104
MM v[52:55],a[188:191],a[248:251],v[52:55]
MM v[80:83],a[128:131],a[24:27],v[80:83]
TR a[176:177],v235 offset:32768
TR a[178:179],v235 offset:34816
MM v[80:83],a[132:135],a[56:59],v[80:83]
v_mov_b32 v252, s69
s_mov_b64 exec, 1
buffer_store_dword v252, off, s[20:23], s19 offset:8 sc1
s_mov_b64 exec, -1
MM v[80:83],a[136:139],a[88:91],v[80:83]
TR a[180:181],v235 offset:36864
TR a[182:183],v235 offset:38912
MM v[80:83],a[140:143],a[120:123],v[80:83]
s_add_u32 s67, s46, 12288
CV v48,v48,v49
CV v49,v50,v51
MM v[88:91],a[140:143],a[124:127],v[88:91]
TR a[184:185],v235 offset:40960
TR a[186:187],v235 offset:43008
MM v[88:91],a[136:139],a[92:95],v[88:91]
s_mov_b64 exec, s[8:9]
buffer_load_dwordx4 a[192:195], v250, s[20:23], s66 offen offset:0 sc1
s_mov_b64 exec, -1
CV v50,v52,v53
CV v51,v54,v55
MM v[88:91],a[132:135],a[60:63],v[88:91]
s_add_u32 s66, s44, 8192
TR a[188:189],v235 offset:45056
TR a[190:191],v235 offset:47104
MM v[88:91],a[128:131],a[28:31],v[88:91]
s_waitcnt lgkmcnt(15)
buffer_store_dwordx4 v[48:51], v249, s[20:23], s67 offen offset:0 sc1
MM v[88:91],a[156:159],a[252:255],v[88:91]
s_add_u32 s67, s47, 12288
TR a[128:129],v230 offset:33280
TR a[130:131],v230 offset:35328
MM v[88:91],a[152:155],a[236:239],v[88:91]
s_waitcnt vmcnt(9)
CV v56,v56,v57
CV v57,v58,v59
MM v[88:91],a[148:151],a[220:223],v[88:91]
TR a[132:133],v230 offset:37376
TR a[134:135],v230 offset:39424
MM v[72:75],v[64:67],a[0:3],0
MM v[72:75],v[64:67],a[4:7],v[72:75]
MM v[72:75],v[68:71],a[8:11],v[72:75]
MM v[72:75],v[68:71],a[12:15],v[72:75]
MM v[88:91],a[144:147],a[204:207],v[88:91]
CV v58,v60,v61
CV v59,v62,v63
MM v[80:83],a[144:147],a[200:203],v[80:83]
TR a[136:137],v230 offset:41472
TR a[138:139],v230 offset:43520
MM v[80:83],a[148:151],a[216:219],v[80:83]
MM v[80:83],a[152:155],a[232:235],v[80:83]
TR a[140:141],v230 offset:45568
TR a[142:143],v230 offset:47616
MM v[80:83],a[156:159],a[248:251],v[80:83]
s_waitcnt lgkmcnt(15)
MM v[84:87],a[160:163],a[24:27],v[84:87]
TR a[144:145],v244 offset:33280
TR a[146:147],v244 offset:35328
MM v[84:87],a[164:167],a[56:59],v[84:87]
buffer_store_dwordx4 v[56:59], v234, s[20:23], s67 offen offset:1024 sc1
MM v[84:87],a[168:171],a[88:91],v[84:87]
TR a[148:149],v244 offset:37376
TR a[150:151],v244 offset:39424
MM v[84:87],a[172:175],a[120:123],v[84:87]
ds_bpermute_b32 v48, v245, v48
ds_bpermute_b32 v49, v245, v49
MM v[92:95],a[172:175],a[124:127],v[92:95]
TR a[152:153],v244 offset:41472
TR a[154:155],v244 offset:43520
MM v[92:95],a[168:171],a[92:95],v[92:95]
ds_bpermute_b32 v50, v245, v50
ds_bpermute_b32 v51, v245, v51
MM v[92:95],a[164:167],a[60:63],v[92:95]
TR a[156:157],v244 offset:45568
TR a[158:159],v244 offset:47616
MM v[92:95],a[160:163],a[28:31],v[92:95]
s_waitcnt lgkmcnt(15)
v_cmp_u_f32 s[30:31], v72, v72
CV v76,v72,v73
MM v[92:95],a[188:191],a[252:255],v[92:95]
TR a[160:161],v231 offset:33280
TR a[162:163],v231 offset:35328
MM v[92:95],a[184:187],a[236:239],v[92:95]
CV v77,v74,v75
MM v[92:95],a[180:183],a[220:223],v[92:95]
TR a[164:165],v231 offset:37376
TR a[166:167],v231 offset:39424
MM v[92:95],a[176:179],a[204:207],v[92:95]
s_waitcnt lgkmcnt(6)
ds_bpermute_b32 v76, v232, v76
v_or_b32 v51, v104, v51
MM v[84:87],a[176:179],a[200:203],v[84:87]
TR a[168:169],v231 offset:41472
TR a[170:171],v231 offset:43520
MM v[84:87],a[180:183],a[216:219],v[84:87]
ds_bpermute_b32 v77, v232, v77
v_lshlrev_b32 v10, 16, v0
MM v[84:87],a[184:187],a[232:235],v[84:87]
TR a[172:173],v231 offset:45568
TR a[174:175],v231 offset:47616
MM v[84:87],a[188:191],a[248:251],v[84:87]
v_and_b32 v11, 0xffff0000, v0
v_lshlrev_b32 v12, 16, v1
MM v[112:115],a[128:131],a[24:27],v[112:115]
s_cmp_eq_u32 s82, 0
s_cbranch_scc1 LY1_%=
v_xor_b32 v132, s69, v132
v_xor_b32 v133, s69, v133
v_xor_b32 v134, s69, v134
v_or3_b32 v139, v132, v133, v134
v_cmp_ne_u32 vcc, 0, v139
s_cbranch_vccnz LRYF_%=
LRYFb_%=:
s_ff1_i32_b32 s76, s82
s_and_b32 s76, s76, 7
s_mov_b32 s79, s52
s_cmp_eq_u32 s76, 1
s_cselect_b32 s79, s53, s79
s_cmp_eq_u32 s76, 2
s_cselect_b32 s79, s54, s79
s_cmp_eq_u32 s76, 3
s_cselect_b32 s79, s55, s79
s_cmp_eq_u32 s76, 4
s_cselect_b32 s79, s56, s79
s_cmp_eq_u32 s76, 5
s_cselect_b32 s79, s57, s79
s_cmp_eq_u32 s76, 6
s_cselect_b32 s79, s58, s79
s_cmp_eq_u32 s76, 7
s_cselect_b32 s79, s59, s79
s_mul_i32 s76, s76, 2560
s_mul_i32 s90, s84, 640
s_add_u32 s76, s76, s90
s_add_u32 s76, s76, 21659648
s_lshl_b32 s90, s79, 2
s_add_u32 s90, s90, 53248
buffer_load_dword v138, off, s[20:23], s90 sc1
s_mul_i32 s90, s79, 1280
s_nop 0
LPG24_%=:
s_cbranch_execz LTR3_%=
s_mul_i32 s77, s84, 320
s_add_u32 s90, s90, s77
s_add_u32 s90, s90, 131072
v_lshrrev_b32 v139, 1, v248
s_mov_b32 exec_lo, -1
s_mov_b32 exec_hi, 255
buffer_load_dwordx2 v[136:137], v139, s[20:23], s90 offen offset:0 sc1
s_mov_b32 exec_lo, -1
s_mov_b32 exec_hi, 255
buffer_load_dwordx4 v[160:163], v248, s[20:23], s76 offen offset:0 sc1
s_add_u32 s76, s76, 20480
buffer_load_dwordx4 v[164:167], v248, s[20:23], s76 offen offset:0 sc1
s_add_u32 s76, s76, 20480
buffer_load_dwordx4 v[168:171], v248, s[20:23], s76 offen offset:0 sc1
s_add_u32 s76, s76, 20480
buffer_load_dwordx4 v[172:175], v248, s[20:23], s76 offen offset:0 sc1
s_add_u32 s76, s76, 20480
buffer_load_dwordx4 v[176:179], v248, s[20:23], s76 offen offset:0 sc1
s_add_u32 s76, s76, 20480
buffer_load_dwordx4 v[180:183], v248, s[20:23], s76 offen offset:0 sc1
s_add_u32 s76, s76, 20480
s_mov_b64 exec, -1
LY1_%=:
TR a[176:177],v235 offset:33280
TR a[178:179],v235 offset:35328
MM v[112:115],a[132:135],a[56:59],v[112:115]
s_mov_b64 exec, s[10:11]
buffer_load_dwordx4 a[196:199], v250, s[20:23], s66 offen offset:0 sc1
s_mov_b64 exec, -1
MM v[112:115],a[136:139],a[88:91],v[112:115]
TR a[180:181],v235 offset:37376
TR a[182:183],v235 offset:39424
MM v[112:115],a[140:143],a[120:123],v[112:115]
s_add_u32 s67, s46, 12288
CV v80,v80,v81
CV v81,v82,v83
MM v[120:123],a[140:143],a[124:127],v[120:123]
TR a[184:185],v235 offset:41472
TR a[186:187],v235 offset:43520
MM v[120:123],a[136:139],a[92:95],v[120:123]
CV v82,v84,v85
CV v83,v86,v87
MM v[120:123],a[132:135],a[60:63],v[120:123]
TR a[188:189],v235 offset:45568
TR a[190:191],v235 offset:47616
MM v[120:123],a[128:131],a[28:31],v[120:123]
s_waitcnt lgkmcnt(15)
buffer_store_dwordx4 v[80:83], v249, s[20:23], s67 offen offset:2048 sc1
MM v[120:123],a[156:159],a[252:255],v[120:123]
s_add_u32 s67, s47, 12288
TR a[128:129],v228 offset:16384
TR a[130:131],v228 offset:17408
MM v[120:123],a[152:155],a[236:239],v[120:123]
CV v88,v88,v89
CV v89,v90,v91
MM v[120:123],a[148:151],a[220:223],v[120:123]
TR a[132:133],v228 offset:18432
TR a[134:135],v228 offset:19456
MM v[120:123],a[144:147],a[204:207],v[120:123]
CV v90,v92,v93
CV v91,v94,v95
MM v[112:115],a[144:147],a[200:203],v[112:115]
TR a[136:137],v228 offset:20480
TR a[138:139],v228 offset:21504
MM v[112:115],a[148:151],a[216:219],v[112:115]
v_and_b32 v13, 0xffff0000, v1
v_lshlrev_b32 v14, 16, v32
MM v[112:115],a[152:155],a[232:235],v[112:115]
TR a[140:141],v228 offset:22528
TR a[142:143],v228 offset:23552
MM v[112:115],a[156:159],a[248:251],v[112:115]
s_waitcnt lgkmcnt(15)
buffer_store_dwordx4 v[88:91], v234, s[20:23], s67 offen offset:3072 sc1
MM v[116:119],a[160:163],a[24:27],v[116:119]
TR a[144:145],v228 offset:24576
TR a[146:147],v228 offset:25600
MM v[116:119],a[164:167],a[56:59],v[116:119]
buffer_load_dword v242, v253, s[20:23], s68 offen offset:8 sc1
buffer_load_dword v142, v253, s[20:23], s45 offen offset:8 sc1
MM v[116:119],a[168:171],a[88:91],v[116:119]
TR a[148:149],v228 offset:26624
TR a[150:151],v228 offset:27648
MM v[116:119],a[172:175],a[120:123],v[116:119]
ds_bpermute_b32 v80, v245, v80
ds_bpermute_b32 v81, v245, v81
MM v[124:127],a[172:175],a[124:127],v[124:127]
TR a[152:153],v228 offset:28672
TR a[154:155],v228 offset:29696
MM v[124:127],a[168:171],a[92:95],v[124:127]
ds_bpermute_b32 v82, v245, v82
ds_bpermute_b32 v83, v245, v83
MM v[124:127],a[164:167],a[60:63],v[124:127]
TR a[156:157],v228 offset:30720
TR a[158:159],v228 offset:31744
MM v[124:127],a[160:163],a[28:31],v[124:127]
s_waitcnt lgkmcnt(15)
v_and_b32 v15, 0xffff0000, v32
v_lshlrev_b32 v42, 16, v33
MM v[124:127],a[188:191],a[252:255],v[124:127]
s_cmp_eq_u32 s82, 0
s_cbranch_scc1 LY2_%=
s_mov_b32 exec_lo, -1
s_mov_b32 exec_hi, 255
buffer_load_dwordx4 v[184:187], v248, s[20:23], s76 offen offset:0 sc1
s_add_u32 s76, s76, 20480
buffer_load_dwordx4 v[188:191], v248, s[20:23], s76 offen offset:0 sc1
s_add_u32 s76, s76, 20480
buffer_load_dwordx4 v[192:195], v248, s[20:23], s76 offen offset:0 sc1
s_add_u32 s76, s76, 20480
buffer_load_dwordx4 v[196:199], v248, s[20:23], s76 offen offset:0 sc1
s_add_u32 s76, s76, 20480
buffer_load_dwordx4 v[200:203], v248, s[20:23], s76 offen offset:0 sc1
s_add_u32 s76, s76, 20480
buffer_load_dwordx4 v[204:207], v248, s[20:23], s76 offen offset:0 sc1
s_add_u32 s76, s76, 20480
s_mov_b64 exec, -1
LY2_%=:
TR a[160:161],v229 offset:16384
TR a[162:163],v229 offset:17408
MM v[124:127],a[184:187],a[236:239],v[124:127]
v_and_b32 v43, 0xffff0000, v33
s_add_u32 s80, s18, 0
MM v[124:127],a[180:183],a[220:223],v[124:127]
TR a[164:165],v229 offset:18432
TR a[166:167],v229 offset:19456
MM v[124:127],a[176:179],a[204:207],v[124:127]
s_waitcnt lgkmcnt(6)
v_or_b32 v83, v104, v83
v_lshlrev_b32 v72, 16, v76
MM v[116:119],a[176:179],a[200:203],v[116:119]
TR a[168:169],v229 offset:20480
TR a[170:171],v229 offset:21504
MM v[116:119],a[180:183],a[216:219],v[116:119]
v_and_b32 v73, 0xffff0000, v76
v_lshlrev_b32 v74, 16, v77
MM v[116:119],a[184:187],a[232:235],v[116:119]
TR a[172:173],v229 offset:22528
TR a[174:175],v229 offset:23552
MM v[116:119],a[188:191],a[248:251],v[116:119]
v_and_b32 v75, 0xffff0000, v77
v_mul_f32 v72, v72, v10
MM v[144:147],a[128:131],a[24:27],v[144:147]
TR a[176:177],v229 offset:24576
TR a[178:179],v229 offset:25600
MM v[144:147],a[132:135],a[56:59],v[144:147]
v_mul_f32 v73, v73, v11
v_mul_f32 v74, v74, v12
MM v[144:147],a[136:139],a[88:91],v[144:147]
TR a[180:181],v229 offset:26624
TR a[182:183],v229 offset:27648
MM v[144:147],a[140:143],a[120:123],v[144:147]
s_add_u32 s67, s46, 16384
CV v112,v112,v113
CV v113,v114,v115
MM v[152:155],a[140:143],a[124:127],v[152:155]
TR a[184:185],v229 offset:28672
TR a[186:187],v229 offset:29696
MM v[152:155],a[136:139],a[92:95],v[152:155]
CV v114,v116,v117
CV v115,v118,v119
MM v[152:155],a[132:135],a[60:63],v[152:155]
TR a[188:189],v229 offset:30720
TR a[190:191],v229 offset:31744
MM v[152:155],a[128:131],a[28:31],v[152:155]
s_waitcnt lgkmcnt(15)
buffer_store_dwordx4 v[112:115], v249, s[20:23], s67 offen offset:0 sc1
MM v[152:155],a[156:159],a[252:255],v[152:155]
s_add_u32 s67, s47, 16384
CV v120,v120,v121
CV v121,v122,v123
MM v[152:155],a[152:155],a[236:239],v[152:155]
CV v122,v124,v125
CV v123,v126,v127
MM v[152:155],a[148:151],a[220:223],v[152:155]
buffer_store_dwordx4 v[120:123], v234, s[20:23], s67 offen offset:1024 sc1
MM v[152:155],a[144:147],a[204:207],v[152:155]
)ASM"
R"ASM(
v_mul_f32 v75, v75, v13
CV v76,v72,v73
MM v[144:147],a[144:147],a[200:203],v[144:147]
ds_bpermute_b32 v112, v245, v112
ds_bpermute_b32 v113, v245, v113
MM v[144:147],a[148:151],a[216:219],v[144:147]
ds_bpermute_b32 v114, v245, v114
ds_bpermute_b32 v115, v245, v115
MM v[144:147],a[152:155],a[232:235],v[144:147]
CV v77,v74,v75
v_lshlrev_b32 v72, 16, v76
MM v[144:147],a[156:159],a[248:251],v[144:147]
s_waitcnt lgkmcnt(12)
v_and_b32 v73, 0xffff0000, v76
v_lshlrev_b32 v74, 16, v77
MM v[148:151],a[160:163],a[24:27],v[148:151]
s_cmp_eq_u32 s82, 0
s_cbranch_scc1 LY3_%=
s_mov_b32 exec_lo, -1
s_mov_b32 exec_hi, 255
buffer_load_dwordx4 v[208:211], v248, s[20:23], s76 offen offset:0 sc1
s_add_u32 s76, s76, 20480
buffer_load_dwordx4 v[212:215], v248, s[20:23], s76 offen offset:0 sc1
s_add_u32 s76, s76, 20480
buffer_load_dwordx4 v[216:219], v248, s[20:23], s76 offen offset:0 sc1
s_add_u32 s76, s76, 20480
buffer_load_dwordx4 v[220:223], v248, s[20:23], s76 offen offset:0 sc1
s_add_u32 s76, s76, 20480
buffer_load_dwordx4 v[96:99], v248, s[20:23], s76 offen offset:0 sc1
s_add_u32 s76, s76, 20480
buffer_load_dwordx4 v[100:103], v248, s[20:23], s76 offen offset:0 sc1
s_mov_b64 exec, -1
LY3_%=:
v_and_b32 v75, 0xffff0000, v77
v_add_f32 v72, v72, v14
MM v[148:151],a[164:167],a[56:59],v[148:151]
v_add_f32 v73, v73, v15
v_add_f32 v74, v74, v42
MM v[148:151],a[168:171],a[88:91],v[148:151]
v_add_f32 v75, v75, v43
CV v44,v72,v73
MM v[148:151],a[172:175],a[120:123],v[148:151]
s_waitcnt lgkmcnt(0)
v_or_b32 v115, v104, v115
CV v45,v74,v75
MM v[156:159],a[172:175],a[124:127],v[156:159]
s_cmp_eq_u64 s[30:31], 0
s_cselect_b32 s94, s51, 0
s_and_b32 s94, s94, 1
MM v[156:159],a[168:171],a[92:95],v[156:159]
s_xor_b32 s17, s17, s94
s_cmp_lg_u32 s94, 0
s_cselect_b64 exec, -1, 0
buffer_store_dwordx2 v[44:45], v251, s[40:43], s80 offen offset:0 nt
s_mov_b64 exec, -1
MM v[156:159],a[164:167],a[60:63],v[156:159]
MM v[156:159],a[160:163],a[28:31],v[156:159]
MM v[156:159],a[188:191],a[252:255],v[156:159]
MM v[156:159],a[184:187],a[236:239],v[156:159]
MM v[156:159],a[180:183],a[220:223],v[156:159]
MM v[156:159],a[176:179],a[204:207],v[156:159]
MM v[148:151],a[176:179],a[200:203],v[148:151]
MM v[148:151],a[180:183],a[216:219],v[148:151]
MM v[148:151],a[184:187],a[232:235],v[148:151]
MM v[148:151],a[188:191],a[248:251],v[148:151]
s_nop 5
s_add_u32 s67, s46, 16384
CV v144,v144,v145
CV v145,v146,v147
CV v146,v148,v149
CV v147,v150,v151
buffer_store_dwordx4 v[144:147], v249, s[20:23], s67 offen offset:2048 sc1
s_add_u32 s67, s47, 16384
CV v152,v152,v153
CV v153,v154,v155
CV v154,v156,v157
CV v155,v158,v159
buffer_store_dwordx4 v[152:155], v234, s[20:23], s67 offen offset:3072 sc1
ds_bpermute_b32 v144, v245, v144
ds_bpermute_b32 v145, v245, v145
ds_bpermute_b32 v146, v245, v146
ds_bpermute_b32 v147, v245, v147
s_waitcnt vmcnt(5)
v_cmp_eq_u32 s[12:13], s69, v242
v_cmp_eq_u32 s[14:15], s69, v142
s_nop 0
s_and_b32 s12, s12, 0xffff0000
s_nop 0
s_add_u32 s66, s16, 10240
s_mov_b64 exec, s[12:13]
buffer_load_dwordx4 v[16:19], v250, s[20:23], s66 offen offset:0 sc1
s_mov_b64 exec, -1
s_add_u32 s66, s44, 10240
s_mov_b64 exec, s[14:15]
buffer_load_dwordx4 a[68:71], v250, s[20:23], s66 offen offset:0 sc1
s_mov_b64 exec, -1
s_add_u32 s66, s16, 12288
s_mov_b64 exec, s[12:13]
buffer_load_dwordx4 v[48:51], v250, s[20:23], s66 offen offset:0 sc1
s_mov_b64 exec, -1
s_add_u32 s66, s44, 12288
s_mov_b64 exec, s[14:15]
buffer_load_dwordx4 a[76:79], v250, s[20:23], s66 offen offset:0 sc1
s_mov_b64 exec, -1
s_add_u32 s66, s16, 14336
s_mov_b64 exec, s[12:13]
buffer_load_dwordx4 v[80:83], v250, s[20:23], s66 offen offset:0 sc1
s_mov_b64 exec, -1
s_add_u32 s66, s44, 14336
s_mov_b64 exec, s[14:15]
buffer_load_dwordx4 a[100:103], v250, s[20:23], s66 offen offset:0 sc1
s_mov_b64 exec, -1
s_add_u32 s66, s16, 16384
s_mov_b64 exec, s[12:13]
buffer_load_dwordx4 v[112:115], v250, s[20:23], s66 offen offset:0 sc1
s_mov_b64 exec, -1
s_add_u32 s66, s44, 16384
s_mov_b64 exec, s[14:15]
buffer_load_dwordx4 a[108:111], v250, s[20:23], s66 offen offset:0 sc1
s_mov_b64 exec, -1
s_waitcnt lgkmcnt(0)
v_or_b32 v147, v104, v147
s_add_u32 s66, s16, 18432
s_mov_b64 exec, s[12:13]
buffer_load_dwordx4 v[144:147], v250, s[20:23], s66 offen offset:0 sc1
s_mov_b64 exec, -1
s_add_u32 s66, s44, 18432
s_mov_b64 exec, s[14:15]
buffer_load_dwordx4 a[212:215], v250, s[20:23], s66 offen offset:0 sc1
s_mov_b64 exec, -1
buffer_load_dwordx4 v[240:243], v253, s[20:23], s68 offen offset:0 sc1
buffer_load_dwordx4 v[140:143], v253, s[20:23], s45 offen offset:0 sc1
v_lshlrev_b32 v10, 16, v246
v_and_b32 v11, 0xffff0000, v246
v_lshlrev_b32 v12, 16, v247
v_and_b32 v13, 0xffff0000, v247
v_mul_f32 v14, 0xbfb8aa3b, v10
v_mul_f32 v15, 0xbfb8aa3b, v11
v_mul_f32 v42, 0xbfb8aa3b, v12
v_mul_f32 v43, 0xbfb8aa3b, v13
EX v14,v14
EX v15,v15
EX v42,v42
EX v43,v43
v_add_f32 v14, 1.0, v14
v_add_f32 v15, 1.0, v15
v_add_f32 v42, 1.0, v42
v_add_f32 v43, 1.0, v43
v_rcp_f32 v14, v14
v_rcp_f32 v15, v15
v_rcp_f32 v42, v42
v_rcp_f32 v43, v43
v_mul_f32 v14, v10, v14
v_mul_f32 v15, v11, v15
v_mul_f32 v42, v12, v42
v_mul_f32 v43, v13, v43
CV v8,v14,v15
CV v9,v42,v43
s_cmp_lg_u32 s82, 0
s_cbranch_scc1 LYS_%=
LYE_%=:
s_mov_b32 s73, 30001
s_mov_b32 s65, 1
s_bitcmp0_b32 s17, 0
s_cbranch_scc1 LSA0_%=
MM v[72:75],v[64:67],a[0:3],0
MM v[72:75],v[64:67],a[4:7],v[72:75]
MM v[72:75],v[68:71],a[8:11],v[72:75]
MM v[72:75],v[68:71],a[12:15],v[72:75]
s_nop 7
v_cmp_u_f32 s[30:31], v72, v72
s_nop 1
s_cmp_lg_u64 s[30:31], 0
s_cbranch_scc1 LSA0_%=
s_bitset0_b32 s17, 0
CV v76,v72,v73
CV v77,v74,v75
ds_bpermute_b32 v76, v232, v76
ds_bpermute_b32 v77, v232, v77
v_lshlrev_b32 v10, 16, v0
v_and_b32 v11, 0xffff0000, v0
v_lshlrev_b32 v12, 16, v1
v_and_b32 v13, 0xffff0000, v1
v_lshlrev_b32 v14, 16, v32
v_and_b32 v15, 0xffff0000, v32
v_lshlrev_b32 v42, 16, v33
v_and_b32 v43, 0xffff0000, v33
s_add_u32 s80, s18, 0
s_waitcnt lgkmcnt(0)
v_lshlrev_b32 v72, 16, v76
v_and_b32 v73, 0xffff0000, v76
v_lshlrev_b32 v74, 16, v77
v_and_b32 v75, 0xffff0000, v77
v_mul_f32 v72, v72, v10
v_mul_f32 v73, v73, v11
v_mul_f32 v74, v74, v12
v_mul_f32 v75, v75, v13
CV v76,v72,v73
CV v77,v74,v75
v_lshlrev_b32 v72, 16, v76
v_and_b32 v73, 0xffff0000, v76
v_lshlrev_b32 v74, 16, v77
v_and_b32 v75, 0xffff0000, v77
v_add_f32 v72, v72, v14
v_add_f32 v73, v73, v15
v_add_f32 v74, v74, v42
v_add_f32 v75, v75, v43
CV v44,v72,v73
CV v45,v74,v75
buffer_store_dwordx2 v[44:45], v251, s[40:43], s80 offen offset:0 nt
s_nop 1
LSA0_%=:
s_bitcmp0_b32 s17, 1
s_cbranch_scc1 LSA1_%=
MM v[72:75],v[64:67],a[32:35],0
MM v[72:75],v[64:67],a[36:39],v[72:75]
MM v[72:75],v[68:71],a[40:43],v[72:75]
MM v[72:75],v[68:71],a[44:47],v[72:75]
s_nop 7
v_cmp_u_f32 s[30:31], v72, v72
s_nop 1
s_cmp_lg_u64 s[30:31], 0
s_cbranch_scc1 LSA1_%=
s_bitset0_b32 s17, 1
CV v76,v72,v73
CV v77,v74,v75
ds_bpermute_b32 v76, v232, v76
ds_bpermute_b32 v77, v232, v77
v_lshlrev_b32 v10, 16, v2
v_and_b32 v11, 0xffff0000, v2
v_lshlrev_b32 v12, 16, v3
v_and_b32 v13, 0xffff0000, v3
v_lshlrev_b32 v14, 16, v34
v_and_b32 v15, 0xffff0000, v34
v_lshlrev_b32 v42, 16, v35
v_and_b32 v43, 0xffff0000, v35
s_add_u32 s80, s18, 128
s_waitcnt lgkmcnt(0)
v_lshlrev_b32 v72, 16, v76
v_and_b32 v73, 0xffff0000, v76
v_lshlrev_b32 v74, 16, v77
v_and_b32 v75, 0xffff0000, v77
v_mul_f32 v72, v72, v10
v_mul_f32 v73, v73, v11
v_mul_f32 v74, v74, v12
v_mul_f32 v75, v75, v13
CV v76,v72,v73
CV v77,v74,v75
v_lshlrev_b32 v72, 16, v76
v_and_b32 v73, 0xffff0000, v76
v_lshlrev_b32 v74, 16, v77
v_and_b32 v75, 0xffff0000, v77
v_add_f32 v72, v72, v14
v_add_f32 v73, v73, v15
v_add_f32 v74, v74, v42
v_add_f32 v75, v75, v43
CV v46,v72,v73
CV v47,v74,v75
buffer_store_dwordx2 v[46:47], v251, s[40:43], s80 offen offset:0 nt
s_nop 1
LSA1_%=:
s_waitcnt vmcnt(0)
s_mov_b64 exec, 1
buffer_store_dword v233, off, s[20:23], s19 offset:12 sc1
s_mov_b64 exec, -1
MM v[72:75],v[64:67],a[192:195],0
MM v[100:103],v[64:67],v[80:83],0
MM v[96:99],v[64:67],v[16:19],0
MM v[72:75],v[64:67],a[196:199],v[72:75]
MM v[100:103],v[64:67],a[100:103],v[100:103]
MM v[96:99],v[64:67],a[68:71],v[96:99]
MM v[72:75],v[68:71],v[144:147],v[72:75]
MM v[100:103],v[68:71],v[112:115],v[100:103]
MM v[96:99],v[68:71],v[48:51],v[96:99]
MM v[72:75],v[68:71],a[212:215],v[72:75]
MM v[100:103],v[68:71],a[108:111],v[100:103]
MM v[96:99],v[68:71],a[76:79],v[96:99]
s_mov_b32 s65, 0
s_mov_b32 s90, 250
s_nop 5
v_cmp_u_f32 vcc, v72, v100
v_cmp_u_f32 s[30:31], v96, v96
s_nop 1
s_or_b64 vcc, vcc, s[30:31]
s_cbranch_scc1 LQS_%=
CV v76,v72,v73
CV v77,v74,v75
CV v108,v100,v101
CV v109,v102,v103
CV v78,v96,v97
CV v79,v98,v99
ds_bpermute_b32 v76, v232, v76
ds_bpermute_b32 v77, v232, v77
ds_bpermute_b32 v108, v232, v108
ds_bpermute_b32 v109, v232, v109
ds_bpermute_b32 v78, v232, v78
ds_bpermute_b32 v79, v232, v79
v_lshlrev_b32 v10, 16, v8
v_and_b32 v11, 0xffff0000, v8
v_lshlrev_b32 v12, 16, v9
v_and_b32 v13, 0xffff0000, v9
v_lshlrev_b32 v14, 16, v40
v_and_b32 v15, 0xffff0000, v40
v_lshlrev_b32 v42, 16, v41
v_and_b32 v43, 0xffff0000, v41
s_add_u32 s80, s63, 0
s_waitcnt lgkmcnt(4)
v_lshlrev_b32 v72, 16, v76
v_and_b32 v73, 0xffff0000, v76
v_lshlrev_b32 v74, 16, v77
v_and_b32 v75, 0xffff0000, v77
v_mul_f32 v72, v72, v10
v_mul_f32 v73, v73, v11
v_mul_f32 v74, v74, v12
v_mul_f32 v75, v75, v13
CV v76,v72,v73
CV v77,v74,v75
v_lshlrev_b32 v72, 16, v76
v_and_b32 v73, 0xffff0000, v76
v_lshlrev_b32 v74, 16, v77
v_and_b32 v75, 0xffff0000, v77
v_add_f32 v72, v72, v14
v_add_f32 v73, v73, v15
v_add_f32 v74, v74, v42
v_add_f32 v75, v75, v43
CV v44,v72,v73
CV v45,v74,v75
buffer_store_dwordx2 v[44:45], v254, s[40:43], s80 offen offset:0 nt
v_lshlrev_b32 v10, 16, v6
v_and_b32 v11, 0xffff0000, v6
v_lshlrev_b32 v12, 16, v7
v_and_b32 v13, 0xffff0000, v7
v_lshlrev_b32 v14, 16, v38
v_and_b32 v15, 0xffff0000, v38
v_lshlrev_b32 v42, 16, v39
v_and_b32 v43, 0xffff0000, v39
s_add_u32 s80, s18, 41088
s_waitcnt lgkmcnt(2)
v_lshlrev_b32 v100, 16, v108
v_and_b32 v101, 0xffff0000, v108
v_lshlrev_b32 v102, 16, v109
v_and_b32 v103, 0xffff0000, v109
v_mul_f32 v100, v100, v10
v_mul_f32 v101, v101, v11
v_mul_f32 v102, v102, v12
v_mul_f32 v103, v103, v13
CV v108,v100,v101
CV v109,v102,v103
v_lshlrev_b32 v100, 16, v108
v_and_b32 v101, 0xffff0000, v108
v_lshlrev_b32 v102, 16, v109
v_and_b32 v103, 0xffff0000, v109
v_add_f32 v100, v100, v14
v_add_f32 v101, v101, v15
v_add_f32 v102, v102, v42
v_add_f32 v103, v103, v43
CV v46,v100,v101
CV v47,v102,v103
buffer_store_dwordx2 v[46:47], v251, s[40:43], s80 offen offset:0 nt
v_lshlrev_b32 v10, 16, v4
v_and_b32 v11, 0xffff0000, v4
v_lshlrev_b32 v12, 16, v5
v_and_b32 v13, 0xffff0000, v5
v_lshlrev_b32 v14, 16, v36
v_and_b32 v15, 0xffff0000, v36
v_lshlrev_b32 v42, 16, v37
v_and_b32 v43, 0xffff0000, v37
s_add_u32 s80, s18, 40960
s_waitcnt lgkmcnt(0)
v_lshlrev_b32 v96, 16, v78
v_and_b32 v97, 0xffff0000, v78
v_lshlrev_b32 v98, 16, v79
v_and_b32 v99, 0xffff0000, v79
v_mul_f32 v96, v96, v10
v_mul_f32 v97, v97, v11
v_mul_f32 v98, v98, v12
v_mul_f32 v99, v99, v13
CV v78,v96,v97
CV v79,v98,v99
v_lshlrev_b32 v96, 16, v78
v_and_b32 v97, 0xffff0000, v78
v_lshlrev_b32 v98, 16, v79
v_and_b32 v99, 0xffff0000, v79
v_add_f32 v96, v96, v14
v_add_f32 v97, v97, v15
v_add_f32 v98, v98, v42
v_add_f32 v99, v99, v43
CV v44,v96,v97
CV v45,v98,v99
buffer_store_dwordx2 v[44:45], v251, s[40:43], s80 offen offset:0 nt
s_and_b32 s17, s17, 3
s_cbranch_scc0 LQE_%=
LQS_%=:
v_cmp_eq_u32 s[12:13], s69, v242
v_cmp_eq_u32 s[14:15], s69, v142
v_cmp_eq_u32 s[8:9], s69, v241
v_cmp_eq_u32 s[10:11], s69, v141
v_cmp_eq_u32 s[36:37], s69, v240
v_cmp_eq_u32 s[38:39], s69, v140
s_mov_b32 s94, 0
s_sub_u32 s90, s90, 1
s_cbranch_scc1 LSW_%=
s_and_b64 vcc, s[12:13], s[14:15]
s_and_b32 s12, s12, 0xffff0000
s_and_b32 s8, s8, 0xffff0000
s_and_b32 s36, s36, 0xffff0000
s_bitcmp0_b32 s17, 4
s_cbranch_scc1 LQG4_%=
AR v108,a195
AR v109,a199
AR v111,a215
v_cmp_eq_u32 s[24:25], -1, v108
v_cmp_eq_u32 s[26:27], -1, v109
v_cmp_eq_u32 s[28:29], -1, v147
v_cmp_eq_u32 s[46:47], -1, v111
s_nop 0
s_or_b64 s[30:31], s[24:25], s[26:27]
s_or_b64 s[30:31], s[30:31], s[28:29]
s_or_b64 s[30:31], s[30:31], s[46:47]
)ASM"
R"ASM(
s_cselect_b32 s76, 256, 16
s_or_b32 s94, s94, s76
s_bitcmp1_b32 s94, 4
s_cbranch_scc1 LQG4_%=
s_and_b64 exec, s[46:47], s[14:15]
s_cbranch_scc0 LQK43_%=
s_add_u32 s66, s44, 18432
buffer_load_dwordx4 a[212:215], v250, s[20:23], s66 offen offset:0 sc1
LQK43_%=:
s_and_b64 exec, s[28:29], s[12:13]
s_cbranch_scc0 LQK42_%=
s_add_u32 s66, s16, 18432
buffer_load_dwordx4 v[144:147], v250, s[20:23], s66 offen offset:0 sc1
LQK42_%=:
s_and_b64 exec, s[26:27], s[10:11]
s_cbranch_scc0 LQK41_%=
s_add_u32 s66, s44, 8192
buffer_load_dwordx4 a[196:199], v250, s[20:23], s66 offen offset:0 sc1
LQK41_%=:
s_and_b64 exec, s[24:25], s[8:9]
s_cbranch_scc0 LQK40_%=
s_add_u32 s66, s16, 8192
buffer_load_dwordx4 a[192:195], v250, s[20:23], s66 offen offset:0 sc1
LQK40_%=:
s_mov_b64 exec, -1
LQG4_%=:
s_bitcmp0_b32 s17, 3
s_cbranch_scc1 LQG3_%=
AR v109,a103
AR v111,a111
v_cmp_eq_u32 s[24:25], -1, v83
v_cmp_eq_u32 s[26:27], -1, v109
v_cmp_eq_u32 s[28:29], -1, v115
v_cmp_eq_u32 s[46:47], -1, v111
s_nop 0
s_or_b64 s[30:31], s[24:25], s[26:27]
s_or_b64 s[30:31], s[30:31], s[28:29]
s_or_b64 s[30:31], s[30:31], s[46:47]
s_cselect_b32 s76, 256, 8
s_or_b32 s94, s94, s76
s_bitcmp1_b32 s94, 3
s_cbranch_scc1 LQG3_%=
s_nop 0
s_nop 0
s_nop 0
s_nop 0
s_nop 0
s_nop 0
s_nop 0
s_nop 0
s_nop 0
s_nop 0
s_nop 0
LPG25_%=:
s_nop 0
s_and_b64 exec, s[46:47], s[14:15]
s_cbranch_scc0 LQK33_%=
s_add_u32 s66, s44, 16384
buffer_load_dwordx4 a[108:111], v250, s[20:23], s66 offen offset:0 sc1
LQK33_%=:
s_and_b64 exec, s[28:29], s[12:13]
s_cbranch_scc0 LQK32_%=
s_add_u32 s66, s16, 16384
buffer_load_dwordx4 v[112:115], v250, s[20:23], s66 offen offset:0 sc1
LQK32_%=:
s_and_b64 exec, s[26:27], s[14:15]
s_cbranch_scc0 LQK31_%=
s_add_u32 s66, s44, 14336
buffer_load_dwordx4 a[100:103], v250, s[20:23], s66 offen offset:0 sc1
LQK31_%=:
s_and_b64 exec, s[24:25], s[12:13]
s_cbranch_scc0 LQK30_%=
s_add_u32 s66, s16, 14336
buffer_load_dwordx4 v[80:83], v250, s[20:23], s66 offen offset:0 sc1
LQK30_%=:
s_mov_b64 exec, -1
LQG3_%=:
s_bitcmp0_b32 s17, 2
s_cbranch_scc1 LQG2_%=
AR v109,a71
AR v111,a79
v_cmp_eq_u32 s[24:25], -1, v19
v_cmp_eq_u32 s[26:27], -1, v109
v_cmp_eq_u32 s[28:29], -1, v51
v_cmp_eq_u32 s[46:47], -1, v111
s_nop 0
s_or_b64 s[30:31], s[24:25], s[26:27]
s_or_b64 s[30:31], s[30:31], s[28:29]
s_or_b64 s[30:31], s[30:31], s[46:47]
s_cselect_b32 s76, 256, 4
s_or_b32 s94, s94, s76
s_bitcmp1_b32 s94, 2
s_cbranch_scc1 LQG2_%=
s_and_b64 exec, s[46:47], s[14:15]
s_cbranch_scc0 LQK23_%=
s_add_u32 s66, s44, 12288
buffer_load_dwordx4 a[76:79], v250, s[20:23], s66 offen offset:0 sc1
LQK23_%=:
s_and_b64 exec, s[28:29], s[12:13]
s_cbranch_scc0 LQK22_%=
s_add_u32 s66, s16, 12288
buffer_load_dwordx4 v[48:51], v250, s[20:23], s66 offen offset:0 sc1
LQK22_%=:
s_and_b64 exec, s[26:27], s[14:15]
s_cbranch_scc0 LQK21_%=
s_add_u32 s66, s44, 10240
buffer_load_dwordx4 a[68:71], v250, s[20:23], s66 offen offset:0 sc1
LQK21_%=:
s_and_b64 exec, s[24:25], s[12:13]
s_cbranch_scc0 LQK20_%=
s_add_u32 s66, s16, 10240
buffer_load_dwordx4 v[16:19], v250, s[20:23], s66 offen offset:0 sc1
LQK20_%=:
s_mov_b64 exec, -1
LQG2_%=:
s_and_b32 s76, s17, 3
s_cbranch_scc1 LQA_%=
LQB_%=:
s_cmp_eq_u64 vcc, -1
s_cbranch_scc1 LQF_%=
buffer_load_dwordx4 v[240:243], v253, s[20:23], s68 offen offset:0 sc1
buffer_load_dwordx4 v[140:143], v253, s[20:23], s45 offen offset:0 sc1
LQF_%=:
s_and_b32 s76, s94, 3
s_cbranch_scc1 LQC_%=
LQD_%=:
s_bitcmp0_b32 s94, 2
s_cbranch_scc1 LQN2_%=
MM v[72:75],v[64:67],v[16:19],0
MM v[72:75],v[64:67],a[68:71],v[72:75]
MM v[72:75],v[68:71],v[48:51],v[72:75]
MM v[72:75],v[68:71],a[76:79],v[72:75]
s_bitset0_b32 s17, 2
s_nop 6
CV v76,v72,v73
CV v77,v74,v75
ds_bpermute_b32 v76, v232, v76
ds_bpermute_b32 v77, v232, v77
v_lshlrev_b32 v10, 16, v4
v_and_b32 v11, 0xffff0000, v4
v_lshlrev_b32 v12, 16, v5
v_and_b32 v13, 0xffff0000, v5
v_lshlrev_b32 v14, 16, v36
v_and_b32 v15, 0xffff0000, v36
v_lshlrev_b32 v42, 16, v37
v_and_b32 v43, 0xffff0000, v37
s_add_u32 s80, s18, 40960
s_waitcnt lgkmcnt(0)
v_lshlrev_b32 v72, 16, v76
v_and_b32 v73, 0xffff0000, v76
v_lshlrev_b32 v74, 16, v77
v_and_b32 v75, 0xffff0000, v77
v_mul_f32 v72, v72, v10
v_mul_f32 v73, v73, v11
v_mul_f32 v74, v74, v12
v_mul_f32 v75, v75, v13
CV v76,v72,v73
CV v77,v74,v75
v_lshlrev_b32 v72, 16, v76
v_and_b32 v73, 0xffff0000, v76
v_lshlrev_b32 v74, 16, v77
v_and_b32 v75, 0xffff0000, v77
v_add_f32 v72, v72, v14
v_add_f32 v73, v73, v15
v_add_f32 v74, v74, v42
v_add_f32 v75, v75, v43
CV v44,v72,v73
CV v45,v74,v75
buffer_store_dwordx2 v[44:45], v251, s[40:43], s80 offen offset:0 nt
s_bitcmp0_b32 s94, 8
s_cbranch_scc1 LQN2_%=
s_waitcnt vmcnt(1)
s_branch LQS_%=
LQN2_%=:
s_bitcmp0_b32 s94, 3
s_cbranch_scc1 LQN3_%=
MM v[72:75],v[64:67],v[80:83],0
MM v[72:75],v[64:67],a[100:103],v[72:75]
MM v[72:75],v[68:71],v[112:115],v[72:75]
MM v[72:75],v[68:71],a[108:111],v[72:75]
s_bitset0_b32 s17, 3
s_nop 6
CV v76,v72,v73
CV v77,v74,v75
ds_bpermute_b32 v76, v232, v76
ds_bpermute_b32 v77, v232, v77
v_lshlrev_b32 v10, 16, v6
v_and_b32 v11, 0xffff0000, v6
v_lshlrev_b32 v12, 16, v7
v_and_b32 v13, 0xffff0000, v7
v_lshlrev_b32 v14, 16, v38
v_and_b32 v15, 0xffff0000, v38
v_lshlrev_b32 v42, 16, v39
v_and_b32 v43, 0xffff0000, v39
s_add_u32 s80, s18, 41088
s_waitcnt lgkmcnt(0)
v_lshlrev_b32 v72, 16, v76
v_and_b32 v73, 0xffff0000, v76
v_lshlrev_b32 v74, 16, v77
v_and_b32 v75, 0xffff0000, v77
v_mul_f32 v72, v72, v10
v_mul_f32 v73, v73, v11
v_mul_f32 v74, v74, v12
v_mul_f32 v75, v75, v13
CV v76,v72,v73
CV v77,v74,v75
v_lshlrev_b32 v72, 16, v76
v_and_b32 v73, 0xffff0000, v76
v_lshlrev_b32 v74, 16, v77
v_and_b32 v75, 0xffff0000, v77
v_add_f32 v72, v72, v14
v_add_f32 v73, v73, v15
v_add_f32 v74, v74, v42
v_add_f32 v75, v75, v43
CV v46,v72,v73
CV v47,v74,v75
buffer_store_dwordx2 v[46:47], v251, s[40:43], s80 offen offset:0 nt
s_bitcmp0_b32 s94, 8
s_cbranch_scc1 LQN3_%=
s_waitcnt vmcnt(1)
s_branch LQS_%=
LQN3_%=:
s_bitcmp0_b32 s94, 4
s_cbranch_scc1 LQN4_%=
MM v[72:75],v[64:67],a[192:195],0
MM v[72:75],v[64:67],a[196:199],v[72:75]
MM v[72:75],v[68:71],v[144:147],v[72:75]
MM v[72:75],v[68:71],a[212:215],v[72:75]
s_bitset0_b32 s17, 4
s_nop 6
CV v76,v72,v73
CV v77,v74,v75
ds_bpermute_b32 v76, v232, v76
ds_bpermute_b32 v77, v232, v77
v_lshlrev_b32 v10, 16, v8
v_and_b32 v11, 0xffff0000, v8
v_lshlrev_b32 v12, 16, v9
v_and_b32 v13, 0xffff0000, v9
v_lshlrev_b32 v14, 16, v40
v_and_b32 v15, 0xffff0000, v40
v_lshlrev_b32 v42, 16, v41
v_and_b32 v43, 0xffff0000, v41
s_add_u32 s80, s63, 0
s_waitcnt lgkmcnt(0)
v_lshlrev_b32 v72, 16, v76
v_and_b32 v73, 0xffff0000, v76
v_lshlrev_b32 v74, 16, v77
v_and_b32 v75, 0xffff0000, v77
v_mul_f32 v72, v72, v10
v_mul_f32 v73, v73, v11
v_mul_f32 v74, v74, v12
v_mul_f32 v75, v75, v13
CV v76,v72,v73
CV v77,v74,v75
v_lshlrev_b32 v72, 16, v76
v_and_b32 v73, 0xffff0000, v76
v_lshlrev_b32 v74, 16, v77
v_and_b32 v75, 0xffff0000, v77
v_add_f32 v72, v72, v14
v_add_f32 v73, v73, v15
v_add_f32 v74, v74, v42
v_add_f32 v75, v75, v43
CV v44,v72,v73
CV v45,v74,v75
buffer_store_dwordx2 v[44:45], v254, s[40:43], s80 offen offset:0 nt
s_bitcmp0_b32 s94, 8
s_cbranch_scc1 LQN4_%=
s_waitcnt vmcnt(1)
s_branch LQS_%=
LQN4_%=:
s_cmp_eq_u32 s17, 0
s_cbranch_scc1 LQE_%=
s_waitcnt vmcnt(0)
s_branch LQS_%=
LQE_%=:
s_endpgm
LYS_%=:
s_waitcnt vmcnt(15)
LYT_%=:
v_ashrrev_i32 v139, 31, v138
v_not_b32 v139, v139
v_or_b32 v139, 0x80000000, v139
v_xor_b32 v138, v138, v139
v_and_b32 v138, 0xffff0000, v138
v_mul_f32 v138, 0xbfb8aa3b, v138
EX v138,v138
v_pk_add_f32 v[160:161], v[160:161], v[196:197]
v_pk_add_f32 v[162:163], v[162:163], v[198:199]
v_pk_add_f32 v[164:165], v[164:165], v[200:201]
v_pk_add_f32 v[166:167], v[166:167], v[202:203]
v_pk_add_f32 v[168:169], v[168:169], v[204:205]
v_pk_add_f32 v[170:171], v[170:171], v[206:207]
v_pk_add_f32 v[172:173], v[172:173], v[208:209]
v_pk_add_f32 v[174:175], v[174:175], v[210:211]
v_pk_add_f32 v[176:177], v[176:177], v[212:213]
v_pk_add_f32 v[178:179], v[178:179], v[214:215]
v_pk_add_f32 v[180:181], v[180:181], v[216:217]
v_pk_add_f32 v[182:183], v[182:183], v[218:219]
v_pk_add_f32 v[184:185], v[184:185], v[220:221]
v_pk_add_f32 v[186:187], v[186:187], v[222:223]
v_pk_add_f32 v[188:189], v[188:189], v[96:97]
v_pk_add_f32 v[190:191], v[190:191], v[98:99]
v_pk_add_f32 v[192:193], v[192:193], v[100:101]
v_pk_add_f32 v[194:195], v[194:195], v[102:103]
v_pk_add_f32 v[160:161], v[160:161], v[192:193]
v_pk_add_f32 v[162:163], v[162:163], v[194:195]
v_add_f32 v138, 1.0, v138
v_rcp_f32 v138, v138
v_pk_add_f32 v[160:161], v[160:161], v[176:177]
v_pk_add_f32 v[162:163], v[162:163], v[178:179]
v_pk_add_f32 v[164:165], v[164:165], v[180:181]
v_pk_add_f32 v[166:167], v[166:167], v[182:183]
v_pk_add_f32 v[168:169], v[168:169], v[184:185]
v_pk_add_f32 v[170:171], v[170:171], v[186:187]
v_pk_add_f32 v[172:173], v[172:173], v[188:189]
v_pk_add_f32 v[174:175], v[174:175], v[190:191]
CV v138,v138,v138
v_and_b32 v138, 0xffff0000, v138
v_pk_add_f32 v[160:161], v[160:161], v[168:169]
v_pk_add_f32 v[162:163], v[162:163], v[170:171]
v_pk_add_f32 v[164:165], v[164:165], v[172:173]
v_pk_add_f32 v[166:167], v[166:167], v[174:175]
v_pk_add_f32 v[160:161], v[160:161], v[164:165]
v_pk_add_f32 v[162:163], v[162:163], v[166:167]
CV v164,v160,v161
CV v165,v162,v163
v_lshlrev_b32 v160, 16, v164
v_and_b32 v161, 0xffff0000, v164
v_lshlrev_b32 v162, 16, v165
v_and_b32 v163, 0xffff0000, v165
v_mul_f32 v160, v160, v138
v_mul_f32 v161, v161, v138
v_mul_f32 v162, v162, v138
v_mul_f32 v163, v163, v138
CV v164,v160,v161
CV v165,v162,v163
v_lshlrev_b32 v160, 16, v164
v_and_b32 v161, 0xffff0000, v164
v_lshlrev_b32 v162, 16, v165
v_and_b32 v163, 0xffff0000, v165
v_lshlrev_b32 v168, 16, v136
v_and_b32 v169, 0xffff0000, v136
v_lshlrev_b32 v170, 16, v137
v_and_b32 v171, 0xffff0000, v137
v_add_f32 v160, v160, v168
v_add_f32 v161, v161, v169
v_add_f32 v162, v162, v170
v_add_f32 v163, v163, v171
CV v164,v160,v161
CV v165,v162,v163
v_lshlrev_b32 v160, 16, v164
v_and_b32 v161, 0xffff0000, v164
v_lshlrev_b32 v162, 16, v165
v_and_b32 v163, 0xffff0000, v165
v_mul_f32 v168, 0xbfb8aa3b, v160
v_mul_f32 v169, 0xbfb8aa3b, v161
v_mul_f32 v170, 0xbfb8aa3b, v162
v_mul_f32 v171, 0xbfb8aa3b, v163
EX v168,v168
EX v169,v169
EX v170,v170
EX v171,v171
v_bfe_u32 v139, v248, 2, 6
v_and_b32 v132, 28, v139
v_add_f32 v168, 1.0, v168
v_add_f32 v169, 1.0, v169
v_add_f32 v170, 1.0, v170
v_add_f32 v171, 1.0, v171
v_rcp_f32 v168, v168
v_rcp_f32 v169, v169
v_rcp_f32 v170, v170
v_rcp_f32 v171, v171
s_and_b32 s77, s84, 1
s_lshl_b32 s76, s77, 5
v_add_u32 v139, s76, v139
s_xor_b32 s77, s77, 1
s_lshl_b32 s77, s77, 7
v_add_u32 v132, s77, v132
v_mul_f32 v168, v160, v168
v_mul_f32 v169, v161, v169
v_mul_f32 v170, v162, v170
v_mul_f32 v171, v163, v171
CV v172,v168,v169
CV v173,v170,v171
ds_bpermute_b32 v176, v139, v172 offset:0
ds_bpermute_b32 v177, v139, v173 offset:0
ds_bpermute_b32 v180, v139, v172 offset:64
ds_bpermute_b32 v181, v139, v173 offset:64
ds_bpermute_b32 v184, v132, v172 offset:0
ds_bpermute_b32 v185, v132, v173 offset:0
s_ff1_i32_b32 s76, s82
s_bitset0_b32 s82, s76
s_and_b32 s77, s79, 3
s_lshl_b32 s77, s77, 4
s_and_b32 s76, s76, 8
s_waitcnt lgkmcnt(0)
s_lshl_b64 vcc, 0xffff, s77
s_cmp_eq_u32 s76, 0
s_cselect_b64 exec, vcc, 0
v_mov_b32 v0, v176
v_mov_b32 v1, v177
v_mov_b32 v2, v180
v_mov_b32 v3, v181
s_cselect_b64 exec, 0, vcc
v_mov_b32 v4, v176
v_mov_b32 v5, v177
v_mov_b32 v6, v180
v_mov_b32 v7, v181
s_add_u32 s77, s77, s76
s_lshl_b64 exec, 0xff, s77
v_mov_b32 v8, v184
v_mov_b32 v9, v185
s_mov_b64 exec, -1
s_cmp_eq_u32 s82, 0
s_cbranch_scc1 LYE_%=
s_ff1_i32_b32 s76, s82
s_and_b32 s76, s76, 7
s_mov_b32 s79, s52
s_cmp_eq_u32 s76, 1
s_cselect_b32 s79, s53, s79
s_cmp_eq_u32 s76, 2
s_cselect_b32 s79, s54, s79
s_cmp_eq_u32 s76, 3
s_cselect_b32 s79, s55, s79
s_cmp_eq_u32 s76, 4
s_cselect_b32 s79, s56, s79
s_cmp_eq_u32 s76, 5
s_cselect_b32 s79, s57, s79
s_cmp_eq_u32 s76, 6
s_cselect_b32 s79, s58, s79
s_cmp_eq_u32 s76, 7
s_cselect_b32 s79, s59, s79
s_mul_i32 s76, s76, 2560
s_mul_i32 s90, s84, 640
s_add_u32 s76, s76, s90
s_add_u32 s76, s76, 21659648
s_lshl_b32 s90, s79, 2
s_add_u32 s90, s90, 53248
buffer_load_dword v138, off, s[20:23], s90 sc1
)ASM"
R"ASM(
s_mul_i32 s90, s79, 1280
s_mul_i32 s77, s84, 320
s_add_u32 s90, s90, s77
s_add_u32 s90, s90, 131072
v_lshrrev_b32 v139, 1, v248
s_mov_b32 exec_lo, -1
s_mov_b32 exec_hi, 255
buffer_load_dwordx2 v[136:137], v139, s[20:23], s90 offen offset:0 sc1
s_mov_b32 exec_lo, -1
s_mov_b32 exec_hi, 255
buffer_load_dwordx4 v[160:163], v248, s[20:23], s76 offen offset:0 sc1
s_add_u32 s76, s76, 20480
buffer_load_dwordx4 v[164:167], v248, s[20:23], s76 offen offset:0 sc1
s_add_u32 s76, s76, 20480
buffer_load_dwordx4 v[168:171], v248, s[20:23], s76 offen offset:0 sc1
s_add_u32 s76, s76, 20480
buffer_load_dwordx4 v[172:175], v248, s[20:23], s76 offen offset:0 sc1
s_add_u32 s76, s76, 20480
buffer_load_dwordx4 v[176:179], v248, s[20:23], s76 offen offset:0 sc1
s_add_u32 s76, s76, 20480
buffer_load_dwordx4 v[180:183], v248, s[20:23], s76 offen offset:0 sc1
s_add_u32 s76, s76, 20480
buffer_load_dwordx4 v[184:187], v248, s[20:23], s76 offen offset:0 sc1
s_add_u32 s76, s76, 20480
buffer_load_dwordx4 v[188:191], v248, s[20:23], s76 offen offset:0 sc1
s_add_u32 s76, s76, 20480
buffer_load_dwordx4 v[192:195], v248, s[20:23], s76 offen offset:0 sc1
s_add_u32 s76, s76, 20480
buffer_load_dwordx4 v[196:199], v248, s[20:23], s76 offen offset:0 sc1
s_add_u32 s76, s76, 20480
buffer_load_dwordx4 v[200:203], v248, s[20:23], s76 offen offset:0 sc1
s_add_u32 s76, s76, 20480
buffer_load_dwordx4 v[204:207], v248, s[20:23], s76 offen offset:0 sc1
s_add_u32 s76, s76, 20480
buffer_load_dwordx4 v[208:211], v248, s[20:23], s76 offen offset:0 sc1
s_add_u32 s76, s76, 20480
buffer_load_dwordx4 v[212:215], v248, s[20:23], s76 offen offset:0 sc1
s_add_u32 s76, s76, 20480
buffer_load_dwordx4 v[216:219], v248, s[20:23], s76 offen offset:0 sc1
s_add_u32 s76, s76, 20480
buffer_load_dwordx4 v[220:223], v248, s[20:23], s76 offen offset:0 sc1
s_add_u32 s76, s76, 20480
buffer_load_dwordx4 v[96:99], v248, s[20:23], s76 offen offset:0 sc1
s_add_u32 s76, s76, 20480
buffer_load_dwordx4 v[100:103], v248, s[20:23], s76 offen offset:0 sc1
s_mov_b64 exec, -1
s_waitcnt vmcnt(0)
s_branch LYT_%=
.fill 406, 4, 0xbf800000
LXP_%=:
LPG26_%=:
s_cbranch_execz LTR1_%=
v_mov_b32 v250, s69
s_mov_b32 s76, 32768
s_mov_b64 exec, 1
buffer_store_dword v250, off, s[20:23], s76 sc1
s_mov_b64 exec, -1
s_add_u32 s79, %[slds], 154112
s_lshl_b32 s80, %[swave], 2
MM v[64:67],a[192:195],v[160:163],v[128:131]
s_add_u32 s64, s64, 16384
TR a[128:129],v232 offset:0
s_add_u32 s81, s64, 896
MM v[64:67],a[196:199],v[176:179],v[64:67]
TR a[130:131],v232 offset:4096
TR a[132:133],v232 offset:8192
MM v[64:67],a[200:203],v[192:195],v[64:67]
TR a[134:135],v232 offset:12288
TR a[136:137],v233 offset:0
MM v[64:67],a[204:207],v[208:211],v[64:67]
TR a[138:139],v233 offset:4096
v_add_f32 v246, v236, v237
MM v[68:71],a[220:223],v[208:211],v[128:131]
TR a[140:141],v233 offset:8192
v_cmp_nge_f32 vcc, s71, v246
MM v[68:71],a[216:219],v[192:195],v[68:71]
s_cbranch_vccnz LSITE4_%=
LBACK4_%=:
TR a[142:143],v233 offset:12288
TR a[144:145],v232 offset:512
MM v[68:71],a[212:215],v[176:179],v[68:71]
s_mov_b32 m0, s67
TR a[146:147],v232 offset:4608
TR a[148:149],v232 offset:8704
MM v[68:71],a[208:211],v[160:163],v[68:71]
buffer_load_dwordx4 v252, s[36:39], s66 offen offset:0 lds
MM v[76:79],a[208:211],v[164:167],v[132:135]
s_add_u32 s66, s66, 55296
EX v64,v64
MM v[76:79],a[212:215],v[180:183],v[76:79]
s_min_u32 s66, s66, s85
EX v65,v65
MM v[76:79],a[216:219],v[196:199],v[76:79]
s_add_u32 s67, s67, 1024
TR a[150:151],v232 offset:12800
CV v64,v64,v65
MM v[76:79],a[220:223],v[212:215],v[76:79]
s_min_u32 s67, s67, s86
EX v66,v66
MM v[72:75],a[204:207],v[212:215],v[132:135]
EX v67,v67
MM v[72:75],a[200:203],v[196:199],v[72:75]
TR a[152:153],v233 offset:512
CV v65,v66,v67
MM v[72:75],a[196:199],v[180:183],v[72:75]
EX v68,v68
MM v[72:75],a[192:195],v[164:167],v[72:75]
EX v69,v69
MM v[80:83],a[192:195],v[168:171],v[136:139]
TR a[154:155],v233 offset:4608
CV v66,v68,v69
MM v[80:83],a[196:199],v[184:187],v[80:83]
EX v70,v70
MM v[80:83],a[200:203],v[200:203],v[80:83]
EX v71,v71
MM v[80:83],a[204:207],v[216:219],v[80:83]
TR a[156:157],v233 offset:8704
CV v67,v70,v71
MM v[84:87],a[220:223],v[216:219],v[136:139]
EX v72,v72
MM v[84:87],a[216:219],v[200:203],v[84:87]
TR a[158:159],v233 offset:12800
TR a[160:161],v232 offset:1024
MM v[84:87],a[212:215],v[184:187],v[84:87]
EX v73,v73
MM v[84:87],a[208:211],v[168:171],v[84:87]
TR a[162:163],v232 offset:5120
CV v72,v72,v73
MM v[92:95],a[208:211],v[172:175],v[140:143]
EX v74,v74
MM v[92:95],a[212:215],v[188:191],v[92:95]
TR a[164:165],v232 offset:9216
TR a[166:167],v232 offset:13312
MM v[92:95],a[216:219],v[204:207],v[92:95]
EX v75,v75
MM v[92:95],a[220:223],v[220:223],v[92:95]
TR a[168:169],v233 offset:1024
CV v73,v74,v75
MM v[88:91],a[204:207],v[220:223],v[140:143]
EX v76,v76
MM v[88:91],a[200:203],v[204:207],v[88:91]
EX v77,v77
MM v[88:91],a[196:199],v[188:191],v[88:91]
TR a[170:171],v233 offset:5120
CV v74,v76,v77
MM v[88:91],a[192:195],v[172:175],v[88:91]
EX v78,v78
MM v[96:99],a[224:227],v[160:163],v[128:131]
TR a[172:173],v233 offset:9216
TR a[174:175],v233 offset:13312
MM v[96:99],a[228:231],v[176:179],v[96:99]
EX v79,v79
MM v[96:99],a[232:235],v[192:195],v[96:99]
TR a[176:177],v232 offset:1536
CV v75,v78,v79
MM v[96:99],a[236:239],v[208:211],v[96:99]
EX v80,v80
MM v[100:103],a[252:255],v[208:211],v[128:131]
EX v81,v81
MM v[100:103],a[248:251],v[192:195],v[100:103]
TR a[178:179],v232 offset:5632
CV v80,v80,v81
MM v[100:103],a[244:247],v[176:179],v[100:103]
EX v82,v82
MM v[100:103],a[240:243],v[160:163],v[100:103]
TR a[180:181],v232 offset:9728
TR a[182:183],v232 offset:13824
MM v[108:111],a[240:243],v[164:167],v[132:135]
v_add_u32 v48, s78, v248
v_mov_b32 v49, s79
MM v[108:111],a[244:247],v[180:183],v[108:111]
EX v83,v83
MM v[108:111],a[248:251],v[196:199],v[108:111]
TR a[184:185],v233 offset:1536
CV v81,v82,v83
MM v[108:111],a[252:255],v[212:215],v[108:111]
EX v84,v84
MM v[104:107],a[236:239],v[212:215],v[132:135]
EX v85,v85
MM v[104:107],a[232:235],v[196:199],v[104:107]
TR a[186:187],v233 offset:5632
CV v82,v84,v85
MM v[104:107],a[228:231],v[180:183],v[104:107]
EX v86,v86
MM v[104:107],a[224:227],v[164:167],v[104:107]
TR a[188:189],v233 offset:9728
TR a[190:191],v233 offset:13824
MM v[112:115],a[224:227],v[168:171],v[136:139]
EX v87,v87
MM v[112:115],a[228:231],v[184:187],v[112:115]
CV v83,v86,v87
MM v[112:115],a[232:235],v[200:203],v[112:115]
EX v88,v88
MM v[112:115],a[236:239],v[216:219],v[112:115]
EX v89,v89
MM v[116:119],a[252:255],v[216:219],v[136:139]
EX v90,v90
MM v[116:119],a[248:251],v[200:203],v[116:119]
EX v91,v91
MM v[116:119],a[244:247],v[184:187],v[116:119]
EX v92,v92
MM v[116:119],a[240:243],v[168:171],v[116:119]
EX v93,v93
MM v[124:127],a[240:243],v[172:175],v[140:143]
EX v94,v94
MM v[124:127],a[244:247],v[188:191],v[124:127]
EX v95,v95
MM v[124:127],a[248:251],v[204:207],v[124:127]
EX v96,v96
MM v[124:127],a[252:255],v[220:223],v[124:127]
EX v97,v97
MM v[120:123],a[236:239],v[220:223],v[140:143]
EX v98,v98
MM v[120:123],a[232:235],v[204:207],v[120:123]
EX v99,v99
MM v[120:123],a[228:231],v[188:191],v[120:123]
EX v100,v100
MM v[120:123],a[224:227],v[172:175],v[120:123]
s_waitcnt vmcnt(13) lgkmcnt(0)
s_barrier
MM a[0:3],a[128:131],v[0:3],a[0:3]
s_add_u32 m0, s65, 0
RD v[32:35],v48 offset:0
RD v[36:39],v48 offset:1024
MM a[0:3],a[132:135],v[4:7],a[0:3]
s_mov_b32 s92, -1
CV v88,v88,v89
CV v89,v90,v91
MM a[8:11],a[132:135],v[12:15],a[8:11]
RD a[192:195],v224 offset:32768
CV v90,v92,v93
MM a[8:11],a[128:131],v[8:11],a[8:11]
RD a[196:199],v225 offset:32768
CV v91,v94,v95
MM a[16:19],a[128:131],v[16:19],a[16:19]
s_waitcnt lgkmcnt(2)
v_sub_u32 v32, s92, v32
v_sub_u32 v33, s92, v33
MM a[16:19],a[132:135],v[20:23],a[16:19]
v_sub_u32 v34, s92, v34
v_sub_u32 v35, s92, v35
MM a[24:27],a[132:135],v[28:31],a[24:27]
v_sub_u32 v36, s92, v36
v_sub_u32 v37, s92, v37
MM a[24:27],a[128:131],v[24:27],a[24:27]
v_sub_u32 v38, s92, v38
v_sub_u32 v39, s92, v39
MM a[28:31],a[136:139],v[24:27],a[28:31]
v_min3_u32 v52, v32, v33, v34
v_min3_u32 v51, v35, v36, v37
MM a[28:31],a[140:143],v[28:31],a[28:31]
v_min3_u32 v52, v52, v38, v39
v_min_u32 v52, v52, v51
MM a[20:23],a[140:143],v[20:23],a[20:23]
s_nop 0
v_min_u32_dpp v52, v52, v52 row_shr:1 row_mask:0xf bank_mask:0xf
CV v68,v96,v97
MM a[20:23],a[136:139],v[16:19],a[20:23]
RD a[200:203],v226 offset:32768
v_min_u32_dpp v52, v52, v52 row_shr:2 row_mask:0xf bank_mask:0xf
MM a[12:15],a[136:139],v[8:11],a[12:15]
s_nop 0
v_min_u32_dpp v52, v52, v52 row_shr:4 row_mask:0xf bank_mask:0xf
CV v69,v98,v99
MM a[12:15],a[140:143],v[12:15],a[12:15]
RD a[204:207],v227 offset:32768
v_min_u32_dpp v52, v52, v52 row_shr:8 row_mask:0xf bank_mask:0xf
MM a[4:7],a[140:143],v[4:7],a[4:7]
RD a[208:211],v224 offset:36864
v_min_u32_dpp v52, v52, v52 row_bcast:15 row_mask:0xa bank_mask:0xf
MM a[4:7],a[136:139],v[0:3],a[4:7]
RD a[212:215],v225 offset:36864
v_min_u32_dpp v52, v52, v52 row_bcast:31 row_mask:0xc bank_mask:0xf
MM a[32:35],a[144:147],v[0:3],a[32:35]
v_readlane_b32 s76, v52, 63
v_lshl_add_u32 v49, s80, 1, v49
MM a[32:35],a[148:151],v[4:7],a[32:35]
s_add_u32 s76, s76, 1
buffer_load_dwordx4 v228, s[8:11], s64 offen offset:0 lds
v_subrev_u32 v32, s76, v32
v_subrev_u32 v33, s76, v33
EX v101,v101
MM a[40:43],a[148:151],v[12:15],a[40:43]
v_subrev_u32 v34, s76, v34
v_subrev_u32 v35, s76, v35
MM a[40:43],a[144:147],v[8:11],a[40:43]
v_subrev_u32 v36, s76, v36
v_subrev_u32 v37, s76, v37
EX v102,v102
MM a[48:51],a[144:147],v[16:19],a[48:51]
v_subrev_u32 v38, s76, v38
v_subrev_u32 v39, s76, v39
EX v103,v103
MM a[48:51],a[148:151],v[20:23],a[48:51]
v_min3_u32 v53, v32, v33, v34
v_min3_u32 v51, v35, v36, v37
MM a[56:59],a[148:151],v[28:31],a[56:59]
v_min3_u32 v53, v53, v38, v39
v_min_u32 v53, v53, v51
EX v104,v104
MM a[56:59],a[144:147],v[24:27],a[56:59]
v_min_u32_dpp v53, v53, v53 row_shr:1 row_mask:0xf bank_mask:0xf
CV v70,v100,v101
MM a[60:63],a[152:155],v[24:27],a[60:63]
v_min_u32_dpp v53, v53, v53 row_shr:2 row_mask:0xf bank_mask:0xf
CV v71,v102,v103
EX v105,v105
MM a[60:63],a[156:159],v[28:31],a[60:63]
buffer_load_dwordx4 v229, s[8:11], s64 offen offset:1024 lds
v_min_u32_dpp v53, v53, v53 row_shr:4 row_mask:0xf bank_mask:0xf
CV v76,v104,v105
MM a[52:55],a[156:159],v[20:23],a[52:55]
RD a[216:219],v226 offset:36864
v_min_u32_dpp v53, v53, v53 row_shr:8 row_mask:0xf bank_mask:0xf
EX v106,v106
MM a[52:55],a[152:155],v[16:19],a[52:55]
RD a[220:223],v227 offset:36864
v_min_u32_dpp v53, v53, v53 row_bcast:15 row_mask:0xa bank_mask:0xf
MM a[44:47],a[152:155],v[8:11],a[44:47]
RD a[224:227],v224 offset:40960
v_min_u32_dpp v53, v53, v53 row_bcast:31 row_mask:0xc bank_mask:0xf
EX v107,v107
MM a[44:47],a[156:159],v[12:15],a[44:47]
v_add_u32 v53, s76, v53
CV v77,v106,v107
MM a[36:39],a[156:159],v[4:7],a[36:39]
s_mov_b32 exec_lo, 0
s_mov_b32 exec_hi, 0x80000000
ds_write_b64 v49, v[52:53] offset:0
s_mov_b64 exec, -1
EX v108,v108
MM a[36:39],a[152:155],v[0:3],a[36:39]
EX v109,v109
MM a[64:67],a[160:163],v[0:3],a[64:67]
buffer_load_dwordx4 v230, s[8:11], s64 offen offset:2048 lds
MM a[64:67],a[164:167],v[4:7],a[64:67]
EX v110,v110
MM a[72:75],a[164:167],v[12:15],a[72:75]
EX v111,v111
MM a[72:75],a[160:163],v[8:11],a[72:75]
buffer_load_dwordx4 v231, s[8:11], s64 offen offset:3072 lds
MM a[80:83],a[160:163],v[16:19],a[80:83]
s_add_u32 m0, s65, 65536
EX v112,v112
MM a[80:83],a[164:167],v[20:23],a[80:83]
CV v78,v108,v109
CV v79,v110,v111
MM a[88:91],a[164:167],v[28:31],a[88:91]
EX v113,v113
MM a[88:91],a[160:163],v[24:27],a[88:91]
EX v114,v114
MM a[92:95],a[168:171],v[24:27],a[92:95]
EX v115,v115
MM a[92:95],a[172:175],v[28:31],a[92:95]
buffer_load_dwordx4 v235, s[12:15], s81 offen offset:0 lds
MM a[84:87],a[172:175],v[20:23],a[84:87]
RD a[228:231],v225 offset:40960
CV v84,v112,v113
MM a[84:87],a[168:171],v[16:19],a[84:87]
EX v116,v116
MM a[76:79],a[168:171],v[8:11],a[76:79]
EX v117,v117
MM a[76:79],a[172:175],v[12:15],a[76:79]
RD a[232:235],v226 offset:40960
CV v85,v114,v115
MM a[68:71],a[172:175],v[4:7],a[68:71]
EX v118,v118
MM a[68:71],a[168:171],v[0:3],a[68:71]
)ASM"
R"ASM(
RD a[236:239],v227 offset:40960
CV v86,v116,v117
MM a[96:99],a[176:179],v[0:3],a[96:99]
EX v119,v119
MM a[96:99],a[180:183],v[4:7],a[96:99]
buffer_load_dwordx4 v235, s[12:15], s64 offen offset:1024 lds
MM a[104:107],a[180:183],v[12:15],a[104:107]
EX v120,v120
MM a[104:107],a[176:179],v[8:11],a[104:107]
RD a[240:243],v224 offset:45056
CV v87,v118,v119
MM a[112:115],a[176:179],v[16:19],a[112:115]
EX v121,v121
MM a[112:115],a[180:183],v[20:23],a[112:115]
EX v122,v122
MM a[120:123],a[180:183],v[28:31],a[120:123]
RD a[244:247],v225 offset:45056
CV v92,v120,v121
MM a[120:123],a[176:179],v[24:27],a[120:123]
EX v123,v123
MM a[124:127],a[184:187],v[24:27],a[124:127]
buffer_load_dwordx4 v235, s[12:15], s81 offen offset:2048 lds
MM a[124:127],a[188:191],v[28:31],a[124:127]
EX v124,v124
MM a[116:119],a[188:191],v[20:23],a[116:119]
RD a[248:251],v226 offset:45056
CV v93,v122,v123
MM a[116:119],a[184:187],v[16:19],a[116:119]
EX v125,v125
MM a[108:111],a[184:187],v[8:11],a[108:111]
EX v126,v126
MM a[108:111],a[188:191],v[12:15],a[108:111]
RD a[252:255],v227 offset:45056
CV v94,v124,v125
MM a[100:103],a[188:191],v[4:7],a[100:103]
EX v127,v127
MM a[100:103],a[184:187],v[0:3],a[100:103]
buffer_load_dwordx4 v235, s[12:15], s64 offen offset:3072 lds
v_smfmac_f32_16x16x64_bf16 v[236:239], v[144:147], v[64:71], v234
CV v95,v126,v127
v_smfmac_f32_16x16x64_bf16 v[236:239], v[148:151], v[72:79], v234
v_smfmac_f32_16x16x64_bf16 v[236:239], v[152:155], v[80:87], v234
v_smfmac_f32_16x16x64_bf16 v[236:239], v[156:159], v[88:95], v234
s_waitcnt lgkmcnt(0)
MM v[0:3],a[192:195],v[160:163],v[128:131]
s_add_u32 s64, s64, 16384
TR a[128:129],v232 offset:16384
s_add_u32 s81, s64, 896
MM v[0:3],a[196:199],v[176:179],v[0:3]
TR a[130:131],v232 offset:20480
TR a[132:133],v232 offset:24576
MM v[0:3],a[200:203],v[192:195],v[0:3]
TR a[134:135],v232 offset:28672
TR a[136:137],v233 offset:16384
MM v[0:3],a[204:207],v[208:211],v[0:3]
TR a[138:139],v233 offset:20480
v_add_f32 v247, v236, v237
MM v[4:7],a[220:223],v[208:211],v[128:131]
TR a[140:141],v233 offset:24576
v_cmp_nge_f32 vcc, s71, v247
MM v[4:7],a[216:219],v[192:195],v[4:7]
s_cbranch_vccnz LSITE5_%=
LBACK5_%=:
TR a[142:143],v233 offset:28672
TR a[144:145],v232 offset:16896
MM v[4:7],a[212:215],v[176:179],v[4:7]
s_mov_b32 m0, s67
TR a[146:147],v232 offset:20992
TR a[148:149],v232 offset:25088
MM v[4:7],a[208:211],v[160:163],v[4:7]
buffer_load_dwordx4 v252, s[36:39], s66 offen offset:0 lds
MM v[12:15],a[208:211],v[164:167],v[132:135]
s_add_u32 s66, s66, 55296
EX v0,v0
MM v[12:15],a[212:215],v[180:183],v[12:15]
s_min_u32 s66, s66, s85
EX v1,v1
MM v[12:15],a[216:219],v[196:199],v[12:15]
s_add_u32 s67, s67, 1024
TR a[150:151],v232 offset:29184
CV v0,v0,v1
MM v[12:15],a[220:223],v[212:215],v[12:15]
s_min_u32 s67, s67, s86
EX v2,v2
MM v[8:11],a[204:207],v[212:215],v[132:135]
EX v3,v3
MM v[8:11],a[200:203],v[196:199],v[8:11]
TR a[152:153],v233 offset:16896
CV v1,v2,v3
MM v[8:11],a[196:199],v[180:183],v[8:11]
EX v4,v4
MM v[8:11],a[192:195],v[164:167],v[8:11]
EX v5,v5
MM v[16:19],a[192:195],v[168:171],v[136:139]
TR a[154:155],v233 offset:20992
CV v2,v4,v5
MM v[16:19],a[196:199],v[184:187],v[16:19]
EX v6,v6
MM v[16:19],a[200:203],v[200:203],v[16:19]
EX v7,v7
MM v[16:19],a[204:207],v[216:219],v[16:19]
TR a[156:157],v233 offset:25088
CV v3,v6,v7
MM v[20:23],a[220:223],v[216:219],v[136:139]
EX v8,v8
MM v[20:23],a[216:219],v[200:203],v[20:23]
TR a[158:159],v233 offset:29184
TR a[160:161],v232 offset:17408
MM v[20:23],a[212:215],v[184:187],v[20:23]
EX v9,v9
MM v[20:23],a[208:211],v[168:171],v[20:23]
TR a[162:163],v232 offset:21504
CV v8,v8,v9
MM v[28:31],a[208:211],v[172:175],v[140:143]
EX v10,v10
MM v[28:31],a[212:215],v[188:191],v[28:31]
TR a[164:165],v232 offset:25600
TR a[166:167],v232 offset:29696
MM v[28:31],a[216:219],v[204:207],v[28:31]
EX v11,v11
MM v[28:31],a[220:223],v[220:223],v[28:31]
TR a[168:169],v233 offset:17408
CV v9,v10,v11
MM v[24:27],a[204:207],v[220:223],v[140:143]
EX v12,v12
MM v[24:27],a[200:203],v[204:207],v[24:27]
EX v13,v13
MM v[24:27],a[196:199],v[188:191],v[24:27]
TR a[170:171],v233 offset:21504
CV v10,v12,v13
MM v[24:27],a[192:195],v[172:175],v[24:27]
EX v14,v14
MM v[32:35],a[224:227],v[160:163],v[128:131]
TR a[172:173],v233 offset:25600
TR a[174:175],v233 offset:29696
MM v[32:35],a[228:231],v[176:179],v[32:35]
EX v15,v15
MM v[32:35],a[232:235],v[192:195],v[32:35]
TR a[176:177],v232 offset:17920
CV v11,v14,v15
MM v[32:35],a[236:239],v[208:211],v[32:35]
EX v16,v16
MM v[36:39],a[252:255],v[208:211],v[128:131]
EX v17,v17
MM v[36:39],a[248:251],v[192:195],v[36:39]
TR a[178:179],v232 offset:22016
CV v16,v16,v17
MM v[36:39],a[244:247],v[176:179],v[36:39]
EX v18,v18
MM v[36:39],a[240:243],v[160:163],v[36:39]
TR a[180:181],v232 offset:26112
TR a[182:183],v232 offset:30208
MM v[44:47],a[240:243],v[164:167],v[132:135]
v_add_u32 v112, s78, v248
v_mov_b32 v113, s79
MM v[44:47],a[244:247],v[180:183],v[44:47]
EX v19,v19
MM v[44:47],a[248:251],v[196:199],v[44:47]
TR a[184:185],v233 offset:17920
CV v17,v18,v19
MM v[44:47],a[252:255],v[212:215],v[44:47]
EX v20,v20
MM v[40:43],a[236:239],v[212:215],v[132:135]
EX v21,v21
MM v[40:43],a[232:235],v[196:199],v[40:43]
TR a[186:187],v233 offset:22016
CV v18,v20,v21
MM v[40:43],a[228:231],v[180:183],v[40:43]
EX v22,v22
MM v[40:43],a[224:227],v[164:167],v[40:43]
TR a[188:189],v233 offset:26112
TR a[190:191],v233 offset:30208
MM v[48:51],a[224:227],v[168:171],v[136:139]
EX v23,v23
MM v[48:51],a[228:231],v[184:187],v[48:51]
CV v19,v22,v23
MM v[48:51],a[232:235],v[200:203],v[48:51]
EX v24,v24
MM v[48:51],a[236:239],v[216:219],v[48:51]
EX v25,v25
MM v[52:55],a[252:255],v[216:219],v[136:139]
EX v26,v26
MM v[52:55],a[248:251],v[200:203],v[52:55]
EX v27,v27
MM v[52:55],a[244:247],v[184:187],v[52:55]
EX v28,v28
MM v[52:55],a[240:243],v[168:171],v[52:55]
EX v29,v29
MM v[60:63],a[240:243],v[172:175],v[140:143]
EX v30,v30
MM v[60:63],a[244:247],v[188:191],v[60:63]
EX v31,v31
MM v[60:63],a[248:251],v[204:207],v[60:63]
EX v32,v32
MM v[60:63],a[252:255],v[220:223],v[60:63]
EX v33,v33
MM v[56:59],a[236:239],v[220:223],v[140:143]
EX v34,v34
MM v[56:59],a[232:235],v[204:207],v[56:59]
EX v35,v35
MM v[56:59],a[228:231],v[188:191],v[56:59]
EX v36,v36
MM v[56:59],a[224:227],v[172:175],v[56:59]
s_waitcnt vmcnt(13) lgkmcnt(0)
s_barrier
MM a[0:3],a[128:131],v[64:67],a[0:3]
s_add_u32 m0, s65, 16384
RD v[104:107],v113 offset:0
RD v[108:111],v113 offset:16
MM a[0:3],a[132:135],v[68:71],a[0:3]
RD v[96:99],v112 offset:0
RD v[100:103],v112 offset:1024
MM a[8:11],a[132:135],v[76:79],a[8:11]
CV v24,v24,v25
CV v25,v26,v27
MM a[8:11],a[128:131],v[72:75],a[8:11]
RD a[192:195],v224 offset:49152
CV v26,v28,v29
MM a[16:19],a[128:131],v[80:83],a[16:19]
RD a[196:199],v225 offset:49152
CV v27,v30,v31
MM a[16:19],a[132:135],v[84:87],a[16:19]
s_waitcnt lgkmcnt(4)
v_min3_u32 v114, v104, v105, v106
v_min3_u32 v115, v107, v108, v109
MM a[24:27],a[132:135],v[92:95],a[24:27]
v_min3_u32 v114, v114, v110, v111
v_min_u32 v114, v114, v115
MM a[24:27],a[128:131],v[88:91],a[24:27]
v_readfirstlane_b32 s76, v114
CV v4,v32,v33
MM a[28:31],a[136:139],v[88:91],a[28:31]
s_sub_u32 s92, s92, s76
buffer_load_dwordx4 v228, s[8:11], s64 offen offset:0 lds
MM a[28:31],a[140:143],v[92:95],a[28:31]
s_add_u32 s76, s76, 1
v_subrev_u32 v104, s76, v104
v_subrev_u32 v105, s76, v105
MM a[20:23],a[140:143],v[84:87],a[20:23]
v_subrev_u32 v106, s76, v106
v_subrev_u32 v107, s76, v107
MM a[20:23],a[136:139],v[80:83],a[20:23]
v_subrev_u32 v108, s76, v108
v_subrev_u32 v109, s76, v109
MM a[12:15],a[136:139],v[72:75],a[12:15]
v_subrev_u32 v110, s76, v110
v_subrev_u32 v111, s76, v111
MM a[12:15],a[140:143],v[76:79],a[12:15]
s_and_b32 s77, s92, 65535
RD a[200:203],v226 offset:49152
CV v5,v34,v35
MM a[4:7],a[140:143],v[68:71],a[4:7]
s_sub_u32 s52, 65535, s77
v_min3_u32 v114, v104, v105, v106
v_min3_u32 v115, v107, v108, v109
MM a[4:7],a[136:139],v[64:67],a[4:7]
v_min3_u32 v114, v114, v110, v111
v_min_u32 v114, v114, v115
MM a[32:35],a[144:147],v[64:67],a[32:35]
RD a[204:207],v227 offset:49152
v_readfirstlane_b32 s76, v114
MM a[32:35],a[148:151],v[68:71],a[32:35]
s_sub_u32 s92, s92, s76
EX v37,v37
MM a[40:43],a[148:151],v[76:79],a[40:43]
s_sub_u32 s92, s92, 1
EX v38,v38
MM a[40:43],a[144:147],v[72:75],a[40:43]
s_and_b32 s77, s92, 65535
EX v39,v39
MM a[48:51],a[144:147],v[80:83],a[48:51]
s_sub_u32 s53, 65535, s77
buffer_load_dwordx4 v229, s[8:11], s64 offen offset:1024 lds
MM a[48:51],a[148:151],v[84:87],a[48:51]
s_sub_u32 s92, s92, 1
EX v40,v40
MM a[56:59],a[148:151],v[92:95],a[56:59]
s_waitcnt lgkmcnt(4)
v_sub_u32 v96, s92, v96
v_sub_u32 v97, s92, v97
MM a[56:59],a[144:147],v[88:91],a[56:59]
v_sub_u32 v98, s92, v98
v_sub_u32 v99, s92, v99
MM a[60:63],a[152:155],v[88:91],a[60:63]
v_sub_u32 v100, s92, v100
v_sub_u32 v101, s92, v101
EX v41,v41
MM a[60:63],a[156:159],v[92:95],a[60:63]
v_sub_u32 v102, s92, v102
v_sub_u32 v103, s92, v103
LPG27_%=:
s_cbranch_execz LTR1_%=
MM a[52:55],a[156:159],v[84:87],a[52:55]
v_min3_u32 v116, v96, v97, v98
v_min3_u32 v115, v99, v100, v101
EX v42,v42
MM a[52:55],a[152:155],v[80:83],a[52:55]
v_min3_u32 v116, v116, v102, v103
v_min_u32 v116, v116, v115
CV v6,v36,v37
MM a[44:47],a[152:155],v[72:75],a[44:47]
v_min_u32_dpp v116, v116, v116 row_shr:1 row_mask:0xf bank_mask:0xf
CV v7,v38,v39
EX v43,v43
MM a[44:47],a[156:159],v[76:79],a[44:47]
RD a[208:211],v224 offset:53248
v_min_u32_dpp v116, v116, v116 row_shr:2 row_mask:0xf bank_mask:0xf
MM a[36:39],a[156:159],v[68:71],a[36:39]
s_nop 0
v_min_u32_dpp v116, v116, v116 row_shr:4 row_mask:0xf bank_mask:0xf
CV v12,v40,v41
EX v44,v44
MM a[36:39],a[152:155],v[64:67],a[36:39]
RD a[212:215],v225 offset:53248
v_min_u32_dpp v116, v116, v116 row_shr:8 row_mask:0xf bank_mask:0xf
MM a[64:67],a[160:163],v[64:67],a[64:67]
s_nop 0
v_min_u32_dpp v116, v116, v116 row_bcast:15 row_mask:0xa bank_mask:0xf
CV v13,v42,v43
EX v45,v45
MM a[64:67],a[164:167],v[68:71],a[64:67]
RD a[216:219],v226 offset:53248
v_min_u32_dpp v116, v116, v116 row_bcast:31 row_mask:0xc bank_mask:0xf
MM a[72:75],a[164:167],v[76:79],a[72:75]
buffer_load_dwordx4 v230, s[8:11], s64 offen offset:2048 lds
v_readlane_b32 s76, v116, 63
v_lshl_add_u32 v113, s80, 1, v113
EX v46,v46
MM a[72:75],a[160:163],v[72:75],a[72:75]
s_add_u32 s76, s76, 1
v_subrev_u32 v96, s76, v96
v_subrev_u32 v97, s76, v97
EX v47,v47
MM a[80:83],a[160:163],v[80:83],a[80:83]
v_subrev_u32 v98, s76, v98
v_subrev_u32 v99, s76, v99
MM a[80:83],a[164:167],v[84:87],a[80:83]
v_subrev_u32 v100, s76, v100
v_subrev_u32 v101, s76, v101
EX v48,v48
MM a[88:91],a[164:167],v[92:95],a[88:91]
v_subrev_u32 v102, s76, v102
v_subrev_u32 v103, s76, v103
MM a[88:91],a[160:163],v[88:91],a[88:91]
v_min3_u32 v117, v96, v97, v98
v_min3_u32 v115, v99, v100, v101
EX v49,v49
MM a[92:95],a[168:171],v[88:91],a[92:95]
v_min3_u32 v117, v117, v102, v103
v_min_u32 v117, v117, v115
MM a[92:95],a[172:175],v[92:95],a[92:95]
s_nop 0
v_min_u32_dpp v117, v117, v117 row_shr:1 row_mask:0xf bank_mask:0xf
CV v14,v44,v45
EX v50,v50
MM a[84:87],a[172:175],v[84:87],a[84:87]
RD a[220:223],v227 offset:53248
buffer_load_dwordx4 v231, s[8:11], s64 offen offset:3072 lds
v_min_u32_dpp v117, v117, v117 row_shr:2 row_mask:0xf bank_mask:0xf
MM a[84:87],a[168:171],v[80:83],a[84:87]
s_add_u32 m0, s65, 81920
v_min_u32_dpp v117, v117, v117 row_shr:4 row_mask:0xf bank_mask:0xf
CV v15,v46,v47
EX v51,v51
MM a[76:79],a[168:171],v[72:75],a[76:79]
RD a[224:227],v224 offset:57344
v_min_u32_dpp v117, v117, v117 row_shr:8 row_mask:0xf bank_mask:0xf
MM a[76:79],a[172:175],v[76:79],a[76:79]
s_nop 0
v_min_u32_dpp v117, v117, v117 row_bcast:15 row_mask:0xa bank_mask:0xf
CV v20,v48,v49
EX v52,v52
MM a[68:71],a[172:175],v[68:71],a[68:71]
RD a[228:231],v225 offset:57344
v_min_u32_dpp v117, v117, v117 row_bcast:31 row_mask:0xc bank_mask:0xf
EX v53,v53
MM a[68:71],a[168:171],v[64:67],a[68:71]
v_add_u32 v117, s76, v117
CV v21,v50,v51
MM a[96:99],a[176:179],v[64:67],a[96:99]
s_mov_b32 exec_lo, 0
s_mov_b32 exec_hi, 0x80000000
ds_write_b64 v113, v[116:117] offset:32
s_mov_b64 exec, -1
EX v54,v54
MM a[96:99],a[180:183],v[68:71],a[96:99]
)ASM"
R"ASM(
buffer_load_dwordx4 v235, s[12:15], s81 offen offset:0 lds
MM a[104:107],a[180:183],v[76:79],a[104:107]
EX v55,v55
MM a[104:107],a[176:179],v[72:75],a[104:107]
EX v56,v56
MM a[112:115],a[176:179],v[80:83],a[112:115]
RD a[232:235],v226 offset:57344
CV v22,v52,v53
MM a[112:115],a[180:183],v[84:87],a[112:115]
EX v57,v57
MM a[120:123],a[180:183],v[92:95],a[120:123]
buffer_load_dwordx4 v235, s[12:15], s64 offen offset:1024 lds
MM a[120:123],a[176:179],v[88:91],a[120:123]
EX v58,v58
MM a[124:127],a[184:187],v[88:91],a[124:127]
RD a[236:239],v227 offset:57344
CV v23,v54,v55
MM a[124:127],a[188:191],v[92:95],a[124:127]
EX v59,v59
MM a[116:119],a[188:191],v[84:87],a[116:119]
EX v60,v60
MM a[116:119],a[184:187],v[80:83],a[116:119]
RD a[240:243],v224 offset:61440
CV v28,v56,v57
MM a[108:111],a[184:187],v[72:75],a[108:111]
EX v61,v61
MM a[108:111],a[188:191],v[76:79],a[108:111]
buffer_load_dwordx4 v235, s[12:15], s81 offen offset:2048 lds
MM a[100:103],a[188:191],v[68:71],a[100:103]
EX v62,v62
MM a[100:103],a[184:187],v[64:67],a[100:103]
RD a[244:247],v225 offset:61440
CV v29,v58,v59
v_smfmac_f32_16x16x64_bf16 v[236:239], v[144:147], v[0:7], v234
RD a[248:251],v226 offset:61440
RD a[252:255],v227 offset:61440
EX v63,v63
v_smfmac_f32_16x16x64_bf16 v[236:239], v[148:151], v[8:15], v234
CV v30,v60,v61
v_smfmac_f32_16x16x64_bf16 v[236:239], v[152:155], v[16:23], v234
CV v31,v62,v63
s_nop 1
v_smfmac_f32_16x16x64_bf16 v[236:239], v[156:159], v[24:31], v234
s_waitcnt lgkmcnt(0)
buffer_load_dwordx4 v235, s[12:15], s64 offen offset:3072 lds
MM v[64:67],a[192:195],v[160:163],v[128:131]
s_add_u32 s64, s64, 16384
TR a[128:129],v232 offset:32768
s_add_u32 s81, s64, 896
MM v[64:67],a[196:199],v[176:179],v[64:67]
TR a[130:131],v232 offset:36864
TR a[132:133],v232 offset:40960
MM v[64:67],a[200:203],v[192:195],v[64:67]
TR a[134:135],v232 offset:45056
TR a[136:137],v233 offset:32768
MM v[64:67],a[204:207],v[208:211],v[64:67]
TR a[138:139],v233 offset:36864
v_add_f32 v246, v236, v237
MM v[68:71],a[220:223],v[208:211],v[128:131]
TR a[140:141],v233 offset:40960
v_cmp_nge_f32 vcc, s71, v246
MM v[68:71],a[216:219],v[192:195],v[68:71]
s_cbranch_vccnz LSITE6_%=
LBACK6_%=:
TR a[142:143],v233 offset:45056
TR a[144:145],v232 offset:33280
MM v[68:71],a[212:215],v[176:179],v[68:71]
s_mov_b32 m0, s67
TR a[146:147],v232 offset:37376
TR a[148:149],v232 offset:41472
MM v[68:71],a[208:211],v[160:163],v[68:71]
buffer_load_dwordx4 v252, s[36:39], s66 offen offset:0 lds
MM v[76:79],a[208:211],v[164:167],v[132:135]
s_add_u32 s66, s66, 55296
EX v64,v64
MM v[76:79],a[212:215],v[180:183],v[76:79]
s_min_u32 s66, s66, s85
EX v65,v65
MM v[76:79],a[216:219],v[196:199],v[76:79]
s_add_u32 s67, s67, 1024
TR a[150:151],v232 offset:45568
CV v64,v64,v65
MM v[76:79],a[220:223],v[212:215],v[76:79]
s_min_u32 s67, s67, s86
EX v66,v66
MM v[72:75],a[204:207],v[212:215],v[132:135]
EX v67,v67
MM v[72:75],a[200:203],v[196:199],v[72:75]
TR a[152:153],v233 offset:33280
CV v65,v66,v67
MM v[72:75],a[196:199],v[180:183],v[72:75]
EX v68,v68
MM v[72:75],a[192:195],v[164:167],v[72:75]
EX v69,v69
MM v[80:83],a[192:195],v[168:171],v[136:139]
TR a[154:155],v233 offset:37376
CV v66,v68,v69
MM v[80:83],a[196:199],v[184:187],v[80:83]
EX v70,v70
MM v[80:83],a[200:203],v[200:203],v[80:83]
EX v71,v71
MM v[80:83],a[204:207],v[216:219],v[80:83]
TR a[156:157],v233 offset:41472
CV v67,v70,v71
MM v[84:87],a[220:223],v[216:219],v[136:139]
EX v72,v72
MM v[84:87],a[216:219],v[200:203],v[84:87]
TR a[158:159],v233 offset:45568
TR a[160:161],v232 offset:33792
MM v[84:87],a[212:215],v[184:187],v[84:87]
EX v73,v73
MM v[84:87],a[208:211],v[168:171],v[84:87]
TR a[162:163],v232 offset:37888
CV v72,v72,v73
MM v[92:95],a[208:211],v[172:175],v[140:143]
EX v74,v74
MM v[92:95],a[212:215],v[188:191],v[92:95]
TR a[164:165],v232 offset:41984
TR a[166:167],v232 offset:46080
MM v[92:95],a[216:219],v[204:207],v[92:95]
EX v75,v75
MM v[92:95],a[220:223],v[220:223],v[92:95]
TR a[168:169],v233 offset:33792
CV v73,v74,v75
MM v[88:91],a[204:207],v[220:223],v[140:143]
EX v76,v76
MM v[88:91],a[200:203],v[204:207],v[88:91]
EX v77,v77
MM v[88:91],a[196:199],v[188:191],v[88:91]
TR a[170:171],v233 offset:37888
CV v74,v76,v77
MM v[88:91],a[192:195],v[172:175],v[88:91]
EX v78,v78
MM v[96:99],a[224:227],v[160:163],v[128:131]
TR a[172:173],v233 offset:41984
TR a[174:175],v233 offset:46080
MM v[96:99],a[228:231],v[176:179],v[96:99]
EX v79,v79
MM v[96:99],a[232:235],v[192:195],v[96:99]
TR a[176:177],v232 offset:34304
CV v75,v78,v79
MM v[96:99],a[236:239],v[208:211],v[96:99]
EX v80,v80
MM v[100:103],a[252:255],v[208:211],v[128:131]
EX v81,v81
MM v[100:103],a[248:251],v[192:195],v[100:103]
TR a[178:179],v232 offset:38400
CV v80,v80,v81
MM v[100:103],a[244:247],v[176:179],v[100:103]
EX v82,v82
MM v[100:103],a[240:243],v[160:163],v[100:103]
TR a[180:181],v232 offset:42496
TR a[182:183],v232 offset:46592
MM v[108:111],a[240:243],v[164:167],v[132:135]
v_add_u32 v48, s78, v248
v_mov_b32 v49, s79
MM v[108:111],a[244:247],v[180:183],v[108:111]
EX v83,v83
MM v[108:111],a[248:251],v[196:199],v[108:111]
TR a[184:185],v233 offset:34304
CV v81,v82,v83
MM v[108:111],a[252:255],v[212:215],v[108:111]
EX v84,v84
MM v[104:107],a[236:239],v[212:215],v[132:135]
EX v85,v85
MM v[104:107],a[232:235],v[196:199],v[104:107]
TR a[186:187],v233 offset:38400
CV v82,v84,v85
MM v[104:107],a[228:231],v[180:183],v[104:107]
EX v86,v86
MM v[104:107],a[224:227],v[164:167],v[104:107]
TR a[188:189],v233 offset:42496
TR a[190:191],v233 offset:46592
MM v[112:115],a[224:227],v[168:171],v[136:139]
EX v87,v87
MM v[112:115],a[228:231],v[184:187],v[112:115]
CV v83,v86,v87
MM v[112:115],a[232:235],v[200:203],v[112:115]
EX v88,v88
MM v[112:115],a[236:239],v[216:219],v[112:115]
EX v89,v89
MM v[116:119],a[252:255],v[216:219],v[136:139]
EX v90,v90
MM v[116:119],a[248:251],v[200:203],v[116:119]
EX v91,v91
MM v[116:119],a[244:247],v[184:187],v[116:119]
EX v92,v92
MM v[116:119],a[240:243],v[168:171],v[116:119]
EX v93,v93
MM v[124:127],a[240:243],v[172:175],v[140:143]
EX v94,v94
MM v[124:127],a[244:247],v[188:191],v[124:127]
EX v95,v95
MM v[124:127],a[248:251],v[204:207],v[124:127]
EX v96,v96
MM v[124:127],a[252:255],v[220:223],v[124:127]
EX v97,v97
MM v[120:123],a[236:239],v[220:223],v[140:143]
EX v98,v98
MM v[120:123],a[232:235],v[204:207],v[120:123]
EX v99,v99
MM v[120:123],a[228:231],v[188:191],v[120:123]
EX v100,v100
MM v[120:123],a[224:227],v[172:175],v[120:123]
s_waitcnt vmcnt(13) lgkmcnt(0)
s_barrier
MM a[0:3],a[128:131],v[0:3],a[0:3]
s_add_u32 m0, s65, 32768
RD v[40:43],v49 offset:32
RD v[44:47],v49 offset:48
MM a[0:3],a[132:135],v[4:7],a[0:3]
RD v[32:35],v48 offset:0
RD v[36:39],v48 offset:1024
MM a[8:11],a[132:135],v[12:15],a[8:11]
CV v88,v88,v89
CV v89,v90,v91
MM a[8:11],a[128:131],v[8:11],a[8:11]
RD a[192:195],v224 offset:0
CV v90,v92,v93
MM a[16:19],a[128:131],v[16:19],a[16:19]
RD a[196:199],v225 offset:0
CV v91,v94,v95
MM a[16:19],a[132:135],v[20:23],a[16:19]
s_waitcnt lgkmcnt(4)
v_min3_u32 v50, v40, v41, v42
v_min3_u32 v51, v43, v44, v45
MM a[24:27],a[132:135],v[28:31],a[24:27]
v_min3_u32 v50, v50, v46, v47
v_min_u32 v50, v50, v51
MM a[24:27],a[128:131],v[24:27],a[24:27]
v_readfirstlane_b32 s76, v50
CV v68,v96,v97
MM a[28:31],a[136:139],v[24:27],a[28:31]
s_sub_u32 s92, s92, s76
buffer_load_dwordx4 v228, s[8:11], s64 offen offset:0 lds
MM a[28:31],a[140:143],v[28:31],a[28:31]
s_add_u32 s76, s76, 1
v_subrev_u32 v40, s76, v40
v_subrev_u32 v41, s76, v41
MM a[20:23],a[140:143],v[20:23],a[20:23]
v_subrev_u32 v42, s76, v42
v_subrev_u32 v43, s76, v43
MM a[20:23],a[136:139],v[16:19],a[20:23]
v_subrev_u32 v44, s76, v44
v_subrev_u32 v45, s76, v45
MM a[12:15],a[136:139],v[8:11],a[12:15]
v_subrev_u32 v46, s76, v46
v_subrev_u32 v47, s76, v47
MM a[12:15],a[140:143],v[12:15],a[12:15]
s_and_b32 s77, s92, 65535
RD a[200:203],v226 offset:0
CV v69,v98,v99
MM a[4:7],a[140:143],v[4:7],a[4:7]
s_sub_u32 s54, 65535, s77
v_min3_u32 v50, v40, v41, v42
v_min3_u32 v51, v43, v44, v45
MM a[4:7],a[136:139],v[0:3],a[4:7]
v_min3_u32 v50, v50, v46, v47
v_min_u32 v50, v50, v51
MM a[32:35],a[144:147],v[0:3],a[32:35]
RD a[204:207],v227 offset:0
v_readfirstlane_b32 s76, v50
MM a[32:35],a[148:151],v[4:7],a[32:35]
s_sub_u32 s92, s92, s76
EX v101,v101
MM a[40:43],a[148:151],v[12:15],a[40:43]
s_sub_u32 s92, s92, 1
EX v102,v102
MM a[40:43],a[144:147],v[8:11],a[40:43]
s_and_b32 s77, s92, 65535
EX v103,v103
MM a[48:51],a[144:147],v[16:19],a[48:51]
s_sub_u32 s55, 65535, s77
buffer_load_dwordx4 v229, s[8:11], s64 offen offset:1024 lds
MM a[48:51],a[148:151],v[20:23],a[48:51]
s_sub_u32 s92, s92, 1
EX v104,v104
MM a[56:59],a[148:151],v[28:31],a[56:59]
s_waitcnt lgkmcnt(4)
v_sub_u32 v32, s92, v32
v_sub_u32 v33, s92, v33
MM a[56:59],a[144:147],v[24:27],a[56:59]
v_sub_u32 v34, s92, v34
v_sub_u32 v35, s92, v35
MM a[60:63],a[152:155],v[24:27],a[60:63]
v_sub_u32 v36, s92, v36
v_sub_u32 v37, s92, v37
EX v105,v105
MM a[60:63],a[156:159],v[28:31],a[60:63]
v_sub_u32 v38, s92, v38
v_sub_u32 v39, s92, v39
MM a[52:55],a[156:159],v[20:23],a[52:55]
v_min3_u32 v52, v32, v33, v34
v_min3_u32 v51, v35, v36, v37
EX v106,v106
MM a[52:55],a[152:155],v[16:19],a[52:55]
v_min3_u32 v52, v52, v38, v39
v_min_u32 v52, v52, v51
CV v70,v100,v101
MM a[44:47],a[152:155],v[8:11],a[44:47]
v_min_u32_dpp v52, v52, v52 row_shr:1 row_mask:0xf bank_mask:0xf
CV v71,v102,v103
EX v107,v107
MM a[44:47],a[156:159],v[12:15],a[44:47]
RD a[208:211],v224 offset:4096
v_min_u32_dpp v52, v52, v52 row_shr:2 row_mask:0xf bank_mask:0xf
MM a[36:39],a[156:159],v[4:7],a[36:39]
s_nop 0
v_min_u32_dpp v52, v52, v52 row_shr:4 row_mask:0xf bank_mask:0xf
CV v76,v104,v105
EX v108,v108
MM a[36:39],a[152:155],v[0:3],a[36:39]
RD a[212:215],v225 offset:4096
v_min_u32_dpp v52, v52, v52 row_shr:8 row_mask:0xf bank_mask:0xf
MM a[64:67],a[160:163],v[0:3],a[64:67]
s_nop 0
v_min_u32_dpp v52, v52, v52 row_bcast:15 row_mask:0xa bank_mask:0xf
CV v77,v106,v107
EX v109,v109
MM a[64:67],a[164:167],v[4:7],a[64:67]
RD a[216:219],v226 offset:4096
v_min_u32_dpp v52, v52, v52 row_bcast:31 row_mask:0xc bank_mask:0xf
MM a[72:75],a[164:167],v[12:15],a[72:75]
buffer_load_dwordx4 v230, s[8:11], s64 offen offset:2048 lds
v_readlane_b32 s76, v52, 63
v_lshl_add_u32 v49, s80, 1, v49
EX v110,v110
MM a[72:75],a[160:163],v[8:11],a[72:75]
s_add_u32 s76, s76, 1
v_subrev_u32 v32, s76, v32
v_subrev_u32 v33, s76, v33
EX v111,v111
MM a[80:83],a[160:163],v[16:19],a[80:83]
v_subrev_u32 v34, s76, v34
v_subrev_u32 v35, s76, v35
MM a[80:83],a[164:167],v[20:23],a[80:83]
v_subrev_u32 v36, s76, v36
v_subrev_u32 v37, s76, v37
EX v112,v112
MM a[88:91],a[164:167],v[28:31],a[88:91]
v_subrev_u32 v38, s76, v38
v_subrev_u32 v39, s76, v39
MM a[88:91],a[160:163],v[24:27],a[88:91]
v_min3_u32 v53, v32, v33, v34
v_min3_u32 v51, v35, v36, v37
EX v113,v113
MM a[92:95],a[168:171],v[24:27],a[92:95]
v_min3_u32 v53, v53, v38, v39
v_min_u32 v53, v53, v51
MM a[92:95],a[172:175],v[28:31],a[92:95]
s_nop 0
v_min_u32_dpp v53, v53, v53 row_shr:1 row_mask:0xf bank_mask:0xf
CV v78,v108,v109
EX v114,v114
MM a[84:87],a[172:175],v[20:23],a[84:87]
RD a[220:223],v227 offset:4096
buffer_load_dwordx4 v231, s[8:11], s64 offen offset:3072 lds
v_min_u32_dpp v53, v53, v53 row_shr:2 row_mask:0xf bank_mask:0xf
MM a[84:87],a[168:171],v[16:19],a[84:87]
s_add_u32 m0, s65, 98304
v_min_u32_dpp v53, v53, v53 row_shr:4 row_mask:0xf bank_mask:0xf
CV v79,v110,v111
EX v115,v115
MM a[76:79],a[168:171],v[8:11],a[76:79]
RD a[224:227],v224 offset:8192
v_min_u32_dpp v53, v53, v53 row_shr:8 row_mask:0xf bank_mask:0xf
MM a[76:79],a[172:175],v[12:15],a[76:79]
s_nop 0
v_min_u32_dpp v53, v53, v53 row_bcast:15 row_mask:0xa bank_mask:0xf
CV v84,v112,v113
EX v116,v116
MM a[68:71],a[172:175],v[4:7],a[68:71]
RD a[228:231],v225 offset:8192
v_min_u32_dpp v53, v53, v53 row_bcast:31 row_mask:0xc bank_mask:0xf
EX v117,v117
MM a[68:71],a[168:171],v[0:3],a[68:71]
v_add_u32 v53, s76, v53
CV v85,v114,v115
MM a[96:99],a[176:179],v[0:3],a[96:99]
s_mov_b32 exec_lo, 0
s_mov_b32 exec_hi, 0x80000000
ds_write_b64 v49, v[52:53] offset:0
s_mov_b64 exec, -1
EX v118,v118
MM a[96:99],a[180:183],v[4:7],a[96:99]
)ASM"
R"ASM(
buffer_load_dwordx4 v235, s[12:15], s81 offen offset:0 lds
MM a[104:107],a[180:183],v[12:15],a[104:107]
EX v119,v119
MM a[104:107],a[176:179],v[8:11],a[104:107]
EX v120,v120
MM a[112:115],a[176:179],v[16:19],a[112:115]
RD a[232:235],v226 offset:8192
CV v86,v116,v117
MM a[112:115],a[180:183],v[20:23],a[112:115]
EX v121,v121
MM a[120:123],a[180:183],v[28:31],a[120:123]
buffer_load_dwordx4 v235, s[12:15], s64 offen offset:1024 lds
MM a[120:123],a[176:179],v[24:27],a[120:123]
EX v122,v122
MM a[124:127],a[184:187],v[24:27],a[124:127]
RD a[236:239],v227 offset:8192
CV v87,v118,v119
MM a[124:127],a[188:191],v[28:31],a[124:127]
EX v123,v123
MM a[116:119],a[188:191],v[20:23],a[116:119]
EX v124,v124
MM a[116:119],a[184:187],v[16:19],a[116:119]
RD a[240:243],v224 offset:12288
CV v92,v120,v121
MM a[108:111],a[184:187],v[8:11],a[108:111]
EX v125,v125
MM a[108:111],a[188:191],v[12:15],a[108:111]
buffer_load_dwordx4 v235, s[12:15], s81 offen offset:2048 lds
MM a[100:103],a[188:191],v[4:7],a[100:103]
EX v126,v126
MM a[100:103],a[184:187],v[0:3],a[100:103]
RD a[244:247],v225 offset:12288
CV v93,v122,v123
v_smfmac_f32_16x16x64_bf16 v[236:239], v[144:147], v[64:71], v234
RD a[248:251],v226 offset:12288
RD a[252:255],v227 offset:12288
EX v127,v127
v_smfmac_f32_16x16x64_bf16 v[236:239], v[148:151], v[72:79], v234
CV v94,v124,v125
v_smfmac_f32_16x16x64_bf16 v[236:239], v[152:155], v[80:87], v234
CV v95,v126,v127
s_nop 1
v_smfmac_f32_16x16x64_bf16 v[236:239], v[156:159], v[88:95], v234
s_waitcnt lgkmcnt(0)
buffer_load_dwordx4 v235, s[12:15], s64 offen offset:3072 lds
MM v[0:3],a[192:195],v[160:163],v[128:131]
s_add_u32 s64, s64, 16384
TR a[128:129],v232 offset:49152
s_add_u32 s81, s64, 896
MM v[0:3],a[196:199],v[176:179],v[0:3]
TR a[130:131],v232 offset:53248
TR a[132:133],v232 offset:57344
MM v[0:3],a[200:203],v[192:195],v[0:3]
TR a[134:135],v232 offset:61440
TR a[136:137],v233 offset:49152
MM v[0:3],a[204:207],v[208:211],v[0:3]
TR a[138:139],v233 offset:53248
v_add_f32 v247, v236, v237
MM v[4:7],a[220:223],v[208:211],v[128:131]
TR a[140:141],v233 offset:57344
v_cmp_nge_f32 vcc, s71, v247
MM v[4:7],a[216:219],v[192:195],v[4:7]
s_cbranch_vccnz LSITE7_%=
LBACK7_%=:
TR a[142:143],v233 offset:61440
TR a[144:145],v232 offset:49664
MM v[4:7],a[212:215],v[176:179],v[4:7]
s_mov_b32 m0, s67
TR a[146:147],v232 offset:53760
TR a[148:149],v232 offset:57856
MM v[4:7],a[208:211],v[160:163],v[4:7]
buffer_load_dwordx4 v252, s[36:39], s66 offen offset:0 lds
MM v[12:15],a[208:211],v[164:167],v[132:135]
s_add_u32 s66, s66, 55296
EX v0,v0
MM v[12:15],a[212:215],v[180:183],v[12:15]
s_min_u32 s66, s66, s85
EX v1,v1
MM v[12:15],a[216:219],v[196:199],v[12:15]
s_add_u32 s67, s67, 1024
TR a[150:151],v232 offset:61952
CV v0,v0,v1
MM v[12:15],a[220:223],v[212:215],v[12:15]
s_min_u32 s67, s67, s86
EX v2,v2
MM v[8:11],a[204:207],v[212:215],v[132:135]
EX v3,v3
MM v[8:11],a[200:203],v[196:199],v[8:11]
TR a[152:153],v233 offset:49664
CV v1,v2,v3
MM v[8:11],a[196:199],v[180:183],v[8:11]
EX v4,v4
MM v[8:11],a[192:195],v[164:167],v[8:11]
EX v5,v5
MM v[16:19],a[192:195],v[168:171],v[136:139]
TR a[154:155],v233 offset:53760
CV v2,v4,v5
MM v[16:19],a[196:199],v[184:187],v[16:19]
EX v6,v6
MM v[16:19],a[200:203],v[200:203],v[16:19]
EX v7,v7
MM v[16:19],a[204:207],v[216:219],v[16:19]
TR a[156:157],v233 offset:57856
CV v3,v6,v7
MM v[20:23],a[220:223],v[216:219],v[136:139]
EX v8,v8
MM v[20:23],a[216:219],v[200:203],v[20:23]
TR a[158:159],v233 offset:61952
TR a[160:161],v232 offset:50176
MM v[20:23],a[212:215],v[184:187],v[20:23]
EX v9,v9
MM v[20:23],a[208:211],v[168:171],v[20:23]
TR a[162:163],v232 offset:54272
CV v8,v8,v9
MM v[28:31],a[208:211],v[172:175],v[140:143]
EX v10,v10
MM v[28:31],a[212:215],v[188:191],v[28:31]
TR a[164:165],v232 offset:58368
TR a[166:167],v232 offset:62464
MM v[28:31],a[216:219],v[204:207],v[28:31]
EX v11,v11
MM v[28:31],a[220:223],v[220:223],v[28:31]
TR a[168:169],v233 offset:50176
CV v9,v10,v11
MM v[24:27],a[204:207],v[220:223],v[140:143]
LPG28_%=:
s_cbranch_execz LTR1_%=
EX v12,v12
MM v[24:27],a[200:203],v[204:207],v[24:27]
EX v13,v13
MM v[24:27],a[196:199],v[188:191],v[24:27]
TR a[170:171],v233 offset:54272
CV v10,v12,v13
MM v[24:27],a[192:195],v[172:175],v[24:27]
EX v14,v14
MM v[32:35],a[224:227],v[160:163],v[128:131]
TR a[172:173],v233 offset:58368
TR a[174:175],v233 offset:62464
MM v[32:35],a[228:231],v[176:179],v[32:35]
EX v15,v15
MM v[32:35],a[232:235],v[192:195],v[32:35]
TR a[176:177],v232 offset:50688
CV v11,v14,v15
MM v[32:35],a[236:239],v[208:211],v[32:35]
EX v16,v16
MM v[36:39],a[252:255],v[208:211],v[128:131]
EX v17,v17
MM v[36:39],a[248:251],v[192:195],v[36:39]
TR a[178:179],v232 offset:54784
CV v16,v16,v17
MM v[36:39],a[244:247],v[176:179],v[36:39]
EX v18,v18
MM v[36:39],a[240:243],v[160:163],v[36:39]
TR a[180:181],v232 offset:58880
TR a[182:183],v232 offset:62976
MM v[44:47],a[240:243],v[164:167],v[132:135]
v_add_u32 v112, s78, v248
v_mov_b32 v113, s79
MM v[44:47],a[244:247],v[180:183],v[44:47]
EX v19,v19
MM v[44:47],a[248:251],v[196:199],v[44:47]
TR a[184:185],v233 offset:50688
CV v17,v18,v19
MM v[44:47],a[252:255],v[212:215],v[44:47]
EX v20,v20
MM v[40:43],a[236:239],v[212:215],v[132:135]
EX v21,v21
MM v[40:43],a[232:235],v[196:199],v[40:43]
TR a[186:187],v233 offset:54784
CV v18,v20,v21
MM v[40:43],a[228:231],v[180:183],v[40:43]
EX v22,v22
MM v[40:43],a[224:227],v[164:167],v[40:43]
TR a[188:189],v233 offset:58880
TR a[190:191],v233 offset:62976
MM v[48:51],a[224:227],v[168:171],v[136:139]
EX v23,v23
MM v[48:51],a[228:231],v[184:187],v[48:51]
CV v19,v22,v23
MM v[48:51],a[232:235],v[200:203],v[48:51]
EX v24,v24
MM v[48:51],a[236:239],v[216:219],v[48:51]
EX v25,v25
MM v[52:55],a[252:255],v[216:219],v[136:139]
EX v26,v26
MM v[52:55],a[248:251],v[200:203],v[52:55]
EX v27,v27
MM v[52:55],a[244:247],v[184:187],v[52:55]
EX v28,v28
MM v[52:55],a[240:243],v[168:171],v[52:55]
EX v29,v29
MM v[60:63],a[240:243],v[172:175],v[140:143]
EX v30,v30
MM v[60:63],a[244:247],v[188:191],v[60:63]
EX v31,v31
MM v[60:63],a[248:251],v[204:207],v[60:63]
EX v32,v32
MM v[60:63],a[252:255],v[220:223],v[60:63]
EX v33,v33
MM v[56:59],a[236:239],v[220:223],v[140:143]
EX v34,v34
MM v[56:59],a[232:235],v[204:207],v[56:59]
EX v35,v35
MM v[56:59],a[228:231],v[188:191],v[56:59]
EX v36,v36
MM v[56:59],a[224:227],v[172:175],v[56:59]
s_waitcnt vmcnt(13) lgkmcnt(0)
s_barrier
MM a[0:3],a[128:131],v[64:67],a[0:3]
s_add_u32 m0, s65, 49152
RD v[104:107],v113 offset:0
RD v[108:111],v113 offset:16
MM a[0:3],a[132:135],v[68:71],a[0:3]
RD v[96:99],v112 offset:0
RD v[100:103],v112 offset:1024
MM a[8:11],a[132:135],v[76:79],a[8:11]
CV v24,v24,v25
CV v25,v26,v27
MM a[8:11],a[128:131],v[72:75],a[8:11]
RD a[192:195],v224 offset:16384
CV v26,v28,v29
MM a[16:19],a[128:131],v[80:83],a[16:19]
RD a[196:199],v225 offset:16384
CV v27,v30,v31
MM a[16:19],a[132:135],v[84:87],a[16:19]
s_waitcnt lgkmcnt(4)
v_min3_u32 v114, v104, v105, v106
v_min3_u32 v115, v107, v108, v109
MM a[24:27],a[132:135],v[92:95],a[24:27]
v_min3_u32 v114, v114, v110, v111
v_min_u32 v114, v114, v115
MM a[24:27],a[128:131],v[88:91],a[24:27]
v_readfirstlane_b32 s76, v114
CV v4,v32,v33
MM a[28:31],a[136:139],v[88:91],a[28:31]
s_sub_u32 s92, s92, s76
buffer_load_dwordx4 v228, s[8:11], s64 offen offset:0 lds
MM a[28:31],a[140:143],v[92:95],a[28:31]
s_add_u32 s76, s76, 1
v_subrev_u32 v104, s76, v104
v_subrev_u32 v105, s76, v105
MM a[20:23],a[140:143],v[84:87],a[20:23]
v_subrev_u32 v106, s76, v106
v_subrev_u32 v107, s76, v107
MM a[20:23],a[136:139],v[80:83],a[20:23]
v_subrev_u32 v108, s76, v108
v_subrev_u32 v109, s76, v109
MM a[12:15],a[136:139],v[72:75],a[12:15]
v_subrev_u32 v110, s76, v110
v_subrev_u32 v111, s76, v111
MM a[12:15],a[140:143],v[76:79],a[12:15]
s_and_b32 s77, s92, 65535
RD a[200:203],v226 offset:16384
CV v5,v34,v35
MM a[4:7],a[140:143],v[68:71],a[4:7]
s_sub_u32 s56, 65535, s77
v_min3_u32 v114, v104, v105, v106
v_min3_u32 v115, v107, v108, v109
MM a[4:7],a[136:139],v[64:67],a[4:7]
v_min3_u32 v114, v114, v110, v111
v_min_u32 v114, v114, v115
MM a[32:35],a[144:147],v[64:67],a[32:35]
RD a[204:207],v227 offset:16384
v_readfirstlane_b32 s76, v114
MM a[32:35],a[148:151],v[68:71],a[32:35]
s_sub_u32 s92, s92, s76
EX v37,v37
MM a[40:43],a[148:151],v[76:79],a[40:43]
s_sub_u32 s92, s92, 1
EX v38,v38
MM a[40:43],a[144:147],v[72:75],a[40:43]
s_and_b32 s77, s92, 65535
EX v39,v39
MM a[48:51],a[144:147],v[80:83],a[48:51]
s_sub_u32 s57, 65535, s77
buffer_load_dwordx4 v229, s[8:11], s64 offen offset:1024 lds
MM a[48:51],a[148:151],v[84:87],a[48:51]
s_sub_u32 s92, s92, 1
EX v40,v40
MM a[56:59],a[148:151],v[92:95],a[56:59]
s_waitcnt lgkmcnt(4)
v_sub_u32 v96, s92, v96
v_sub_u32 v97, s92, v97
MM a[56:59],a[144:147],v[88:91],a[56:59]
v_sub_u32 v98, s92, v98
v_sub_u32 v99, s92, v99
MM a[60:63],a[152:155],v[88:91],a[60:63]
v_sub_u32 v100, s92, v100
v_sub_u32 v101, s92, v101
EX v41,v41
MM a[60:63],a[156:159],v[92:95],a[60:63]
v_sub_u32 v102, s92, v102
v_sub_u32 v103, s92, v103
MM a[52:55],a[156:159],v[84:87],a[52:55]
v_min3_u32 v116, v96, v97, v98
v_min3_u32 v115, v99, v100, v101
EX v42,v42
MM a[52:55],a[152:155],v[80:83],a[52:55]
v_min3_u32 v116, v116, v102, v103
v_min_u32 v116, v116, v115
CV v6,v36,v37
MM a[44:47],a[152:155],v[72:75],a[44:47]
v_min_u32_dpp v116, v116, v116 row_shr:1 row_mask:0xf bank_mask:0xf
CV v7,v38,v39
EX v43,v43
MM a[44:47],a[156:159],v[76:79],a[44:47]
RD a[208:211],v224 offset:20480
v_min_u32_dpp v116, v116, v116 row_shr:2 row_mask:0xf bank_mask:0xf
MM a[36:39],a[156:159],v[68:71],a[36:39]
s_nop 0
v_min_u32_dpp v116, v116, v116 row_shr:4 row_mask:0xf bank_mask:0xf
CV v12,v40,v41
EX v44,v44
MM a[36:39],a[152:155],v[64:67],a[36:39]
RD a[212:215],v225 offset:20480
v_min_u32_dpp v116, v116, v116 row_shr:8 row_mask:0xf bank_mask:0xf
MM a[64:67],a[160:163],v[64:67],a[64:67]
s_nop 0
v_min_u32_dpp v116, v116, v116 row_bcast:15 row_mask:0xa bank_mask:0xf
CV v13,v42,v43
EX v45,v45
MM a[64:67],a[164:167],v[68:71],a[64:67]
RD a[216:219],v226 offset:20480
v_min_u32_dpp v116, v116, v116 row_bcast:31 row_mask:0xc bank_mask:0xf
MM a[72:75],a[164:167],v[76:79],a[72:75]
buffer_load_dwordx4 v230, s[8:11], s64 offen offset:2048 lds
v_readlane_b32 s76, v116, 63
v_lshl_add_u32 v113, s80, 1, v113
EX v46,v46
MM a[72:75],a[160:163],v[72:75],a[72:75]
s_add_u32 s76, s76, 1
v_subrev_u32 v96, s76, v96
v_subrev_u32 v97, s76, v97
EX v47,v47
MM a[80:83],a[160:163],v[80:83],a[80:83]
v_subrev_u32 v98, s76, v98
v_subrev_u32 v99, s76, v99
MM a[80:83],a[164:167],v[84:87],a[80:83]
v_subrev_u32 v100, s76, v100
v_subrev_u32 v101, s76, v101
EX v48,v48
MM a[88:91],a[164:167],v[92:95],a[88:91]
v_subrev_u32 v102, s76, v102
v_subrev_u32 v103, s76, v103
MM a[88:91],a[160:163],v[88:91],a[88:91]
v_min3_u32 v117, v96, v97, v98
v_min3_u32 v115, v99, v100, v101
EX v49,v49
MM a[92:95],a[168:171],v[88:91],a[92:95]
v_min3_u32 v117, v117, v102, v103
v_min_u32 v117, v117, v115
MM a[92:95],a[172:175],v[92:95],a[92:95]
s_nop 0
v_min_u32_dpp v117, v117, v117 row_shr:1 row_mask:0xf bank_mask:0xf
CV v14,v44,v45
EX v50,v50
MM a[84:87],a[172:175],v[84:87],a[84:87]
RD a[220:223],v227 offset:20480
buffer_load_dwordx4 v231, s[8:11], s64 offen offset:3072 lds
v_min_u32_dpp v117, v117, v117 row_shr:2 row_mask:0xf bank_mask:0xf
MM a[84:87],a[168:171],v[80:83],a[84:87]
s_add_u32 m0, s65, 114688
v_min_u32_dpp v117, v117, v117 row_shr:4 row_mask:0xf bank_mask:0xf
CV v15,v46,v47
EX v51,v51
MM a[76:79],a[168:171],v[72:75],a[76:79]
RD a[224:227],v224 offset:24576
v_min_u32_dpp v117, v117, v117 row_shr:8 row_mask:0xf bank_mask:0xf
MM a[76:79],a[172:175],v[76:79],a[76:79]
s_nop 0
v_min_u32_dpp v117, v117, v117 row_bcast:15 row_mask:0xa bank_mask:0xf
CV v20,v48,v49
EX v52,v52
MM a[68:71],a[172:175],v[68:71],a[68:71]
RD a[228:231],v225 offset:24576
v_min_u32_dpp v117, v117, v117 row_bcast:31 row_mask:0xc bank_mask:0xf
EX v53,v53
MM a[68:71],a[168:171],v[64:67],a[68:71]
v_add_u32 v117, s76, v117
CV v21,v50,v51
MM a[96:99],a[176:179],v[64:67],a[96:99]
s_mov_b32 exec_lo, 0
s_mov_b32 exec_hi, 0x80000000
ds_write_b64 v113, v[116:117] offset:32
s_mov_b64 exec, -1
)ASM"
R"ASM(
EX v54,v54
MM a[96:99],a[180:183],v[68:71],a[96:99]
buffer_load_dwordx4 v235, s[12:15], s81 offen offset:0 lds
MM a[104:107],a[180:183],v[76:79],a[104:107]
EX v55,v55
MM a[104:107],a[176:179],v[72:75],a[104:107]
EX v56,v56
MM a[112:115],a[176:179],v[80:83],a[112:115]
RD a[232:235],v226 offset:24576
CV v22,v52,v53
MM a[112:115],a[180:183],v[84:87],a[112:115]
EX v57,v57
MM a[120:123],a[180:183],v[92:95],a[120:123]
buffer_load_dwordx4 v235, s[12:15], s64 offen offset:1024 lds
MM a[120:123],a[176:179],v[88:91],a[120:123]
EX v58,v58
MM a[124:127],a[184:187],v[88:91],a[124:127]
RD a[236:239],v227 offset:24576
CV v23,v54,v55
MM a[124:127],a[188:191],v[92:95],a[124:127]
EX v59,v59
MM a[116:119],a[188:191],v[84:87],a[116:119]
EX v60,v60
MM a[116:119],a[184:187],v[80:83],a[116:119]
RD a[240:243],v224 offset:28672
CV v28,v56,v57
MM a[108:111],a[184:187],v[72:75],a[108:111]
EX v61,v61
MM a[108:111],a[188:191],v[76:79],a[108:111]
buffer_load_dwordx4 v235, s[12:15], s81 offen offset:2048 lds
MM a[100:103],a[188:191],v[68:71],a[100:103]
EX v62,v62
MM a[100:103],a[184:187],v[64:67],a[100:103]
RD a[244:247],v225 offset:28672
CV v29,v58,v59
v_smfmac_f32_16x16x64_bf16 v[236:239], v[144:147], v[0:7], v234
RD a[248:251],v226 offset:28672
RD a[252:255],v227 offset:28672
EX v63,v63
v_smfmac_f32_16x16x64_bf16 v[236:239], v[148:151], v[8:15], v234
CV v30,v60,v61
v_smfmac_f32_16x16x64_bf16 v[236:239], v[152:155], v[16:23], v234
CV v31,v62,v63
s_nop 1
v_smfmac_f32_16x16x64_bf16 v[236:239], v[156:159], v[24:31], v234
s_waitcnt lgkmcnt(0)
buffer_load_dwordx4 v235, s[12:15], s64 offen offset:3072 lds
s_waitcnt lgkmcnt(0)
s_barrier
v_mov_b32 v113, s79
RD v[104:107],v113 offset:32
RD v[108:111],v113 offset:48
s_waitcnt lgkmcnt(0)
v_min3_u32 v114, v104, v105, v106
v_min3_u32 v115, v107, v108, v109
v_min3_u32 v114, v114, v110, v111
v_min_u32 v114, v114, v115
s_nop 0
v_readfirstlane_b32 s76, v114
s_nop 0
s_sub_u32 s92, s92, s76
s_add_u32 s76, s76, 1
v_subrev_u32 v104, s76, v104
v_subrev_u32 v105, s76, v105
v_subrev_u32 v106, s76, v106
v_subrev_u32 v107, s76, v107
v_subrev_u32 v108, s76, v108
v_subrev_u32 v109, s76, v109
v_subrev_u32 v110, s76, v110
v_subrev_u32 v111, s76, v111
s_and_b32 s77, s92, 65535
s_sub_u32 s58, 65535, s77
v_min3_u32 v114, v104, v105, v106
v_min3_u32 v115, v107, v108, v109
v_min3_u32 v114, v114, v110, v111
v_min_u32 v114, v114, v115
s_nop 0
v_readfirstlane_b32 s76, v114
s_nop 0
s_sub_u32 s92, s92, s76
s_sub_u32 s92, s92, 1
s_and_b32 s77, s92, 65535
s_sub_u32 s59, 65535, s77
s_sub_u32 s92, s92, 1
s_lshl_b32 s90, %[swave], 8
s_add_u32 s90, s90, %[sb]
s_cmp_lg_u32 s90, 0
s_cbranch_scc1 LIX_%=
v_writelane_b32 v112, s52, 0
v_writelane_b32 v112, s53, 1
v_writelane_b32 v112, s54, 2
v_writelane_b32 v112, s55, 3
v_writelane_b32 v112, s56, 4
v_writelane_b32 v112, s57, 5
v_writelane_b32 v112, s58, 6
v_writelane_b32 v112, s59, 7
v_mov_b32 v113, s69
v_lshrrev_b32 v116, 1, v248
s_mov_b32 s90, 86016
s_mov_b64 exec, 0xff
buffer_store_dwordx2 v[112:113], v116, s[20:23], s90 offen offset:0 sc1
s_mov_b64 exec, -1
s_nop 1
LIX_%=:
s_mov_b32 s70, 1
s_branch LLA_%=
.fill 435, 4, 0xbf800000
LPG29_%=:
s_cbranch_execz LTR1_%=
s_nop 0
LRG1o_%=:
s_mov_b32 s70, 30001
LRG1ol_%=:
s_sleep 2
buffer_load_dwordx4 v[200:203], v208, s[20:23], s93 offen offset:0 sc1
buffer_load_dwordx4 v[204:207], v208, s[20:23], s93 offen offset:256 sc1
s_waitcnt vmcnt(0)
v_xor_b32 v201, s69, v201
v_xor_b32 v203, s69, v203
v_xor_b32 v205, s69, v205
v_xor_b32 v207, s69, v207
v_or3_b32 v209, v201, v203, v205
v_or_b32 v209, v209, v207
v_cmp_ne_u32 vcc, 0, v209
s_cbranch_vccz LRG1ob_%=
s_sub_u32 s70, s70, 1
s_cbranch_scc0 LRG1ol_%=
s_branch LRG1ob_%=
LRFR_%=:
s_mov_b32 s91, 30001
LRFRl_%=:
s_sleep 2
buffer_load_dwordx4 v[48:51], v248, s[20:23], s90 offen offset:0 sc1
s_waitcnt vmcnt(0)
v_xor_b32 v48, s69, v48
v_xor_b32 v49, s69, v49
v_xor_b32 v50, s69, v50
v_xor_b32 v51, s69, v51
v_or3_b32 v52, v48, v49, v50
v_or_b32 v52, v52, v51
v_cmp_ne_u32 vcc, 0, v52
s_cbranch_vccz LRFRb_%=
s_sub_u32 s91, s91, 1
s_cbranch_scc0 LRFRl_%=
s_branch LRFRb_%=
LRIX_%=:
s_mov_b32 s91, 30001
LRIXl_%=:
s_sleep 2
buffer_load_dwordx2 v[48:49], v52, s[20:23], s90 offen offset:0 sc1
s_waitcnt vmcnt(0)
v_cmp_ne_u32 vcc, s69, v49
s_cbranch_vccz LRIXb_%=
s_sub_u32 s91, s91, 1
s_cbranch_scc0 LRIXl_%=
s_branch LRIXb_%=
LRFG_%=:
s_mov_b32 s91, 30001
LRFGl_%=:
s_sleep 2
buffer_load_dword v252, v253, s[20:23], s76 offen offset:0 sc1
s_waitcnt vmcnt(0)
v_cmp_ne_u32 vcc, s69, v252
s_cbranch_vccz LRFGb_%=
s_sub_u32 s91, s91, 1
s_cbranch_scc0 LRFGl_%=
s_branch LRFGb_%=
LRFH_%=:
s_mov_b32 s91, 30001
LRFHl_%=:
s_sleep 2
buffer_load_dword v252, v253, s[20:23], s76 offen offset:0 sc1
s_waitcnt vmcnt(0)
v_cmp_ne_u32 vcc, s69, v252
s_cbranch_vccz LRFHb_%=
s_sub_u32 s91, s91, 1
s_cbranch_scc0 LRFHl_%=
s_branch LRFHb_%=
LRPO0_%=:
s_mov_b32 s97, 30001
LRPO0l_%=:
s_sleep 2
buffer_load_dword v242, v243, s[20:23], s96 offen offset:0 sc1
s_waitcnt vmcnt(0)
v_cmp_ne_u32 vcc, s69, v242
s_cbranch_vccz LRPO0b_%=
s_sub_u32 s97, s97, 1
s_cbranch_scc0 LRPO0l_%=
s_branch LRPO0b_%=
LRPO1_%=:
s_mov_b32 s97, 30001
LRPO1l_%=:
s_sleep 2
buffer_load_dword v242, v243, s[20:23], s96 offen offset:16 sc1
s_waitcnt vmcnt(0)
v_cmp_ne_u32 vcc, s69, v242
s_cbranch_vccz LRPO1b_%=
s_sub_u32 s97, s97, 1
s_cbranch_scc0 LRPO1l_%=
s_branch LRPO1b_%=
LRYF_%=:
s_mov_b32 s91, 30001
LRYFl_%=:
s_sleep 2
buffer_load_dword v132, v135, s[20:23], s78 offen offset:0 sc1
buffer_load_dword v133, v135, s[20:23], s78 offen offset:960 sc1
buffer_load_dword v134, v135, s[20:23], s78 offen offset:1920 sc1
s_waitcnt vmcnt(0)
v_xor_b32 v132, s69, v132
v_xor_b32 v133, s69, v133
v_xor_b32 v134, s69, v134
v_or3_b32 v139, v132, v133, v134
v_cmp_ne_u32 vcc, 0, v139
s_cbranch_vccz LRYFb_%=
s_sub_u32 s91, s91, 1
s_cbranch_scc0 LRYFl_%=
s_branch LRYFb_%=
LQA_%=:
s_bitcmp0_b32 s17, 1
s_cbranch_scc1 LQG1_%=
AR v108,a35
AR v109,a39
AR v110,a43
AR v111,a47
v_cmp_eq_u32 s[24:25], -1, v108
v_cmp_eq_u32 s[26:27], -1, v109
v_cmp_eq_u32 s[28:29], -1, v110
v_cmp_eq_u32 s[46:47], -1, v111
s_nop 0
s_or_b64 s[30:31], s[24:25], s[26:27]
s_or_b64 s[30:31], s[30:31], s[28:29]
s_or_b64 s[30:31], s[30:31], s[46:47]
s_cselect_b32 s76, 256, 2
s_or_b32 s94, s94, s76
s_bitcmp1_b32 s94, 1
s_cbranch_scc1 LQG1_%=
s_and_b64 exec, s[46:47], s[10:11]
s_cbranch_scc0 LQK13_%=
s_add_u32 s66, s44, 6144
buffer_load_dwordx4 a[44:47], v250, s[20:23], s66 offen offset:0 sc1
LQK13_%=:
s_and_b64 exec, s[28:29], s[8:9]
s_cbranch_scc0 LQK12_%=
s_add_u32 s66, s16, 6144
buffer_load_dwordx4 a[40:43], v250, s[20:23], s66 offen offset:0 sc1
LQK12_%=:
s_and_b64 exec, s[26:27], s[10:11]
s_cbranch_scc0 LQK11_%=
s_add_u32 s66, s44, 4096
buffer_load_dwordx4 a[36:39], v250, s[20:23], s66 offen offset:0 sc1
LQK11_%=:
s_and_b64 exec, s[24:25], s[8:9]
s_cbranch_scc0 LQK10_%=
s_add_u32 s66, s16, 4096
buffer_load_dwordx4 a[32:35], v250, s[20:23], s66 offen offset:0 sc1
LQK10_%=:
s_mov_b64 exec, -1
LQG1_%=:
s_bitcmp0_b32 s17, 0
s_cbranch_scc1 LQG0_%=
AR v108,a3
AR v109,a7
AR v110,a11
AR v111,a15
v_cmp_eq_u32 s[24:25], -1, v108
v_cmp_eq_u32 s[26:27], -1, v109
v_cmp_eq_u32 s[28:29], -1, v110
v_cmp_eq_u32 s[46:47], -1, v111
s_nop 0
s_or_b64 s[30:31], s[24:25], s[26:27]
s_or_b64 s[30:31], s[30:31], s[28:29]
s_or_b64 s[30:31], s[30:31], s[46:47]
s_cselect_b32 s76, 256, 1
s_or_b32 s94, s94, s76
s_bitcmp1_b32 s94, 0
s_cbranch_scc1 LQG0_%=
s_and_b64 exec, s[46:47], s[38:39]
s_cbranch_scc0 LQK03_%=
s_add_u32 s66, s44, 2048
buffer_load_dwordx4 a[12:15], v250, s[20:23], s66 offen offset:0 sc1
LQK03_%=:
s_and_b64 exec, s[28:29], s[36:37]
s_cbranch_scc0 LQK02_%=
s_add_u32 s66, s16, 2048
buffer_load_dwordx4 a[8:11], v250, s[20:23], s66 offen offset:0 sc1
LQK02_%=:
s_and_b64 exec, s[26:27], s[38:39]
s_cbranch_scc0 LQK01_%=
s_add_u32 s66, s44, 0
buffer_load_dwordx4 a[4:7], v250, s[20:23], s66 offen offset:0 sc1
LQK01_%=:
s_and_b64 exec, s[24:25], s[36:37]
s_cbranch_scc0 LQK00_%=
s_add_u32 s66, s16, 0
buffer_load_dwordx4 a[0:3], v250, s[20:23], s66 offen offset:0 sc1
LQK00_%=:
s_mov_b64 exec, -1
LQG0_%=:
s_branch LQB_%=
LQC_%=:
s_bitcmp0_b32 s94, 0
s_cbranch_scc1 LQN0_%=
MM v[72:75],v[64:67],a[0:3],0
MM v[72:75],v[64:67],a[4:7],v[72:75]
MM v[72:75],v[68:71],a[8:11],v[72:75]
MM v[72:75],v[68:71],a[12:15],v[72:75]
s_bitset0_b32 s17, 0
s_nop 6
CV v76,v72,v73
CV v77,v74,v75
ds_bpermute_b32 v76, v232, v76
ds_bpermute_b32 v77, v232, v77
v_lshlrev_b32 v10, 16, v0
v_and_b32 v11, 0xffff0000, v0
v_lshlrev_b32 v12, 16, v1
v_and_b32 v13, 0xffff0000, v1
v_lshlrev_b32 v14, 16, v32
v_and_b32 v15, 0xffff0000, v32
v_lshlrev_b32 v42, 16, v33
v_and_b32 v43, 0xffff0000, v33
s_add_u32 s80, s18, 0
s_waitcnt lgkmcnt(0)
v_lshlrev_b32 v72, 16, v76
v_and_b32 v73, 0xffff0000, v76
v_lshlrev_b32 v74, 16, v77
v_and_b32 v75, 0xffff0000, v77
v_mul_f32 v72, v72, v10
v_mul_f32 v73, v73, v11
v_mul_f32 v74, v74, v12
v_mul_f32 v75, v75, v13
CV v76,v72,v73
CV v77,v74,v75
v_lshlrev_b32 v72, 16, v76
v_and_b32 v73, 0xffff0000, v76
v_lshlrev_b32 v74, 16, v77
v_and_b32 v75, 0xffff0000, v77
v_add_f32 v72, v72, v14
v_add_f32 v73, v73, v15
v_add_f32 v74, v74, v42
v_add_f32 v75, v75, v43
CV v44,v72,v73
CV v45,v74,v75
buffer_store_dwordx2 v[44:45], v251, s[40:43], s80 offen offset:0 nt
s_bitcmp0_b32 s94, 8
s_cbranch_scc1 LQN0_%=
s_waitcnt vmcnt(1)
s_branch LQS_%=
LQN0_%=:
s_bitcmp0_b32 s94, 1
s_cbranch_scc1 LQN1_%=
MM v[72:75],v[64:67],a[32:35],0
MM v[72:75],v[64:67],a[36:39],v[72:75]
MM v[72:75],v[68:71],a[40:43],v[72:75]
MM v[72:75],v[68:71],a[44:47],v[72:75]
s_bitset0_b32 s17, 1
s_nop 6
CV v76,v72,v73
CV v77,v74,v75
ds_bpermute_b32 v76, v232, v76
ds_bpermute_b32 v77, v232, v77
v_lshlrev_b32 v10, 16, v2
v_and_b32 v11, 0xffff0000, v2
v_lshlrev_b32 v12, 16, v3
v_and_b32 v13, 0xffff0000, v3
v_lshlrev_b32 v14, 16, v34
v_and_b32 v15, 0xffff0000, v34
v_lshlrev_b32 v42, 16, v35
v_and_b32 v43, 0xffff0000, v35
s_add_u32 s80, s18, 128
s_waitcnt lgkmcnt(0)
v_lshlrev_b32 v72, 16, v76
v_and_b32 v73, 0xffff0000, v76
v_lshlrev_b32 v74, 16, v77
v_and_b32 v75, 0xffff0000, v77
v_mul_f32 v72, v72, v10
v_mul_f32 v73, v73, v11
v_mul_f32 v74, v74, v12
v_mul_f32 v75, v75, v13
CV v76,v72,v73
CV v77,v74,v75
v_lshlrev_b32 v72, 16, v76
v_and_b32 v73, 0xffff0000, v76
v_lshlrev_b32 v74, 16, v77
v_and_b32 v75, 0xffff0000, v77
v_add_f32 v72, v72, v14
v_add_f32 v73, v73, v15
v_add_f32 v74, v74, v42
v_add_f32 v75, v75, v43
CV v46,v72,v73
CV v47,v74,v75
buffer_store_dwordx2 v[46:47], v251, s[40:43], s80 offen offset:0 nt
s_bitcmp0_b32 s94, 8
s_cbranch_scc1 LQN1_%=
s_waitcnt vmcnt(1)
s_branch LQS_%=
LQN1_%=:
s_branch LQD_%=
LSW_%=:
s_sleep 1
s_waitcnt vmcnt(0)
s_cmp_eq_u32 s65, 0
s_cbranch_scc1 LSX_%=
s_mov_b64 exec, 1
buffer_store_dword v233, off, s[20:23], s19 offset:12 sc1
s_mov_b64 exec, -1
s_mov_b32 s65, 0
LSX_%=:
v_cmp_ne_u32 vcc, s69, v243
v_cmp_ne_u32 s[36:37], s69, v143
s_nop 1
s_or_b64 vcc, vcc, s[36:37]
s_cselect_b32 s76, 0, 1
s_add_u32 s72, s72, s76
s_sub_u32 s73, s73, 1
s_cselect_b32 s72, 2, s72
s_bitcmp0_b32 s17, 4
s_cbranch_scc1 LSN4_%=
MM v[72:75],v[64:67],a[192:195],0
MM v[72:75],v[64:67],a[196:199],v[72:75]
MM v[72:75],v[68:71],v[144:147],v[72:75]
MM v[72:75],v[68:71],a[212:215],v[72:75]
AR v76,a195
AR v77,a199
v_mov_b32 v78, v147
AR v79,a215
s_nop 3
v_cmp_u_f32 s[30:31], v72, v72
s_nop 1
s_cmp_eq_u64 s[30:31], 0
s_cbranch_scc1 LSF4_%=
s_cmp_ge_u32 s72, 2
s_cbranch_scc1 LSF4_%=
v_cmp_eq_u32 vcc, s69, v242
v_cmp_eq_u32 s[36:37], -1, v78
s_add_u32 s66, s16, 18432
s_and_b64 vcc, vcc, s[36:37]
s_cmp_eq_u32 s72, 0
s_cselect_b64 exec, vcc, -1
s_and_b32 exec_lo, exec_lo, 0xffff0000
buffer_load_dwordx4 v[144:147], v250, s[20:23], s66 offen offset:0 sc1
s_mov_b64 exec, -1
v_cmp_eq_u32 vcc, s69, v142
v_cmp_eq_u32 s[36:37], -1, v79
s_add_u32 s66, s44, 18432
s_and_b64 vcc, vcc, s[36:37]
s_cmp_eq_u32 s72, 0
s_cselect_b64 exec, vcc, -1
buffer_load_dwordx4 a[212:215], v250, s[20:23], s66 offen offset:0 sc1
s_mov_b64 exec, -1
v_cmp_eq_u32 vcc, s69, v241
v_cmp_eq_u32 s[36:37], -1, v76
s_add_u32 s66, s16, 8192
s_and_b64 vcc, vcc, s[36:37]
s_cmp_eq_u32 s72, 0
s_cselect_b64 exec, vcc, -1
s_and_b32 exec_lo, exec_lo, 0xffff0000
)ASM"
R"ASM(
buffer_load_dwordx4 a[192:195], v250, s[20:23], s66 offen offset:0 sc1
s_mov_b64 exec, -1
v_cmp_eq_u32 vcc, s69, v141
v_cmp_eq_u32 s[36:37], -1, v77
s_add_u32 s66, s44, 8192
s_and_b64 vcc, vcc, s[36:37]
s_cmp_eq_u32 s72, 0
s_cselect_b64 exec, vcc, -1
buffer_load_dwordx4 a[196:199], v250, s[20:23], s66 offen offset:0 sc1
s_mov_b64 exec, -1
s_branch LSN4_%=
LSF4_%=:
s_bitset0_b32 s17, 4
CV v76,v72,v73
CV v77,v74,v75
ds_bpermute_b32 v76, v232, v76
ds_bpermute_b32 v77, v232, v77
v_lshlrev_b32 v10, 16, v8
v_and_b32 v11, 0xffff0000, v8
v_lshlrev_b32 v12, 16, v9
v_and_b32 v13, 0xffff0000, v9
v_lshlrev_b32 v14, 16, v40
v_and_b32 v15, 0xffff0000, v40
v_lshlrev_b32 v42, 16, v41
v_and_b32 v43, 0xffff0000, v41
s_add_u32 s80, s63, 0
s_waitcnt lgkmcnt(0)
v_lshlrev_b32 v72, 16, v76
v_and_b32 v73, 0xffff0000, v76
v_lshlrev_b32 v74, 16, v77
v_and_b32 v75, 0xffff0000, v77
v_mul_f32 v72, v72, v10
v_mul_f32 v73, v73, v11
v_mul_f32 v74, v74, v12
v_mul_f32 v75, v75, v13
CV v76,v72,v73
CV v77,v74,v75
v_lshlrev_b32 v72, 16, v76
v_and_b32 v73, 0xffff0000, v76
v_lshlrev_b32 v74, 16, v77
v_and_b32 v75, 0xffff0000, v77
v_add_f32 v72, v72, v14
v_add_f32 v73, v73, v15
v_add_f32 v74, v74, v42
v_add_f32 v75, v75, v43
CV v44,v72,v73
CV v45,v74,v75
buffer_store_dwordx2 v[44:45], v254, s[40:43], s80 offen offset:0 nt
s_nop 1
LSN4_%=:
s_bitcmp0_b32 s17, 3
s_cbranch_scc1 LSN3_%=
MM v[72:75],v[64:67],v[80:83],0
MM v[72:75],v[64:67],a[100:103],v[72:75]
MM v[72:75],v[68:71],v[112:115],v[72:75]
MM v[72:75],v[68:71],a[108:111],v[72:75]
v_mov_b32 v76, v83
AR v77,a103
v_mov_b32 v78, v115
AR v79,a111
s_nop 3
v_cmp_u_f32 s[30:31], v72, v72
s_nop 1
s_cmp_eq_u64 s[30:31], 0
s_cbranch_scc1 LSF3_%=
s_cmp_ge_u32 s72, 2
s_cbranch_scc1 LSF3_%=
v_cmp_eq_u32 vcc, s69, v242
v_cmp_eq_u32 s[36:37], -1, v78
s_add_u32 s66, s16, 16384
s_and_b64 vcc, vcc, s[36:37]
s_cmp_eq_u32 s72, 0
s_cselect_b64 exec, vcc, -1
s_and_b32 exec_lo, exec_lo, 0xffff0000
buffer_load_dwordx4 v[112:115], v250, s[20:23], s66 offen offset:0 sc1
s_mov_b64 exec, -1
v_cmp_eq_u32 vcc, s69, v142
v_cmp_eq_u32 s[36:37], -1, v79
s_add_u32 s66, s44, 16384
s_and_b64 vcc, vcc, s[36:37]
s_cmp_eq_u32 s72, 0
s_cselect_b64 exec, vcc, -1
buffer_load_dwordx4 a[108:111], v250, s[20:23], s66 offen offset:0 sc1
s_mov_b64 exec, -1
v_cmp_eq_u32 vcc, s69, v242
v_cmp_eq_u32 s[36:37], -1, v76
s_add_u32 s66, s16, 14336
s_and_b64 vcc, vcc, s[36:37]
s_cmp_eq_u32 s72, 0
s_cselect_b64 exec, vcc, -1
s_and_b32 exec_lo, exec_lo, 0xffff0000
buffer_load_dwordx4 v[80:83], v250, s[20:23], s66 offen offset:0 sc1
s_mov_b64 exec, -1
v_cmp_eq_u32 vcc, s69, v142
v_cmp_eq_u32 s[36:37], -1, v77
s_add_u32 s66, s44, 14336
s_and_b64 vcc, vcc, s[36:37]
s_cmp_eq_u32 s72, 0
s_cselect_b64 exec, vcc, -1
buffer_load_dwordx4 a[100:103], v250, s[20:23], s66 offen offset:0 sc1
s_mov_b64 exec, -1
s_branch LSN3_%=
LSF3_%=:
s_bitset0_b32 s17, 3
CV v76,v72,v73
CV v77,v74,v75
ds_bpermute_b32 v76, v232, v76
ds_bpermute_b32 v77, v232, v77
v_lshlrev_b32 v10, 16, v6
v_and_b32 v11, 0xffff0000, v6
v_lshlrev_b32 v12, 16, v7
v_and_b32 v13, 0xffff0000, v7
v_lshlrev_b32 v14, 16, v38
v_and_b32 v15, 0xffff0000, v38
v_lshlrev_b32 v42, 16, v39
v_and_b32 v43, 0xffff0000, v39
s_add_u32 s80, s18, 41088
s_waitcnt lgkmcnt(0)
v_lshlrev_b32 v72, 16, v76
v_and_b32 v73, 0xffff0000, v76
v_lshlrev_b32 v74, 16, v77
v_and_b32 v75, 0xffff0000, v77
v_mul_f32 v72, v72, v10
v_mul_f32 v73, v73, v11
v_mul_f32 v74, v74, v12
v_mul_f32 v75, v75, v13
CV v76,v72,v73
CV v77,v74,v75
v_lshlrev_b32 v72, 16, v76
v_and_b32 v73, 0xffff0000, v76
v_lshlrev_b32 v74, 16, v77
v_and_b32 v75, 0xffff0000, v77
v_add_f32 v72, v72, v14
v_add_f32 v73, v73, v15
v_add_f32 v74, v74, v42
v_add_f32 v75, v75, v43
CV v46,v72,v73
CV v47,v74,v75
buffer_store_dwordx2 v[46:47], v251, s[40:43], s80 offen offset:0 nt
s_nop 1
LSN3_%=:
s_bitcmp0_b32 s17, 2
s_cbranch_scc1 LSN2_%=
MM v[72:75],v[64:67],v[16:19],0
MM v[72:75],v[64:67],a[68:71],v[72:75]
MM v[72:75],v[68:71],v[48:51],v[72:75]
MM v[72:75],v[68:71],a[76:79],v[72:75]
v_mov_b32 v76, v19
AR v77,a71
v_mov_b32 v78, v51
AR v79,a79
s_nop 3
v_cmp_u_f32 s[30:31], v72, v72
s_nop 1
s_cmp_eq_u64 s[30:31], 0
s_cbranch_scc1 LSF2_%=
s_cmp_ge_u32 s72, 2
s_cbranch_scc1 LSF2_%=
v_cmp_eq_u32 vcc, s69, v242
v_cmp_eq_u32 s[36:37], -1, v78
s_add_u32 s66, s16, 12288
s_and_b64 vcc, vcc, s[36:37]
s_cmp_eq_u32 s72, 0
s_cselect_b64 exec, vcc, -1
s_and_b32 exec_lo, exec_lo, 0xffff0000
buffer_load_dwordx4 v[48:51], v250, s[20:23], s66 offen offset:0 sc1
s_mov_b64 exec, -1
v_cmp_eq_u32 vcc, s69, v142
v_cmp_eq_u32 s[36:37], -1, v79
s_add_u32 s66, s44, 12288
s_and_b64 vcc, vcc, s[36:37]
s_cmp_eq_u32 s72, 0
s_cselect_b64 exec, vcc, -1
buffer_load_dwordx4 a[76:79], v250, s[20:23], s66 offen offset:0 sc1
s_mov_b64 exec, -1
v_cmp_eq_u32 vcc, s69, v242
v_cmp_eq_u32 s[36:37], -1, v76
s_add_u32 s66, s16, 10240
s_and_b64 vcc, vcc, s[36:37]
s_cmp_eq_u32 s72, 0
s_cselect_b64 exec, vcc, -1
s_and_b32 exec_lo, exec_lo, 0xffff0000
buffer_load_dwordx4 v[16:19], v250, s[20:23], s66 offen offset:0 sc1
s_mov_b64 exec, -1
v_cmp_eq_u32 vcc, s69, v142
v_cmp_eq_u32 s[36:37], -1, v77
s_add_u32 s66, s44, 10240
s_and_b64 vcc, vcc, s[36:37]
s_cmp_eq_u32 s72, 0
s_cselect_b64 exec, vcc, -1
buffer_load_dwordx4 a[68:71], v250, s[20:23], s66 offen offset:0 sc1
s_mov_b64 exec, -1
s_branch LSN2_%=
LSF2_%=:
s_bitset0_b32 s17, 2
CV v76,v72,v73
CV v77,v74,v75
ds_bpermute_b32 v76, v232, v76
ds_bpermute_b32 v77, v232, v77
v_lshlrev_b32 v10, 16, v4
v_and_b32 v11, 0xffff0000, v4
v_lshlrev_b32 v12, 16, v5
v_and_b32 v13, 0xffff0000, v5
v_lshlrev_b32 v14, 16, v36
v_and_b32 v15, 0xffff0000, v36
v_lshlrev_b32 v42, 16, v37
v_and_b32 v43, 0xffff0000, v37
s_add_u32 s80, s18, 40960
s_waitcnt lgkmcnt(0)
v_lshlrev_b32 v72, 16, v76
v_and_b32 v73, 0xffff0000, v76
v_lshlrev_b32 v74, 16, v77
v_and_b32 v75, 0xffff0000, v77
v_mul_f32 v72, v72, v10
v_mul_f32 v73, v73, v11
v_mul_f32 v74, v74, v12
v_mul_f32 v75, v75, v13
CV v76,v72,v73
CV v77,v74,v75
v_lshlrev_b32 v72, 16, v76
v_and_b32 v73, 0xffff0000, v76
v_lshlrev_b32 v74, 16, v77
v_and_b32 v75, 0xffff0000, v77
v_add_f32 v72, v72, v14
v_add_f32 v73, v73, v15
v_add_f32 v74, v74, v42
v_add_f32 v75, v75, v43
CV v44,v72,v73
CV v45,v74,v75
buffer_store_dwordx2 v[44:45], v251, s[40:43], s80 offen offset:0 nt
s_nop 1
LSN2_%=:
s_bitcmp0_b32 s17, 1
s_cbranch_scc1 LSN1_%=
MM v[72:75],v[64:67],a[32:35],0
MM v[72:75],v[64:67],a[36:39],v[72:75]
MM v[72:75],v[68:71],a[40:43],v[72:75]
MM v[72:75],v[68:71],a[44:47],v[72:75]
AR v76,a35
AR v77,a39
AR v78,a43
AR v79,a47
s_nop 3
v_cmp_u_f32 s[30:31], v72, v72
s_nop 1
s_cmp_eq_u64 s[30:31], 0
s_cbranch_scc1 LSF1_%=
s_cmp_ge_u32 s72, 2
s_cbranch_scc1 LSF1_%=
v_cmp_eq_u32 vcc, s69, v241
v_cmp_eq_u32 s[36:37], -1, v78
s_add_u32 s66, s16, 6144
s_and_b64 vcc, vcc, s[36:37]
s_cmp_eq_u32 s72, 0
s_cselect_b64 exec, vcc, -1
s_and_b32 exec_lo, exec_lo, 0xffff0000
buffer_load_dwordx4 a[40:43], v250, s[20:23], s66 offen offset:0 sc1
s_mov_b64 exec, -1
v_cmp_eq_u32 vcc, s69, v141
v_cmp_eq_u32 s[36:37], -1, v79
s_add_u32 s66, s44, 6144
s_and_b64 vcc, vcc, s[36:37]
s_cmp_eq_u32 s72, 0
s_cselect_b64 exec, vcc, -1
buffer_load_dwordx4 a[44:47], v250, s[20:23], s66 offen offset:0 sc1
s_mov_b64 exec, -1
v_cmp_eq_u32 vcc, s69, v241
v_cmp_eq_u32 s[36:37], -1, v76
s_add_u32 s66, s16, 4096
s_and_b64 vcc, vcc, s[36:37]
s_cmp_eq_u32 s72, 0
s_cselect_b64 exec, vcc, -1
s_and_b32 exec_lo, exec_lo, 0xffff0000
buffer_load_dwordx4 a[32:35], v250, s[20:23], s66 offen offset:0 sc1
s_mov_b64 exec, -1
v_cmp_eq_u32 vcc, s69, v141
v_cmp_eq_u32 s[36:37], -1, v77
s_add_u32 s66, s44, 4096
s_and_b64 vcc, vcc, s[36:37]
s_cmp_eq_u32 s72, 0
s_cselect_b64 exec, vcc, -1
buffer_load_dwordx4 a[36:39], v250, s[20:23], s66 offen offset:0 sc1
s_mov_b64 exec, -1
s_branch LSN1_%=
LSF1_%=:
s_bitset0_b32 s17, 1
CV v76,v72,v73
CV v77,v74,v75
ds_bpermute_b32 v76, v232, v76
ds_bpermute_b32 v77, v232, v77
v_lshlrev_b32 v10, 16, v2
v_and_b32 v11, 0xffff0000, v2
v_lshlrev_b32 v12, 16, v3
v_and_b32 v13, 0xffff0000, v3
v_lshlrev_b32 v14, 16, v34
v_and_b32 v15, 0xffff0000, v34
v_lshlrev_b32 v42, 16, v35
v_and_b32 v43, 0xffff0000, v35
s_add_u32 s80, s18, 128
s_waitcnt lgkmcnt(0)
v_lshlrev_b32 v72, 16, v76
v_and_b32 v73, 0xffff0000, v76
v_lshlrev_b32 v74, 16, v77
v_and_b32 v75, 0xffff0000, v77
v_mul_f32 v72, v72, v10
v_mul_f32 v73, v73, v11
v_mul_f32 v74, v74, v12
v_mul_f32 v75, v75, v13
CV v76,v72,v73
CV v77,v74,v75
v_lshlrev_b32 v72, 16, v76
v_and_b32 v73, 0xffff0000, v76
v_lshlrev_b32 v74, 16, v77
v_and_b32 v75, 0xffff0000, v77
v_add_f32 v72, v72, v14
v_add_f32 v73, v73, v15
v_add_f32 v74, v74, v42
v_add_f32 v75, v75, v43
CV v46,v72,v73
CV v47,v74,v75
buffer_store_dwordx2 v[46:47], v251, s[40:43], s80 offen offset:0 nt
s_nop 1
LSN1_%=:
s_bitcmp0_b32 s17, 0
s_cbranch_scc1 LSN0_%=
MM v[72:75],v[64:67],a[0:3],0
MM v[72:75],v[64:67],a[4:7],v[72:75]
MM v[72:75],v[68:71],a[8:11],v[72:75]
MM v[72:75],v[68:71],a[12:15],v[72:75]
AR v76,a3
AR v77,a7
AR v78,a11
AR v79,a15
s_nop 3
v_cmp_u_f32 s[30:31], v72, v72
s_nop 1
s_cmp_eq_u64 s[30:31], 0
s_cbranch_scc1 LSF0_%=
s_cmp_ge_u32 s72, 2
s_cbranch_scc1 LSF0_%=
v_cmp_eq_u32 vcc, s69, v240
v_cmp_eq_u32 s[36:37], -1, v78
s_add_u32 s66, s16, 2048
s_and_b64 vcc, vcc, s[36:37]
s_cmp_eq_u32 s72, 0
s_cselect_b64 exec, vcc, -1
s_and_b32 exec_lo, exec_lo, 0xffff0000
buffer_load_dwordx4 a[8:11], v250, s[20:23], s66 offen offset:0 sc1
s_mov_b64 exec, -1
v_cmp_eq_u32 vcc, s69, v140
v_cmp_eq_u32 s[36:37], -1, v79
s_add_u32 s66, s44, 2048
s_and_b64 vcc, vcc, s[36:37]
s_cmp_eq_u32 s72, 0
s_cselect_b64 exec, vcc, -1
buffer_load_dwordx4 a[12:15], v250, s[20:23], s66 offen offset:0 sc1
s_mov_b64 exec, -1
v_cmp_eq_u32 vcc, s69, v240
v_cmp_eq_u32 s[36:37], -1, v76
s_add_u32 s66, s16, 0
s_and_b64 vcc, vcc, s[36:37]
s_cmp_eq_u32 s72, 0
s_cselect_b64 exec, vcc, -1
s_and_b32 exec_lo, exec_lo, 0xffff0000
buffer_load_dwordx4 a[0:3], v250, s[20:23], s66 offen offset:0 sc1
s_mov_b64 exec, -1
v_cmp_eq_u32 vcc, s69, v140
v_cmp_eq_u32 s[36:37], -1, v77
s_add_u32 s66, s44, 0
s_and_b64 vcc, vcc, s[36:37]
s_cmp_eq_u32 s72, 0
s_cselect_b64 exec, vcc, -1
buffer_load_dwordx4 a[4:7], v250, s[20:23], s66 offen offset:0 sc1
s_mov_b64 exec, -1
s_branch LSN0_%=
LSF0_%=:
s_bitset0_b32 s17, 0
CV v76,v72,v73
CV v77,v74,v75
ds_bpermute_b32 v76, v232, v76
ds_bpermute_b32 v77, v232, v77
v_lshlrev_b32 v10, 16, v0
v_and_b32 v11, 0xffff0000, v0
v_lshlrev_b32 v12, 16, v1
v_and_b32 v13, 0xffff0000, v1
v_lshlrev_b32 v14, 16, v32
v_and_b32 v15, 0xffff0000, v32
v_lshlrev_b32 v42, 16, v33
v_and_b32 v43, 0xffff0000, v33
s_add_u32 s80, s18, 0
s_waitcnt lgkmcnt(0)
v_lshlrev_b32 v72, 16, v76
v_and_b32 v73, 0xffff0000, v76
v_lshlrev_b32 v74, 16, v77
v_and_b32 v75, 0xffff0000, v77
v_mul_f32 v72, v72, v10
v_mul_f32 v73, v73, v11
v_mul_f32 v74, v74, v12
v_mul_f32 v75, v75, v13
CV v76,v72,v73
CV v77,v74,v75
v_lshlrev_b32 v72, 16, v76
v_and_b32 v73, 0xffff0000, v76
v_lshlrev_b32 v74, 16, v77
v_and_b32 v75, 0xffff0000, v77
v_add_f32 v72, v72, v14
v_add_f32 v73, v73, v15
v_add_f32 v74, v74, v42
v_add_f32 v75, v75, v43
CV v44,v72,v73
CV v45,v74,v75
buffer_store_dwordx2 v[44:45], v251, s[40:43], s80 offen offset:0 nt
s_nop 1
LSN0_%=:
v_cmp_ne_u32 vcc, s69, v242
v_cmp_ne_u32 s[36:37], s69, v142
s_nop 1
s_or_b64 vcc, vcc, s[36:37]
s_cselect_b32 s76, 0, 15
s_and_b32 s76, s76, s73
s_cbranch_scc1 LSU_%=
buffer_load_dwordx4 v[240:243], v253, s[20:23], s68 offen offset:0 sc1
buffer_load_dwordx4 v[140:143], v253, s[20:23], s45 offen offset:0 sc1
LSU_%=:
s_or_b32 s76, s17, s65
s_cbranch_scc1 LSW_%=
s_branch LQE_%=
LKEND_ka:
LSITE0_%=:
s_mov_b32 s73, 0
s_mov_b32 s75, 16384
s_mov_b32 s74, 0
s_branch LRARE002_%=
LSITE2_%=:
s_mov_b32 s73, 2
s_mov_b32 s75, 49152
s_mov_b32 s74, 32768
s_branch LRARE002_%=
LRARE002_%=:
s_nop 15
s_waitcnt vmcnt(0) lgkmcnt(0)
v_add_u32 v249, s74, v224
RD a[192:195],v249 offset:0
RD a[208:211],v249 offset:4096
RD a[224:227],v249 offset:8192
RD a[240:243],v249 offset:12288
v_add_u32 v250, s74, v225
RD a[196:199],v250 offset:0
RD a[212:215],v250 offset:4096
RD a[228:231],v250 offset:8192
RD a[244:247],v250 offset:12288
v_add_u32 v251, s74, v226
)ASM"
R"ASM(
RD a[200:203],v251 offset:0
RD a[216:219],v251 offset:4096
RD a[232:235],v251 offset:8192
RD a[248:251],v251 offset:12288
v_add_u32 v254, s74, v227
RD a[204:207],v254 offset:0
RD a[220:223],v254 offset:4096
RD a[236:239],v254 offset:8192
RD a[252:255],v254 offset:12288
s_waitcnt lgkmcnt(0)
MM v[0:3],a[192:195],v[160:163],v[128:131]
MM v[0:3],a[196:199],v[176:179],v[0:3]
MM v[0:3],a[200:203],v[192:195],v[0:3]
MM v[0:3],a[204:207],v[208:211],v[0:3]
MM v[4:7],a[220:223],v[208:211],v[128:131]
MM v[4:7],a[216:219],v[192:195],v[4:7]
MM v[4:7],a[212:215],v[176:179],v[4:7]
MM v[4:7],a[208:211],v[160:163],v[4:7]
MM v[12:15],a[208:211],v[164:167],v[132:135]
MM v[12:15],a[212:215],v[180:183],v[12:15]
MM v[12:15],a[216:219],v[196:199],v[12:15]
MM v[12:15],a[220:223],v[212:215],v[12:15]
MM v[8:11],a[204:207],v[212:215],v[132:135]
MM v[8:11],a[200:203],v[196:199],v[8:11]
MM v[8:11],a[196:199],v[180:183],v[8:11]
MM v[8:11],a[192:195],v[164:167],v[8:11]
MM v[32:35],a[224:227],v[160:163],v[128:131]
MM v[32:35],a[228:231],v[176:179],v[32:35]
MM v[32:35],a[232:235],v[192:195],v[32:35]
MM v[32:35],a[236:239],v[208:211],v[32:35]
MM v[36:39],a[252:255],v[208:211],v[128:131]
MM v[36:39],a[248:251],v[192:195],v[36:39]
MM v[36:39],a[244:247],v[176:179],v[36:39]
MM v[36:39],a[240:243],v[160:163],v[36:39]
MM v[44:47],a[240:243],v[164:167],v[132:135]
MM v[44:47],a[244:247],v[180:183],v[44:47]
MM v[44:47],a[248:251],v[196:199],v[44:47]
MM v[44:47],a[252:255],v[212:215],v[44:47]
MM v[40:43],a[236:239],v[212:215],v[132:135]
MM v[40:43],a[232:235],v[196:199],v[40:43]
MM v[40:43],a[228:231],v[180:183],v[40:43]
MM v[40:43],a[224:227],v[164:167],v[40:43]
s_nop 15
v_add_u32 v249, s75, v224
RD a[192:195],v249 offset:0
RD a[208:211],v249 offset:4096
RD a[224:227],v249 offset:8192
RD a[240:243],v249 offset:12288
v_add_u32 v250, s75, v225
RD a[196:199],v250 offset:0
RD a[212:215],v250 offset:4096
RD a[228:231],v250 offset:8192
RD a[244:247],v250 offset:12288
v_add_u32 v251, s75, v226
RD a[200:203],v251 offset:0
RD a[216:219],v251 offset:4096
RD a[232:235],v251 offset:8192
RD a[248:251],v251 offset:12288
v_add_u32 v254, s75, v227
RD a[204:207],v254 offset:0
RD a[220:223],v254 offset:4096
RD a[236:239],v254 offset:8192
RD a[252:255],v254 offset:12288
s_waitcnt lgkmcnt(0)
v_mov_b32 v236, v247
v_mov_b32 v237, 0
v_max3_f32 v249, v0, v1, v2
v_max3_f32 v251, v32, v33, v34
v_max3_f32 v249, v249, v3, v4
v_max3_f32 v251, v251, v35, v36
v_max3_f32 v249, v249, v5, v6
v_max3_f32 v251, v251, v37, v38
v_max3_f32 v249, v249, v7, v39
v_max_f32 v249, v249, v251
v_log_f32 v250, v247
s_lshl_b64 exec, 0xffff, 0
v_max_f32 v249, v249, v250
s_mov_b64 exec, -1
v_mov_b32 v251, v249
s_nop 1
v_permlane16_swap_b32 v249, v251
v_max_f32 v249, v249, v251
v_mov_b32 v251, v249
s_nop 1
v_permlane32_swap_b32 v249, v251
v_max_f32 v249, v249, v251
v_max_f32 v249, 0, v249
v_sub_f32 v250, 0, v249
EX v250,v250
v_sub_f32 v128, v128, v249
v_sub_f32 v129, v129, v249
v_sub_f32 v130, v130, v249
v_sub_f32 v131, v131, v249
v_sub_f32 v64, v64, v249
v_sub_f32 v0, v0, v249
v_sub_f32 v65, v65, v249
v_sub_f32 v1, v1, v249
v_sub_f32 v66, v66, v249
v_sub_f32 v2, v2, v249
v_sub_f32 v67, v67, v249
v_sub_f32 v3, v3, v249
v_sub_f32 v68, v68, v249
v_sub_f32 v4, v4, v249
v_sub_f32 v69, v69, v249
v_sub_f32 v5, v5, v249
v_sub_f32 v70, v70, v249
v_sub_f32 v6, v6, v249
v_sub_f32 v71, v71, v249
v_sub_f32 v7, v7, v249
v_sub_f32 v96, v96, v249
v_sub_f32 v32, v32, v249
v_sub_f32 v97, v97, v249
v_sub_f32 v33, v33, v249
v_sub_f32 v98, v98, v249
v_sub_f32 v34, v34, v249
v_sub_f32 v99, v99, v249
v_sub_f32 v35, v35, v249
v_sub_f32 v100, v100, v249
v_sub_f32 v36, v36, v249
v_sub_f32 v101, v101, v249
v_sub_f32 v37, v37, v249
v_sub_f32 v102, v102, v249
v_sub_f32 v38, v38, v249
v_sub_f32 v103, v103, v249
v_sub_f32 v39, v39, v249
s_lshl_b64 exec, 0xffff, 0
v_mul_f32 v236, v236, v250
s_mov_b64 exec, -1
AR v254,a0
v_mul_f32 v254, v254, v250
AW a0,v254
AR v251,a1
v_mul_f32 v251, v251, v250
AW a1,v251
AR v254,a2
v_mul_f32 v254, v254, v250
AW a2,v254
AR v251,a3
v_mul_f32 v251, v251, v250
AW a3,v251
AR v254,a4
v_mul_f32 v254, v254, v250
AW a4,v254
AR v251,a5
v_mul_f32 v251, v251, v250
AW a5,v251
AR v254,a6
v_mul_f32 v254, v254, v250
AW a6,v254
AR v251,a7
v_mul_f32 v251, v251, v250
AW a7,v251
AR v254,a32
v_mul_f32 v254, v254, v250
AW a32,v254
AR v251,a33
v_mul_f32 v251, v251, v250
AW a33,v251
AR v254,a34
v_mul_f32 v254, v254, v250
AW a34,v254
AR v251,a35
v_mul_f32 v251, v251, v250
AW a35,v251
AR v254,a36
v_mul_f32 v254, v254, v250
AW a36,v254
AR v251,a37
v_mul_f32 v251, v251, v250
AW a37,v251
AR v254,a38
v_mul_f32 v254, v254, v250
AW a38,v254
AR v251,a39
v_mul_f32 v251, v251, v250
AW a39,v251
AR v254,a64
v_mul_f32 v254, v254, v250
AW a64,v254
AR v251,a65
v_mul_f32 v251, v251, v250
AW a65,v251
AR v254,a66
v_mul_f32 v254, v254, v250
AW a66,v254
AR v251,a67
v_mul_f32 v251, v251, v250
AW a67,v251
AR v254,a68
v_mul_f32 v254, v254, v250
AW a68,v254
AR v251,a69
v_mul_f32 v251, v251, v250
AW a69,v251
AR v254,a70
v_mul_f32 v254, v254, v250
AW a70,v254
AR v251,a71
v_mul_f32 v251, v251, v250
AW a71,v251
AR v254,a96
v_mul_f32 v254, v254, v250
AW a96,v254
AR v251,a97
v_mul_f32 v251, v251, v250
AW a97,v251
AR v254,a98
v_mul_f32 v254, v254, v250
AW a98,v254
AR v251,a99
v_mul_f32 v251, v251, v250
AW a99,v251
AR v254,a100
v_mul_f32 v254, v254, v250
AW a100,v254
AR v251,a101
v_mul_f32 v251, v251, v250
AW a101,v251
AR v254,a102
v_mul_f32 v254, v254, v250
AW a102,v254
AR v251,a103
v_mul_f32 v251, v251, v250
AW a103,v251
v_max3_f32 v249, v8, v9, v10
v_max3_f32 v251, v40, v41, v42
v_max3_f32 v249, v249, v11, v12
v_max3_f32 v251, v251, v43, v44
v_max3_f32 v249, v249, v13, v14
v_max3_f32 v251, v251, v45, v46
v_max3_f32 v249, v249, v15, v47
v_max_f32 v249, v249, v251
v_log_f32 v250, v247
s_lshl_b64 exec, 0xffff, 16
v_max_f32 v249, v249, v250
s_mov_b64 exec, -1
v_mov_b32 v251, v249
s_nop 1
v_permlane16_swap_b32 v249, v251
v_max_f32 v249, v249, v251
v_mov_b32 v251, v249
s_nop 1
v_permlane32_swap_b32 v249, v251
v_max_f32 v249, v249, v251
v_max_f32 v249, 0, v249
v_sub_f32 v250, 0, v249
EX v250,v250
v_sub_f32 v132, v132, v249
v_sub_f32 v133, v133, v249
v_sub_f32 v134, v134, v249
v_sub_f32 v135, v135, v249
v_sub_f32 v72, v72, v249
v_sub_f32 v8, v8, v249
v_sub_f32 v73, v73, v249
v_sub_f32 v9, v9, v249
v_sub_f32 v74, v74, v249
v_sub_f32 v10, v10, v249
v_sub_f32 v75, v75, v249
v_sub_f32 v11, v11, v249
v_sub_f32 v76, v76, v249
v_sub_f32 v12, v12, v249
v_sub_f32 v77, v77, v249
v_sub_f32 v13, v13, v249
v_sub_f32 v78, v78, v249
v_sub_f32 v14, v14, v249
v_sub_f32 v79, v79, v249
v_sub_f32 v15, v15, v249
v_sub_f32 v104, v104, v249
v_sub_f32 v40, v40, v249
v_sub_f32 v105, v105, v249
v_sub_f32 v41, v41, v249
v_sub_f32 v106, v106, v249
v_sub_f32 v42, v42, v249
v_sub_f32 v107, v107, v249
v_sub_f32 v43, v43, v249
v_sub_f32 v108, v108, v249
v_sub_f32 v44, v44, v249
v_sub_f32 v109, v109, v249
v_sub_f32 v45, v45, v249
v_sub_f32 v110, v110, v249
v_sub_f32 v46, v46, v249
v_sub_f32 v111, v111, v249
v_sub_f32 v47, v47, v249
s_lshl_b64 exec, 0xffff, 16
v_mul_f32 v236, v236, v250
s_mov_b64 exec, -1
AR v254,a8
v_mul_f32 v254, v254, v250
AW a8,v254
AR v251,a9
v_mul_f32 v251, v251, v250
AW a9,v251
AR v254,a10
v_mul_f32 v254, v254, v250
AW a10,v254
AR v251,a11
v_mul_f32 v251, v251, v250
AW a11,v251
AR v254,a12
v_mul_f32 v254, v254, v250
AW a12,v254
AR v251,a13
v_mul_f32 v251, v251, v250
AW a13,v251
AR v254,a14
v_mul_f32 v254, v254, v250
AW a14,v254
AR v251,a15
v_mul_f32 v251, v251, v250
AW a15,v251
AR v254,a40
v_mul_f32 v254, v254, v250
AW a40,v254
AR v251,a41
v_mul_f32 v251, v251, v250
AW a41,v251
AR v254,a42
v_mul_f32 v254, v254, v250
AW a42,v254
AR v251,a43
v_mul_f32 v251, v251, v250
AW a43,v251
AR v254,a44
v_mul_f32 v254, v254, v250
AW a44,v254
AR v251,a45
v_mul_f32 v251, v251, v250
AW a45,v251
AR v254,a46
v_mul_f32 v254, v254, v250
AW a46,v254
AR v251,a47
v_mul_f32 v251, v251, v250
AW a47,v251
AR v254,a72
v_mul_f32 v254, v254, v250
AW a72,v254
AR v251,a73
v_mul_f32 v251, v251, v250
AW a73,v251
AR v254,a74
v_mul_f32 v254, v254, v250
AW a74,v254
AR v251,a75
v_mul_f32 v251, v251, v250
AW a75,v251
AR v254,a76
v_mul_f32 v254, v254, v250
AW a76,v254
AR v251,a77
v_mul_f32 v251, v251, v250
AW a77,v251
AR v254,a78
v_mul_f32 v254, v254, v250
AW a78,v254
AR v251,a79
v_mul_f32 v251, v251, v250
AW a79,v251
AR v254,a104
v_mul_f32 v254, v254, v250
AW a104,v254
AR v251,a105
v_mul_f32 v251, v251, v250
AW a105,v251
AR v254,a106
v_mul_f32 v254, v254, v250
AW a106,v254
AR v251,a107
v_mul_f32 v251, v251, v250
AW a107,v251
AR v254,a108
v_mul_f32 v254, v254, v250
AW a108,v254
AR v251,a109
v_mul_f32 v251, v251, v250
AW a109,v251
AR v254,a110
v_mul_f32 v254, v254, v250
AW a110,v254
AR v251,a111
v_mul_f32 v251, v251, v250
AW a111,v251
EX v0,v0
EX v1,v1
EX v2,v2
EX v3,v3
EX v4,v4
EX v5,v5
EX v6,v6
EX v7,v7
EX v8,v8
EX v9,v9
EX v10,v10
EX v11,v11
EX v12,v12
EX v13,v13
EX v14,v14
EX v15,v15
EX v32,v32
EX v33,v33
EX v34,v34
EX v35,v35
EX v36,v36
EX v37,v37
EX v38,v38
EX v39,v39
EX v40,v40
EX v41,v41
EX v42,v42
EX v43,v43
EX v44,v44
EX v45,v45
EX v46,v46
EX v47,v47
CV v0,v0,v1
CV v1,v2,v3
CV v2,v4,v5
CV v3,v6,v7
CV v8,v8,v9
CV v9,v10,v11
CV v10,v12,v13
CV v11,v14,v15
CV v4,v32,v33
CV v5,v34,v35
CV v6,v36,v37
CV v7,v38,v39
CV v12,v40,v41
CV v13,v42,v43
CV v14,v44,v45
CV v15,v46,v47
v_smfmac_f32_16x16x64_bf16 v[236:239], v[144:147], v[0:7], v234
s_nop 0
v_smfmac_f32_16x16x64_bf16 v[236:239], v[148:151], v[8:15], v234
s_nop 9
v_add_f32 v246, v236, v237
s_cmp_eq_u32 s73, 0
s_cbranch_scc1 LBACK0_%=
s_cmp_eq_u32 s73, 2
s_cbranch_scc1 LBACK2_%=
s_endpgm
LSITE1_%=:
s_mov_b32 s73, 1
s_mov_b32 s75, 32768
s_mov_b32 s74, 16384
s_branch LRARE102_%=
LRARE102_%=:
s_nop 15
s_waitcnt vmcnt(0) lgkmcnt(0)
v_add_u32 v249, s74, v224
RD a[192:195],v249 offset:0
RD a[208:211],v249 offset:4096
RD a[224:227],v249 offset:8192
RD a[240:243],v249 offset:12288
v_add_u32 v250, s74, v225
RD a[196:199],v250 offset:0
RD a[212:215],v250 offset:4096
RD a[228:231],v250 offset:8192
RD a[244:247],v250 offset:12288
v_add_u32 v251, s74, v226
RD a[200:203],v251 offset:0
RD a[216:219],v251 offset:4096
RD a[232:235],v251 offset:8192
RD a[248:251],v251 offset:12288
v_add_u32 v254, s74, v227
RD a[204:207],v254 offset:0
RD a[220:223],v254 offset:4096
RD a[236:239],v254 offset:8192
RD a[252:255],v254 offset:12288
s_waitcnt lgkmcnt(0)
MM v[64:67],a[192:195],v[160:163],v[128:131]
MM v[64:67],a[196:199],v[176:179],v[64:67]
MM v[64:67],a[200:203],v[192:195],v[64:67]
MM v[64:67],a[204:207],v[208:211],v[64:67]
MM v[68:71],a[220:223],v[208:211],v[128:131]
MM v[68:71],a[216:219],v[192:195],v[68:71]
MM v[68:71],a[212:215],v[176:179],v[68:71]
MM v[68:71],a[208:211],v[160:163],v[68:71]
MM v[76:79],a[208:211],v[164:167],v[132:135]
MM v[76:79],a[212:215],v[180:183],v[76:79]
MM v[76:79],a[216:219],v[196:199],v[76:79]
MM v[76:79],a[220:223],v[212:215],v[76:79]
MM v[72:75],a[204:207],v[212:215],v[132:135]
MM v[72:75],a[200:203],v[196:199],v[72:75]
MM v[72:75],a[196:199],v[180:183],v[72:75]
MM v[72:75],a[192:195],v[164:167],v[72:75]
MM v[96:99],a[224:227],v[160:163],v[128:131]
MM v[96:99],a[228:231],v[176:179],v[96:99]
MM v[96:99],a[232:235],v[192:195],v[96:99]
MM v[96:99],a[236:239],v[208:211],v[96:99]
MM v[100:103],a[252:255],v[208:211],v[128:131]
MM v[100:103],a[248:251],v[192:195],v[100:103]
MM v[100:103],a[244:247],v[176:179],v[100:103]
MM v[100:103],a[240:243],v[160:163],v[100:103]
MM v[108:111],a[240:243],v[164:167],v[132:135]
MM v[108:111],a[244:247],v[180:183],v[108:111]
MM v[108:111],a[248:251],v[196:199],v[108:111]
MM v[108:111],a[252:255],v[212:215],v[108:111]
MM v[104:107],a[236:239],v[212:215],v[132:135]
MM v[104:107],a[232:235],v[196:199],v[104:107]
MM v[104:107],a[228:231],v[180:183],v[104:107]
MM v[104:107],a[224:227],v[164:167],v[104:107]
s_nop 15
v_add_u32 v249, s75, v224
RD a[192:195],v249 offset:0
RD a[208:211],v249 offset:4096
RD a[224:227],v249 offset:8192
RD a[240:243],v249 offset:12288
v_add_u32 v250, s75, v225
RD a[196:199],v250 offset:0
RD a[212:215],v250 offset:4096
RD a[228:231],v250 offset:8192
RD a[244:247],v250 offset:12288
)ASM"
R"ASM(
v_add_u32 v251, s75, v226
RD a[200:203],v251 offset:0
RD a[216:219],v251 offset:4096
RD a[232:235],v251 offset:8192
RD a[248:251],v251 offset:12288
v_add_u32 v254, s75, v227
RD a[204:207],v254 offset:0
RD a[220:223],v254 offset:4096
RD a[236:239],v254 offset:8192
RD a[252:255],v254 offset:12288
s_waitcnt lgkmcnt(0)
v_mov_b32 v236, v246
v_mov_b32 v237, 0
v_max3_f32 v249, v64, v65, v66
v_max3_f32 v251, v96, v97, v98
v_max3_f32 v249, v249, v67, v68
v_max3_f32 v251, v251, v99, v100
v_max3_f32 v249, v249, v69, v70
v_max3_f32 v251, v251, v101, v102
v_max3_f32 v249, v249, v71, v103
v_max_f32 v249, v249, v251
v_log_f32 v250, v246
s_lshl_b64 exec, 0xffff, 0
v_max_f32 v249, v249, v250
s_mov_b64 exec, -1
v_mov_b32 v251, v249
s_nop 1
v_permlane16_swap_b32 v249, v251
v_max_f32 v249, v249, v251
v_mov_b32 v251, v249
s_nop 1
v_permlane32_swap_b32 v249, v251
v_max_f32 v249, v249, v251
v_max_f32 v249, 0, v249
v_sub_f32 v250, 0, v249
EX v250,v250
v_sub_f32 v128, v128, v249
v_sub_f32 v129, v129, v249
v_sub_f32 v130, v130, v249
v_sub_f32 v131, v131, v249
v_sub_f32 v0, v0, v249
v_sub_f32 v64, v64, v249
v_sub_f32 v1, v1, v249
v_sub_f32 v65, v65, v249
v_sub_f32 v2, v2, v249
v_sub_f32 v66, v66, v249
v_sub_f32 v3, v3, v249
v_sub_f32 v67, v67, v249
v_sub_f32 v4, v4, v249
v_sub_f32 v68, v68, v249
v_sub_f32 v5, v5, v249
v_sub_f32 v69, v69, v249
v_sub_f32 v6, v6, v249
v_sub_f32 v70, v70, v249
v_sub_f32 v7, v7, v249
v_sub_f32 v71, v71, v249
v_sub_f32 v32, v32, v249
v_sub_f32 v96, v96, v249
v_sub_f32 v33, v33, v249
v_sub_f32 v97, v97, v249
v_sub_f32 v34, v34, v249
v_sub_f32 v98, v98, v249
v_sub_f32 v35, v35, v249
v_sub_f32 v99, v99, v249
v_sub_f32 v36, v36, v249
v_sub_f32 v100, v100, v249
v_sub_f32 v37, v37, v249
v_sub_f32 v101, v101, v249
v_sub_f32 v38, v38, v249
v_sub_f32 v102, v102, v249
v_sub_f32 v39, v39, v249
v_sub_f32 v103, v103, v249
s_lshl_b64 exec, 0xffff, 0
v_mul_f32 v236, v236, v250
s_mov_b64 exec, -1
AR v254,a0
v_mul_f32 v254, v254, v250
AW a0,v254
AR v251,a1
v_mul_f32 v251, v251, v250
AW a1,v251
AR v254,a2
v_mul_f32 v254, v254, v250
AW a2,v254
AR v251,a3
v_mul_f32 v251, v251, v250
AW a3,v251
AR v254,a4
v_mul_f32 v254, v254, v250
AW a4,v254
AR v251,a5
v_mul_f32 v251, v251, v250
AW a5,v251
AR v254,a6
v_mul_f32 v254, v254, v250
AW a6,v254
AR v251,a7
v_mul_f32 v251, v251, v250
AW a7,v251
AR v254,a32
v_mul_f32 v254, v254, v250
AW a32,v254
AR v251,a33
v_mul_f32 v251, v251, v250
AW a33,v251
AR v254,a34
v_mul_f32 v254, v254, v250
AW a34,v254
AR v251,a35
v_mul_f32 v251, v251, v250
AW a35,v251
AR v254,a36
v_mul_f32 v254, v254, v250
AW a36,v254
AR v251,a37
v_mul_f32 v251, v251, v250
AW a37,v251
AR v254,a38
v_mul_f32 v254, v254, v250
AW a38,v254
AR v251,a39
v_mul_f32 v251, v251, v250
AW a39,v251
AR v254,a64
v_mul_f32 v254, v254, v250
AW a64,v254
AR v251,a65
v_mul_f32 v251, v251, v250
AW a65,v251
AR v254,a66
v_mul_f32 v254, v254, v250
AW a66,v254
AR v251,a67
v_mul_f32 v251, v251, v250
AW a67,v251
AR v254,a68
v_mul_f32 v254, v254, v250
AW a68,v254
AR v251,a69
v_mul_f32 v251, v251, v250
AW a69,v251
AR v254,a70
v_mul_f32 v254, v254, v250
AW a70,v254
AR v251,a71
v_mul_f32 v251, v251, v250
AW a71,v251
AR v254,a96
v_mul_f32 v254, v254, v250
AW a96,v254
AR v251,a97
v_mul_f32 v251, v251, v250
AW a97,v251
AR v254,a98
v_mul_f32 v254, v254, v250
AW a98,v254
AR v251,a99
v_mul_f32 v251, v251, v250
AW a99,v251
AR v254,a100
v_mul_f32 v254, v254, v250
AW a100,v254
AR v251,a101
v_mul_f32 v251, v251, v250
AW a101,v251
AR v254,a102
v_mul_f32 v254, v254, v250
AW a102,v254
AR v251,a103
v_mul_f32 v251, v251, v250
AW a103,v251
v_max3_f32 v249, v72, v73, v74
v_max3_f32 v251, v104, v105, v106
v_max3_f32 v249, v249, v75, v76
v_max3_f32 v251, v251, v107, v108
v_max3_f32 v249, v249, v77, v78
v_max3_f32 v251, v251, v109, v110
v_max3_f32 v249, v249, v79, v111
v_max_f32 v249, v249, v251
v_log_f32 v250, v246
s_lshl_b64 exec, 0xffff, 16
v_max_f32 v249, v249, v250
s_mov_b64 exec, -1
v_mov_b32 v251, v249
s_nop 1
v_permlane16_swap_b32 v249, v251
v_max_f32 v249, v249, v251
v_mov_b32 v251, v249
s_nop 1
v_permlane32_swap_b32 v249, v251
v_max_f32 v249, v249, v251
v_max_f32 v249, 0, v249
v_sub_f32 v250, 0, v249
EX v250,v250
v_sub_f32 v132, v132, v249
v_sub_f32 v133, v133, v249
v_sub_f32 v134, v134, v249
v_sub_f32 v135, v135, v249
v_sub_f32 v8, v8, v249
v_sub_f32 v72, v72, v249
v_sub_f32 v9, v9, v249
v_sub_f32 v73, v73, v249
v_sub_f32 v10, v10, v249
v_sub_f32 v74, v74, v249
v_sub_f32 v11, v11, v249
v_sub_f32 v75, v75, v249
v_sub_f32 v12, v12, v249
v_sub_f32 v76, v76, v249
v_sub_f32 v13, v13, v249
v_sub_f32 v77, v77, v249
v_sub_f32 v14, v14, v249
v_sub_f32 v78, v78, v249
v_sub_f32 v15, v15, v249
v_sub_f32 v79, v79, v249
v_sub_f32 v40, v40, v249
v_sub_f32 v104, v104, v249
v_sub_f32 v41, v41, v249
v_sub_f32 v105, v105, v249
v_sub_f32 v42, v42, v249
v_sub_f32 v106, v106, v249
v_sub_f32 v43, v43, v249
v_sub_f32 v107, v107, v249
v_sub_f32 v44, v44, v249
v_sub_f32 v108, v108, v249
v_sub_f32 v45, v45, v249
v_sub_f32 v109, v109, v249
v_sub_f32 v46, v46, v249
v_sub_f32 v110, v110, v249
v_sub_f32 v47, v47, v249
v_sub_f32 v111, v111, v249
s_lshl_b64 exec, 0xffff, 16
v_mul_f32 v236, v236, v250
s_mov_b64 exec, -1
AR v254,a8
v_mul_f32 v254, v254, v250
AW a8,v254
AR v251,a9
v_mul_f32 v251, v251, v250
AW a9,v251
AR v254,a10
v_mul_f32 v254, v254, v250
AW a10,v254
AR v251,a11
v_mul_f32 v251, v251, v250
AW a11,v251
AR v254,a12
v_mul_f32 v254, v254, v250
AW a12,v254
AR v251,a13
v_mul_f32 v251, v251, v250
AW a13,v251
AR v254,a14
v_mul_f32 v254, v254, v250
AW a14,v254
AR v251,a15
v_mul_f32 v251, v251, v250
AW a15,v251
AR v254,a40
v_mul_f32 v254, v254, v250
AW a40,v254
AR v251,a41
v_mul_f32 v251, v251, v250
AW a41,v251
AR v254,a42
v_mul_f32 v254, v254, v250
AW a42,v254
AR v251,a43
v_mul_f32 v251, v251, v250
AW a43,v251
AR v254,a44
v_mul_f32 v254, v254, v250
AW a44,v254
AR v251,a45
v_mul_f32 v251, v251, v250
AW a45,v251
AR v254,a46
v_mul_f32 v254, v254, v250
AW a46,v254
AR v251,a47
v_mul_f32 v251, v251, v250
AW a47,v251
AR v254,a72
v_mul_f32 v254, v254, v250
AW a72,v254
AR v251,a73
v_mul_f32 v251, v251, v250
AW a73,v251
AR v254,a74
v_mul_f32 v254, v254, v250
AW a74,v254
AR v251,a75
v_mul_f32 v251, v251, v250
AW a75,v251
AR v254,a76
v_mul_f32 v254, v254, v250
AW a76,v254
AR v251,a77
v_mul_f32 v251, v251, v250
AW a77,v251
AR v254,a78
v_mul_f32 v254, v254, v250
AW a78,v254
AR v251,a79
v_mul_f32 v251, v251, v250
AW a79,v251
AR v254,a104
v_mul_f32 v254, v254, v250
AW a104,v254
AR v251,a105
v_mul_f32 v251, v251, v250
AW a105,v251
AR v254,a106
v_mul_f32 v254, v254, v250
AW a106,v254
AR v251,a107
v_mul_f32 v251, v251, v250
AW a107,v251
AR v254,a108
v_mul_f32 v254, v254, v250
AW a108,v254
AR v251,a109
v_mul_f32 v251, v251, v250
AW a109,v251
AR v254,a110
v_mul_f32 v254, v254, v250
AW a110,v254
AR v251,a111
v_mul_f32 v251, v251, v250
AW a111,v251
EX v64,v64
EX v65,v65
EX v66,v66
EX v67,v67
EX v68,v68
EX v69,v69
EX v70,v70
EX v71,v71
EX v72,v72
EX v73,v73
EX v74,v74
EX v75,v75
EX v76,v76
EX v77,v77
EX v78,v78
EX v79,v79
EX v96,v96
EX v97,v97
EX v98,v98
EX v99,v99
EX v100,v100
EX v101,v101
EX v102,v102
EX v103,v103
EX v104,v104
EX v105,v105
EX v106,v106
EX v107,v107
EX v108,v108
EX v109,v109
EX v110,v110
EX v111,v111
CV v64,v64,v65
CV v65,v66,v67
CV v66,v68,v69
CV v67,v70,v71
CV v72,v72,v73
CV v73,v74,v75
CV v74,v76,v77
CV v75,v78,v79
CV v68,v96,v97
CV v69,v98,v99
CV v70,v100,v101
CV v71,v102,v103
CV v76,v104,v105
CV v77,v106,v107
CV v78,v108,v109
CV v79,v110,v111
v_smfmac_f32_16x16x64_bf16 v[236:239], v[144:147], v[64:71], v234
s_nop 0
v_smfmac_f32_16x16x64_bf16 v[236:239], v[148:151], v[72:79], v234
s_nop 9
v_add_f32 v247, v236, v237
s_cmp_eq_u32 s73, 1
s_cbranch_scc1 LBACK1_%=
s_endpgm
LSITE3_%=:
s_mov_b32 s73, 3
s_mov_b32 s75, 0
s_mov_b32 s74, 49152
s_branch LRARE104_%=
LSITE5_%=:
s_mov_b32 s73, 5
s_mov_b32 s75, 32768
s_mov_b32 s74, 16384
s_branch LRARE104_%=
LSITE7_%=:
s_mov_b32 s73, 7
s_mov_b32 s75, 0
s_mov_b32 s74, 49152
s_branch LRARE104_%=
LSITE9_%=:
s_mov_b32 s73, 9
s_mov_b32 s75, 32768
s_mov_b32 s74, 16384
s_branch LRARE104_%=
LSITE11_%=:
s_mov_b32 s73, 11
s_mov_b32 s75, 0
s_mov_b32 s74, 49152
s_branch LRARE104_%=
LSITE13_%=:
s_mov_b32 s73, 13
s_mov_b32 s75, 32768
s_mov_b32 s74, 16384
s_branch LRARE104_%=
LSITE15_%=:
s_mov_b32 s73, 15
s_mov_b32 s75, 0
s_mov_b32 s74, 49152
s_branch LRARE104_%=
LSITE17_%=:
s_mov_b32 s73, 17
s_mov_b32 s75, 32768
s_mov_b32 s74, 16384
s_branch LRARE104_%=
LSITE19_%=:
s_mov_b32 s73, 19
s_mov_b32 s75, 0
s_mov_b32 s74, 49152
s_branch LRARE104_%=
LSITE21_%=:
s_mov_b32 s73, 21
s_mov_b32 s75, 32768
s_mov_b32 s74, 16384
s_branch LRARE104_%=
LSITE23_%=:
s_mov_b32 s73, 23
s_mov_b32 s75, 0
s_mov_b32 s74, 49152
s_branch LRARE104_%=
LSITE25_%=:
s_mov_b32 s73, 25
s_mov_b32 s75, 32768
s_mov_b32 s74, 16384
s_branch LRARE104_%=
LSITE27_%=:
s_mov_b32 s73, 27
s_mov_b32 s75, 0
s_mov_b32 s74, 49152
s_branch LRARE104_%=
LSITE29_%=:
s_mov_b32 s73, 29
s_mov_b32 s75, 32768
s_mov_b32 s74, 16384
s_branch LRARE104_%=
LSITE31_%=:
s_mov_b32 s73, 31
s_mov_b32 s75, 0
s_mov_b32 s74, 49152
s_branch LRARE104_%=
LRARE104_%=:
s_nop 15
s_waitcnt vmcnt(0) lgkmcnt(0)
v_add_u32 v249, s74, v224
RD a[192:195],v249 offset:0
RD a[208:211],v249 offset:4096
RD a[224:227],v249 offset:8192
RD a[240:243],v249 offset:12288
v_add_u32 v250, s74, v225
RD a[196:199],v250 offset:0
RD a[212:215],v250 offset:4096
RD a[228:231],v250 offset:8192
RD a[244:247],v250 offset:12288
v_add_u32 v251, s74, v226
RD a[200:203],v251 offset:0
RD a[216:219],v251 offset:4096
RD a[232:235],v251 offset:8192
RD a[248:251],v251 offset:12288
v_add_u32 v254, s74, v227
RD a[204:207],v254 offset:0
RD a[220:223],v254 offset:4096
RD a[236:239],v254 offset:8192
RD a[252:255],v254 offset:12288
s_waitcnt lgkmcnt(0)
MM v[64:67],a[192:195],v[160:163],v[128:131]
MM v[64:67],a[196:199],v[176:179],v[64:67]
MM v[64:67],a[200:203],v[192:195],v[64:67]
MM v[64:67],a[204:207],v[208:211],v[64:67]
MM v[68:71],a[220:223],v[208:211],v[128:131]
MM v[68:71],a[216:219],v[192:195],v[68:71]
MM v[68:71],a[212:215],v[176:179],v[68:71]
MM v[68:71],a[208:211],v[160:163],v[68:71]
MM v[76:79],a[208:211],v[164:167],v[132:135]
MM v[76:79],a[212:215],v[180:183],v[76:79]
MM v[76:79],a[216:219],v[196:199],v[76:79]
MM v[76:79],a[220:223],v[212:215],v[76:79]
MM v[72:75],a[204:207],v[212:215],v[132:135]
MM v[72:75],a[200:203],v[196:199],v[72:75]
MM v[72:75],a[196:199],v[180:183],v[72:75]
MM v[72:75],a[192:195],v[164:167],v[72:75]
MM v[80:83],a[192:195],v[168:171],v[136:139]
MM v[80:83],a[196:199],v[184:187],v[80:83]
MM v[80:83],a[200:203],v[200:203],v[80:83]
MM v[80:83],a[204:207],v[216:219],v[80:83]
MM v[84:87],a[220:223],v[216:219],v[136:139]
MM v[84:87],a[216:219],v[200:203],v[84:87]
MM v[84:87],a[212:215],v[184:187],v[84:87]
MM v[84:87],a[208:211],v[168:171],v[84:87]
MM v[92:95],a[208:211],v[172:175],v[140:143]
MM v[92:95],a[212:215],v[188:191],v[92:95]
MM v[92:95],a[216:219],v[204:207],v[92:95]
MM v[92:95],a[220:223],v[220:223],v[92:95]
MM v[88:91],a[204:207],v[220:223],v[140:143]
MM v[88:91],a[200:203],v[204:207],v[88:91]
MM v[88:91],a[196:199],v[188:191],v[88:91]
MM v[88:91],a[192:195],v[172:175],v[88:91]
MM v[96:99],a[224:227],v[160:163],v[128:131]
MM v[96:99],a[228:231],v[176:179],v[96:99]
MM v[96:99],a[232:235],v[192:195],v[96:99]
MM v[96:99],a[236:239],v[208:211],v[96:99]
MM v[100:103],a[252:255],v[208:211],v[128:131]
MM v[100:103],a[248:251],v[192:195],v[100:103]
MM v[100:103],a[244:247],v[176:179],v[100:103]
MM v[100:103],a[240:243],v[160:163],v[100:103]
MM v[108:111],a[240:243],v[164:167],v[132:135]
MM v[108:111],a[244:247],v[180:183],v[108:111]
MM v[108:111],a[248:251],v[196:199],v[108:111]
MM v[108:111],a[252:255],v[212:215],v[108:111]
MM v[104:107],a[236:239],v[212:215],v[132:135]
MM v[104:107],a[232:235],v[196:199],v[104:107]
MM v[104:107],a[228:231],v[180:183],v[104:107]
MM v[104:107],a[224:227],v[164:167],v[104:107]
MM v[112:115],a[224:227],v[168:171],v[136:139]
MM v[112:115],a[228:231],v[184:187],v[112:115]
MM v[112:115],a[232:235],v[200:203],v[112:115]
MM v[112:115],a[236:239],v[216:219],v[112:115]
MM v[116:119],a[252:255],v[216:219],v[136:139]
)ASM"
R"ASM(
MM v[116:119],a[248:251],v[200:203],v[116:119]
MM v[116:119],a[244:247],v[184:187],v[116:119]
MM v[116:119],a[240:243],v[168:171],v[116:119]
MM v[124:127],a[240:243],v[172:175],v[140:143]
MM v[124:127],a[244:247],v[188:191],v[124:127]
MM v[124:127],a[248:251],v[204:207],v[124:127]
MM v[124:127],a[252:255],v[220:223],v[124:127]
MM v[120:123],a[236:239],v[220:223],v[140:143]
MM v[120:123],a[232:235],v[204:207],v[120:123]
MM v[120:123],a[228:231],v[188:191],v[120:123]
MM v[120:123],a[224:227],v[172:175],v[120:123]
s_nop 15
v_add_u32 v249, s75, v224
RD a[192:195],v249 offset:0
RD a[208:211],v249 offset:4096
RD a[224:227],v249 offset:8192
RD a[240:243],v249 offset:12288
v_add_u32 v250, s75, v225
RD a[196:199],v250 offset:0
RD a[212:215],v250 offset:4096
RD a[228:231],v250 offset:8192
RD a[244:247],v250 offset:12288
v_add_u32 v251, s75, v226
RD a[200:203],v251 offset:0
RD a[216:219],v251 offset:4096
RD a[232:235],v251 offset:8192
RD a[248:251],v251 offset:12288
v_add_u32 v254, s75, v227
RD a[204:207],v254 offset:0
RD a[220:223],v254 offset:4096
RD a[236:239],v254 offset:8192
RD a[252:255],v254 offset:12288
s_waitcnt lgkmcnt(0)
v_mov_b32 v236, v246
v_mov_b32 v237, 0
v_max3_f32 v249, v64, v65, v66
v_max3_f32 v251, v96, v97, v98
v_max3_f32 v249, v249, v67, v68
v_max3_f32 v251, v251, v99, v100
v_max3_f32 v249, v249, v69, v70
v_max3_f32 v251, v251, v101, v102
v_max3_f32 v249, v249, v71, v103
v_max_f32 v249, v249, v251
v_log_f32 v250, v246
s_lshl_b64 exec, 0xffff, 0
v_max_f32 v249, v249, v250
s_mov_b64 exec, -1
v_mov_b32 v251, v249
s_nop 1
v_permlane16_swap_b32 v249, v251
v_max_f32 v249, v249, v251
v_mov_b32 v251, v249
s_nop 1
v_permlane32_swap_b32 v249, v251
v_max_f32 v249, v249, v251
v_max_f32 v249, 0, v249
v_sub_f32 v250, 0, v249
EX v250,v250
v_sub_f32 v128, v128, v249
v_sub_f32 v129, v129, v249
v_sub_f32 v130, v130, v249
v_sub_f32 v131, v131, v249
v_sub_f32 v0, v0, v249
v_sub_f32 v64, v64, v249
v_sub_f32 v1, v1, v249
v_sub_f32 v65, v65, v249
v_sub_f32 v2, v2, v249
v_sub_f32 v66, v66, v249
v_sub_f32 v3, v3, v249
v_sub_f32 v67, v67, v249
v_sub_f32 v4, v4, v249
v_sub_f32 v68, v68, v249
v_sub_f32 v5, v5, v249
v_sub_f32 v69, v69, v249
v_sub_f32 v6, v6, v249
v_sub_f32 v70, v70, v249
v_sub_f32 v7, v7, v249
v_sub_f32 v71, v71, v249
v_sub_f32 v32, v32, v249
v_sub_f32 v96, v96, v249
v_sub_f32 v33, v33, v249
v_sub_f32 v97, v97, v249
v_sub_f32 v34, v34, v249
v_sub_f32 v98, v98, v249
v_sub_f32 v35, v35, v249
v_sub_f32 v99, v99, v249
v_sub_f32 v36, v36, v249
v_sub_f32 v100, v100, v249
v_sub_f32 v37, v37, v249
v_sub_f32 v101, v101, v249
v_sub_f32 v38, v38, v249
v_sub_f32 v102, v102, v249
v_sub_f32 v39, v39, v249
v_sub_f32 v103, v103, v249
s_lshl_b64 exec, 0xffff, 0
v_mul_f32 v236, v236, v250
s_mov_b64 exec, -1
AR v254,a0
v_mul_f32 v254, v254, v250
AW a0,v254
AR v251,a1
v_mul_f32 v251, v251, v250
AW a1,v251
AR v254,a2
v_mul_f32 v254, v254, v250
AW a2,v254
AR v251,a3
v_mul_f32 v251, v251, v250
AW a3,v251
AR v254,a4
v_mul_f32 v254, v254, v250
AW a4,v254
AR v251,a5
v_mul_f32 v251, v251, v250
AW a5,v251
AR v254,a6
v_mul_f32 v254, v254, v250
AW a6,v254
AR v251,a7
v_mul_f32 v251, v251, v250
AW a7,v251
AR v254,a32
v_mul_f32 v254, v254, v250
AW a32,v254
AR v251,a33
v_mul_f32 v251, v251, v250
AW a33,v251
AR v254,a34
v_mul_f32 v254, v254, v250
AW a34,v254
AR v251,a35
v_mul_f32 v251, v251, v250
AW a35,v251
AR v254,a36
v_mul_f32 v254, v254, v250
AW a36,v254
AR v251,a37
v_mul_f32 v251, v251, v250
AW a37,v251
AR v254,a38
v_mul_f32 v254, v254, v250
AW a38,v254
AR v251,a39
v_mul_f32 v251, v251, v250
AW a39,v251
AR v254,a64
v_mul_f32 v254, v254, v250
AW a64,v254
AR v251,a65
v_mul_f32 v251, v251, v250
AW a65,v251
AR v254,a66
v_mul_f32 v254, v254, v250
AW a66,v254
AR v251,a67
v_mul_f32 v251, v251, v250
AW a67,v251
AR v254,a68
v_mul_f32 v254, v254, v250
AW a68,v254
AR v251,a69
v_mul_f32 v251, v251, v250
AW a69,v251
AR v254,a70
v_mul_f32 v254, v254, v250
AW a70,v254
AR v251,a71
v_mul_f32 v251, v251, v250
AW a71,v251
AR v254,a96
v_mul_f32 v254, v254, v250
AW a96,v254
AR v251,a97
v_mul_f32 v251, v251, v250
AW a97,v251
AR v254,a98
v_mul_f32 v254, v254, v250
AW a98,v254
AR v251,a99
v_mul_f32 v251, v251, v250
AW a99,v251
AR v254,a100
v_mul_f32 v254, v254, v250
AW a100,v254
AR v251,a101
v_mul_f32 v251, v251, v250
AW a101,v251
AR v254,a102
v_mul_f32 v254, v254, v250
AW a102,v254
AR v251,a103
v_mul_f32 v251, v251, v250
AW a103,v251
v_max3_f32 v249, v72, v73, v74
v_max3_f32 v251, v104, v105, v106
v_max3_f32 v249, v249, v75, v76
v_max3_f32 v251, v251, v107, v108
v_max3_f32 v249, v249, v77, v78
v_max3_f32 v251, v251, v109, v110
v_max3_f32 v249, v249, v79, v111
v_max_f32 v249, v249, v251
v_log_f32 v250, v246
s_lshl_b64 exec, 0xffff, 16
v_max_f32 v249, v249, v250
s_mov_b64 exec, -1
v_mov_b32 v251, v249
s_nop 1
v_permlane16_swap_b32 v249, v251
v_max_f32 v249, v249, v251
v_mov_b32 v251, v249
s_nop 1
v_permlane32_swap_b32 v249, v251
v_max_f32 v249, v249, v251
v_max_f32 v249, 0, v249
v_sub_f32 v250, 0, v249
EX v250,v250
v_sub_f32 v132, v132, v249
v_sub_f32 v133, v133, v249
v_sub_f32 v134, v134, v249
v_sub_f32 v135, v135, v249
v_sub_f32 v8, v8, v249
v_sub_f32 v72, v72, v249
v_sub_f32 v9, v9, v249
v_sub_f32 v73, v73, v249
v_sub_f32 v10, v10, v249
v_sub_f32 v74, v74, v249
v_sub_f32 v11, v11, v249
v_sub_f32 v75, v75, v249
v_sub_f32 v12, v12, v249
v_sub_f32 v76, v76, v249
v_sub_f32 v13, v13, v249
v_sub_f32 v77, v77, v249
v_sub_f32 v14, v14, v249
v_sub_f32 v78, v78, v249
v_sub_f32 v15, v15, v249
v_sub_f32 v79, v79, v249
v_sub_f32 v40, v40, v249
v_sub_f32 v104, v104, v249
v_sub_f32 v41, v41, v249
v_sub_f32 v105, v105, v249
v_sub_f32 v42, v42, v249
v_sub_f32 v106, v106, v249
v_sub_f32 v43, v43, v249
v_sub_f32 v107, v107, v249
v_sub_f32 v44, v44, v249
v_sub_f32 v108, v108, v249
v_sub_f32 v45, v45, v249
v_sub_f32 v109, v109, v249
v_sub_f32 v46, v46, v249
v_sub_f32 v110, v110, v249
v_sub_f32 v47, v47, v249
v_sub_f32 v111, v111, v249
s_lshl_b64 exec, 0xffff, 16
v_mul_f32 v236, v236, v250
s_mov_b64 exec, -1
AR v254,a8
v_mul_f32 v254, v254, v250
AW a8,v254
AR v251,a9
v_mul_f32 v251, v251, v250
AW a9,v251
AR v254,a10
v_mul_f32 v254, v254, v250
AW a10,v254
AR v251,a11
v_mul_f32 v251, v251, v250
AW a11,v251
AR v254,a12
v_mul_f32 v254, v254, v250
AW a12,v254
AR v251,a13
v_mul_f32 v251, v251, v250
AW a13,v251
AR v254,a14
v_mul_f32 v254, v254, v250
AW a14,v254
AR v251,a15
v_mul_f32 v251, v251, v250
AW a15,v251
AR v254,a40
v_mul_f32 v254, v254, v250
AW a40,v254
AR v251,a41
v_mul_f32 v251, v251, v250
AW a41,v251
AR v254,a42
v_mul_f32 v254, v254, v250
AW a42,v254
AR v251,a43
v_mul_f32 v251, v251, v250
AW a43,v251
AR v254,a44
v_mul_f32 v254, v254, v250
AW a44,v254
AR v251,a45
v_mul_f32 v251, v251, v250
AW a45,v251
AR v254,a46
v_mul_f32 v254, v254, v250
AW a46,v254
AR v251,a47
v_mul_f32 v251, v251, v250
AW a47,v251
AR v254,a72
v_mul_f32 v254, v254, v250
AW a72,v254
AR v251,a73
v_mul_f32 v251, v251, v250
AW a73,v251
AR v254,a74
v_mul_f32 v254, v254, v250
AW a74,v254
AR v251,a75
v_mul_f32 v251, v251, v250
AW a75,v251
AR v254,a76
v_mul_f32 v254, v254, v250
AW a76,v254
AR v251,a77
v_mul_f32 v251, v251, v250
AW a77,v251
AR v254,a78
v_mul_f32 v254, v254, v250
AW a78,v254
AR v251,a79
v_mul_f32 v251, v251, v250
AW a79,v251
AR v254,a104
v_mul_f32 v254, v254, v250
AW a104,v254
AR v251,a105
v_mul_f32 v251, v251, v250
AW a105,v251
AR v254,a106
v_mul_f32 v254, v254, v250
AW a106,v254
AR v251,a107
v_mul_f32 v251, v251, v250
AW a107,v251
AR v254,a108
v_mul_f32 v254, v254, v250
AW a108,v254
AR v251,a109
v_mul_f32 v251, v251, v250
AW a109,v251
AR v254,a110
v_mul_f32 v254, v254, v250
AW a110,v254
AR v251,a111
v_mul_f32 v251, v251, v250
AW a111,v251
v_max3_f32 v249, v80, v81, v82
v_max3_f32 v251, v112, v113, v114
v_max3_f32 v249, v249, v83, v84
v_max3_f32 v251, v251, v115, v116
v_max3_f32 v249, v249, v85, v86
v_max3_f32 v251, v251, v117, v118
v_max3_f32 v249, v249, v87, v119
v_max_f32 v249, v249, v251
v_log_f32 v250, v246
s_lshl_b64 exec, 0xffff, 32
v_max_f32 v249, v249, v250
s_mov_b64 exec, -1
v_mov_b32 v251, v249
s_nop 1
v_permlane16_swap_b32 v249, v251
v_max_f32 v249, v249, v251
v_mov_b32 v251, v249
s_nop 1
v_permlane32_swap_b32 v249, v251
v_max_f32 v249, v249, v251
v_max_f32 v249, 0, v249
v_sub_f32 v250, 0, v249
EX v250,v250
v_sub_f32 v136, v136, v249
v_sub_f32 v137, v137, v249
v_sub_f32 v138, v138, v249
v_sub_f32 v139, v139, v249
v_sub_f32 v16, v16, v249
v_sub_f32 v80, v80, v249
v_sub_f32 v17, v17, v249
v_sub_f32 v81, v81, v249
v_sub_f32 v18, v18, v249
v_sub_f32 v82, v82, v249
v_sub_f32 v19, v19, v249
v_sub_f32 v83, v83, v249
v_sub_f32 v20, v20, v249
v_sub_f32 v84, v84, v249
v_sub_f32 v21, v21, v249
v_sub_f32 v85, v85, v249
v_sub_f32 v22, v22, v249
v_sub_f32 v86, v86, v249
v_sub_f32 v23, v23, v249
v_sub_f32 v87, v87, v249
v_sub_f32 v48, v48, v249
v_sub_f32 v112, v112, v249
v_sub_f32 v49, v49, v249
v_sub_f32 v113, v113, v249
v_sub_f32 v50, v50, v249
v_sub_f32 v114, v114, v249
v_sub_f32 v51, v51, v249
v_sub_f32 v115, v115, v249
v_sub_f32 v52, v52, v249
v_sub_f32 v116, v116, v249
v_sub_f32 v53, v53, v249
v_sub_f32 v117, v117, v249
v_sub_f32 v54, v54, v249
v_sub_f32 v118, v118, v249
v_sub_f32 v55, v55, v249
v_sub_f32 v119, v119, v249
s_lshl_b64 exec, 0xffff, 32
v_mul_f32 v236, v236, v250
s_mov_b64 exec, -1
AR v254,a16
v_mul_f32 v254, v254, v250
AW a16,v254
AR v251,a17
v_mul_f32 v251, v251, v250
AW a17,v251
AR v254,a18
v_mul_f32 v254, v254, v250
AW a18,v254
AR v251,a19
v_mul_f32 v251, v251, v250
AW a19,v251
AR v254,a20
v_mul_f32 v254, v254, v250
AW a20,v254
AR v251,a21
v_mul_f32 v251, v251, v250
AW a21,v251
AR v254,a22
v_mul_f32 v254, v254, v250
AW a22,v254
AR v251,a23
v_mul_f32 v251, v251, v250
AW a23,v251
AR v254,a48
v_mul_f32 v254, v254, v250
AW a48,v254
AR v251,a49
v_mul_f32 v251, v251, v250
AW a49,v251
AR v254,a50
v_mul_f32 v254, v254, v250
AW a50,v254
AR v251,a51
v_mul_f32 v251, v251, v250
AW a51,v251
AR v254,a52
v_mul_f32 v254, v254, v250
AW a52,v254
AR v251,a53
v_mul_f32 v251, v251, v250
AW a53,v251
AR v254,a54
v_mul_f32 v254, v254, v250
AW a54,v254
AR v251,a55
v_mul_f32 v251, v251, v250
AW a55,v251
AR v254,a80
v_mul_f32 v254, v254, v250
AW a80,v254
AR v251,a81
v_mul_f32 v251, v251, v250
AW a81,v251
AR v254,a82
v_mul_f32 v254, v254, v250
AW a82,v254
AR v251,a83
v_mul_f32 v251, v251, v250
AW a83,v251
AR v254,a84
v_mul_f32 v254, v254, v250
AW a84,v254
AR v251,a85
v_mul_f32 v251, v251, v250
AW a85,v251
AR v254,a86
v_mul_f32 v254, v254, v250
AW a86,v254
AR v251,a87
v_mul_f32 v251, v251, v250
AW a87,v251
AR v254,a112
v_mul_f32 v254, v254, v250
AW a112,v254
AR v251,a113
v_mul_f32 v251, v251, v250
AW a113,v251
AR v254,a114
v_mul_f32 v254, v254, v250
AW a114,v254
AR v251,a115
v_mul_f32 v251, v251, v250
AW a115,v251
AR v254,a116
v_mul_f32 v254, v254, v250
AW a116,v254
AR v251,a117
v_mul_f32 v251, v251, v250
AW a117,v251
AR v254,a118
v_mul_f32 v254, v254, v250
AW a118,v254
AR v251,a119
v_mul_f32 v251, v251, v250
AW a119,v251
v_max3_f32 v249, v88, v89, v90
v_max3_f32 v251, v120, v121, v122
v_max3_f32 v249, v249, v91, v92
v_max3_f32 v251, v251, v123, v124
v_max3_f32 v249, v249, v93, v94
v_max3_f32 v251, v251, v125, v126
v_max3_f32 v249, v249, v95, v127
v_max_f32 v249, v249, v251
v_log_f32 v250, v246
s_lshl_b64 exec, 0xffff, 48
v_max_f32 v249, v249, v250
s_mov_b64 exec, -1
v_mov_b32 v251, v249
s_nop 1
v_permlane16_swap_b32 v249, v251
v_max_f32 v249, v249, v251
v_mov_b32 v251, v249
s_nop 1
v_permlane32_swap_b32 v249, v251
v_max_f32 v249, v249, v251
v_max_f32 v249, 0, v249
v_sub_f32 v250, 0, v249
EX v250,v250
v_sub_f32 v140, v140, v249
v_sub_f32 v141, v141, v249
v_sub_f32 v142, v142, v249
v_sub_f32 v143, v143, v249
v_sub_f32 v24, v24, v249
v_sub_f32 v88, v88, v249
v_sub_f32 v25, v25, v249
v_sub_f32 v89, v89, v249
v_sub_f32 v26, v26, v249
v_sub_f32 v90, v90, v249
v_sub_f32 v27, v27, v249
v_sub_f32 v91, v91, v249
v_sub_f32 v28, v28, v249
v_sub_f32 v92, v92, v249
v_sub_f32 v29, v29, v249
v_sub_f32 v93, v93, v249
v_sub_f32 v30, v30, v249
v_sub_f32 v94, v94, v249
v_sub_f32 v31, v31, v249
v_sub_f32 v95, v95, v249
v_sub_f32 v56, v56, v249
v_sub_f32 v120, v120, v249
v_sub_f32 v57, v57, v249
)ASM"
R"ASM(
v_sub_f32 v121, v121, v249
v_sub_f32 v58, v58, v249
v_sub_f32 v122, v122, v249
v_sub_f32 v59, v59, v249
v_sub_f32 v123, v123, v249
v_sub_f32 v60, v60, v249
v_sub_f32 v124, v124, v249
v_sub_f32 v61, v61, v249
v_sub_f32 v125, v125, v249
v_sub_f32 v62, v62, v249
v_sub_f32 v126, v126, v249
v_sub_f32 v63, v63, v249
v_sub_f32 v127, v127, v249
s_lshl_b64 exec, 0xffff, 48
v_mul_f32 v236, v236, v250
s_mov_b64 exec, -1
AR v254,a24
v_mul_f32 v254, v254, v250
AW a24,v254
AR v251,a25
v_mul_f32 v251, v251, v250
AW a25,v251
AR v254,a26
v_mul_f32 v254, v254, v250
AW a26,v254
AR v251,a27
v_mul_f32 v251, v251, v250
AW a27,v251
AR v254,a28
v_mul_f32 v254, v254, v250
AW a28,v254
AR v251,a29
v_mul_f32 v251, v251, v250
AW a29,v251
AR v254,a30
v_mul_f32 v254, v254, v250
AW a30,v254
AR v251,a31
v_mul_f32 v251, v251, v250
AW a31,v251
AR v254,a56
v_mul_f32 v254, v254, v250
AW a56,v254
AR v251,a57
v_mul_f32 v251, v251, v250
AW a57,v251
AR v254,a58
v_mul_f32 v254, v254, v250
AW a58,v254
AR v251,a59
v_mul_f32 v251, v251, v250
AW a59,v251
AR v254,a60
v_mul_f32 v254, v254, v250
AW a60,v254
AR v251,a61
v_mul_f32 v251, v251, v250
AW a61,v251
AR v254,a62
v_mul_f32 v254, v254, v250
AW a62,v254
AR v251,a63
v_mul_f32 v251, v251, v250
AW a63,v251
AR v254,a88
v_mul_f32 v254, v254, v250
AW a88,v254
AR v251,a89
v_mul_f32 v251, v251, v250
AW a89,v251
AR v254,a90
v_mul_f32 v254, v254, v250
AW a90,v254
AR v251,a91
v_mul_f32 v251, v251, v250
AW a91,v251
AR v254,a92
v_mul_f32 v254, v254, v250
AW a92,v254
AR v251,a93
v_mul_f32 v251, v251, v250
AW a93,v251
AR v254,a94
v_mul_f32 v254, v254, v250
AW a94,v254
AR v251,a95
v_mul_f32 v251, v251, v250
AW a95,v251
AR v254,a120
v_mul_f32 v254, v254, v250
AW a120,v254
AR v251,a121
v_mul_f32 v251, v251, v250
AW a121,v251
AR v254,a122
v_mul_f32 v254, v254, v250
AW a122,v254
AR v251,a123
v_mul_f32 v251, v251, v250
AW a123,v251
AR v254,a124
v_mul_f32 v254, v254, v250
AW a124,v254
AR v251,a125
v_mul_f32 v251, v251, v250
AW a125,v251
AR v254,a126
v_mul_f32 v254, v254, v250
AW a126,v254
AR v251,a127
v_mul_f32 v251, v251, v250
AW a127,v251
EX v64,v64
EX v65,v65
EX v66,v66
EX v67,v67
EX v68,v68
EX v69,v69
EX v70,v70
EX v71,v71
EX v72,v72
EX v73,v73
EX v74,v74
EX v75,v75
EX v76,v76
EX v77,v77
EX v78,v78
EX v79,v79
EX v80,v80
EX v81,v81
EX v82,v82
EX v83,v83
EX v84,v84
EX v85,v85
EX v86,v86
EX v87,v87
EX v88,v88
EX v89,v89
EX v90,v90
EX v91,v91
EX v92,v92
EX v93,v93
EX v94,v94
EX v95,v95
EX v96,v96
EX v97,v97
EX v98,v98
EX v99,v99
EX v100,v100
EX v101,v101
EX v102,v102
EX v103,v103
EX v104,v104
EX v105,v105
EX v106,v106
EX v107,v107
EX v108,v108
EX v109,v109
EX v110,v110
EX v111,v111
EX v112,v112
EX v113,v113
EX v114,v114
EX v115,v115
EX v116,v116
EX v117,v117
EX v118,v118
EX v119,v119
EX v120,v120
EX v121,v121
EX v122,v122
EX v123,v123
EX v124,v124
EX v125,v125
EX v126,v126
EX v127,v127
CV v64,v64,v65
CV v65,v66,v67
CV v66,v68,v69
CV v67,v70,v71
CV v72,v72,v73
CV v73,v74,v75
CV v74,v76,v77
CV v75,v78,v79
CV v80,v80,v81
CV v81,v82,v83
CV v82,v84,v85
CV v83,v86,v87
CV v88,v88,v89
CV v89,v90,v91
CV v90,v92,v93
CV v91,v94,v95
CV v68,v96,v97
CV v69,v98,v99
CV v70,v100,v101
CV v71,v102,v103
CV v76,v104,v105
CV v77,v106,v107
CV v78,v108,v109
CV v79,v110,v111
CV v84,v112,v113
CV v85,v114,v115
CV v86,v116,v117
CV v87,v118,v119
CV v92,v120,v121
CV v93,v122,v123
CV v94,v124,v125
CV v95,v126,v127
v_smfmac_f32_16x16x64_bf16 v[236:239], v[144:147], v[64:71], v234
v_smfmac_f32_16x16x64_bf16 v[236:239], v[148:151], v[72:79], v234
v_smfmac_f32_16x16x64_bf16 v[236:239], v[152:155], v[80:87], v234
v_smfmac_f32_16x16x64_bf16 v[236:239], v[156:159], v[88:95], v234
s_nop 9
v_add_f32 v247, v236, v237
s_cmp_eq_u32 s73, 3
s_cbranch_scc1 LBACK3_%=
s_cmp_eq_u32 s73, 5
s_cbranch_scc1 LBACK5_%=
s_cmp_eq_u32 s73, 7
s_cbranch_scc1 LBACK7_%=
s_cmp_eq_u32 s73, 9
s_cbranch_scc1 LBACK9_%=
s_cmp_eq_u32 s73, 11
s_cbranch_scc1 LBACK11_%=
s_cmp_eq_u32 s73, 13
s_cbranch_scc1 LBACK13_%=
s_cmp_eq_u32 s73, 15
s_cbranch_scc1 LBACK15_%=
s_cmp_eq_u32 s73, 17
s_cbranch_scc1 LBACK17_%=
s_cmp_eq_u32 s73, 19
s_cbranch_scc1 LBACK19_%=
s_cmp_eq_u32 s73, 21
s_cbranch_scc1 LBACK21_%=
s_cmp_eq_u32 s73, 23
s_cbranch_scc1 LBACK23_%=
s_cmp_eq_u32 s73, 25
s_cbranch_scc1 LBACK25_%=
s_cmp_eq_u32 s73, 27
s_cbranch_scc1 LBACK27_%=
s_cmp_eq_u32 s73, 29
s_cbranch_scc1 LBACK29_%=
s_cmp_eq_u32 s73, 31
s_cbranch_scc1 LBACK31_%=
s_endpgm
LSITE4_%=:
s_mov_b32 s73, 4
s_mov_b32 s75, 16384
s_mov_b32 s74, 0
s_branch LRARE004_%=
LSITE6_%=:
s_mov_b32 s73, 6
s_mov_b32 s75, 49152
s_mov_b32 s74, 32768
s_branch LRARE004_%=
LSITE8_%=:
s_mov_b32 s73, 8
s_mov_b32 s75, 16384
s_mov_b32 s74, 0
s_branch LRARE004_%=
LSITE10_%=:
s_mov_b32 s73, 10
s_mov_b32 s75, 49152
s_mov_b32 s74, 32768
s_branch LRARE004_%=
LSITE12_%=:
s_mov_b32 s73, 12
s_mov_b32 s75, 16384
s_mov_b32 s74, 0
s_branch LRARE004_%=
LSITE14_%=:
s_mov_b32 s73, 14
s_mov_b32 s75, 49152
s_mov_b32 s74, 32768
s_branch LRARE004_%=
LSITE16_%=:
s_mov_b32 s73, 16
s_mov_b32 s75, 16384
s_mov_b32 s74, 0
s_branch LRARE004_%=
LSITE18_%=:
s_mov_b32 s73, 18
s_mov_b32 s75, 49152
s_mov_b32 s74, 32768
s_branch LRARE004_%=
LSITE20_%=:
s_mov_b32 s73, 20
s_mov_b32 s75, 16384
s_mov_b32 s74, 0
s_branch LRARE004_%=
LSITE22_%=:
s_mov_b32 s73, 22
s_mov_b32 s75, 49152
s_mov_b32 s74, 32768
s_branch LRARE004_%=
LSITE24_%=:
s_mov_b32 s73, 24
s_mov_b32 s75, 16384
s_mov_b32 s74, 0
s_branch LRARE004_%=
LSITE26_%=:
s_mov_b32 s73, 26
s_mov_b32 s75, 49152
s_mov_b32 s74, 32768
s_branch LRARE004_%=
LSITE28_%=:
s_mov_b32 s73, 28
s_mov_b32 s75, 16384
s_mov_b32 s74, 0
s_branch LRARE004_%=
LSITE30_%=:
s_mov_b32 s73, 30
s_mov_b32 s75, 49152
s_mov_b32 s74, 32768
s_branch LRARE004_%=
LRARE004_%=:
s_nop 15
s_waitcnt vmcnt(0) lgkmcnt(0)
v_add_u32 v249, s74, v224
RD a[192:195],v249 offset:0
RD a[208:211],v249 offset:4096
RD a[224:227],v249 offset:8192
RD a[240:243],v249 offset:12288
v_add_u32 v250, s74, v225
RD a[196:199],v250 offset:0
RD a[212:215],v250 offset:4096
RD a[228:231],v250 offset:8192
RD a[244:247],v250 offset:12288
v_add_u32 v251, s74, v226
RD a[200:203],v251 offset:0
RD a[216:219],v251 offset:4096
RD a[232:235],v251 offset:8192
RD a[248:251],v251 offset:12288
v_add_u32 v254, s74, v227
RD a[204:207],v254 offset:0
RD a[220:223],v254 offset:4096
RD a[236:239],v254 offset:8192
RD a[252:255],v254 offset:12288
s_waitcnt lgkmcnt(0)
MM v[0:3],a[192:195],v[160:163],v[128:131]
MM v[0:3],a[196:199],v[176:179],v[0:3]
MM v[0:3],a[200:203],v[192:195],v[0:3]
MM v[0:3],a[204:207],v[208:211],v[0:3]
MM v[4:7],a[220:223],v[208:211],v[128:131]
MM v[4:7],a[216:219],v[192:195],v[4:7]
MM v[4:7],a[212:215],v[176:179],v[4:7]
MM v[4:7],a[208:211],v[160:163],v[4:7]
MM v[12:15],a[208:211],v[164:167],v[132:135]
MM v[12:15],a[212:215],v[180:183],v[12:15]
MM v[12:15],a[216:219],v[196:199],v[12:15]
MM v[12:15],a[220:223],v[212:215],v[12:15]
MM v[8:11],a[204:207],v[212:215],v[132:135]
MM v[8:11],a[200:203],v[196:199],v[8:11]
MM v[8:11],a[196:199],v[180:183],v[8:11]
MM v[8:11],a[192:195],v[164:167],v[8:11]
MM v[16:19],a[192:195],v[168:171],v[136:139]
MM v[16:19],a[196:199],v[184:187],v[16:19]
MM v[16:19],a[200:203],v[200:203],v[16:19]
MM v[16:19],a[204:207],v[216:219],v[16:19]
MM v[20:23],a[220:223],v[216:219],v[136:139]
MM v[20:23],a[216:219],v[200:203],v[20:23]
MM v[20:23],a[212:215],v[184:187],v[20:23]
MM v[20:23],a[208:211],v[168:171],v[20:23]
MM v[28:31],a[208:211],v[172:175],v[140:143]
MM v[28:31],a[212:215],v[188:191],v[28:31]
MM v[28:31],a[216:219],v[204:207],v[28:31]
MM v[28:31],a[220:223],v[220:223],v[28:31]
MM v[24:27],a[204:207],v[220:223],v[140:143]
MM v[24:27],a[200:203],v[204:207],v[24:27]
MM v[24:27],a[196:199],v[188:191],v[24:27]
MM v[24:27],a[192:195],v[172:175],v[24:27]
MM v[32:35],a[224:227],v[160:163],v[128:131]
MM v[32:35],a[228:231],v[176:179],v[32:35]
MM v[32:35],a[232:235],v[192:195],v[32:35]
MM v[32:35],a[236:239],v[208:211],v[32:35]
MM v[36:39],a[252:255],v[208:211],v[128:131]
MM v[36:39],a[248:251],v[192:195],v[36:39]
MM v[36:39],a[244:247],v[176:179],v[36:39]
MM v[36:39],a[240:243],v[160:163],v[36:39]
MM v[44:47],a[240:243],v[164:167],v[132:135]
MM v[44:47],a[244:247],v[180:183],v[44:47]
MM v[44:47],a[248:251],v[196:199],v[44:47]
MM v[44:47],a[252:255],v[212:215],v[44:47]
MM v[40:43],a[236:239],v[212:215],v[132:135]
MM v[40:43],a[232:235],v[196:199],v[40:43]
MM v[40:43],a[228:231],v[180:183],v[40:43]
MM v[40:43],a[224:227],v[164:167],v[40:43]
MM v[48:51],a[224:227],v[168:171],v[136:139]
MM v[48:51],a[228:231],v[184:187],v[48:51]
MM v[48:51],a[232:235],v[200:203],v[48:51]
MM v[48:51],a[236:239],v[216:219],v[48:51]
MM v[52:55],a[252:255],v[216:219],v[136:139]
MM v[52:55],a[248:251],v[200:203],v[52:55]
MM v[52:55],a[244:247],v[184:187],v[52:55]
MM v[52:55],a[240:243],v[168:171],v[52:55]
MM v[60:63],a[240:243],v[172:175],v[140:143]
MM v[60:63],a[244:247],v[188:191],v[60:63]
MM v[60:63],a[248:251],v[204:207],v[60:63]
MM v[60:63],a[252:255],v[220:223],v[60:63]
MM v[56:59],a[236:239],v[220:223],v[140:143]
MM v[56:59],a[232:235],v[204:207],v[56:59]
MM v[56:59],a[228:231],v[188:191],v[56:59]
MM v[56:59],a[224:227],v[172:175],v[56:59]
s_nop 15
v_add_u32 v249, s75, v224
RD a[192:195],v249 offset:0
RD a[208:211],v249 offset:4096
RD a[224:227],v249 offset:8192
RD a[240:243],v249 offset:12288
v_add_u32 v250, s75, v225
RD a[196:199],v250 offset:0
RD a[212:215],v250 offset:4096
RD a[228:231],v250 offset:8192
RD a[244:247],v250 offset:12288
v_add_u32 v251, s75, v226
RD a[200:203],v251 offset:0
RD a[216:219],v251 offset:4096
RD a[232:235],v251 offset:8192
RD a[248:251],v251 offset:12288
v_add_u32 v254, s75, v227
RD a[204:207],v254 offset:0
RD a[220:223],v254 offset:4096
RD a[236:239],v254 offset:8192
RD a[252:255],v254 offset:12288
s_waitcnt lgkmcnt(0)
v_mov_b32 v236, v247
v_mov_b32 v237, 0
v_max3_f32 v249, v0, v1, v2
v_max3_f32 v251, v32, v33, v34
v_max3_f32 v249, v249, v3, v4
v_max3_f32 v251, v251, v35, v36
v_max3_f32 v249, v249, v5, v6
v_max3_f32 v251, v251, v37, v38
v_max3_f32 v249, v249, v7, v39
v_max_f32 v249, v249, v251
v_log_f32 v250, v247
s_lshl_b64 exec, 0xffff, 0
v_max_f32 v249, v249, v250
s_mov_b64 exec, -1
v_mov_b32 v251, v249
s_nop 1
v_permlane16_swap_b32 v249, v251
v_max_f32 v249, v249, v251
v_mov_b32 v251, v249
s_nop 1
v_permlane32_swap_b32 v249, v251
v_max_f32 v249, v249, v251
v_max_f32 v249, 0, v249
v_sub_f32 v250, 0, v249
EX v250,v250
v_sub_f32 v128, v128, v249
v_sub_f32 v129, v129, v249
v_sub_f32 v130, v130, v249
v_sub_f32 v131, v131, v249
v_sub_f32 v64, v64, v249
v_sub_f32 v0, v0, v249
v_sub_f32 v65, v65, v249
v_sub_f32 v1, v1, v249
v_sub_f32 v66, v66, v249
v_sub_f32 v2, v2, v249
v_sub_f32 v67, v67, v249
v_sub_f32 v3, v3, v249
v_sub_f32 v68, v68, v249
v_sub_f32 v4, v4, v249
v_sub_f32 v69, v69, v249
v_sub_f32 v5, v5, v249
v_sub_f32 v70, v70, v249
v_sub_f32 v6, v6, v249
v_sub_f32 v71, v71, v249
v_sub_f32 v7, v7, v249
v_sub_f32 v96, v96, v249
v_sub_f32 v32, v32, v249
v_sub_f32 v97, v97, v249
v_sub_f32 v33, v33, v249
v_sub_f32 v98, v98, v249
v_sub_f32 v34, v34, v249
v_sub_f32 v99, v99, v249
v_sub_f32 v35, v35, v249
v_sub_f32 v100, v100, v249
v_sub_f32 v36, v36, v249
v_sub_f32 v101, v101, v249
v_sub_f32 v37, v37, v249
v_sub_f32 v102, v102, v249
v_sub_f32 v38, v38, v249
v_sub_f32 v103, v103, v249
v_sub_f32 v39, v39, v249
s_lshl_b64 exec, 0xffff, 0
v_mul_f32 v236, v236, v250
s_mov_b64 exec, -1
AR v254,a0
v_mul_f32 v254, v254, v250
AW a0,v254
AR v251,a1
v_mul_f32 v251, v251, v250
AW a1,v251
AR v254,a2
v_mul_f32 v254, v254, v250
AW a2,v254
AR v251,a3
v_mul_f32 v251, v251, v250
AW a3,v251
AR v254,a4
v_mul_f32 v254, v254, v250
AW a4,v254
AR v251,a5
v_mul_f32 v251, v251, v250
AW a5,v251
AR v254,a6
v_mul_f32 v254, v254, v250
AW a6,v254
AR v251,a7
v_mul_f32 v251, v251, v250
AW a7,v251
AR v254,a32
v_mul_f32 v254, v254, v250
AW a32,v254
AR v251,a33
v_mul_f32 v251, v251, v250
AW a33,v251
AR v254,a34
v_mul_f32 v254, v254, v250
AW a34,v254
AR v251,a35
v_mul_f32 v251, v251, v250
AW a35,v251
AR v254,a36
v_mul_f32 v254, v254, v250
AW a36,v254
AR v251,a37
v_mul_f32 v251, v251, v250
AW a37,v251
AR v254,a38
v_mul_f32 v254, v254, v250
AW a38,v254
AR v251,a39
v_mul_f32 v251, v251, v250
)ASM"
R"ASM(
AW a39,v251
AR v254,a64
v_mul_f32 v254, v254, v250
AW a64,v254
AR v251,a65
v_mul_f32 v251, v251, v250
AW a65,v251
AR v254,a66
v_mul_f32 v254, v254, v250
AW a66,v254
AR v251,a67
v_mul_f32 v251, v251, v250
AW a67,v251
AR v254,a68
v_mul_f32 v254, v254, v250
AW a68,v254
AR v251,a69
v_mul_f32 v251, v251, v250
AW a69,v251
AR v254,a70
v_mul_f32 v254, v254, v250
AW a70,v254
AR v251,a71
v_mul_f32 v251, v251, v250
AW a71,v251
AR v254,a96
v_mul_f32 v254, v254, v250
AW a96,v254
AR v251,a97
v_mul_f32 v251, v251, v250
AW a97,v251
AR v254,a98
v_mul_f32 v254, v254, v250
AW a98,v254
AR v251,a99
v_mul_f32 v251, v251, v250
AW a99,v251
AR v254,a100
v_mul_f32 v254, v254, v250
AW a100,v254
AR v251,a101
v_mul_f32 v251, v251, v250
AW a101,v251
AR v254,a102
v_mul_f32 v254, v254, v250
AW a102,v254
AR v251,a103
v_mul_f32 v251, v251, v250
AW a103,v251
v_max3_f32 v249, v8, v9, v10
v_max3_f32 v251, v40, v41, v42
v_max3_f32 v249, v249, v11, v12
v_max3_f32 v251, v251, v43, v44
v_max3_f32 v249, v249, v13, v14
v_max3_f32 v251, v251, v45, v46
v_max3_f32 v249, v249, v15, v47
v_max_f32 v249, v249, v251
v_log_f32 v250, v247
s_lshl_b64 exec, 0xffff, 16
v_max_f32 v249, v249, v250
s_mov_b64 exec, -1
v_mov_b32 v251, v249
s_nop 1
v_permlane16_swap_b32 v249, v251
v_max_f32 v249, v249, v251
v_mov_b32 v251, v249
s_nop 1
v_permlane32_swap_b32 v249, v251
v_max_f32 v249, v249, v251
v_max_f32 v249, 0, v249
v_sub_f32 v250, 0, v249
EX v250,v250
v_sub_f32 v132, v132, v249
v_sub_f32 v133, v133, v249
v_sub_f32 v134, v134, v249
v_sub_f32 v135, v135, v249
v_sub_f32 v72, v72, v249
v_sub_f32 v8, v8, v249
v_sub_f32 v73, v73, v249
v_sub_f32 v9, v9, v249
v_sub_f32 v74, v74, v249
v_sub_f32 v10, v10, v249
v_sub_f32 v75, v75, v249
v_sub_f32 v11, v11, v249
v_sub_f32 v76, v76, v249
v_sub_f32 v12, v12, v249
v_sub_f32 v77, v77, v249
v_sub_f32 v13, v13, v249
v_sub_f32 v78, v78, v249
v_sub_f32 v14, v14, v249
v_sub_f32 v79, v79, v249
v_sub_f32 v15, v15, v249
v_sub_f32 v104, v104, v249
v_sub_f32 v40, v40, v249
v_sub_f32 v105, v105, v249
v_sub_f32 v41, v41, v249
v_sub_f32 v106, v106, v249
v_sub_f32 v42, v42, v249
v_sub_f32 v107, v107, v249
v_sub_f32 v43, v43, v249
v_sub_f32 v108, v108, v249
v_sub_f32 v44, v44, v249
v_sub_f32 v109, v109, v249
v_sub_f32 v45, v45, v249
v_sub_f32 v110, v110, v249
v_sub_f32 v46, v46, v249
v_sub_f32 v111, v111, v249
v_sub_f32 v47, v47, v249
s_lshl_b64 exec, 0xffff, 16
v_mul_f32 v236, v236, v250
s_mov_b64 exec, -1
AR v254,a8
v_mul_f32 v254, v254, v250
AW a8,v254
AR v251,a9
v_mul_f32 v251, v251, v250
AW a9,v251
AR v254,a10
v_mul_f32 v254, v254, v250
AW a10,v254
AR v251,a11
v_mul_f32 v251, v251, v250
AW a11,v251
AR v254,a12
v_mul_f32 v254, v254, v250
AW a12,v254
AR v251,a13
v_mul_f32 v251, v251, v250
AW a13,v251
AR v254,a14
v_mul_f32 v254, v254, v250
AW a14,v254
AR v251,a15
v_mul_f32 v251, v251, v250
AW a15,v251
AR v254,a40
v_mul_f32 v254, v254, v250
AW a40,v254
AR v251,a41
v_mul_f32 v251, v251, v250
AW a41,v251
AR v254,a42
v_mul_f32 v254, v254, v250
AW a42,v254
AR v251,a43
v_mul_f32 v251, v251, v250
AW a43,v251
AR v254,a44
v_mul_f32 v254, v254, v250
AW a44,v254
AR v251,a45
v_mul_f32 v251, v251, v250
AW a45,v251
AR v254,a46
v_mul_f32 v254, v254, v250
AW a46,v254
AR v251,a47
v_mul_f32 v251, v251, v250
AW a47,v251
AR v254,a72
v_mul_f32 v254, v254, v250
AW a72,v254
AR v251,a73
v_mul_f32 v251, v251, v250
AW a73,v251
AR v254,a74
v_mul_f32 v254, v254, v250
AW a74,v254
AR v251,a75
v_mul_f32 v251, v251, v250
AW a75,v251
AR v254,a76
v_mul_f32 v254, v254, v250
AW a76,v254
AR v251,a77
v_mul_f32 v251, v251, v250
AW a77,v251
AR v254,a78
v_mul_f32 v254, v254, v250
AW a78,v254
AR v251,a79
v_mul_f32 v251, v251, v250
AW a79,v251
AR v254,a104
v_mul_f32 v254, v254, v250
AW a104,v254
AR v251,a105
v_mul_f32 v251, v251, v250
AW a105,v251
AR v254,a106
v_mul_f32 v254, v254, v250
AW a106,v254
AR v251,a107
v_mul_f32 v251, v251, v250
AW a107,v251
AR v254,a108
v_mul_f32 v254, v254, v250
AW a108,v254
AR v251,a109
v_mul_f32 v251, v251, v250
AW a109,v251
AR v254,a110
v_mul_f32 v254, v254, v250
AW a110,v254
AR v251,a111
v_mul_f32 v251, v251, v250
AW a111,v251
v_max3_f32 v249, v16, v17, v18
v_max3_f32 v251, v48, v49, v50
v_max3_f32 v249, v249, v19, v20
v_max3_f32 v251, v251, v51, v52
v_max3_f32 v249, v249, v21, v22
v_max3_f32 v251, v251, v53, v54
v_max3_f32 v249, v249, v23, v55
v_max_f32 v249, v249, v251
v_log_f32 v250, v247
s_lshl_b64 exec, 0xffff, 32
v_max_f32 v249, v249, v250
s_mov_b64 exec, -1
v_mov_b32 v251, v249
s_nop 1
v_permlane16_swap_b32 v249, v251
v_max_f32 v249, v249, v251
v_mov_b32 v251, v249
s_nop 1
v_permlane32_swap_b32 v249, v251
v_max_f32 v249, v249, v251
v_max_f32 v249, 0, v249
v_sub_f32 v250, 0, v249
EX v250,v250
v_sub_f32 v136, v136, v249
v_sub_f32 v137, v137, v249
v_sub_f32 v138, v138, v249
v_sub_f32 v139, v139, v249
v_sub_f32 v80, v80, v249
v_sub_f32 v16, v16, v249
v_sub_f32 v81, v81, v249
v_sub_f32 v17, v17, v249
v_sub_f32 v82, v82, v249
v_sub_f32 v18, v18, v249
v_sub_f32 v83, v83, v249
v_sub_f32 v19, v19, v249
v_sub_f32 v84, v84, v249
v_sub_f32 v20, v20, v249
v_sub_f32 v85, v85, v249
v_sub_f32 v21, v21, v249
v_sub_f32 v86, v86, v249
v_sub_f32 v22, v22, v249
v_sub_f32 v87, v87, v249
v_sub_f32 v23, v23, v249
v_sub_f32 v112, v112, v249
v_sub_f32 v48, v48, v249
v_sub_f32 v113, v113, v249
v_sub_f32 v49, v49, v249
v_sub_f32 v114, v114, v249
v_sub_f32 v50, v50, v249
v_sub_f32 v115, v115, v249
v_sub_f32 v51, v51, v249
v_sub_f32 v116, v116, v249
v_sub_f32 v52, v52, v249
v_sub_f32 v117, v117, v249
v_sub_f32 v53, v53, v249
v_sub_f32 v118, v118, v249
v_sub_f32 v54, v54, v249
v_sub_f32 v119, v119, v249
v_sub_f32 v55, v55, v249
s_lshl_b64 exec, 0xffff, 32
v_mul_f32 v236, v236, v250
s_mov_b64 exec, -1
AR v254,a16
v_mul_f32 v254, v254, v250
AW a16,v254
AR v251,a17
v_mul_f32 v251, v251, v250
AW a17,v251
AR v254,a18
v_mul_f32 v254, v254, v250
AW a18,v254
AR v251,a19
v_mul_f32 v251, v251, v250
AW a19,v251
AR v254,a20
v_mul_f32 v254, v254, v250
AW a20,v254
AR v251,a21
v_mul_f32 v251, v251, v250
AW a21,v251
AR v254,a22
v_mul_f32 v254, v254, v250
AW a22,v254
AR v251,a23
v_mul_f32 v251, v251, v250
AW a23,v251
AR v254,a48
v_mul_f32 v254, v254, v250
AW a48,v254
AR v251,a49
v_mul_f32 v251, v251, v250
AW a49,v251
AR v254,a50
v_mul_f32 v254, v254, v250
AW a50,v254
AR v251,a51
v_mul_f32 v251, v251, v250
AW a51,v251
AR v254,a52
v_mul_f32 v254, v254, v250
AW a52,v254
AR v251,a53
v_mul_f32 v251, v251, v250
AW a53,v251
AR v254,a54
v_mul_f32 v254, v254, v250
AW a54,v254
AR v251,a55
v_mul_f32 v251, v251, v250
AW a55,v251
AR v254,a80
v_mul_f32 v254, v254, v250
AW a80,v254
AR v251,a81
v_mul_f32 v251, v251, v250
AW a81,v251
AR v254,a82
v_mul_f32 v254, v254, v250
AW a82,v254
AR v251,a83
v_mul_f32 v251, v251, v250
AW a83,v251
AR v254,a84
v_mul_f32 v254, v254, v250
AW a84,v254
AR v251,a85
v_mul_f32 v251, v251, v250
AW a85,v251
AR v254,a86
v_mul_f32 v254, v254, v250
AW a86,v254
AR v251,a87
v_mul_f32 v251, v251, v250
AW a87,v251
AR v254,a112
v_mul_f32 v254, v254, v250
AW a112,v254
AR v251,a113
v_mul_f32 v251, v251, v250
AW a113,v251
AR v254,a114
v_mul_f32 v254, v254, v250
AW a114,v254
AR v251,a115
v_mul_f32 v251, v251, v250
AW a115,v251
AR v254,a116
v_mul_f32 v254, v254, v250
AW a116,v254
AR v251,a117
v_mul_f32 v251, v251, v250
AW a117,v251
AR v254,a118
v_mul_f32 v254, v254, v250
AW a118,v254
AR v251,a119
v_mul_f32 v251, v251, v250
AW a119,v251
v_max3_f32 v249, v24, v25, v26
v_max3_f32 v251, v56, v57, v58
v_max3_f32 v249, v249, v27, v28
v_max3_f32 v251, v251, v59, v60
v_max3_f32 v249, v249, v29, v30
v_max3_f32 v251, v251, v61, v62
v_max3_f32 v249, v249, v31, v63
v_max_f32 v249, v249, v251
v_log_f32 v250, v247
s_lshl_b64 exec, 0xffff, 48
v_max_f32 v249, v249, v250
s_mov_b64 exec, -1
v_mov_b32 v251, v249
s_nop 1
v_permlane16_swap_b32 v249, v251
v_max_f32 v249, v249, v251
v_mov_b32 v251, v249
s_nop 1
v_permlane32_swap_b32 v249, v251
v_max_f32 v249, v249, v251
v_max_f32 v249, 0, v249
v_sub_f32 v250, 0, v249
EX v250,v250
v_sub_f32 v140, v140, v249
v_sub_f32 v141, v141, v249
v_sub_f32 v142, v142, v249
v_sub_f32 v143, v143, v249
v_sub_f32 v88, v88, v249
v_sub_f32 v24, v24, v249
v_sub_f32 v89, v89, v249
v_sub_f32 v25, v25, v249
v_sub_f32 v90, v90, v249
v_sub_f32 v26, v26, v249
v_sub_f32 v91, v91, v249
v_sub_f32 v27, v27, v249
v_sub_f32 v92, v92, v249
v_sub_f32 v28, v28, v249
v_sub_f32 v93, v93, v249
v_sub_f32 v29, v29, v249
v_sub_f32 v94, v94, v249
v_sub_f32 v30, v30, v249
v_sub_f32 v95, v95, v249
v_sub_f32 v31, v31, v249
v_sub_f32 v120, v120, v249
v_sub_f32 v56, v56, v249
v_sub_f32 v121, v121, v249
v_sub_f32 v57, v57, v249
v_sub_f32 v122, v122, v249
v_sub_f32 v58, v58, v249
v_sub_f32 v123, v123, v249
v_sub_f32 v59, v59, v249
v_sub_f32 v124, v124, v249
v_sub_f32 v60, v60, v249
v_sub_f32 v125, v125, v249
v_sub_f32 v61, v61, v249
v_sub_f32 v126, v126, v249
v_sub_f32 v62, v62, v249
v_sub_f32 v127, v127, v249
v_sub_f32 v63, v63, v249
s_lshl_b64 exec, 0xffff, 48
v_mul_f32 v236, v236, v250
s_mov_b64 exec, -1
AR v254,a24
v_mul_f32 v254, v254, v250
AW a24,v254
AR v251,a25
v_mul_f32 v251, v251, v250
AW a25,v251
AR v254,a26
v_mul_f32 v254, v254, v250
AW a26,v254
AR v251,a27
v_mul_f32 v251, v251, v250
AW a27,v251
AR v254,a28
v_mul_f32 v254, v254, v250
AW a28,v254
AR v251,a29
v_mul_f32 v251, v251, v250
AW a29,v251
AR v254,a30
v_mul_f32 v254, v254, v250
AW a30,v254
AR v251,a31
v_mul_f32 v251, v251, v250
AW a31,v251
AR v254,a56
v_mul_f32 v254, v254, v250
AW a56,v254
AR v251,a57
v_mul_f32 v251, v251, v250
AW a57,v251
AR v254,a58
v_mul_f32 v254, v254, v250
AW a58,v254
AR v251,a59
v_mul_f32 v251, v251, v250
AW a59,v251
AR v254,a60
v_mul_f32 v254, v254, v250
AW a60,v254
AR v251,a61
v_mul_f32 v251, v251, v250
AW a61,v251
AR v254,a62
v_mul_f32 v254, v254, v250
AW a62,v254
AR v251,a63
v_mul_f32 v251, v251, v250
AW a63,v251
AR v254,a88
v_mul_f32 v254, v254, v250
AW a88,v254
AR v251,a89
v_mul_f32 v251, v251, v250
AW a89,v251
AR v254,a90
v_mul_f32 v254, v254, v250
AW a90,v254
AR v251,a91
v_mul_f32 v251, v251, v250
AW a91,v251
AR v254,a92
v_mul_f32 v254, v254, v250
AW a92,v254
AR v251,a93
v_mul_f32 v251, v251, v250
AW a93,v251
AR v254,a94
v_mul_f32 v254, v254, v250
AW a94,v254
AR v251,a95
v_mul_f32 v251, v251, v250
AW a95,v251
AR v254,a120
v_mul_f32 v254, v254, v250
AW a120,v254
AR v251,a121
v_mul_f32 v251, v251, v250
AW a121,v251
AR v254,a122
v_mul_f32 v254, v254, v250
AW a122,v254
AR v251,a123
v_mul_f32 v251, v251, v250
AW a123,v251
AR v254,a124
v_mul_f32 v254, v254, v250
AW a124,v254
AR v251,a125
v_mul_f32 v251, v251, v250
AW a125,v251
AR v254,a126
v_mul_f32 v254, v254, v250
AW a126,v254
AR v251,a127
v_mul_f32 v251, v251, v250
AW a127,v251
EX v0,v0
EX v1,v1
EX v2,v2
EX v3,v3
EX v4,v4
EX v5,v5
EX v6,v6
EX v7,v7
EX v8,v8
EX v9,v9
EX v10,v10
EX v11,v11
EX v12,v12
EX v13,v13
EX v14,v14
EX v15,v15
EX v16,v16
EX v17,v17
EX v18,v18
EX v19,v19
EX v20,v20
EX v21,v21
EX v22,v22
EX v23,v23
EX v24,v24
EX v25,v25
EX v26,v26
EX v27,v27
EX v28,v28
EX v29,v29
EX v30,v30
EX v31,v31
EX v32,v32
EX v33,v33
EX v34,v34
EX v35,v35
EX v36,v36
EX v37,v37
EX v38,v38
EX v39,v39
EX v40,v40
EX v41,v41
EX v42,v42
EX v43,v43
EX v44,v44
EX v45,v45
EX v46,v46
EX v47,v47
EX v48,v48
EX v49,v49
EX v50,v50
EX v51,v51
EX v52,v52
EX v53,v53
EX v54,v54
EX v55,v55
EX v56,v56
EX v57,v57
EX v58,v58
EX v59,v59
EX v60,v60
EX v61,v61
EX v62,v62
EX v63,v63
CV v0,v0,v1
CV v1,v2,v3
CV v2,v4,v5
CV v3,v6,v7
CV v8,v8,v9
CV v9,v10,v11
CV v10,v12,v13
CV v11,v14,v15
CV v16,v16,v17
CV v17,v18,v19
CV v18,v20,v21
CV v19,v22,v23
CV v24,v24,v25
CV v25,v26,v27
CV v26,v28,v29
CV v27,v30,v31
CV v4,v32,v33
CV v5,v34,v35
CV v6,v36,v37
CV v7,v38,v39
CV v12,v40,v41
CV v13,v42,v43
CV v14,v44,v45
CV v15,v46,v47
CV v20,v48,v49
CV v21,v50,v51
CV v22,v52,v53
CV v23,v54,v55
CV v28,v56,v57
CV v29,v58,v59
CV v30,v60,v61
CV v31,v62,v63
v_smfmac_f32_16x16x64_bf16 v[236:239], v[144:147], v[0:7], v234
v_smfmac_f32_16x16x64_bf16 v[236:239], v[148:151], v[8:15], v234
v_smfmac_f32_16x16x64_bf16 v[236:239], v[152:155], v[16:23], v234
v_smfmac_f32_16x16x64_bf16 v[236:239], v[156:159], v[24:31], v234
s_nop 9
v_add_f32 v246, v236, v237
s_cmp_eq_u32 s73, 4
s_cbranch_scc1 LBACK4_%=
s_cmp_eq_u32 s73, 6
)ASM"
R"ASM(
s_cbranch_scc1 LBACK6_%=
s_cmp_eq_u32 s73, 8
s_cbranch_scc1 LBACK8_%=
s_cmp_eq_u32 s73, 10
s_cbranch_scc1 LBACK10_%=
s_cmp_eq_u32 s73, 12
s_cbranch_scc1 LBACK12_%=
s_cmp_eq_u32 s73, 14
s_cbranch_scc1 LBACK14_%=
s_cmp_eq_u32 s73, 16
s_cbranch_scc1 LBACK16_%=
s_cmp_eq_u32 s73, 18
s_cbranch_scc1 LBACK18_%=
s_cmp_eq_u32 s73, 20
s_cbranch_scc1 LBACK20_%=
s_cmp_eq_u32 s73, 22
s_cbranch_scc1 LBACK22_%=
s_cmp_eq_u32 s73, 24
s_cbranch_scc1 LBACK24_%=
s_cmp_eq_u32 s73, 26
s_cbranch_scc1 LBACK26_%=
s_cmp_eq_u32 s73, 28
s_cbranch_scc1 LBACK28_%=
s_cmp_eq_u32 s73, 30
s_cbranch_scc1 LBACK30_%=
s_endpgm
LSITE32_%=:
s_mov_b32 s73, 32
s_mov_b32 s75, 16384
s_mov_b32 s74, 0
s_branch LRARE022_%=
LSITE34_%=:
s_mov_b32 s73, 34
s_mov_b32 s75, 49152
s_mov_b32 s74, 32768
s_branch LRARE022_%=
LRARE022_%=:
s_nop 15
s_waitcnt vmcnt(0) lgkmcnt(0)
v_add_u32 v249, s74, v224
RD a[192:195],v249 offset:0
RD a[208:211],v249 offset:4096
RD a[224:227],v249 offset:8192
RD a[240:243],v249 offset:12288
v_add_u32 v250, s74, v225
RD a[196:199],v250 offset:0
RD a[212:215],v250 offset:4096
RD a[228:231],v250 offset:8192
RD a[244:247],v250 offset:12288
v_add_u32 v251, s74, v226
RD a[200:203],v251 offset:0
RD a[216:219],v251 offset:4096
RD a[232:235],v251 offset:8192
RD a[248:251],v251 offset:12288
v_add_u32 v254, s74, v227
RD a[204:207],v254 offset:0
RD a[220:223],v254 offset:4096
RD a[236:239],v254 offset:8192
RD a[252:255],v254 offset:12288
s_waitcnt lgkmcnt(0)
MM v[16:19],a[192:195],v[168:171],v[136:139]
MM v[16:19],a[196:199],v[184:187],v[16:19]
MM v[16:19],a[200:203],v[200:203],v[16:19]
MM v[16:19],a[204:207],v[216:219],v[16:19]
MM v[20:23],a[220:223],v[216:219],v[136:139]
MM v[20:23],a[216:219],v[200:203],v[20:23]
MM v[20:23],a[212:215],v[184:187],v[20:23]
MM v[20:23],a[208:211],v[168:171],v[20:23]
MM v[28:31],a[208:211],v[172:175],v[140:143]
MM v[28:31],a[212:215],v[188:191],v[28:31]
MM v[28:31],a[216:219],v[204:207],v[28:31]
MM v[28:31],a[220:223],v[220:223],v[28:31]
MM v[24:27],a[204:207],v[220:223],v[140:143]
MM v[24:27],a[200:203],v[204:207],v[24:27]
MM v[24:27],a[196:199],v[188:191],v[24:27]
MM v[24:27],a[192:195],v[172:175],v[24:27]
MM v[48:51],a[224:227],v[168:171],v[136:139]
MM v[48:51],a[228:231],v[184:187],v[48:51]
MM v[48:51],a[232:235],v[200:203],v[48:51]
MM v[48:51],a[236:239],v[216:219],v[48:51]
MM v[52:55],a[252:255],v[216:219],v[136:139]
MM v[52:55],a[248:251],v[200:203],v[52:55]
MM v[52:55],a[244:247],v[184:187],v[52:55]
MM v[52:55],a[240:243],v[168:171],v[52:55]
MM v[60:63],a[240:243],v[172:175],v[140:143]
MM v[60:63],a[244:247],v[188:191],v[60:63]
MM v[60:63],a[248:251],v[204:207],v[60:63]
MM v[60:63],a[252:255],v[220:223],v[60:63]
MM v[56:59],a[236:239],v[220:223],v[140:143]
MM v[56:59],a[232:235],v[204:207],v[56:59]
MM v[56:59],a[228:231],v[188:191],v[56:59]
MM v[56:59],a[224:227],v[172:175],v[56:59]
s_nop 15
v_add_u32 v249, s75, v224
RD a[192:195],v249 offset:0
RD a[208:211],v249 offset:4096
RD a[224:227],v249 offset:8192
RD a[240:243],v249 offset:12288
v_add_u32 v250, s75, v225
RD a[196:199],v250 offset:0
RD a[212:215],v250 offset:4096
RD a[228:231],v250 offset:8192
RD a[244:247],v250 offset:12288
v_add_u32 v251, s75, v226
RD a[200:203],v251 offset:0
RD a[216:219],v251 offset:4096
RD a[232:235],v251 offset:8192
RD a[248:251],v251 offset:12288
v_add_u32 v254, s75, v227
RD a[204:207],v254 offset:0
RD a[220:223],v254 offset:4096
RD a[236:239],v254 offset:8192
RD a[252:255],v254 offset:12288
s_waitcnt lgkmcnt(0)
v_mov_b32 v236, v247
v_mov_b32 v237, 0
v_max3_f32 v249, v16, v17, v18
v_max3_f32 v251, v48, v49, v50
v_max3_f32 v249, v249, v19, v20
v_max3_f32 v251, v251, v51, v52
v_max3_f32 v249, v249, v21, v22
v_max3_f32 v251, v251, v53, v54
v_max3_f32 v249, v249, v23, v55
v_max_f32 v249, v249, v251
v_log_f32 v250, v247
s_lshl_b64 exec, 0xffff, 32
v_max_f32 v249, v249, v250
s_mov_b64 exec, -1
v_mov_b32 v251, v249
s_nop 1
v_permlane16_swap_b32 v249, v251
v_max_f32 v249, v249, v251
v_mov_b32 v251, v249
s_nop 1
v_permlane32_swap_b32 v249, v251
v_max_f32 v249, v249, v251
v_max_f32 v249, 0, v249
v_sub_f32 v250, 0, v249
EX v250,v250
v_sub_f32 v136, v136, v249
v_sub_f32 v137, v137, v249
v_sub_f32 v138, v138, v249
v_sub_f32 v139, v139, v249
v_sub_f32 v80, v80, v249
v_sub_f32 v16, v16, v249
v_sub_f32 v81, v81, v249
v_sub_f32 v17, v17, v249
v_sub_f32 v82, v82, v249
v_sub_f32 v18, v18, v249
v_sub_f32 v83, v83, v249
v_sub_f32 v19, v19, v249
v_sub_f32 v84, v84, v249
v_sub_f32 v20, v20, v249
v_sub_f32 v85, v85, v249
v_sub_f32 v21, v21, v249
v_sub_f32 v86, v86, v249
v_sub_f32 v22, v22, v249
v_sub_f32 v87, v87, v249
v_sub_f32 v23, v23, v249
v_sub_f32 v112, v112, v249
v_sub_f32 v48, v48, v249
v_sub_f32 v113, v113, v249
v_sub_f32 v49, v49, v249
v_sub_f32 v114, v114, v249
v_sub_f32 v50, v50, v249
v_sub_f32 v115, v115, v249
v_sub_f32 v51, v51, v249
v_sub_f32 v116, v116, v249
v_sub_f32 v52, v52, v249
v_sub_f32 v117, v117, v249
v_sub_f32 v53, v53, v249
v_sub_f32 v118, v118, v249
v_sub_f32 v54, v54, v249
v_sub_f32 v119, v119, v249
v_sub_f32 v55, v55, v249
s_lshl_b64 exec, 0xffff, 32
v_mul_f32 v236, v236, v250
s_mov_b64 exec, -1
AR v254,a16
v_mul_f32 v254, v254, v250
AW a16,v254
AR v251,a17
v_mul_f32 v251, v251, v250
AW a17,v251
AR v254,a18
v_mul_f32 v254, v254, v250
AW a18,v254
AR v251,a19
v_mul_f32 v251, v251, v250
AW a19,v251
AR v254,a20
v_mul_f32 v254, v254, v250
AW a20,v254
AR v251,a21
v_mul_f32 v251, v251, v250
AW a21,v251
AR v254,a22
v_mul_f32 v254, v254, v250
AW a22,v254
AR v251,a23
v_mul_f32 v251, v251, v250
AW a23,v251
AR v254,a48
v_mul_f32 v254, v254, v250
AW a48,v254
AR v251,a49
v_mul_f32 v251, v251, v250
AW a49,v251
AR v254,a50
v_mul_f32 v254, v254, v250
AW a50,v254
AR v251,a51
v_mul_f32 v251, v251, v250
AW a51,v251
AR v254,a52
v_mul_f32 v254, v254, v250
AW a52,v254
AR v251,a53
v_mul_f32 v251, v251, v250
AW a53,v251
AR v254,a54
v_mul_f32 v254, v254, v250
AW a54,v254
AR v251,a55
v_mul_f32 v251, v251, v250
AW a55,v251
AR v254,a80
v_mul_f32 v254, v254, v250
AW a80,v254
AR v251,a81
v_mul_f32 v251, v251, v250
AW a81,v251
AR v254,a82
v_mul_f32 v254, v254, v250
AW a82,v254
AR v251,a83
v_mul_f32 v251, v251, v250
AW a83,v251
AR v254,a84
v_mul_f32 v254, v254, v250
AW a84,v254
AR v251,a85
v_mul_f32 v251, v251, v250
AW a85,v251
AR v254,a86
v_mul_f32 v254, v254, v250
AW a86,v254
AR v251,a87
v_mul_f32 v251, v251, v250
AW a87,v251
AR v254,a112
v_mul_f32 v254, v254, v250
AW a112,v254
AR v251,a113
v_mul_f32 v251, v251, v250
AW a113,v251
AR v254,a114
v_mul_f32 v254, v254, v250
AW a114,v254
AR v251,a115
v_mul_f32 v251, v251, v250
AW a115,v251
AR v254,a116
v_mul_f32 v254, v254, v250
AW a116,v254
AR v251,a117
v_mul_f32 v251, v251, v250
AW a117,v251
AR v254,a118
v_mul_f32 v254, v254, v250
AW a118,v254
AR v251,a119
v_mul_f32 v251, v251, v250
AW a119,v251
v_max3_f32 v249, v24, v25, v26
v_max3_f32 v251, v56, v57, v58
v_max3_f32 v249, v249, v27, v28
v_max3_f32 v251, v251, v59, v60
v_max3_f32 v249, v249, v29, v30
v_max3_f32 v251, v251, v61, v62
v_max3_f32 v249, v249, v31, v63
v_max_f32 v249, v249, v251
v_log_f32 v250, v247
s_lshl_b64 exec, 0xffff, 48
v_max_f32 v249, v249, v250
s_mov_b64 exec, -1
v_mov_b32 v251, v249
s_nop 1
v_permlane16_swap_b32 v249, v251
v_max_f32 v249, v249, v251
v_mov_b32 v251, v249
s_nop 1
v_permlane32_swap_b32 v249, v251
v_max_f32 v249, v249, v251
v_max_f32 v249, 0, v249
v_sub_f32 v250, 0, v249
EX v250,v250
v_sub_f32 v140, v140, v249
v_sub_f32 v141, v141, v249
v_sub_f32 v142, v142, v249
v_sub_f32 v143, v143, v249
v_sub_f32 v88, v88, v249
v_sub_f32 v24, v24, v249
v_sub_f32 v89, v89, v249
v_sub_f32 v25, v25, v249
v_sub_f32 v90, v90, v249
v_sub_f32 v26, v26, v249
v_sub_f32 v91, v91, v249
v_sub_f32 v27, v27, v249
v_sub_f32 v92, v92, v249
v_sub_f32 v28, v28, v249
v_sub_f32 v93, v93, v249
v_sub_f32 v29, v29, v249
v_sub_f32 v94, v94, v249
v_sub_f32 v30, v30, v249
v_sub_f32 v95, v95, v249
v_sub_f32 v31, v31, v249
v_sub_f32 v120, v120, v249
v_sub_f32 v56, v56, v249
v_sub_f32 v121, v121, v249
v_sub_f32 v57, v57, v249
v_sub_f32 v122, v122, v249
v_sub_f32 v58, v58, v249
v_sub_f32 v123, v123, v249
v_sub_f32 v59, v59, v249
v_sub_f32 v124, v124, v249
v_sub_f32 v60, v60, v249
v_sub_f32 v125, v125, v249
v_sub_f32 v61, v61, v249
v_sub_f32 v126, v126, v249
v_sub_f32 v62, v62, v249
v_sub_f32 v127, v127, v249
v_sub_f32 v63, v63, v249
s_lshl_b64 exec, 0xffff, 48
v_mul_f32 v236, v236, v250
s_mov_b64 exec, -1
AR v254,a24
v_mul_f32 v254, v254, v250
AW a24,v254
AR v251,a25
v_mul_f32 v251, v251, v250
AW a25,v251
AR v254,a26
v_mul_f32 v254, v254, v250
AW a26,v254
AR v251,a27
v_mul_f32 v251, v251, v250
AW a27,v251
AR v254,a28
v_mul_f32 v254, v254, v250
AW a28,v254
AR v251,a29
v_mul_f32 v251, v251, v250
AW a29,v251
AR v254,a30
v_mul_f32 v254, v254, v250
AW a30,v254
AR v251,a31
v_mul_f32 v251, v251, v250
AW a31,v251
AR v254,a56
v_mul_f32 v254, v254, v250
AW a56,v254
AR v251,a57
v_mul_f32 v251, v251, v250
AW a57,v251
AR v254,a58
v_mul_f32 v254, v254, v250
AW a58,v254
AR v251,a59
v_mul_f32 v251, v251, v250
AW a59,v251
AR v254,a60
v_mul_f32 v254, v254, v250
AW a60,v254
AR v251,a61
v_mul_f32 v251, v251, v250
AW a61,v251
AR v254,a62
v_mul_f32 v254, v254, v250
AW a62,v254
AR v251,a63
v_mul_f32 v251, v251, v250
AW a63,v251
AR v254,a88
v_mul_f32 v254, v254, v250
AW a88,v254
AR v251,a89
v_mul_f32 v251, v251, v250
AW a89,v251
AR v254,a90
v_mul_f32 v254, v254, v250
AW a90,v254
AR v251,a91
v_mul_f32 v251, v251, v250
AW a91,v251
AR v254,a92
v_mul_f32 v254, v254, v250
AW a92,v254
AR v251,a93
v_mul_f32 v251, v251, v250
AW a93,v251
AR v254,a94
v_mul_f32 v254, v254, v250
AW a94,v254
AR v251,a95
v_mul_f32 v251, v251, v250
AW a95,v251
AR v254,a120
v_mul_f32 v254, v254, v250
AW a120,v254
AR v251,a121
v_mul_f32 v251, v251, v250
AW a121,v251
AR v254,a122
v_mul_f32 v254, v254, v250
AW a122,v254
AR v251,a123
v_mul_f32 v251, v251, v250
AW a123,v251
AR v254,a124
v_mul_f32 v254, v254, v250
AW a124,v254
AR v251,a125
v_mul_f32 v251, v251, v250
AW a125,v251
AR v254,a126
v_mul_f32 v254, v254, v250
AW a126,v254
AR v251,a127
v_mul_f32 v251, v251, v250
AW a127,v251
EX v16,v16
EX v17,v17
EX v18,v18
EX v19,v19
EX v20,v20
EX v21,v21
EX v22,v22
EX v23,v23
EX v24,v24
EX v25,v25
EX v26,v26
EX v27,v27
EX v28,v28
EX v29,v29
EX v30,v30
EX v31,v31
EX v48,v48
EX v49,v49
EX v50,v50
EX v51,v51
EX v52,v52
EX v53,v53
EX v54,v54
EX v55,v55
EX v56,v56
EX v57,v57
EX v58,v58
EX v59,v59
EX v60,v60
EX v61,v61
EX v62,v62
EX v63,v63
CV v16,v16,v17
CV v17,v18,v19
CV v18,v20,v21
CV v19,v22,v23
CV v24,v24,v25
CV v25,v26,v27
CV v26,v28,v29
CV v27,v30,v31
CV v20,v48,v49
CV v21,v50,v51
CV v22,v52,v53
CV v23,v54,v55
CV v28,v56,v57
CV v29,v58,v59
CV v30,v60,v61
CV v31,v62,v63
v_smfmac_f32_16x16x64_bf16 v[236:239], v[152:155], v[16:23], v234
s_nop 0
v_smfmac_f32_16x16x64_bf16 v[236:239], v[156:159], v[24:31], v234
s_nop 9
v_add_f32 v246, v236, v237
s_cmp_eq_u32 s73, 32
s_cbranch_scc1 LBACK32_%=
s_cmp_eq_u32 s73, 34
s_cbranch_scc1 LBACK34_%=
s_endpgm
LSITE33_%=:
s_mov_b32 s73, 33
s_mov_b32 s75, 32768
s_mov_b32 s74, 16384
s_branch LRARE122_%=
LRARE122_%=:
s_nop 15
s_waitcnt vmcnt(0) lgkmcnt(0)
v_add_u32 v249, s74, v224
RD a[192:195],v249 offset:0
RD a[208:211],v249 offset:4096
RD a[224:227],v249 offset:8192
RD a[240:243],v249 offset:12288
v_add_u32 v250, s74, v225
RD a[196:199],v250 offset:0
RD a[212:215],v250 offset:4096
RD a[228:231],v250 offset:8192
RD a[244:247],v250 offset:12288
v_add_u32 v251, s74, v226
RD a[200:203],v251 offset:0
RD a[216:219],v251 offset:4096
RD a[232:235],v251 offset:8192
RD a[248:251],v251 offset:12288
v_add_u32 v254, s74, v227
RD a[204:207],v254 offset:0
RD a[220:223],v254 offset:4096
RD a[236:239],v254 offset:8192
RD a[252:255],v254 offset:12288
s_waitcnt lgkmcnt(0)
MM v[80:83],a[192:195],v[168:171],v[136:139]
MM v[80:83],a[196:199],v[184:187],v[80:83]
MM v[80:83],a[200:203],v[200:203],v[80:83]
MM v[80:83],a[204:207],v[216:219],v[80:83]
MM v[84:87],a[220:223],v[216:219],v[136:139]
MM v[84:87],a[216:219],v[200:203],v[84:87]
MM v[84:87],a[212:215],v[184:187],v[84:87]
MM v[84:87],a[208:211],v[168:171],v[84:87]
MM v[92:95],a[208:211],v[172:175],v[140:143]
MM v[92:95],a[212:215],v[188:191],v[92:95]
)ASM"
R"ASM(
MM v[92:95],a[216:219],v[204:207],v[92:95]
MM v[92:95],a[220:223],v[220:223],v[92:95]
MM v[88:91],a[204:207],v[220:223],v[140:143]
MM v[88:91],a[200:203],v[204:207],v[88:91]
MM v[88:91],a[196:199],v[188:191],v[88:91]
MM v[88:91],a[192:195],v[172:175],v[88:91]
MM v[112:115],a[224:227],v[168:171],v[136:139]
MM v[112:115],a[228:231],v[184:187],v[112:115]
MM v[112:115],a[232:235],v[200:203],v[112:115]
MM v[112:115],a[236:239],v[216:219],v[112:115]
MM v[116:119],a[252:255],v[216:219],v[136:139]
MM v[116:119],a[248:251],v[200:203],v[116:119]
MM v[116:119],a[244:247],v[184:187],v[116:119]
MM v[116:119],a[240:243],v[168:171],v[116:119]
MM v[124:127],a[240:243],v[172:175],v[140:143]
MM v[124:127],a[244:247],v[188:191],v[124:127]
MM v[124:127],a[248:251],v[204:207],v[124:127]
MM v[124:127],a[252:255],v[220:223],v[124:127]
MM v[120:123],a[236:239],v[220:223],v[140:143]
MM v[120:123],a[232:235],v[204:207],v[120:123]
MM v[120:123],a[228:231],v[188:191],v[120:123]
MM v[120:123],a[224:227],v[172:175],v[120:123]
s_nop 15
v_add_u32 v249, s75, v224
RD a[192:195],v249 offset:0
RD a[208:211],v249 offset:4096
RD a[224:227],v249 offset:8192
RD a[240:243],v249 offset:12288
v_add_u32 v250, s75, v225
RD a[196:199],v250 offset:0
RD a[212:215],v250 offset:4096
RD a[228:231],v250 offset:8192
RD a[244:247],v250 offset:12288
v_add_u32 v251, s75, v226
RD a[200:203],v251 offset:0
RD a[216:219],v251 offset:4096
RD a[232:235],v251 offset:8192
RD a[248:251],v251 offset:12288
v_add_u32 v254, s75, v227
RD a[204:207],v254 offset:0
RD a[220:223],v254 offset:4096
RD a[236:239],v254 offset:8192
RD a[252:255],v254 offset:12288
s_waitcnt lgkmcnt(0)
v_mov_b32 v236, v246
v_mov_b32 v237, 0
v_max3_f32 v249, v80, v81, v82
v_max3_f32 v251, v112, v113, v114
v_max3_f32 v249, v249, v83, v84
v_max3_f32 v251, v251, v115, v116
v_max3_f32 v249, v249, v85, v86
v_max3_f32 v251, v251, v117, v118
v_max3_f32 v249, v249, v87, v119
v_max_f32 v249, v249, v251
v_log_f32 v250, v246
s_lshl_b64 exec, 0xffff, 32
v_max_f32 v249, v249, v250
s_mov_b64 exec, -1
v_mov_b32 v251, v249
s_nop 1
v_permlane16_swap_b32 v249, v251
v_max_f32 v249, v249, v251
v_mov_b32 v251, v249
s_nop 1
v_permlane32_swap_b32 v249, v251
v_max_f32 v249, v249, v251
v_max_f32 v249, 0, v249
v_sub_f32 v250, 0, v249
EX v250,v250
v_sub_f32 v136, v136, v249
v_sub_f32 v137, v137, v249
v_sub_f32 v138, v138, v249
v_sub_f32 v139, v139, v249
v_sub_f32 v16, v16, v249
v_sub_f32 v80, v80, v249
v_sub_f32 v17, v17, v249
v_sub_f32 v81, v81, v249
v_sub_f32 v18, v18, v249
v_sub_f32 v82, v82, v249
v_sub_f32 v19, v19, v249
v_sub_f32 v83, v83, v249
v_sub_f32 v20, v20, v249
v_sub_f32 v84, v84, v249
v_sub_f32 v21, v21, v249
v_sub_f32 v85, v85, v249
v_sub_f32 v22, v22, v249
v_sub_f32 v86, v86, v249
v_sub_f32 v23, v23, v249
v_sub_f32 v87, v87, v249
v_sub_f32 v48, v48, v249
v_sub_f32 v112, v112, v249
v_sub_f32 v49, v49, v249
v_sub_f32 v113, v113, v249
v_sub_f32 v50, v50, v249
v_sub_f32 v114, v114, v249
v_sub_f32 v51, v51, v249
v_sub_f32 v115, v115, v249
v_sub_f32 v52, v52, v249
v_sub_f32 v116, v116, v249
v_sub_f32 v53, v53, v249
v_sub_f32 v117, v117, v249
v_sub_f32 v54, v54, v249
v_sub_f32 v118, v118, v249
v_sub_f32 v55, v55, v249
v_sub_f32 v119, v119, v249
s_lshl_b64 exec, 0xffff, 32
v_mul_f32 v236, v236, v250
s_mov_b64 exec, -1
AR v254,a16
v_mul_f32 v254, v254, v250
AW a16,v254
AR v251,a17
v_mul_f32 v251, v251, v250
AW a17,v251
AR v254,a18
v_mul_f32 v254, v254, v250
AW a18,v254
AR v251,a19
v_mul_f32 v251, v251, v250
AW a19,v251
AR v254,a20
v_mul_f32 v254, v254, v250
AW a20,v254
AR v251,a21
v_mul_f32 v251, v251, v250
AW a21,v251
AR v254,a22
v_mul_f32 v254, v254, v250
AW a22,v254
AR v251,a23
v_mul_f32 v251, v251, v250
AW a23,v251
AR v254,a48
v_mul_f32 v254, v254, v250
AW a48,v254
AR v251,a49
v_mul_f32 v251, v251, v250
AW a49,v251
AR v254,a50
v_mul_f32 v254, v254, v250
AW a50,v254
AR v251,a51
v_mul_f32 v251, v251, v250
AW a51,v251
AR v254,a52
v_mul_f32 v254, v254, v250
AW a52,v254
AR v251,a53
v_mul_f32 v251, v251, v250
AW a53,v251
AR v254,a54
v_mul_f32 v254, v254, v250
AW a54,v254
AR v251,a55
v_mul_f32 v251, v251, v250
AW a55,v251
AR v254,a80
v_mul_f32 v254, v254, v250
AW a80,v254
AR v251,a81
v_mul_f32 v251, v251, v250
AW a81,v251
AR v254,a82
v_mul_f32 v254, v254, v250
AW a82,v254
AR v251,a83
v_mul_f32 v251, v251, v250
AW a83,v251
AR v254,a84
v_mul_f32 v254, v254, v250
AW a84,v254
AR v251,a85
v_mul_f32 v251, v251, v250
AW a85,v251
AR v254,a86
v_mul_f32 v254, v254, v250
AW a86,v254
AR v251,a87
v_mul_f32 v251, v251, v250
AW a87,v251
AR v254,a112
v_mul_f32 v254, v254, v250
AW a112,v254
AR v251,a113
v_mul_f32 v251, v251, v250
AW a113,v251
AR v254,a114
v_mul_f32 v254, v254, v250
AW a114,v254
AR v251,a115
v_mul_f32 v251, v251, v250
AW a115,v251
AR v254,a116
v_mul_f32 v254, v254, v250
AW a116,v254
AR v251,a117
v_mul_f32 v251, v251, v250
AW a117,v251
AR v254,a118
v_mul_f32 v254, v254, v250
AW a118,v254
AR v251,a119
v_mul_f32 v251, v251, v250
AW a119,v251
v_max3_f32 v249, v88, v89, v90
v_max3_f32 v251, v120, v121, v122
v_max3_f32 v249, v249, v91, v92
v_max3_f32 v251, v251, v123, v124
v_max3_f32 v249, v249, v93, v94
v_max3_f32 v251, v251, v125, v126
v_max3_f32 v249, v249, v95, v127
v_max_f32 v249, v249, v251
v_log_f32 v250, v246
s_lshl_b64 exec, 0xffff, 48
v_max_f32 v249, v249, v250
s_mov_b64 exec, -1
v_mov_b32 v251, v249
s_nop 1
v_permlane16_swap_b32 v249, v251
v_max_f32 v249, v249, v251
v_mov_b32 v251, v249
s_nop 1
v_permlane32_swap_b32 v249, v251
v_max_f32 v249, v249, v251
v_max_f32 v249, 0, v249
v_sub_f32 v250, 0, v249
EX v250,v250
v_sub_f32 v140, v140, v249
v_sub_f32 v141, v141, v249
v_sub_f32 v142, v142, v249
v_sub_f32 v143, v143, v249
v_sub_f32 v24, v24, v249
v_sub_f32 v88, v88, v249
v_sub_f32 v25, v25, v249
v_sub_f32 v89, v89, v249
v_sub_f32 v26, v26, v249
v_sub_f32 v90, v90, v249
v_sub_f32 v27, v27, v249
v_sub_f32 v91, v91, v249
v_sub_f32 v28, v28, v249
v_sub_f32 v92, v92, v249
v_sub_f32 v29, v29, v249
v_sub_f32 v93, v93, v249
v_sub_f32 v30, v30, v249
v_sub_f32 v94, v94, v249
v_sub_f32 v31, v31, v249
v_sub_f32 v95, v95, v249
v_sub_f32 v56, v56, v249
v_sub_f32 v120, v120, v249
v_sub_f32 v57, v57, v249
v_sub_f32 v121, v121, v249
v_sub_f32 v58, v58, v249
v_sub_f32 v122, v122, v249
v_sub_f32 v59, v59, v249
v_sub_f32 v123, v123, v249
v_sub_f32 v60, v60, v249
v_sub_f32 v124, v124, v249
v_sub_f32 v61, v61, v249
v_sub_f32 v125, v125, v249
v_sub_f32 v62, v62, v249
v_sub_f32 v126, v126, v249
v_sub_f32 v63, v63, v249
v_sub_f32 v127, v127, v249
s_lshl_b64 exec, 0xffff, 48
v_mul_f32 v236, v236, v250
s_mov_b64 exec, -1
AR v254,a24
v_mul_f32 v254, v254, v250
AW a24,v254
AR v251,a25
v_mul_f32 v251, v251, v250
AW a25,v251
AR v254,a26
v_mul_f32 v254, v254, v250
AW a26,v254
AR v251,a27
v_mul_f32 v251, v251, v250
AW a27,v251
AR v254,a28
v_mul_f32 v254, v254, v250
AW a28,v254
AR v251,a29
v_mul_f32 v251, v251, v250
AW a29,v251
AR v254,a30
v_mul_f32 v254, v254, v250
AW a30,v254
AR v251,a31
v_mul_f32 v251, v251, v250
AW a31,v251
AR v254,a56
v_mul_f32 v254, v254, v250
AW a56,v254
AR v251,a57
v_mul_f32 v251, v251, v250
AW a57,v251
AR v254,a58
v_mul_f32 v254, v254, v250
AW a58,v254
AR v251,a59
v_mul_f32 v251, v251, v250
AW a59,v251
AR v254,a60
v_mul_f32 v254, v254, v250
AW a60,v254
AR v251,a61
v_mul_f32 v251, v251, v250
AW a61,v251
AR v254,a62
v_mul_f32 v254, v254, v250
AW a62,v254
AR v251,a63
v_mul_f32 v251, v251, v250
AW a63,v251
AR v254,a88
v_mul_f32 v254, v254, v250
AW a88,v254
AR v251,a89
v_mul_f32 v251, v251, v250
AW a89,v251
AR v254,a90
v_mul_f32 v254, v254, v250
AW a90,v254
AR v251,a91
v_mul_f32 v251, v251, v250
AW a91,v251
AR v254,a92
v_mul_f32 v254, v254, v250
AW a92,v254
AR v251,a93
v_mul_f32 v251, v251, v250
AW a93,v251
AR v254,a94
v_mul_f32 v254, v254, v250
AW a94,v254
AR v251,a95
v_mul_f32 v251, v251, v250
AW a95,v251
AR v254,a120
v_mul_f32 v254, v254, v250
AW a120,v254
AR v251,a121
v_mul_f32 v251, v251, v250
AW a121,v251
AR v254,a122
v_mul_f32 v254, v254, v250
AW a122,v254
AR v251,a123
v_mul_f32 v251, v251, v250
AW a123,v251
AR v254,a124
v_mul_f32 v254, v254, v250
AW a124,v254
AR v251,a125
v_mul_f32 v251, v251, v250
AW a125,v251
AR v254,a126
v_mul_f32 v254, v254, v250
AW a126,v254
AR v251,a127
v_mul_f32 v251, v251, v250
AW a127,v251
EX v80,v80
EX v81,v81
EX v82,v82
EX v83,v83
EX v84,v84
EX v85,v85
EX v86,v86
EX v87,v87
EX v88,v88
EX v89,v89
EX v90,v90
EX v91,v91
EX v92,v92
EX v93,v93
EX v94,v94
EX v95,v95
EX v112,v112
EX v113,v113
EX v114,v114
EX v115,v115
EX v116,v116
EX v117,v117
EX v118,v118
EX v119,v119
EX v120,v120
EX v121,v121
EX v122,v122
EX v123,v123
EX v124,v124
EX v125,v125
EX v126,v126
EX v127,v127
CV v80,v80,v81
CV v81,v82,v83
CV v82,v84,v85
CV v83,v86,v87
CV v88,v88,v89
CV v89,v90,v91
CV v90,v92,v93
CV v91,v94,v95
CV v84,v112,v113
CV v85,v114,v115
CV v86,v116,v117
CV v87,v118,v119
CV v92,v120,v121
CV v93,v122,v123
CV v94,v124,v125
CV v95,v126,v127
v_smfmac_f32_16x16x64_bf16 v[236:239], v[152:155], v[80:87], v234
s_nop 0
v_smfmac_f32_16x16x64_bf16 v[236:239], v[156:159], v[88:95], v234
s_nop 9
v_add_f32 v247, v236, v237
s_cmp_eq_u32 s73, 33
s_cbranch_scc1 LBACK33_%=
s_endpgm
LEND_%=:
)ASM"

    :: [ka] "s"(__builtin_amdgcn_kernarg_segment_ptr()), [slds] "s"(slds), [swave] "s"(w), [sb] "s"(blockIdx.x)
    : "memory", "vcc", "scc", CLOB_V, CLOB_A, CLOB_S);
}

extern "C" void kernel_launch(void* const* d_in, const int* in_sizes, int n_in, void* d_out, int out_size, void* d_ws, size_t ws_size, hipStream_t stream) {
  hipFuncSetAttribute((const void*)ka_fused, hipFuncAttributeMaxDynamicSharedMemorySize, 163840);
  ka_fused<<<256, 256, 163840, stream>>>((const unsigned short*)d_in[0], (const unsigned short*)d_in[1], (const unsigned short*)d_in[2], (const unsigned short*)d_in[3],
                                         (const unsigned short*)d_in[4], (const unsigned short*)d_in[6], (const unsigned short*)d_in[7], (const unsigned short*)d_in[8],
                                         (const unsigned short*)d_in[9], (char*)d_ws, (unsigned short*)d_out
                                         );
}
```

```cpp
#include <hip/hip_runtime.h>
#include <stdint.h>

using f32x4  = __attribute__((ext_vector_type(4))) float;
using u32x4  = __attribute__((ext_vector_type(4))) unsigned;
using u32x2  = __attribute__((ext_vector_type(2))) unsigned;

constexpr int SEQ = 2048, DM = 640, NH = 32, DH = 128, HD = NH * DH, FF = 1728, TOPC = 8;

constexpr unsigned WS_FO  = 0;
constexpr unsigned WS_EP  = 32768;
constexpr unsigned WS_G1  = 36864;
constexpr unsigned WS_G2  = 53248;
constexpr unsigned WS_FH  = 69632;
constexpr unsigned WS_FY  = 73728;
constexpr unsigned WS_FG  = 77824;
constexpr unsigned WS_FR  = 81920;
constexpr unsigned WS_IX  = 86016;
constexpr unsigned WS_XN  = 131072;
constexpr unsigned WS_OF  = WS_XN + SEQ * DM * 2;
constexpr unsigned WS_H   = WS_OF + (18u << 20);
constexpr unsigned WS_Y   = WS_H + 32768;
constexpr unsigned WS_P   = WS_Y + 1048576;
constexpr unsigned WS_SPAN = 64u << 20;
constexpr int DN_FIRST = 76, YP = 18, SPIN = 30000;

#define CLOB_V "v0","v1","v2","v3","v4","v5","v6","v7","v8","v9","v10","v11","v12","v13","v14","v15","v16","v17","v18","v19","v20","v21","v22","v23","v24","v25","v26","v27","v28","v29","v30","v31","v32","v33","v34","v35","v36","v37","v38","v39","v40","v41","v42","v43","v44","v45","v46","v47","v48","v49","v50","v51","v52","v53","v54","v55","v56","v57","v58","v59","v60","v61","v62","v63","v64","v65","v66","v67","v68","v69","v70","v71","v72","v73","v74","v75","v76","v77","v78","v79","v80","v81","v82","v83","v84","v85","v86","v87","v88","v89","v90","v91","v92","v93","v94","v95","v96","v97","v98","v99","v100","v101","v102","v103","v104","v105","v106","v107","v108","v109","v110","v111","v112","v113","v114","v115","v116","v117","v118","v119","v120","v121","v122","v123","v124","v125","v126","v127","v128","v129","v130","v131","v132","v133","v134","v135","v136","v137","v138","v139","v140","v141","v142","v143","v144","v145","v146","v147","v148","v149","v150","v151","v152","v153","v154","v155","v156","v157","v158","v159","v160","v161","v162","v163","v164","v165","v166","v167","v168","v169","v170","v171","v172","v173","v174","v175","v176","v177","v178","v179","v180","v181","v182","v183","v184","v185","v186","v187","v188","v189","v190","v191","v192","v193","v194","v195","v196","v197","v198","v199","v200","v201","v202","v203","v204","v205","v206","v207","v208","v209","v210","v211","v212","v213","v214","v215","v216","v217","v218","v219","v220","v221","v222","v223","v224","v225","v226","v227","v228","v229","v230","v231","v232","v233","v234","v235","v236","v237","v238","v239","v240","v241","v242","v243","v244","v245","v246","v247","v248","v249","v250","v251","v252","v253","v254","v255"
#define CLOB_A "a0","a1","a2","a3","a4","a5","a6","a7","a8","a9","a10","a11","a12","a13","a14","a15","a16","a17","a18","a19","a20","a21","a22","a23","a24","a25","a26","a27","a28","a29","a30","a31","a32","a33","a34","a35","a36","a37","a38","a39","a40","a41","a42","a43","a44","a45","a46","a47","a48","a49","a50","a51","a52","a53","a54","a55","a56","a57","a58","a59","a60","a61","a62","a63","a64","a65","a66","a67","a68","a69","a70","a71","a72","a73","a74","a75","a76","a77","a78","a79","a80","a81","a82","a83","a84","a85","a86","a87","a88","a89","a90","a91","a92","a93","a94","a95","a96","a97","a98","a99","a100","a101","a102","a103","a104","a105","a106","a107","a108","a109","a110","a111","a112","a113","a114","a115","a116","a117","a118","a119","a120","a121","a122","a123","a124","a125","a126","a127","a128","a129","a130","a131","a132","a133","a134","a135","a136","a137","a138","a139","a140","a141","a142","a143","a144","a145","a146","a147","a148","a149","a150","a151","a152","a153","a154","a155","a156","a157","a158","a159","a160","a161","a162","a163","a164","a165","a166","a167","a168","a169","a170","a171","a172","a173","a174","a175","a176","a177","a178","a179","a180","a181","a182","a183","a184","a185","a186","a187","a188","a189","a190","a191","a192","a193","a194","a195","a196","a197","a198","a199","a200","a201","a202","a203","a204","a205","a206","a207","a208","a209","a210","a211","a212","a213","a214","a215","a216","a217","a218","a219","a220","a221","a222","a223","a224","a225","a226","a227","a228","a229","a230","a231","a232","a233","a234","a235","a236","a237","a238","a239","a240","a241","a242","a243","a244","a245","a246","a247","a248","a249","a250","a251","a252","a253","a254","a255"
#define CLOB_S "s8","s9","s10","s11","s12","s13","s14","s15","s16","s17","s18","s19","s20","s21","s22","s23","s24","s25","s26","s27","s28","s29","s30","s31","s32","s33","s34","s35","s36","s37","s38","s39","s40","s41","s42","s43","s44","s45","s46","s47","s48","s49","s50","s51","s52","s53","s54","s55","s56","s57","s58","s59","s60","s61","s62","s63","s64","s65","s66","s67","s68","s69","s70","s71","s72","s73","s74","s75","s76","s77","s78","s79","s80","s81","s82","s83","s84","s85","s86","s87","s88","s89","s90","s91","s92","s93","s94","s95","s96","s97","s98","s99"
__global__ __launch_bounds__(256, 1) __attribute__((aligned(4096))) void ka_fused(const unsigned short* __restrict__ x, const unsigned short* __restrict__ Kc, const unsigned short* __restrict__ Vc,
                                                   const unsigned short* __restrict__ wq, const unsigned short* __restrict__ wo,
                                                   const unsigned short* __restrict__ wr, const unsigned short* __restrict__ wg, const unsigned short* __restrict__ wu,
                                                   const unsigned short* __restrict__ wd,
                                                   char* __restrict__ ws, unsigned short* __restrict__ out
                                                   ) {
  extern __shared__ __attribute__((aligned(16))) char lds[];
  const unsigned w = __builtin_amdgcn_readfirstlane(threadIdx.x >> 6);
  const unsigned slds = __builtin_amdgcn_readfirstlane((unsigned)(size_t)lds);
  asm volatile(
R"ASM(
.macro MM a, b, c, d:vararg
v_mfma_f32_16x16x32_bf16 \a, \b, \c, \d
.endm
.macro TR a, b:vararg
ds_read_b64_tr_b16 \a, \b
.endm
.macro CV a, b, c:vararg
v_cvt_pk_bf16_f32 \a, \b, \c
.endm
.macro AR a, b:vararg
v_accvgpr_read_b32 \a, \b
.endm
.macro AW a, b:vararg
v_accvgpr_write_b32 \a, \b
.endm
.macro EX a, b:vararg
v_exp_f32 \a, \b
.endm
.macro RD a, b:vararg
ds_read_b128 \a, \b
.endm
s_branch LPG0_%=
.p2align 6
LPG0_%=:
s_load_dwordx2 s[16:17], %[ka], 24
s_load_dwordx2 s[32:33], %[ka], 0
s_load_dwordx2 s[20:21], %[ka], 72
s_load_dwordx2 s[8:9], %[ka], 8
s_load_dwordx2 s[12:13], %[ka], 16
s_load_dwordx2 s[24:25], %[ka], 32
s_load_dwordx2 s[44:45], %[ka], 40
s_load_dwordx2 s[28:29], %[ka], 64
s_load_dwordx2 s[40:41], %[ka], 80
s_lshl_b32 s77, %[swave], 2
s_and_b32 s77, s77, 8
s_add_u32 s77, s77, 48
s_load_dwordx2 s[36:37], %[ka], s77
v_mbcnt_lo_u32_b32 v127, -1, 0
v_mbcnt_hi_u32_b32 v127, -1, v127
v_lshlrev_b32 v248, 4, v127
s_lshr_b32 s77, %[sb], 3
s_lshl_b32 s77, s77, 2
s_add_u32 s77, s77, %[swave]
s_lshl_b32 s77, s77, 10
v_add_u32 v118, s77, v248
s_mov_b32 s77, LKEND_ka-LPC_%=-16
v_min_u32 v118, s77, v118
s_getpc_b64 s[98:99]
LPC_%=:
global_load_dwordx4 a[252:255], v118, s[98:99]
s_and_b32 s77, %[sb], 7
s_lshl_b32 s77, s77, 2
s_bfe_u32 s84, %[sb], 131075
s_add_u32 s49, s77, s84
s_lshr_b32 s50, %[sb], 5
s_lshr_b32 s76, %[sb], 8
s_lshl_b32 s76, s76, 7
s_and_b32 s77, %[sb], 127
s_or_b32 s76, s76, s77
s_lshl_b32 s76, s76, 2
s_add_u32 s76, s76, %[swave]
s_lshl_b32 s76, s76, 1
s_bitcmp1_b32 %[sb], 7
s_cselect_b64 s[52:53], -1, 0
s_and_b32 s54, s52, 1
s_mov_b32 s55, s54
s_and_b32 s54, s52, 0x10001
s_mov_b32 s55, s54
v_lshrrev_b32 v180, 5, v127
s_mul_i32 s82, s76, 2560
v_and_b32 v182, 240, v248
v_lshrrev_b32 v181, 4, v127
v_mul_u32_u24 v168, 1280, v181
v_add_u32 v168, v168, v182
s_lshl_b32 s77, %[swave], 12
s_add_u32 s65, s77, %[slds]
s_lshl_b32 s89, %[swave], 17
v_bfe_u32 v210, v127, 2, 3
v_and_b32 v212, 3, v127
v_lshlrev_b32 v213, 4, v212
v_lshl_add_u32 v213, v180, 6, v213
v_lshl_add_u32 v253, v210, 13, v213
v_add_u32 v254, 128, v253
s_mul_i32 s96, %[swave], 4112
s_add_u32 s96, s96, %[slds]
s_lshl_b32 s77, s50, 2
s_add_u32 s77, s77, %[swave]
s_mul_i32 s68, s77, 81920
s_mul_i32 s83, s77, 560128
s_add_u32 s83, s83, 2752512
s_lshl_b32 s93, s77, 9
s_lshl_b32 s94, %[swave], 13
s_add_u32 s94, s94, %[slds]
v_lshrrev_b32 v211, 3, v127
v_mul_u32_u24 v220, 1280, v211
v_lshrrev_b32 v212, 4, v127
v_and_b32 v211, 7, v127
v_xor_b32 v211, v211, v212
v_lshl_add_u32 v220, v211, 4, v220
v_xor_b32 v221, 64, v220
s_bitcmp1_b32 s84, 0
s_mov_b32 s77, 0x77552200
s_cselect_b32 s77, 0x77552200, s77
s_lshl_b32 s91, s50, 2
s_lshr_b32 s77, s77, s91
s_and_b32 s77, s77, 15
s_lshl_b32 s86, s77, 7
s_lshl_b32 s85, s77, 19
s_lshl_b32 s90, s49, 8
s_waitcnt lgkmcnt(0)
s_add_u32 s16, s16, s90
s_addc_u32 s17, s17, 0
s_sub_u32 s18, 5242880, s90
s_mov_b32 s19, 131072
s_mov_b32 s34, 2621440
s_mov_b32 s35, 131072
s_mov_b32 s22, 67108864
s_mov_b32 s23, 131072
s_bitcmp1_b32 %[sb], 7
s_cbranch_scc1 LCA_%=
s_add_u32 s87, s89, s85
s_add_u32 s85, s85, 524288
s_cmp_ge_u32 s85, 5242880
s_cselect_b32 s85, 0, s85
s_add_u32 m0, s96, 65536
s_nop 0
buffer_load_dwordx4 v253, s[16:19], s87 offen offset:0 lds
s_add_u32 m0, s96, 66560
s_nop 0
buffer_load_dwordx4 v254, s[16:19], s87 offen offset:0 lds
s_add_u32 s87, s87, 65536
s_add_u32 m0, s96, 67592
s_nop 0
buffer_load_dwordx4 v253, s[16:19], s87 offen offset:0 lds
s_add_u32 m0, s96, 68616
s_nop 0
buffer_load_dwordx4 v254, s[16:19], s87 offen offset:0 lds
s_add_u32 s88, s68, s86
s_add_u32 s86, s86, 128
s_cmp_ge_u32 s86, 1280
s_cselect_b32 s86, 0, s86
s_add_u32 m0, s94, 0
s_add_u32 s95, s88, 0
buffer_load_dwordx4 v220, s[32:35], s95 offen offset:0 lds
s_add_u32 s95, s88, 9216
buffer_load_dwordx4 v221, s[32:35], s95 offen offset:1024 lds
s_add_u32 s95, s88, 18432
buffer_load_dwordx4 v220, s[32:35], s95 offen offset:2048 lds
s_add_u32 s95, s88, 27648
buffer_load_dwordx4 v221, s[32:35], s95 offen offset:3072 lds
s_add_u32 m0, s94, 4096
s_add_u32 s95, s88, 40960
buffer_load_dwordx4 v220, s[32:35], s95 offen offset:0 lds
s_add_u32 s95, s88, 50176
buffer_load_dwordx4 v221, s[32:35], s95 offen offset:1024 lds
s_add_u32 s95, s88, 59392
buffer_load_dwordx4 v220, s[32:35], s95 offen offset:2048 lds
s_add_u32 s95, s88, 68608
buffer_load_dwordx4 v221, s[32:35], s95 offen offset:3072 lds
s_add_u32 s87, s89, s85
s_add_u32 s85, s85, 524288
s_cmp_ge_u32 s85, 5242880
s_cselect_b32 s85, 0, s85
s_add_u32 m0, s96, 81984
s_nop 0
buffer_load_dwordx4 v253, s[16:19], s87 offen offset:0 lds
s_add_u32 m0, s96, 83008
s_nop 0
buffer_load_dwordx4 v254, s[16:19], s87 offen offset:0 lds
s_add_u32 s87, s87, 65536
s_add_u32 m0, s96, 84040
s_nop 0
buffer_load_dwordx4 v253, s[16:19], s87 offen offset:0 lds
s_add_u32 m0, s96, 85064
s_nop 0
buffer_load_dwordx4 v254, s[16:19], s87 offen offset:0 lds
s_branch LCT_%=
LCA_%=:
buffer_load_dwordx4 v[128:131], v168, s[32:35], s82 offen offset:0
buffer_load_dwordx4 v[132:135], v168, s[32:35], s82 offen offset:256
buffer_load_dwordx4 v[136:139], v168, s[32:35], s82 offen offset:512
buffer_load_dwordx4 v[140:143], v168, s[32:35], s82 offen offset:768
buffer_load_dwordx4 v[144:147], v168, s[32:35], s82 offen offset:1024
global_load_dwordx4 v[100:103], v182, s[44:45] offset:0
global_load_dwordx4 v[104:107], v182, s[44:45] offset:256
global_load_dwordx4 v[108:111], v182, s[44:45] offset:512
global_load_dwordx4 v[112:115], v182, s[44:45] offset:768
global_load_dwordx4 v[116:119], v182, s[44:45] offset:1024
LCT_%=:
v_and_b32 v211, 15, v127
v_bfe_u32 v222, v127, 1, 3
v_xor_b32 v222, v222, v212
v_lshlrev_b32 v222, 4, v222
v_lshl_add_u32 v222, v211, 7, v222
s_lshl_b32 s77, %[sb], 11
s_and_b32 s77, s77, 2048
v_add_u32 v222, s77, v222
v_xor_b32 v223, 64, v222
v_xor_b32 v217, 2048, v222
v_add_u32 v217, s94, v217
v_xor_b32 v218, 2048, v223
v_add_u32 v218, s94, v218
v_add_u32 v222, s94, v222
v_add_u32 v223, s94, v223
v_and_b32 v214, 1, v212
v_lshlrev_b32 v215, 5, v214
v_xor_b32 v215, v215, v213
v_lshl_add_u32 v235, v210, 8, v215
v_add_u32 v235, 128, v235
v_and_b32 v234, 1, v127
v_mul_u32_u24 v234, 43690, v234
v_add_u32 v234, 17476, v234
v_bfe_u32 v215, v127, 2, 2
v_and_b32 v216, 3, v127
v_mul_u32_u24 v252, 2056, v212
v_lshl_add_u32 v252, v215, 6, v252
v_lshl_add_u32 v252, v216, 4, v252
s_xor_b32 s77, %[swave], 0
s_lshl_b32 s77, s77, 9
s_add_u32 s77, s77, %[slds]
s_add_u32 s77, s77, 65536
v_add_u32 v192, s77, v252
v_add_u32 v196, 49344, v192
s_xor_b32 s77, %[swave], 1
s_lshl_b32 s77, s77, 9
s_add_u32 s77, s77, %[slds]
s_add_u32 s77, s77, 65536
v_add_u32 v193, s77, v252
v_add_u32 v197, 49344, v193
s_xor_b32 s77, %[swave], 2
s_lshl_b32 s77, s77, 9
s_add_u32 s77, s77, %[slds]
s_add_u32 s77, s77, 65536
v_add_u32 v194, s77, v252
v_add_u32 v198, 49344, v194
s_xor_b32 s77, %[swave], 3
s_lshl_b32 s77, s77, 9
s_add_u32 s77, s77, %[slds]
s_add_u32 s77, s77, 65536
v_add_u32 v195, s77, v252
v_add_u32 v199, 49344, v195
v_mov_b32 v236, 0x3f803f80
v_mov_b32 v184, 0
v_mov_b32 v237, 0x3f803f80
v_mov_b32 v185, 0
v_mov_b32 v238, 0x3f803f80
v_mov_b32 v188, 0
v_mov_b32 v239, 0x3f803f80
v_mov_b32 v189, 0
s_getpc_b64 s[98:99]
LTA0_%=:
s_lshl_b32 s73, %[swave], 12
s_add_u32 s73, s73, LPG1_%=-LTA0_%=
s_add_u32 s98, s98, s73
s_addc_u32 s99, s99, 0
s_mov_b64 exec, 0
s_setpc_b64 s[98:99]
LTR0_%=:
s_mov_b64 exec, -1
s_nop 0
s_bitcmp1_b32 %[sb], 7
s_cbranch_scc1 LCB_%=
s_add_u32 s88, s68, s86
s_add_u32 s86, s86, 128
s_cmp_ge_u32 s86, 1280
s_cselect_b32 s86, 0, s86
s_add_u32 m0, s94, 32768
s_add_u32 s95, s88, 0
buffer_load_dwordx4 v220, s[32:35], s95 offen offset:0 lds
s_add_u32 s95, s88, 9216
buffer_load_dwordx4 v221, s[32:35], s95 offen offset:1024 lds
s_add_u32 s95, s88, 18432
buffer_load_dwordx4 v220, s[32:35], s95 offen offset:2048 lds
s_add_u32 s95, s88, 27648
buffer_load_dwordx4 v221, s[32:35], s95 offen offset:3072 lds
s_add_u32 m0, s94, 36864
s_add_u32 s95, s88, 40960
buffer_load_dwordx4 v220, s[32:35], s95 offen offset:0 lds
s_add_u32 s95, s88, 50176
buffer_load_dwordx4 v221, s[32:35], s95 offen offset:1024 lds
s_add_u32 s95, s88, 59392
buffer_load_dwordx4 v220, s[32:35], s95 offen offset:2048 lds
s_add_u32 s95, s88, 68608
buffer_load_dwordx4 v221, s[32:35], s95 offen offset:3072 lds
s_branch LCM_%=
LCB_%=:
s_waitcnt vmcnt(5)
s_mov_b32 s61, 0x3acccccd
s_add_u32 s87, s89, s85
s_add_u32 s85, s85, 524288
s_cmp_ge_u32 s85, 5242880
s_cselect_b32 s85, 0, s85
s_add_u32 m0, s96, 65536
s_nop 0
buffer_load_dwordx4 v253, s[16:19], s87 offen offset:0 lds
s_lshl_b32 s60, s76, 1
s_sub_u32 s60, 65535, s60
v_lshlrev_b32 v0, 16, v128
v_and_b32 v1, 0xffff0000, v128
v_lshlrev_b32 v2, 16, v129
v_and_b32 v3, 0xffff0000, v129
v_lshlrev_b32 v4, 16, v130
v_and_b32 v5, 0xffff0000, v130
s_add_u32 m0, s96, 66560
s_nop 0
buffer_load_dwordx4 v254, s[16:19], s87 offen offset:0 lds
v_lshlrev_b32 v6, 16, v131
v_and_b32 v7, 0xffff0000, v131
v_lshlrev_b32 v8, 16, v132
v_and_b32 v9, 0xffff0000, v132
v_lshlrev_b32 v10, 16, v133
v_and_b32 v11, 0xffff0000, v133
v_lshlrev_b32 v12, 16, v134
v_and_b32 v13, 0xffff0000, v134
s_add_u32 s87, s87, 65536
s_add_u32 m0, s96, 67592
s_nop 0
buffer_load_dwordx4 v253, s[16:19], s87 offen offset:0 lds
v_lshlrev_b32 v14, 16, v135
v_and_b32 v15, 0xffff0000, v135
v_lshlrev_b32 v16, 16, v136
v_and_b32 v17, 0xffff0000, v136
v_lshlrev_b32 v18, 16, v137
v_and_b32 v19, 0xffff0000, v137
v_lshlrev_b32 v20, 16, v138
v_and_b32 v21, 0xffff0000, v138
s_add_u32 m0, s96, 68616
s_nop 0
buffer_load_dwordx4 v254, s[16:19], s87 offen offset:0 lds
v_lshlrev_b32 v22, 16, v139
v_and_b32 v23, 0xffff0000, v139
v_lshlrev_b32 v24, 16, v140
v_and_b32 v25, 0xffff0000, v140
v_lshlrev_b32 v26, 16, v141
v_and_b32 v27, 0xffff0000, v141
v_lshlrev_b32 v28, 16, v142
v_and_b32 v29, 0xffff0000, v142
s_add_u32 s88, s68, s86
s_add_u32 s86, s86, 128
s_cmp_ge_u32 s86, 1280
s_cselect_b32 s86, 0, s86
s_add_u32 m0, s94, 0
s_add_u32 s95, s88, 0
buffer_load_dwordx4 v220, s[32:35], s95 offen offset:0 lds
v_lshlrev_b32 v30, 16, v143
v_and_b32 v31, 0xffff0000, v143
v_lshlrev_b32 v32, 16, v144
v_and_b32 v33, 0xffff0000, v144
v_lshlrev_b32 v34, 16, v145
v_and_b32 v35, 0xffff0000, v145
v_lshlrev_b32 v36, 16, v146
v_and_b32 v37, 0xffff0000, v146
s_add_u32 s95, s88, 9216
buffer_load_dwordx4 v221, s[32:35], s95 offen offset:1024 lds
v_lshlrev_b32 v38, 16, v147
v_and_b32 v39, 0xffff0000, v147
v_pk_add_f32 v[40:41], v[0:1], v[2:3]
v_pk_add_f32 v[42:43], v[4:5], v[6:7]
v_pk_add_f32 v[40:41], v[40:41], v[8:9]
v_pk_add_f32 v[42:43], v[42:43], v[10:11]
v_pk_add_f32 v[40:41], v[40:41], v[12:13]
v_pk_add_f32 v[42:43], v[42:43], v[14:15]
s_add_u32 s95, s88, 18432
buffer_load_dwordx4 v220, s[32:35], s95 offen offset:2048 lds
v_pk_add_f32 v[40:41], v[40:41], v[16:17]
v_pk_add_f32 v[42:43], v[42:43], v[18:19]
v_pk_add_f32 v[40:41], v[40:41], v[20:21]
v_pk_add_f32 v[42:43], v[42:43], v[22:23]
v_pk_add_f32 v[40:41], v[40:41], v[24:25]
v_pk_add_f32 v[42:43], v[42:43], v[26:27]
v_pk_add_f32 v[40:41], v[40:41], v[28:29]
v_pk_add_f32 v[42:43], v[42:43], v[30:31]
s_add_u32 s95, s88, 27648
buffer_load_dwordx4 v221, s[32:35], s95 offen offset:3072 lds
v_pk_add_f32 v[40:41], v[40:41], v[32:33]
v_pk_add_f32 v[42:43], v[42:43], v[34:35]
v_pk_add_f32 v[40:41], v[40:41], v[36:37]
v_pk_add_f32 v[42:43], v[42:43], v[38:39]
v_pk_add_f32 v[40:41], v[40:41], v[42:43]
v_add_f32 v48, v40, v41
s_nop 1
v_add_f32_dpp v48, v48, v48 row_ror:8 row_mask:0xf bank_mask:0xf
s_nop 1
v_add_f32_dpp v48, v48, v48 row_ror:4 row_mask:0xf bank_mask:0xf
s_add_u32 m0, s94, 4096
s_add_u32 s95, s88, 40960
buffer_load_dwordx4 v220, s[32:35], s95 offen offset:0 lds
v_add_f32_dpp v48, v48, v48 row_ror:2 row_mask:0xf bank_mask:0xf
s_nop 1
v_add_f32_dpp v48, v48, v48 row_ror:1 row_mask:0xf bank_mask:0xf
v_mul_f32 v48, 0x3acccccd, v48
CV v44,v48,v48
v_and_b32 v44, 0xffff0000, v44
v_sub_f32 v48, v48, v44
v_pk_add_f32 v[0:1], v[0:1], v[44:45] op_sel_hi:[1,0] neg_lo:[0,1] neg_hi:[0,1]
v_pk_add_f32 v[2:3], v[2:3], v[44:45] op_sel_hi:[1,0] neg_lo:[0,1] neg_hi:[0,1]
s_add_u32 s95, s88, 50176
buffer_load_dwordx4 v221, s[32:35], s95 offen offset:1024 lds
v_pk_add_f32 v[4:5], v[4:5], v[44:45] op_sel_hi:[1,0] neg_lo:[0,1] neg_hi:[0,1]
v_pk_add_f32 v[6:7], v[6:7], v[44:45] op_sel_hi:[1,0] neg_lo:[0,1] neg_hi:[0,1]
v_pk_add_f32 v[8:9], v[8:9], v[44:45] op_sel_hi:[1,0] neg_lo:[0,1] neg_hi:[0,1]
v_pk_add_f32 v[10:11], v[10:11], v[44:45] op_sel_hi:[1,0] neg_lo:[0,1] neg_hi:[0,1]
v_pk_add_f32 v[12:13], v[12:13], v[44:45] op_sel_hi:[1,0] neg_lo:[0,1] neg_hi:[0,1]
)ASM"
R"ASM(
v_pk_add_f32 v[14:15], v[14:15], v[44:45] op_sel_hi:[1,0] neg_lo:[0,1] neg_hi:[0,1]
v_pk_add_f32 v[16:17], v[16:17], v[44:45] op_sel_hi:[1,0] neg_lo:[0,1] neg_hi:[0,1]
v_pk_add_f32 v[18:19], v[18:19], v[44:45] op_sel_hi:[1,0] neg_lo:[0,1] neg_hi:[0,1]
s_add_u32 s95, s88, 59392
buffer_load_dwordx4 v220, s[32:35], s95 offen offset:2048 lds
v_pk_add_f32 v[20:21], v[20:21], v[44:45] op_sel_hi:[1,0] neg_lo:[0,1] neg_hi:[0,1]
v_pk_add_f32 v[22:23], v[22:23], v[44:45] op_sel_hi:[1,0] neg_lo:[0,1] neg_hi:[0,1]
v_pk_add_f32 v[24:25], v[24:25], v[44:45] op_sel_hi:[1,0] neg_lo:[0,1] neg_hi:[0,1]
v_pk_add_f32 v[26:27], v[26:27], v[44:45] op_sel_hi:[1,0] neg_lo:[0,1] neg_hi:[0,1]
v_pk_add_f32 v[28:29], v[28:29], v[44:45] op_sel_hi:[1,0] neg_lo:[0,1] neg_hi:[0,1]
v_pk_add_f32 v[30:31], v[30:31], v[44:45] op_sel_hi:[1,0] neg_lo:[0,1] neg_hi:[0,1]
v_pk_add_f32 v[32:33], v[32:33], v[44:45] op_sel_hi:[1,0] neg_lo:[0,1] neg_hi:[0,1]
v_pk_add_f32 v[34:35], v[34:35], v[44:45] op_sel_hi:[1,0] neg_lo:[0,1] neg_hi:[0,1]
s_add_u32 s95, s88, 68608
buffer_load_dwordx4 v221, s[32:35], s95 offen offset:3072 lds
v_pk_add_f32 v[36:37], v[36:37], v[44:45] op_sel_hi:[1,0] neg_lo:[0,1] neg_hi:[0,1]
v_pk_add_f32 v[38:39], v[38:39], v[44:45] op_sel_hi:[1,0] neg_lo:[0,1] neg_hi:[0,1]
v_pk_mul_f32 v[40:41], v[0:1], v[0:1]
v_pk_mul_f32 v[42:43], v[2:3], v[2:3]
v_pk_fma_f32 v[40:41], v[4:5], v[4:5], v[40:41]
v_pk_fma_f32 v[42:43], v[6:7], v[6:7], v[42:43]
v_pk_fma_f32 v[40:41], v[8:9], v[8:9], v[40:41]
v_pk_fma_f32 v[42:43], v[10:11], v[10:11], v[42:43]
s_add_u32 s87, s89, s85
s_add_u32 s85, s85, 524288
s_cmp_ge_u32 s85, 5242880
s_cselect_b32 s85, 0, s85
s_add_u32 m0, s96, 81984
s_nop 0
buffer_load_dwordx4 v253, s[16:19], s87 offen offset:0 lds
v_pk_fma_f32 v[40:41], v[12:13], v[12:13], v[40:41]
v_pk_fma_f32 v[42:43], v[14:15], v[14:15], v[42:43]
v_pk_fma_f32 v[40:41], v[16:17], v[16:17], v[40:41]
v_pk_fma_f32 v[42:43], v[18:19], v[18:19], v[42:43]
v_pk_fma_f32 v[40:41], v[20:21], v[20:21], v[40:41]
v_pk_fma_f32 v[42:43], v[22:23], v[22:23], v[42:43]
v_pk_fma_f32 v[40:41], v[24:25], v[24:25], v[40:41]
v_pk_fma_f32 v[42:43], v[26:27], v[26:27], v[42:43]
s_add_u32 m0, s96, 83008
s_nop 0
buffer_load_dwordx4 v254, s[16:19], s87 offen offset:0 lds
v_pk_fma_f32 v[40:41], v[28:29], v[28:29], v[40:41]
v_pk_fma_f32 v[42:43], v[30:31], v[30:31], v[42:43]
v_pk_fma_f32 v[40:41], v[32:33], v[32:33], v[40:41]
v_pk_fma_f32 v[42:43], v[34:35], v[34:35], v[42:43]
v_pk_fma_f32 v[40:41], v[36:37], v[36:37], v[40:41]
v_pk_fma_f32 v[42:43], v[38:39], v[38:39], v[42:43]
v_mul_f32 v48, v48, v48
v_pk_add_f32 v[40:41], v[40:41], v[42:43]
s_add_u32 s87, s87, 65536
s_add_u32 m0, s96, 84040
s_nop 0
buffer_load_dwordx4 v253, s[16:19], s87 offen offset:0 lds
v_add_f32 v49, v40, v41
CV v128,v0,v1
CV v129,v2,v3
v_add_f32_dpp v49, v49, v49 row_ror:8 row_mask:0xf bank_mask:0xf
CV v130,v4,v5
CV v131,v6,v7
v_add_f32_dpp v49, v49, v49 row_ror:4 row_mask:0xf bank_mask:0xf
CV v132,v8,v9
s_add_u32 m0, s96, 85064
s_nop 0
buffer_load_dwordx4 v254, s[16:19], s87 offen offset:0 lds
CV v133,v10,v11
v_add_f32_dpp v49, v49, v49 row_ror:2 row_mask:0xf bank_mask:0xf
CV v134,v12,v13
CV v135,v14,v15
v_add_f32_dpp v49, v49, v49 row_ror:1 row_mask:0xf bank_mask:0xf
v_fma_f32 v49, v49, s61, -v48
CV v136,v16,v17
v_max_f32 v49, 0, v49
s_add_u32 s88, s68, s86
s_add_u32 s86, s86, 128
s_cmp_ge_u32 s86, 1280
s_cselect_b32 s86, 0, s86
s_add_u32 m0, s94, 32768
s_add_u32 s95, s88, 0
buffer_load_dwordx4 v220, s[32:35], s95 offen offset:0 lds
CV v137,v18,v19
CV v49,v49,v49
CV v138,v20,v21
v_and_b32 v49, 0xffff0000, v49
CV v139,v22,v23
v_add_f32 v49, 0x3727c5ac, v49
CV v140,v24,v25
CV v49,v49,v49
s_add_u32 s95, s88, 9216
buffer_load_dwordx4 v221, s[32:35], s95 offen offset:1024 lds
CV v141,v26,v27
v_and_b32 v49, 0xffff0000, v49
CV v142,v28,v29
v_sqrt_f32 v49, v49
CV v143,v30,v31
CV v178,v44,v49
CV v144,v32,v33
v_and_b32 v49, 0xffff0000, v178
s_add_u32 s95, s88, 18432
buffer_load_dwordx4 v220, s[32:35], s95 offen offset:2048 lds
CV v145,v34,v35
v_rcp_f32 v46, v49
CV v146,v36,v37
CV v147,v38,v39
v_lshlrev_b32 v0, 16, v128
v_and_b32 v1, 0xffff0000, v128
v_lshlrev_b32 v2, 16, v129
v_and_b32 v3, 0xffff0000, v129
s_add_u32 s95, s88, 27648
buffer_load_dwordx4 v221, s[32:35], s95 offen offset:3072 lds
v_lshlrev_b32 v4, 16, v130
v_and_b32 v5, 0xffff0000, v130
v_lshlrev_b32 v6, 16, v131
v_and_b32 v7, 0xffff0000, v131
v_lshlrev_b32 v8, 16, v132
v_and_b32 v9, 0xffff0000, v132
v_lshlrev_b32 v10, 16, v133
v_and_b32 v11, 0xffff0000, v133
s_add_u32 m0, s94, 36864
s_add_u32 s95, s88, 40960
buffer_load_dwordx4 v220, s[32:35], s95 offen offset:0 lds
v_lshlrev_b32 v12, 16, v134
v_and_b32 v13, 0xffff0000, v134
v_lshlrev_b32 v14, 16, v135
v_and_b32 v15, 0xffff0000, v135
v_lshlrev_b32 v16, 16, v136
v_and_b32 v17, 0xffff0000, v136
v_lshlrev_b32 v18, 16, v137
v_and_b32 v19, 0xffff0000, v137
s_add_u32 s95, s88, 50176
buffer_load_dwordx4 v221, s[32:35], s95 offen offset:1024 lds
v_lshlrev_b32 v20, 16, v138
v_and_b32 v21, 0xffff0000, v138
v_lshlrev_b32 v22, 16, v139
v_and_b32 v23, 0xffff0000, v139
v_lshlrev_b32 v24, 16, v140
v_and_b32 v25, 0xffff0000, v140
v_lshlrev_b32 v26, 16, v141
v_and_b32 v27, 0xffff0000, v141
s_add_u32 s95, s88, 59392
buffer_load_dwordx4 v220, s[32:35], s95 offen offset:2048 lds
v_lshlrev_b32 v28, 16, v142
v_and_b32 v29, 0xffff0000, v142
v_lshlrev_b32 v30, 16, v143
v_and_b32 v31, 0xffff0000, v143
v_lshlrev_b32 v32, 16, v144
v_and_b32 v33, 0xffff0000, v144
v_lshlrev_b32 v34, 16, v145
v_and_b32 v35, 0xffff0000, v145
s_add_u32 s95, s88, 68608
buffer_load_dwordx4 v221, s[32:35], s95 offen offset:3072 lds
v_lshlrev_b32 v36, 16, v146
v_and_b32 v37, 0xffff0000, v146
v_lshlrev_b32 v38, 16, v147
v_and_b32 v39, 0xffff0000, v147
v_pk_mul_f32 v[0:1], v[0:1], v[46:47] op_sel_hi:[1,0]
v_pk_mul_f32 v[2:3], v[2:3], v[46:47] op_sel_hi:[1,0]
v_pk_mul_f32 v[4:5], v[4:5], v[46:47] op_sel_hi:[1,0]
v_pk_mul_f32 v[6:7], v[6:7], v[46:47] op_sel_hi:[1,0]
v_pk_mul_f32 v[8:9], v[8:9], v[46:47] op_sel_hi:[1,0]
v_pk_mul_f32 v[10:11], v[10:11], v[46:47] op_sel_hi:[1,0]
v_pk_mul_f32 v[12:13], v[12:13], v[46:47] op_sel_hi:[1,0]
v_pk_mul_f32 v[14:15], v[14:15], v[46:47] op_sel_hi:[1,0]
v_pk_mul_f32 v[16:17], v[16:17], v[46:47] op_sel_hi:[1,0]
v_pk_mul_f32 v[18:19], v[18:19], v[46:47] op_sel_hi:[1,0]
v_pk_mul_f32 v[20:21], v[20:21], v[46:47] op_sel_hi:[1,0]
v_pk_mul_f32 v[22:23], v[22:23], v[46:47] op_sel_hi:[1,0]
v_pk_mul_f32 v[24:25], v[24:25], v[46:47] op_sel_hi:[1,0]
v_pk_mul_f32 v[26:27], v[26:27], v[46:47] op_sel_hi:[1,0]
v_pk_mul_f32 v[28:29], v[28:29], v[46:47] op_sel_hi:[1,0]
v_pk_mul_f32 v[30:31], v[30:31], v[46:47] op_sel_hi:[1,0]
v_pk_mul_f32 v[32:33], v[32:33], v[46:47] op_sel_hi:[1,0]
v_pk_mul_f32 v[34:35], v[34:35], v[46:47] op_sel_hi:[1,0]
v_pk_mul_f32 v[36:37], v[36:37], v[46:47] op_sel_hi:[1,0]
v_pk_mul_f32 v[38:39], v[38:39], v[46:47] op_sel_hi:[1,0]
CV v128,v0,v1
CV v129,v2,v3
CV v130,v4,v5
CV v131,v6,v7
CV v132,v8,v9
CV v133,v10,v11
CV v134,v12,v13
CV v135,v14,v15
CV v136,v16,v17
CV v137,v18,v19
CV v138,v20,v21
CV v139,v22,v23
CV v140,v24,v25
CV v141,v26,v27
CV v142,v28,v29
CV v143,v30,v31
CV v144,v32,v33
CV v145,v34,v35
CV v146,v36,v37
CV v147,v38,v39
s_waitcnt vmcnt(24)
v_dot2_f32_bf16 v40, v128, v100, 0
v_dot2_f32_bf16 v41, v129, v101, 0
v_dot2_f32_bf16 v42, v130, v102, 0
v_dot2_f32_bf16 v43, v131, v103, 0
v_dot2_f32_bf16 v40, v132, v104, v40
v_dot2_f32_bf16 v41, v133, v105, v41
v_dot2_f32_bf16 v42, v134, v106, v42
v_dot2_f32_bf16 v43, v135, v107, v43
v_dot2_f32_bf16 v40, v136, v108, v40
v_dot2_f32_bf16 v41, v137, v109, v41
v_dot2_f32_bf16 v42, v138, v110, v42
v_dot2_f32_bf16 v43, v139, v111, v43
v_dot2_f32_bf16 v40, v140, v112, v40
v_dot2_f32_bf16 v41, v141, v113, v41
v_dot2_f32_bf16 v42, v142, v114, v42
v_dot2_f32_bf16 v43, v143, v115, v43
v_dot2_f32_bf16 v40, v144, v116, v40
v_dot2_f32_bf16 v41, v145, v117, v41
v_dot2_f32_bf16 v42, v146, v118, v42
v_dot2_f32_bf16 v43, v147, v119, v43
s_nop 2
v_pk_add_f32 v[40:41], v[40:41], v[42:43]
v_add_f32 v160, v40, v41
s_nop 1
v_add_f32_dpp v160, v160, v160 row_ror:8 row_mask:0xf bank_mask:0xf
s_nop 1
v_add_f32_dpp v160, v160, v160 row_ror:4 row_mask:0xf bank_mask:0xf
s_nop 1
v_add_f32_dpp v160, v160, v160 row_ror:2 row_mask:0xf bank_mask:0xf
s_nop 1
v_add_f32_dpp v160, v160, v160 row_ror:1 row_mask:0xf bank_mask:0xf
CV v160,v160,v160
v_ashrrev_i32 v50, 31, v160
v_or_b32 v50, 0x80000000, v50
v_xor_b32 v160, v160, v50
v_and_b32 v160, 0xffff0000, v160
v_sub_u32 v50, s60, v181
v_or_b32 v160, v160, v50
LCM_%=:
s_waitcnt vmcnt(12)
LPG1_%=:
s_cbranch_execz LPG5_%=
RD a[128:131],v222 offset:0
RD a[132:135],v223 offset:0
RD a[136:139],v217 offset:0
RD a[140:143],v218 offset:0
RD a[144:147],v222 offset:4096
RD a[148:151],v223 offset:4096
RD a[152:155],v217 offset:4096
RD a[156:159],v218 offset:4096
s_barrier
TR a[0:1],v192 offset:0
TR a[2:3],v192 offset:256
TR a[4:5],v192 offset:8224
TR a[6:7],v192 offset:8480
TR a[8:9],v192 offset:8
TR a[10:11],v192 offset:264
TR a[12:13],v192 offset:8232
TR a[14:15],v192 offset:8488
s_waitcnt lgkmcnt(8)
s_waitcnt lgkmcnt(4)
MM v[0:3],a[0:3],a[128:131],0
s_mov_b32 s66, 32768
TR a[16:17],v193 offset:0
TR a[18:19],v193 offset:256
MM v[0:3],a[4:7],a[132:135],v[0:3]
s_lshl_b32 s90, s76, 1
TR a[20:21],v193 offset:8224
TR a[22:23],v193 offset:8480
MM v[8:11],a[4:7],a[140:143],0
s_and_b32 s77, s90, 15
TR a[24:25],v193 offset:8
TR a[26:27],v193 offset:264
buffer_load_dword v169, off, s[20:23], s66 sc1
MM v[8:11],a[0:3],a[136:139],v[8:11]
s_add_u32 s82, s82, 131072
TR a[28:29],v193 offset:8232
TR a[30:31],v193 offset:8488
MM v[16:19],a[0:3],a[144:147],0
s_add_u32 s87, s89, s85
TR a[32:33],v194 offset:0
TR a[34:35],v194 offset:256
MM v[16:19],a[4:7],a[148:151],v[16:19]
s_add_u32 s85, s85, 524288
TR a[36:37],v194 offset:8224
TR a[38:39],v194 offset:8480
MM v[24:27],a[4:7],a[156:159],0
s_lshl_b32 s77, s77, 1
TR a[40:41],v194 offset:8
TR a[42:43],v194 offset:264
MM v[24:27],a[0:3],a[152:155],v[24:27]
s_waitcnt lgkmcnt(14)
TR a[44:45],v194 offset:8232
TR a[46:47],v194 offset:8488
s_cmp_ge_u32 s85, 5242880
s_cselect_b32 s85, 0, s85
MM v[4:7],a[8:11],a[128:131],0
s_add_u32 m0, s96, 98432
TR a[48:49],v195 offset:0
TR a[50:51],v195 offset:256
MM v[4:7],a[12:15],a[132:135],v[4:7]
s_bfe_u32 s91, s90, 65540
TR a[52:53],v195 offset:8224
TR a[54:55],v195 offset:8480
MM v[12:15],a[12:15],a[140:143],0
s_add_u32 s77, s77, s91
TR a[56:57],v195 offset:8
TR a[58:59],v195 offset:264
buffer_load_dwordx4 v253, s[16:19], s87 offen offset:0 lds
MM v[12:15],a[8:11],a[136:139],v[12:15]
s_add_u32 m0, s96, 99456
TR a[60:61],v195 offset:8232
TR a[62:63],v195 offset:8488
MM v[20:23],a[8:11],a[144:147],0
s_and_b32 s91, s90, 0xffffffe0
buffer_load_dwordx4 v254, s[16:19], s87 offen offset:0 lds
MM v[20:23],a[12:15],a[148:151],v[20:23]
s_add_u32 s87, s87, 65536
s_add_u32 m0, s96, 100488
s_add_u32 s77, s77, s91
MM v[28:31],a[12:15],a[156:159],0
s_lshl_b32 s77, s77, 3
s_add_u32 s97, s77, 36864
s_sub_u32 s79, 65535, s90
MM v[28:31],a[8:11],a[152:155],v[28:31]
s_lshl_b32 s78, s90, 2
s_add_u32 s78, s78, 53248
s_lshl_b32 s80, s76, 2
v_smfmac_f32_16x16x64_bf16 v[184:187], v[236:239], a[0:7], v234
s_andn2_b32 s91, 8192, s52
s_add_u32 s80, s80, s91
v_smfmac_f32_16x16x64_bf16 v[188:191], v[236:239], a[8:15], v234
s_waitcnt lgkmcnt(15)
s_add_u32 s80, s80, 81920
MM v[32:35],a[16:19],a[128:131],0
buffer_load_dwordx4 v253, s[16:19], s87 offen offset:0 lds
MM v[32:35],a[20:23],a[132:135],v[32:35]
s_add_u32 m0, s96, 101512
v_lshlrev_b32 v232, 11, v180
v_lshl_add_u32 v232, v214, 8, v232
MM v[40:43],a[20:23],a[140:143],0
s_waitcnt vmcnt(12)
s_barrier
v_lshl_add_u32 v232, v215, 6, v232
MM v[40:43],a[16:19],a[136:139],v[40:43]
TR a[0:1],v192 offset:16448
TR a[2:3],v192 offset:16704
MM v[48:51],a[16:19],a[144:147],0
TR a[4:5],v192 offset:24672
TR a[6:7],v192 offset:24928
buffer_load_dwordx4 v254, s[16:19], s87 offen offset:0 lds
MM v[48:51],a[20:23],a[148:151],v[48:51]
TR a[8:9],v192 offset:16456
TR a[10:11],v192 offset:16712
MM v[56:59],a[20:23],a[156:159],0
s_add_u32 s88, s68, s86
TR a[12:13],v192 offset:24680
)ASM"
R"ASM(
TR a[14:15],v192 offset:24936
MM v[56:59],a[16:19],a[152:155],v[56:59]
s_add_u32 s86, s86, 128
s_cmp_ge_u32 s86, 1280
s_cselect_b32 s86, 0, s86
MM v[36:39],a[24:27],a[128:131],0
s_add_u32 m0, s94, 0
TR a[16:17],v193 offset:16448
TR a[18:19],v193 offset:16704
MM v[36:39],a[28:31],a[132:135],v[36:39]
s_add_u32 s95, s88, 0
TR a[20:21],v193 offset:24672
TR a[22:23],v193 offset:24928
MM v[44:47],a[28:31],a[140:143],0
buffer_load_dwordx4 v220, s[32:35], s95 offen offset:0 lds
MM v[44:47],a[24:27],a[136:139],v[44:47]
s_add_u32 s95, s88, 9216
v_lshl_add_u32 v232, v216, 3, v232
v_lshl_add_u32 v232, v214, 5, v232
MM v[52:55],a[24:27],a[144:147],0
buffer_load_dwordx4 v221, s[32:35], s95 offen offset:1024 lds
MM v[52:55],a[28:31],a[148:151],v[52:55]
s_add_u32 s95, s88, 18432
v_xor_b32 v233, 32, v232
s_add_u32 s77, %[slds], 65536
MM v[60:63],a[28:31],a[156:159],0
v_add_u32 v232, s77, v232
v_add_u32 v233, s77, v233
MM v[60:63],a[24:27],a[152:155],v[60:63]
s_waitcnt lgkmcnt(15)
v_lshlrev_b32 v214, 8, v211
v_add_u32 v214, %[slds], v214
MM v[64:67],a[32:35],a[128:131],0
TR a[24:25],v193 offset:16456
TR a[26:27],v193 offset:16712
MM v[64:67],a[36:39],a[132:135],v[64:67]
TR a[28:29],v193 offset:24680
TR a[30:31],v193 offset:24936
MM v[72:75],a[36:39],a[140:143],0
buffer_load_dwordx4 v220, s[32:35], s95 offen offset:2048 lds
MM v[72:75],a[32:35],a[136:139],v[72:75]
s_add_u32 s95, s88, 27648
v_xor_b32 v215, v211, v212
s_lshl_b32 s77, %[swave], 2
MM v[80:83],a[32:35],a[144:147],0
buffer_load_dwordx4 v221, s[32:35], s95 offen offset:3072 lds
MM v[80:83],a[36:39],a[148:151],v[80:83]
s_waitcnt vmcnt(9)
RD a[160:163],v222 offset:32768
v_xor_b32 v213, s77, v215
MM v[88:91],a[36:39],a[156:159],0
RD a[164:167],v223 offset:32768
v_xor_b32 v216, 0, v213
MM v[88:91],a[32:35],a[152:155],v[88:91]
RD a[168:171],v217 offset:32768
v_lshl_add_u32 v224, v216, 4, v214
MM v[68:71],a[40:43],a[128:131],0
TR a[32:33],v194 offset:16448
TR a[34:35],v194 offset:16704
MM v[68:71],a[44:47],a[132:135],v[68:71]
s_waitcnt vmcnt(8)
TR a[36:37],v194 offset:24672
TR a[38:39],v194 offset:24928
MM v[76:79],a[44:47],a[140:143],0
RD a[172:175],v218 offset:32768
v_readfirstlane_b32 s69, v169
MM v[76:79],a[40:43],a[136:139],v[76:79]
RD a[176:179],v222 offset:36864
v_lshlrev_b32 v173, 4, v181
MM v[84:87],a[40:43],a[144:147],0
s_add_u32 s69, s69, 1
RD a[180:183],v223 offset:36864
v_mov_b32 v179, s69
MM v[84:87],a[44:47],a[148:151],v[84:87]
RD a[184:187],v217 offset:36864
v_xor_b32 v216, 4, v213
MM v[92:95],a[44:47],a[156:159],0
RD a[188:191],v218 offset:36864
v_lshl_add_u32 v225, v216, 4, v214
MM v[92:95],a[40:43],a[152:155],v[92:95]
s_add_u32 m0, s94, 4096
s_add_u32 s95, s88, 40960
s_mov_b64 exec, s[54:55]
buffer_store_dwordx2 v[178:179], v173, s[20:23], s97 offen offset:0 sc1
s_mov_b64 exec, -1
v_xor_b32 v216, 8, v213
MM v[96:99],a[48:51],a[128:131],0
TR a[40:41],v194 offset:16456
TR a[42:43],v194 offset:16712
MM v[96:99],a[52:55],a[132:135],v[96:99]
TR a[44:45],v194 offset:24680
TR a[46:47],v194 offset:24936
buffer_load_dwordx4 v220, s[32:35], s95 offen offset:0 lds
MM v[104:107],a[52:55],a[140:143],0
s_add_u32 s95, s88, 50176
v_lshl_add_u32 v226, v216, 4, v214
v_xor_b32 v216, 12, v213
MM v[104:107],a[48:51],a[136:139],v[104:107]
buffer_load_dwordx4 v221, s[32:35], s95 offen offset:1024 lds
MM v[112:115],a[48:51],a[144:147],0
s_add_u32 s95, s88, 59392
v_lshl_add_u32 v227, v216, 4, v214
v_lshlrev_b32 v214, 8, v212
MM v[112:115],a[52:55],a[148:151],v[112:115]
v_xor_b32 v216, 0, v215
v_lshl_add_u32 v228, v216, 4, v214
MM v[120:123],a[52:55],a[156:159],0
v_xor_b32 v216, 4, v215
v_lshl_add_u32 v229, v216, 4, v214
MM v[120:123],a[48:51],a[152:155],v[120:123]
s_waitcnt lgkmcnt(15)
buffer_load_dwordx4 v220, s[32:35], s95 offen offset:2048 lds
MM v[100:103],a[56:59],a[128:131],0
s_add_u32 s95, s88, 68608
TR a[48:49],v195 offset:16448
TR a[50:51],v195 offset:16704
MM v[100:103],a[60:63],a[132:135],v[100:103]
TR a[52:53],v195 offset:24672
TR a[54:55],v195 offset:24928
MM v[108:111],a[60:63],a[140:143],0
v_xor_b32 v216, 8, v215
v_lshl_add_u32 v230, v216, 4, v214
MM v[108:111],a[56:59],a[136:139],v[108:111]
buffer_load_dwordx4 v221, s[32:35], s95 offen offset:3072 lds
MM v[116:119],a[56:59],a[144:147],0
s_add_u32 s87, s89, s85
s_add_u32 s85, s85, 524288
v_xor_b32 v216, 12, v215
MM v[116:119],a[60:63],a[148:151],v[116:119]
s_cmp_ge_u32 s85, 5242880
s_cselect_b32 s85, 0, s85
s_add_u32 m0, s96, 114880
MM v[124:127],a[60:63],a[156:159],0
v_lshl_add_u32 v231, v216, 4, v214
s_lshl_b32 s90, s49, 19
MM v[124:127],a[56:59],a[152:155],v[124:127]
buffer_load_dwordx4 v253, s[16:19], s87 offen offset:0 lds
s_waitcnt vmcnt(10) lgkmcnt(8)
s_barrier
MM v[0:3],a[0:3],a[160:163],v[0:3]
s_add_u32 m0, s96, 115904
TR a[56:57],v195 offset:16456
TR a[58:59],v195 offset:16712
MM v[0:3],a[4:7],a[164:167],v[0:3]
TR a[60:61],v195 offset:24680
TR a[62:63],v195 offset:24936
MM v[8:11],a[4:7],a[172:175],v[8:11]
s_add_u32 s8, s8, s90
s_addc_u32 s9, s9, 0
s_mov_b32 s10, 524288
MM v[8:11],a[0:3],a[168:171],v[8:11]
s_mov_b32 s11, 131072
buffer_load_dwordx4 v254, s[16:19], s87 offen offset:0 lds
MM v[16:19],a[0:3],a[176:179],v[16:19]
s_add_u32 s87, s87, 65536
s_add_u32 s12, s12, s90
s_addc_u32 s13, s13, 0
MM v[16:19],a[4:7],a[180:183],v[16:19]
s_add_u32 m0, s96, 116936
s_mov_b32 s14, 525312
s_mov_b32 s15, 131072
MM v[24:27],a[4:7],a[188:191],v[24:27]
s_sub_u32 s12, s12, 1024
s_subb_u32 s13, s13, 0
s_lshr_b32 s90, s49, 2
MM v[24:27],a[0:3],a[184:187],v[24:27]
s_mul_i32 s90, s90, 655360
buffer_load_dwordx4 v253, s[16:19], s87 offen offset:0 lds
MM v[4:7],a[8:11],a[160:163],v[4:7]
s_add_u32 m0, s96, 117960
s_mul_i32 s77, s84, 320
s_add_u32 s90, s90, s77
MM v[4:7],a[12:15],a[164:167],v[4:7]
s_add_u32 s24, s24, s90
s_addc_u32 s25, s25, 0
s_sub_u32 s26, 5242880, s90
MM v[12:15],a[12:15],a[172:175],v[12:15]
s_mov_b32 s27, 131072
s_sub_u32 s77, %[sb], 76
s_min_u32 s47, s77, %[sb]
MM v[12:15],a[8:11],a[168:171],v[12:15]
s_mul_i32 s77, s47, 205
buffer_load_dwordx4 v254, s[16:19], s87 offen offset:0 lds
MM v[20:23],a[8:11],a[176:179],v[20:23]
s_lshr_b32 s46, s77, 11
s_mul_i32 s90, s46, 121600
s_lshl_b32 s77, s47, 7
MM v[20:23],a[12:15],a[180:183],v[20:23]
s_add_u32 s90, s90, s77
s_add_u32 s28, s28, s90
s_addc_u32 s29, s29, 0
MM v[28:31],a[12:15],a[188:191],v[28:31]
s_add_u32 s88, s68, s86
s_add_u32 s86, s86, 128
MM v[28:31],a[8:11],a[184:187],v[28:31]
s_sub_u32 s30, 2211840, s90
s_cmp_ge_u32 s86, 1280
s_cselect_b32 s86, 0, s86
v_smfmac_f32_16x16x64_bf16 v[184:187], v[236:239], a[0:7], v234
s_add_u32 m0, s94, 32768
s_add_u32 s95, s88, 0
v_smfmac_f32_16x16x64_bf16 v[188:191], v[236:239], a[8:15], v234
s_mov_b32 s31, 131072
s_mov_b32 s38, 2211840
s_mov_b64 exec, s[52:53]
buffer_store_dwordx4 v[128:131], v168, s[20:23], s82 offen offset:0 sc1
s_mov_b64 exec, -1
MM v[32:35],a[16:19],a[160:163],v[32:35]
s_mov_b32 s39, 131072
TR a[0:1],v192 offset:32896
TR a[2:3],v192 offset:33152
MM v[32:35],a[20:23],a[164:167],v[32:35]
s_mov_b32 s42, 2621440
TR a[4:5],v192 offset:41120
TR a[6:7],v192 offset:41376
buffer_load_dwordx4 v220, s[32:35], s95 offen offset:0 lds
MM v[40:43],a[20:23],a[172:175],v[40:43]
s_add_u32 s95, s88, 9216
TR a[8:9],v192 offset:32904
TR a[10:11],v192 offset:33160
MM v[40:43],a[16:19],a[168:171],v[40:43]
s_mov_b32 s43, 131072
TR a[12:13],v192 offset:41128
TR a[14:15],v192 offset:41384
MM v[48:51],a[16:19],a[176:179],v[48:51]
s_lshr_b32 s77, %[sb], 6
s_lshl_b32 s77, s77, 3
s_and_b32 s90, %[sb], 7
MM v[48:51],a[20:23],a[180:183],v[48:51]
s_add_u32 s77, s77, s90
buffer_load_dwordx4 v221, s[32:35], s95 offen offset:1024 lds
MM v[56:59],a[20:23],a[188:191],v[56:59]
s_add_u32 s95, s88, 18432
s_sub_u32 s90, s77, 8
MM v[56:59],a[16:19],a[184:187],v[56:59]
s_cmp_ge_u32 s77, 27
s_cselect_b32 s77, s90, s77
s_lshl_b32 s77, s77, 7
MM v[36:39],a[24:27],a[160:163],v[36:39]
s_lshl_b32 s90, %[sb], 1
TR a[16:17],v193 offset:32896
TR a[18:19],v193 offset:33152
MM v[36:39],a[28:31],a[164:167],v[36:39]
s_and_b32 s90, s90, 112
TR a[20:21],v193 offset:41120
TR a[22:23],v193 offset:41376
MM v[44:47],a[28:31],a[172:175],v[44:47]
s_add_u32 s48, s77, s90
buffer_load_dwordx4 v220, s[32:35], s95 offen offset:2048 lds
MM v[44:47],a[24:27],a[168:171],v[44:47]
s_add_u32 s95, s88, 27648
s_mov_b32 s71, 0x4e800000
s_mov_b32 s72, 0x3e0293ee
MM v[52:55],a[24:27],a[176:179],v[52:55]
s_lshl_b32 s64, %[swave], 12
MM v[52:55],a[28:31],a[180:183],v[52:55]
buffer_load_dwordx4 v221, s[32:35], s95 offen offset:3072 lds
MM v[60:63],a[28:31],a[188:191],v[60:63]
s_add_u32 m0, s94, 36864
s_add_u32 s95, s88, 40960
MM v[60:63],a[24:27],a[184:187],v[60:63]
MM v[64:67],a[32:35],a[160:163],v[64:67]
TR a[24:25],v193 offset:32904
TR a[26:27],v193 offset:33160
MM v[64:67],a[36:39],a[164:167],v[64:67]
TR a[28:29],v193 offset:41128
TR a[30:31],v193 offset:41384
MM v[72:75],a[36:39],a[172:175],v[72:75]
buffer_load_dwordx4 v220, s[32:35], s95 offen offset:0 lds
MM v[72:75],a[32:35],a[168:171],v[72:75]
s_add_u32 s95, s88, 50176
MM v[80:83],a[32:35],a[176:179],v[80:83]
MM v[80:83],a[36:39],a[180:183],v[80:83]
buffer_load_dwordx4 v221, s[32:35], s95 offen offset:1024 lds
MM v[88:91],a[36:39],a[188:191],v[88:91]
s_add_u32 s95, s88, 59392
MM v[88:91],a[32:35],a[184:187],v[88:91]
s_waitcnt lgkmcnt(15)
MM v[68:71],a[40:43],a[160:163],v[68:71]
TR a[32:33],v194 offset:32896
TR a[34:35],v194 offset:33152
MM v[68:71],a[44:47],a[164:167],v[68:71]
TR a[36:37],v194 offset:41120
TR a[38:39],v194 offset:41376
s_mov_b64 exec, s[52:53]
buffer_store_dwordx4 v[132:135], v168, s[20:23], s82 offen offset:256 sc1
s_mov_b64 exec, -1
MM v[76:79],a[44:47],a[172:175],v[76:79]
MM v[76:79],a[40:43],a[168:171],v[76:79]
MM v[84:87],a[40:43],a[176:179],v[84:87]
MM v[84:87],a[44:47],a[180:183],v[84:87]
buffer_load_dwordx4 v220, s[32:35], s95 offen offset:2048 lds
MM v[92:95],a[44:47],a[188:191],v[92:95]
s_add_u32 s95, s88, 68608
MM v[92:95],a[40:43],a[184:187],v[92:95]
MM v[96:99],a[48:51],a[160:163],v[96:99]
TR a[40:41],v194 offset:32904
TR a[42:43],v194 offset:33160
MM v[96:99],a[52:55],a[164:167],v[96:99]
TR a[44:45],v194 offset:41128
TR a[46:47],v194 offset:41384
MM v[104:107],a[52:55],a[172:175],v[104:107]
buffer_load_dwordx4 v221, s[32:35], s95 offen offset:3072 lds
MM v[104:107],a[48:51],a[168:171],v[104:107]
s_add_u32 s87, s89, s85
s_add_u32 s85, s85, 524288
MM v[112:115],a[48:51],a[176:179],v[112:115]
s_cmp_ge_u32 s85, 5242880
s_cselect_b32 s85, 0, s85
s_add_u32 m0, s96, 131328
MM v[112:115],a[52:55],a[180:183],v[112:115]
buffer_load_dwordx4 v253, s[16:19], s87 offen offset:0 lds
MM v[120:123],a[52:55],a[188:191],v[120:123]
s_add_u32 m0, s96, 132352
MM v[120:123],a[48:51],a[184:187],v[120:123]
MM v[100:103],a[56:59],a[160:163],v[100:103]
TR a[48:49],v195 offset:32896
TR a[50:51],v195 offset:33152
MM v[100:103],a[60:63],a[164:167],v[100:103]
TR a[52:53],v195 offset:41120
TR a[54:55],v195 offset:41376
MM v[108:111],a[60:63],a[172:175],v[108:111]
buffer_load_dwordx4 v254, s[16:19], s87 offen offset:0 lds
MM v[108:111],a[56:59],a[168:171],v[108:111]
s_add_u32 s87, s87, 65536
s_add_u32 m0, s96, 133384
MM v[116:119],a[56:59],a[176:179],v[116:119]
MM v[116:119],a[60:63],a[180:183],v[116:119]
buffer_load_dwordx4 v253, s[16:19], s87 offen offset:0 lds
MM v[124:127],a[60:63],a[188:191],v[124:127]
s_add_u32 m0, s96, 134408
MM v[124:127],a[56:59],a[184:187],v[124:127]
buffer_load_dwordx4 v254, s[16:19], s87 offen offset:0 lds
s_waitcnt vmcnt(18)
RD a[192:195],v222 offset:0
RD a[196:199],v223 offset:0
RD a[200:203],v217 offset:0
RD a[204:207],v218 offset:0
RD a[208:211],v222 offset:4096
RD a[212:215],v223 offset:4096
RD a[216:219],v217 offset:4096
RD a[220:223],v218 offset:4096
s_waitcnt vmcnt(14) lgkmcnt(0)
s_barrier
MM v[0:3],a[0:3],a[192:195],v[0:3]
TR a[56:57],v195 offset:32904
TR a[58:59],v195 offset:33160
MM v[0:3],a[4:7],a[196:199],v[0:3]
TR a[60:61],v195 offset:41128
TR a[62:63],v195 offset:41384
s_mov_b64 exec, s[52:53]
buffer_store_dwordx4 v[136:139], v168, s[20:23], s82 offen offset:512 sc1
s_mov_b64 exec, -1
MM v[8:11],a[4:7],a[204:207],v[8:11]
MM v[8:11],a[0:3],a[200:203],v[8:11]
)ASM"
R"ASM(
s_add_u32 s88, s68, s86
s_add_u32 s86, s86, 128
MM v[16:19],a[0:3],a[208:211],v[16:19]
s_cmp_ge_u32 s86, 1280
s_cselect_b32 s86, 0, s86
s_add_u32 m0, s94, 0
MM v[16:19],a[4:7],a[212:215],v[16:19]
s_add_u32 s95, s88, 0
buffer_load_dwordx4 v220, s[32:35], s95 offen offset:0 lds
MM v[24:27],a[4:7],a[220:223],v[24:27]
s_add_u32 s95, s88, 9216
MM v[24:27],a[0:3],a[216:219],v[24:27]
MM v[4:7],a[8:11],a[192:195],v[4:7]
MM v[4:7],a[12:15],a[196:199],v[4:7]
buffer_load_dwordx4 v221, s[32:35], s95 offen offset:1024 lds
MM v[12:15],a[12:15],a[204:207],v[12:15]
s_add_u32 s95, s88, 18432
MM v[12:15],a[8:11],a[200:203],v[12:15]
MM v[20:23],a[8:11],a[208:211],v[20:23]
MM v[20:23],a[12:15],a[212:215],v[20:23]
buffer_load_dwordx4 v220, s[32:35], s95 offen offset:2048 lds
MM v[28:31],a[12:15],a[220:223],v[28:31]
s_add_u32 s95, s88, 27648
MM v[28:31],a[8:11],a[216:219],v[28:31]
v_smfmac_f32_16x16x64_bf16 v[184:187], v[236:239], a[0:7], v234
v_smfmac_f32_16x16x64_bf16 v[188:191], v[236:239], a[8:15], v234
MM v[32:35],a[16:19],a[192:195],v[32:35]
TR a[0:1],v196 offset:0
TR a[2:3],v196 offset:256
MM v[32:35],a[20:23],a[196:199],v[32:35]
TR a[4:5],v196 offset:8224
TR a[6:7],v196 offset:8480
buffer_load_dwordx4 v221, s[32:35], s95 offen offset:3072 lds
MM v[40:43],a[20:23],a[204:207],v[40:43]
s_add_u32 m0, s94, 4096
TR a[8:9],v196 offset:8
TR a[10:11],v196 offset:264
MM v[40:43],a[16:19],a[200:203],v[40:43]
s_add_u32 s95, s88, 40960
TR a[12:13],v196 offset:8232
TR a[14:15],v196 offset:8488
s_mov_b64 exec, s[52:53]
buffer_store_dwordx4 v[140:143], v168, s[20:23], s82 offen offset:768 sc1
s_mov_b64 exec, -1
MM v[48:51],a[16:19],a[208:211],v[48:51]
MM v[48:51],a[20:23],a[212:215],v[48:51]
MM v[56:59],a[20:23],a[220:223],v[56:59]
MM v[56:59],a[16:19],a[216:219],v[56:59]
buffer_load_dwordx4 v220, s[32:35], s95 offen offset:0 lds
MM v[36:39],a[24:27],a[192:195],v[36:39]
s_add_u32 s95, s88, 50176
TR a[16:17],v197 offset:0
TR a[18:19],v197 offset:256
MM v[36:39],a[28:31],a[196:199],v[36:39]
TR a[20:21],v197 offset:8224
TR a[22:23],v197 offset:8480
MM v[44:47],a[28:31],a[204:207],v[44:47]
MM v[44:47],a[24:27],a[200:203],v[44:47]
buffer_load_dwordx4 v221, s[32:35], s95 offen offset:1024 lds
MM v[52:55],a[24:27],a[208:211],v[52:55]
s_add_u32 s95, s88, 59392
MM v[52:55],a[28:31],a[212:215],v[52:55]
MM v[60:63],a[28:31],a[220:223],v[60:63]
MM v[60:63],a[24:27],a[216:219],v[60:63]
buffer_load_dwordx4 v220, s[32:35], s95 offen offset:2048 lds
MM v[64:67],a[32:35],a[192:195],v[64:67]
s_add_u32 s95, s88, 68608
TR a[24:25],v197 offset:8
TR a[26:27],v197 offset:264
MM v[64:67],a[36:39],a[196:199],v[64:67]
TR a[28:29],v197 offset:8232
TR a[30:31],v197 offset:8488
MM v[72:75],a[36:39],a[204:207],v[72:75]
MM v[72:75],a[32:35],a[200:203],v[72:75]
buffer_load_dwordx4 v221, s[32:35], s95 offen offset:3072 lds
MM v[80:83],a[32:35],a[208:211],v[80:83]
s_add_u32 s87, s89, s85
s_add_u32 s85, s85, 524288
MM v[80:83],a[36:39],a[212:215],v[80:83]
s_cmp_ge_u32 s85, 5242880
s_cselect_b32 s85, 0, s85
s_add_u32 m0, s96, 65536
MM v[88:91],a[36:39],a[220:223],v[88:91]
MM v[88:91],a[32:35],a[216:219],v[88:91]
buffer_load_dwordx4 v253, s[16:19], s87 offen offset:0 lds
MM v[68:71],a[40:43],a[192:195],v[68:71]
s_add_u32 m0, s96, 66560
TR a[32:33],v198 offset:0
TR a[34:35],v198 offset:256
MM v[68:71],a[44:47],a[196:199],v[68:71]
TR a[36:37],v198 offset:8224
TR a[38:39],v198 offset:8480
MM v[76:79],a[44:47],a[204:207],v[76:79]
s_nop 0
LPG2_%=:
s_cbranch_execz LPG6_%=
MM v[76:79],a[40:43],a[200:203],v[76:79]
buffer_load_dwordx4 v254, s[16:19], s87 offen offset:0 lds
MM v[84:87],a[40:43],a[208:211],v[84:87]
s_add_u32 s87, s87, 65536
s_add_u32 m0, s96, 67592
MM v[84:87],a[44:47],a[212:215],v[84:87]
MM v[92:95],a[44:47],a[220:223],v[92:95]
MM v[92:95],a[40:43],a[216:219],v[92:95]
buffer_load_dwordx4 v253, s[16:19], s87 offen offset:0 lds
MM v[96:99],a[48:51],a[192:195],v[96:99]
s_add_u32 m0, s96, 68616
TR a[40:41],v198 offset:8
TR a[42:43],v198 offset:264
MM v[96:99],a[52:55],a[196:199],v[96:99]
TR a[44:45],v198 offset:8232
TR a[46:47],v198 offset:8488
MM v[104:107],a[52:55],a[204:207],v[104:107]
MM v[104:107],a[48:51],a[200:203],v[104:107]
buffer_load_dwordx4 v254, s[16:19], s87 offen offset:0 lds
MM v[112:115],a[48:51],a[208:211],v[112:115]
s_waitcnt vmcnt(18)
RD a[224:227],v222 offset:32768
MM v[112:115],a[52:55],a[212:215],v[112:115]
RD a[228:231],v223 offset:32768
MM v[120:123],a[52:55],a[220:223],v[120:123]
RD a[232:235],v217 offset:32768
MM v[120:123],a[48:51],a[216:219],v[120:123]
s_waitcnt lgkmcnt(15)
s_mov_b64 exec, s[52:53]
buffer_store_dwordx4 v[144:147], v168, s[20:23], s82 offen offset:1024 sc1
s_mov_b64 exec, -1
MM v[100:103],a[56:59],a[192:195],v[100:103]
TR a[48:49],v199 offset:0
TR a[50:51],v199 offset:256
MM v[100:103],a[60:63],a[196:199],v[100:103]
TR a[52:53],v199 offset:8224
TR a[54:55],v199 offset:8480
RD a[236:239],v218 offset:32768
MM v[108:111],a[60:63],a[204:207],v[108:111]
RD a[240:243],v222 offset:36864
MM v[108:111],a[56:59],a[200:203],v[108:111]
RD a[244:247],v223 offset:36864
MM v[116:119],a[56:59],a[208:211],v[116:119]
RD a[248:251],v217 offset:36864
MM v[116:119],a[60:63],a[212:215],v[116:119]
RD a[252:255],v218 offset:36864
v_lshlrev_b32 v173, 2, v181
MM v[124:127],a[60:63],a[220:223],v[124:127]
s_mov_b64 exec, s[54:55]
buffer_store_dword v160, v173, s[20:23], s78 offen offset:0 sc1
s_mov_b64 exec, -1
MM v[124:127],a[56:59],a[216:219],v[124:127]
v_mov_b32 v170, s69
v_mov_b32 v171, s69
s_waitcnt vmcnt(16) lgkmcnt(0)
s_barrier
MM v[0:3],a[0:3],a[224:227],v[0:3]
TR a[56:57],v199 offset:8
TR a[58:59],v199 offset:264
MM v[0:3],a[4:7],a[228:231],v[0:3]
TR a[60:61],v199 offset:8232
TR a[62:63],v199 offset:8488
MM v[8:11],a[4:7],a[236:239],v[8:11]
s_add_u32 s88, s68, s86
s_add_u32 s86, s86, 128
MM v[8:11],a[0:3],a[232:235],v[8:11]
s_cmp_ge_u32 s86, 1280
s_cselect_b32 s86, 0, s86
s_add_u32 m0, s94, 32768
MM v[16:19],a[0:3],a[240:243],v[16:19]
s_add_u32 s95, s88, 0
buffer_load_dwordx4 v220, s[32:35], s95 offen offset:0 lds
MM v[16:19],a[4:7],a[244:247],v[16:19]
s_add_u32 s95, s88, 9216
MM v[24:27],a[4:7],a[252:255],v[24:27]
buffer_load_dwordx4 v221, s[32:35], s95 offen offset:1024 lds
MM v[24:27],a[0:3],a[248:251],v[24:27]
s_add_u32 s95, s88, 18432
MM v[4:7],a[8:11],a[224:227],v[4:7]
MM v[4:7],a[12:15],a[228:231],v[4:7]
MM v[12:15],a[12:15],a[236:239],v[12:15]
buffer_load_dwordx4 v220, s[32:35], s95 offen offset:2048 lds
MM v[12:15],a[8:11],a[232:235],v[12:15]
s_add_u32 s95, s88, 27648
MM v[20:23],a[8:11],a[240:243],v[20:23]
MM v[20:23],a[12:15],a[244:247],v[20:23]
MM v[28:31],a[12:15],a[252:255],v[28:31]
buffer_load_dwordx4 v221, s[32:35], s95 offen offset:3072 lds
MM v[28:31],a[8:11],a[248:251],v[28:31]
s_add_u32 m0, s94, 36864
s_add_u32 s95, s88, 40960
v_smfmac_f32_16x16x64_bf16 v[184:187], v[236:239], a[0:7], v234
v_smfmac_f32_16x16x64_bf16 v[188:191], v[236:239], a[8:15], v234
MM v[32:35],a[16:19],a[224:227],v[32:35]
TR a[0:1],v196 offset:16448
TR a[2:3],v196 offset:16704
MM v[32:35],a[20:23],a[228:231],v[32:35]
TR a[4:5],v196 offset:24672
TR a[6:7],v196 offset:24928
MM v[40:43],a[20:23],a[236:239],v[40:43]
TR a[8:9],v196 offset:16456
TR a[10:11],v196 offset:16712
buffer_load_dwordx4 v220, s[32:35], s95 offen offset:0 lds
MM v[40:43],a[16:19],a[232:235],v[40:43]
s_add_u32 s95, s88, 50176
TR a[12:13],v196 offset:24680
TR a[14:15],v196 offset:24936
MM v[48:51],a[16:19],a[240:243],v[48:51]
buffer_load_dwordx4 v221, s[32:35], s95 offen offset:1024 lds
MM v[48:51],a[20:23],a[244:247],v[48:51]
s_add_u32 s95, s88, 59392
MM v[56:59],a[20:23],a[252:255],v[56:59]
MM v[56:59],a[16:19],a[248:251],v[56:59]
MM v[36:39],a[24:27],a[224:227],v[36:39]
TR a[16:17],v197 offset:16448
TR a[18:19],v197 offset:16704
MM v[36:39],a[28:31],a[228:231],v[36:39]
TR a[20:21],v197 offset:24672
TR a[22:23],v197 offset:24928
MM v[44:47],a[28:31],a[236:239],v[44:47]
buffer_load_dwordx4 v220, s[32:35], s95 offen offset:2048 lds
MM v[44:47],a[24:27],a[232:235],v[44:47]
s_add_u32 s95, s88, 68608
MM v[52:55],a[24:27],a[240:243],v[52:55]
buffer_load_dwordx4 v221, s[32:35], s95 offen offset:3072 lds
MM v[52:55],a[28:31],a[244:247],v[52:55]
s_add_u32 s87, s89, s85
s_add_u32 s85, s85, 524288
MM v[60:63],a[28:31],a[252:255],v[60:63]
s_cmp_ge_u32 s85, 5242880
s_cselect_b32 s85, 0, s85
s_add_u32 m0, s96, 81984
MM v[60:63],a[24:27],a[248:251],v[60:63]
MM v[64:67],a[32:35],a[224:227],v[64:67]
TR a[24:25],v197 offset:16456
TR a[26:27],v197 offset:16712
MM v[64:67],a[36:39],a[228:231],v[64:67]
TR a[28:29],v197 offset:24680
TR a[30:31],v197 offset:24936
MM v[72:75],a[36:39],a[236:239],v[72:75]
buffer_load_dwordx4 v253, s[16:19], s87 offen offset:0 lds
MM v[72:75],a[32:35],a[232:235],v[72:75]
s_add_u32 m0, s96, 83008
MM v[80:83],a[32:35],a[240:243],v[80:83]
buffer_load_dwordx4 v254, s[16:19], s87 offen offset:0 lds
MM v[80:83],a[36:39],a[244:247],v[80:83]
s_add_u32 s87, s87, 65536
s_add_u32 m0, s96, 84040
MM v[88:91],a[36:39],a[252:255],v[88:91]
MM v[88:91],a[32:35],a[248:251],v[88:91]
MM v[68:71],a[40:43],a[224:227],v[68:71]
TR a[32:33],v198 offset:16448
TR a[34:35],v198 offset:16704
MM v[68:71],a[44:47],a[228:231],v[68:71]
TR a[36:37],v198 offset:24672
TR a[38:39],v198 offset:24928
MM v[76:79],a[44:47],a[236:239],v[76:79]
buffer_load_dwordx4 v253, s[16:19], s87 offen offset:0 lds
MM v[76:79],a[40:43],a[232:235],v[76:79]
s_add_u32 m0, s96, 85064
MM v[84:87],a[40:43],a[240:243],v[84:87]
buffer_load_dwordx4 v254, s[16:19], s87 offen offset:0 lds
MM v[84:87],a[44:47],a[244:247],v[84:87]
s_waitcnt vmcnt(18)
RD a[128:131],v222 offset:0
MM v[92:95],a[44:47],a[252:255],v[92:95]
RD a[132:135],v223 offset:0
MM v[92:95],a[40:43],a[248:251],v[92:95]
RD a[136:139],v217 offset:0
MM v[96:99],a[48:51],a[224:227],v[96:99]
TR a[40:41],v198 offset:16456
TR a[42:43],v198 offset:16712
MM v[96:99],a[52:55],a[228:231],v[96:99]
TR a[44:45],v198 offset:24680
TR a[46:47],v198 offset:24936
MM v[104:107],a[52:55],a[236:239],v[104:107]
RD a[140:143],v218 offset:0
MM v[104:107],a[48:51],a[232:235],v[104:107]
RD a[144:147],v222 offset:4096
MM v[112:115],a[48:51],a[240:243],v[112:115]
RD a[148:151],v223 offset:4096
MM v[112:115],a[52:55],a[244:247],v[112:115]
RD a[152:155],v217 offset:4096
MM v[120:123],a[52:55],a[252:255],v[120:123]
RD a[156:159],v218 offset:4096
MM v[120:123],a[48:51],a[248:251],v[120:123]
s_waitcnt lgkmcnt(15)
MM v[100:103],a[56:59],a[224:227],v[100:103]
TR a[48:49],v199 offset:16448
TR a[50:51],v199 offset:16704
MM v[100:103],a[60:63],a[228:231],v[100:103]
TR a[52:53],v199 offset:24672
TR a[54:55],v199 offset:24928
MM v[108:111],a[60:63],a[236:239],v[108:111]
s_waitcnt lgkmcnt(4)
s_add_u32 s88, s68, s86
s_add_u32 s86, s86, 128
MM v[108:111],a[56:59],a[232:235],v[108:111]
s_cmp_ge_u32 s86, 1280
s_cselect_b32 s86, 0, s86
s_add_u32 m0, s94, 0
MM v[116:119],a[56:59],a[240:243],v[116:119]
s_add_u32 s95, s88, 0
buffer_load_dwordx4 v220, s[32:35], s95 offen offset:0 lds
MM v[116:119],a[60:63],a[244:247],v[116:119]
s_add_u32 s95, s88, 9216
MM v[124:127],a[60:63],a[252:255],v[124:127]
buffer_load_dwordx4 v221, s[32:35], s95 offen offset:1024 lds
MM v[124:127],a[56:59],a[248:251],v[124:127]
s_add_u32 s95, s88, 18432
s_waitcnt vmcnt(16)
s_barrier
MM v[0:3],a[0:3],a[128:131],v[0:3]
TR a[56:57],v199 offset:16456
TR a[58:59],v199 offset:16712
MM v[0:3],a[4:7],a[132:135],v[0:3]
TR a[60:61],v199 offset:24680
TR a[62:63],v199 offset:24936
MM v[8:11],a[4:7],a[140:143],v[8:11]
buffer_load_dwordx4 v220, s[32:35], s95 offen offset:2048 lds
MM v[8:11],a[0:3],a[136:139],v[8:11]
s_add_u32 s95, s88, 27648
MM v[16:19],a[0:3],a[144:147],v[16:19]
MM v[16:19],a[4:7],a[148:151],v[16:19]
MM v[24:27],a[4:7],a[156:159],v[24:27]
buffer_load_dwordx4 v221, s[32:35], s95 offen offset:3072 lds
MM v[24:27],a[0:3],a[152:155],v[24:27]
s_add_u32 m0, s94, 4096
s_add_u32 s95, s88, 40960
MM v[4:7],a[8:11],a[128:131],v[4:7]
MM v[4:7],a[12:15],a[132:135],v[4:7]
MM v[12:15],a[12:15],a[140:143],v[12:15]
buffer_load_dwordx4 v220, s[32:35], s95 offen offset:0 lds
MM v[12:15],a[8:11],a[136:139],v[12:15]
s_add_u32 s95, s88, 50176
)ASM"
R"ASM(
MM v[20:23],a[8:11],a[144:147],v[20:23]
MM v[20:23],a[12:15],a[148:151],v[20:23]
MM v[28:31],a[12:15],a[156:159],v[28:31]
buffer_load_dwordx4 v221, s[32:35], s95 offen offset:1024 lds
MM v[28:31],a[8:11],a[152:155],v[28:31]
s_add_u32 s95, s88, 59392
v_smfmac_f32_16x16x64_bf16 v[184:187], v[236:239], a[0:7], v234
v_smfmac_f32_16x16x64_bf16 v[188:191], v[236:239], a[8:15], v234
MM v[32:35],a[16:19],a[128:131],v[32:35]
TR a[0:1],v192 offset:0
TR a[2:3],v192 offset:256
MM v[32:35],a[20:23],a[132:135],v[32:35]
TR a[4:5],v192 offset:8224
TR a[6:7],v192 offset:8480
MM v[40:43],a[20:23],a[140:143],v[40:43]
TR a[8:9],v192 offset:8
TR a[10:11],v192 offset:264
buffer_load_dwordx4 v220, s[32:35], s95 offen offset:2048 lds
MM v[40:43],a[16:19],a[136:139],v[40:43]
s_add_u32 s95, s88, 68608
TR a[12:13],v192 offset:8232
TR a[14:15],v192 offset:8488
MM v[48:51],a[16:19],a[144:147],v[48:51]
buffer_load_dwordx4 v221, s[32:35], s95 offen offset:3072 lds
MM v[48:51],a[20:23],a[148:151],v[48:51]
s_add_u32 s87, s89, s85
s_add_u32 s85, s85, 524288
MM v[56:59],a[20:23],a[156:159],v[56:59]
s_cmp_ge_u32 s85, 5242880
s_cselect_b32 s85, 0, s85
s_add_u32 m0, s96, 98432
MM v[56:59],a[16:19],a[152:155],v[56:59]
MM v[36:39],a[24:27],a[128:131],v[36:39]
TR a[16:17],v193 offset:0
TR a[18:19],v193 offset:256
MM v[36:39],a[28:31],a[132:135],v[36:39]
TR a[20:21],v193 offset:8224
TR a[22:23],v193 offset:8480
MM v[44:47],a[28:31],a[140:143],v[44:47]
buffer_load_dwordx4 v253, s[16:19], s87 offen offset:0 lds
MM v[44:47],a[24:27],a[136:139],v[44:47]
s_add_u32 m0, s96, 99456
MM v[52:55],a[24:27],a[144:147],v[52:55]
buffer_load_dwordx4 v254, s[16:19], s87 offen offset:0 lds
MM v[52:55],a[28:31],a[148:151],v[52:55]
s_add_u32 s87, s87, 65536
s_add_u32 m0, s96, 100488
MM v[60:63],a[28:31],a[156:159],v[60:63]
MM v[60:63],a[24:27],a[152:155],v[60:63]
MM v[64:67],a[32:35],a[128:131],v[64:67]
TR a[24:25],v193 offset:8
TR a[26:27],v193 offset:264
MM v[64:67],a[36:39],a[132:135],v[64:67]
TR a[28:29],v193 offset:8232
TR a[30:31],v193 offset:8488
MM v[72:75],a[36:39],a[140:143],v[72:75]
buffer_load_dwordx4 v253, s[16:19], s87 offen offset:0 lds
MM v[72:75],a[32:35],a[136:139],v[72:75]
s_add_u32 m0, s96, 101512
MM v[80:83],a[32:35],a[144:147],v[80:83]
buffer_load_dwordx4 v254, s[16:19], s87 offen offset:0 lds
MM v[80:83],a[36:39],a[148:151],v[80:83]
s_waitcnt vmcnt(16)
RD a[160:163],v222 offset:32768
MM v[88:91],a[36:39],a[156:159],v[88:91]
RD a[164:167],v223 offset:32768
MM v[88:91],a[32:35],a[152:155],v[88:91]
RD a[168:171],v217 offset:32768
MM v[68:71],a[40:43],a[128:131],v[68:71]
TR a[32:33],v194 offset:0
TR a[34:35],v194 offset:256
MM v[68:71],a[44:47],a[132:135],v[68:71]
TR a[36:37],v194 offset:8224
TR a[38:39],v194 offset:8480
MM v[76:79],a[44:47],a[140:143],v[76:79]
RD a[172:175],v218 offset:32768
MM v[76:79],a[40:43],a[136:139],v[76:79]
RD a[176:179],v222 offset:36864
MM v[84:87],a[40:43],a[144:147],v[84:87]
RD a[180:183],v223 offset:36864
MM v[84:87],a[44:47],a[148:151],v[84:87]
RD a[184:187],v217 offset:36864
MM v[92:95],a[44:47],a[156:159],v[92:95]
RD a[188:191],v218 offset:36864
MM v[92:95],a[40:43],a[152:155],v[92:95]
s_waitcnt lgkmcnt(15)
MM v[96:99],a[48:51],a[128:131],v[96:99]
TR a[40:41],v194 offset:8
TR a[42:43],v194 offset:264
MM v[96:99],a[52:55],a[132:135],v[96:99]
TR a[44:45],v194 offset:8232
TR a[46:47],v194 offset:8488
MM v[104:107],a[52:55],a[140:143],v[104:107]
s_waitcnt lgkmcnt(4)
s_add_u32 s88, s68, s86
s_add_u32 s86, s86, 128
MM v[104:107],a[48:51],a[136:139],v[104:107]
s_cmp_ge_u32 s86, 1280
s_cselect_b32 s86, 0, s86
s_add_u32 m0, s94, 32768
MM v[112:115],a[48:51],a[144:147],v[112:115]
s_add_u32 s95, s88, 0
buffer_load_dwordx4 v220, s[32:35], s95 offen offset:0 lds
MM v[112:115],a[52:55],a[148:151],v[112:115]
s_add_u32 s95, s88, 9216
MM v[120:123],a[52:55],a[156:159],v[120:123]
buffer_load_dwordx4 v221, s[32:35], s95 offen offset:1024 lds
MM v[120:123],a[48:51],a[152:155],v[120:123]
s_add_u32 s95, s88, 18432
MM v[100:103],a[56:59],a[128:131],v[100:103]
TR a[48:49],v195 offset:0
TR a[50:51],v195 offset:256
MM v[100:103],a[60:63],a[132:135],v[100:103]
TR a[52:53],v195 offset:8224
TR a[54:55],v195 offset:8480
MM v[108:111],a[60:63],a[140:143],v[108:111]
buffer_load_dwordx4 v220, s[32:35], s95 offen offset:2048 lds
MM v[108:111],a[56:59],a[136:139],v[108:111]
s_add_u32 s95, s88, 27648
MM v[116:119],a[56:59],a[144:147],v[116:119]
MM v[116:119],a[60:63],a[148:151],v[116:119]
MM v[124:127],a[60:63],a[156:159],v[124:127]
buffer_load_dwordx4 v221, s[32:35], s95 offen offset:3072 lds
MM v[124:127],a[56:59],a[152:155],v[124:127]
s_add_u32 m0, s94, 36864
s_add_u32 s95, s88, 40960
s_waitcnt vmcnt(16)
s_barrier
MM v[0:3],a[0:3],a[160:163],v[0:3]
TR a[56:57],v195 offset:8
TR a[58:59],v195 offset:264
MM v[0:3],a[4:7],a[164:167],v[0:3]
TR a[60:61],v195 offset:8232
TR a[62:63],v195 offset:8488
MM v[8:11],a[4:7],a[172:175],v[8:11]
buffer_load_dwordx4 v220, s[32:35], s95 offen offset:0 lds
MM v[8:11],a[0:3],a[168:171],v[8:11]
s_add_u32 s95, s88, 50176
MM v[16:19],a[0:3],a[176:179],v[16:19]
MM v[16:19],a[4:7],a[180:183],v[16:19]
MM v[24:27],a[4:7],a[188:191],v[24:27]
buffer_load_dwordx4 v221, s[32:35], s95 offen offset:1024 lds
MM v[24:27],a[0:3],a[184:187],v[24:27]
s_add_u32 s95, s88, 59392
MM v[4:7],a[8:11],a[160:163],v[4:7]
MM v[4:7],a[12:15],a[164:167],v[4:7]
MM v[12:15],a[12:15],a[172:175],v[12:15]
buffer_load_dwordx4 v220, s[32:35], s95 offen offset:2048 lds
MM v[12:15],a[8:11],a[168:171],v[12:15]
s_add_u32 s95, s88, 68608
MM v[20:23],a[8:11],a[176:179],v[20:23]
MM v[20:23],a[12:15],a[180:183],v[20:23]
MM v[28:31],a[12:15],a[188:191],v[28:31]
buffer_load_dwordx4 v221, s[32:35], s95 offen offset:3072 lds
MM v[28:31],a[8:11],a[184:187],v[28:31]
s_add_u32 s87, s89, s85
s_add_u32 s85, s85, 524288
v_smfmac_f32_16x16x64_bf16 v[184:187], v[236:239], a[0:7], v234
s_cmp_ge_u32 s85, 5242880
s_cselect_b32 s85, 0, s85
v_smfmac_f32_16x16x64_bf16 v[188:191], v[236:239], a[8:15], v234
s_add_u32 m0, s96, 114880
MM v[32:35],a[16:19],a[160:163],v[32:35]
TR a[0:1],v192 offset:16448
TR a[2:3],v192 offset:16704
MM v[32:35],a[20:23],a[164:167],v[32:35]
TR a[4:5],v192 offset:24672
TR a[6:7],v192 offset:24928
MM v[40:43],a[20:23],a[172:175],v[40:43]
TR a[8:9],v192 offset:16456
TR a[10:11],v192 offset:16712
buffer_load_dwordx4 v253, s[16:19], s87 offen offset:0 lds
MM v[40:43],a[16:19],a[168:171],v[40:43]
s_add_u32 m0, s96, 115904
TR a[12:13],v192 offset:24680
TR a[14:15],v192 offset:24936
MM v[48:51],a[16:19],a[176:179],v[48:51]
buffer_load_dwordx4 v254, s[16:19], s87 offen offset:0 lds
MM v[48:51],a[20:23],a[180:183],v[48:51]
s_add_u32 s87, s87, 65536
s_add_u32 m0, s96, 116936
MM v[56:59],a[20:23],a[188:191],v[56:59]
MM v[56:59],a[16:19],a[184:187],v[56:59]
MM v[36:39],a[24:27],a[160:163],v[36:39]
TR a[16:17],v193 offset:16448
TR a[18:19],v193 offset:16704
MM v[36:39],a[28:31],a[164:167],v[36:39]
TR a[20:21],v193 offset:24672
TR a[22:23],v193 offset:24928
MM v[44:47],a[28:31],a[172:175],v[44:47]
buffer_load_dwordx4 v253, s[16:19], s87 offen offset:0 lds
MM v[44:47],a[24:27],a[168:171],v[44:47]
s_add_u32 m0, s96, 117960
MM v[52:55],a[24:27],a[176:179],v[52:55]
buffer_load_dwordx4 v254, s[16:19], s87 offen offset:0 lds
MM v[52:55],a[28:31],a[180:183],v[52:55]
s_waitcnt vmcnt(16)
RD a[192:195],v222 offset:0
MM v[60:63],a[28:31],a[188:191],v[60:63]
RD a[196:199],v223 offset:0
MM v[60:63],a[24:27],a[184:187],v[60:63]
RD a[200:203],v217 offset:0
MM v[64:67],a[32:35],a[160:163],v[64:67]
TR a[24:25],v193 offset:16456
TR a[26:27],v193 offset:16712
MM v[64:67],a[36:39],a[164:167],v[64:67]
TR a[28:29],v193 offset:24680
TR a[30:31],v193 offset:24936
MM v[72:75],a[36:39],a[172:175],v[72:75]
RD a[204:207],v218 offset:0
MM v[72:75],a[32:35],a[168:171],v[72:75]
RD a[208:211],v222 offset:4096
MM v[80:83],a[32:35],a[176:179],v[80:83]
RD a[212:215],v223 offset:4096
MM v[80:83],a[36:39],a[180:183],v[80:83]
RD a[216:219],v217 offset:4096
MM v[88:91],a[36:39],a[188:191],v[88:91]
RD a[220:223],v218 offset:4096
MM v[88:91],a[32:35],a[184:187],v[88:91]
s_waitcnt lgkmcnt(15)
MM v[68:71],a[40:43],a[160:163],v[68:71]
TR a[32:33],v194 offset:16448
TR a[34:35],v194 offset:16704
MM v[68:71],a[44:47],a[164:167],v[68:71]
TR a[36:37],v194 offset:24672
TR a[38:39],v194 offset:24928
MM v[76:79],a[44:47],a[172:175],v[76:79]
s_waitcnt lgkmcnt(4)
s_add_u32 s88, s68, s86
s_add_u32 s86, s86, 128
MM v[76:79],a[40:43],a[168:171],v[76:79]
s_cmp_ge_u32 s86, 1280
s_cselect_b32 s86, 0, s86
s_add_u32 m0, s94, 0
MM v[84:87],a[40:43],a[176:179],v[84:87]
s_add_u32 s95, s88, 0
buffer_load_dwordx4 v220, s[32:35], s95 offen offset:0 lds
MM v[84:87],a[44:47],a[180:183],v[84:87]
s_add_u32 s95, s88, 9216
MM v[92:95],a[44:47],a[188:191],v[92:95]
buffer_load_dwordx4 v221, s[32:35], s95 offen offset:1024 lds
MM v[92:95],a[40:43],a[184:187],v[92:95]
s_add_u32 s95, s88, 18432
MM v[96:99],a[48:51],a[160:163],v[96:99]
TR a[40:41],v194 offset:16456
TR a[42:43],v194 offset:16712
MM v[96:99],a[52:55],a[164:167],v[96:99]
TR a[44:45],v194 offset:24680
TR a[46:47],v194 offset:24936
MM v[104:107],a[52:55],a[172:175],v[104:107]
buffer_load_dwordx4 v220, s[32:35], s95 offen offset:2048 lds
MM v[104:107],a[48:51],a[168:171],v[104:107]
s_add_u32 s95, s88, 27648
MM v[112:115],a[48:51],a[176:179],v[112:115]
MM v[112:115],a[52:55],a[180:183],v[112:115]
MM v[120:123],a[52:55],a[188:191],v[120:123]
buffer_load_dwordx4 v221, s[32:35], s95 offen offset:3072 lds
MM v[120:123],a[48:51],a[184:187],v[120:123]
s_add_u32 m0, s94, 4096
s_add_u32 s95, s88, 40960
MM v[100:103],a[56:59],a[160:163],v[100:103]
TR a[48:49],v195 offset:16448
TR a[50:51],v195 offset:16704
MM v[100:103],a[60:63],a[164:167],v[100:103]
TR a[52:53],v195 offset:24672
TR a[54:55],v195 offset:24928
MM v[108:111],a[60:63],a[172:175],v[108:111]
buffer_load_dwordx4 v220, s[32:35], s95 offen offset:0 lds
MM v[108:111],a[56:59],a[168:171],v[108:111]
s_add_u32 s95, s88, 50176
MM v[116:119],a[56:59],a[176:179],v[116:119]
MM v[116:119],a[60:63],a[180:183],v[116:119]
MM v[124:127],a[60:63],a[188:191],v[124:127]
buffer_load_dwordx4 v221, s[32:35], s95 offen offset:1024 lds
MM v[124:127],a[56:59],a[184:187],v[124:127]
s_add_u32 s95, s88, 59392
s_waitcnt vmcnt(18)
s_barrier
MM v[0:3],a[0:3],a[192:195],v[0:3]
TR a[56:57],v195 offset:16456
TR a[58:59],v195 offset:16712
MM v[0:3],a[4:7],a[196:199],v[0:3]
TR a[60:61],v195 offset:24680
TR a[62:63],v195 offset:24936
MM v[8:11],a[4:7],a[204:207],v[8:11]
buffer_load_dwordx4 v220, s[32:35], s95 offen offset:2048 lds
v_and_b32 v208, 240, v248
MM v[8:11],a[0:3],a[200:203],v[8:11]
s_add_u32 s95, s88, 68608
s_add_u32 s93, s93, 36864
MM v[16:19],a[0:3],a[208:211],v[16:19]
MM v[16:19],a[4:7],a[212:215],v[16:19]
MM v[24:27],a[4:7],a[220:223],v[24:27]
buffer_load_dwordx4 v221, s[32:35], s95 offen offset:3072 lds
MM v[24:27],a[0:3],a[216:219],v[24:27]
s_add_u32 s87, s89, s85
s_add_u32 s85, s85, 524288
MM v[4:7],a[8:11],a[192:195],v[4:7]
s_cmp_ge_u32 s85, 5242880
s_cselect_b32 s85, 0, s85
s_nop 0
LPG3_%=:
s_cbranch_execz LPG7_%=
s_add_u32 m0, s96, 131328
MM v[4:7],a[12:15],a[196:199],v[4:7]
MM v[12:15],a[12:15],a[204:207],v[12:15]
buffer_load_dwordx4 v253, s[16:19], s87 offen offset:0 lds
MM v[12:15],a[8:11],a[200:203],v[12:15]
s_add_u32 m0, s96, 132352
MM v[20:23],a[8:11],a[208:211],v[20:23]
MM v[20:23],a[12:15],a[212:215],v[20:23]
MM v[28:31],a[12:15],a[220:223],v[28:31]
buffer_load_dwordx4 v254, s[16:19], s87 offen offset:0 lds
MM v[28:31],a[8:11],a[216:219],v[28:31]
s_add_u32 s87, s87, 65536
s_add_u32 m0, s96, 133384
v_smfmac_f32_16x16x64_bf16 v[184:187], v[236:239], a[0:7], v234
v_smfmac_f32_16x16x64_bf16 v[188:191], v[236:239], a[8:15], v234
MM v[32:35],a[16:19],a[192:195],v[32:35]
TR a[0:1],v192 offset:32896
TR a[2:3],v192 offset:33152
MM v[32:35],a[20:23],a[196:199],v[32:35]
TR a[4:5],v192 offset:41120
TR a[6:7],v192 offset:41376
MM v[40:43],a[20:23],a[204:207],v[40:43]
TR a[8:9],v192 offset:32904
TR a[10:11],v192 offset:33160
buffer_load_dwordx4 v253, s[16:19], s87 offen offset:0 lds
MM v[40:43],a[16:19],a[200:203],v[40:43]
)ASM"
R"ASM(
s_add_u32 m0, s96, 134408
TR a[12:13],v192 offset:41128
TR a[14:15],v192 offset:41384
MM v[48:51],a[16:19],a[208:211],v[48:51]
buffer_load_dwordx4 v254, s[16:19], s87 offen offset:0 lds
MM v[48:51],a[20:23],a[212:215],v[48:51]
s_waitcnt vmcnt(16)
RD a[224:227],v222 offset:32768
MM v[56:59],a[20:23],a[220:223],v[56:59]
RD a[228:231],v223 offset:32768
MM v[56:59],a[16:19],a[216:219],v[56:59]
RD a[232:235],v217 offset:32768
MM v[36:39],a[24:27],a[192:195],v[36:39]
TR a[16:17],v193 offset:32896
TR a[18:19],v193 offset:33152
buffer_load_dwordx4 v[200:203], v208, s[20:23], s93 offen offset:0 sc1
MM v[36:39],a[28:31],a[196:199],v[36:39]
TR a[20:21],v193 offset:41120
TR a[22:23],v193 offset:41376
MM v[44:47],a[28:31],a[204:207],v[44:47]
RD a[236:239],v218 offset:32768
MM v[44:47],a[24:27],a[200:203],v[44:47]
RD a[240:243],v222 offset:36864
MM v[52:55],a[24:27],a[208:211],v[52:55]
buffer_load_dwordx4 v[204:207], v208, s[20:23], s93 offen offset:256 sc1
MM v[52:55],a[28:31],a[212:215],v[52:55]
RD a[244:247],v223 offset:36864
MM v[60:63],a[28:31],a[220:223],v[60:63]
RD a[248:251],v217 offset:36864
MM v[60:63],a[24:27],a[216:219],v[60:63]
s_waitcnt lgkmcnt(15)
RD a[252:255],v218 offset:36864
MM v[64:67],a[32:35],a[192:195],v[64:67]
TR a[24:25],v193 offset:32904
TR a[26:27],v193 offset:33160
MM v[64:67],a[36:39],a[196:199],v[64:67]
TR a[28:29],v193 offset:41128
TR a[30:31],v193 offset:41384
MM v[72:75],a[36:39],a[204:207],v[72:75]
MM v[72:75],a[32:35],a[200:203],v[72:75]
s_waitcnt lgkmcnt(4)
s_add_u32 s88, s68, s86
s_add_u32 s86, s86, 128
MM v[80:83],a[32:35],a[208:211],v[80:83]
s_cmp_ge_u32 s86, 1280
s_cselect_b32 s86, 0, s86
s_add_u32 m0, s94, 32768
MM v[80:83],a[36:39],a[212:215],v[80:83]
s_add_u32 s95, s88, 0
buffer_load_dwordx4 v220, s[32:35], s95 offen offset:0 lds
MM v[88:91],a[36:39],a[220:223],v[88:91]
s_add_u32 s95, s88, 9216
MM v[88:91],a[32:35],a[216:219],v[88:91]
buffer_load_dwordx4 v221, s[32:35], s95 offen offset:1024 lds
MM v[68:71],a[40:43],a[192:195],v[68:71]
s_add_u32 s95, s88, 18432
TR a[32:33],v194 offset:32896
TR a[34:35],v194 offset:33152
MM v[68:71],a[44:47],a[196:199],v[68:71]
TR a[36:37],v194 offset:41120
TR a[38:39],v194 offset:41376
MM v[76:79],a[44:47],a[204:207],v[76:79]
MM v[76:79],a[40:43],a[200:203],v[76:79]
buffer_load_dwordx4 v220, s[32:35], s95 offen offset:2048 lds
MM v[84:87],a[40:43],a[208:211],v[84:87]
s_add_u32 s95, s88, 27648
MM v[84:87],a[44:47],a[212:215],v[84:87]
MM v[92:95],a[44:47],a[220:223],v[92:95]
MM v[92:95],a[40:43],a[216:219],v[92:95]
buffer_load_dwordx4 v221, s[32:35], s95 offen offset:3072 lds
MM v[96:99],a[48:51],a[192:195],v[96:99]
s_waitcnt vmcnt(10)
TR a[40:41],v194 offset:32904
TR a[42:43],v194 offset:33160
MM v[96:99],a[52:55],a[196:199],v[96:99]
TR a[44:45],v194 offset:41128
TR a[46:47],v194 offset:41384
MM v[104:107],a[52:55],a[204:207],v[104:107]
RD a[128:131],v222 offset:0
MM v[104:107],a[48:51],a[200:203],v[104:107]
RD a[132:135],v223 offset:0
MM v[112:115],a[48:51],a[208:211],v[112:115]
RD a[136:139],v217 offset:0
MM v[112:115],a[52:55],a[212:215],v[112:115]
RD a[140:143],v218 offset:0
MM v[120:123],a[52:55],a[220:223],v[120:123]
RD a[144:147],v222 offset:4096
MM v[120:123],a[48:51],a[216:219],v[120:123]
RD a[148:151],v223 offset:4096
MM v[100:103],a[56:59],a[192:195],v[100:103]
TR a[48:49],v195 offset:32896
TR a[50:51],v195 offset:33152
MM v[100:103],a[60:63],a[196:199],v[100:103]
TR a[52:53],v195 offset:41120
TR a[54:55],v195 offset:41376
MM v[108:111],a[60:63],a[204:207],v[108:111]
RD a[152:155],v217 offset:4096
MM v[108:111],a[56:59],a[200:203],v[108:111]
RD a[156:159],v218 offset:4096
MM v[116:119],a[56:59],a[208:211],v[116:119]
s_add_u32 m0, s94, 36864
s_add_u32 s95, s88, 40960
MM v[116:119],a[60:63],a[212:215],v[116:119]
buffer_load_dwordx4 v220, s[32:35], s95 offen offset:0 lds
MM v[124:127],a[60:63],a[220:223],v[124:127]
s_add_u32 s95, s88, 50176
MM v[124:127],a[56:59],a[216:219],v[124:127]
buffer_load_dwordx4 v221, s[32:35], s95 offen offset:1024 lds
s_add_u32 s95, s88, 59392
buffer_load_dwordx4 v220, s[32:35], s95 offen offset:2048 lds
s_add_u32 s95, s88, 68608
buffer_load_dwordx4 v221, s[32:35], s95 offen offset:3072 lds
s_waitcnt lgkmcnt(0)
s_waitcnt vmcnt(10)
s_barrier
MM v[0:3],a[0:3],a[224:227],v[0:3]
s_add_u32 s77, s64, 0
TR a[56:57],v195 offset:32904
TR a[58:59],v195 offset:33160
MM v[0:3],a[4:7],a[228:231],v[0:3]
s_add_u32 m0, s65, 0
TR a[60:61],v195 offset:41128
TR a[62:63],v195 offset:41384
MM v[8:11],a[4:7],a[236:239],v[8:11]
s_waitcnt vmcnt(8)
TR a[64:65],v196 offset:0
TR a[66:67],v196 offset:256
MM v[8:11],a[0:3],a[232:235],v[8:11]
s_lshl_b32 s76, %[swave], 7
v_lshrrev_b32 v253, 8, v248
v_lshlrev_b32 v253, 5, v253
MM v[16:19],a[0:3],a[240:243],v[16:19]
s_add_u32 s76, s76, %[slds]
TR a[68:69],v196 offset:8224
TR a[70:71],v196 offset:8480
v_xor_b32 v201, s69, v201
MM v[16:19],a[4:7],a[244:247],v[16:19]
buffer_load_dwordx4 v228, s[8:11], s77 offen offset:0 lds
s_mov_b64 exec, 1
buffer_store_dwordx2 v[170:171], off, s[20:23], s80 sc1
s_mov_b64 exec, -1
v_add_u32 v253, s76, v253
v_add_u32 v253, 151552, v253
MM v[24:27],a[4:7],a[252:255],v[24:27]
TR a[72:73],v196 offset:16448
TR a[74:75],v196 offset:16704
MM v[24:27],a[0:3],a[248:251],v[24:27]
TR a[76:77],v196 offset:24672
TR a[78:79],v196 offset:24928
v_xor_b32 v203, s69, v203
MM v[4:7],a[8:11],a[224:227],v[4:7]
TR a[80:81],v196 offset:8
TR a[82:83],v196 offset:264
MM v[4:7],a[12:15],a[228:231],v[4:7]
TR a[84:85],v196 offset:8232
TR a[86:87],v196 offset:8488
buffer_load_dwordx4 v229, s[8:11], s77 offen offset:1024 lds
MM v[12:15],a[12:15],a[236:239],v[12:15]
TR a[88:89],v196 offset:16456
TR a[90:91],v196 offset:16712
v_xor_b32 v205, s69, v205
MM v[12:15],a[8:11],a[232:235],v[12:15]
TR a[92:93],v196 offset:24680
TR a[94:95],v196 offset:24936
MM v[20:23],a[8:11],a[240:243],v[20:23]
TR a[96:97],v197 offset:0
TR a[98:99],v197 offset:256
MM v[20:23],a[12:15],a[244:247],v[20:23]
TR a[100:101],v197 offset:8224
TR a[102:103],v197 offset:8480
buffer_load_dwordx4 v230, s[8:11], s77 offen offset:2048 lds
v_xor_b32 v207, s69, v207
MM v[28:31],a[12:15],a[252:255],v[28:31]
TR a[104:105],v197 offset:16448
TR a[106:107],v197 offset:16704
MM v[28:31],a[8:11],a[248:251],v[28:31]
TR a[108:109],v197 offset:24672
TR a[110:111],v197 offset:24928
v_smfmac_f32_16x16x64_bf16 v[184:187], v[236:239], a[0:7], v234
TR a[112:113],v197 offset:8
v_or3_b32 v209, v201, v203, v205
v_smfmac_f32_16x16x64_bf16 v[188:191], v[236:239], a[8:15], v234
TR a[114:115],v197 offset:264
buffer_load_dwordx4 v231, s[8:11], s77 offen offset:3072 lds
MM v[32:35],a[16:19],a[224:227],v[32:35]
s_add_u32 s77, s64, 16384
TR a[116:117],v197 offset:8232
TR a[118:119],v197 offset:8488
MM v[32:35],a[20:23],a[228:231],v[32:35]
s_add_u32 m0, s65, 16384
TR a[120:121],v197 offset:16456
TR a[122:123],v197 offset:16712
v_or_b32 v209, v209, v207
MM v[40:43],a[20:23],a[236:239],v[40:43]
TR a[124:125],v197 offset:24680
TR a[126:127],v197 offset:24936
MM v[40:43],a[16:19],a[232:235],v[40:43]
TR a[0:1],v198 offset:0
TR a[2:3],v198 offset:256
MM v[48:51],a[16:19],a[240:243],v[48:51]
TR a[4:5],v198 offset:8224
TR a[6:7],v198 offset:8480
v_cmp_ne_u32 vcc, 0, v209
s_cbranch_vccnz LRG1o_%=
LRG1ob_%=:
MM v[48:51],a[20:23],a[244:247],v[48:51]
TR a[8:9],v198 offset:16448
TR a[10:11],v198 offset:16704
buffer_load_dwordx4 v228, s[8:11], s77 offen offset:0 lds
MM v[56:59],a[20:23],a[252:255],v[56:59]
TR a[12:13],v198 offset:24672
TR a[14:15],v198 offset:24928
MM v[56:59],a[16:19],a[248:251],v[56:59]
s_bitcmp1_b32 %[sb], 0
s_cselect_b64 exec, -1, 0
v_swap_b32 v200, v202
v_swap_b32 v204, v206
s_mov_b64 exec, -1
MM v[36:39],a[24:27],a[224:227],v[36:39]
v_lshlrev_b32 v240, 16, v200
v_and_b32 v200, 0xffff0000, v200
MM v[36:39],a[28:31],a[228:231],v[36:39]
v_rcp_f32 v246, v200
MM v[44:47],a[28:31],a[236:239],v[44:47]
v_xor_b32 v240, 0x80000000, v240
v_mul_f32 v246, s72, v246
MM v[44:47],a[24:27],a[232:235],v[44:47]
v_lshlrev_b32 v241, 16, v202
v_and_b32 v202, 0xffff0000, v202
MM v[52:55],a[24:27],a[240:243],v[52:55]
v_rcp_f32 v247, v202
MM v[52:55],a[28:31],a[244:247],v[52:55]
buffer_load_dwordx4 v229, s[8:11], s77 offen offset:1024 lds
MM v[60:63],a[28:31],a[252:255],v[60:63]
v_xor_b32 v241, 0x80000000, v241
v_mul_f32 v247, s72, v247
MM v[60:63],a[24:27],a[248:251],v[60:63]
v_lshlrev_b32 v242, 16, v204
v_and_b32 v204, 0xffff0000, v204
MM v[64:67],a[32:35],a[224:227],v[64:67]
TR a[16:17],v198 offset:8
TR a[18:19],v198 offset:264
MM v[64:67],a[36:39],a[228:231],v[64:67]
TR a[20:21],v198 offset:8232
TR a[22:23],v198 offset:8488
v_rcp_f32 v244, v204
MM v[72:75],a[36:39],a[236:239],v[72:75]
TR a[24:25],v198 offset:16456
TR a[26:27],v198 offset:16712
MM v[72:75],a[32:35],a[232:235],v[72:75]
TR a[28:29],v198 offset:24680
TR a[30:31],v198 offset:24936
MM v[80:83],a[32:35],a[240:243],v[80:83]
v_xor_b32 v242, 0x80000000, v242
v_mul_f32 v244, s72, v244
MM v[80:83],a[36:39],a[244:247],v[80:83]
v_lshlrev_b32 v243, 16, v206
v_and_b32 v206, 0xffff0000, v206
MM v[88:91],a[36:39],a[252:255],v[88:91]
v_rcp_f32 v245, v206
MM v[88:91],a[32:35],a[248:251],v[88:91]
s_waitcnt lgkmcnt(15)
buffer_load_dwordx4 v230, s[8:11], s77 offen offset:2048 lds
v_smfmac_f32_16x16x64_bf16 v[184:187], v[236:239], a[64:71], v234
v_xor_b32 v243, 0x80000000, v243
v_smfmac_f32_16x16x64_bf16 v[184:187], v[236:239], a[72:79], v234
v_mul_f32 v245, s72, v245
v_smfmac_f32_16x16x64_bf16 v[188:191], v[236:239], a[80:87], v234
v_smfmac_f32_16x16x64_bf16 v[188:191], v[236:239], a[88:95], v234
MM v[68:71],a[40:43],a[224:227],v[68:71]
v_bfe_u32 v183, v248, 6, 2
v_lshlrev_b32 v183, 5, v183
MM v[68:71],a[44:47],a[228:231],v[68:71]
v_bfe_u32 v182, v248, 4, 2
v_lshl_add_u32 v183, v182, 2, v183
MM v[76:79],a[44:47],a[236:239],v[76:79]
v_add_u32 v183, s76, v183
v_add_u32 v183, 151552, v183
MM v[76:79],a[40:43],a[232:235],v[76:79]
MM v[84:87],a[40:43],a[240:243],v[84:87]
MM v[84:87],a[44:47],a[244:247],v[84:87]
buffer_load_dwordx4 v231, s[8:11], s77 offen offset:3072 lds
MM v[92:95],a[44:47],a[252:255],v[92:95]
s_waitcnt vmcnt(9)
RD a[160:163],v222 offset:32768
MM v[92:95],a[40:43],a[248:251],v[92:95]
v_add_f32 v184, v184, v185
v_add_f32 v188, v188, v189
MM v[96:99],a[48:51],a[224:227],v[96:99]
TR a[32:33],v199 offset:0
TR a[34:35],v199 offset:256
MM v[96:99],a[52:55],a[228:231],v[96:99]
RD a[164:167],v223 offset:32768
ds_write_b32 v183, v184 offset:0
ds_write_b32 v183, v188 offset:16
MM v[104:107],a[52:55],a[236:239],v[104:107]
TR a[36:37],v199 offset:8224
TR a[38:39],v199 offset:8480
MM v[104:107],a[48:51],a[232:235],v[104:107]
TR a[40:41],v199 offset:16448
TR a[42:43],v199 offset:16704
MM v[112:115],a[48:51],a[240:243],v[112:115]
TR a[44:45],v199 offset:24672
TR a[46:47],v199 offset:24928
RD a[168:171],v217 offset:32768
MM v[112:115],a[52:55],a[244:247],v[112:115]
RD a[172:175],v218 offset:32768
MM v[120:123],a[52:55],a[252:255],v[120:123]
RD a[176:179],v222 offset:36864
MM v[120:123],a[48:51],a[248:251],v[120:123]
RD a[180:183],v223 offset:36864
MM v[100:103],a[56:59],a[224:227],v[100:103]
RD a[184:187],v217 offset:36864
MM v[100:103],a[60:63],a[228:231],v[100:103]
RD a[188:191],v218 offset:36864
MM v[108:111],a[60:63],a[236:239],v[108:111]
s_add_u32 s77, s64, 0
s_add_u32 s81, s77, 896
s_add_u32 m0, s65, 65536
MM v[108:111],a[56:59],a[232:235],v[108:111]
buffer_load_dwordx4 v235, s[12:15], s81 offen offset:0 lds
MM v[116:119],a[56:59],a[240:243],v[116:119]
MM v[116:119],a[60:63],a[244:247],v[116:119]
MM v[124:127],a[60:63],a[252:255],v[124:127]
MM v[124:127],a[56:59],a[248:251],v[124:127]
s_waitcnt lgkmcnt(0)
s_barrier
MM v[0:3],a[64:67],a[128:131],v[0:3]
s_mov_b32 s56, 0x000f000f
TR a[48:49],v199 offset:8
TR a[50:51],v199 offset:264
MM v[0:3],a[68:71],a[132:135],v[0:3]
s_mov_b32 s57, s56
TR a[52:53],v199 offset:8232
TR a[54:55],v199 offset:8488
MM v[0:3],a[72:75],a[160:163],v[0:3]
s_lshl_b64 s[58:59], s[56:57], 4
TR a[56:57],v199 offset:16456
TR a[58:59],v199 offset:16712
MM v[0:3],a[76:79],a[164:167],v[0:3]
TR a[60:61],v199 offset:24680
TR a[62:63],v199 offset:24936
buffer_load_dwordx4 v235, s[12:15], s77 offen offset:1024 lds
)ASM"
R"ASM(
MM v[8:11],a[76:79],a[172:175],v[8:11]
v_xor_b32 v151, 0, v253
v_xor_b32 v143, 0, v253
MM v[8:11],a[72:75],a[168:171],v[8:11]
RD v[144:147],v151 offset:0
RD v[148:151],v151 offset:16
MM v[8:11],a[68:71],a[140:143],v[8:11]
RD v[136:139],v143 offset:0
RD v[140:143],v143 offset:16
MM v[8:11],a[64:67],a[136:139],v[8:11]
MM v[4:7],a[80:83],a[128:131],v[4:7]
MM v[4:7],a[84:87],a[132:135],v[4:7]
s_waitcnt lgkmcnt(2)
MM v[4:7],a[88:91],a[160:163],v[4:7]
s_waitcnt lgkmcnt(0)
MM v[4:7],a[92:95],a[164:167],v[4:7]
buffer_load_dwordx4 v235, s[12:15], s81 offen offset:2048 lds
MM v[12:15],a[92:95],a[172:175],v[12:15]
MM v[12:15],a[88:91],a[168:171],v[12:15]
MM v[12:15],a[84:87],a[140:143],v[12:15]
MM v[12:15],a[80:83],a[136:139],v[12:15]
MM v[32:35],a[96:99],a[128:131],v[32:35]
MM v[32:35],a[100:103],a[132:135],v[32:35]
MM v[32:35],a[104:107],a[160:163],v[32:35]
MM v[32:35],a[108:111],a[164:167],v[32:35]
v_pk_fma_f32 v[0:1], v[240:241], v[144:145], v[0:1] op_sel:[0,0,0] op_sel_hi:[0,1,1]
v_pk_fma_f32 v[2:3], v[240:241], v[146:147], v[2:3] op_sel:[0,0,0] op_sel_hi:[0,1,1]
v_pk_fma_f32 v[4:5], v[240:241], v[148:149], v[4:5] op_sel:[0,0,0] op_sel_hi:[0,1,1]
v_pk_fma_f32 v[6:7], v[240:241], v[150:151], v[6:7] op_sel:[0,0,0] op_sel_hi:[0,1,1]
v_pk_mul_f32 v[0:1], v[246:247], v[0:1] op_sel:[0,0] op_sel_hi:[0,1]
v_pk_mul_f32 v[2:3], v[246:247], v[2:3] op_sel:[0,0] op_sel_hi:[0,1]
v_pk_mul_f32 v[4:5], v[246:247], v[4:5] op_sel:[0,0] op_sel_hi:[0,1]
v_pk_mul_f32 v[6:7], v[246:247], v[6:7] op_sel:[0,0] op_sel_hi:[0,1]
MM v[40:43],a[108:111],a[172:175],v[40:43]
CV v160,v0,v1
CV v161,v2,v3
MM v[40:43],a[104:107],a[168:171],v[40:43]
buffer_load_dwordx4 v235, s[12:15], s77 offen offset:3072 lds
MM v[40:43],a[100:103],a[140:143],v[40:43]
s_add_u32 s77, s64, 32768
s_add_u32 m0, s65, 32768
CV v162,v4,v5
MM v[40:43],a[96:99],a[136:139],v[40:43]
CV v163,v6,v7
MM v[36:39],a[112:115],a[128:131],v[36:39]
v_pk_fma_f32 v[8:9], v[240:241], v[144:145], v[8:9] op_sel:[1,0,0] op_sel_hi:[1,1,1]
v_pk_fma_f32 v[10:11], v[240:241], v[146:147], v[10:11] op_sel:[1,0,0] op_sel_hi:[1,1,1]
v_pk_fma_f32 v[12:13], v[240:241], v[148:149], v[12:13] op_sel:[1,0,0] op_sel_hi:[1,1,1]
v_pk_fma_f32 v[14:15], v[240:241], v[150:151], v[14:15] op_sel:[1,0,0] op_sel_hi:[1,1,1]
v_pk_mul_f32 v[8:9], v[246:247], v[8:9] op_sel:[1,0] op_sel_hi:[1,1]
v_pk_mul_f32 v[10:11], v[246:247], v[10:11] op_sel:[1,0] op_sel_hi:[1,1]
v_pk_mul_f32 v[12:13], v[246:247], v[12:13] op_sel:[1,0] op_sel_hi:[1,1]
v_pk_mul_f32 v[14:15], v[246:247], v[14:15] op_sel:[1,0] op_sel_hi:[1,1]
MM v[36:39],a[116:119],a[132:135],v[36:39]
CV v164,v8,v9
CV v165,v10,v11
MM v[36:39],a[120:123],a[160:163],v[36:39]
CV v166,v12,v13
CV v167,v14,v15
MM v[36:39],a[124:127],a[164:167],v[36:39]
v_xor_b32 v159, 128, v253
MM v[44:47],a[124:127],a[172:175],v[44:47]
RD v[152:155],v159 offset:0
RD v[156:159],v159 offset:16
MM v[44:47],a[120:123],a[168:171],v[44:47]
MM v[44:47],a[116:119],a[140:143],v[44:47]
MM v[44:47],a[112:115],a[136:139],v[44:47]
buffer_load_dwordx4 v228, s[8:11], s77 offen offset:0 lds
MM v[64:67],a[0:3],a[128:131],v[64:67]
s_waitcnt lgkmcnt(0)
MM v[64:67],a[4:7],a[132:135],v[64:67]
MM v[64:67],a[8:11],a[160:163],v[64:67]
MM v[64:67],a[12:15],a[164:167],v[64:67]
v_pk_fma_f32 v[32:33], v[240:241], v[152:153], v[32:33] op_sel:[0,0,0] op_sel_hi:[0,1,1]
v_pk_fma_f32 v[34:35], v[240:241], v[154:155], v[34:35] op_sel:[0,0,0] op_sel_hi:[0,1,1]
v_pk_fma_f32 v[36:37], v[240:241], v[156:157], v[36:37] op_sel:[0,0,0] op_sel_hi:[0,1,1]
v_pk_fma_f32 v[38:39], v[240:241], v[158:159], v[38:39] op_sel:[0,0,0] op_sel_hi:[0,1,1]
v_pk_mul_f32 v[32:33], v[246:247], v[32:33] op_sel:[0,0] op_sel_hi:[0,1]
v_pk_mul_f32 v[34:35], v[246:247], v[34:35] op_sel:[0,0] op_sel_hi:[0,1]
v_pk_mul_f32 v[36:37], v[246:247], v[36:37] op_sel:[0,0] op_sel_hi:[0,1]
v_pk_mul_f32 v[38:39], v[246:247], v[38:39] op_sel:[0,0] op_sel_hi:[0,1]
MM v[72:75],a[12:15],a[172:175],v[72:75]
CV v176,v32,v33
CV v177,v34,v35
MM v[72:75],a[8:11],a[168:171],v[72:75]
CV v178,v36,v37
CV v179,v38,v39
MM v[72:75],a[4:7],a[140:143],v[72:75]
v_pk_fma_f32 v[40:41], v[240:241], v[152:153], v[40:41] op_sel:[1,0,0] op_sel_hi:[1,1,1]
v_pk_fma_f32 v[42:43], v[240:241], v[154:155], v[42:43] op_sel:[1,0,0] op_sel_hi:[1,1,1]
v_pk_fma_f32 v[44:45], v[240:241], v[156:157], v[44:45] op_sel:[1,0,0] op_sel_hi:[1,1,1]
v_pk_fma_f32 v[46:47], v[240:241], v[158:159], v[46:47] op_sel:[1,0,0] op_sel_hi:[1,1,1]
v_pk_mul_f32 v[40:41], v[246:247], v[40:41] op_sel:[1,0] op_sel_hi:[1,1]
v_pk_mul_f32 v[42:43], v[246:247], v[42:43] op_sel:[1,0] op_sel_hi:[1,1]
v_pk_mul_f32 v[44:45], v[246:247], v[44:45] op_sel:[1,0] op_sel_hi:[1,1]
v_pk_mul_f32 v[46:47], v[246:247], v[46:47] op_sel:[1,0] op_sel_hi:[1,1]
MM v[72:75],a[0:3],a[136:139],v[72:75]
CV v180,v40,v41
CV v181,v42,v43
MM v[68:71],a[16:19],a[128:131],v[68:71]
CV v182,v44,v45
CV v183,v46,v47
MM v[68:71],a[20:23],a[132:135],v[68:71]
buffer_load_dwordx4 v229, s[8:11], s77 offen offset:1024 lds
MM v[68:71],a[24:27],a[160:163],v[68:71]
v_xor_b32 v151, 256, v253
MM v[68:71],a[28:31],a[164:167],v[68:71]
RD v[144:147],v151 offset:0
RD v[148:151],v151 offset:16
MM v[76:79],a[28:31],a[172:175],v[76:79]
MM v[76:79],a[24:27],a[168:171],v[76:79]
MM v[76:79],a[20:23],a[140:143],v[76:79]
MM v[76:79],a[16:19],a[136:139],v[76:79]
s_waitcnt lgkmcnt(0)
MM v[96:99],a[32:35],a[128:131],v[96:99]
MM v[96:99],a[36:39],a[132:135],v[96:99]
MM v[96:99],a[40:43],a[160:163],v[96:99]
MM v[96:99],a[44:47],a[164:167],v[96:99]
buffer_load_dwordx4 v230, s[8:11], s77 offen offset:2048 lds
MM v[104:107],a[44:47],a[172:175],v[104:107]
v_pk_fma_f32 v[64:65], v[240:241], v[144:145], v[64:65] op_sel:[0,0,0] op_sel_hi:[0,1,1]
v_pk_fma_f32 v[66:67], v[240:241], v[146:147], v[66:67] op_sel:[0,0,0] op_sel_hi:[0,1,1]
v_pk_fma_f32 v[68:69], v[240:241], v[148:149], v[68:69] op_sel:[0,0,0] op_sel_hi:[0,1,1]
v_pk_fma_f32 v[70:71], v[240:241], v[150:151], v[70:71] op_sel:[0,0,0] op_sel_hi:[0,1,1]
v_pk_mul_f32 v[64:65], v[246:247], v[64:65] op_sel:[0,0] op_sel_hi:[0,1]
v_pk_mul_f32 v[66:67], v[246:247], v[66:67] op_sel:[0,0] op_sel_hi:[0,1]
v_pk_mul_f32 v[68:69], v[246:247], v[68:69] op_sel:[0,0] op_sel_hi:[0,1]
v_pk_mul_f32 v[70:71], v[246:247], v[70:71] op_sel:[0,0] op_sel_hi:[0,1]
MM v[104:107],a[40:43],a[168:171],v[104:107]
CV v192,v64,v65
CV v193,v66,v67
MM v[104:107],a[36:39],a[140:143],v[104:107]
CV v194,v68,v69
CV v195,v70,v71
MM v[104:107],a[32:35],a[136:139],v[104:107]
v_pk_fma_f32 v[72:73], v[240:241], v[144:145], v[72:73] op_sel:[1,0,0] op_sel_hi:[1,1,1]
v_pk_fma_f32 v[74:75], v[240:241], v[146:147], v[74:75] op_sel:[1,0,0] op_sel_hi:[1,1,1]
v_pk_fma_f32 v[76:77], v[240:241], v[148:149], v[76:77] op_sel:[1,0,0] op_sel_hi:[1,1,1]
v_pk_fma_f32 v[78:79], v[240:241], v[150:151], v[78:79] op_sel:[1,0,0] op_sel_hi:[1,1,1]
v_pk_mul_f32 v[72:73], v[246:247], v[72:73] op_sel:[1,0] op_sel_hi:[1,1]
v_pk_mul_f32 v[74:75], v[246:247], v[74:75] op_sel:[1,0] op_sel_hi:[1,1]
v_pk_mul_f32 v[76:77], v[246:247], v[76:77] op_sel:[1,0] op_sel_hi:[1,1]
v_pk_mul_f32 v[78:79], v[246:247], v[78:79] op_sel:[1,0] op_sel_hi:[1,1]
MM v[100:103],a[48:51],a[128:131],v[100:103]
CV v196,v72,v73
CV v197,v74,v75
MM v[100:103],a[52:55],a[132:135],v[100:103]
CV v198,v76,v77
CV v199,v78,v79
MM v[100:103],a[56:59],a[160:163],v[100:103]
v_xor_b32 v159, 384, v253
MM v[100:103],a[60:63],a[164:167],v[100:103]
RD v[152:155],v159 offset:0
RD v[156:159],v159 offset:16
MM v[108:111],a[60:63],a[172:175],v[108:111]
MM v[108:111],a[56:59],a[168:171],v[108:111]
buffer_load_dwordx4 v231, s[8:11], s77 offen offset:3072 lds
MM v[108:111],a[52:55],a[140:143],v[108:111]
s_add_u32 s77, s64, 16384
s_add_u32 s81, s77, 896
s_add_u32 m0, s65, 81920
MM v[108:111],a[48:51],a[136:139],v[108:111]
s_waitcnt lgkmcnt(0)
MM v[16:19],a[64:67],a[144:147],v[16:19]
MM v[16:19],a[68:71],a[148:151],v[16:19]
MM v[16:19],a[72:75],a[176:179],v[16:19]
MM v[16:19],a[76:79],a[180:183],v[16:19]
v_pk_fma_f32 v[96:97], v[240:241], v[152:153], v[96:97] op_sel:[0,0,0] op_sel_hi:[0,1,1]
v_pk_fma_f32 v[98:99], v[240:241], v[154:155], v[98:99] op_sel:[0,0,0] op_sel_hi:[0,1,1]
v_pk_fma_f32 v[100:101], v[240:241], v[156:157], v[100:101] op_sel:[0,0,0] op_sel_hi:[0,1,1]
v_pk_fma_f32 v[102:103], v[240:241], v[158:159], v[102:103] op_sel:[0,0,0] op_sel_hi:[0,1,1]
v_pk_mul_f32 v[96:97], v[246:247], v[96:97] op_sel:[0,0] op_sel_hi:[0,1]
v_pk_mul_f32 v[98:99], v[246:247], v[98:99] op_sel:[0,0] op_sel_hi:[0,1]
v_pk_mul_f32 v[100:101], v[246:247], v[100:101] op_sel:[0,0] op_sel_hi:[0,1]
s_nop 0
LPG4_%=:
s_cbranch_execz LPG8_%=
v_pk_mul_f32 v[102:103], v[246:247], v[102:103] op_sel:[0,0] op_sel_hi:[0,1]
MM v[24:27],a[76:79],a[188:191],v[24:27]
CV v208,v96,v97
CV v209,v98,v99
MM v[24:27],a[72:75],a[184:187],v[24:27]
CV v210,v100,v101
CV v211,v102,v103
MM v[24:27],a[68:71],a[156:159],v[24:27]
s_waitcnt vmcnt(12)
s_barrier
RD a[192:195],v224 offset:0
MM v[24:27],a[64:67],a[152:155],v[24:27]
buffer_load_dwordx4 v235, s[12:15], s81 offen offset:0 lds
MM v[20:23],a[80:83],a[144:147],v[20:23]
RD a[196:199],v225 offset:0
RD a[200:203],v226 offset:0
MM v[20:23],a[84:87],a[148:151],v[20:23]
RD a[204:207],v227 offset:0
RD a[208:211],v224 offset:4096
MM v[20:23],a[88:91],a[176:179],v[20:23]
RD a[212:215],v225 offset:4096
RD a[216:219],v226 offset:4096
MM v[20:23],a[92:95],a[180:183],v[20:23]
RD a[220:223],v227 offset:4096
RD a[224:227],v224 offset:8192
MM v[28:31],a[92:95],a[188:191],v[28:31]
RD a[228:231],v225 offset:8192
RD a[232:235],v226 offset:8192
MM v[28:31],a[88:91],a[184:187],v[28:31]
RD a[236:239],v227 offset:8192
RD a[240:243],v224 offset:12288
MM v[28:31],a[84:87],a[156:159],v[28:31]
RD a[244:247],v225 offset:12288
RD a[248:251],v226 offset:12288
MM v[28:31],a[80:83],a[152:155],v[28:31]
RD a[252:255],v227 offset:12288
MM v[48:51],a[96:99],a[144:147],v[48:51]
v_pk_fma_f32 v[104:105], v[240:241], v[152:153], v[104:105] op_sel:[1,0,0] op_sel_hi:[1,1,1]
v_pk_fma_f32 v[106:107], v[240:241], v[154:155], v[106:107] op_sel:[1,0,0] op_sel_hi:[1,1,1]
v_pk_fma_f32 v[108:109], v[240:241], v[156:157], v[108:109] op_sel:[1,0,0] op_sel_hi:[1,1,1]
v_pk_fma_f32 v[110:111], v[240:241], v[158:159], v[110:111] op_sel:[1,0,0] op_sel_hi:[1,1,1]
v_pk_mul_f32 v[104:105], v[246:247], v[104:105] op_sel:[1,0] op_sel_hi:[1,1]
v_pk_mul_f32 v[106:107], v[246:247], v[106:107] op_sel:[1,0] op_sel_hi:[1,1]
v_pk_mul_f32 v[108:109], v[246:247], v[108:109] op_sel:[1,0] op_sel_hi:[1,1]
v_pk_mul_f32 v[110:111], v[246:247], v[110:111] op_sel:[1,0] op_sel_hi:[1,1]
MM v[48:51],a[100:103],a[148:151],v[48:51]
buffer_load_dwordx4 v235, s[12:15], s77 offen offset:1024 lds
MM v[48:51],a[104:107],a[176:179],v[48:51]
CV v212,v104,v105
CV v213,v106,v107
MM v[48:51],a[108:111],a[180:183],v[48:51]
CV v214,v108,v109
CV v215,v110,v111
MM v[56:59],a[108:111],a[188:191],v[56:59]
v_mov_b32 v247, 0
v_mov_b32 v236, 0
MM v[56:59],a[104:107],a[184:187],v[56:59]
v_mov_b32 v237, 0
v_cndmask_b32 v144, 0, v238, s[56:57]
MM v[56:59],a[100:103],a[156:159],v[56:59]
v_cndmask_b32 v145, 0, v238, s[56:57]
v_cndmask_b32 v146, 0, v238, s[56:57]
MM v[56:59],a[96:99],a[152:155],v[56:59]
v_cndmask_b32 v147, 0, v238, s[56:57]
v_cndmask_b32 v148, 0, v238, s[58:59]
MM v[52:55],a[112:115],a[144:147],v[52:55]
v_cndmask_b32 v149, 0, v238, s[58:59]
v_cndmask_b32 v150, 0, v238, s[58:59]
MM v[52:55],a[116:119],a[148:151],v[52:55]
v_pk_fma_f32 v[16:17], v[242:243], v[136:137], v[16:17] op_sel:[0,0,0] op_sel_hi:[0,1,1]
v_pk_fma_f32 v[18:19], v[242:243], v[138:139], v[18:19] op_sel:[0,0,0] op_sel_hi:[0,1,1]
v_pk_fma_f32 v[20:21], v[242:243], v[140:141], v[20:21] op_sel:[0,0,0] op_sel_hi:[0,1,1]
v_pk_fma_f32 v[22:23], v[242:243], v[142:143], v[22:23] op_sel:[0,0,0] op_sel_hi:[0,1,1]
v_pk_mul_f32 v[16:17], v[244:245], v[16:17] op_sel:[0,0] op_sel_hi:[0,1]
v_pk_mul_f32 v[18:19], v[244:245], v[18:19] op_sel:[0,0] op_sel_hi:[0,1]
v_pk_mul_f32 v[20:21], v[244:245], v[20:21] op_sel:[0,0] op_sel_hi:[0,1]
v_pk_mul_f32 v[22:23], v[244:245], v[22:23] op_sel:[0,0] op_sel_hi:[0,1]
MM v[52:55],a[120:123],a[176:179],v[52:55]
v_cndmask_b32 v151, 0, v238, s[58:59]
)ASM"
R"ASM(
CV v168,v16,v17
MM v[52:55],a[124:127],a[180:183],v[52:55]
buffer_load_dwordx4 v235, s[12:15], s81 offen offset:2048 lds
s_waitcnt lgkmcnt(0)
MM v[0:3],a[192:195],v[160:163],0
CV v169,v18,v19
CV v170,v20,v21
MM v[0:3],a[196:199],v[176:179],v[0:3]
CV v171,v22,v23
MM v[0:3],a[200:203],v[192:195],v[0:3]
v_pk_fma_f32 v[24:25], v[242:243], v[136:137], v[24:25] op_sel:[1,0,0] op_sel_hi:[1,1,1]
v_pk_fma_f32 v[26:27], v[242:243], v[138:139], v[26:27] op_sel:[1,0,0] op_sel_hi:[1,1,1]
v_pk_fma_f32 v[28:29], v[242:243], v[140:141], v[28:29] op_sel:[1,0,0] op_sel_hi:[1,1,1]
v_pk_fma_f32 v[30:31], v[242:243], v[142:143], v[30:31] op_sel:[1,0,0] op_sel_hi:[1,1,1]
v_pk_mul_f32 v[24:25], v[244:245], v[24:25] op_sel:[1,0] op_sel_hi:[1,1]
v_pk_mul_f32 v[26:27], v[244:245], v[26:27] op_sel:[1,0] op_sel_hi:[1,1]
v_pk_mul_f32 v[28:29], v[244:245], v[28:29] op_sel:[1,0] op_sel_hi:[1,1]
v_pk_mul_f32 v[30:31], v[244:245], v[30:31] op_sel:[1,0] op_sel_hi:[1,1]
MM v[0:3],a[204:207],v[208:211],v[0:3]
CV v172,v24,v25
CV v173,v26,v27
MM v[8:11],a[192:195],v[164:167],0
CV v174,v28,v29
CV v175,v30,v31
MM v[8:11],a[196:199],v[180:183],v[8:11]
v_xor_b32 v143, 128, v253
MM v[8:11],a[200:203],v[196:199],v[8:11]
RD v[136:139],v143 offset:0
RD v[140:143],v143 offset:16
MM v[8:11],a[204:207],v[212:215],v[8:11]
MM v[4:7],a[208:211],v[160:163],0
v_max3_f32 v152, v0, v1, v2
v_max_f32 v152, v152, v3
MM v[4:7],a[212:215],v[176:179],v[4:7]
buffer_load_dwordx4 v235, s[12:15], s77 offen offset:3072 lds
MM v[4:7],a[216:219],v[192:195],v[4:7]
s_add_u32 s77, s64, 32768
s_add_u32 s81, s77, 896
s_add_u32 m0, s65, 98304
v_mov_b32 v153, v152
MM v[4:7],a[220:223],v[208:211],v[4:7]
s_waitcnt lgkmcnt(0)
v_permlane16_swap_b32 v152, v153
v_max_f32 v152, v152, v153
MM v[12:15],a[208:211],v[164:167],0
s_waitcnt vmcnt(8)
s_barrier
RD a[192:195],v224 offset:16384
v_mov_b32 v153, v152
MM v[12:15],a[212:215],v[180:183],v[12:15]
RD a[196:199],v225 offset:16384
RD a[200:203],v226 offset:16384
v_permlane32_swap_b32 v152, v153
v_max_f32 v152, v152, v153
v_sub_f32 v128, 0, v152
MM v[12:15],a[216:219],v[196:199],v[12:15]
RD a[204:207],v227 offset:16384
v_sub_f32 v129, 0, v152
v_sub_f32 v130, 0, v152
MM v[12:15],a[220:223],v[212:215],v[12:15]
v_sub_f32 v131, 0, v152
v_max3_f32 v154, v8, v9, v10
MM v[32:35],a[224:227],v[160:163],0
RD a[208:211],v224 offset:20480
RD a[212:215],v225 offset:20480
v_max_f32 v154, v154, v11
v_mov_b32 v155, v154
MM v[32:35],a[228:231],v[176:179],v[32:35]
RD a[216:219],v226 offset:20480
RD a[220:223],v227 offset:20480
v_permlane16_swap_b32 v154, v155
v_max_f32 v154, v154, v155
MM v[32:35],a[232:235],v[192:195],v[32:35]
v_mov_b32 v155, v154
v_sub_f32 v0, v0, v152
MM v[32:35],a[236:239],v[208:211],v[32:35]
buffer_load_dwordx4 v235, s[12:15], s81 offen offset:0 lds
v_permlane32_swap_b32 v154, v155
v_max_f32 v154, v154, v155
v_sub_f32 v132, 0, v154
MM v[40:43],a[224:227],v[164:167],0
v_sub_f32 v133, 0, v154
v_sub_f32 v134, 0, v154
v_sub_f32 v135, 0, v154
v_sub_f32 v1, v1, v152
EX v0,v0
MM v[40:43],a[228:231],v[180:183],v[40:43]
EX v1,v1
MM v[40:43],a[232:235],v[196:199],v[40:43]
v_sub_f32 v2, v2, v152
v_sub_f32 v3, v3, v152
MM v[40:43],a[236:239],v[212:215],v[40:43]
EX v2,v2
MM v[36:39],a[240:243],v[160:163],v[128:131]
RD a[224:227],v224 offset:24576
RD a[228:231],v225 offset:24576
EX v3,v3
MM v[36:39],a[244:247],v[176:179],v[36:39]
RD a[232:235],v226 offset:24576
RD a[236:239],v227 offset:24576
CV v0,v0,v1
v_sub_f32 v4, v4, v152
MM v[36:39],a[248:251],v[192:195],v[36:39]
v_sub_f32 v5, v5, v152
EX v4,v4
MM v[36:39],a[252:255],v[208:211],v[36:39]
EX v5,v5
MM v[44:47],a[240:243],v[164:167],v[132:135]
CV v1,v2,v3
v_sub_f32 v6, v6, v152
MM v[44:47],a[244:247],v[180:183],v[44:47]
buffer_load_dwordx4 v235, s[12:15], s77 offen offset:1024 lds
v_sub_f32 v7, v7, v152
MM v[44:47],a[248:251],v[196:199],v[44:47]
EX v6,v6
EX v7,v7
MM v[44:47],a[252:255],v[212:215],v[44:47]
CV v2,v4,v5
v_sub_f32 v8, v8, v154
MM v[60:63],a[124:127],a[188:191],v[60:63]
RD a[240:243],v224 offset:28672
RD a[244:247],v225 offset:28672
v_sub_f32 v9, v9, v154
MM v[60:63],a[120:123],a[184:187],v[60:63]
RD a[248:251],v226 offset:28672
RD a[252:255],v227 offset:28672
EX v8,v8
MM v[60:63],a[116:119],a[156:159],v[60:63]
EX v9,v9
MM v[60:63],a[112:115],a[152:155],v[60:63]
CV v3,v6,v7
v_sub_f32 v10, v10, v154
MM v[80:83],a[0:3],a[144:147],v[80:83]
v_sub_f32 v11, v11, v154
EX v10,v10
MM v[80:83],a[4:7],a[148:151],v[80:83]
EX v11,v11
MM v[80:83],a[8:11],a[176:179],v[80:83]
CV v8,v8,v9
v_sub_f32 v12, v12, v154
MM v[80:83],a[12:15],a[180:183],v[80:83]
buffer_load_dwordx4 v235, s[12:15], s81 offen offset:2048 lds
v_sub_f32 v13, v13, v154
EX v12,v12
MM v[88:91],a[12:15],a[188:191],v[88:91]
EX v13,v13
MM v[88:91],a[8:11],a[184:187],v[88:91]
v_pk_fma_f32 v[48:49], v[242:243], v[136:137], v[48:49] op_sel:[0,0,0] op_sel_hi:[0,1,1]
v_pk_fma_f32 v[50:51], v[242:243], v[138:139], v[50:51] op_sel:[0,0,0] op_sel_hi:[0,1,1]
v_pk_fma_f32 v[52:53], v[242:243], v[140:141], v[52:53] op_sel:[0,0,0] op_sel_hi:[0,1,1]
v_pk_fma_f32 v[54:55], v[242:243], v[142:143], v[54:55] op_sel:[0,0,0] op_sel_hi:[0,1,1]
v_pk_mul_f32 v[48:49], v[244:245], v[48:49] op_sel:[0,0] op_sel_hi:[0,1]
v_pk_mul_f32 v[50:51], v[244:245], v[50:51] op_sel:[0,0] op_sel_hi:[0,1]
v_pk_mul_f32 v[52:53], v[244:245], v[52:53] op_sel:[0,0] op_sel_hi:[0,1]
v_pk_mul_f32 v[54:55], v[244:245], v[54:55] op_sel:[0,0] op_sel_hi:[0,1]
CV v9,v10,v11
MM v[88:91],a[4:7],a[156:159],v[88:91]
v_sub_f32 v14, v14, v154
v_sub_f32 v15, v15, v154
EX v14,v14
MM v[88:91],a[0:3],a[152:155],v[88:91]
EX v15,v15
MM v[84:87],a[16:19],a[144:147],v[84:87]
CV v10,v12,v13
v_sub_f32 v32, v32, v152
MM v[84:87],a[20:23],a[148:151],v[84:87]
v_sub_f32 v33, v33, v152
CV v184,v48,v49
MM v[84:87],a[24:27],a[176:179],v[84:87]
EX v32,v32
MM v[84:87],a[28:31],a[180:183],v[84:87]
EX v33,v33
MM v[92:95],a[28:31],a[188:191],v[92:95]
CV v11,v14,v15
v_sub_f32 v34, v34, v152
v_sub_f32 v35, v35, v152
MM v[92:95],a[24:27],a[184:187],v[92:95]
buffer_load_dwordx4 v235, s[12:15], s77 offen offset:3072 lds
EX v34,v34
MM v[92:95],a[20:23],a[156:159],v[92:95]
s_add_u32 s77, s64, 49152
s_add_u32 m0, s65, 49152
CV v185,v50,v51
EX v35,v35
MM v[92:95],a[16:19],a[152:155],v[92:95]
buffer_load_dwordx4 v228, s[8:11], s77 offen offset:0 lds
CV v4,v32,v33
EX v36,v36
MM v[112:115],a[32:35],a[144:147],v[112:115]
EX v37,v37
MM v[112:115],a[36:39],a[148:151],v[112:115]
CV v5,v34,v35
CV v186,v52,v53
MM v[112:115],a[40:43],a[176:179],v[112:115]
EX v38,v38
EX v39,v39
MM v[112:115],a[44:47],a[180:183],v[112:115]
buffer_load_dwordx4 v229, s[8:11], s77 offen offset:1024 lds
CV v6,v36,v37
MM v[120:123],a[44:47],a[188:191],v[120:123]
v_sub_f32 v40, v40, v154
v_sub_f32 v41, v41, v154
MM v[120:123],a[40:43],a[184:187],v[120:123]
EX v40,v40
MM v[120:123],a[36:39],a[156:159],v[120:123]
EX v41,v41
MM v[120:123],a[32:35],a[152:155],v[120:123]
buffer_load_dwordx4 v230, s[8:11], s77 offen offset:2048 lds
CV v7,v38,v39
v_sub_f32 v42, v42, v154
MM v[116:119],a[48:51],a[144:147],v[116:119]
v_sub_f32 v43, v43, v154
CV v187,v54,v55
EX v42,v42
MM v[116:119],a[52:55],a[148:151],v[116:119]
EX v43,v43
MM v[116:119],a[56:59],a[176:179],v[116:119]
v_pk_fma_f32 v[56:57], v[242:243], v[136:137], v[56:57] op_sel:[1,0,0] op_sel_hi:[1,1,1]
v_pk_fma_f32 v[58:59], v[242:243], v[138:139], v[58:59] op_sel:[1,0,0] op_sel_hi:[1,1,1]
v_pk_fma_f32 v[60:61], v[242:243], v[140:141], v[60:61] op_sel:[1,0,0] op_sel_hi:[1,1,1]
v_pk_fma_f32 v[62:63], v[242:243], v[142:143], v[62:63] op_sel:[1,0,0] op_sel_hi:[1,1,1]
v_pk_mul_f32 v[56:57], v[244:245], v[56:57] op_sel:[1,0] op_sel_hi:[1,1]
v_pk_mul_f32 v[58:59], v[244:245], v[58:59] op_sel:[1,0] op_sel_hi:[1,1]
v_pk_mul_f32 v[60:61], v[244:245], v[60:61] op_sel:[1,0] op_sel_hi:[1,1]
v_pk_mul_f32 v[62:63], v[244:245], v[62:63] op_sel:[1,0] op_sel_hi:[1,1]
CV v12,v40,v41
EX v44,v44
MM v[116:119],a[60:63],a[180:183],v[116:119]
buffer_load_dwordx4 v231, s[8:11], s77 offen offset:3072 lds
EX v45,v45
MM v[124:127],a[60:63],a[188:191],v[124:127]
s_add_u32 s77, s64, 49152
s_add_u32 s81, s77, 896
s_add_u32 m0, s65, 114688
CV v13,v42,v43
MM v[124:127],a[56:59],a[184:187],v[124:127]
EX v46,v46
MM v[124:127],a[52:55],a[156:159],v[124:127]
EX v47,v47
CV v14,v44,v45
CV v15,v46,v47
MM v[124:127],a[48:51],a[152:155],v[124:127]
buffer_load_dwordx4 v235, s[12:15], s81 offen offset:0 lds
v_smfmac_f32_16x16x64_bf16 v[236:239], v[144:147], v[0:7], v234
CV v188,v56,v57
v_smfmac_f32_16x16x64_bf16 v[236:239], v[148:151], v[8:15], v234
s_waitcnt lgkmcnt(0)
CV v189,v58,v59
buffer_load_dwordx4 v235, s[12:15], s77 offen offset:1024 lds
buffer_load_dwordx4 v235, s[12:15], s81 offen offset:2048 lds
buffer_load_dwordx4 v235, s[12:15], s77 offen offset:3072 lds
s_add_u32 s64, s64, 49152
MM v[64:67],a[192:195],v[160:163],v[128:131]
s_add_u32 s64, s64, 16384
TR a[128:129],v232 offset:0
CV v190,v60,v61
MM v[64:67],a[196:199],v[176:179],v[64:67]
s_add_u32 s81, s64, 896
TR a[130:131],v232 offset:4096
CV v191,v62,v63
MM v[64:67],a[200:203],v[192:195],v[64:67]
TR a[132:133],v232 offset:8192
TR a[134:135],v232 offset:12288
MM v[64:67],a[204:207],v[208:211],v[64:67]
v_add_f32 v246, v236, v237
v_xor_b32 v143, 256, v253
MM v[68:71],a[220:223],v[208:211],v[128:131]
TR a[136:137],v233 offset:0
v_cmp_nge_f32 vcc, s71, v246
MM v[68:71],a[216:219],v[192:195],v[68:71]
s_cbranch_vccnz LSITE0_%=
LBACK0_%=:
TR a[138:139],v233 offset:4096
RD v[136:139],v143 offset:0
MM v[68:71],a[212:215],v[176:179],v[68:71]
TR a[140:141],v233 offset:8192
RD v[140:143],v143 offset:16
MM v[68:71],a[208:211],v[160:163],v[68:71]
TR a[142:143],v233 offset:12288
TR a[144:145],v232 offset:512
MM v[76:79],a[208:211],v[164:167],v[132:135]
TR a[146:147],v232 offset:4608
TR a[148:149],v232 offset:8704
MM v[76:79],a[212:215],v[180:183],v[76:79]
TR a[150:151],v232 offset:12800
TR a[152:153],v233 offset:512
MM v[76:79],a[216:219],v[196:199],v[76:79]
s_waitcnt lgkmcnt(6)
v_pk_fma_f32 v[80:81], v[242:243], v[136:137], v[80:81] op_sel:[0,0,0] op_sel_hi:[0,1,1]
v_pk_fma_f32 v[82:83], v[242:243], v[138:139], v[82:83] op_sel:[0,0,0] op_sel_hi:[0,1,1]
v_pk_fma_f32 v[84:85], v[242:243], v[140:141], v[84:85] op_sel:[0,0,0] op_sel_hi:[0,1,1]
v_pk_fma_f32 v[86:87], v[242:243], v[142:143], v[86:87] op_sel:[0,0,0] op_sel_hi:[0,1,1]
v_pk_mul_f32 v[80:81], v[244:245], v[80:81] op_sel:[0,0] op_sel_hi:[0,1]
v_pk_mul_f32 v[82:83], v[244:245], v[82:83] op_sel:[0,0] op_sel_hi:[0,1]
v_pk_mul_f32 v[84:85], v[244:245], v[84:85] op_sel:[0,0] op_sel_hi:[0,1]
v_pk_mul_f32 v[86:87], v[244:245], v[86:87] op_sel:[0,0] op_sel_hi:[0,1]
MM v[76:79],a[220:223],v[212:215],v[76:79]
EX v64,v64
MM v[72:75],a[204:207],v[212:215],v[132:135]
EX v65,v65
MM v[72:75],a[200:203],v[196:199],v[72:75]
TR a[154:155],v233 offset:4608
TR a[156:157],v233 offset:8704
MM v[72:75],a[196:199],v[180:183],v[72:75]
TR a[158:159],v233 offset:12800
TR a[160:161],v232 offset:1024
MM v[72:75],a[192:195],v[164:167],v[72:75]
EX v66,v66
MM v[96:99],a[224:227],v[160:163],v[128:131]
EX v67,v67
MM v[96:99],a[228:231],v[176:179],v[96:99]
TR a[162:163],v232 offset:5120
TR a[164:165],v232 offset:9216
MM v[96:99],a[232:235],v[192:195],v[96:99]
TR a[166:167],v232 offset:13312
CV v200,v80,v81
EX v68,v68
MM v[96:99],a[236:239],v[208:211],v[96:99]
TR a[168:169],v233 offset:1024
CV v201,v82,v83
MM v[100:103],a[252:255],v[208:211],v[128:131]
TR a[170:171],v233 offset:5120
EX v69,v69
MM v[100:103],a[248:251],v[192:195],v[100:103]
TR a[172:173],v233 offset:9216
TR a[174:175],v233 offset:13312
CV v202,v84,v85
MM v[100:103],a[244:247],v[176:179],v[100:103]
TR a[176:177],v232 offset:1536
EX v70,v70
CV v203,v86,v87
MM v[100:103],a[240:243],v[160:163],v[100:103]
TR a[178:179],v232 offset:5632
v_pk_fma_f32 v[88:89], v[242:243], v[136:137], v[88:89] op_sel:[1,0,0] op_sel_hi:[1,1,1]
v_pk_fma_f32 v[90:91], v[242:243], v[138:139], v[90:91] op_sel:[1,0,0] op_sel_hi:[1,1,1]
v_pk_fma_f32 v[92:93], v[242:243], v[140:141], v[92:93] op_sel:[1,0,0] op_sel_hi:[1,1,1]
v_pk_fma_f32 v[94:95], v[242:243], v[142:143], v[94:95] op_sel:[1,0,0] op_sel_hi:[1,1,1]
v_pk_mul_f32 v[88:89], v[244:245], v[88:89] op_sel:[1,0] op_sel_hi:[1,1]
)ASM"
R"ASM(
v_pk_mul_f32 v[90:91], v[244:245], v[90:91] op_sel:[1,0] op_sel_hi:[1,1]
v_pk_mul_f32 v[92:93], v[244:245], v[92:93] op_sel:[1,0] op_sel_hi:[1,1]
v_pk_mul_f32 v[94:95], v[244:245], v[94:95] op_sel:[1,0] op_sel_hi:[1,1]
EX v71,v71
MM v[108:111],a[240:243],v[164:167],v[132:135]
TR a[180:181],v232 offset:9728
CV v64,v64,v65
MM v[108:111],a[244:247],v[180:183],v[108:111]
TR a[182:183],v232 offset:13824
TR a[184:185],v233 offset:1536
EX v72,v72
MM v[108:111],a[248:251],v[196:199],v[108:111]
TR a[186:187],v233 offset:5632
TR a[188:189],v233 offset:9728
TR a[190:191],v233 offset:13824
EX v73,v73
MM v[108:111],a[252:255],v[212:215],v[108:111]
CV v65,v66,v67
CV v66,v68,v69
MM v[104:107],a[236:239],v[212:215],v[132:135]
EX v74,v74
MM v[104:107],a[232:235],v[196:199],v[104:107]
EX v75,v75
MM v[104:107],a[228:231],v[180:183],v[104:107]
CV v67,v70,v71
CV v204,v88,v89
MM v[104:107],a[224:227],v[164:167],v[104:107]
s_waitcnt vmcnt(12) lgkmcnt(0)
s_barrier
CV v205,v90,v91
EX v76,v76
MM a[0:3],a[128:131],v[0:3],0
s_add_u32 m0, s65, 0
EX v77,v77
MM a[0:3],a[132:135],v[4:7],a[0:3]
CV v206,v92,v93
CV v72,v72,v73
MM a[8:11],a[132:135],v[12:15],0
CV v207,v94,v95
v_xor_b32 v143, 384, v253
MM a[8:11],a[128:131],v[8:11],a[8:11]
EX v78,v78
MM a[12:15],a[136:139],v[8:11],0
EX v79,v79
MM a[12:15],a[140:143],v[12:15],a[12:15]
RD v[136:139],v143 offset:0
RD v[140:143],v143 offset:16
MM a[4:7],a[140:143],v[4:7],0
EX v96,v96
MM a[4:7],a[136:139],v[0:3],a[4:7]
RD a[192:195],v224 offset:32768
CV v73,v74,v75
MM a[32:35],a[144:147],v[0:3],0
EX v97,v97
MM a[32:35],a[148:151],v[4:7],a[32:35]
s_waitcnt lgkmcnt(1)
buffer_load_dwordx4 v228, s[8:11], s64 offen offset:0 lds
v_pk_fma_f32 v[112:113], v[242:243], v[136:137], v[112:113] op_sel:[0,0,0] op_sel_hi:[0,1,1]
v_pk_fma_f32 v[114:115], v[242:243], v[138:139], v[114:115] op_sel:[0,0,0] op_sel_hi:[0,1,1]
v_pk_fma_f32 v[116:117], v[242:243], v[140:141], v[116:117] op_sel:[0,0,0] op_sel_hi:[0,1,1]
v_pk_fma_f32 v[118:119], v[242:243], v[142:143], v[118:119] op_sel:[0,0,0] op_sel_hi:[0,1,1]
v_pk_mul_f32 v[112:113], v[244:245], v[112:113] op_sel:[0,0] op_sel_hi:[0,1]
v_pk_mul_f32 v[114:115], v[244:245], v[114:115] op_sel:[0,0] op_sel_hi:[0,1]
v_pk_mul_f32 v[116:117], v[244:245], v[116:117] op_sel:[0,0] op_sel_hi:[0,1]
v_pk_mul_f32 v[118:119], v[244:245], v[118:119] op_sel:[0,0] op_sel_hi:[0,1]
EX v98,v98
MM a[40:43],a[148:151],v[12:15],0
RD a[196:199],v225 offset:32768
CV v74,v76,v77
MM a[40:43],a[144:147],v[8:11],a[40:43]
RD a[200:203],v226 offset:32768
EX v99,v99
MM a[44:47],a[152:155],v[8:11],0
buffer_load_dwordx4 v229, s[8:11], s64 offen offset:1024 lds
MM a[44:47],a[156:159],v[12:15],a[44:47]
RD a[204:207],v227 offset:32768
EX v100,v100
MM a[36:39],a[156:159],v[4:7],0
EX v101,v101
CV v75,v78,v79
MM a[36:39],a[152:155],v[0:3],a[36:39]
RD a[208:211],v224 offset:36864
CV v216,v112,v113
MM a[64:67],a[160:163],v[0:3],0
EX v102,v102
MM a[64:67],a[164:167],v[4:7],a[64:67]
buffer_load_dwordx4 v230, s[8:11], s64 offen offset:2048 lds
RD a[212:215],v225 offset:36864
CV v217,v114,v115
MM a[72:75],a[164:167],v[12:15],0
EX v103,v103
CV v68,v96,v97
CV v218,v116,v117
MM a[72:75],a[160:163],v[8:11],a[72:75]
RD a[216:219],v226 offset:36864
CV v219,v118,v119
EX v104,v104
MM a[76:79],a[168:171],v[8:11],0
buffer_load_dwordx4 v231, s[8:11], s64 offen offset:3072 lds
RD a[220:223],v227 offset:36864
MM a[76:79],a[172:175],v[12:15],a[76:79]
s_add_u32 m0, s65, 65536
v_pk_fma_f32 v[120:121], v[242:243], v[136:137], v[120:121] op_sel:[1,0,0] op_sel_hi:[1,1,1]
v_pk_fma_f32 v[122:123], v[242:243], v[138:139], v[122:123] op_sel:[1,0,0] op_sel_hi:[1,1,1]
v_pk_fma_f32 v[124:125], v[242:243], v[140:141], v[124:125] op_sel:[1,0,0] op_sel_hi:[1,1,1]
v_pk_fma_f32 v[126:127], v[242:243], v[142:143], v[126:127] op_sel:[1,0,0] op_sel_hi:[1,1,1]
v_pk_mul_f32 v[120:121], v[244:245], v[120:121] op_sel:[1,0] op_sel_hi:[1,1]
v_pk_mul_f32 v[122:123], v[244:245], v[122:123] op_sel:[1,0] op_sel_hi:[1,1]
v_pk_mul_f32 v[124:125], v[244:245], v[124:125] op_sel:[1,0] op_sel_hi:[1,1]
v_pk_mul_f32 v[126:127], v[244:245], v[126:127] op_sel:[1,0] op_sel_hi:[1,1]
EX v105,v105
CV v69,v98,v99
MM a[68:71],a[172:175],v[4:7],0
RD a[224:227],v224 offset:40960
CV v70,v100,v101
MM a[68:71],a[168:171],v[0:3],a[68:71]
EX v106,v106
MM a[96:99],a[176:179],v[0:3],0
RD a[228:231],v225 offset:40960
buffer_load_dwordx4 v235, s[12:15], s81 offen offset:0 lds
EX v107,v107
MM a[96:99],a[180:183],v[4:7],a[96:99]
RD a[232:235],v226 offset:40960
CV v71,v102,v103
MM a[104:107],a[180:183],v[12:15],0
EX v108,v108
MM a[104:107],a[176:179],v[8:11],a[104:107]
EX v109,v109
MM a[108:111],a[184:187],v[8:11],0
RD a[236:239],v227 offset:40960
RD a[240:243],v224 offset:45056
buffer_load_dwordx4 v235, s[12:15], s64 offen offset:1024 lds
MM a[108:111],a[188:191],v[12:15],a[108:111]
EX v110,v110
CV v220,v120,v121
MM a[100:103],a[188:191],v[4:7],0
RD a[244:247],v225 offset:45056
RD a[248:251],v226 offset:45056
RD a[252:255],v227 offset:45056
CV v221,v122,v123
EX v111,v111
CV v222,v124,v125
CV v223,v126,v127
MM a[100:103],a[184:187],v[0:3],a[100:103]
buffer_load_dwordx4 v235, s[12:15], s81 offen offset:2048 lds
CV v76,v104,v105
v_smfmac_f32_16x16x64_bf16 v[236:239], v[144:147], v[64:71], v234
CV v77,v106,v107
CV v78,v108,v109
CV v79,v110,v111
s_nop 1
v_smfmac_f32_16x16x64_bf16 v[236:239], v[148:151], v[72:79], v234
s_waitcnt lgkmcnt(0)
buffer_load_dwordx4 v235, s[12:15], s64 offen offset:3072 lds
MM v[0:3],a[192:195],v[160:163],v[128:131]
s_add_u32 s64, s64, 16384
TR a[128:129],v232 offset:16384
TR a[130:131],v232 offset:20480
MM v[0:3],a[196:199],v[176:179],v[0:3]
s_add_u32 s81, s64, 896
s_nop 0
LPG5_%=:
s_cbranch_execz LPG9_%=
TR a[132:133],v232 offset:24576
TR a[134:135],v232 offset:28672
MM v[0:3],a[200:203],v[192:195],v[0:3]
TR a[136:137],v233 offset:16384
TR a[138:139],v233 offset:20480
MM v[0:3],a[204:207],v[208:211],v[0:3]
TR a[140:141],v233 offset:24576
v_add_f32 v247, v236, v237
MM v[4:7],a[220:223],v[208:211],v[128:131]
TR a[142:143],v233 offset:28672
v_cmp_nge_f32 vcc, s71, v247
MM v[4:7],a[216:219],v[192:195],v[4:7]
s_cbranch_vccnz LSITE1_%=
LBACK1_%=:
TR a[144:145],v232 offset:16896
TR a[146:147],v232 offset:20992
MM v[4:7],a[212:215],v[176:179],v[4:7]
s_cmp_lg_u32 %[sb], 0
s_cbranch_scc1 LKA_%=
s_lshl_b32 s90, %[swave], 10
s_add_u32 s90, s90, 81920
buffer_load_dwordx4 v[48:51], v248, s[20:23], s90 offen offset:0 sc1
s_add_u32 s78, s90, 8192
buffer_load_dwordx4 v[56:59], v248, s[20:23], s78 offen offset:0 sc1
LKA_%=:
MM v[4:7],a[208:211],v[160:163],v[4:7]
TR a[148:149],v232 offset:25088
TR a[150:151],v232 offset:29184
MM v[12:15],a[208:211],v[164:167],v[132:135]
EX v0,v0
MM v[12:15],a[212:215],v[180:183],v[12:15]
EX v1,v1
MM v[12:15],a[216:219],v[196:199],v[12:15]
TR a[152:153],v233 offset:16896
CV v0,v0,v1
MM v[12:15],a[220:223],v[212:215],v[12:15]
EX v2,v2
MM v[8:11],a[204:207],v[212:215],v[132:135]
TR a[154:155],v233 offset:20992
TR a[156:157],v233 offset:25088
MM v[8:11],a[200:203],v[196:199],v[8:11]
EX v3,v3
MM v[8:11],a[196:199],v[180:183],v[8:11]
TR a[158:159],v233 offset:29184
CV v1,v2,v3
MM v[8:11],a[192:195],v[164:167],v[8:11]
EX v4,v4
MM v[32:35],a[224:227],v[160:163],v[128:131]
TR a[160:161],v232 offset:17408
TR a[162:163],v232 offset:21504
MM v[32:35],a[228:231],v[176:179],v[32:35]
EX v5,v5
MM v[32:35],a[232:235],v[192:195],v[32:35]
TR a[164:165],v232 offset:25600
CV v2,v4,v5
MM v[32:35],a[236:239],v[208:211],v[32:35]
TR a[166:167],v232 offset:29696
TR a[168:169],v233 offset:17408
MM v[36:39],a[252:255],v[208:211],v[128:131]
EX v6,v6
MM v[36:39],a[248:251],v[192:195],v[36:39]
EX v7,v7
MM v[36:39],a[244:247],v[176:179],v[36:39]
TR a[170:171],v233 offset:21504
CV v3,v6,v7
MM v[36:39],a[240:243],v[160:163],v[36:39]
TR a[172:173],v233 offset:25600
TR a[174:175],v233 offset:29696
MM v[44:47],a[240:243],v[164:167],v[132:135]
EX v8,v8
MM v[44:47],a[244:247],v[180:183],v[44:47]
TR a[176:177],v232 offset:17920
TR a[178:179],v232 offset:22016
MM v[44:47],a[248:251],v[196:199],v[44:47]
EX v9,v9
MM v[44:47],a[252:255],v[212:215],v[44:47]
TR a[180:181],v232 offset:26112
CV v8,v8,v9
MM v[40:43],a[236:239],v[212:215],v[132:135]
EX v10,v10
MM v[40:43],a[232:235],v[196:199],v[40:43]
TR a[182:183],v232 offset:30208
TR a[184:185],v233 offset:17920
MM v[40:43],a[228:231],v[180:183],v[40:43]
EX v11,v11
MM v[40:43],a[224:227],v[164:167],v[40:43]
TR a[186:187],v233 offset:22016
CV v9,v10,v11
MM v[16:19],a[192:195],v[168:171],0
EX v12,v12
MM v[16:19],a[196:199],v[184:187],v[16:19]
TR a[188:189],v233 offset:26112
TR a[190:191],v233 offset:30208
MM v[16:19],a[200:203],v[200:203],v[16:19]
EX v13,v13
MM v[16:19],a[204:207],v[216:219],v[16:19]
EX v14,v14
MM v[24:27],a[192:195],v[172:175],0
EX v15,v15
MM v[24:27],a[196:199],v[188:191],v[24:27]
EX v32,v32
MM v[24:27],a[200:203],v[204:207],v[24:27]
EX v33,v33
MM v[24:27],a[204:207],v[220:223],v[24:27]
s_waitcnt vmcnt(12) lgkmcnt(0)
s_barrier
CV v10,v12,v13
MM a[0:3],a[128:131],v[64:67],a[0:3]
s_add_u32 m0, s65, 16384
RD a[192:195],v224 offset:49152
CV v11,v14,v15
MM a[0:3],a[132:135],v[68:71],a[0:3]
CV v4,v32,v33
v_max3_f32 v154, v16, v17, v18
MM a[8:11],a[132:135],v[76:79],a[8:11]
buffer_load_dwordx4 v228, s[8:11], s64 offen offset:0 lds
MM a[8:11],a[128:131],v[72:75],a[8:11]
RD a[196:199],v225 offset:49152
v_max_f32 v154, v154, v19
MM a[12:15],a[136:139],v[72:75],a[12:15]
EX v34,v34
MM a[12:15],a[140:143],v[76:79],a[12:15]
RD a[200:203],v226 offset:49152
v_mov_b32 v155, v154
MM a[4:7],a[140:143],v[68:71],a[4:7]
s_nop 0
v_permlane16_swap_b32 v154, v155
v_max_f32 v154, v154, v155
MM a[4:7],a[136:139],v[64:67],a[4:7]
EX v35,v35
MM a[32:35],a[144:147],v[64:67],a[32:35]
RD a[204:207],v227 offset:49152
v_mov_b32 v155, v154
MM a[32:35],a[148:151],v[68:71],a[32:35]
s_nop 0
v_permlane32_swap_b32 v154, v155
v_max_f32 v154, v154, v155
MM a[40:43],a[148:151],v[76:79],a[40:43]
EX v36,v36
MM a[40:43],a[144:147],v[72:75],a[40:43]
v_sub_f32 v136, 0, v154
v_sub_f32 v137, 0, v154
MM a[44:47],a[152:155],v[72:75],a[44:47]
EX v37,v37
MM a[44:47],a[156:159],v[76:79],a[44:47]
buffer_load_dwordx4 v229, s[8:11], s64 offen offset:1024 lds
v_sub_f32 v138, 0, v154
v_sub_f32 v139, 0, v154
MM a[36:39],a[156:159],v[68:71],a[36:39]
EX v38,v38
MM a[36:39],a[152:155],v[64:67],a[36:39]
RD a[208:211],v224 offset:53248
v_mov_b32_dpp v152, v144 row_ror:8 row_mask:0xf bank_mask:0xf
v_mov_b32_dpp v153, v145 row_ror:8 row_mask:0xf bank_mask:0xf
MM a[64:67],a[160:163],v[64:67],a[64:67]
EX v39,v39
v_mov_b32_dpp v154, v146 row_ror:8 row_mask:0xf bank_mask:0xf
MM a[64:67],a[164:167],v[68:71],a[64:67]
RD a[212:215],v225 offset:53248
buffer_load_dwordx4 v230, s[8:11], s64 offen offset:2048 lds
v_mov_b32_dpp v155, v147 row_ror:8 row_mask:0xf bank_mask:0xf
MM a[72:75],a[164:167],v[76:79],a[72:75]
RD a[216:219],v226 offset:53248
v_max3_f32 v158, v24, v25, v26
EX v40,v40
MM a[72:75],a[160:163],v[72:75],a[72:75]
v_max_f32 v158, v158, v27
v_mov_b32 v159, v158
CV v5,v34,v35
MM a[76:79],a[168:171],v[72:75],a[76:79]
buffer_load_dwordx4 v231, s[8:11], s64 offen offset:3072 lds
RD a[220:223],v227 offset:53248
EX v41,v41
v_permlane16_swap_b32 v158, v159
MM a[76:79],a[172:175],v[76:79],a[76:79]
s_add_u32 m0, s65, 81920
v_max_f32 v158, v158, v159
CV v6,v36,v37
MM a[68:71],a[172:175],v[68:71],a[68:71]
RD a[224:227],v224 offset:57344
EX v42,v42
v_mov_b32 v159, v158
MM a[68:71],a[168:171],v[64:67],a[68:71]
s_nop 0
v_permlane32_swap_b32 v158, v159
v_max_f32 v158, v158, v159
EX v43,v43
MM a[96:99],a[176:179],v[64:67],a[96:99]
RD a[228:231],v225 offset:57344
buffer_load_dwordx4 v235, s[12:15], s81 offen offset:0 lds
v_sub_f32 v140, 0, v158
MM a[96:99],a[180:183],v[68:71],a[96:99]
v_sub_f32 v141, 0, v158
CV v7,v38,v39
EX v44,v44
MM a[104:107],a[180:183],v[76:79],a[104:107]
RD a[232:235],v226 offset:57344
v_sub_f32 v142, 0, v158
MM a[104:107],a[176:179],v[72:75],a[104:107]
v_sub_f32 v143, 0, v158
v_mov_b32_dpp v156, v148 row_ror:8 row_mask:0xf bank_mask:0xf
EX v45,v45
MM a[108:111],a[184:187],v[72:75],a[108:111]
RD a[236:239],v227 offset:57344
buffer_load_dwordx4 v235, s[12:15], s64 offen offset:1024 lds
)ASM"
R"ASM(
v_mov_b32_dpp v157, v149 row_ror:8 row_mask:0xf bank_mask:0xf
v_mov_b32_dpp v158, v150 row_ror:8 row_mask:0xf bank_mask:0xf
v_mov_b32_dpp v159, v151 row_ror:8 row_mask:0xf bank_mask:0xf
MM a[108:111],a[188:191],v[76:79],a[108:111]
EX v46,v46
MM a[100:103],a[188:191],v[68:71],a[100:103]
RD a[240:243],v224 offset:61440
RD a[244:247],v225 offset:61440
RD a[248:251],v226 offset:61440
RD a[252:255],v227 offset:61440
EX v47,v47
MM a[100:103],a[184:187],v[64:67],a[100:103]
buffer_load_dwordx4 v235, s[12:15], s81 offen offset:2048 lds
CV v12,v40,v41
v_smfmac_f32_16x16x64_bf16 v[236:239], v[144:147], v[0:7], v234
CV v13,v42,v43
CV v14,v44,v45
CV v15,v46,v47
s_nop 1
v_smfmac_f32_16x16x64_bf16 v[236:239], v[148:151], v[8:15], v234
s_waitcnt lgkmcnt(0)
buffer_load_dwordx4 v235, s[12:15], s64 offen offset:3072 lds
MM v[64:67],a[192:195],v[160:163],v[128:131]
s_add_u32 s64, s64, 16384
TR a[128:129],v232 offset:32768
s_add_u32 s81, s64, 896
MM v[64:67],a[196:199],v[176:179],v[64:67]
TR a[130:131],v232 offset:36864
TR a[132:133],v232 offset:40960
MM v[64:67],a[200:203],v[192:195],v[64:67]
TR a[134:135],v232 offset:45056
TR a[136:137],v233 offset:32768
MM v[64:67],a[204:207],v[208:211],v[64:67]
TR a[138:139],v233 offset:36864
v_add_f32 v246, v236, v237
MM v[68:71],a[220:223],v[208:211],v[128:131]
TR a[140:141],v233 offset:40960
v_cmp_nge_f32 vcc, s71, v246
MM v[68:71],a[216:219],v[192:195],v[68:71]
s_cbranch_vccnz LSITE2_%=
LBACK2_%=:
TR a[142:143],v233 offset:45056
TR a[144:145],v232 offset:33280
MM v[68:71],a[212:215],v[176:179],v[68:71]
TR a[146:147],v232 offset:37376
TR a[148:149],v232 offset:41472
MM v[68:71],a[208:211],v[160:163],v[68:71]
TR a[150:151],v232 offset:45568
TR a[152:153],v233 offset:33280
MM v[76:79],a[208:211],v[164:167],v[132:135]
EX v64,v64
MM v[76:79],a[212:215],v[180:183],v[76:79]
EX v65,v65
MM v[76:79],a[216:219],v[196:199],v[76:79]
TR a[154:155],v233 offset:37376
CV v64,v64,v65
MM v[76:79],a[220:223],v[212:215],v[76:79]
EX v66,v66
MM v[72:75],a[204:207],v[212:215],v[132:135]
EX v67,v67
MM v[72:75],a[200:203],v[196:199],v[72:75]
TR a[156:157],v233 offset:41472
CV v65,v66,v67
MM v[72:75],a[196:199],v[180:183],v[72:75]
EX v68,v68
MM v[72:75],a[192:195],v[164:167],v[72:75]
EX v69,v69
MM v[80:83],a[192:195],v[168:171],v[136:139]
TR a[158:159],v233 offset:45568
CV v66,v68,v69
MM v[80:83],a[196:199],v[184:187],v[80:83]
EX v70,v70
MM v[80:83],a[200:203],v[200:203],v[80:83]
EX v71,v71
MM v[80:83],a[204:207],v[216:219],v[80:83]
TR a[160:161],v232 offset:33792
CV v67,v70,v71
MM v[84:87],a[220:223],v[216:219],v[136:139]
s_cmp_lg_u32 %[sb], 0
s_cbranch_scc1 LKB_%=
s_waitcnt vmcnt(8)
v_xor_b32 v48, s69, v48
v_xor_b32 v49, s69, v49
v_xor_b32 v50, s69, v50
v_xor_b32 v51, s69, v51
v_xor_b32 v56, s69, v56
v_xor_b32 v57, s69, v57
v_xor_b32 v58, s69, v58
v_xor_b32 v59, s69, v59
v_or3_b32 v52, v48, v49, v50
v_or3_b32 v52, v52, v51, v56
v_or3_b32 v52, v52, v57, v58
v_or_b32 v52, v52, v59
v_cmp_ne_u32 vcc, 0, v52
s_cbranch_vccnz LRFR_%=
LRFRb_%=:
s_lshl_b32 s90, %[swave], 11
s_add_u32 s78, s90, %[slds]
s_add_u32 s78, s78, 155648
s_add_u32 s90, s90, 53248
s_mov_b32 m0, s78
s_nop 0
buffer_load_dwordx4 v248, s[20:23], s90 offen offset:0 sc1 lds
buffer_load_dwordx4 v248, s[20:23], s90 offen offset:1024 sc1 lds
LKB_%=:
MM v[84:87],a[216:219],v[200:203],v[84:87]
EX v72,v72
MM v[84:87],a[212:215],v[184:187],v[84:87]
EX v73,v73
MM v[84:87],a[208:211],v[168:171],v[84:87]
EX v74,v74
MM v[92:95],a[208:211],v[172:175],v[140:143]
EX v75,v75
MM v[92:95],a[212:215],v[188:191],v[92:95]
CV v72,v72,v73
CV v73,v74,v75
MM v[92:95],a[216:219],v[204:207],v[92:95]
EX v76,v76
MM v[92:95],a[220:223],v[220:223],v[92:95]
EX v77,v77
MM v[88:91],a[204:207],v[220:223],v[140:143]
EX v78,v78
MM v[88:91],a[200:203],v[204:207],v[88:91]
EX v79,v79
MM v[88:91],a[196:199],v[188:191],v[88:91]
CV v74,v76,v77
CV v75,v78,v79
MM v[88:91],a[192:195],v[172:175],v[88:91]
EX v80,v80
MM v[96:99],a[224:227],v[160:163],v[128:131]
EX v81,v81
MM v[96:99],a[228:231],v[176:179],v[96:99]
TR a[162:163],v232 offset:37888
TR a[164:165],v232 offset:41984
MM v[96:99],a[232:235],v[192:195],v[96:99]
EX v82,v82
MM v[96:99],a[236:239],v[208:211],v[96:99]
EX v83,v83
MM v[100:103],a[252:255],v[208:211],v[128:131]
TR a[166:167],v232 offset:46080
CV v80,v80,v81
MM v[100:103],a[248:251],v[192:195],v[100:103]
EX v84,v84
MM v[100:103],a[244:247],v[176:179],v[100:103]
EX v85,v85
MM v[100:103],a[240:243],v[160:163],v[100:103]
TR a[168:169],v233 offset:33792
CV v81,v82,v83
MM v[108:111],a[240:243],v[164:167],v[132:135]
EX v86,v86
MM v[108:111],a[244:247],v[180:183],v[108:111]
EX v87,v87
MM v[108:111],a[248:251],v[196:199],v[108:111]
TR a[170:171],v233 offset:37888
CV v82,v84,v85
MM v[108:111],a[252:255],v[212:215],v[108:111]
EX v88,v88
MM v[104:107],a[236:239],v[212:215],v[132:135]
EX v89,v89
MM v[104:107],a[232:235],v[196:199],v[104:107]
TR a[172:173],v233 offset:41984
TR a[174:175],v233 offset:46080
MM v[104:107],a[228:231],v[180:183],v[104:107]
EX v90,v90
MM v[104:107],a[224:227],v[164:167],v[104:107]
EX v91,v91
MM v[112:115],a[224:227],v[168:171],v[136:139]
TR a[176:177],v232 offset:34304
CV v83,v86,v87
MM v[112:115],a[228:231],v[184:187],v[112:115]
EX v92,v92
MM v[112:115],a[232:235],v[200:203],v[112:115]
EX v93,v93
MM v[112:115],a[236:239],v[216:219],v[112:115]
TR a[178:179],v232 offset:38400
CV v88,v88,v89
MM v[116:119],a[252:255],v[216:219],v[136:139]
EX v94,v94
MM v[116:119],a[248:251],v[200:203],v[116:119]
EX v95,v95
MM v[116:119],a[244:247],v[184:187],v[116:119]
TR a[180:181],v232 offset:42496
TR a[182:183],v232 offset:46592
MM v[116:119],a[240:243],v[168:171],v[116:119]
EX v96,v96
MM v[124:127],a[240:243],v[172:175],v[140:143]
EX v97,v97
CV v89,v90,v91
MM v[124:127],a[244:247],v[188:191],v[124:127]
EX v98,v98
MM v[124:127],a[248:251],v[204:207],v[124:127]
TR a[184:185],v233 offset:34304
TR a[186:187],v233 offset:38400
TR a[188:189],v233 offset:42496
TR a[190:191],v233 offset:46592
MM v[124:127],a[252:255],v[220:223],v[124:127]
EX v99,v99
CV v90,v92,v93
MM v[120:123],a[236:239],v[220:223],v[140:143]
EX v100,v100
MM v[120:123],a[232:235],v[204:207],v[120:123]
EX v101,v101
CV v91,v94,v95
MM v[120:123],a[228:231],v[188:191],v[120:123]
EX v102,v102
MM v[120:123],a[224:227],v[172:175],v[120:123]
s_waitcnt vmcnt(12) lgkmcnt(0)
s_barrier
CV v68,v96,v97
MM a[0:3],a[128:131],v[0:3],a[0:3]
s_add_u32 m0, s65, 32768
EX v103,v103
MM a[0:3],a[132:135],v[4:7],a[0:3]
EX v104,v104
MM a[8:11],a[132:135],v[12:15],a[8:11]
CV v69,v98,v99
CV v70,v100,v101
MM a[8:11],a[128:131],v[8:11],a[8:11]
EX v105,v105
MM a[12:15],a[136:139],v[8:11],a[12:15]
EX v106,v106
MM a[12:15],a[140:143],v[12:15],a[12:15]
EX v107,v107
MM a[4:7],a[140:143],v[4:7],a[4:7]
EX v108,v108
MM a[4:7],a[136:139],v[0:3],a[4:7]
RD a[192:195],v224 offset:0
CV v71,v102,v103
MM a[32:35],a[144:147],v[0:3],a[32:35]
EX v109,v109
MM a[32:35],a[148:151],v[4:7],a[32:35]
buffer_load_dwordx4 v228, s[8:11], s64 offen offset:0 lds
MM a[40:43],a[148:151],v[12:15],a[40:43]
RD a[196:199],v225 offset:0
EX v110,v110
MM a[40:43],a[144:147],v[8:11],a[40:43]
EX v111,v111
CV v76,v104,v105
MM a[44:47],a[152:155],v[8:11],a[44:47]
RD a[200:203],v226 offset:0
EX v112,v112
MM a[44:47],a[156:159],v[12:15],a[44:47]
buffer_load_dwordx4 v229, s[8:11], s64 offen offset:1024 lds
MM a[36:39],a[156:159],v[4:7],a[36:39]
RD a[204:207],v227 offset:0
EX v113,v113
CV v77,v106,v107
MM a[36:39],a[152:155],v[0:3],a[36:39]
EX v114,v114
MM a[64:67],a[160:163],v[0:3],a[64:67]
RD a[208:211],v224 offset:4096
EX v115,v115
CV v78,v108,v109
MM a[64:67],a[164:167],v[4:7],a[64:67]
buffer_load_dwordx4 v230, s[8:11], s64 offen offset:2048 lds
MM a[72:75],a[164:167],v[12:15],a[72:75]
RD a[212:215],v225 offset:4096
EX v116,v116
MM a[72:75],a[160:163],v[8:11],a[72:75]
EX v117,v117
CV v79,v110,v111
MM a[76:79],a[168:171],v[8:11],a[76:79]
RD a[216:219],v226 offset:4096
CV v84,v112,v113
EX v118,v118
MM a[76:79],a[172:175],v[12:15],a[76:79]
buffer_load_dwordx4 v231, s[8:11], s64 offen offset:3072 lds
MM a[68:71],a[172:175],v[4:7],a[68:71]
s_add_u32 m0, s65, 98304
RD a[220:223],v227 offset:4096
EX v119,v119
MM a[68:71],a[168:171],v[0:3],a[68:71]
EX v120,v120
MM a[96:99],a[176:179],v[0:3],a[96:99]
RD a[224:227],v224 offset:8192
CV v85,v114,v115
EX v121,v121
MM a[96:99],a[180:183],v[4:7],a[96:99]
RD a[228:231],v225 offset:8192
CV v86,v116,v117
MM a[104:107],a[180:183],v[12:15],a[104:107]
buffer_load_dwordx4 v235, s[12:15], s81 offen offset:0 lds
EX v122,v122
MM a[104:107],a[176:179],v[8:11],a[104:107]
EX v123,v123
MM a[108:111],a[184:187],v[8:11],a[108:111]
RD a[232:235],v226 offset:8192
CV v87,v118,v119
MM a[108:111],a[188:191],v[12:15],a[108:111]
EX v124,v124
MM a[100:103],a[188:191],v[4:7],a[100:103]
RD a[236:239],v227 offset:8192
buffer_load_dwordx4 v235, s[12:15], s64 offen offset:1024 lds
EX v125,v125
MM a[100:103],a[184:187],v[0:3],a[100:103]
EX v126,v126
v_smfmac_f32_16x16x64_bf16 v[236:239], v[144:147], v[64:71], v234
RD a[240:243],v224 offset:12288
RD a[244:247],v225 offset:12288
RD a[248:251],v226 offset:12288
RD a[252:255],v227 offset:12288
EX v127,v127
v_smfmac_f32_16x16x64_bf16 v[236:239], v[148:151], v[72:79], v234
CV v92,v120,v121
v_smfmac_f32_16x16x64_bf16 v[236:239], v[152:155], v[80:87], v234
buffer_load_dwordx4 v235, s[12:15], s81 offen offset:2048 lds
CV v93,v122,v123
CV v94,v124,v125
CV v95,v126,v127
s_nop 1
v_smfmac_f32_16x16x64_bf16 v[236:239], v[156:159], v[88:95], v234
s_waitcnt lgkmcnt(0)
buffer_load_dwordx4 v235, s[12:15], s64 offen offset:3072 lds
MM v[0:3],a[192:195],v[160:163],v[128:131]
s_add_u32 s64, s64, 16384
TR a[128:129],v232 offset:49152
s_add_u32 s81, s64, 896
MM v[0:3],a[196:199],v[176:179],v[0:3]
TR a[130:131],v232 offset:53248
TR a[132:133],v232 offset:57344
MM v[0:3],a[200:203],v[192:195],v[0:3]
TR a[134:135],v232 offset:61440
TR a[136:137],v233 offset:49152
MM v[0:3],a[204:207],v[208:211],v[0:3]
TR a[138:139],v233 offset:53248
v_add_f32 v247, v236, v237
MM v[4:7],a[220:223],v[208:211],v[128:131]
TR a[140:141],v233 offset:57344
v_cmp_nge_f32 vcc, s71, v247
MM v[4:7],a[216:219],v[192:195],v[4:7]
s_cbranch_vccnz LSITE3_%=
LBACK3_%=:
TR a[142:143],v233 offset:61440
TR a[144:145],v232 offset:49664
MM v[4:7],a[212:215],v[176:179],v[4:7]
v_lshrrev_b32 v252, 6, v248
v_mul_u32_u24 v252, 3456, v252
MM v[4:7],a[208:211],v[160:163],v[4:7]
v_and_b32 v251, 48, v248
v_add_u32 v252, v252, v251
MM v[12:15],a[208:211],v[164:167],v[132:135]
s_and_b32 s90, %[swave], 1
EX v0,v0
MM v[12:15],a[212:215],v[180:183],v[12:15]
s_mul_i32 s90, s90, 276480
EX v1,v1
MM v[12:15],a[216:219],v[196:199],v[12:15]
s_bfe_u32 s91, s48, 131077
TR a[146:147],v232 offset:53760
CV v0,v0,v1
MM v[12:15],a[220:223],v[212:215],v[12:15]
s_mul_i32 s91, s91, 552960
EX v2,v2
MM v[8:11],a[204:207],v[212:215],v[132:135]
s_add_u32 s90, s90, s91
EX v3,v3
MM v[8:11],a[200:203],v[196:199],v[8:11]
s_and_b32 s91, s48, 0xffffff80
TR a[148:149],v232 offset:57856
CV v1,v2,v3
MM v[8:11],a[196:199],v[180:183],v[8:11]
s_add_u32 s90, s90, s91
EX v4,v4
MM v[8:11],a[192:195],v[164:167],v[8:11]
s_and_b32 s91, s48, 16
TR a[150:151],v232 offset:61952
TR a[152:153],v233 offset:49664
MM v[16:19],a[192:195],v[168:171],v[136:139]
s_lshl_b32 s91, s91, 2
EX v5,v5
MM v[16:19],a[196:199],v[184:187],v[16:19]
s_add_u32 s66, s90, s91
TR a[154:155],v233 offset:53760
CV v2,v4,v5
MM v[16:19],a[200:203],v[200:203],v[16:19]
s_mul_i32 s91, %[swave], 5120
EX v6,v6
MM v[16:19],a[204:207],v[216:219],v[16:19]
s_add_u32 s91, s91, %[slds]
EX v7,v7
MM v[20:23],a[220:223],v[216:219],v[136:139]
s_add_u32 s67, s91, 131072
TR a[156:157],v233 offset:57856
CV v3,v6,v7
MM v[20:23],a[216:219],v[200:203],v[20:23]
s_add_u32 s85, s66, 221184
EX v8,v8
MM v[20:23],a[212:215],v[184:187],v[20:23]
s_add_u32 s86, s67, 4096
TR a[158:159],v233 offset:61952
TR a[160:161],v232 offset:50176
MM v[20:23],a[208:211],v[168:171],v[20:23]
EX v9,v9
MM v[28:31],a[208:211],v[172:175],v[140:143]
TR a[162:163],v232 offset:54272
CV v8,v8,v9
MM v[28:31],a[212:215],v[188:191],v[28:31]
EX v10,v10
MM v[28:31],a[216:219],v[204:207],v[28:31]
EX v11,v11
MM v[28:31],a[220:223],v[220:223],v[28:31]
)ASM"
R"ASM(
TR a[164:165],v232 offset:58368
CV v9,v10,v11
LPG6_%=:
s_cbranch_execz LPG10_%=
MM v[24:27],a[204:207],v[220:223],v[140:143]
EX v12,v12
MM v[24:27],a[200:203],v[204:207],v[24:27]
TR a[166:167],v232 offset:62464
TR a[168:169],v233 offset:50176
MM v[24:27],a[196:199],v[188:191],v[24:27]
EX v13,v13
MM v[24:27],a[192:195],v[172:175],v[24:27]
TR a[170:171],v233 offset:54272
CV v10,v12,v13
MM v[32:35],a[224:227],v[160:163],v[128:131]
EX v14,v14
MM v[32:35],a[228:231],v[176:179],v[32:35]
EX v15,v15
MM v[32:35],a[232:235],v[192:195],v[32:35]
TR a[172:173],v233 offset:58368
CV v11,v14,v15
MM v[32:35],a[236:239],v[208:211],v[32:35]
TR a[174:175],v233 offset:62464
TR a[176:177],v232 offset:50688
MM v[36:39],a[252:255],v[208:211],v[128:131]
EX v16,v16
MM v[36:39],a[248:251],v[192:195],v[36:39]
EX v17,v17
MM v[36:39],a[244:247],v[176:179],v[36:39]
TR a[178:179],v232 offset:54784
CV v16,v16,v17
MM v[36:39],a[240:243],v[160:163],v[36:39]
EX v18,v18
MM v[44:47],a[240:243],v[164:167],v[132:135]
TR a[180:181],v232 offset:58880
TR a[182:183],v232 offset:62976
MM v[44:47],a[244:247],v[180:183],v[44:47]
EX v19,v19
MM v[44:47],a[248:251],v[196:199],v[44:47]
TR a[184:185],v233 offset:50688
CV v17,v18,v19
MM v[44:47],a[252:255],v[212:215],v[44:47]
EX v20,v20
MM v[40:43],a[236:239],v[212:215],v[132:135]
EX v21,v21
MM v[40:43],a[232:235],v[196:199],v[40:43]
TR a[186:187],v233 offset:54784
CV v18,v20,v21
MM v[40:43],a[228:231],v[180:183],v[40:43]
EX v22,v22
MM v[40:43],a[224:227],v[164:167],v[40:43]
TR a[188:189],v233 offset:58880
TR a[190:191],v233 offset:62976
MM v[48:51],a[224:227],v[168:171],v[136:139]
EX v23,v23
MM v[48:51],a[228:231],v[184:187],v[48:51]
CV v19,v22,v23
MM v[48:51],a[232:235],v[200:203],v[48:51]
EX v24,v24
MM v[48:51],a[236:239],v[216:219],v[48:51]
EX v25,v25
MM v[52:55],a[252:255],v[216:219],v[136:139]
EX v26,v26
MM v[52:55],a[248:251],v[200:203],v[52:55]
EX v27,v27
MM v[52:55],a[244:247],v[184:187],v[52:55]
EX v28,v28
MM v[52:55],a[240:243],v[168:171],v[52:55]
EX v29,v29
MM v[60:63],a[240:243],v[172:175],v[140:143]
EX v30,v30
MM v[60:63],a[244:247],v[188:191],v[60:63]
EX v31,v31
MM v[60:63],a[248:251],v[204:207],v[60:63]
EX v32,v32
MM v[60:63],a[252:255],v[220:223],v[60:63]
EX v33,v33
MM v[56:59],a[236:239],v[220:223],v[140:143]
EX v34,v34
MM v[56:59],a[232:235],v[204:207],v[56:59]
EX v35,v35
MM v[56:59],a[228:231],v[188:191],v[56:59]
EX v36,v36
MM v[56:59],a[224:227],v[172:175],v[56:59]
s_waitcnt vmcnt(8) lgkmcnt(0)
s_barrier
MM a[0:3],a[128:131],v[64:67],a[0:3]
s_add_u32 m0, s65, 49152
CV v24,v24,v25
CV v25,v26,v27
MM a[0:3],a[132:135],v[68:71],a[0:3]
RD a[192:195],v224 offset:16384
CV v26,v28,v29
MM a[8:11],a[132:135],v[76:79],a[8:11]
RD a[196:199],v225 offset:16384
CV v27,v30,v31
MM a[8:11],a[128:131],v[72:75],a[8:11]
buffer_load_dwordx4 v228, s[8:11], s64 offen offset:0 lds
MM a[16:19],a[128:131],v[80:83],0
RD a[200:203],v226 offset:16384
CV v4,v32,v33
MM a[16:19],a[132:135],v[84:87],a[16:19]
RD a[204:207],v227 offset:16384
CV v5,v34,v35
MM a[24:27],a[132:135],v[92:95],0
buffer_load_dwordx4 v229, s[8:11], s64 offen offset:1024 lds
MM a[24:27],a[128:131],v[88:91],a[24:27]
RD a[208:211],v224 offset:20480
RD a[212:215],v225 offset:20480
MM a[28:31],a[136:139],v[88:91],0
buffer_load_dwordx4 v230, s[8:11], s64 offen offset:2048 lds
MM a[28:31],a[140:143],v[92:95],a[28:31]
RD a[216:219],v226 offset:20480
RD a[220:223],v227 offset:20480
MM a[20:23],a[140:143],v[84:87],0
EX v37,v37
MM a[20:23],a[136:139],v[80:83],a[20:23]
RD a[224:227],v224 offset:24576
CV v6,v36,v37
MM a[12:15],a[136:139],v[72:75],a[12:15]
EX v38,v38
MM a[12:15],a[140:143],v[76:79],a[12:15]
buffer_load_dwordx4 v231, s[8:11], s64 offen offset:3072 lds
MM a[4:7],a[140:143],v[68:71],a[4:7]
s_add_u32 m0, s65, 114688
EX v39,v39
MM a[4:7],a[136:139],v[64:67],a[4:7]
RD a[228:231],v225 offset:24576
CV v7,v38,v39
MM a[32:35],a[144:147],v[64:67],a[32:35]
EX v40,v40
MM a[32:35],a[148:151],v[68:71],a[32:35]
EX v41,v41
MM a[40:43],a[148:151],v[76:79],a[40:43]
RD a[232:235],v226 offset:24576
CV v12,v40,v41
MM a[40:43],a[144:147],v[72:75],a[40:43]
EX v42,v42
MM a[48:51],a[144:147],v[80:83],0
buffer_load_dwordx4 v235, s[12:15], s81 offen offset:0 lds
MM a[48:51],a[148:151],v[84:87],a[48:51]
EX v43,v43
MM a[56:59],a[148:151],v[92:95],0
RD a[236:239],v227 offset:24576
CV v13,v42,v43
MM a[56:59],a[144:147],v[88:91],a[56:59]
EX v44,v44
MM a[60:63],a[152:155],v[88:91],0
EX v45,v45
MM a[60:63],a[156:159],v[92:95],a[60:63]
RD a[240:243],v224 offset:28672
CV v14,v44,v45
MM a[52:55],a[156:159],v[84:87],0
EX v46,v46
MM a[52:55],a[152:155],v[80:83],a[52:55]
buffer_load_dwordx4 v235, s[12:15], s64 offen offset:1024 lds
MM a[44:47],a[152:155],v[72:75],a[44:47]
EX v47,v47
MM a[44:47],a[156:159],v[76:79],a[44:47]
RD a[244:247],v225 offset:28672
CV v15,v46,v47
MM a[36:39],a[156:159],v[68:71],a[36:39]
EX v48,v48
MM a[36:39],a[152:155],v[64:67],a[36:39]
EX v49,v49
MM a[64:67],a[160:163],v[64:67],a[64:67]
RD a[248:251],v226 offset:28672
CV v20,v48,v49
MM a[64:67],a[164:167],v[68:71],a[64:67]
EX v50,v50
MM a[72:75],a[164:167],v[76:79],a[72:75]
buffer_load_dwordx4 v235, s[12:15], s81 offen offset:2048 lds
MM a[72:75],a[160:163],v[72:75],a[72:75]
EX v51,v51
MM a[80:83],a[160:163],v[80:83],0
RD a[252:255],v227 offset:28672
CV v21,v50,v51
MM a[80:83],a[164:167],v[84:87],a[80:83]
EX v52,v52
MM a[88:91],a[164:167],v[92:95],0
EX v53,v53
MM a[88:91],a[160:163],v[88:91],a[88:91]
buffer_load_dwordx4 v235, s[12:15], s64 offen offset:3072 lds
MM a[92:95],a[168:171],v[88:91],0
EX v54,v54
MM a[92:95],a[172:175],v[92:95],a[92:95]
EX v55,v55
MM a[84:87],a[172:175],v[84:87],0
EX v56,v56
MM a[84:87],a[168:171],v[80:83],a[84:87]
EX v57,v57
MM a[76:79],a[168:171],v[72:75],a[76:79]
EX v58,v58
MM a[76:79],a[172:175],v[76:79],a[76:79]
EX v59,v59
MM a[68:71],a[172:175],v[68:71],a[68:71]
EX v60,v60
MM a[68:71],a[168:171],v[64:67],a[68:71]
EX v61,v61
MM a[96:99],a[176:179],v[64:67],a[96:99]
EX v62,v62
MM a[96:99],a[180:183],v[68:71],a[96:99]
EX v63,v63
MM a[104:107],a[180:183],v[76:79],a[104:107]
CV v22,v52,v53
CV v23,v54,v55
MM a[104:107],a[176:179],v[72:75],a[104:107]
CV v28,v56,v57
CV v29,v58,v59
MM a[112:115],a[176:179],v[80:83],0
CV v30,v60,v61
CV v31,v62,v63
MM a[112:115],a[180:183],v[84:87],a[112:115]
MM a[120:123],a[180:183],v[92:95],0
MM a[120:123],a[176:179],v[88:91],a[120:123]
MM a[124:127],a[184:187],v[88:91],0
MM a[124:127],a[188:191],v[92:95],a[124:127]
MM a[116:119],a[188:191],v[84:87],0
MM a[116:119],a[184:187],v[80:83],a[116:119]
MM a[108:111],a[184:187],v[72:75],a[108:111]
MM a[108:111],a[188:191],v[76:79],a[108:111]
MM a[100:103],a[188:191],v[68:71],a[100:103]
MM a[100:103],a[184:187],v[64:67],a[100:103]
v_smfmac_f32_16x16x64_bf16 v[236:239], v[144:147], v[0:7], v234
v_smfmac_f32_16x16x64_bf16 v[236:239], v[148:151], v[8:15], v234
v_smfmac_f32_16x16x64_bf16 v[236:239], v[152:155], v[16:23], v234
v_smfmac_f32_16x16x64_bf16 v[236:239], v[156:159], v[24:31], v234
s_waitcnt lgkmcnt(0)
s_cmp_lg_u32 %[sb], 0
s_cbranch_scc1 LXS_%=
s_getpc_b64 s[98:99]
LTA1_%=:
s_lshl_b32 s73, %[swave], 12
s_add_u32 s73, s73, LPG26_%=-LTA1_%=
s_ashr_i32 s74, s73, 31
s_add_u32 s98, s98, s73
s_addc_u32 s99, s99, s74
s_mov_b64 exec, 0
s_setpc_b64 s[98:99]
LTR1_%=:
s_mov_b64 exec, -1
s_branch LXP_%=
LXS_%=:
s_mov_b32 s70, 2
LLA_%=:
MM v[64:67],a[192:195],v[160:163],v[128:131]
s_add_u32 s64, s64, 16384
TR a[128:129],v232 offset:0
s_add_u32 s81, s64, 896
MM v[64:67],a[196:199],v[176:179],v[64:67]
TR a[130:131],v232 offset:4096
TR a[132:133],v232 offset:8192
MM v[64:67],a[200:203],v[192:195],v[64:67]
TR a[134:135],v232 offset:12288
TR a[136:137],v233 offset:0
MM v[64:67],a[204:207],v[208:211],v[64:67]
TR a[138:139],v233 offset:4096
v_add_f32 v246, v236, v237
MM v[68:71],a[220:223],v[208:211],v[128:131]
TR a[140:141],v233 offset:8192
v_cmp_nge_f32 vcc, s71, v246
MM v[68:71],a[216:219],v[192:195],v[68:71]
s_cbranch_vccnz LSITE8_%=
LBACK8_%=:
TR a[142:143],v233 offset:12288
TR a[144:145],v232 offset:512
MM v[68:71],a[212:215],v[176:179],v[68:71]
s_mov_b32 m0, s67
TR a[146:147],v232 offset:4608
TR a[148:149],v232 offset:8704
MM v[68:71],a[208:211],v[160:163],v[68:71]
buffer_load_dwordx4 v252, s[36:39], s66 offen offset:0 lds
MM v[76:79],a[208:211],v[164:167],v[132:135]
s_add_u32 s66, s66, 55296
EX v64,v64
MM v[76:79],a[212:215],v[180:183],v[76:79]
s_min_u32 s66, s66, s85
EX v65,v65
MM v[76:79],a[216:219],v[196:199],v[76:79]
s_add_u32 s67, s67, 1024
TR a[150:151],v232 offset:12800
CV v64,v64,v65
MM v[76:79],a[220:223],v[212:215],v[76:79]
s_min_u32 s67, s67, s86
EX v66,v66
MM v[72:75],a[204:207],v[212:215],v[132:135]
EX v67,v67
MM v[72:75],a[200:203],v[196:199],v[72:75]
TR a[152:153],v233 offset:512
CV v65,v66,v67
MM v[72:75],a[196:199],v[180:183],v[72:75]
EX v68,v68
MM v[72:75],a[192:195],v[164:167],v[72:75]
EX v69,v69
MM v[80:83],a[192:195],v[168:171],v[136:139]
TR a[154:155],v233 offset:4608
CV v66,v68,v69
MM v[80:83],a[196:199],v[184:187],v[80:83]
EX v70,v70
MM v[80:83],a[200:203],v[200:203],v[80:83]
EX v71,v71
MM v[80:83],a[204:207],v[216:219],v[80:83]
TR a[156:157],v233 offset:8704
CV v67,v70,v71
MM v[84:87],a[220:223],v[216:219],v[136:139]
EX v72,v72
MM v[84:87],a[216:219],v[200:203],v[84:87]
TR a[158:159],v233 offset:12800
TR a[160:161],v232 offset:1024
MM v[84:87],a[212:215],v[184:187],v[84:87]
EX v73,v73
MM v[84:87],a[208:211],v[168:171],v[84:87]
TR a[162:163],v232 offset:5120
CV v72,v72,v73
MM v[92:95],a[208:211],v[172:175],v[140:143]
EX v74,v74
MM v[92:95],a[212:215],v[188:191],v[92:95]
TR a[164:165],v232 offset:9216
TR a[166:167],v232 offset:13312
MM v[92:95],a[216:219],v[204:207],v[92:95]
EX v75,v75
MM v[92:95],a[220:223],v[220:223],v[92:95]
TR a[168:169],v233 offset:1024
CV v73,v74,v75
MM v[88:91],a[204:207],v[220:223],v[140:143]
EX v76,v76
MM v[88:91],a[200:203],v[204:207],v[88:91]
EX v77,v77
MM v[88:91],a[196:199],v[188:191],v[88:91]
TR a[170:171],v233 offset:5120
CV v74,v76,v77
MM v[88:91],a[192:195],v[172:175],v[88:91]
EX v78,v78
MM v[96:99],a[224:227],v[160:163],v[128:131]
TR a[172:173],v233 offset:9216
TR a[174:175],v233 offset:13312
MM v[96:99],a[228:231],v[176:179],v[96:99]
EX v79,v79
MM v[96:99],a[232:235],v[192:195],v[96:99]
TR a[176:177],v232 offset:1536
CV v75,v78,v79
MM v[96:99],a[236:239],v[208:211],v[96:99]
EX v80,v80
MM v[100:103],a[252:255],v[208:211],v[128:131]
EX v81,v81
MM v[100:103],a[248:251],v[192:195],v[100:103]
TR a[178:179],v232 offset:5632
CV v80,v80,v81
MM v[100:103],a[244:247],v[176:179],v[100:103]
EX v82,v82
MM v[100:103],a[240:243],v[160:163],v[100:103]
TR a[180:181],v232 offset:9728
TR a[182:183],v232 offset:13824
MM v[108:111],a[240:243],v[164:167],v[132:135]
EX v83,v83
MM v[108:111],a[244:247],v[180:183],v[108:111]
TR a[184:185],v233 offset:1536
CV v81,v82,v83
MM v[108:111],a[248:251],v[196:199],v[108:111]
EX v84,v84
MM v[108:111],a[252:255],v[212:215],v[108:111]
EX v85,v85
MM v[104:107],a[236:239],v[212:215],v[132:135]
TR a[186:187],v233 offset:5632
CV v82,v84,v85
MM v[104:107],a[232:235],v[196:199],v[104:107]
EX v86,v86
MM v[104:107],a[228:231],v[180:183],v[104:107]
TR a[188:189],v233 offset:9728
TR a[190:191],v233 offset:13824
MM v[104:107],a[224:227],v[164:167],v[104:107]
EX v87,v87
MM v[112:115],a[224:227],v[168:171],v[136:139]
CV v83,v86,v87
MM v[112:115],a[228:231],v[184:187],v[112:115]
EX v88,v88
MM v[112:115],a[232:235],v[200:203],v[112:115]
EX v89,v89
MM v[112:115],a[236:239],v[216:219],v[112:115]
EX v90,v90
MM v[116:119],a[252:255],v[216:219],v[136:139]
EX v91,v91
MM v[116:119],a[248:251],v[200:203],v[116:119]
EX v92,v92
MM v[116:119],a[244:247],v[184:187],v[116:119]
EX v93,v93
MM v[116:119],a[240:243],v[168:171],v[116:119]
EX v94,v94
MM v[124:127],a[240:243],v[172:175],v[140:143]
EX v95,v95
MM v[124:127],a[244:247],v[188:191],v[124:127]
EX v96,v96
MM v[124:127],a[248:251],v[204:207],v[124:127]
EX v97,v97
MM v[124:127],a[252:255],v[220:223],v[124:127]
EX v98,v98
MM v[120:123],a[236:239],v[220:223],v[140:143]
EX v99,v99
MM v[120:123],a[232:235],v[204:207],v[120:123]
EX v100,v100
MM v[120:123],a[228:231],v[188:191],v[120:123]
EX v101,v101
)ASM"
R"ASM(
MM v[120:123],a[224:227],v[172:175],v[120:123]
s_waitcnt vmcnt(13) lgkmcnt(0)
s_barrier
MM a[0:3],a[128:131],v[0:3],a[0:3]
s_add_u32 m0, s65, 0
CV v88,v88,v89
CV v89,v90,v91
MM a[0:3],a[132:135],v[4:7],a[0:3]
RD a[192:195],v224 offset:32768
CV v90,v92,v93
MM a[8:11],a[132:135],v[12:15],a[8:11]
RD a[196:199],v225 offset:32768
CV v91,v94,v95
MM a[8:11],a[128:131],v[8:11],a[8:11]
buffer_load_dwordx4 v228, s[8:11], s64 offen offset:0 lds
MM a[16:19],a[128:131],v[16:19],a[16:19]
RD a[200:203],v226 offset:32768
CV v68,v96,v97
MM a[16:19],a[132:135],v[20:23],a[16:19]
RD a[204:207],v227 offset:32768
CV v69,v98,v99
MM a[24:27],a[132:135],v[28:31],a[24:27]
buffer_load_dwordx4 v229, s[8:11], s64 offen offset:1024 lds
MM a[24:27],a[128:131],v[24:27],a[24:27]
RD a[208:211],v224 offset:36864
CV v70,v100,v101
MM a[28:31],a[136:139],v[24:27],a[28:31]
RD a[212:215],v225 offset:36864
RD a[216:219],v226 offset:36864
MM a[28:31],a[140:143],v[28:31],a[28:31]
buffer_load_dwordx4 v230, s[8:11], s64 offen offset:2048 lds
MM a[20:23],a[140:143],v[20:23],a[20:23]
RD a[220:223],v227 offset:36864
RD a[224:227],v224 offset:40960
MM a[20:23],a[136:139],v[16:19],a[20:23]
EX v102,v102
MM a[12:15],a[136:139],v[8:11],a[12:15]
buffer_load_dwordx4 v231, s[8:11], s64 offen offset:3072 lds
MM a[12:15],a[140:143],v[12:15],a[12:15]
s_add_u32 m0, s65, 65536
EX v103,v103
MM a[4:7],a[140:143],v[4:7],a[4:7]
RD a[228:231],v225 offset:40960
CV v71,v102,v103
MM a[4:7],a[136:139],v[0:3],a[4:7]
EX v104,v104
MM a[32:35],a[144:147],v[0:3],a[32:35]
EX v105,v105
MM a[32:35],a[148:151],v[4:7],a[32:35]
RD a[232:235],v226 offset:40960
CV v76,v104,v105
MM a[40:43],a[148:151],v[12:15],a[40:43]
EX v106,v106
MM a[40:43],a[144:147],v[8:11],a[40:43]
buffer_load_dwordx4 v235, s[12:15], s81 offen offset:0 lds
MM a[48:51],a[144:147],v[16:19],a[48:51]
EX v107,v107
MM a[48:51],a[148:151],v[20:23],a[48:51]
RD a[236:239],v227 offset:40960
CV v77,v106,v107
MM a[56:59],a[148:151],v[28:31],a[56:59]
EX v108,v108
MM a[56:59],a[144:147],v[24:27],a[56:59]
EX v109,v109
MM a[60:63],a[152:155],v[24:27],a[60:63]
RD a[240:243],v224 offset:45056
CV v78,v108,v109
MM a[60:63],a[156:159],v[28:31],a[60:63]
buffer_load_dwordx4 v235, s[12:15], s64 offen offset:1024 lds
MM a[52:55],a[156:159],v[20:23],a[52:55]
EX v110,v110
MM a[52:55],a[152:155],v[16:19],a[52:55]
EX v111,v111
MM a[44:47],a[152:155],v[8:11],a[44:47]
RD a[244:247],v225 offset:45056
CV v79,v110,v111
MM a[44:47],a[156:159],v[12:15],a[44:47]
EX v112,v112
MM a[36:39],a[156:159],v[4:7],a[36:39]
EX v113,v113
MM a[36:39],a[152:155],v[0:3],a[36:39]
RD a[248:251],v226 offset:45056
CV v84,v112,v113
MM a[64:67],a[160:163],v[0:3],a[64:67]
buffer_load_dwordx4 v235, s[12:15], s81 offen offset:2048 lds
MM a[64:67],a[164:167],v[4:7],a[64:67]
EX v114,v114
MM a[72:75],a[164:167],v[12:15],a[72:75]
EX v115,v115
MM a[72:75],a[160:163],v[8:11],a[72:75]
RD a[252:255],v227 offset:45056
CV v85,v114,v115
MM a[80:83],a[160:163],v[16:19],a[80:83]
EX v116,v116
MM a[80:83],a[164:167],v[20:23],a[80:83]
buffer_load_dwordx4 v235, s[12:15], s64 offen offset:3072 lds
MM a[88:91],a[164:167],v[28:31],a[88:91]
EX v117,v117
MM a[88:91],a[160:163],v[24:27],a[88:91]
EX v118,v118
MM a[92:95],a[168:171],v[24:27],a[92:95]
EX v119,v119
MM a[92:95],a[172:175],v[28:31],a[92:95]
EX v120,v120
MM a[84:87],a[172:175],v[20:23],a[84:87]
EX v121,v121
MM a[84:87],a[168:171],v[16:19],a[84:87]
EX v122,v122
MM a[76:79],a[168:171],v[8:11],a[76:79]
EX v123,v123
MM a[76:79],a[172:175],v[12:15],a[76:79]
EX v124,v124
MM a[68:71],a[172:175],v[4:7],a[68:71]
EX v125,v125
MM a[68:71],a[168:171],v[0:3],a[68:71]
EX v126,v126
MM a[96:99],a[176:179],v[0:3],a[96:99]
EX v127,v127
MM a[96:99],a[180:183],v[4:7],a[96:99]
CV v86,v116,v117
CV v87,v118,v119
MM a[104:107],a[180:183],v[12:15],a[104:107]
CV v92,v120,v121
CV v93,v122,v123
MM a[104:107],a[176:179],v[8:11],a[104:107]
CV v94,v124,v125
CV v95,v126,v127
MM a[112:115],a[176:179],v[16:19],a[112:115]
MM a[112:115],a[180:183],v[20:23],a[112:115]
MM a[120:123],a[180:183],v[28:31],a[120:123]
MM a[120:123],a[176:179],v[24:27],a[120:123]
MM a[124:127],a[184:187],v[24:27],a[124:127]
MM a[124:127],a[188:191],v[28:31],a[124:127]
MM a[116:119],a[188:191],v[20:23],a[116:119]
MM a[116:119],a[184:187],v[16:19],a[116:119]
MM a[108:111],a[184:187],v[8:11],a[108:111]
MM a[108:111],a[188:191],v[12:15],a[108:111]
MM a[100:103],a[188:191],v[4:7],a[100:103]
MM a[100:103],a[184:187],v[0:3],a[100:103]
v_smfmac_f32_16x16x64_bf16 v[236:239], v[144:147], v[64:71], v234
v_smfmac_f32_16x16x64_bf16 v[236:239], v[148:151], v[72:79], v234
v_smfmac_f32_16x16x64_bf16 v[236:239], v[152:155], v[80:87], v234
v_smfmac_f32_16x16x64_bf16 v[236:239], v[156:159], v[88:95], v234
s_waitcnt lgkmcnt(0)
MM v[0:3],a[192:195],v[160:163],v[128:131]
s_add_u32 s64, s64, 16384
TR a[128:129],v232 offset:16384
s_add_u32 s81, s64, 896
MM v[0:3],a[196:199],v[176:179],v[0:3]
TR a[130:131],v232 offset:20480
TR a[132:133],v232 offset:24576
MM v[0:3],a[200:203],v[192:195],v[0:3]
TR a[134:135],v232 offset:28672
TR a[136:137],v233 offset:16384
MM v[0:3],a[204:207],v[208:211],v[0:3]
TR a[138:139],v233 offset:20480
v_add_f32 v247, v236, v237
MM v[4:7],a[220:223],v[208:211],v[128:131]
TR a[140:141],v233 offset:24576
v_cmp_nge_f32 vcc, s71, v247
MM v[4:7],a[216:219],v[192:195],v[4:7]
s_cbranch_vccnz LSITE9_%=
LBACK9_%=:
TR a[142:143],v233 offset:28672
TR a[144:145],v232 offset:16896
MM v[4:7],a[212:215],v[176:179],v[4:7]
s_mov_b32 m0, s67
TR a[146:147],v232 offset:20992
TR a[148:149],v232 offset:25088
MM v[4:7],a[208:211],v[160:163],v[4:7]
buffer_load_dwordx4 v252, s[36:39], s66 offen offset:0 lds
MM v[12:15],a[208:211],v[164:167],v[132:135]
s_add_u32 s66, s66, 55296
EX v0,v0
MM v[12:15],a[212:215],v[180:183],v[12:15]
s_min_u32 s66, s66, s85
EX v1,v1
MM v[12:15],a[216:219],v[196:199],v[12:15]
s_add_u32 s67, s67, 1024
TR a[150:151],v232 offset:29184
CV v0,v0,v1
MM v[12:15],a[220:223],v[212:215],v[12:15]
s_min_u32 s67, s67, s86
EX v2,v2
MM v[8:11],a[204:207],v[212:215],v[132:135]
EX v3,v3
MM v[8:11],a[200:203],v[196:199],v[8:11]
TR a[152:153],v233 offset:16896
CV v1,v2,v3
MM v[8:11],a[196:199],v[180:183],v[8:11]
EX v4,v4
MM v[8:11],a[192:195],v[164:167],v[8:11]
EX v5,v5
MM v[16:19],a[192:195],v[168:171],v[136:139]
TR a[154:155],v233 offset:20992
CV v2,v4,v5
LPG7_%=:
s_cbranch_execz LPG11_%=
MM v[16:19],a[196:199],v[184:187],v[16:19]
EX v6,v6
MM v[16:19],a[200:203],v[200:203],v[16:19]
EX v7,v7
MM v[16:19],a[204:207],v[216:219],v[16:19]
TR a[156:157],v233 offset:25088
CV v3,v6,v7
MM v[20:23],a[220:223],v[216:219],v[136:139]
EX v8,v8
MM v[20:23],a[216:219],v[200:203],v[20:23]
TR a[158:159],v233 offset:29184
TR a[160:161],v232 offset:17408
MM v[20:23],a[212:215],v[184:187],v[20:23]
EX v9,v9
MM v[20:23],a[208:211],v[168:171],v[20:23]
TR a[162:163],v232 offset:21504
CV v8,v8,v9
MM v[28:31],a[208:211],v[172:175],v[140:143]
EX v10,v10
MM v[28:31],a[212:215],v[188:191],v[28:31]
TR a[164:165],v232 offset:25600
TR a[166:167],v232 offset:29696
MM v[28:31],a[216:219],v[204:207],v[28:31]
EX v11,v11
MM v[28:31],a[220:223],v[220:223],v[28:31]
TR a[168:169],v233 offset:17408
CV v9,v10,v11
MM v[24:27],a[204:207],v[220:223],v[140:143]
EX v12,v12
MM v[24:27],a[200:203],v[204:207],v[24:27]
EX v13,v13
MM v[24:27],a[196:199],v[188:191],v[24:27]
TR a[170:171],v233 offset:21504
CV v10,v12,v13
MM v[24:27],a[192:195],v[172:175],v[24:27]
EX v14,v14
MM v[32:35],a[224:227],v[160:163],v[128:131]
TR a[172:173],v233 offset:25600
TR a[174:175],v233 offset:29696
MM v[32:35],a[228:231],v[176:179],v[32:35]
EX v15,v15
MM v[32:35],a[232:235],v[192:195],v[32:35]
TR a[176:177],v232 offset:17920
CV v11,v14,v15
MM v[32:35],a[236:239],v[208:211],v[32:35]
EX v16,v16
MM v[36:39],a[252:255],v[208:211],v[128:131]
EX v17,v17
MM v[36:39],a[248:251],v[192:195],v[36:39]
TR a[178:179],v232 offset:22016
CV v16,v16,v17
MM v[36:39],a[244:247],v[176:179],v[36:39]
EX v18,v18
MM v[36:39],a[240:243],v[160:163],v[36:39]
TR a[180:181],v232 offset:26112
TR a[182:183],v232 offset:30208
MM v[44:47],a[240:243],v[164:167],v[132:135]
EX v19,v19
MM v[44:47],a[244:247],v[180:183],v[44:47]
TR a[184:185],v233 offset:17920
CV v17,v18,v19
MM v[44:47],a[248:251],v[196:199],v[44:47]
EX v20,v20
MM v[44:47],a[252:255],v[212:215],v[44:47]
EX v21,v21
MM v[40:43],a[236:239],v[212:215],v[132:135]
TR a[186:187],v233 offset:22016
CV v18,v20,v21
MM v[40:43],a[232:235],v[196:199],v[40:43]
EX v22,v22
MM v[40:43],a[228:231],v[180:183],v[40:43]
TR a[188:189],v233 offset:26112
TR a[190:191],v233 offset:30208
MM v[40:43],a[224:227],v[164:167],v[40:43]
EX v23,v23
MM v[48:51],a[224:227],v[168:171],v[136:139]
CV v19,v22,v23
MM v[48:51],a[228:231],v[184:187],v[48:51]
EX v24,v24
MM v[48:51],a[232:235],v[200:203],v[48:51]
EX v25,v25
MM v[48:51],a[236:239],v[216:219],v[48:51]
EX v26,v26
MM v[52:55],a[252:255],v[216:219],v[136:139]
EX v27,v27
MM v[52:55],a[248:251],v[200:203],v[52:55]
EX v28,v28
MM v[52:55],a[244:247],v[184:187],v[52:55]
EX v29,v29
MM v[52:55],a[240:243],v[168:171],v[52:55]
EX v30,v30
MM v[60:63],a[240:243],v[172:175],v[140:143]
EX v31,v31
MM v[60:63],a[244:247],v[188:191],v[60:63]
EX v32,v32
MM v[60:63],a[248:251],v[204:207],v[60:63]
EX v33,v33
MM v[60:63],a[252:255],v[220:223],v[60:63]
EX v34,v34
MM v[56:59],a[236:239],v[220:223],v[140:143]
EX v35,v35
MM v[56:59],a[232:235],v[204:207],v[56:59]
EX v36,v36
MM v[56:59],a[228:231],v[188:191],v[56:59]
EX v37,v37
MM v[56:59],a[224:227],v[172:175],v[56:59]
s_waitcnt vmcnt(13) lgkmcnt(0)
s_barrier
MM a[0:3],a[128:131],v[64:67],a[0:3]
s_add_u32 m0, s65, 16384
CV v24,v24,v25
CV v25,v26,v27
MM a[0:3],a[132:135],v[68:71],a[0:3]
RD a[192:195],v224 offset:49152
CV v26,v28,v29
MM a[8:11],a[132:135],v[76:79],a[8:11]
RD a[196:199],v225 offset:49152
CV v27,v30,v31
MM a[8:11],a[128:131],v[72:75],a[8:11]
buffer_load_dwordx4 v228, s[8:11], s64 offen offset:0 lds
MM a[16:19],a[128:131],v[80:83],a[16:19]
RD a[200:203],v226 offset:49152
CV v4,v32,v33
MM a[16:19],a[132:135],v[84:87],a[16:19]
RD a[204:207],v227 offset:49152
CV v5,v34,v35
MM a[24:27],a[132:135],v[92:95],a[24:27]
buffer_load_dwordx4 v229, s[8:11], s64 offen offset:1024 lds
MM a[24:27],a[128:131],v[88:91],a[24:27]
RD a[208:211],v224 offset:53248
CV v6,v36,v37
MM a[28:31],a[136:139],v[88:91],a[28:31]
RD a[212:215],v225 offset:53248
RD a[216:219],v226 offset:53248
MM a[28:31],a[140:143],v[92:95],a[28:31]
buffer_load_dwordx4 v230, s[8:11], s64 offen offset:2048 lds
MM a[20:23],a[140:143],v[84:87],a[20:23]
RD a[220:223],v227 offset:53248
RD a[224:227],v224 offset:57344
MM a[20:23],a[136:139],v[80:83],a[20:23]
EX v38,v38
MM a[12:15],a[136:139],v[72:75],a[12:15]
buffer_load_dwordx4 v231, s[8:11], s64 offen offset:3072 lds
MM a[12:15],a[140:143],v[76:79],a[12:15]
s_add_u32 m0, s65, 81920
EX v39,v39
MM a[4:7],a[140:143],v[68:71],a[4:7]
RD a[228:231],v225 offset:57344
CV v7,v38,v39
MM a[4:7],a[136:139],v[64:67],a[4:7]
EX v40,v40
MM a[32:35],a[144:147],v[64:67],a[32:35]
EX v41,v41
MM a[32:35],a[148:151],v[68:71],a[32:35]
RD a[232:235],v226 offset:57344
CV v12,v40,v41
MM a[40:43],a[148:151],v[76:79],a[40:43]
EX v42,v42
MM a[40:43],a[144:147],v[72:75],a[40:43]
buffer_load_dwordx4 v235, s[12:15], s81 offen offset:0 lds
MM a[48:51],a[144:147],v[80:83],a[48:51]
EX v43,v43
MM a[48:51],a[148:151],v[84:87],a[48:51]
RD a[236:239],v227 offset:57344
CV v13,v42,v43
MM a[56:59],a[148:151],v[92:95],a[56:59]
EX v44,v44
MM a[56:59],a[144:147],v[88:91],a[56:59]
EX v45,v45
MM a[60:63],a[152:155],v[88:91],a[60:63]
RD a[240:243],v224 offset:61440
CV v14,v44,v45
MM a[60:63],a[156:159],v[92:95],a[60:63]
buffer_load_dwordx4 v235, s[12:15], s64 offen offset:1024 lds
MM a[52:55],a[156:159],v[84:87],a[52:55]
EX v46,v46
MM a[52:55],a[152:155],v[80:83],a[52:55]
EX v47,v47
MM a[44:47],a[152:155],v[72:75],a[44:47]
RD a[244:247],v225 offset:61440
CV v15,v46,v47
MM a[44:47],a[156:159],v[76:79],a[44:47]
EX v48,v48
MM a[36:39],a[156:159],v[68:71],a[36:39]
EX v49,v49
MM a[36:39],a[152:155],v[64:67],a[36:39]
RD a[248:251],v226 offset:61440
CV v20,v48,v49
MM a[64:67],a[160:163],v[64:67],a[64:67]
buffer_load_dwordx4 v235, s[12:15], s81 offen offset:2048 lds
)ASM"
R"ASM(
MM a[64:67],a[164:167],v[68:71],a[64:67]
EX v50,v50
MM a[72:75],a[164:167],v[76:79],a[72:75]
EX v51,v51
MM a[72:75],a[160:163],v[72:75],a[72:75]
RD a[252:255],v227 offset:61440
CV v21,v50,v51
MM a[80:83],a[160:163],v[80:83],a[80:83]
EX v52,v52
MM a[80:83],a[164:167],v[84:87],a[80:83]
buffer_load_dwordx4 v235, s[12:15], s64 offen offset:3072 lds
MM a[88:91],a[164:167],v[92:95],a[88:91]
EX v53,v53
MM a[88:91],a[160:163],v[88:91],a[88:91]
EX v54,v54
MM a[92:95],a[168:171],v[88:91],a[92:95]
EX v55,v55
MM a[92:95],a[172:175],v[92:95],a[92:95]
EX v56,v56
MM a[84:87],a[172:175],v[84:87],a[84:87]
EX v57,v57
MM a[84:87],a[168:171],v[80:83],a[84:87]
EX v58,v58
MM a[76:79],a[168:171],v[72:75],a[76:79]
EX v59,v59
MM a[76:79],a[172:175],v[76:79],a[76:79]
EX v60,v60
MM a[68:71],a[172:175],v[68:71],a[68:71]
EX v61,v61
MM a[68:71],a[168:171],v[64:67],a[68:71]
EX v62,v62
MM a[96:99],a[176:179],v[64:67],a[96:99]
EX v63,v63
MM a[96:99],a[180:183],v[68:71],a[96:99]
CV v22,v52,v53
CV v23,v54,v55
MM a[104:107],a[180:183],v[76:79],a[104:107]
CV v28,v56,v57
CV v29,v58,v59
MM a[104:107],a[176:179],v[72:75],a[104:107]
CV v30,v60,v61
CV v31,v62,v63
MM a[112:115],a[176:179],v[80:83],a[112:115]
MM a[112:115],a[180:183],v[84:87],a[112:115]
MM a[120:123],a[180:183],v[92:95],a[120:123]
MM a[120:123],a[176:179],v[88:91],a[120:123]
MM a[124:127],a[184:187],v[88:91],a[124:127]
MM a[124:127],a[188:191],v[92:95],a[124:127]
MM a[116:119],a[188:191],v[84:87],a[116:119]
MM a[116:119],a[184:187],v[80:83],a[116:119]
MM a[108:111],a[184:187],v[72:75],a[108:111]
MM a[108:111],a[188:191],v[76:79],a[108:111]
MM a[100:103],a[188:191],v[68:71],a[100:103]
MM a[100:103],a[184:187],v[64:67],a[100:103]
v_smfmac_f32_16x16x64_bf16 v[236:239], v[144:147], v[0:7], v234
v_smfmac_f32_16x16x64_bf16 v[236:239], v[148:151], v[8:15], v234
v_smfmac_f32_16x16x64_bf16 v[236:239], v[152:155], v[16:23], v234
v_smfmac_f32_16x16x64_bf16 v[236:239], v[156:159], v[24:31], v234
s_waitcnt lgkmcnt(0)
MM v[64:67],a[192:195],v[160:163],v[128:131]
s_add_u32 s64, s64, 16384
TR a[128:129],v232 offset:32768
s_add_u32 s81, s64, 896
MM v[64:67],a[196:199],v[176:179],v[64:67]
TR a[130:131],v232 offset:36864
TR a[132:133],v232 offset:40960
MM v[64:67],a[200:203],v[192:195],v[64:67]
TR a[134:135],v232 offset:45056
TR a[136:137],v233 offset:32768
MM v[64:67],a[204:207],v[208:211],v[64:67]
TR a[138:139],v233 offset:36864
v_add_f32 v246, v236, v237
MM v[68:71],a[220:223],v[208:211],v[128:131]
TR a[140:141],v233 offset:40960
v_cmp_nge_f32 vcc, s71, v246
MM v[68:71],a[216:219],v[192:195],v[68:71]
s_cbranch_vccnz LSITE10_%=
LBACK10_%=:
TR a[142:143],v233 offset:45056
TR a[144:145],v232 offset:33280
MM v[68:71],a[212:215],v[176:179],v[68:71]
s_mov_b32 m0, s67
TR a[146:147],v232 offset:37376
TR a[148:149],v232 offset:41472
MM v[68:71],a[208:211],v[160:163],v[68:71]
buffer_load_dwordx4 v252, s[36:39], s66 offen offset:0 lds
MM v[76:79],a[208:211],v[164:167],v[132:135]
s_add_u32 s66, s66, 55296
EX v64,v64
MM v[76:79],a[212:215],v[180:183],v[76:79]
s_min_u32 s66, s66, s85
EX v65,v65
MM v[76:79],a[216:219],v[196:199],v[76:79]
s_add_u32 s67, s67, 1024
TR a[150:151],v232 offset:45568
CV v64,v64,v65
MM v[76:79],a[220:223],v[212:215],v[76:79]
s_min_u32 s67, s67, s86
EX v66,v66
MM v[72:75],a[204:207],v[212:215],v[132:135]
EX v67,v67
MM v[72:75],a[200:203],v[196:199],v[72:75]
TR a[152:153],v233 offset:33280
CV v65,v66,v67
MM v[72:75],a[196:199],v[180:183],v[72:75]
EX v68,v68
MM v[72:75],a[192:195],v[164:167],v[72:75]
EX v69,v69
MM v[80:83],a[192:195],v[168:171],v[136:139]
TR a[154:155],v233 offset:37376
CV v66,v68,v69
MM v[80:83],a[196:199],v[184:187],v[80:83]
EX v70,v70
MM v[80:83],a[200:203],v[200:203],v[80:83]
EX v71,v71
MM v[80:83],a[204:207],v[216:219],v[80:83]
TR a[156:157],v233 offset:41472
CV v67,v70,v71
MM v[84:87],a[220:223],v[216:219],v[136:139]
EX v72,v72
MM v[84:87],a[216:219],v[200:203],v[84:87]
TR a[158:159],v233 offset:45568
TR a[160:161],v232 offset:33792
MM v[84:87],a[212:215],v[184:187],v[84:87]
EX v73,v73
MM v[84:87],a[208:211],v[168:171],v[84:87]
TR a[162:163],v232 offset:37888
CV v72,v72,v73
MM v[92:95],a[208:211],v[172:175],v[140:143]
EX v74,v74
MM v[92:95],a[212:215],v[188:191],v[92:95]
TR a[164:165],v232 offset:41984
TR a[166:167],v232 offset:46080
MM v[92:95],a[216:219],v[204:207],v[92:95]
EX v75,v75
MM v[92:95],a[220:223],v[220:223],v[92:95]
TR a[168:169],v233 offset:33792
CV v73,v74,v75
MM v[88:91],a[204:207],v[220:223],v[140:143]
EX v76,v76
MM v[88:91],a[200:203],v[204:207],v[88:91]
EX v77,v77
MM v[88:91],a[196:199],v[188:191],v[88:91]
TR a[170:171],v233 offset:37888
CV v74,v76,v77
MM v[88:91],a[192:195],v[172:175],v[88:91]
EX v78,v78
MM v[96:99],a[224:227],v[160:163],v[128:131]
TR a[172:173],v233 offset:41984
TR a[174:175],v233 offset:46080
MM v[96:99],a[228:231],v[176:179],v[96:99]
EX v79,v79
MM v[96:99],a[232:235],v[192:195],v[96:99]
TR a[176:177],v232 offset:34304
CV v75,v78,v79
MM v[96:99],a[236:239],v[208:211],v[96:99]
EX v80,v80
MM v[100:103],a[252:255],v[208:211],v[128:131]
EX v81,v81
MM v[100:103],a[248:251],v[192:195],v[100:103]
TR a[178:179],v232 offset:38400
CV v80,v80,v81
MM v[100:103],a[244:247],v[176:179],v[100:103]
EX v82,v82
MM v[100:103],a[240:243],v[160:163],v[100:103]
TR a[180:181],v232 offset:42496
TR a[182:183],v232 offset:46592
MM v[108:111],a[240:243],v[164:167],v[132:135]
EX v83,v83
MM v[108:111],a[244:247],v[180:183],v[108:111]
TR a[184:185],v233 offset:34304
CV v81,v82,v83
MM v[108:111],a[248:251],v[196:199],v[108:111]
EX v84,v84
MM v[108:111],a[252:255],v[212:215],v[108:111]
EX v85,v85
MM v[104:107],a[236:239],v[212:215],v[132:135]
TR a[186:187],v233 offset:38400
CV v82,v84,v85
MM v[104:107],a[232:235],v[196:199],v[104:107]
EX v86,v86
MM v[104:107],a[228:231],v[180:183],v[104:107]
TR a[188:189],v233 offset:42496
TR a[190:191],v233 offset:46592
MM v[104:107],a[224:227],v[164:167],v[104:107]
EX v87,v87
MM v[112:115],a[224:227],v[168:171],v[136:139]
CV v83,v86,v87
MM v[112:115],a[228:231],v[184:187],v[112:115]
EX v88,v88
MM v[112:115],a[232:235],v[200:203],v[112:115]
EX v89,v89
MM v[112:115],a[236:239],v[216:219],v[112:115]
EX v90,v90
MM v[116:119],a[252:255],v[216:219],v[136:139]
EX v91,v91
MM v[116:119],a[248:251],v[200:203],v[116:119]
EX v92,v92
MM v[116:119],a[244:247],v[184:187],v[116:119]
EX v93,v93
MM v[116:119],a[240:243],v[168:171],v[116:119]
EX v94,v94
MM v[124:127],a[240:243],v[172:175],v[140:143]
EX v95,v95
MM v[124:127],a[244:247],v[188:191],v[124:127]
EX v96,v96
MM v[124:127],a[248:251],v[204:207],v[124:127]
EX v97,v97
MM v[124:127],a[252:255],v[220:223],v[124:127]
EX v98,v98
MM v[120:123],a[236:239],v[220:223],v[140:143]
EX v99,v99
MM v[120:123],a[232:235],v[204:207],v[120:123]
EX v100,v100
MM v[120:123],a[228:231],v[188:191],v[120:123]
EX v101,v101
MM v[120:123],a[224:227],v[172:175],v[120:123]
s_waitcnt vmcnt(13) lgkmcnt(0)
s_barrier
MM a[0:3],a[128:131],v[0:3],a[0:3]
s_add_u32 m0, s65, 32768
CV v88,v88,v89
CV v89,v90,v91
MM a[0:3],a[132:135],v[4:7],a[0:3]
RD a[192:195],v224 offset:0
CV v90,v92,v93
MM a[8:11],a[132:135],v[12:15],a[8:11]
RD a[196:199],v225 offset:0
CV v91,v94,v95
MM a[8:11],a[128:131],v[8:11],a[8:11]
buffer_load_dwordx4 v228, s[8:11], s64 offen offset:0 lds
MM a[16:19],a[128:131],v[16:19],a[16:19]
RD a[200:203],v226 offset:0
CV v68,v96,v97
MM a[16:19],a[132:135],v[20:23],a[16:19]
RD a[204:207],v227 offset:0
CV v69,v98,v99
MM a[24:27],a[132:135],v[28:31],a[24:27]
buffer_load_dwordx4 v229, s[8:11], s64 offen offset:1024 lds
MM a[24:27],a[128:131],v[24:27],a[24:27]
RD a[208:211],v224 offset:4096
CV v70,v100,v101
MM a[28:31],a[136:139],v[24:27],a[28:31]
RD a[212:215],v225 offset:4096
RD a[216:219],v226 offset:4096
MM a[28:31],a[140:143],v[28:31],a[28:31]
buffer_load_dwordx4 v230, s[8:11], s64 offen offset:2048 lds
MM a[20:23],a[140:143],v[20:23],a[20:23]
RD a[220:223],v227 offset:4096
RD a[224:227],v224 offset:8192
MM a[20:23],a[136:139],v[16:19],a[20:23]
EX v102,v102
MM a[12:15],a[136:139],v[8:11],a[12:15]
buffer_load_dwordx4 v231, s[8:11], s64 offen offset:3072 lds
MM a[12:15],a[140:143],v[12:15],a[12:15]
s_add_u32 m0, s65, 98304
EX v103,v103
MM a[4:7],a[140:143],v[4:7],a[4:7]
RD a[228:231],v225 offset:8192
CV v71,v102,v103
MM a[4:7],a[136:139],v[0:3],a[4:7]
EX v104,v104
MM a[32:35],a[144:147],v[0:3],a[32:35]
EX v105,v105
MM a[32:35],a[148:151],v[4:7],a[32:35]
RD a[232:235],v226 offset:8192
CV v76,v104,v105
MM a[40:43],a[148:151],v[12:15],a[40:43]
EX v106,v106
MM a[40:43],a[144:147],v[8:11],a[40:43]
buffer_load_dwordx4 v235, s[12:15], s81 offen offset:0 lds
MM a[48:51],a[144:147],v[16:19],a[48:51]
EX v107,v107
MM a[48:51],a[148:151],v[20:23],a[48:51]
RD a[236:239],v227 offset:8192
CV v77,v106,v107
MM a[56:59],a[148:151],v[28:31],a[56:59]
EX v108,v108
MM a[56:59],a[144:147],v[24:27],a[56:59]
EX v109,v109
MM a[60:63],a[152:155],v[24:27],a[60:63]
RD a[240:243],v224 offset:12288
CV v78,v108,v109
MM a[60:63],a[156:159],v[28:31],a[60:63]
buffer_load_dwordx4 v235, s[12:15], s64 offen offset:1024 lds
MM a[52:55],a[156:159],v[20:23],a[52:55]
EX v110,v110
MM a[52:55],a[152:155],v[16:19],a[52:55]
EX v111,v111
MM a[44:47],a[152:155],v[8:11],a[44:47]
RD a[244:247],v225 offset:12288
CV v79,v110,v111
MM a[44:47],a[156:159],v[12:15],a[44:47]
EX v112,v112
MM a[36:39],a[156:159],v[4:7],a[36:39]
EX v113,v113
MM a[36:39],a[152:155],v[0:3],a[36:39]
RD a[248:251],v226 offset:12288
CV v84,v112,v113
MM a[64:67],a[160:163],v[0:3],a[64:67]
buffer_load_dwordx4 v235, s[12:15], s81 offen offset:2048 lds
MM a[64:67],a[164:167],v[4:7],a[64:67]
EX v114,v114
MM a[72:75],a[164:167],v[12:15],a[72:75]
EX v115,v115
MM a[72:75],a[160:163],v[8:11],a[72:75]
RD a[252:255],v227 offset:12288
CV v85,v114,v115
MM a[80:83],a[160:163],v[16:19],a[80:83]
EX v116,v116
MM a[80:83],a[164:167],v[20:23],a[80:83]
buffer_load_dwordx4 v235, s[12:15], s64 offen offset:3072 lds
MM a[88:91],a[164:167],v[28:31],a[88:91]
EX v117,v117
MM a[88:91],a[160:163],v[24:27],a[88:91]
EX v118,v118
MM a[92:95],a[168:171],v[24:27],a[92:95]
EX v119,v119
MM a[92:95],a[172:175],v[28:31],a[92:95]
EX v120,v120
MM a[84:87],a[172:175],v[20:23],a[84:87]
EX v121,v121
MM a[84:87],a[168:171],v[16:19],a[84:87]
EX v122,v122
MM a[76:79],a[168:171],v[8:11],a[76:79]
EX v123,v123
MM a[76:79],a[172:175],v[12:15],a[76:79]
EX v124,v124
MM a[68:71],a[172:175],v[4:7],a[68:71]
EX v125,v125
MM a[68:71],a[168:171],v[0:3],a[68:71]
EX v126,v126
MM a[96:99],a[176:179],v[0:3],a[96:99]
EX v127,v127
MM a[96:99],a[180:183],v[4:7],a[96:99]
CV v86,v116,v117
CV v87,v118,v119
MM a[104:107],a[180:183],v[12:15],a[104:107]
CV v92,v120,v121
CV v93,v122,v123
MM a[104:107],a[176:179],v[8:11],a[104:107]
CV v94,v124,v125
CV v95,v126,v127
MM a[112:115],a[176:179],v[16:19],a[112:115]
MM a[112:115],a[180:183],v[20:23],a[112:115]
MM a[120:123],a[180:183],v[28:31],a[120:123]
MM a[120:123],a[176:179],v[24:27],a[120:123]
MM a[124:127],a[184:187],v[24:27],a[124:127]
MM a[124:127],a[188:191],v[28:31],a[124:127]
MM a[116:119],a[188:191],v[20:23],a[116:119]
MM a[116:119],a[184:187],v[16:19],a[116:119]
MM a[108:111],a[184:187],v[8:11],a[108:111]
MM a[108:111],a[188:191],v[12:15],a[108:111]
MM a[100:103],a[188:191],v[4:7],a[100:103]
MM a[100:103],a[184:187],v[0:3],a[100:103]
v_smfmac_f32_16x16x64_bf16 v[236:239], v[144:147], v[64:71], v234
v_smfmac_f32_16x16x64_bf16 v[236:239], v[148:151], v[72:79], v234
v_smfmac_f32_16x16x64_bf16 v[236:239], v[152:155], v[80:87], v234
v_smfmac_f32_16x16x64_bf16 v[236:239], v[156:159], v[88:95], v234
s_waitcnt lgkmcnt(0)
MM v[0:3],a[192:195],v[160:163],v[128:131]
s_add_u32 s64, s64, 16384
TR a[128:129],v232 offset:49152
s_add_u32 s81, s64, 896
MM v[0:3],a[196:199],v[176:179],v[0:3]
TR a[130:131],v232 offset:53248
TR a[132:133],v232 offset:57344
MM v[0:3],a[200:203],v[192:195],v[0:3]
TR a[134:135],v232 offset:61440
TR a[136:137],v233 offset:49152
MM v[0:3],a[204:207],v[208:211],v[0:3]
TR a[138:139],v233 offset:53248
v_add_f32 v247, v236, v237
MM v[4:7],a[220:223],v[208:211],v[128:131]
TR a[140:141],v233 offset:57344
v_cmp_nge_f32 vcc, s71, v247
MM v[4:7],a[216:219],v[192:195],v[4:7]
s_cbranch_vccnz LSITE11_%=
)ASM"
R"ASM(
LBACK11_%=:
TR a[142:143],v233 offset:61440
TR a[144:145],v232 offset:49664
MM v[4:7],a[212:215],v[176:179],v[4:7]
s_mov_b32 m0, s67
TR a[146:147],v232 offset:53760
TR a[148:149],v232 offset:57856
MM v[4:7],a[208:211],v[160:163],v[4:7]
buffer_load_dwordx4 v252, s[36:39], s66 offen offset:0 lds
MM v[12:15],a[208:211],v[164:167],v[132:135]
s_add_u32 s66, s66, 55296
EX v0,v0
MM v[12:15],a[212:215],v[180:183],v[12:15]
s_min_u32 s66, s66, s85
EX v1,v1
MM v[12:15],a[216:219],v[196:199],v[12:15]
LPG8_%=:
s_cbranch_execz LPG12_%=
s_add_u32 s67, s67, 1024
TR a[150:151],v232 offset:61952
CV v0,v0,v1
MM v[12:15],a[220:223],v[212:215],v[12:15]
s_min_u32 s67, s67, s86
EX v2,v2
MM v[8:11],a[204:207],v[212:215],v[132:135]
EX v3,v3
MM v[8:11],a[200:203],v[196:199],v[8:11]
TR a[152:153],v233 offset:49664
CV v1,v2,v3
MM v[8:11],a[196:199],v[180:183],v[8:11]
EX v4,v4
MM v[8:11],a[192:195],v[164:167],v[8:11]
EX v5,v5
MM v[16:19],a[192:195],v[168:171],v[136:139]
TR a[154:155],v233 offset:53760
CV v2,v4,v5
MM v[16:19],a[196:199],v[184:187],v[16:19]
EX v6,v6
MM v[16:19],a[200:203],v[200:203],v[16:19]
EX v7,v7
MM v[16:19],a[204:207],v[216:219],v[16:19]
TR a[156:157],v233 offset:57856
CV v3,v6,v7
MM v[20:23],a[220:223],v[216:219],v[136:139]
EX v8,v8
MM v[20:23],a[216:219],v[200:203],v[20:23]
TR a[158:159],v233 offset:61952
TR a[160:161],v232 offset:50176
MM v[20:23],a[212:215],v[184:187],v[20:23]
EX v9,v9
MM v[20:23],a[208:211],v[168:171],v[20:23]
TR a[162:163],v232 offset:54272
CV v8,v8,v9
MM v[28:31],a[208:211],v[172:175],v[140:143]
EX v10,v10
MM v[28:31],a[212:215],v[188:191],v[28:31]
TR a[164:165],v232 offset:58368
TR a[166:167],v232 offset:62464
MM v[28:31],a[216:219],v[204:207],v[28:31]
EX v11,v11
MM v[28:31],a[220:223],v[220:223],v[28:31]
TR a[168:169],v233 offset:50176
CV v9,v10,v11
MM v[24:27],a[204:207],v[220:223],v[140:143]
EX v12,v12
MM v[24:27],a[200:203],v[204:207],v[24:27]
EX v13,v13
MM v[24:27],a[196:199],v[188:191],v[24:27]
TR a[170:171],v233 offset:54272
CV v10,v12,v13
MM v[24:27],a[192:195],v[172:175],v[24:27]
EX v14,v14
MM v[32:35],a[224:227],v[160:163],v[128:131]
TR a[172:173],v233 offset:58368
TR a[174:175],v233 offset:62464
MM v[32:35],a[228:231],v[176:179],v[32:35]
EX v15,v15
MM v[32:35],a[232:235],v[192:195],v[32:35]
TR a[176:177],v232 offset:50688
CV v11,v14,v15
MM v[32:35],a[236:239],v[208:211],v[32:35]
EX v16,v16
MM v[36:39],a[252:255],v[208:211],v[128:131]
EX v17,v17
MM v[36:39],a[248:251],v[192:195],v[36:39]
TR a[178:179],v232 offset:54784
CV v16,v16,v17
MM v[36:39],a[244:247],v[176:179],v[36:39]
EX v18,v18
MM v[36:39],a[240:243],v[160:163],v[36:39]
TR a[180:181],v232 offset:58880
TR a[182:183],v232 offset:62976
MM v[44:47],a[240:243],v[164:167],v[132:135]
EX v19,v19
MM v[44:47],a[244:247],v[180:183],v[44:47]
TR a[184:185],v233 offset:50688
CV v17,v18,v19
MM v[44:47],a[248:251],v[196:199],v[44:47]
EX v20,v20
MM v[44:47],a[252:255],v[212:215],v[44:47]
EX v21,v21
MM v[40:43],a[236:239],v[212:215],v[132:135]
TR a[186:187],v233 offset:54784
CV v18,v20,v21
MM v[40:43],a[232:235],v[196:199],v[40:43]
EX v22,v22
MM v[40:43],a[228:231],v[180:183],v[40:43]
TR a[188:189],v233 offset:58880
TR a[190:191],v233 offset:62976
MM v[40:43],a[224:227],v[164:167],v[40:43]
EX v23,v23
MM v[48:51],a[224:227],v[168:171],v[136:139]
CV v19,v22,v23
MM v[48:51],a[228:231],v[184:187],v[48:51]
EX v24,v24
MM v[48:51],a[232:235],v[200:203],v[48:51]
EX v25,v25
MM v[48:51],a[236:239],v[216:219],v[48:51]
EX v26,v26
MM v[52:55],a[252:255],v[216:219],v[136:139]
EX v27,v27
MM v[52:55],a[248:251],v[200:203],v[52:55]
EX v28,v28
MM v[52:55],a[244:247],v[184:187],v[52:55]
EX v29,v29
MM v[52:55],a[240:243],v[168:171],v[52:55]
EX v30,v30
MM v[60:63],a[240:243],v[172:175],v[140:143]
EX v31,v31
MM v[60:63],a[244:247],v[188:191],v[60:63]
EX v32,v32
MM v[60:63],a[248:251],v[204:207],v[60:63]
EX v33,v33
MM v[60:63],a[252:255],v[220:223],v[60:63]
EX v34,v34
MM v[56:59],a[236:239],v[220:223],v[140:143]
EX v35,v35
MM v[56:59],a[232:235],v[204:207],v[56:59]
EX v36,v36
MM v[56:59],a[228:231],v[188:191],v[56:59]
EX v37,v37
MM v[56:59],a[224:227],v[172:175],v[56:59]
s_waitcnt vmcnt(13) lgkmcnt(0)
s_barrier
MM a[0:3],a[128:131],v[64:67],a[0:3]
s_add_u32 m0, s65, 49152
CV v24,v24,v25
CV v25,v26,v27
MM a[0:3],a[132:135],v[68:71],a[0:3]
RD a[192:195],v224 offset:16384
CV v26,v28,v29
MM a[8:11],a[132:135],v[76:79],a[8:11]
RD a[196:199],v225 offset:16384
CV v27,v30,v31
MM a[8:11],a[128:131],v[72:75],a[8:11]
buffer_load_dwordx4 v228, s[8:11], s64 offen offset:0 lds
MM a[16:19],a[128:131],v[80:83],a[16:19]
RD a[200:203],v226 offset:16384
CV v4,v32,v33
MM a[16:19],a[132:135],v[84:87],a[16:19]
RD a[204:207],v227 offset:16384
CV v5,v34,v35
MM a[24:27],a[132:135],v[92:95],a[24:27]
buffer_load_dwordx4 v229, s[8:11], s64 offen offset:1024 lds
MM a[24:27],a[128:131],v[88:91],a[24:27]
RD a[208:211],v224 offset:20480
CV v6,v36,v37
MM a[28:31],a[136:139],v[88:91],a[28:31]
RD a[212:215],v225 offset:20480
RD a[216:219],v226 offset:20480
MM a[28:31],a[140:143],v[92:95],a[28:31]
buffer_load_dwordx4 v230, s[8:11], s64 offen offset:2048 lds
MM a[20:23],a[140:143],v[84:87],a[20:23]
RD a[220:223],v227 offset:20480
RD a[224:227],v224 offset:24576
MM a[20:23],a[136:139],v[80:83],a[20:23]
EX v38,v38
MM a[12:15],a[136:139],v[72:75],a[12:15]
buffer_load_dwordx4 v231, s[8:11], s64 offen offset:3072 lds
MM a[12:15],a[140:143],v[76:79],a[12:15]
s_add_u32 m0, s65, 114688
EX v39,v39
MM a[4:7],a[140:143],v[68:71],a[4:7]
RD a[228:231],v225 offset:24576
CV v7,v38,v39
MM a[4:7],a[136:139],v[64:67],a[4:7]
EX v40,v40
MM a[32:35],a[144:147],v[64:67],a[32:35]
EX v41,v41
MM a[32:35],a[148:151],v[68:71],a[32:35]
RD a[232:235],v226 offset:24576
CV v12,v40,v41
MM a[40:43],a[148:151],v[76:79],a[40:43]
EX v42,v42
MM a[40:43],a[144:147],v[72:75],a[40:43]
buffer_load_dwordx4 v235, s[12:15], s81 offen offset:0 lds
MM a[48:51],a[144:147],v[80:83],a[48:51]
EX v43,v43
MM a[48:51],a[148:151],v[84:87],a[48:51]
RD a[236:239],v227 offset:24576
CV v13,v42,v43
MM a[56:59],a[148:151],v[92:95],a[56:59]
EX v44,v44
MM a[56:59],a[144:147],v[88:91],a[56:59]
EX v45,v45
MM a[60:63],a[152:155],v[88:91],a[60:63]
RD a[240:243],v224 offset:28672
CV v14,v44,v45
MM a[60:63],a[156:159],v[92:95],a[60:63]
buffer_load_dwordx4 v235, s[12:15], s64 offen offset:1024 lds
MM a[52:55],a[156:159],v[84:87],a[52:55]
EX v46,v46
MM a[52:55],a[152:155],v[80:83],a[52:55]
EX v47,v47
MM a[44:47],a[152:155],v[72:75],a[44:47]
RD a[244:247],v225 offset:28672
CV v15,v46,v47
MM a[44:47],a[156:159],v[76:79],a[44:47]
EX v48,v48
MM a[36:39],a[156:159],v[68:71],a[36:39]
EX v49,v49
MM a[36:39],a[152:155],v[64:67],a[36:39]
RD a[248:251],v226 offset:28672
CV v20,v48,v49
MM a[64:67],a[160:163],v[64:67],a[64:67]
buffer_load_dwordx4 v235, s[12:15], s81 offen offset:2048 lds
MM a[64:67],a[164:167],v[68:71],a[64:67]
EX v50,v50
MM a[72:75],a[164:167],v[76:79],a[72:75]
EX v51,v51
MM a[72:75],a[160:163],v[72:75],a[72:75]
RD a[252:255],v227 offset:28672
CV v21,v50,v51
MM a[80:83],a[160:163],v[80:83],a[80:83]
EX v52,v52
MM a[80:83],a[164:167],v[84:87],a[80:83]
buffer_load_dwordx4 v235, s[12:15], s64 offen offset:3072 lds
MM a[88:91],a[164:167],v[92:95],a[88:91]
EX v53,v53
MM a[88:91],a[160:163],v[88:91],a[88:91]
EX v54,v54
MM a[92:95],a[168:171],v[88:91],a[92:95]
EX v55,v55
MM a[92:95],a[172:175],v[92:95],a[92:95]
EX v56,v56
MM a[84:87],a[172:175],v[84:87],a[84:87]
EX v57,v57
MM a[84:87],a[168:171],v[80:83],a[84:87]
EX v58,v58
MM a[76:79],a[168:171],v[72:75],a[76:79]
EX v59,v59
MM a[76:79],a[172:175],v[76:79],a[76:79]
EX v60,v60
MM a[68:71],a[172:175],v[68:71],a[68:71]
EX v61,v61
MM a[68:71],a[168:171],v[64:67],a[68:71]
EX v62,v62
MM a[96:99],a[176:179],v[64:67],a[96:99]
EX v63,v63
MM a[96:99],a[180:183],v[68:71],a[96:99]
CV v22,v52,v53
CV v23,v54,v55
MM a[104:107],a[180:183],v[76:79],a[104:107]
CV v28,v56,v57
CV v29,v58,v59
MM a[104:107],a[176:179],v[72:75],a[104:107]
CV v30,v60,v61
CV v31,v62,v63
MM a[112:115],a[176:179],v[80:83],a[112:115]
MM a[112:115],a[180:183],v[84:87],a[112:115]
MM a[120:123],a[180:183],v[92:95],a[120:123]
MM a[120:123],a[176:179],v[88:91],a[120:123]
MM a[124:127],a[184:187],v[88:91],a[124:127]
MM a[124:127],a[188:191],v[92:95],a[124:127]
MM a[116:119],a[188:191],v[84:87],a[116:119]
MM a[116:119],a[184:187],v[80:83],a[116:119]
MM a[108:111],a[184:187],v[72:75],a[108:111]
MM a[108:111],a[188:191],v[76:79],a[108:111]
MM a[100:103],a[188:191],v[68:71],a[100:103]
MM a[100:103],a[184:187],v[64:67],a[100:103]
v_smfmac_f32_16x16x64_bf16 v[236:239], v[144:147], v[0:7], v234
v_smfmac_f32_16x16x64_bf16 v[236:239], v[148:151], v[8:15], v234
v_smfmac_f32_16x16x64_bf16 v[236:239], v[152:155], v[16:23], v234
v_smfmac_f32_16x16x64_bf16 v[236:239], v[156:159], v[24:31], v234
s_waitcnt lgkmcnt(0)
s_sub_u32 s70, s70, 1
s_cmp_lg_u32 s70, 0
s_cbranch_scc1 LLA_%=
MM v[64:67],a[192:195],v[160:163],v[128:131]
s_add_u32 s64, s64, 16384
TR a[128:129],v232 offset:0
s_add_u32 s81, s64, 896
MM v[64:67],a[196:199],v[176:179],v[64:67]
TR a[130:131],v232 offset:4096
TR a[132:133],v232 offset:8192
MM v[64:67],a[200:203],v[192:195],v[64:67]
TR a[134:135],v232 offset:12288
TR a[136:137],v233 offset:0
MM v[64:67],a[204:207],v[208:211],v[64:67]
TR a[138:139],v233 offset:4096
v_add_f32 v246, v236, v237
MM v[68:71],a[220:223],v[208:211],v[128:131]
TR a[140:141],v233 offset:8192
v_cmp_nge_f32 vcc, s71, v246
MM v[68:71],a[216:219],v[192:195],v[68:71]
s_cbranch_vccnz LSITE12_%=
LBACK12_%=:
TR a[142:143],v233 offset:12288
TR a[144:145],v232 offset:512
MM v[68:71],a[212:215],v[176:179],v[68:71]
v_lshrrev_b32 v52, 1, v248
v_and_b32 v52, 56, v52
MM v[68:71],a[208:211],v[160:163],v[68:71]
s_mov_b32 s90, 86016
buffer_load_dwordx2 v[48:49], v52, s[20:23], s90 offen offset:0 sc1
MM v[76:79],a[208:211],v[164:167],v[132:135]
EX v64,v64
MM v[76:79],a[212:215],v[180:183],v[76:79]
EX v65,v65
MM v[76:79],a[216:219],v[196:199],v[76:79]
TR a[146:147],v232 offset:4608
CV v64,v64,v65
MM v[76:79],a[220:223],v[212:215],v[76:79]
EX v66,v66
MM v[72:75],a[204:207],v[212:215],v[132:135]
EX v67,v67
MM v[72:75],a[200:203],v[196:199],v[72:75]
TR a[148:149],v232 offset:8704
CV v65,v66,v67
MM v[72:75],a[196:199],v[180:183],v[72:75]
EX v68,v68
MM v[72:75],a[192:195],v[164:167],v[72:75]
TR a[150:151],v232 offset:12800
TR a[152:153],v233 offset:512
MM v[80:83],a[192:195],v[168:171],v[136:139]
EX v69,v69
MM v[80:83],a[196:199],v[184:187],v[80:83]
TR a[154:155],v233 offset:4608
CV v66,v68,v69
MM v[80:83],a[200:203],v[200:203],v[80:83]
EX v70,v70
MM v[80:83],a[204:207],v[216:219],v[80:83]
EX v71,v71
MM v[84:87],a[220:223],v[216:219],v[136:139]
TR a[156:157],v233 offset:8704
CV v67,v70,v71
MM v[84:87],a[216:219],v[200:203],v[84:87]
EX v72,v72
MM v[84:87],a[212:215],v[184:187],v[84:87]
TR a[158:159],v233 offset:12800
TR a[160:161],v232 offset:1024
MM v[84:87],a[208:211],v[168:171],v[84:87]
EX v73,v73
MM v[92:95],a[208:211],v[172:175],v[140:143]
TR a[162:163],v232 offset:5120
CV v72,v72,v73
MM v[92:95],a[212:215],v[188:191],v[92:95]
EX v74,v74
MM v[92:95],a[216:219],v[204:207],v[92:95]
EX v75,v75
MM v[92:95],a[220:223],v[220:223],v[92:95]
TR a[164:165],v232 offset:9216
CV v73,v74,v75
MM v[88:91],a[204:207],v[220:223],v[140:143]
EX v76,v76
MM v[88:91],a[200:203],v[204:207],v[88:91]
TR a[166:167],v232 offset:13312
TR a[168:169],v233 offset:1024
MM v[88:91],a[196:199],v[188:191],v[88:91]
EX v77,v77
MM v[88:91],a[192:195],v[172:175],v[88:91]
TR a[170:171],v233 offset:5120
CV v74,v76,v77
MM v[96:99],a[224:227],v[160:163],v[128:131]
EX v78,v78
MM v[96:99],a[228:231],v[176:179],v[96:99]
EX v79,v79
MM v[96:99],a[232:235],v[192:195],v[96:99]
TR a[172:173],v233 offset:9216
CV v75,v78,v79
MM v[96:99],a[236:239],v[208:211],v[96:99]
TR a[174:175],v233 offset:13312
TR a[176:177],v232 offset:1536
MM v[100:103],a[252:255],v[208:211],v[128:131]
EX v80,v80
MM v[100:103],a[248:251],v[192:195],v[100:103]
EX v81,v81
MM v[100:103],a[244:247],v[176:179],v[100:103]
TR a[178:179],v232 offset:5632
CV v80,v80,v81
MM v[100:103],a[240:243],v[160:163],v[100:103]
EX v82,v82
MM v[108:111],a[240:243],v[164:167],v[132:135]
)ASM"
R"ASM(
TR a[180:181],v232 offset:9728
TR a[182:183],v232 offset:13824
MM v[108:111],a[244:247],v[180:183],v[108:111]
EX v83,v83
MM v[108:111],a[248:251],v[196:199],v[108:111]
TR a[184:185],v233 offset:1536
CV v81,v82,v83
MM v[108:111],a[252:255],v[212:215],v[108:111]
EX v84,v84
MM v[104:107],a[236:239],v[212:215],v[132:135]
EX v85,v85
MM v[104:107],a[232:235],v[196:199],v[104:107]
TR a[186:187],v233 offset:5632
CV v82,v84,v85
MM v[104:107],a[228:231],v[180:183],v[104:107]
EX v86,v86
MM v[104:107],a[224:227],v[164:167],v[104:107]
TR a[188:189],v233 offset:9728
TR a[190:191],v233 offset:13824
MM v[112:115],a[224:227],v[168:171],v[136:139]
EX v87,v87
MM v[112:115],a[228:231],v[184:187],v[112:115]
CV v83,v86,v87
MM v[112:115],a[232:235],v[200:203],v[112:115]
EX v88,v88
MM v[112:115],a[236:239],v[216:219],v[112:115]
EX v89,v89
MM v[116:119],a[252:255],v[216:219],v[136:139]
EX v90,v90
MM v[116:119],a[248:251],v[200:203],v[116:119]
EX v91,v91
MM v[116:119],a[244:247],v[184:187],v[116:119]
EX v92,v92
MM v[116:119],a[240:243],v[168:171],v[116:119]
EX v93,v93
MM v[124:127],a[240:243],v[172:175],v[140:143]
EX v94,v94
MM v[124:127],a[244:247],v[188:191],v[124:127]
EX v95,v95
MM v[124:127],a[248:251],v[204:207],v[124:127]
EX v96,v96
MM v[124:127],a[252:255],v[220:223],v[124:127]
EX v97,v97
MM v[120:123],a[236:239],v[220:223],v[140:143]
EX v98,v98
MM v[120:123],a[232:235],v[204:207],v[120:123]
EX v99,v99
MM v[120:123],a[228:231],v[188:191],v[120:123]
EX v100,v100
MM v[120:123],a[224:227],v[172:175],v[120:123]
s_waitcnt vmcnt(14) lgkmcnt(0)
s_barrier
MM a[0:3],a[128:131],v[0:3],a[0:3]
s_waitcnt vmcnt(0)
v_cmp_ne_u32 s[76:77], s69, v49
CV v88,v88,v89
MM a[0:3],a[132:135],v[4:7],a[0:3]
s_add_u32 m0, s65, 0
CV v89,v90,v91
CV v90,v92,v93
MM a[8:11],a[132:135],v[12:15],a[8:11]
s_cmp_lg_u64 s[76:77], 0
s_cbranch_scc1 LRIX_%=
LRIXb_%=:
v_readlane_b32 s52, v48, 0
MM a[8:11],a[128:131],v[8:11],a[8:11]
v_readlane_b32 s53, v48, 1
v_readlane_b32 s54, v48, 2
MM a[16:19],a[128:131],v[16:19],a[16:19]
v_readlane_b32 s55, v48, 3
v_readlane_b32 s56, v48, 4
MM a[16:19],a[132:135],v[20:23],a[16:19]
v_readlane_b32 s57, v48, 5
v_readlane_b32 s58, v48, 6
MM a[24:27],a[132:135],v[28:31],a[24:27]
RD a[192:195],v224 offset:32768
v_readlane_b32 s59, v48, 7
MM a[24:27],a[128:131],v[24:27],a[24:27]
s_lshl_b32 s90, %[swave], 1
RD a[196:199],v225 offset:32768
CV v91,v94,v95
MM a[28:31],a[136:139],v[24:27],a[28:31]
s_add_u32 s91, s90, 1
v_readlane_b32 s66, v48, s90
v_readlane_b32 s67, v48, s91
MM a[28:31],a[140:143],v[28:31],a[28:31]
s_mul_i32 s91, %[swave], 640
buffer_load_dwordx4 v228, s[8:11], s64 offen offset:0 lds
MM a[20:23],a[140:143],v[20:23],a[20:23]
s_add_u32 s91, s91, %[slds]
RD a[200:203],v226 offset:32768
CV v68,v96,v97
MM a[20:23],a[136:139],v[16:19],a[20:23]
s_bfe_u32 s90, s48, 131077
CV v69,v98,v99
MM a[12:15],a[136:139],v[8:11],a[12:15]
s_mul_i32 s90, s90, 320
buffer_load_dwordx4 v229, s[8:11], s64 offen offset:1024 lds
MM a[12:15],a[140:143],v[12:15],a[12:15]
s_add_u32 s90, s90, 131072
EX v101,v101
MM a[4:7],a[140:143],v[4:7],a[4:7]
s_mul_i32 s66, s66, 1280
RD a[204:207],v227 offset:32768
CV v70,v100,v101
MM a[4:7],a[136:139],v[0:3],a[4:7]
s_add_u32 s66, s66, s90
s_add_u32 m0, s91, 151552
s_mov_b64 exec, 0xfffff
buffer_load_dwordx4 v248, s[20:23], s66 offen offset:0 sc1 lds
s_mov_b64 exec, -1
MM a[32:35],a[144:147],v[0:3],a[32:35]
s_mul_i32 s67, s67, 1280
EX v102,v102
MM a[32:35],a[148:151],v[4:7],a[32:35]
s_add_u32 s67, s67, s90
s_add_u32 m0, s91, 151872
s_mov_b64 exec, 0xfffff
buffer_load_dwordx4 v248, s[20:23], s67 offen offset:0 sc1 lds
s_mov_b64 exec, -1
MM a[40:43],a[148:151],v[12:15],a[40:43]
s_add_u32 m0, s65, 0
EX v103,v103
MM a[40:43],a[144:147],v[8:11],a[40:43]
RD a[208:211],v224 offset:36864
CV v71,v102,v103
MM a[48:51],a[144:147],v[16:19],a[48:51]
EX v104,v104
MM a[48:51],a[148:151],v[20:23],a[48:51]
EX v105,v105
MM a[56:59],a[148:151],v[28:31],a[56:59]
buffer_load_dwordx4 v230, s[8:11], s64 offen offset:2048 lds
MM a[56:59],a[144:147],v[24:27],a[56:59]
EX v106,v106
MM a[60:63],a[152:155],v[24:27],a[60:63]
RD a[212:215],v225 offset:36864
CV v76,v104,v105
MM a[60:63],a[156:159],v[28:31],a[60:63]
EX v107,v107
MM a[52:55],a[156:159],v[20:23],a[52:55]
RD a[216:219],v226 offset:36864
CV v77,v106,v107
MM a[52:55],a[152:155],v[16:19],a[52:55]
EX v108,v108
MM a[44:47],a[152:155],v[8:11],a[44:47]
EX v109,v109
MM a[44:47],a[156:159],v[12:15],a[44:47]
buffer_load_dwordx4 v231, s[8:11], s64 offen offset:3072 lds
MM a[36:39],a[156:159],v[4:7],a[36:39]
s_add_u32 m0, s65, 65536
RD a[220:223],v227 offset:36864
CV v78,v108,v109
MM a[36:39],a[152:155],v[0:3],a[36:39]
EX v110,v110
MM a[64:67],a[160:163],v[0:3],a[64:67]
EX v111,v111
MM a[64:67],a[164:167],v[4:7],a[64:67]
RD a[224:227],v224 offset:40960
CV v79,v110,v111
MM a[72:75],a[164:167],v[12:15],a[72:75]
EX v112,v112
MM a[72:75],a[160:163],v[8:11],a[72:75]
buffer_load_dwordx4 v235, s[12:15], s81 offen offset:0 lds
MM a[80:83],a[160:163],v[16:19],a[80:83]
EX v113,v113
MM a[80:83],a[164:167],v[20:23],a[80:83]
RD a[228:231],v225 offset:40960
CV v84,v112,v113
MM a[88:91],a[164:167],v[28:31],a[88:91]
EX v114,v114
MM a[88:91],a[160:163],v[24:27],a[88:91]
EX v115,v115
MM a[92:95],a[168:171],v[24:27],a[92:95]
RD a[232:235],v226 offset:40960
CV v85,v114,v115
MM a[92:95],a[172:175],v[28:31],a[92:95]
EX v116,v116
MM a[84:87],a[172:175],v[20:23],a[84:87]
buffer_load_dwordx4 v235, s[12:15], s64 offen offset:1024 lds
MM a[84:87],a[168:171],v[16:19],a[84:87]
EX v117,v117
MM a[76:79],a[168:171],v[8:11],a[76:79]
RD a[236:239],v227 offset:40960
CV v86,v116,v117
MM a[76:79],a[172:175],v[12:15],a[76:79]
EX v118,v118
MM a[68:71],a[172:175],v[4:7],a[68:71]
EX v119,v119
MM a[68:71],a[168:171],v[0:3],a[68:71]
RD a[240:243],v224 offset:45056
CV v87,v118,v119
MM a[96:99],a[176:179],v[0:3],a[96:99]
EX v120,v120
MM a[96:99],a[180:183],v[4:7],a[96:99]
buffer_load_dwordx4 v235, s[12:15], s81 offen offset:2048 lds
MM a[104:107],a[180:183],v[12:15],a[104:107]
EX v121,v121
MM a[104:107],a[176:179],v[8:11],a[104:107]
RD a[244:247],v225 offset:45056
CV v92,v120,v121
MM a[112:115],a[176:179],v[16:19],a[112:115]
EX v122,v122
MM a[112:115],a[180:183],v[20:23],a[112:115]
RD a[248:251],v226 offset:45056
RD a[252:255],v227 offset:45056
MM a[120:123],a[180:183],v[28:31],a[120:123]
EX v123,v123
MM a[120:123],a[176:179],v[24:27],a[120:123]
EX v124,v124
MM a[124:127],a[184:187],v[24:27],a[124:127]
buffer_load_dwordx4 v235, s[12:15], s64 offen offset:3072 lds
MM a[124:127],a[188:191],v[28:31],a[124:127]
EX v125,v125
s_nop 0
LPG9_%=:
s_cbranch_execz LTR0_%=
MM a[116:119],a[188:191],v[20:23],a[116:119]
EX v126,v126
MM a[116:119],a[184:187],v[16:19],a[116:119]
EX v127,v127
MM a[108:111],a[184:187],v[8:11],a[108:111]
CV v93,v122,v123
CV v94,v124,v125
MM a[108:111],a[188:191],v[12:15],a[108:111]
CV v95,v126,v127
MM a[100:103],a[188:191],v[4:7],a[100:103]
MM a[100:103],a[184:187],v[0:3],a[100:103]
v_smfmac_f32_16x16x64_bf16 v[236:239], v[144:147], v[64:71], v234
v_smfmac_f32_16x16x64_bf16 v[236:239], v[148:151], v[72:79], v234
v_smfmac_f32_16x16x64_bf16 v[236:239], v[152:155], v[80:87], v234
v_smfmac_f32_16x16x64_bf16 v[236:239], v[156:159], v[88:95], v234
s_waitcnt lgkmcnt(0)
MM v[0:3],a[192:195],v[160:163],v[128:131]
s_add_u32 s64, s64, 16384
TR a[128:129],v232 offset:16384
s_add_u32 s81, s64, 896
MM v[0:3],a[196:199],v[176:179],v[0:3]
TR a[130:131],v232 offset:20480
TR a[132:133],v232 offset:24576
MM v[0:3],a[200:203],v[192:195],v[0:3]
TR a[134:135],v232 offset:28672
TR a[136:137],v233 offset:16384
MM v[0:3],a[204:207],v[208:211],v[0:3]
TR a[138:139],v233 offset:20480
v_add_f32 v247, v236, v237
MM v[4:7],a[220:223],v[208:211],v[128:131]
TR a[140:141],v233 offset:24576
v_cmp_nge_f32 vcc, s71, v247
MM v[4:7],a[216:219],v[192:195],v[4:7]
s_cbranch_vccnz LSITE13_%=
LBACK13_%=:
TR a[142:143],v233 offset:28672
TR a[144:145],v232 offset:16896
MM v[4:7],a[212:215],v[176:179],v[4:7]
v_lshrrev_b32 v127, 8, v248
v_lshlrev_b32 v124, 9, v127
MM v[4:7],a[208:211],v[160:163],v[4:7]
v_and_b32 v126, 192, v248
v_add_u32 v124, v124, v126
MM v[12:15],a[208:211],v[164:167],v[132:135]
v_bfe_u32 v126, v248, 4, 2
v_lshl_add_u32 v124, v126, 3, v124
MM v[12:15],a[212:215],v[180:183],v[12:15]
s_lshr_b32 s76, %[swave], 1
EX v0,v0
MM v[12:15],a[216:219],v[196:199],v[12:15]
s_mul_i32 s76, s76, 10240
EX v1,v1
MM v[12:15],a[220:223],v[212:215],v[12:15]
s_and_b32 s77, %[swave], 1
TR a[146:147],v232 offset:20992
CV v0,v0,v1
MM v[8:11],a[204:207],v[212:215],v[132:135]
s_lshl_b32 s77, s77, 5
EX v2,v2
MM v[8:11],a[200:203],v[196:199],v[8:11]
s_add_u32 s76, s76, s77
EX v3,v3
MM v[8:11],a[196:199],v[180:183],v[8:11]
s_add_u32 s76, s76, %[slds]
TR a[148:149],v232 offset:25088
CV v1,v2,v3
MM v[8:11],a[192:195],v[164:167],v[8:11]
s_add_u32 s76, s76, 131072
v_add_u32 v124, s76, v124
v_bfe_u32 v126, v248, 4, 3
MM v[16:19],a[192:195],v[168:171],v[136:139]
v_mul_u32_u24 v125, 320, v126
v_lshl_add_u32 v125, v127, 4, v125
MM v[16:19],a[196:199],v[184:187],v[16:19]
s_add_u32 s76, %[slds], 151552
v_add_u32 v125, s76, v125
v_lshl_add_u32 v126, v127, 3, v126
MM v[16:19],a[200:203],v[200:203],v[16:19]
TR a[150:151],v232 offset:29184
v_lshlrev_b32 v126, 4, v126
MM v[16:19],a[204:207],v[216:219],v[16:19]
s_lshr_b32 s76, s48, 7
EX v4,v4
MM v[20:23],a[220:223],v[216:219],v[136:139]
s_lshl_b32 s76, s76, 5
EX v5,v5
MM v[20:23],a[216:219],v[200:203],v[20:23]
s_and_b32 s77, s48, 16
EX v6,v6
MM v[20:23],a[212:215],v[184:187],v[20:23]
s_add_u32 s76, s76, s77
EX v7,v7
MM v[20:23],a[208:211],v[168:171],v[20:23]
s_bfe_u32 s77, s48, 131077
TR a[152:153],v233 offset:16896
CV v2,v4,v5
MM v[28:31],a[208:211],v[172:175],v[140:143]
s_lshl_b32 s77, s77, 2
EX v8,v8
MM v[28:31],a[212:215],v[188:191],v[28:31]
s_add_u32 s76, s76, s77
TR a[154:155],v233 offset:20992
CV v3,v6,v7
MM v[28:31],a[216:219],v[204:207],v[28:31]
s_add_u32 s76, s76, %[swave]
EX v9,v9
MM v[28:31],a[220:223],v[220:223],v[28:31]
s_lshl_b32 s76, s76, 9
TR a[156:157],v233 offset:25088
TR a[158:159],v233 offset:29184
MM v[24:27],a[204:207],v[220:223],v[140:143]
s_add_u32 s76, s76, 22183936
EX v10,v10
MM v[24:27],a[200:203],v[204:207],v[24:27]
TR a[160:161],v232 offset:17408
CV v8,v8,v9
MM v[24:27],a[196:199],v[188:191],v[24:27]
EX v11,v11
MM v[24:27],a[192:195],v[172:175],v[24:27]
EX v12,v12
MM v[32:35],a[224:227],v[160:163],v[128:131]
TR a[162:163],v232 offset:21504
CV v9,v10,v11
MM v[32:35],a[228:231],v[176:179],v[32:35]
EX v13,v13
MM v[32:35],a[232:235],v[192:195],v[32:35]
TR a[164:165],v232 offset:25600
CV v10,v12,v13
MM v[32:35],a[236:239],v[208:211],v[32:35]
EX v14,v14
MM v[36:39],a[252:255],v[208:211],v[128:131]
TR a[166:167],v232 offset:29696
TR a[168:169],v233 offset:17408
MM v[36:39],a[248:251],v[192:195],v[36:39]
EX v15,v15
MM v[36:39],a[244:247],v[176:179],v[36:39]
TR a[170:171],v233 offset:21504
CV v11,v14,v15
MM v[36:39],a[240:243],v[160:163],v[36:39]
EX v16,v16
MM v[44:47],a[240:243],v[164:167],v[132:135]
TR a[172:173],v233 offset:25600
TR a[174:175],v233 offset:29696
MM v[44:47],a[244:247],v[180:183],v[44:47]
EX v17,v17
MM v[44:47],a[248:251],v[196:199],v[44:47]
EX v18,v18
MM v[44:47],a[252:255],v[212:215],v[44:47]
TR a[176:177],v232 offset:17920
CV v16,v16,v17
MM v[40:43],a[236:239],v[212:215],v[132:135]
EX v19,v19
MM v[40:43],a[232:235],v[196:199],v[40:43]
TR a[178:179],v232 offset:22016
CV v17,v18,v19
MM v[40:43],a[228:231],v[180:183],v[40:43]
EX v20,v20
MM v[40:43],a[224:227],v[164:167],v[40:43]
TR a[180:181],v232 offset:26112
TR a[182:183],v232 offset:30208
MM v[48:51],a[224:227],v[168:171],v[136:139]
EX v21,v21
MM v[48:51],a[228:231],v[184:187],v[48:51]
TR a[184:185],v233 offset:17920
CV v18,v20,v21
MM v[48:51],a[232:235],v[200:203],v[48:51]
EX v22,v22
MM v[48:51],a[236:239],v[216:219],v[48:51]
EX v23,v23
MM v[52:55],a[252:255],v[216:219],v[136:139]
TR a[186:187],v233 offset:22016
CV v19,v22,v23
MM v[52:55],a[248:251],v[200:203],v[52:55]
EX v24,v24
MM v[52:55],a[244:247],v[184:187],v[52:55]
TR a[188:189],v233 offset:26112
TR a[190:191],v233 offset:30208
MM v[52:55],a[240:243],v[168:171],v[52:55]
)ASM"
R"ASM(
EX v25,v25
MM v[60:63],a[240:243],v[172:175],v[140:143]
EX v26,v26
MM v[60:63],a[244:247],v[188:191],v[60:63]
EX v27,v27
MM v[60:63],a[248:251],v[204:207],v[60:63]
EX v28,v28
MM v[60:63],a[252:255],v[220:223],v[60:63]
EX v29,v29
MM v[56:59],a[236:239],v[220:223],v[140:143]
EX v30,v30
MM v[56:59],a[232:235],v[204:207],v[56:59]
EX v31,v31
MM v[56:59],a[228:231],v[188:191],v[56:59]
EX v32,v32
MM v[56:59],a[224:227],v[172:175],v[56:59]
s_waitcnt vmcnt(6) lgkmcnt(0)
s_barrier
MM a[0:3],a[128:131],v[64:67],a[0:3]
s_add_u32 m0, s65, 16384
CV v24,v24,v25
CV v25,v26,v27
MM a[0:3],a[132:135],v[68:71],a[0:3]
RD a[192:195],v224 offset:49152
CV v26,v28,v29
MM a[8:11],a[132:135],v[76:79],a[8:11]
RD a[196:199],v225 offset:49152
CV v27,v30,v31
MM a[8:11],a[128:131],v[72:75],a[8:11]
buffer_load_dwordx4 v228, s[8:11], s64 offen offset:0 lds
MM a[16:19],a[128:131],v[80:83],a[16:19]
RD a[200:203],v226 offset:49152
RD a[204:207],v227 offset:49152
MM a[16:19],a[132:135],v[84:87],a[16:19]
EX v33,v33
MM a[24:27],a[132:135],v[92:95],a[24:27]
RD a[208:211],v224 offset:53248
CV v4,v32,v33
MM a[24:27],a[128:131],v[88:91],a[24:27]
EX v34,v34
MM a[28:31],a[136:139],v[88:91],a[28:31]
buffer_load_dwordx4 v229, s[8:11], s64 offen offset:1024 lds
MM a[28:31],a[140:143],v[92:95],a[28:31]
EX v35,v35
MM a[20:23],a[140:143],v[84:87],a[20:23]
RD a[212:215],v225 offset:53248
CV v5,v34,v35
MM a[20:23],a[136:139],v[80:83],a[20:23]
EX v36,v36
MM a[12:15],a[136:139],v[72:75],a[12:15]
TR v[96:97],v124 offset:0
TR v[98:99],v124 offset:256
MM a[12:15],a[140:143],v[76:79],a[12:15]
RD v[108:111],v125 offset:0
TR v[100:101],v124 offset:2048
MM a[4:7],a[140:143],v[68:71],a[4:7]
TR v[102:103],v124 offset:2304
RD v[112:115],v125 offset:64
MM a[4:7],a[136:139],v[64:67],a[4:7]
TR v[104:105],v124 offset:4096
TR v[106:107],v124 offset:4352
MM a[32:35],a[144:147],v[64:67],a[32:35]
RD v[116:119],v125 offset:128
RD a[216:219],v226 offset:53248
MM a[32:35],a[148:151],v[68:71],a[32:35]
EX v37,v37
MM a[40:43],a[148:151],v[76:79],a[40:43]
EX v38,v38
MM a[40:43],a[144:147],v[72:75],a[40:43]
EX v39,v39
MM a[48:51],a[144:147],v[80:83],a[48:51]
CV v6,v36,v37
CV v7,v38,v39
MM a[48:51],a[148:151],v[84:87],a[48:51]
EX v40,v40
MM a[56:59],a[148:151],v[92:95],a[56:59]
EX v41,v41
MM a[56:59],a[144:147],v[88:91],a[56:59]
buffer_load_dwordx4 v230, s[8:11], s64 offen offset:2048 lds
MM a[60:63],a[152:155],v[88:91],a[60:63]
s_waitcnt lgkmcnt(1)
EX v42,v42
MM a[60:63],a[156:159],v[92:95],a[60:63]
EX v43,v43
MM v[120:123],v[96:99],v[108:111],0
MM v[120:123],v[100:103],v[112:115],v[120:123]
MM v[120:123],v[104:107],v[116:119],v[120:123]
MM a[52:55],a[156:159],v[84:87],a[52:55]
TR v[96:97],v124 offset:6144
TR v[98:99],v124 offset:6400
MM a[52:55],a[152:155],v[80:83],a[52:55]
RD v[108:111],v125 offset:192
TR v[100:101],v124 offset:8192
MM a[44:47],a[152:155],v[72:75],a[44:47]
TR v[102:103],v124 offset:8448
RD v[112:115],v125 offset:256
MM a[44:47],a[156:159],v[76:79],a[44:47]
EX v44,v44
MM a[36:39],a[156:159],v[68:71],a[36:39]
EX v45,v45
MM a[36:39],a[152:155],v[64:67],a[36:39]
CV v12,v40,v41
CV v13,v42,v43
MM a[64:67],a[160:163],v[64:67],a[64:67]
EX v46,v46
MM a[64:67],a[164:167],v[68:71],a[64:67]
EX v47,v47
MM a[72:75],a[164:167],v[76:79],a[72:75]
buffer_load_dwordx4 v231, s[8:11], s64 offen offset:3072 lds
MM a[72:75],a[160:163],v[72:75],a[72:75]
s_add_u32 m0, s65, 81920
RD a[220:223],v227 offset:53248
CV v14,v44,v45
MM a[80:83],a[160:163],v[80:83],a[80:83]
s_waitcnt lgkmcnt(1)
EX v48,v48
MM a[80:83],a[164:167],v[84:87],a[80:83]
EX v49,v49
MM v[120:123],v[96:99],v[108:111],v[120:123]
MM v[120:123],v[100:103],v[112:115],v[120:123]
MM a[88:91],a[164:167],v[92:95],a[88:91]
RD a[224:227],v224 offset:57344
CV v15,v46,v47
MM a[88:91],a[160:163],v[88:91],a[88:91]
EX v50,v50
MM a[92:95],a[168:171],v[88:91],a[92:95]
EX v51,v51
MM a[92:95],a[172:175],v[92:95],a[92:95]
buffer_load_dwordx4 v235, s[12:15], s81 offen offset:0 lds
MM a[84:87],a[172:175],v[84:87],a[84:87]
RD a[228:231],v225 offset:57344
CV v20,v48,v49
MM a[84:87],a[168:171],v[80:83],a[84:87]
EX v52,v52
MM a[76:79],a[168:171],v[72:75],a[76:79]
s_mov_b32 exec_lo, 0x00ff00ff
s_mov_b32 exec_hi, 0x00ff00ff
buffer_store_dwordx4 v[120:123], v126, s[20:23], s76 offen offset:0 sc1
s_mov_b64 exec, -1
s_nop 0
MM a[76:79],a[172:175],v[76:79],a[76:79]
s_lshl_b32 s76, s50, 2
EX v53,v53
MM a[68:71],a[172:175],v[68:71],a[68:71]
s_add_u32 s76, s76, s84
RD a[232:235],v226 offset:57344
CV v21,v50,v51
MM a[68:71],a[168:171],v[64:67],a[68:71]
s_lshl_b32 s76, s76, 3
EX v54,v54
MM a[96:99],a[176:179],v[64:67],a[96:99]
s_lshr_b32 s77, s49, 2
EX v55,v55
MM a[96:99],a[180:183],v[68:71],a[96:99]
s_add_u32 s76, s76, s77
RD a[236:239],v227 offset:57344
CV v22,v52,v53
MM a[104:107],a[180:183],v[76:79],a[104:107]
s_mul_i32 s95, s76, 82944
EX v56,v56
MM a[104:107],a[176:179],v[72:75],a[104:107]
s_mul_i32 s76, %[swave], 20480
buffer_load_dwordx4 v235, s[12:15], s64 offen offset:1024 lds
MM a[112:115],a[176:179],v[80:83],a[112:115]
s_add_u32 s95, s95, s76
EX v57,v57
MM a[112:115],a[180:183],v[84:87],a[112:115]
s_add_u32 s95, s95, 22708224
v_mov_b32 v240, -1
v_mov_b32 v241, -1
MM a[120:123],a[180:183],v[92:95],a[120:123]
v_mov_b32 v242, -1
v_mov_b32 v243, -1
MM a[120:123],a[176:179],v[88:91],a[120:123]
EX v58,v58
MM a[124:127],a[184:187],v[88:91],a[124:127]
RD a[240:243],v224 offset:61440
CV v23,v54,v55
MM a[124:127],a[188:191],v[92:95],a[124:127]
EX v59,v59
MM a[116:119],a[188:191],v[84:87],a[116:119]
EX v60,v60
MM a[116:119],a[184:187],v[80:83],a[116:119]
RD a[244:247],v225 offset:61440
CV v28,v56,v57
MM a[108:111],a[184:187],v[72:75],a[108:111]
EX v61,v61
MM a[108:111],a[188:191],v[76:79],a[108:111]
buffer_load_dwordx4 v235, s[12:15], s81 offen offset:2048 lds
MM a[100:103],a[188:191],v[68:71],a[100:103]
EX v62,v62
MM a[100:103],a[184:187],v[64:67],a[100:103]
RD a[248:251],v226 offset:61440
CV v29,v58,v59
v_smfmac_f32_16x16x64_bf16 v[236:239], v[144:147], v[0:7], v234
RD a[252:255],v227 offset:61440
EX v63,v63
v_smfmac_f32_16x16x64_bf16 v[236:239], v[148:151], v[8:15], v234
CV v30,v60,v61
v_smfmac_f32_16x16x64_bf16 v[236:239], v[152:155], v[16:23], v234
CV v31,v62,v63
s_nop 1
v_smfmac_f32_16x16x64_bf16 v[236:239], v[156:159], v[24:31], v234
s_waitcnt lgkmcnt(0)
buffer_load_dwordx4 v235, s[12:15], s64 offen offset:3072 lds
MM v[64:67],a[192:195],v[160:163],v[128:131]
s_add_u32 s64, s64, 16384
TR a[128:129],v232 offset:32768
s_add_u32 s81, s64, 896
MM v[64:67],a[196:199],v[176:179],v[64:67]
TR a[130:131],v232 offset:36864
TR a[132:133],v232 offset:40960
MM v[64:67],a[200:203],v[192:195],v[64:67]
TR a[134:135],v232 offset:45056
TR a[136:137],v233 offset:32768
MM v[64:67],a[204:207],v[208:211],v[64:67]
TR a[138:139],v233 offset:36864
v_add_f32 v246, v236, v237
MM v[68:71],a[220:223],v[208:211],v[128:131]
TR a[140:141],v233 offset:40960
v_cmp_nge_f32 vcc, s71, v246
MM v[68:71],a[216:219],v[192:195],v[68:71]
s_cbranch_vccnz LSITE14_%=
LBACK14_%=:
TR a[142:143],v233 offset:45056
TR a[144:145],v232 offset:33280
MM v[68:71],a[212:215],v[176:179],v[68:71]
s_lshr_b32 s77, %[sb], 3
TR a[146:147],v232 offset:37376
TR a[148:149],v232 offset:41472
MM v[68:71],a[208:211],v[160:163],v[68:71]
s_lshl_b32 s77, s77, 2
TR a[150:151],v232 offset:45568
TR a[152:153],v233 offset:33280
MM v[76:79],a[208:211],v[164:167],v[132:135]
s_add_u32 s77, s77, %[swave]
EX v64,v64
MM v[76:79],a[212:215],v[180:183],v[76:79]
s_lshl_b32 s77, s77, 9
EX v65,v65
MM v[76:79],a[216:219],v[196:199],v[76:79]
s_add_u32 s77, s77, 0
v_lshrrev_b32 v253, 1, v248
v_add_u32 v253, s77, v253
MM v[76:79],a[220:223],v[212:215],v[76:79]
s_mov_b32 s77, LKEND_ka-LPD0_%=-16
v_min_u32 v253, s77, v253
CV v64,v64,v65
MM v[72:75],a[204:207],v[212:215],v[132:135]
s_getpc_b64 s[98:99]
LPD0_%=:
global_load_dword v253, v253, s[98:99]
MM v[72:75],a[200:203],v[196:199],v[72:75]
EX v66,v66
MM v[72:75],a[196:199],v[180:183],v[72:75]
EX v67,v67
MM v[72:75],a[192:195],v[164:167],v[72:75]
TR a[154:155],v233 offset:37376
CV v65,v66,v67
MM v[80:83],a[192:195],v[168:171],v[136:139]
EX v68,v68
MM v[80:83],a[196:199],v[184:187],v[80:83]
EX v69,v69
MM v[80:83],a[200:203],v[200:203],v[80:83]
TR a[156:157],v233 offset:41472
CV v66,v68,v69
MM v[80:83],a[204:207],v[216:219],v[80:83]
EX v70,v70
MM v[84:87],a[220:223],v[216:219],v[136:139]
EX v71,v71
MM v[84:87],a[216:219],v[200:203],v[84:87]
TR a[158:159],v233 offset:45568
CV v67,v70,v71
MM v[84:87],a[212:215],v[184:187],v[84:87]
EX v72,v72
MM v[84:87],a[208:211],v[168:171],v[84:87]
EX v73,v73
MM v[92:95],a[208:211],v[172:175],v[140:143]
TR a[160:161],v232 offset:33792
CV v72,v72,v73
MM v[92:95],a[212:215],v[188:191],v[92:95]
EX v74,v74
MM v[92:95],a[216:219],v[204:207],v[92:95]
TR a[162:163],v232 offset:37888
TR a[164:165],v232 offset:41984
MM v[92:95],a[220:223],v[220:223],v[92:95]
EX v75,v75
MM v[88:91],a[204:207],v[220:223],v[140:143]
TR a[166:167],v232 offset:46080
CV v73,v74,v75
MM v[88:91],a[200:203],v[204:207],v[88:91]
EX v76,v76
MM v[88:91],a[196:199],v[188:191],v[88:91]
EX v77,v77
MM v[88:91],a[192:195],v[172:175],v[88:91]
TR a[168:169],v233 offset:33792
CV v74,v76,v77
MM v[96:99],a[224:227],v[160:163],v[128:131]
EX v78,v78
MM v[96:99],a[228:231],v[176:179],v[96:99]
TR a[170:171],v233 offset:37888
TR a[172:173],v233 offset:41984
MM v[96:99],a[232:235],v[192:195],v[96:99]
EX v79,v79
MM v[96:99],a[236:239],v[208:211],v[96:99]
TR a[174:175],v233 offset:46080
CV v75,v78,v79
MM v[100:103],a[252:255],v[208:211],v[128:131]
EX v80,v80
MM v[100:103],a[248:251],v[192:195],v[100:103]
EX v81,v81
MM v[100:103],a[244:247],v[176:179],v[100:103]
TR a[176:177],v232 offset:34304
CV v80,v80,v81
MM v[100:103],a[240:243],v[160:163],v[100:103]
EX v82,v82
MM v[108:111],a[240:243],v[164:167],v[132:135]
s_add_u32 s67, s95, 10240
buffer_store_dwordx4 v[240:243], v248, s[20:23], s67 offen offset:0 sc1
MM v[108:111],a[244:247],v[180:183],v[108:111]
TR a[178:179],v232 offset:38400
TR a[180:181],v232 offset:42496
MM v[108:111],a[248:251],v[196:199],v[108:111]
EX v83,v83
MM v[108:111],a[252:255],v[212:215],v[108:111]
TR a[182:183],v232 offset:46592
CV v81,v82,v83
MM v[104:107],a[236:239],v[212:215],v[132:135]
EX v84,v84
MM v[104:107],a[232:235],v[196:199],v[104:107]
EX v85,v85
MM v[104:107],a[228:231],v[180:183],v[104:107]
TR a[184:185],v233 offset:34304
CV v82,v84,v85
MM v[104:107],a[224:227],v[164:167],v[104:107]
EX v86,v86
MM v[112:115],a[224:227],v[168:171],v[136:139]
TR a[186:187],v233 offset:38400
TR a[188:189],v233 offset:42496
MM v[112:115],a[228:231],v[184:187],v[112:115]
EX v87,v87
MM v[112:115],a[232:235],v[200:203],v[112:115]
TR a[190:191],v233 offset:46592
CV v83,v86,v87
MM v[112:115],a[236:239],v[216:219],v[112:115]
EX v88,v88
MM v[116:119],a[252:255],v[216:219],v[136:139]
EX v89,v89
MM v[116:119],a[248:251],v[200:203],v[116:119]
EX v90,v90
MM v[116:119],a[244:247],v[184:187],v[116:119]
EX v91,v91
MM v[116:119],a[240:243],v[168:171],v[116:119]
EX v92,v92
MM v[124:127],a[240:243],v[172:175],v[140:143]
EX v93,v93
MM v[124:127],a[244:247],v[188:191],v[124:127]
EX v94,v94
MM v[124:127],a[248:251],v[204:207],v[124:127]
EX v95,v95
MM v[124:127],a[252:255],v[220:223],v[124:127]
EX v96,v96
MM v[120:123],a[236:239],v[220:223],v[140:143]
EX v97,v97
MM v[120:123],a[232:235],v[204:207],v[120:123]
EX v98,v98
MM v[120:123],a[228:231],v[188:191],v[120:123]
EX v99,v99
MM v[120:123],a[224:227],v[172:175],v[120:123]
s_waitcnt vmcnt(15) lgkmcnt(0)
s_barrier
MM a[0:3],a[128:131],v[0:3],a[0:3]
s_add_u32 m0, s65, 32768
CV v88,v88,v89
CV v89,v90,v91
MM a[0:3],a[132:135],v[4:7],a[0:3]
RD a[192:195],v224 offset:0
CV v90,v92,v93
MM a[8:11],a[132:135],v[12:15],a[8:11]
RD a[196:199],v225 offset:0
CV v91,v94,v95
MM a[8:11],a[128:131],v[8:11],a[8:11]
buffer_load_dwordx4 v228, s[8:11], s64 offen offset:0 lds
MM a[16:19],a[128:131],v[16:19],a[16:19]
RD a[200:203],v226 offset:0
CV v68,v96,v97
MM a[16:19],a[132:135],v[20:23],a[16:19]
RD a[204:207],v227 offset:0
CV v69,v98,v99
MM a[24:27],a[132:135],v[28:31],a[24:27]
buffer_load_dwordx4 v229, s[8:11], s64 offen offset:1024 lds
MM a[24:27],a[128:131],v[24:27],a[24:27]
)ASM"
R"ASM(
RD a[208:211],v224 offset:4096
RD a[212:215],v225 offset:4096
MM a[28:31],a[136:139],v[24:27],a[28:31]
buffer_load_dwordx4 v230, s[8:11], s64 offen offset:2048 lds
MM a[28:31],a[140:143],v[28:31],a[28:31]
EX v100,v100
MM a[20:23],a[140:143],v[20:23],a[20:23]
EX v101,v101
MM a[20:23],a[136:139],v[16:19],a[20:23]
RD a[216:219],v226 offset:4096
CV v70,v100,v101
MM a[12:15],a[136:139],v[8:11],a[12:15]
EX v102,v102
MM a[12:15],a[140:143],v[12:15],a[12:15]
RD a[220:223],v227 offset:4096
RD a[224:227],v224 offset:8192
MM a[4:7],a[140:143],v[4:7],a[4:7]
EX v103,v103
MM a[4:7],a[136:139],v[0:3],a[4:7]
RD a[228:231],v225 offset:8192
CV v71,v102,v103
MM a[32:35],a[144:147],v[0:3],a[32:35]
EX v104,v104
MM a[32:35],a[148:151],v[4:7],a[32:35]
buffer_load_dwordx4 v231, s[8:11], s64 offen offset:3072 lds
s_nop 0
LPG10_%=:
s_cbranch_execz LTR0_%=
MM a[40:43],a[148:151],v[12:15],a[40:43]
s_add_u32 m0, s65, 98304
EX v105,v105
MM a[40:43],a[144:147],v[8:11],a[40:43]
RD a[232:235],v226 offset:8192
CV v76,v104,v105
MM a[48:51],a[144:147],v[16:19],a[48:51]
EX v106,v106
MM a[48:51],a[148:151],v[20:23],a[48:51]
EX v107,v107
MM a[56:59],a[148:151],v[28:31],a[56:59]
RD a[236:239],v227 offset:8192
CV v77,v106,v107
MM a[56:59],a[144:147],v[24:27],a[56:59]
EX v108,v108
MM a[60:63],a[152:155],v[24:27],a[60:63]
buffer_load_dwordx4 v235, s[12:15], s81 offen offset:0 lds
MM a[60:63],a[156:159],v[28:31],a[60:63]
EX v109,v109
MM a[52:55],a[156:159],v[20:23],a[52:55]
RD a[240:243],v224 offset:12288
CV v78,v108,v109
MM a[52:55],a[152:155],v[16:19],a[52:55]
EX v110,v110
MM a[44:47],a[152:155],v[8:11],a[44:47]
EX v111,v111
MM a[44:47],a[156:159],v[12:15],a[44:47]
RD a[244:247],v225 offset:12288
CV v79,v110,v111
MM a[36:39],a[156:159],v[4:7],a[36:39]
buffer_load_dwordx4 v235, s[12:15], s64 offen offset:1024 lds
MM a[36:39],a[152:155],v[0:3],a[36:39]
EX v112,v112
MM a[64:67],a[160:163],v[0:3],a[64:67]
EX v113,v113
MM a[64:67],a[164:167],v[4:7],a[64:67]
RD a[248:251],v226 offset:12288
CV v84,v112,v113
MM a[72:75],a[164:167],v[12:15],a[72:75]
EX v114,v114
MM a[72:75],a[160:163],v[8:11],a[72:75]
EX v115,v115
MM a[80:83],a[160:163],v[16:19],a[80:83]
RD a[252:255],v227 offset:12288
CV v85,v114,v115
MM a[80:83],a[164:167],v[20:23],a[80:83]
buffer_load_dwordx4 v235, s[12:15], s81 offen offset:2048 lds
MM a[88:91],a[164:167],v[28:31],a[88:91]
EX v116,v116
MM a[88:91],a[160:163],v[24:27],a[88:91]
EX v117,v117
MM a[92:95],a[168:171],v[24:27],a[92:95]
EX v118,v118
MM a[92:95],a[172:175],v[28:31],a[92:95]
buffer_load_dwordx4 v235, s[12:15], s64 offen offset:3072 lds
MM a[84:87],a[172:175],v[20:23],a[84:87]
EX v119,v119
MM a[84:87],a[168:171],v[16:19],a[84:87]
EX v120,v120
MM a[76:79],a[168:171],v[8:11],a[76:79]
EX v121,v121
MM a[76:79],a[172:175],v[12:15],a[76:79]
EX v122,v122
MM a[68:71],a[172:175],v[4:7],a[68:71]
EX v123,v123
MM a[68:71],a[168:171],v[0:3],a[68:71]
EX v124,v124
MM a[96:99],a[176:179],v[0:3],a[96:99]
EX v125,v125
MM a[96:99],a[180:183],v[4:7],a[96:99]
EX v126,v126
MM a[104:107],a[180:183],v[12:15],a[104:107]
EX v127,v127
MM a[104:107],a[176:179],v[8:11],a[104:107]
CV v86,v116,v117
CV v87,v118,v119
MM a[112:115],a[176:179],v[16:19],a[112:115]
CV v92,v120,v121
CV v93,v122,v123
MM a[112:115],a[180:183],v[20:23],a[112:115]
CV v94,v124,v125
CV v95,v126,v127
MM a[120:123],a[180:183],v[28:31],a[120:123]
MM a[120:123],a[176:179],v[24:27],a[120:123]
MM a[124:127],a[184:187],v[24:27],a[124:127]
MM a[124:127],a[188:191],v[28:31],a[124:127]
MM a[116:119],a[188:191],v[20:23],a[116:119]
MM a[116:119],a[184:187],v[16:19],a[116:119]
MM a[108:111],a[184:187],v[8:11],a[108:111]
MM a[108:111],a[188:191],v[12:15],a[108:111]
MM a[100:103],a[188:191],v[4:7],a[100:103]
MM a[100:103],a[184:187],v[0:3],a[100:103]
v_smfmac_f32_16x16x64_bf16 v[236:239], v[144:147], v[64:71], v234
v_smfmac_f32_16x16x64_bf16 v[236:239], v[148:151], v[72:79], v234
v_smfmac_f32_16x16x64_bf16 v[236:239], v[152:155], v[80:87], v234
v_smfmac_f32_16x16x64_bf16 v[236:239], v[156:159], v[88:95], v234
s_waitcnt lgkmcnt(0)
MM v[0:3],a[192:195],v[160:163],v[128:131]
s_add_u32 s64, s64, 16384
TR a[128:129],v232 offset:49152
s_add_u32 s81, s64, 896
MM v[0:3],a[196:199],v[176:179],v[0:3]
TR a[130:131],v232 offset:53248
TR a[132:133],v232 offset:57344
MM v[0:3],a[200:203],v[192:195],v[0:3]
TR a[134:135],v232 offset:61440
TR a[136:137],v233 offset:49152
MM v[0:3],a[204:207],v[208:211],v[0:3]
TR a[138:139],v233 offset:53248
v_add_f32 v247, v236, v237
MM v[4:7],a[220:223],v[208:211],v[128:131]
TR a[140:141],v233 offset:57344
v_cmp_nge_f32 vcc, s71, v247
MM v[4:7],a[216:219],v[192:195],v[4:7]
s_cbranch_vccnz LSITE15_%=
LBACK15_%=:
TR a[142:143],v233 offset:61440
TR a[144:145],v232 offset:49664
MM v[4:7],a[212:215],v[176:179],v[4:7]
s_add_u32 s67, s95, 11264
buffer_store_dwordx4 v[240:243], v248, s[20:23], s67 offen offset:0 sc1
MM v[4:7],a[208:211],v[160:163],v[4:7]
TR a[146:147],v232 offset:53760
TR a[148:149],v232 offset:57856
MM v[12:15],a[208:211],v[164:167],v[132:135]
EX v0,v0
MM v[12:15],a[212:215],v[180:183],v[12:15]
EX v1,v1
MM v[12:15],a[216:219],v[196:199],v[12:15]
TR a[150:151],v232 offset:61952
CV v0,v0,v1
MM v[12:15],a[220:223],v[212:215],v[12:15]
EX v2,v2
MM v[8:11],a[204:207],v[212:215],v[132:135]
EX v3,v3
MM v[8:11],a[200:203],v[196:199],v[8:11]
TR a[152:153],v233 offset:49664
CV v1,v2,v3
MM v[8:11],a[196:199],v[180:183],v[8:11]
EX v4,v4
MM v[8:11],a[192:195],v[164:167],v[8:11]
EX v5,v5
MM v[16:19],a[192:195],v[168:171],v[136:139]
TR a[154:155],v233 offset:53760
CV v2,v4,v5
MM v[16:19],a[196:199],v[184:187],v[16:19]
EX v6,v6
MM v[16:19],a[200:203],v[200:203],v[16:19]
EX v7,v7
MM v[16:19],a[204:207],v[216:219],v[16:19]
TR a[156:157],v233 offset:57856
CV v3,v6,v7
MM v[20:23],a[220:223],v[216:219],v[136:139]
EX v8,v8
MM v[20:23],a[216:219],v[200:203],v[20:23]
TR a[158:159],v233 offset:61952
TR a[160:161],v232 offset:50176
MM v[20:23],a[212:215],v[184:187],v[20:23]
EX v9,v9
MM v[20:23],a[208:211],v[168:171],v[20:23]
TR a[162:163],v232 offset:54272
CV v8,v8,v9
MM v[28:31],a[208:211],v[172:175],v[140:143]
EX v10,v10
MM v[28:31],a[212:215],v[188:191],v[28:31]
TR a[164:165],v232 offset:58368
TR a[166:167],v232 offset:62464
MM v[28:31],a[216:219],v[204:207],v[28:31]
EX v11,v11
MM v[28:31],a[220:223],v[220:223],v[28:31]
TR a[168:169],v233 offset:50176
CV v9,v10,v11
MM v[24:27],a[204:207],v[220:223],v[140:143]
EX v12,v12
MM v[24:27],a[200:203],v[204:207],v[24:27]
EX v13,v13
MM v[24:27],a[196:199],v[188:191],v[24:27]
TR a[170:171],v233 offset:54272
CV v10,v12,v13
MM v[24:27],a[192:195],v[172:175],v[24:27]
EX v14,v14
MM v[32:35],a[224:227],v[160:163],v[128:131]
TR a[172:173],v233 offset:58368
TR a[174:175],v233 offset:62464
MM v[32:35],a[228:231],v[176:179],v[32:35]
EX v15,v15
MM v[32:35],a[232:235],v[192:195],v[32:35]
TR a[176:177],v232 offset:50688
CV v11,v14,v15
MM v[32:35],a[236:239],v[208:211],v[32:35]
EX v16,v16
MM v[36:39],a[252:255],v[208:211],v[128:131]
EX v17,v17
MM v[36:39],a[248:251],v[192:195],v[36:39]
TR a[178:179],v232 offset:54784
CV v16,v16,v17
MM v[36:39],a[244:247],v[176:179],v[36:39]
EX v18,v18
MM v[36:39],a[240:243],v[160:163],v[36:39]
TR a[180:181],v232 offset:58880
TR a[182:183],v232 offset:62976
MM v[44:47],a[240:243],v[164:167],v[132:135]
s_add_u32 s67, s95, 12288
buffer_store_dwordx4 v[240:243], v248, s[20:23], s67 offen offset:0 sc1
MM v[44:47],a[244:247],v[180:183],v[44:47]
EX v19,v19
MM v[44:47],a[248:251],v[196:199],v[44:47]
TR a[184:185],v233 offset:50688
CV v17,v18,v19
MM v[44:47],a[252:255],v[212:215],v[44:47]
EX v20,v20
MM v[40:43],a[236:239],v[212:215],v[132:135]
EX v21,v21
MM v[40:43],a[232:235],v[196:199],v[40:43]
TR a[186:187],v233 offset:54784
CV v18,v20,v21
MM v[40:43],a[228:231],v[180:183],v[40:43]
EX v22,v22
MM v[40:43],a[224:227],v[164:167],v[40:43]
TR a[188:189],v233 offset:58880
TR a[190:191],v233 offset:62976
MM v[48:51],a[224:227],v[168:171],v[136:139]
EX v23,v23
MM v[48:51],a[228:231],v[184:187],v[48:51]
CV v19,v22,v23
MM v[48:51],a[232:235],v[200:203],v[48:51]
EX v24,v24
MM v[48:51],a[236:239],v[216:219],v[48:51]
EX v25,v25
MM v[52:55],a[252:255],v[216:219],v[136:139]
EX v26,v26
MM v[52:55],a[248:251],v[200:203],v[52:55]
EX v27,v27
MM v[52:55],a[244:247],v[184:187],v[52:55]
EX v28,v28
MM v[52:55],a[240:243],v[168:171],v[52:55]
EX v29,v29
MM v[60:63],a[240:243],v[172:175],v[140:143]
EX v30,v30
MM v[60:63],a[244:247],v[188:191],v[60:63]
EX v31,v31
MM v[60:63],a[248:251],v[204:207],v[60:63]
EX v32,v32
MM v[60:63],a[252:255],v[220:223],v[60:63]
EX v33,v33
MM v[56:59],a[236:239],v[220:223],v[140:143]
EX v34,v34
MM v[56:59],a[232:235],v[204:207],v[56:59]
EX v35,v35
MM v[56:59],a[228:231],v[188:191],v[56:59]
EX v36,v36
MM v[56:59],a[224:227],v[172:175],v[56:59]
s_waitcnt vmcnt(17) lgkmcnt(0)
s_barrier
MM a[0:3],a[128:131],v[64:67],a[0:3]
s_lshr_b32 s91, s48, 7
CV v24,v24,v25
CV v25,v26,v27
MM a[0:3],a[132:135],v[68:71],a[0:3]
s_lshl_b32 s91, s91, 5
RD a[192:195],v224 offset:16384
CV v26,v28,v29
MM a[8:11],a[132:135],v[76:79],a[8:11]
s_and_b32 s77, s48, 16
s_add_u32 m0, s65, 49152
CV v27,v30,v31
MM a[8:11],a[128:131],v[72:75],a[8:11]
s_add_u32 s91, s91, s77
buffer_load_dwordx4 v228, s[8:11], s64 offen offset:0 lds
MM a[16:19],a[128:131],v[80:83],a[16:19]
s_bfe_u32 s77, s48, 131077
RD a[196:199],v225 offset:16384
CV v4,v32,v33
MM a[16:19],a[132:135],v[84:87],a[16:19]
s_lshl_b32 s77, s77, 2
RD a[200:203],v226 offset:16384
CV v5,v34,v35
MM a[24:27],a[132:135],v[92:95],a[24:27]
s_add_u32 s91, s91, s77
RD a[204:207],v227 offset:16384
MM a[24:27],a[128:131],v[88:91],a[24:27]
s_add_u32 s91, s91, %[swave]
buffer_load_dwordx4 v229, s[8:11], s64 offen offset:1024 lds
MM a[28:31],a[136:139],v[88:91],a[28:31]
s_lshl_b32 s91, s91, 2
RD a[208:211],v224 offset:20480
MM a[28:31],a[140:143],v[92:95],a[28:31]
s_add_u32 s91, s91, 77824
RD a[212:215],v225 offset:20480
RD a[216:219],v226 offset:20480
MM a[20:23],a[140:143],v[84:87],a[20:23]
s_waitcnt vmcnt(17)
v_mov_b32 v250, s69
s_mov_b64 exec, 1
buffer_store_dword v250, off, s[20:23], s91 offset:0 sc1
s_mov_b64 exec, -1
MM a[20:23],a[136:139],v[80:83],a[20:23]
EX v37,v37
MM a[12:15],a[136:139],v[72:75],a[12:15]
RD a[220:223],v227 offset:20480
CV v6,v36,v37
MM a[12:15],a[140:143],v[76:79],a[12:15]
buffer_load_dwordx4 v230, s[8:11], s64 offen offset:2048 lds
MM a[4:7],a[140:143],v[68:71],a[4:7]
EX v38,v38
MM a[4:7],a[136:139],v[64:67],a[4:7]
EX v39,v39
MM a[32:35],a[144:147],v[64:67],a[32:35]
RD a[224:227],v224 offset:24576
CV v7,v38,v39
MM a[32:35],a[148:151],v[68:71],a[32:35]
EX v40,v40
MM a[40:43],a[148:151],v[76:79],a[40:43]
EX v41,v41
MM a[40:43],a[144:147],v[72:75],a[40:43]
RD a[228:231],v225 offset:24576
CV v12,v40,v41
MM a[48:51],a[144:147],v[80:83],a[48:51]
buffer_load_dwordx4 v231, s[8:11], s64 offen offset:3072 lds
MM a[48:51],a[148:151],v[84:87],a[48:51]
s_add_u32 m0, s65, 114688
EX v42,v42
MM a[56:59],a[148:151],v[92:95],a[56:59]
EX v43,v43
MM a[56:59],a[144:147],v[88:91],a[56:59]
RD a[232:235],v226 offset:24576
CV v13,v42,v43
MM a[60:63],a[152:155],v[88:91],a[60:63]
EX v44,v44
MM a[60:63],a[156:159],v[92:95],a[60:63]
buffer_load_dwordx4 v235, s[12:15], s81 offen offset:0 lds
MM a[52:55],a[156:159],v[84:87],a[52:55]
EX v45,v45
MM a[52:55],a[152:155],v[80:83],a[52:55]
RD a[236:239],v227 offset:24576
CV v14,v44,v45
MM a[44:47],a[152:155],v[72:75],a[44:47]
EX v46,v46
MM a[44:47],a[156:159],v[76:79],a[44:47]
EX v47,v47
MM a[36:39],a[156:159],v[68:71],a[36:39]
RD a[240:243],v224 offset:28672
CV v15,v46,v47
MM a[36:39],a[152:155],v[64:67],a[36:39]
EX v48,v48
MM a[64:67],a[160:163],v[64:67],a[64:67]
buffer_load_dwordx4 v235, s[12:15], s64 offen offset:1024 lds
MM a[64:67],a[164:167],v[68:71],a[64:67]
EX v49,v49
MM a[72:75],a[164:167],v[76:79],a[72:75]
RD a[244:247],v225 offset:28672
CV v20,v48,v49
MM a[72:75],a[160:163],v[72:75],a[72:75]
EX v50,v50
MM a[80:83],a[160:163],v[80:83],a[80:83]
EX v51,v51
MM a[80:83],a[164:167],v[84:87],a[80:83]
RD a[248:251],v226 offset:28672
CV v21,v50,v51
MM a[88:91],a[164:167],v[92:95],a[88:91]
EX v52,v52
MM a[88:91],a[160:163],v[88:91],a[88:91]
buffer_load_dwordx4 v235, s[12:15], s81 offen offset:2048 lds
)ASM"
R"ASM(
MM a[92:95],a[168:171],v[88:91],a[92:95]
EX v53,v53
MM a[92:95],a[172:175],v[92:95],a[92:95]
RD a[252:255],v227 offset:28672
CV v22,v52,v53
MM a[84:87],a[172:175],v[84:87],a[84:87]
EX v54,v54
MM a[84:87],a[168:171],v[80:83],a[84:87]
EX v55,v55
MM a[76:79],a[168:171],v[72:75],a[76:79]
CV v23,v54,v55
MM a[76:79],a[172:175],v[76:79],a[76:79]
EX v56,v56
MM a[68:71],a[172:175],v[68:71],a[68:71]
buffer_load_dwordx4 v235, s[12:15], s64 offen offset:3072 lds
MM a[68:71],a[168:171],v[64:67],a[68:71]
EX v57,v57
MM a[96:99],a[176:179],v[64:67],a[96:99]
EX v58,v58
MM a[96:99],a[180:183],v[68:71],a[96:99]
EX v59,v59
MM a[104:107],a[180:183],v[76:79],a[104:107]
EX v60,v60
MM a[104:107],a[176:179],v[72:75],a[104:107]
EX v61,v61
MM a[112:115],a[176:179],v[80:83],a[112:115]
EX v62,v62
MM a[112:115],a[180:183],v[84:87],a[112:115]
EX v63,v63
MM a[120:123],a[180:183],v[92:95],a[120:123]
CV v28,v56,v57
CV v29,v58,v59
MM a[120:123],a[176:179],v[88:91],a[120:123]
CV v30,v60,v61
CV v31,v62,v63
MM a[124:127],a[184:187],v[88:91],a[124:127]
MM a[124:127],a[188:191],v[92:95],a[124:127]
MM a[116:119],a[188:191],v[84:87],a[116:119]
MM a[116:119],a[184:187],v[80:83],a[116:119]
MM a[108:111],a[184:187],v[72:75],a[108:111]
MM a[108:111],a[188:191],v[76:79],a[108:111]
MM a[100:103],a[188:191],v[68:71],a[100:103]
MM a[100:103],a[184:187],v[64:67],a[100:103]
v_smfmac_f32_16x16x64_bf16 v[236:239], v[144:147], v[0:7], v234
v_smfmac_f32_16x16x64_bf16 v[236:239], v[148:151], v[8:15], v234
v_smfmac_f32_16x16x64_bf16 v[236:239], v[152:155], v[16:23], v234
v_smfmac_f32_16x16x64_bf16 v[236:239], v[156:159], v[24:31], v234
s_waitcnt lgkmcnt(0)
MM v[64:67],a[192:195],v[160:163],v[128:131]
s_add_u32 s64, s64, 16384
TR a[128:129],v232 offset:0
s_add_u32 s81, s64, 896
MM v[64:67],a[196:199],v[176:179],v[64:67]
TR a[130:131],v232 offset:4096
TR a[132:133],v232 offset:8192
MM v[64:67],a[200:203],v[192:195],v[64:67]
TR a[134:135],v232 offset:12288
TR a[136:137],v233 offset:0
MM v[64:67],a[204:207],v[208:211],v[64:67]
TR a[138:139],v233 offset:4096
v_add_f32 v246, v236, v237
MM v[68:71],a[220:223],v[208:211],v[128:131]
TR a[140:141],v233 offset:8192
v_cmp_nge_f32 vcc, s71, v246
MM v[68:71],a[216:219],v[192:195],v[68:71]
s_cbranch_vccnz LSITE16_%=
LBACK16_%=:
TR a[142:143],v233 offset:12288
TR a[144:145],v232 offset:512
MM v[68:71],a[212:215],v[176:179],v[68:71]
s_add_u32 s67, s95, 13312
buffer_store_dwordx4 v[240:243], v248, s[20:23], s67 offen offset:0 sc1
MM v[68:71],a[208:211],v[160:163],v[68:71]
TR a[146:147],v232 offset:4608
TR a[148:149],v232 offset:8704
MM v[76:79],a[208:211],v[164:167],v[132:135]
EX v64,v64
MM v[76:79],a[212:215],v[180:183],v[76:79]
EX v65,v65
MM v[76:79],a[216:219],v[196:199],v[76:79]
TR a[150:151],v232 offset:12800
CV v64,v64,v65
MM v[76:79],a[220:223],v[212:215],v[76:79]
EX v66,v66
MM v[72:75],a[204:207],v[212:215],v[132:135]
EX v67,v67
MM v[72:75],a[200:203],v[196:199],v[72:75]
TR a[152:153],v233 offset:512
CV v65,v66,v67
MM v[72:75],a[196:199],v[180:183],v[72:75]
EX v68,v68
MM v[72:75],a[192:195],v[164:167],v[72:75]
EX v69,v69
MM v[80:83],a[192:195],v[168:171],v[136:139]
TR a[154:155],v233 offset:4608
CV v66,v68,v69
MM v[80:83],a[196:199],v[184:187],v[80:83]
EX v70,v70
MM v[80:83],a[200:203],v[200:203],v[80:83]
EX v71,v71
MM v[80:83],a[204:207],v[216:219],v[80:83]
TR a[156:157],v233 offset:8704
CV v67,v70,v71
MM v[84:87],a[220:223],v[216:219],v[136:139]
EX v72,v72
MM v[84:87],a[216:219],v[200:203],v[84:87]
TR a[158:159],v233 offset:12800
TR a[160:161],v232 offset:1024
MM v[84:87],a[212:215],v[184:187],v[84:87]
EX v73,v73
MM v[84:87],a[208:211],v[168:171],v[84:87]
TR a[162:163],v232 offset:5120
CV v72,v72,v73
MM v[92:95],a[208:211],v[172:175],v[140:143]
s_and_b32 s44, s50, 7
EX v74,v74
MM v[92:95],a[212:215],v[188:191],v[92:95]
s_lshl_b32 s44, s44, 2
TR a[164:165],v232 offset:9216
TR a[166:167],v232 offset:13312
MM v[92:95],a[216:219],v[204:207],v[92:95]
s_add_u32 s44, s44, s84
EX v75,v75
MM v[92:95],a[220:223],v[220:223],v[92:95]
s_lshl_b32 s44, s44, 2
TR a[168:169],v233 offset:1024
CV v73,v74,v75
MM v[88:91],a[204:207],v[220:223],v[140:143]
s_add_u32 s44, s44, %[swave]
EX v76,v76
MM v[88:91],a[200:203],v[204:207],v[88:91]
s_mul_i32 s44, s44, 5120
EX v77,v77
MM v[88:91],a[196:199],v[188:191],v[88:91]
s_mul_i32 s45, s84, 320
TR a[170:171],v233 offset:5120
CV v74,v76,v77
MM v[88:91],a[192:195],v[172:175],v[88:91]
s_sub_u32 s44, s44, s45
EX v78,v78
MM v[96:99],a[224:227],v[160:163],v[128:131]
s_lshl_b32 s45, %[swave], 10
TR a[172:173],v233 offset:9216
TR a[174:175],v233 offset:13312
MM v[96:99],a[228:231],v[176:179],v[96:99]
s_add_u32 s45, s45, %[slds]
s_add_u32 m0, s45, 147456
s_add_u32 s44, s44, 0
buffer_load_dwordx4 v248, s[24:27], s44 offen offset:0 lds
MM v[96:99],a[232:235],v[192:195],v[96:99]
EX v79,v79
MM v[96:99],a[236:239],v[208:211],v[96:99]
TR a[176:177],v232 offset:1536
CV v75,v78,v79
MM v[100:103],a[252:255],v[208:211],v[128:131]
EX v80,v80
MM v[100:103],a[248:251],v[192:195],v[100:103]
EX v81,v81
MM v[100:103],a[244:247],v[176:179],v[100:103]
TR a[178:179],v232 offset:5632
CV v80,v80,v81
MM v[100:103],a[240:243],v[160:163],v[100:103]
EX v82,v82
MM v[108:111],a[240:243],v[164:167],v[132:135]
s_add_u32 s67, s95, 14336
buffer_store_dwordx4 v[240:243], v248, s[20:23], s67 offen offset:0 sc1
MM v[108:111],a[244:247],v[180:183],v[108:111]
EX v83,v83
MM v[108:111],a[248:251],v[196:199],v[108:111]
TR a[180:181],v232 offset:9728
CV v81,v82,v83
MM v[108:111],a[252:255],v[212:215],v[108:111]
EX v84,v84
MM v[104:107],a[236:239],v[212:215],v[132:135]
TR a[182:183],v232 offset:13824
TR a[184:185],v233 offset:1536
MM v[104:107],a[232:235],v[196:199],v[104:107]
EX v85,v85
MM v[104:107],a[228:231],v[180:183],v[104:107]
TR a[186:187],v233 offset:5632
CV v82,v84,v85
MM v[104:107],a[224:227],v[164:167],v[104:107]
EX v86,v86
MM v[112:115],a[224:227],v[168:171],v[136:139]
TR a[188:189],v233 offset:9728
TR a[190:191],v233 offset:13824
MM v[112:115],a[228:231],v[184:187],v[112:115]
EX v87,v87
MM v[112:115],a[232:235],v[200:203],v[112:115]
CV v83,v86,v87
MM v[112:115],a[236:239],v[216:219],v[112:115]
EX v88,v88
MM v[116:119],a[252:255],v[216:219],v[136:139]
EX v89,v89
MM v[116:119],a[248:251],v[200:203],v[116:119]
EX v90,v90
MM v[116:119],a[244:247],v[184:187],v[116:119]
EX v91,v91
MM v[116:119],a[240:243],v[168:171],v[116:119]
EX v92,v92
MM v[124:127],a[240:243],v[172:175],v[140:143]
EX v93,v93
MM v[124:127],a[244:247],v[188:191],v[124:127]
EX v94,v94
MM v[124:127],a[248:251],v[204:207],v[124:127]
EX v95,v95
MM v[124:127],a[252:255],v[220:223],v[124:127]
EX v96,v96
MM v[120:123],a[236:239],v[220:223],v[140:143]
EX v97,v97
MM v[120:123],a[232:235],v[204:207],v[120:123]
EX v98,v98
MM v[120:123],a[228:231],v[188:191],v[120:123]
EX v99,v99
MM v[120:123],a[224:227],v[172:175],v[120:123]
s_waitcnt vmcnt(18) lgkmcnt(0)
s_barrier
MM a[0:3],a[128:131],v[0:3],a[0:3]
s_add_u32 m0, s65, 0
CV v88,v88,v89
CV v89,v90,v91
MM a[0:3],a[132:135],v[4:7],a[0:3]
RD a[192:195],v224 offset:32768
CV v90,v92,v93
MM a[8:11],a[132:135],v[12:15],a[8:11]
RD a[196:199],v225 offset:32768
CV v91,v94,v95
MM a[8:11],a[128:131],v[8:11],a[8:11]
buffer_load_dwordx4 v228, s[8:11], s64 offen offset:0 lds
LPG11_%=:
s_cbranch_execz LTR0_%=
MM a[16:19],a[128:131],v[16:19],a[16:19]
RD a[200:203],v226 offset:32768
CV v68,v96,v97
MM a[16:19],a[132:135],v[20:23],a[16:19]
RD a[204:207],v227 offset:32768
CV v69,v98,v99
MM a[24:27],a[132:135],v[28:31],a[24:27]
buffer_load_dwordx4 v229, s[8:11], s64 offen offset:1024 lds
MM a[24:27],a[128:131],v[24:27],a[24:27]
RD a[208:211],v224 offset:36864
RD a[212:215],v225 offset:36864
MM a[28:31],a[136:139],v[24:27],a[28:31]
buffer_load_dwordx4 v230, s[8:11], s64 offen offset:2048 lds
MM a[28:31],a[140:143],v[28:31],a[28:31]
EX v100,v100
MM a[20:23],a[140:143],v[20:23],a[20:23]
EX v101,v101
MM a[20:23],a[136:139],v[16:19],a[20:23]
RD a[216:219],v226 offset:36864
CV v70,v100,v101
MM a[12:15],a[136:139],v[8:11],a[12:15]
EX v102,v102
MM a[12:15],a[140:143],v[12:15],a[12:15]
RD a[220:223],v227 offset:36864
RD a[224:227],v224 offset:40960
MM a[4:7],a[140:143],v[4:7],a[4:7]
EX v103,v103
MM a[4:7],a[136:139],v[0:3],a[4:7]
RD a[228:231],v225 offset:40960
CV v71,v102,v103
MM a[32:35],a[144:147],v[0:3],a[32:35]
EX v104,v104
MM a[32:35],a[148:151],v[4:7],a[32:35]
buffer_load_dwordx4 v231, s[8:11], s64 offen offset:3072 lds
MM a[40:43],a[148:151],v[12:15],a[40:43]
s_add_u32 m0, s65, 65536
EX v105,v105
MM a[40:43],a[144:147],v[8:11],a[40:43]
RD a[232:235],v226 offset:40960
CV v76,v104,v105
MM a[48:51],a[144:147],v[16:19],a[48:51]
EX v106,v106
MM a[48:51],a[148:151],v[20:23],a[48:51]
EX v107,v107
MM a[56:59],a[148:151],v[28:31],a[56:59]
RD a[236:239],v227 offset:40960
CV v77,v106,v107
MM a[56:59],a[144:147],v[24:27],a[56:59]
EX v108,v108
MM a[60:63],a[152:155],v[24:27],a[60:63]
buffer_load_dwordx4 v235, s[12:15], s81 offen offset:0 lds
MM a[60:63],a[156:159],v[28:31],a[60:63]
EX v109,v109
MM a[52:55],a[156:159],v[20:23],a[52:55]
RD a[240:243],v224 offset:45056
CV v78,v108,v109
MM a[52:55],a[152:155],v[16:19],a[52:55]
EX v110,v110
MM a[44:47],a[152:155],v[8:11],a[44:47]
EX v111,v111
MM a[44:47],a[156:159],v[12:15],a[44:47]
RD a[244:247],v225 offset:45056
CV v79,v110,v111
MM a[36:39],a[156:159],v[4:7],a[36:39]
buffer_load_dwordx4 v235, s[12:15], s64 offen offset:1024 lds
MM a[36:39],a[152:155],v[0:3],a[36:39]
EX v112,v112
MM a[64:67],a[160:163],v[0:3],a[64:67]
EX v113,v113
MM a[64:67],a[164:167],v[4:7],a[64:67]
RD a[248:251],v226 offset:45056
CV v84,v112,v113
MM a[72:75],a[164:167],v[12:15],a[72:75]
EX v114,v114
MM a[72:75],a[160:163],v[8:11],a[72:75]
EX v115,v115
MM a[80:83],a[160:163],v[16:19],a[80:83]
RD a[252:255],v227 offset:45056
CV v85,v114,v115
MM a[80:83],a[164:167],v[20:23],a[80:83]
buffer_load_dwordx4 v235, s[12:15], s81 offen offset:2048 lds
MM a[88:91],a[164:167],v[28:31],a[88:91]
EX v116,v116
MM a[88:91],a[160:163],v[24:27],a[88:91]
EX v117,v117
MM a[92:95],a[168:171],v[24:27],a[92:95]
EX v118,v118
MM a[92:95],a[172:175],v[28:31],a[92:95]
buffer_load_dwordx4 v235, s[12:15], s64 offen offset:3072 lds
MM a[84:87],a[172:175],v[20:23],a[84:87]
EX v119,v119
MM a[84:87],a[168:171],v[16:19],a[84:87]
EX v120,v120
MM a[76:79],a[168:171],v[8:11],a[76:79]
EX v121,v121
MM a[76:79],a[172:175],v[12:15],a[76:79]
EX v122,v122
MM a[68:71],a[172:175],v[4:7],a[68:71]
EX v123,v123
MM a[68:71],a[168:171],v[0:3],a[68:71]
EX v124,v124
MM a[96:99],a[176:179],v[0:3],a[96:99]
EX v125,v125
MM a[96:99],a[180:183],v[4:7],a[96:99]
EX v126,v126
MM a[104:107],a[180:183],v[12:15],a[104:107]
EX v127,v127
MM a[104:107],a[176:179],v[8:11],a[104:107]
CV v86,v116,v117
CV v87,v118,v119
MM a[112:115],a[176:179],v[16:19],a[112:115]
CV v92,v120,v121
CV v93,v122,v123
MM a[112:115],a[180:183],v[20:23],a[112:115]
CV v94,v124,v125
CV v95,v126,v127
MM a[120:123],a[180:183],v[28:31],a[120:123]
MM a[120:123],a[176:179],v[24:27],a[120:123]
MM a[124:127],a[184:187],v[24:27],a[124:127]
MM a[124:127],a[188:191],v[28:31],a[124:127]
MM a[116:119],a[188:191],v[20:23],a[116:119]
MM a[116:119],a[184:187],v[16:19],a[116:119]
MM a[108:111],a[184:187],v[8:11],a[108:111]
MM a[108:111],a[188:191],v[12:15],a[108:111]
MM a[100:103],a[188:191],v[4:7],a[100:103]
MM a[100:103],a[184:187],v[0:3],a[100:103]
v_smfmac_f32_16x16x64_bf16 v[236:239], v[144:147], v[64:71], v234
v_smfmac_f32_16x16x64_bf16 v[236:239], v[148:151], v[72:79], v234
v_smfmac_f32_16x16x64_bf16 v[236:239], v[152:155], v[80:87], v234
v_smfmac_f32_16x16x64_bf16 v[236:239], v[156:159], v[88:95], v234
s_waitcnt lgkmcnt(0)
MM v[0:3],a[192:195],v[160:163],v[128:131]
s_add_u32 s64, s64, 16384
TR a[128:129],v232 offset:16384
s_add_u32 s81, s64, 896
MM v[0:3],a[196:199],v[176:179],v[0:3]
TR a[130:131],v232 offset:20480
TR a[132:133],v232 offset:24576
MM v[0:3],a[200:203],v[192:195],v[0:3]
TR a[134:135],v232 offset:28672
TR a[136:137],v233 offset:16384
MM v[0:3],a[204:207],v[208:211],v[0:3]
TR a[138:139],v233 offset:20480
v_add_f32 v247, v236, v237
MM v[4:7],a[220:223],v[208:211],v[128:131]
TR a[140:141],v233 offset:24576
v_cmp_nge_f32 vcc, s71, v247
)ASM"
R"ASM(
MM v[4:7],a[216:219],v[192:195],v[4:7]
s_cbranch_vccnz LSITE17_%=
LBACK17_%=:
TR a[142:143],v233 offset:28672
TR a[144:145],v232 offset:16896
MM v[4:7],a[212:215],v[176:179],v[4:7]
TR a[146:147],v232 offset:20992
TR a[148:149],v232 offset:25088
MM v[4:7],a[208:211],v[160:163],v[4:7]
TR a[150:151],v232 offset:29184
TR a[152:153],v233 offset:16896
MM v[12:15],a[208:211],v[164:167],v[132:135]
EX v0,v0
MM v[12:15],a[212:215],v[180:183],v[12:15]
EX v1,v1
MM v[12:15],a[216:219],v[196:199],v[12:15]
TR a[154:155],v233 offset:20992
CV v0,v0,v1
MM v[12:15],a[220:223],v[212:215],v[12:15]
EX v2,v2
MM v[8:11],a[204:207],v[212:215],v[132:135]
EX v3,v3
MM v[8:11],a[200:203],v[196:199],v[8:11]
TR a[156:157],v233 offset:25088
CV v1,v2,v3
MM v[8:11],a[196:199],v[180:183],v[8:11]
EX v4,v4
MM v[8:11],a[192:195],v[164:167],v[8:11]
EX v5,v5
MM v[16:19],a[192:195],v[168:171],v[136:139]
TR a[158:159],v233 offset:29184
CV v2,v4,v5
MM v[16:19],a[196:199],v[184:187],v[16:19]
EX v6,v6
MM v[16:19],a[200:203],v[200:203],v[16:19]
EX v7,v7
MM v[16:19],a[204:207],v[216:219],v[16:19]
TR a[160:161],v232 offset:17408
CV v3,v6,v7
MM v[20:23],a[220:223],v[216:219],v[136:139]
EX v8,v8
MM v[20:23],a[216:219],v[200:203],v[20:23]
EX v9,v9
MM v[20:23],a[212:215],v[184:187],v[20:23]
TR a[162:163],v232 offset:21504
CV v8,v8,v9
MM v[20:23],a[208:211],v[168:171],v[20:23]
EX v10,v10
MM v[28:31],a[208:211],v[172:175],v[140:143]
s_add_u32 m0, s45, 147456
s_add_u32 s44, s44, 1024
buffer_load_dwordx4 v248, s[24:27], s44 offen offset:0 lds
MM v[28:31],a[212:215],v[188:191],v[28:31]
TR a[164:165],v232 offset:25600
TR a[166:167],v232 offset:29696
MM v[28:31],a[216:219],v[204:207],v[28:31]
EX v11,v11
MM v[28:31],a[220:223],v[220:223],v[28:31]
TR a[168:169],v233 offset:17408
CV v9,v10,v11
MM v[24:27],a[204:207],v[220:223],v[140:143]
EX v12,v12
MM v[24:27],a[200:203],v[204:207],v[24:27]
EX v13,v13
MM v[24:27],a[196:199],v[188:191],v[24:27]
TR a[170:171],v233 offset:21504
CV v10,v12,v13
MM v[24:27],a[192:195],v[172:175],v[24:27]
EX v14,v14
MM v[32:35],a[224:227],v[160:163],v[128:131]
TR a[172:173],v233 offset:25600
TR a[174:175],v233 offset:29696
MM v[32:35],a[228:231],v[176:179],v[32:35]
EX v15,v15
MM v[32:35],a[232:235],v[192:195],v[32:35]
TR a[176:177],v232 offset:17920
CV v11,v14,v15
MM v[32:35],a[236:239],v[208:211],v[32:35]
EX v16,v16
MM v[36:39],a[252:255],v[208:211],v[128:131]
EX v17,v17
MM v[36:39],a[248:251],v[192:195],v[36:39]
TR a[178:179],v232 offset:22016
CV v16,v16,v17
MM v[36:39],a[244:247],v[176:179],v[36:39]
EX v18,v18
MM v[36:39],a[240:243],v[160:163],v[36:39]
TR a[180:181],v232 offset:26112
TR a[182:183],v232 offset:30208
MM v[44:47],a[240:243],v[164:167],v[132:135]
EX v19,v19
MM v[44:47],a[244:247],v[180:183],v[44:47]
TR a[184:185],v233 offset:17920
CV v17,v18,v19
MM v[44:47],a[248:251],v[196:199],v[44:47]
EX v20,v20
MM v[44:47],a[252:255],v[212:215],v[44:47]
EX v21,v21
MM v[40:43],a[236:239],v[212:215],v[132:135]
TR a[186:187],v233 offset:22016
CV v18,v20,v21
MM v[40:43],a[232:235],v[196:199],v[40:43]
EX v22,v22
MM v[40:43],a[228:231],v[180:183],v[40:43]
TR a[188:189],v233 offset:26112
TR a[190:191],v233 offset:30208
MM v[40:43],a[224:227],v[164:167],v[40:43]
EX v23,v23
MM v[48:51],a[224:227],v[168:171],v[136:139]
CV v19,v22,v23
MM v[48:51],a[228:231],v[184:187],v[48:51]
EX v24,v24
MM v[48:51],a[232:235],v[200:203],v[48:51]
EX v25,v25
MM v[48:51],a[236:239],v[216:219],v[48:51]
EX v26,v26
MM v[52:55],a[252:255],v[216:219],v[136:139]
EX v27,v27
MM v[52:55],a[248:251],v[200:203],v[52:55]
EX v28,v28
MM v[52:55],a[244:247],v[184:187],v[52:55]
EX v29,v29
MM v[52:55],a[240:243],v[168:171],v[52:55]
EX v30,v30
MM v[60:63],a[240:243],v[172:175],v[140:143]
EX v31,v31
MM v[60:63],a[244:247],v[188:191],v[60:63]
EX v32,v32
MM v[60:63],a[248:251],v[204:207],v[60:63]
EX v33,v33
MM v[60:63],a[252:255],v[220:223],v[60:63]
EX v34,v34
MM v[56:59],a[236:239],v[220:223],v[140:143]
EX v35,v35
MM v[56:59],a[232:235],v[204:207],v[56:59]
EX v36,v36
MM v[56:59],a[228:231],v[188:191],v[56:59]
EX v37,v37
MM v[56:59],a[224:227],v[172:175],v[56:59]
s_waitcnt vmcnt(16) lgkmcnt(0)
s_barrier
MM a[0:3],a[128:131],v[64:67],a[0:3]
s_lshl_b32 s60, s50, 2
CV v24,v24,v25
CV v25,v26,v27
MM a[0:3],a[132:135],v[68:71],a[0:3]
s_add_u32 s60, s60, %[swave]
RD a[192:195],v224 offset:49152
CV v26,v28,v29
MM a[8:11],a[132:135],v[76:79],a[8:11]
s_lshl_b32 s60, s60, 2
s_add_u32 m0, s65, 16384
CV v27,v30,v31
MM a[8:11],a[128:131],v[72:75],a[8:11]
s_bfe_u32 s62, s49, 65538
buffer_load_dwordx4 v228, s[8:11], s64 offen offset:0 lds
MM a[16:19],a[128:131],v[80:83],a[16:19]
s_add_u32 s93, s60, s62
RD a[196:199],v225 offset:49152
CV v4,v32,v33
MM a[16:19],a[132:135],v[84:87],a[16:19]
s_lshl_b32 s93, s93, 2
RD a[200:203],v226 offset:49152
CV v5,v34,v35
MM a[24:27],a[132:135],v[92:95],a[24:27]
s_lshr_b32 s61, s49, 3
RD a[204:207],v227 offset:49152
MM a[24:27],a[128:131],v[88:91],a[24:27]
s_add_u32 s93, s93, s61
buffer_load_dwordx4 v229, s[8:11], s64 offen offset:1024 lds
MM a[28:31],a[136:139],v[88:91],a[28:31]
s_mul_i32 s18, s93, 5120
RD a[208:211],v224 offset:53248
CV v6,v36,v37
MM a[28:31],a[140:143],v[92:95],a[28:31]
s_mul_i32 s60, s84, 320
RD a[212:215],v225 offset:53248
MM a[20:23],a[140:143],v[84:87],a[20:23]
s_add_u32 s18, s18, s60
buffer_load_dwordx4 v230, s[8:11], s64 offen offset:2048 lds
MM a[20:23],a[136:139],v[80:83],a[20:23]
s_and_b32 s60, s84, 1
RD a[216:219],v226 offset:53248
MM a[12:15],a[136:139],v[72:75],a[12:15]
s_xor_b32 s60, s60, 1
EX v38,v38
MM a[12:15],a[140:143],v[76:79],a[12:15]
s_lshl_b32 s60, s60, 8
RD a[220:223],v227 offset:53248
RD a[224:227],v224 offset:57344
MM a[4:7],a[140:143],v[68:71],a[4:7]
s_add_u32 s63, s18, s60
EX v39,v39
MM a[4:7],a[136:139],v[64:67],a[4:7]
s_and_b32 s60, s84, 1
RD a[228:231],v225 offset:57344
CV v7,v38,v39
MM a[32:35],a[144:147],v[64:67],a[32:35]
s_lshl_b32 s60, s60, 6
EX v40,v40
MM a[32:35],a[148:151],v[68:71],a[32:35]
s_add_u32 s18, s18, s60
buffer_load_dwordx4 v231, s[8:11], s64 offen offset:3072 lds
MM a[40:43],a[148:151],v[76:79],a[40:43]
s_lshl_b32 s60, s50, 2
EX v41,v41
MM a[40:43],a[144:147],v[72:75],a[40:43]
s_add_u32 s60, s60, s84
s_add_u32 m0, s65, 81920
CV v12,v40,v41
MM a[48:51],a[144:147],v[80:83],a[48:51]
s_mul_i32 s16, s60, 663552
EX v42,v42
MM a[48:51],a[148:151],v[84:87],a[48:51]
s_lshl_b32 s62, s62, 2
EX v43,v43
MM a[56:59],a[148:151],v[92:95],a[56:59]
s_add_u32 s62, s62, s61
RD a[232:235],v226 offset:57344
CV v13,v42,v43
MM a[56:59],a[144:147],v[88:91],a[56:59]
s_lshl_b32 s62, s62, 8
EX v44,v44
MM a[60:63],a[152:155],v[88:91],a[60:63]
s_add_u32 s16, s16, s62
buffer_load_dwordx4 v235, s[12:15], s81 offen offset:0 lds
MM a[60:63],a[156:159],v[92:95],a[60:63]
s_mul_i32 s60, %[swave], 20480
EX v45,v45
MM a[52:55],a[156:159],v[84:87],a[52:55]
s_add_u32 s16, s16, s60
RD a[236:239],v227 offset:57344
CV v14,v44,v45
MM a[52:55],a[152:155],v[80:83],a[52:55]
s_lshr_b32 s61, s49, 4
EX v46,v46
MM a[44:47],a[152:155],v[72:75],a[44:47]
s_mul_i32 s61, s61, 331776
EX v47,v47
MM a[44:47],a[156:159],v[76:79],a[44:47]
s_add_u32 s16, s16, s61
CV v15,v46,v47
MM a[36:39],a[156:159],v[68:71],a[36:39]
s_add_u32 s16, s16, 22708224
buffer_load_dwordx4 v235, s[12:15], s64 offen offset:1024 lds
MM a[36:39],a[152:155],v[64:67],a[36:39]
s_mov_b32 s17, 31
EX v48,v48
MM a[64:67],a[160:163],v[64:67],a[64:67]
s_mov_b32 s72, 0
EX v49,v49
MM a[64:67],a[164:167],v[68:71],a[64:67]
s_lshl_b32 s60, s50, 2
RD a[240:243],v224 offset:61440
CV v20,v48,v49
MM a[72:75],a[164:167],v[76:79],a[72:75]
s_add_u32 s60, s60, s84
EX v50,v50
MM a[72:75],a[160:163],v[72:75],a[72:75]
s_lshl_b32 s60, s60, 2
RD a[244:247],v225 offset:61440
MM a[80:83],a[160:163],v[80:83],a[80:83]
s_add_u32 s60, s60, %[swave]
EX v51,v51
MM a[80:83],a[164:167],v[84:87],a[80:83]
s_lshl_b32 s60, s60, 5
RD a[248:251],v226 offset:61440
CV v21,v50,v51
MM a[88:91],a[164:167],v[92:95],a[88:91]
s_and_b32 s61, s49, 28
EX v52,v52
MM a[88:91],a[160:163],v[88:91],a[88:91]
s_add_u32 s19, s60, s61
buffer_load_dwordx4 v235, s[12:15], s81 offen offset:2048 lds
MM a[92:95],a[168:171],v[88:91],a[92:95]
s_lshl_b32 s19, s19, 2
EX v53,v53
MM a[92:95],a[172:175],v[92:95],a[92:95]
s_add_u32 s19, s19, 16384
RD a[252:255],v227 offset:61440
CV v22,v52,v53
MM a[84:87],a[172:175],v[84:87],a[84:87]
s_and_b32 s68, s19, 0xffffffc0
EX v54,v54
MM a[84:87],a[168:171],v[80:83],a[84:87]
s_mov_b32 s82, 0
EX v55,v55
MM a[76:79],a[168:171],v[72:75],a[76:79]
s_lshr_b32 s61, s52, 2
CV v23,v54,v55
MM a[76:79],a[172:175],v[76:79],a[76:79]
s_sub_u32 s61, s61, s93
s_cmp_eq_u32 s61, 0
s_cselect_b32 s60, 1, 0
MM a[68:71],a[172:175],v[68:71],a[68:71]
s_or_b32 s82, s82, s60
s_cmp_eq_u32 s61, 8
s_cselect_b32 s60, 256, 0
MM a[68:71],a[168:171],v[64:67],a[68:71]
s_or_b32 s82, s82, s60
EX v56,v56
MM a[96:99],a[176:179],v[64:67],a[96:99]
s_lshr_b32 s61, s53, 2
EX v57,v57
MM a[96:99],a[180:183],v[68:71],a[96:99]
s_sub_u32 s61, s61, s93
s_cmp_eq_u32 s61, 0
s_cselect_b32 s60, 2, 0
MM a[104:107],a[180:183],v[76:79],a[104:107]
s_or_b32 s82, s82, s60
s_cmp_eq_u32 s61, 8
s_cselect_b32 s60, 512, 0
MM a[104:107],a[176:179],v[72:75],a[104:107]
s_or_b32 s82, s82, s60
EX v58,v58
MM a[112:115],a[176:179],v[80:83],a[112:115]
s_lshr_b32 s61, s54, 2
EX v59,v59
MM a[112:115],a[180:183],v[84:87],a[112:115]
s_sub_u32 s61, s61, s93
s_cmp_eq_u32 s61, 0
s_cselect_b32 s60, 4, 0
MM a[120:123],a[180:183],v[92:95],a[120:123]
s_or_b32 s82, s82, s60
s_cmp_eq_u32 s61, 8
s_cselect_b32 s60, 1024, 0
MM a[120:123],a[176:179],v[88:91],a[120:123]
s_or_b32 s82, s82, s60
EX v60,v60
MM a[124:127],a[184:187],v[88:91],a[124:127]
s_lshr_b32 s61, s55, 2
buffer_load_dwordx4 v235, s[12:15], s64 offen offset:3072 lds
MM a[124:127],a[188:191],v[92:95],a[124:127]
s_sub_u32 s61, s61, s93
s_cmp_eq_u32 s61, 0
s_cselect_b32 s60, 8, 0
MM a[116:119],a[188:191],v[84:87],a[116:119]
s_or_b32 s82, s82, s60
s_cmp_eq_u32 s61, 8
s_cselect_b32 s60, 2048, 0
MM a[116:119],a[184:187],v[80:83],a[116:119]
s_or_b32 s82, s82, s60
EX v61,v61
MM a[108:111],a[184:187],v[72:75],a[108:111]
s_lshr_b32 s61, s56, 2
EX v62,v62
MM a[108:111],a[188:191],v[76:79],a[108:111]
s_sub_u32 s61, s61, s93
s_cmp_eq_u32 s61, 0
s_cselect_b32 s60, 16, 0
MM a[100:103],a[188:191],v[68:71],a[100:103]
s_or_b32 s82, s82, s60
s_cmp_eq_u32 s61, 8
s_cselect_b32 s60, 4096, 0
MM a[100:103],a[184:187],v[64:67],a[100:103]
s_or_b32 s82, s82, s60
EX v63,v63
v_smfmac_f32_16x16x64_bf16 v[236:239], v[144:147], v[0:7], v234
s_lshr_b32 s61, s57, 2
CV v28,v56,v57
v_smfmac_f32_16x16x64_bf16 v[236:239], v[148:151], v[8:15], v234
s_sub_u32 s61, s61, s93
CV v29,v58,v59
v_smfmac_f32_16x16x64_bf16 v[236:239], v[152:155], v[16:23], v234
CV v30,v60,v61
CV v31,v62,v63
s_nop 1
v_smfmac_f32_16x16x64_bf16 v[236:239], v[156:159], v[24:31], v234
s_waitcnt lgkmcnt(0)
s_cmp_eq_u32 s61, 0
s_cselect_b32 s60, 32, 0
MM v[64:67],a[192:195],v[160:163],v[128:131]
s_or_b32 s82, s82, s60
s_cmp_eq_u32 s61, 8
s_cselect_b32 s60, 8192, 0
MM v[64:67],a[196:199],v[176:179],v[64:67]
s_or_b32 s82, s82, s60
TR a[128:129],v232 offset:32768
TR a[130:131],v232 offset:36864
MM v[64:67],a[200:203],v[192:195],v[64:67]
s_lshr_b32 s61, s58, 2
s_add_u32 s64, s64, 16384
s_add_u32 s81, s64, 896
MM v[64:67],a[204:207],v[208:211],v[64:67]
s_sub_u32 s61, s61, s93
TR a[132:133],v232 offset:40960
v_add_f32 v246, v236, v237
MM v[68:71],a[220:223],v[208:211],v[128:131]
s_cmp_eq_u32 s61, 0
s_cselect_b32 s60, 64, 0
v_cmp_nge_f32 vcc, s71, v246
MM v[68:71],a[216:219],v[192:195],v[68:71]
s_cbranch_vccnz LSITE18_%=
LBACK18_%=:
TR a[134:135],v232 offset:45056
TR a[136:137],v233 offset:32768
MM v[68:71],a[212:215],v[176:179],v[68:71]
v_bfe_u32 v253, v248, 4, 4
v_lshlrev_b32 v253, 2, v253
MM v[68:71],a[208:211],v[160:163],v[68:71]
s_and_b32 s76, s48, 0xffffffc0
TR a[138:139],v233 offset:36864
TR a[140:141],v233 offset:40960
MM v[76:79],a[208:211],v[164:167],v[132:135]
s_add_u32 s76, s76, 77824
buffer_load_dword v252, v253, s[20:23], s76 offen offset:0 sc1
MM v[76:79],a[212:215],v[180:183],v[76:79]
s_or_b32 s82, s82, s60
s_cmp_eq_u32 s61, 8
s_cselect_b32 s60, 16384, 0
MM v[76:79],a[216:219],v[196:199],v[76:79]
)ASM"
R"ASM(
s_or_b32 s82, s82, s60
EX v64,v64
MM v[76:79],a[220:223],v[212:215],v[76:79]
s_lshr_b32 s61, s59, 2
EX v65,v65
MM v[72:75],a[204:207],v[212:215],v[132:135]
s_sub_u32 s61, s61, s93
s_cmp_eq_u32 s61, 0
s_cselect_b32 s60, 128, 0
MM v[72:75],a[200:203],v[196:199],v[72:75]
s_or_b32 s82, s82, s60
s_cmp_eq_u32 s61, 8
s_cselect_b32 s60, 32768, 0
MM v[72:75],a[196:199],v[180:183],v[72:75]
s_or_b32 s82, s82, s60
s_and_b32 s60, s82, 255
s_cselect_b32 s51, 0, 31
MM v[72:75],a[192:195],v[164:167],v[72:75]
EX v66,v66
MM v[80:83],a[192:195],v[168:171],v[136:139]
EX v67,v67
MM v[80:83],a[196:199],v[184:187],v[80:83]
TR a[142:143],v233 offset:45056
TR a[144:145],v232 offset:33280
MM v[80:83],a[200:203],v[200:203],v[80:83]
EX v68,v68
MM v[80:83],a[204:207],v[216:219],v[80:83]
TR a[146:147],v232 offset:37376
CV v64,v64,v65
MM v[84:87],a[220:223],v[216:219],v[136:139]
EX v69,v69
MM v[84:87],a[216:219],v[200:203],v[84:87]
TR a[148:149],v232 offset:41472
CV v65,v66,v67
MM v[84:87],a[212:215],v[184:187],v[84:87]
EX v70,v70
MM v[84:87],a[208:211],v[168:171],v[84:87]
EX v71,v71
MM v[92:95],a[208:211],v[172:175],v[140:143]
TR a[150:151],v232 offset:45568
TR a[152:153],v233 offset:33280
MM v[92:95],a[212:215],v[188:191],v[92:95]
EX v72,v72
MM v[92:95],a[216:219],v[204:207],v[92:95]
TR a[154:155],v233 offset:37376
CV v66,v68,v69
MM v[92:95],a[220:223],v[220:223],v[92:95]
EX v73,v73
MM v[88:91],a[204:207],v[220:223],v[140:143]
TR a[156:157],v233 offset:41472
CV v67,v70,v71
MM v[88:91],a[200:203],v[204:207],v[88:91]
EX v74,v74
MM v[88:91],a[196:199],v[188:191],v[88:91]
TR a[158:159],v233 offset:45568
CV v72,v72,v73
MM v[88:91],a[192:195],v[172:175],v[88:91]
EX v75,v75
MM v[96:99],a[224:227],v[160:163],v[128:131]
LPG12_%=:
s_cbranch_execz LTR0_%=
TR a[160:161],v232 offset:33792
TR a[162:163],v232 offset:37888
MM v[96:99],a[228:231],v[176:179],v[96:99]
EX v76,v76
MM v[96:99],a[232:235],v[192:195],v[96:99]
EX v77,v77
MM v[96:99],a[236:239],v[208:211],v[96:99]
TR a[164:165],v232 offset:41984
CV v73,v74,v75
MM v[100:103],a[252:255],v[208:211],v[128:131]
EX v78,v78
MM v[100:103],a[248:251],v[192:195],v[100:103]
TR a[166:167],v232 offset:46080
CV v74,v76,v77
MM v[100:103],a[244:247],v[176:179],v[100:103]
EX v79,v79
MM v[100:103],a[240:243],v[160:163],v[100:103]
TR a[168:169],v233 offset:33792
TR a[170:171],v233 offset:37888
MM v[108:111],a[240:243],v[164:167],v[132:135]
EX v80,v80
MM v[108:111],a[244:247],v[180:183],v[108:111]
EX v81,v81
MM v[108:111],a[248:251],v[196:199],v[108:111]
TR a[172:173],v233 offset:41984
CV v75,v78,v79
MM v[108:111],a[252:255],v[212:215],v[108:111]
EX v82,v82
MM v[104:107],a[236:239],v[212:215],v[132:135]
TR a[174:175],v233 offset:46080
CV v80,v80,v81
MM v[104:107],a[232:235],v[196:199],v[104:107]
EX v83,v83
MM v[104:107],a[228:231],v[180:183],v[104:107]
TR a[176:177],v232 offset:34304
TR a[178:179],v232 offset:38400
MM v[104:107],a[224:227],v[164:167],v[104:107]
EX v84,v84
MM v[112:115],a[224:227],v[168:171],v[136:139]
EX v85,v85
MM v[112:115],a[228:231],v[184:187],v[112:115]
TR a[180:181],v232 offset:42496
CV v81,v82,v83
MM v[112:115],a[232:235],v[200:203],v[112:115]
EX v86,v86
MM v[112:115],a[236:239],v[216:219],v[112:115]
TR a[182:183],v232 offset:46592
CV v82,v84,v85
MM v[116:119],a[252:255],v[216:219],v[136:139]
EX v87,v87
MM v[116:119],a[248:251],v[200:203],v[116:119]
TR a[184:185],v233 offset:34304
TR a[186:187],v233 offset:38400
MM v[116:119],a[244:247],v[184:187],v[116:119]
EX v88,v88
MM v[116:119],a[240:243],v[168:171],v[116:119]
TR a[188:189],v233 offset:42496
CV v83,v86,v87
MM v[124:127],a[240:243],v[172:175],v[140:143]
EX v89,v89
MM v[124:127],a[244:247],v[188:191],v[124:127]
TR a[190:191],v233 offset:46592
CV v88,v88,v89
MM v[124:127],a[248:251],v[204:207],v[124:127]
EX v90,v90
MM v[124:127],a[252:255],v[220:223],v[124:127]
EX v91,v91
MM v[120:123],a[236:239],v[220:223],v[140:143]
EX v92,v92
MM v[120:123],a[232:235],v[204:207],v[120:123]
EX v93,v93
MM v[120:123],a[228:231],v[188:191],v[120:123]
s_getpc_b64 s[98:99]
LTA2_%=:
s_lshl_b32 s73, %[swave], 12
s_add_u32 s73, s73, LPG13_%=-LTA2_%=
s_add_u32 s98, s98, s73
s_addc_u32 s99, s99, 0
s_mov_b64 exec, 0
s_setpc_b64 s[98:99]
LTR2_%=:
s_mov_b64 exec, -1
s_nop 0
CV v89,v90,v91
MM v[120:123],a[224:227],v[172:175],v[120:123]
s_waitcnt vmcnt(14) lgkmcnt(0)
s_barrier
MM a[0:3],a[128:131],v[0:3],a[0:3]
s_waitcnt vmcnt(0)
v_cmp_ne_u32 s[90:91], s69, v252
CV v90,v92,v93
MM a[0:3],a[132:135],v[4:7],a[0:3]
s_add_u32 m0, s65, 32768
EX v94,v94
MM a[8:11],a[132:135],v[12:15],a[8:11]
s_cmp_lg_u64 s[90:91], 0
s_cbranch_scc1 LRFG_%=
LRFGb_%=:
v_lshrrev_b32 v253, 8, v248
MM a[8:11],a[128:131],v[8:11],a[8:11]
v_lshlrev_b32 v253, 10, v253
v_and_b32 v252, 240, v248
MM a[16:19],a[128:131],v[16:19],a[16:19]
RD a[192:195],v224 offset:0
v_add_u32 v253, v253, v252
MM a[16:19],a[132:135],v[20:23],a[16:19]
s_lshr_b32 s76, s48, 6
EX v95,v95
MM a[24:27],a[132:135],v[28:31],a[24:27]
s_lshl_b32 s76, s76, 13
EX v96,v96
MM a[24:27],a[128:131],v[24:27],a[24:27]
s_bfe_u32 s90, s48, 131076
EX v97,v97
MM a[28:31],a[136:139],v[24:27],a[28:31]
s_lshl_b32 s90, s90, 8
RD a[196:199],v225 offset:0
CV v91,v94,v95
MM a[28:31],a[140:143],v[28:31],a[28:31]
s_add_u32 s76, s76, s90
EX v98,v98
MM a[20:23],a[140:143],v[20:23],a[20:23]
s_add_u32 s76, s76, 22183936
buffer_load_dwordx4 v228, s[8:11], s64 offen offset:0 lds
MM a[20:23],a[136:139],v[16:19],a[20:23]
s_lshl_b32 s90, %[swave], 11
EX v99,v99
MM a[12:15],a[136:139],v[8:11],a[12:15]
s_add_u32 s90, s90, %[slds]
s_add_u32 m0, s90, 131072
s_nop 0
buffer_load_dwordx4 v253, s[20:23], s76 offen offset:0 sc1 lds
MM a[12:15],a[140:143],v[12:15],a[12:15]
s_add_u32 m0, s65, 32768
EX v100,v100
MM a[4:7],a[140:143],v[4:7],a[4:7]
s_add_u32 m0, s90, 132096
s_add_u32 s76, s76, 4096
buffer_load_dwordx4 v253, s[20:23], s76 offen offset:0 sc1 lds
MM a[4:7],a[136:139],v[0:3],a[4:7]
EX v101,v101
MM a[32:35],a[144:147],v[0:3],a[32:35]
RD a[200:203],v226 offset:0
CV v68,v96,v97
MM a[32:35],a[148:151],v[4:7],a[32:35]
EX v102,v102
MM a[40:43],a[148:151],v[12:15],a[40:43]
RD a[204:207],v227 offset:0
CV v69,v98,v99
MM a[40:43],a[144:147],v[8:11],a[40:43]
EX v103,v103
MM a[48:51],a[144:147],v[16:19],a[48:51]
s_add_u32 m0, s65, 32768
s_nop 0
buffer_load_dwordx4 v229, s[8:11], s64 offen offset:1024 lds
MM a[48:51],a[148:151],v[20:23],a[48:51]
EX v104,v104
MM a[56:59],a[148:151],v[28:31],a[56:59]
CV v70,v100,v101
CV v71,v102,v103
MM a[56:59],a[144:147],v[24:27],a[56:59]
EX v105,v105
MM a[60:63],a[152:155],v[24:27],a[60:63]
EX v106,v106
MM a[60:63],a[156:159],v[28:31],a[60:63]
RD a[208:211],v224 offset:4096
RD a[212:215],v225 offset:4096
MM a[52:55],a[156:159],v[20:23],a[52:55]
EX v107,v107
MM a[52:55],a[152:155],v[16:19],a[52:55]
buffer_load_dwordx4 v230, s[8:11], s64 offen offset:2048 lds
MM a[44:47],a[152:155],v[8:11],a[44:47]
EX v108,v108
MM a[44:47],a[156:159],v[12:15],a[44:47]
CV v76,v104,v105
CV v77,v106,v107
MM a[36:39],a[156:159],v[4:7],a[36:39]
EX v109,v109
MM a[36:39],a[152:155],v[0:3],a[36:39]
RD a[216:219],v226 offset:4096
CV v78,v108,v109
MM a[64:67],a[160:163],v[0:3],a[64:67]
EX v110,v110
MM a[64:67],a[164:167],v[4:7],a[64:67]
EX v111,v111
MM a[72:75],a[164:167],v[12:15],a[72:75]
buffer_load_dwordx4 v231, s[8:11], s64 offen offset:3072 lds
MM a[72:75],a[160:163],v[8:11],a[72:75]
s_add_u32 m0, s65, 98304
EX v112,v112
MM a[80:83],a[160:163],v[16:19],a[80:83]
RD a[220:223],v227 offset:4096
CV v79,v110,v111
MM a[80:83],a[164:167],v[20:23],a[80:83]
EX v113,v113
MM a[88:91],a[164:167],v[28:31],a[88:91]
RD a[224:227],v224 offset:8192
CV v84,v112,v113
MM a[88:91],a[160:163],v[24:27],a[88:91]
EX v114,v114
MM a[92:95],a[168:171],v[24:27],a[92:95]
EX v115,v115
MM a[92:95],a[172:175],v[28:31],a[92:95]
buffer_load_dwordx4 v235, s[12:15], s81 offen offset:0 lds
MM a[84:87],a[172:175],v[20:23],a[84:87]
RD a[228:231],v225 offset:8192
CV v85,v114,v115
MM a[84:87],a[168:171],v[16:19],a[84:87]
EX v116,v116
MM a[76:79],a[168:171],v[8:11],a[76:79]
EX v117,v117
MM a[76:79],a[172:175],v[12:15],a[76:79]
RD a[232:235],v226 offset:8192
CV v86,v116,v117
MM a[68:71],a[172:175],v[4:7],a[68:71]
EX v118,v118
MM a[68:71],a[168:171],v[0:3],a[68:71]
buffer_load_dwordx4 v235, s[12:15], s64 offen offset:1024 lds
MM a[96:99],a[176:179],v[0:3],a[96:99]
EX v119,v119
MM a[96:99],a[180:183],v[4:7],a[96:99]
RD a[236:239],v227 offset:8192
CV v87,v118,v119
MM a[104:107],a[180:183],v[12:15],a[104:107]
EX v120,v120
MM a[104:107],a[176:179],v[8:11],a[104:107]
EX v121,v121
MM a[112:115],a[176:179],v[16:19],a[112:115]
RD a[240:243],v224 offset:12288
CV v92,v120,v121
MM a[112:115],a[180:183],v[20:23],a[112:115]
EX v122,v122
MM a[120:123],a[180:183],v[28:31],a[120:123]
buffer_load_dwordx4 v235, s[12:15], s81 offen offset:2048 lds
MM a[120:123],a[176:179],v[24:27],a[120:123]
EX v123,v123
MM a[124:127],a[184:187],v[24:27],a[124:127]
RD a[244:247],v225 offset:12288
CV v93,v122,v123
MM a[124:127],a[188:191],v[28:31],a[124:127]
EX v124,v124
MM a[116:119],a[188:191],v[20:23],a[116:119]
EX v125,v125
MM a[116:119],a[184:187],v[16:19],a[116:119]
RD a[248:251],v226 offset:12288
CV v94,v124,v125
MM a[108:111],a[184:187],v[8:11],a[108:111]
EX v126,v126
MM a[108:111],a[188:191],v[12:15],a[108:111]
buffer_load_dwordx4 v235, s[12:15], s64 offen offset:3072 lds
MM a[100:103],a[188:191],v[4:7],a[100:103]
EX v127,v127
MM a[100:103],a[184:187],v[0:3],a[100:103]
RD a[252:255],v227 offset:12288
CV v95,v126,v127
v_smfmac_f32_16x16x64_bf16 v[236:239], v[144:147], v[64:71], v234
v_smfmac_f32_16x16x64_bf16 v[236:239], v[148:151], v[72:79], v234
v_smfmac_f32_16x16x64_bf16 v[236:239], v[152:155], v[80:87], v234
v_smfmac_f32_16x16x64_bf16 v[236:239], v[156:159], v[88:95], v234
s_waitcnt lgkmcnt(0)
MM v[0:3],a[192:195],v[160:163],v[128:131]
s_add_u32 s64, s64, 16384
TR a[128:129],v232 offset:49152
s_add_u32 s81, s64, 896
MM v[0:3],a[196:199],v[176:179],v[0:3]
TR a[130:131],v232 offset:53248
TR a[132:133],v232 offset:57344
MM v[0:3],a[200:203],v[192:195],v[0:3]
TR a[134:135],v232 offset:61440
TR a[136:137],v233 offset:49152
MM v[0:3],a[204:207],v[208:211],v[0:3]
TR a[138:139],v233 offset:53248
v_add_f32 v247, v236, v237
MM v[4:7],a[220:223],v[208:211],v[128:131]
TR a[140:141],v233 offset:57344
v_cmp_nge_f32 vcc, s71, v247
MM v[4:7],a[216:219],v[192:195],v[4:7]
s_cbranch_vccnz LSITE19_%=
LBACK19_%=:
TR a[142:143],v233 offset:61440
TR a[144:145],v232 offset:49664
MM v[4:7],a[212:215],v[176:179],v[4:7]
v_lshrrev_b32 v253, 7, v248
v_mul_u32_u24 v253, 1280, v253
MM v[4:7],a[208:211],v[160:163],v[4:7]
v_and_b32 v252, 112, v248
v_add_u32 v253, v253, v252
MM v[12:15],a[208:211],v[164:167],v[132:135]
s_mul_i32 s90, %[swave], 30720
EX v0,v0
MM v[12:15],a[212:215],v[180:183],v[12:15]
s_mul_i32 s91, %[swave], 3072
EX v1,v1
MM v[12:15],a[216:219],v[196:199],v[12:15]
s_add_u32 s91, s91, %[slds]
s_add_u32 m0, s91, 151552
s_add_u32 s77, s90, 0
buffer_load_dwordx4 v253, s[28:31], s77 offen offset:0 lds
MM v[12:15],a[220:223],v[212:215],v[12:15]
TR a[146:147],v232 offset:53760
CV v0,v0,v1
MM v[8:11],a[204:207],v[212:215],v[132:135]
EX v2,v2
MM v[8:11],a[200:203],v[196:199],v[8:11]
s_add_u32 m0, s91, 152576
s_add_u32 s77, s90, 10240
buffer_load_dwordx4 v253, s[28:31], s77 offen offset:0 lds
MM v[8:11],a[196:199],v[180:183],v[8:11]
EX v3,v3
MM v[8:11],a[192:195],v[164:167],v[8:11]
TR a[148:149],v232 offset:57856
CV v1,v2,v3
MM v[16:19],a[192:195],v[168:171],v[136:139]
s_add_u32 m0, s91, 153600
s_add_u32 s77, s90, 20480
buffer_load_dwordx4 v253, s[28:31], s77 offen offset:0 lds
MM v[16:19],a[196:199],v[184:187],v[16:19]
EX v4,v4
MM v[16:19],a[200:203],v[200:203],v[16:19]
EX v5,v5
MM v[16:19],a[204:207],v[216:219],v[16:19]
TR a[150:151],v232 offset:61952
CV v2,v4,v5
MM v[20:23],a[220:223],v[216:219],v[136:139]
EX v6,v6
MM v[20:23],a[216:219],v[200:203],v[20:23]
TR a[152:153],v233 offset:49664
TR a[154:155],v233 offset:53760
MM v[20:23],a[212:215],v[184:187],v[20:23]
EX v7,v7
MM v[20:23],a[208:211],v[168:171],v[20:23]
TR a[156:157],v233 offset:57856
CV v3,v6,v7
MM v[28:31],a[208:211],v[172:175],v[140:143]
EX v8,v8
MM v[28:31],a[212:215],v[188:191],v[28:31]
)ASM"
R"ASM(
EX v9,v9
MM v[28:31],a[216:219],v[204:207],v[28:31]
TR a[158:159],v233 offset:61952
CV v8,v8,v9
MM v[28:31],a[220:223],v[220:223],v[28:31]
EX v10,v10
MM v[24:27],a[204:207],v[220:223],v[140:143]
TR a[160:161],v232 offset:50176
TR a[162:163],v232 offset:54272
MM v[24:27],a[200:203],v[204:207],v[24:27]
EX v11,v11
MM v[24:27],a[196:199],v[188:191],v[24:27]
TR a[164:165],v232 offset:58368
CV v9,v10,v11
MM v[24:27],a[192:195],v[172:175],v[24:27]
EX v12,v12
MM v[32:35],a[224:227],v[160:163],v[128:131]
EX v13,v13
MM v[32:35],a[228:231],v[176:179],v[32:35]
TR a[166:167],v232 offset:62464
CV v10,v12,v13
MM v[32:35],a[232:235],v[192:195],v[32:35]
EX v14,v14
MM v[32:35],a[236:239],v[208:211],v[32:35]
TR a[168:169],v233 offset:50176
TR a[170:171],v233 offset:54272
MM v[36:39],a[252:255],v[208:211],v[128:131]
EX v15,v15
MM v[36:39],a[248:251],v[192:195],v[36:39]
TR a[172:173],v233 offset:58368
CV v11,v14,v15
MM v[36:39],a[244:247],v[176:179],v[36:39]
EX v16,v16
MM v[36:39],a[240:243],v[160:163],v[36:39]
EX v17,v17
MM v[44:47],a[240:243],v[164:167],v[132:135]
TR a[174:175],v233 offset:62464
CV v16,v16,v17
MM v[44:47],a[244:247],v[180:183],v[44:47]
EX v18,v18
MM v[44:47],a[248:251],v[196:199],v[44:47]
TR a[176:177],v232 offset:50688
TR a[178:179],v232 offset:54784
MM v[44:47],a[252:255],v[212:215],v[44:47]
EX v19,v19
MM v[40:43],a[236:239],v[212:215],v[132:135]
TR a[180:181],v232 offset:58880
CV v17,v18,v19
MM v[40:43],a[232:235],v[196:199],v[40:43]
EX v20,v20
MM v[40:43],a[228:231],v[180:183],v[40:43]
TR a[182:183],v232 offset:62976
TR a[184:185],v233 offset:50688
MM v[40:43],a[224:227],v[164:167],v[40:43]
EX v21,v21
MM v[48:51],a[224:227],v[168:171],v[136:139]
TR a[186:187],v233 offset:54784
CV v18,v20,v21
MM v[48:51],a[228:231],v[184:187],v[48:51]
EX v22,v22
MM v[48:51],a[232:235],v[200:203],v[48:51]
EX v23,v23
MM v[48:51],a[236:239],v[216:219],v[48:51]
TR a[188:189],v233 offset:58880
CV v19,v22,v23
MM v[52:55],a[252:255],v[216:219],v[136:139]
EX v24,v24
MM v[52:55],a[248:251],v[200:203],v[52:55]
EX v25,v25
MM v[52:55],a[244:247],v[184:187],v[52:55]
TR a[190:191],v233 offset:62976
CV v24,v24,v25
MM v[52:55],a[240:243],v[168:171],v[52:55]
EX v26,v26
MM v[60:63],a[240:243],v[172:175],v[140:143]
EX v27,v27
MM v[60:63],a[244:247],v[188:191],v[60:63]
EX v28,v28
MM v[60:63],a[248:251],v[204:207],v[60:63]
EX v29,v29
MM v[60:63],a[252:255],v[220:223],v[60:63]
EX v30,v30
MM v[56:59],a[236:239],v[220:223],v[140:143]
EX v31,v31
MM v[56:59],a[232:235],v[204:207],v[56:59]
EX v32,v32
MM v[56:59],a[228:231],v[188:191],v[56:59]
EX v33,v33
MM v[56:59],a[224:227],v[172:175],v[56:59]
s_waitcnt lgkmcnt(0)
s_barrier
MM a[0:3],a[128:131],v[64:67],a[0:3]
s_add_u32 m0, s65, 49152
CV v25,v26,v27
CV v26,v28,v29
MM a[0:3],a[132:135],v[68:71],a[0:3]
RD a[192:195],v224 offset:16384
CV v27,v30,v31
MM a[8:11],a[132:135],v[76:79],a[8:11]
buffer_load_dwordx4 v228, s[8:11], s64 offen offset:0 lds
MM a[8:11],a[128:131],v[72:75],a[8:11]
RD a[196:199],v225 offset:16384
CV v4,v32,v33
MM a[16:19],a[128:131],v[80:83],a[16:19]
RD a[200:203],v226 offset:16384
RD a[204:207],v227 offset:16384
MM a[16:19],a[132:135],v[84:87],a[16:19]
buffer_load_dwordx4 v229, s[8:11], s64 offen offset:1024 lds
MM a[24:27],a[132:135],v[92:95],a[24:27]
EX v34,v34
MM a[24:27],a[128:131],v[88:91],a[24:27]
RD a[208:211],v224 offset:20480
RD a[212:215],v225 offset:20480
MM a[28:31],a[136:139],v[88:91],a[28:31]
EX v35,v35
MM a[28:31],a[140:143],v[92:95],a[28:31]
RD a[216:219],v226 offset:20480
CV v5,v34,v35
MM a[20:23],a[140:143],v[84:87],a[20:23]
EX v36,v36
MM a[20:23],a[136:139],v[80:83],a[20:23]
EX v37,v37
MM a[12:15],a[136:139],v[72:75],a[12:15]
RD a[220:223],v227 offset:20480
CV v6,v36,v37
MM a[12:15],a[140:143],v[76:79],a[12:15]
buffer_load_dwordx4 v230, s[8:11], s64 offen offset:2048 lds
MM a[4:7],a[140:143],v[68:71],a[4:7]
EX v38,v38
MM a[4:7],a[136:139],v[64:67],a[4:7]
EX v39,v39
MM a[32:35],a[144:147],v[64:67],a[32:35]
RD a[224:227],v224 offset:24576
CV v7,v38,v39
MM a[32:35],a[148:151],v[68:71],a[32:35]
EX v40,v40
MM a[40:43],a[148:151],v[76:79],a[40:43]
EX v41,v41
MM a[40:43],a[144:147],v[72:75],a[40:43]
RD a[228:231],v225 offset:24576
CV v12,v40,v41
MM a[48:51],a[144:147],v[80:83],a[48:51]
buffer_load_dwordx4 v231, s[8:11], s64 offen offset:3072 lds
MM a[48:51],a[148:151],v[84:87],a[48:51]
s_add_u32 m0, s65, 114688
EX v42,v42
MM a[56:59],a[148:151],v[92:95],a[56:59]
EX v43,v43
MM a[56:59],a[144:147],v[88:91],a[56:59]
RD a[232:235],v226 offset:24576
CV v13,v42,v43
MM a[60:63],a[152:155],v[88:91],a[60:63]
EX v44,v44
MM a[60:63],a[156:159],v[92:95],a[60:63]
buffer_load_dwordx4 v235, s[12:15], s81 offen offset:0 lds
MM a[52:55],a[156:159],v[84:87],a[52:55]
EX v45,v45
MM a[52:55],a[152:155],v[80:83],a[52:55]
RD a[236:239],v227 offset:24576
CV v14,v44,v45
MM a[44:47],a[152:155],v[72:75],a[44:47]
EX v46,v46
MM a[44:47],a[156:159],v[76:79],a[44:47]
EX v47,v47
MM a[36:39],a[156:159],v[68:71],a[36:39]
RD a[240:243],v224 offset:28672
CV v15,v46,v47
MM a[36:39],a[152:155],v[64:67],a[36:39]
EX v48,v48
MM a[64:67],a[160:163],v[64:67],a[64:67]
buffer_load_dwordx4 v235, s[12:15], s64 offen offset:1024 lds
MM a[64:67],a[164:167],v[68:71],a[64:67]
EX v49,v49
MM a[72:75],a[164:167],v[76:79],a[72:75]
RD a[244:247],v225 offset:28672
CV v20,v48,v49
MM a[72:75],a[160:163],v[72:75],a[72:75]
EX v50,v50
MM a[80:83],a[160:163],v[80:83],a[80:83]
EX v51,v51
MM a[80:83],a[164:167],v[84:87],a[80:83]
RD a[248:251],v226 offset:28672
CV v21,v50,v51
MM a[88:91],a[164:167],v[92:95],a[88:91]
EX v52,v52
MM a[88:91],a[160:163],v[88:91],a[88:91]
buffer_load_dwordx4 v235, s[12:15], s81 offen offset:2048 lds
MM a[92:95],a[168:171],v[88:91],a[92:95]
EX v53,v53
MM a[92:95],a[172:175],v[92:95],a[92:95]
RD a[252:255],v227 offset:28672
CV v22,v52,v53
MM a[84:87],a[172:175],v[84:87],a[84:87]
EX v54,v54
MM a[84:87],a[168:171],v[80:83],a[84:87]
EX v55,v55
MM a[76:79],a[168:171],v[72:75],a[76:79]
CV v23,v54,v55
MM a[76:79],a[172:175],v[76:79],a[76:79]
EX v56,v56
MM a[68:71],a[172:175],v[68:71],a[68:71]
buffer_load_dwordx4 v235, s[12:15], s64 offen offset:3072 lds
MM a[68:71],a[168:171],v[64:67],a[68:71]
EX v57,v57
MM a[96:99],a[176:179],v[64:67],a[96:99]
EX v58,v58
MM a[96:99],a[180:183],v[68:71],a[96:99]
EX v59,v59
MM a[104:107],a[180:183],v[76:79],a[104:107]
EX v60,v60
MM a[104:107],a[176:179],v[72:75],a[104:107]
EX v61,v61
MM a[112:115],a[176:179],v[80:83],a[112:115]
EX v62,v62
MM a[112:115],a[180:183],v[84:87],a[112:115]
EX v63,v63
MM a[120:123],a[180:183],v[92:95],a[120:123]
CV v28,v56,v57
CV v29,v58,v59
MM a[120:123],a[176:179],v[88:91],a[120:123]
CV v30,v60,v61
CV v31,v62,v63
MM a[124:127],a[184:187],v[88:91],a[124:127]
MM a[124:127],a[188:191],v[92:95],a[124:127]
MM a[116:119],a[188:191],v[84:87],a[116:119]
MM a[116:119],a[184:187],v[80:83],a[116:119]
MM a[108:111],a[184:187],v[72:75],a[108:111]
MM a[108:111],a[188:191],v[76:79],a[108:111]
MM a[100:103],a[188:191],v[68:71],a[100:103]
MM a[100:103],a[184:187],v[64:67],a[100:103]
v_smfmac_f32_16x16x64_bf16 v[236:239], v[144:147], v[0:7], v234
v_smfmac_f32_16x16x64_bf16 v[236:239], v[148:151], v[8:15], v234
v_smfmac_f32_16x16x64_bf16 v[236:239], v[152:155], v[16:23], v234
v_smfmac_f32_16x16x64_bf16 v[236:239], v[156:159], v[24:31], v234
s_waitcnt lgkmcnt(0)
MM v[64:67],a[192:195],v[160:163],v[128:131]
s_add_u32 s64, s64, 16384
TR a[128:129],v232 offset:0
s_add_u32 s81, s64, 896
MM v[64:67],a[196:199],v[176:179],v[64:67]
TR a[130:131],v232 offset:4096
TR a[132:133],v232 offset:8192
MM v[64:67],a[200:203],v[192:195],v[64:67]
TR a[134:135],v232 offset:12288
TR a[136:137],v233 offset:0
MM v[64:67],a[204:207],v[208:211],v[64:67]
TR a[138:139],v233 offset:4096
v_add_f32 v246, v236, v237
MM v[68:71],a[220:223],v[208:211],v[128:131]
TR a[140:141],v233 offset:8192
v_cmp_nge_f32 vcc, s71, v246
MM v[68:71],a[216:219],v[192:195],v[68:71]
s_cbranch_vccnz LSITE20_%=
LBACK20_%=:
TR a[142:143],v233 offset:12288
TR a[144:145],v232 offset:512
MM v[68:71],a[212:215],v[176:179],v[68:71]
v_lshrrev_b32 v43, 7, v248
LPG13_%=:
s_cbranch_execz LTR2_%=
v_bfe_u32 v42, v248, 6, 1
MM v[68:71],a[208:211],v[160:163],v[68:71]
v_lshl_add_u32 v40, v42, 3, v43
v_lshlrev_b32 v40, 4, v40
MM v[76:79],a[208:211],v[164:167],v[132:135]
v_bfe_u32 v42, v248, 4, 2
v_lshl_add_u32 v40, v42, 2, v40
MM v[76:79],a[212:215],v[180:183],v[76:79]
s_lshl_b32 s76, %[swave], 11
EX v64,v64
MM v[76:79],a[216:219],v[196:199],v[76:79]
s_add_u32 s76, s76, %[slds]
EX v65,v65
MM v[76:79],a[220:223],v[212:215],v[76:79]
s_add_u32 s76, s76, 131072
v_add_u32 v40, s76, v40
CV v64,v64,v65
MM v[72:75],a[204:207],v[212:215],v[132:135]
s_waitcnt vmcnt(18)
ds_read_b32 v32, v40 offset:0
ds_read_b32 v36, v40 offset:256
MM v[72:75],a[200:203],v[196:199],v[72:75]
ds_read_b32 v33, v40 offset:512
ds_read_b32 v37, v40 offset:768
MM v[72:75],a[196:199],v[180:183],v[72:75]
ds_read_b32 v34, v40 offset:1024
ds_read_b32 v38, v40 offset:1280
MM v[72:75],a[192:195],v[164:167],v[72:75]
ds_read_b32 v35, v40 offset:1536
ds_read_b32 v39, v40 offset:1792
MM v[80:83],a[192:195],v[168:171],v[136:139]
v_mul_u32_u24 v41, 3456, v43
v_bfe_u32 v42, v248, 4, 3
MM v[80:83],a[196:199],v[184:187],v[80:83]
TR a[146:147],v232 offset:4608
v_lshl_add_u32 v41, v42, 1, v41
MM v[80:83],a[200:203],v[200:203],v[80:83]
s_add_u32 s76, s48, 21626880
EX v66,v66
MM v[80:83],a[204:207],v[216:219],v[80:83]
s_lshl_b32 s77, %[swave], 4
EX v67,v67
MM v[84:87],a[220:223],v[216:219],v[136:139]
EX v68,v68
MM v[84:87],a[216:219],v[200:203],v[84:87]
EX v69,v69
MM v[84:87],a[212:215],v[184:187],v[84:87]
EX v70,v70
MM v[84:87],a[208:211],v[168:171],v[84:87]
TR a[148:149],v232 offset:8704
CV v65,v66,v67
MM v[92:95],a[208:211],v[172:175],v[140:143]
EX v71,v71
MM v[92:95],a[212:215],v[188:191],v[92:95]
TR a[150:151],v232 offset:12800
TR a[152:153],v233 offset:512
MM v[92:95],a[216:219],v[204:207],v[92:95]
EX v72,v72
MM v[92:95],a[220:223],v[220:223],v[92:95]
s_waitcnt lgkmcnt(4)
v_add_f32 v32, v32, v33
v_add_f32 v36, v36, v37
MM v[88:91],a[204:207],v[220:223],v[140:143]
v_add_f32 v34, v34, v35
v_add_f32 v38, v38, v39
MM v[88:91],a[200:203],v[204:207],v[88:91]
v_add_f32 v32, v32, v34
v_add_f32 v36, v36, v38
EX v73,v73
MM v[88:91],a[196:199],v[188:191],v[88:91]
TR a[154:155],v233 offset:4608
CV v42,v32,v36
v_lshlrev_b32 v32, 16, v42
MM v[88:91],a[192:195],v[172:175],v[88:91]
v_and_b32 v36, 0xffff0000, v42
v_mul_f32 v42, 0xbfb8aa3b, v32
EX v74,v74
MM v[96:99],a[224:227],v[160:163],v[128:131]
TR a[156:157],v233 offset:8704
EX v42,v42
EX v75,v75
CV v66,v68,v69
MM v[96:99],a[228:231],v[176:179],v[96:99]
TR a[158:159],v233 offset:12800
v_add_f32 v42, 1.0, v42
MM v[96:99],a[232:235],v[192:195],v[96:99]
v_rcp_f32 v42, v42
EX v76,v76
MM v[96:99],a[236:239],v[208:211],v[96:99]
v_mul_f32 v42, v32, v42
CV v42,v42,v42
MM v[100:103],a[252:255],v[208:211],v[128:131]
TR a[160:161],v232 offset:1024
v_lshlrev_b32 v42, 16, v42
v_mul_f32 v32, v42, v36
EX v77,v77
CV v67,v70,v71
MM v[100:103],a[248:251],v[192:195],v[100:103]
TR a[162:163],v232 offset:5120
CV v43,v32,v32
MM v[100:103],a[244:247],v[176:179],v[100:103]
s_lshl_b64 exec, 0xffff, s77
buffer_store_short v43, v41, s[20:23], s76 offen offset:0 sc1
s_mov_b64 exec, -1
s_nop 0
EX v78,v78
MM v[100:103],a[240:243],v[160:163],v[100:103]
TR a[164:165],v232 offset:9216
CV v72,v72,v73
MM v[108:111],a[240:243],v[164:167],v[132:135]
EX v79,v79
MM v[108:111],a[244:247],v[180:183],v[108:111]
TR a[166:167],v232 offset:13312
CV v73,v74,v75
MM v[108:111],a[248:251],v[196:199],v[108:111]
EX v80,v80
MM v[108:111],a[252:255],v[212:215],v[108:111]
TR a[168:169],v233 offset:1024
TR a[170:171],v233 offset:5120
EX v81,v81
MM v[104:107],a[236:239],v[212:215],v[132:135]
TR a[172:173],v233 offset:9216
CV v74,v76,v77
MM v[104:107],a[232:235],v[196:199],v[104:107]
EX v82,v82
MM v[104:107],a[228:231],v[180:183],v[104:107]
EX v83,v83
MM v[104:107],a[224:227],v[164:167],v[104:107]
TR a[174:175],v233 offset:13312
CV v75,v78,v79
MM v[112:115],a[224:227],v[168:171],v[136:139]
EX v84,v84
MM v[112:115],a[228:231],v[184:187],v[112:115]
TR a[176:177],v232 offset:1536
)ASM"
R"ASM(
TR a[178:179],v232 offset:5632
MM v[112:115],a[232:235],v[200:203],v[112:115]
EX v85,v85
MM v[112:115],a[236:239],v[216:219],v[112:115]
TR a[180:181],v232 offset:9728
CV v80,v80,v81
MM v[116:119],a[252:255],v[216:219],v[136:139]
EX v86,v86
MM v[116:119],a[248:251],v[200:203],v[116:119]
TR a[182:183],v232 offset:13824
CV v81,v82,v83
MM v[116:119],a[244:247],v[184:187],v[116:119]
EX v87,v87
MM v[116:119],a[240:243],v[168:171],v[116:119]
TR a[184:185],v233 offset:1536
CV v82,v84,v85
MM v[124:127],a[240:243],v[172:175],v[140:143]
EX v88,v88
MM v[124:127],a[244:247],v[188:191],v[124:127]
TR a[186:187],v233 offset:5632
TR a[188:189],v233 offset:9728
MM v[124:127],a[248:251],v[204:207],v[124:127]
TR a[190:191],v233 offset:13824
CV v83,v86,v87
MM v[124:127],a[252:255],v[220:223],v[124:127]
EX v89,v89
MM v[120:123],a[236:239],v[220:223],v[140:143]
EX v90,v90
MM v[120:123],a[232:235],v[204:207],v[120:123]
EX v91,v91
MM v[120:123],a[228:231],v[188:191],v[120:123]
EX v92,v92
MM v[120:123],a[224:227],v[172:175],v[120:123]
s_waitcnt vmcnt(16) lgkmcnt(0)
s_barrier
MM a[0:3],a[128:131],v[0:3],a[0:3]
s_add_u32 m0, s65, 0
CV v88,v88,v89
CV v89,v90,v91
MM a[0:3],a[132:135],v[4:7],a[0:3]
EX v93,v93
MM a[8:11],a[132:135],v[12:15],a[8:11]
EX v94,v94
MM a[8:11],a[128:131],v[8:11],a[8:11]
EX v95,v95
MM a[16:19],a[128:131],v[16:19],a[16:19]
RD a[192:195],v224 offset:32768
CV v90,v92,v93
MM a[16:19],a[132:135],v[20:23],a[16:19]
EX v96,v96
MM a[24:27],a[132:135],v[28:31],a[24:27]
RD a[196:199],v225 offset:32768
CV v91,v94,v95
MM a[24:27],a[128:131],v[24:27],a[24:27]
EX v97,v97
MM a[28:31],a[136:139],v[24:27],a[28:31]
buffer_load_dwordx4 v228, s[8:11], s64 offen offset:0 lds
MM a[28:31],a[140:143],v[28:31],a[28:31]
EX v98,v98
MM a[20:23],a[140:143],v[20:23],a[20:23]
EX v99,v99
MM a[20:23],a[136:139],v[16:19],a[20:23]
RD a[200:203],v226 offset:32768
CV v68,v96,v97
MM a[12:15],a[136:139],v[8:11],a[12:15]
EX v100,v100
MM a[12:15],a[140:143],v[12:15],a[12:15]
RD a[204:207],v227 offset:32768
CV v69,v98,v99
MM a[4:7],a[140:143],v[4:7],a[4:7]
EX v101,v101
MM a[4:7],a[136:139],v[0:3],a[4:7]
buffer_load_dwordx4 v229, s[8:11], s64 offen offset:1024 lds
MM a[32:35],a[144:147],v[0:3],a[32:35]
EX v102,v102
MM a[32:35],a[148:151],v[4:7],a[32:35]
RD a[208:211],v224 offset:36864
CV v70,v100,v101
MM a[40:43],a[148:151],v[12:15],a[40:43]
EX v103,v103
MM a[40:43],a[144:147],v[8:11],a[40:43]
RD a[212:215],v225 offset:36864
CV v71,v102,v103
MM a[48:51],a[144:147],v[16:19],a[48:51]
EX v104,v104
MM a[48:51],a[148:151],v[20:23],a[48:51]
EX v105,v105
MM a[56:59],a[148:151],v[28:31],a[56:59]
buffer_load_dwordx4 v230, s[8:11], s64 offen offset:2048 lds
MM a[56:59],a[144:147],v[24:27],a[56:59]
EX v106,v106
MM a[60:63],a[152:155],v[24:27],a[60:63]
RD a[216:219],v226 offset:36864
CV v76,v104,v105
MM a[60:63],a[156:159],v[28:31],a[60:63]
EX v107,v107
MM a[52:55],a[156:159],v[20:23],a[52:55]
RD a[220:223],v227 offset:36864
CV v77,v106,v107
MM a[52:55],a[152:155],v[16:19],a[52:55]
EX v108,v108
MM a[44:47],a[152:155],v[8:11],a[44:47]
EX v109,v109
MM a[44:47],a[156:159],v[12:15],a[44:47]
buffer_load_dwordx4 v231, s[8:11], s64 offen offset:3072 lds
MM a[36:39],a[156:159],v[4:7],a[36:39]
s_add_u32 m0, s65, 65536
RD a[224:227],v224 offset:40960
CV v78,v108,v109
MM a[36:39],a[152:155],v[0:3],a[36:39]
EX v110,v110
MM a[64:67],a[160:163],v[0:3],a[64:67]
EX v111,v111
MM a[64:67],a[164:167],v[4:7],a[64:67]
RD a[228:231],v225 offset:40960
CV v79,v110,v111
MM a[72:75],a[164:167],v[12:15],a[72:75]
EX v112,v112
MM a[72:75],a[160:163],v[8:11],a[72:75]
buffer_load_dwordx4 v235, s[12:15], s81 offen offset:0 lds
MM a[80:83],a[160:163],v[16:19],a[80:83]
EX v113,v113
MM a[80:83],a[164:167],v[20:23],a[80:83]
RD a[232:235],v226 offset:40960
CV v84,v112,v113
MM a[88:91],a[164:167],v[28:31],a[88:91]
EX v114,v114
MM a[88:91],a[160:163],v[24:27],a[88:91]
EX v115,v115
MM a[92:95],a[168:171],v[24:27],a[92:95]
RD a[236:239],v227 offset:40960
CV v85,v114,v115
MM a[92:95],a[172:175],v[28:31],a[92:95]
EX v116,v116
MM a[84:87],a[172:175],v[20:23],a[84:87]
buffer_load_dwordx4 v235, s[12:15], s64 offen offset:1024 lds
MM a[84:87],a[168:171],v[16:19],a[84:87]
EX v117,v117
MM a[76:79],a[168:171],v[8:11],a[76:79]
RD a[240:243],v224 offset:45056
CV v86,v116,v117
MM a[76:79],a[172:175],v[12:15],a[76:79]
EX v118,v118
MM a[68:71],a[172:175],v[4:7],a[68:71]
EX v119,v119
MM a[68:71],a[168:171],v[0:3],a[68:71]
RD a[244:247],v225 offset:45056
CV v87,v118,v119
MM a[96:99],a[176:179],v[0:3],a[96:99]
EX v120,v120
MM a[96:99],a[180:183],v[4:7],a[96:99]
buffer_load_dwordx4 v235, s[12:15], s81 offen offset:2048 lds
MM a[104:107],a[180:183],v[12:15],a[104:107]
EX v121,v121
MM a[104:107],a[176:179],v[8:11],a[104:107]
RD a[248:251],v226 offset:45056
CV v92,v120,v121
MM a[112:115],a[176:179],v[16:19],a[112:115]
EX v122,v122
MM a[112:115],a[180:183],v[20:23],a[112:115]
EX v123,v123
MM a[120:123],a[180:183],v[28:31],a[120:123]
RD a[252:255],v227 offset:45056
CV v93,v122,v123
MM a[120:123],a[176:179],v[24:27],a[120:123]
EX v124,v124
MM a[124:127],a[184:187],v[24:27],a[124:127]
buffer_load_dwordx4 v235, s[12:15], s64 offen offset:3072 lds
MM a[124:127],a[188:191],v[28:31],a[124:127]
EX v125,v125
MM a[116:119],a[188:191],v[20:23],a[116:119]
EX v126,v126
MM a[116:119],a[184:187],v[16:19],a[116:119]
EX v127,v127
MM a[108:111],a[184:187],v[8:11],a[108:111]
CV v94,v124,v125
CV v95,v126,v127
MM a[108:111],a[188:191],v[12:15],a[108:111]
MM a[100:103],a[188:191],v[4:7],a[100:103]
MM a[100:103],a[184:187],v[0:3],a[100:103]
v_smfmac_f32_16x16x64_bf16 v[236:239], v[144:147], v[64:71], v234
v_smfmac_f32_16x16x64_bf16 v[236:239], v[148:151], v[72:79], v234
v_smfmac_f32_16x16x64_bf16 v[236:239], v[152:155], v[80:87], v234
v_smfmac_f32_16x16x64_bf16 v[236:239], v[156:159], v[88:95], v234
s_waitcnt lgkmcnt(0)
MM v[0:3],a[192:195],v[160:163],v[128:131]
s_add_u32 s64, s64, 16384
TR a[128:129],v232 offset:16384
s_add_u32 s81, s64, 896
MM v[0:3],a[196:199],v[176:179],v[0:3]
TR a[130:131],v232 offset:20480
TR a[132:133],v232 offset:24576
MM v[0:3],a[200:203],v[192:195],v[0:3]
TR a[134:135],v232 offset:28672
TR a[136:137],v233 offset:16384
MM v[0:3],a[204:207],v[208:211],v[0:3]
TR a[138:139],v233 offset:20480
v_add_f32 v247, v236, v237
MM v[4:7],a[220:223],v[208:211],v[128:131]
TR a[140:141],v233 offset:24576
v_cmp_nge_f32 vcc, s71, v247
MM v[4:7],a[216:219],v[192:195],v[4:7]
s_cbranch_vccnz LSITE21_%=
LBACK21_%=:
TR a[142:143],v233 offset:28672
TR a[144:145],v232 offset:16896
MM v[4:7],a[212:215],v[176:179],v[4:7]
s_lshr_b32 s77, %[sb], 3
TR a[146:147],v232 offset:20992
TR a[148:149],v232 offset:25088
MM v[4:7],a[208:211],v[160:163],v[4:7]
s_lshl_b32 s77, s77, 2
TR a[150:151],v232 offset:29184
TR a[152:153],v233 offset:16896
MM v[12:15],a[208:211],v[164:167],v[132:135]
s_add_u32 s77, s77, %[swave]
EX v0,v0
MM v[12:15],a[212:215],v[180:183],v[12:15]
s_lshl_b32 s77, s77, 9
EX v1,v1
MM v[12:15],a[216:219],v[196:199],v[12:15]
s_add_u32 s77, s77, 0
v_lshrrev_b32 v253, 1, v248
v_add_u32 v253, s77, v253
MM v[12:15],a[220:223],v[212:215],v[12:15]
s_mov_b32 s77, LKEND_ka-LPD1_%=-16
v_min_u32 v253, s77, v253
CV v0,v0,v1
MM v[8:11],a[204:207],v[212:215],v[132:135]
s_getpc_b64 s[98:99]
LPD1_%=:
global_load_dword v253, v253, s[98:99]
MM v[8:11],a[200:203],v[196:199],v[8:11]
EX v2,v2
MM v[8:11],a[196:199],v[180:183],v[8:11]
EX v3,v3
MM v[8:11],a[192:195],v[164:167],v[8:11]
TR a[154:155],v233 offset:20992
CV v1,v2,v3
MM v[16:19],a[192:195],v[168:171],v[136:139]
EX v4,v4
MM v[16:19],a[196:199],v[184:187],v[16:19]
EX v5,v5
MM v[16:19],a[200:203],v[200:203],v[16:19]
TR a[156:157],v233 offset:25088
CV v2,v4,v5
MM v[16:19],a[204:207],v[216:219],v[16:19]
EX v6,v6
MM v[20:23],a[220:223],v[216:219],v[136:139]
EX v7,v7
MM v[20:23],a[216:219],v[200:203],v[20:23]
TR a[158:159],v233 offset:29184
CV v3,v6,v7
MM v[20:23],a[212:215],v[184:187],v[20:23]
EX v8,v8
MM v[20:23],a[208:211],v[168:171],v[20:23]
EX v9,v9
MM v[28:31],a[208:211],v[172:175],v[140:143]
s_add_u32 m0, s45, 147456
s_add_u32 s44, s44, 1024
buffer_load_dwordx4 v248, s[24:27], s44 offen offset:0 lds
MM v[28:31],a[212:215],v[188:191],v[28:31]
TR a[160:161],v232 offset:17408
CV v8,v8,v9
MM v[28:31],a[216:219],v[204:207],v[28:31]
EX v10,v10
MM v[28:31],a[220:223],v[220:223],v[28:31]
EX v11,v11
MM v[24:27],a[204:207],v[220:223],v[140:143]
TR a[162:163],v232 offset:21504
CV v9,v10,v11
MM v[24:27],a[200:203],v[204:207],v[24:27]
TR a[164:165],v232 offset:25600
TR a[166:167],v232 offset:29696
MM v[24:27],a[196:199],v[188:191],v[24:27]
EX v12,v12
MM v[24:27],a[192:195],v[172:175],v[24:27]
EX v13,v13
MM v[32:35],a[224:227],v[160:163],v[128:131]
TR a[168:169],v233 offset:17408
CV v10,v12,v13
MM v[32:35],a[228:231],v[176:179],v[32:35]
EX v14,v14
MM v[32:35],a[232:235],v[192:195],v[32:35]
TR a[170:171],v233 offset:21504
TR a[172:173],v233 offset:25600
MM v[32:35],a[236:239],v[208:211],v[32:35]
EX v15,v15
MM v[36:39],a[252:255],v[208:211],v[128:131]
TR a[174:175],v233 offset:29696
CV v11,v14,v15
MM v[36:39],a[248:251],v[192:195],v[36:39]
EX v16,v16
MM v[36:39],a[244:247],v[176:179],v[36:39]
EX v17,v17
MM v[36:39],a[240:243],v[160:163],v[36:39]
TR a[176:177],v232 offset:17920
CV v16,v16,v17
MM v[44:47],a[240:243],v[164:167],v[132:135]
s_add_u32 s67, s95, 15360
buffer_store_dwordx4 v[240:243], v248, s[20:23], s67 offen offset:0 sc1
MM v[44:47],a[244:247],v[180:183],v[44:47]
EX v18,v18
MM v[44:47],a[248:251],v[196:199],v[44:47]
TR a[178:179],v232 offset:22016
TR a[180:181],v232 offset:26112
MM v[44:47],a[252:255],v[212:215],v[44:47]
EX v19,v19
MM v[40:43],a[236:239],v[212:215],v[132:135]
TR a[182:183],v232 offset:30208
CV v17,v18,v19
MM v[40:43],a[232:235],v[196:199],v[40:43]
EX v20,v20
MM v[40:43],a[228:231],v[180:183],v[40:43]
EX v21,v21
MM v[40:43],a[224:227],v[164:167],v[40:43]
TR a[184:185],v233 offset:17920
CV v18,v20,v21
MM v[48:51],a[224:227],v[168:171],v[136:139]
EX v22,v22
MM v[48:51],a[228:231],v[184:187],v[48:51]
TR a[186:187],v233 offset:22016
TR a[188:189],v233 offset:26112
MM v[48:51],a[232:235],v[200:203],v[48:51]
EX v23,v23
MM v[48:51],a[236:239],v[216:219],v[48:51]
TR a[190:191],v233 offset:30208
CV v19,v22,v23
MM v[52:55],a[252:255],v[216:219],v[136:139]
EX v24,v24
MM v[52:55],a[248:251],v[200:203],v[52:55]
EX v25,v25
MM v[52:55],a[244:247],v[184:187],v[52:55]
EX v26,v26
MM v[52:55],a[240:243],v[168:171],v[52:55]
EX v27,v27
MM v[60:63],a[240:243],v[172:175],v[140:143]
EX v28,v28
MM v[60:63],a[244:247],v[188:191],v[60:63]
EX v29,v29
MM v[60:63],a[248:251],v[204:207],v[60:63]
EX v30,v30
MM v[60:63],a[252:255],v[220:223],v[60:63]
EX v31,v31
MM v[56:59],a[236:239],v[220:223],v[140:143]
EX v32,v32
MM v[56:59],a[232:235],v[204:207],v[56:59]
EX v33,v33
MM v[56:59],a[228:231],v[188:191],v[56:59]
EX v34,v34
MM v[56:59],a[224:227],v[172:175],v[56:59]
s_waitcnt vmcnt(16) lgkmcnt(0)
s_barrier
MM a[0:3],a[128:131],v[64:67],a[0:3]
s_add_u32 m0, s65, 16384
CV v24,v24,v25
CV v25,v26,v27
MM a[0:3],a[132:135],v[68:71],a[0:3]
RD a[192:195],v224 offset:49152
CV v26,v28,v29
MM a[8:11],a[132:135],v[76:79],a[8:11]
RD a[196:199],v225 offset:49152
CV v27,v30,v31
MM a[8:11],a[128:131],v[72:75],a[8:11]
buffer_load_dwordx4 v228, s[8:11], s64 offen offset:0 lds
MM a[16:19],a[128:131],v[80:83],a[16:19]
RD a[200:203],v226 offset:49152
CV v4,v32,v33
MM a[16:19],a[132:135],v[84:87],a[16:19]
buffer_load_dwordx4 v229, s[8:11], s64 offen offset:1024 lds
MM a[24:27],a[132:135],v[92:95],a[24:27]
RD a[204:207],v227 offset:49152
RD a[208:211],v224 offset:53248
MM a[24:27],a[128:131],v[88:91],a[24:27]
EX v35,v35
MM a[28:31],a[136:139],v[88:91],a[28:31]
RD a[212:215],v225 offset:53248
CV v5,v34,v35
MM a[28:31],a[140:143],v[92:95],a[28:31]
EX v36,v36
MM a[20:23],a[140:143],v[84:87],a[20:23]
buffer_load_dwordx4 v230, s[8:11], s64 offen offset:2048 lds
MM a[20:23],a[136:139],v[80:83],a[20:23]
EX v37,v37
MM a[12:15],a[136:139],v[72:75],a[12:15]
RD a[216:219],v226 offset:53248
CV v6,v36,v37
MM a[12:15],a[140:143],v[76:79],a[12:15]
EX v38,v38
MM a[4:7],a[140:143],v[68:71],a[4:7]
EX v39,v39
MM a[4:7],a[136:139],v[64:67],a[4:7]
)ASM"
R"ASM(
RD a[220:223],v227 offset:53248
CV v7,v38,v39
MM a[32:35],a[144:147],v[64:67],a[32:35]
EX v40,v40
MM a[32:35],a[148:151],v[68:71],a[32:35]
buffer_load_dwordx4 v231, s[8:11], s64 offen offset:3072 lds
MM a[40:43],a[148:151],v[76:79],a[40:43]
s_add_u32 m0, s65, 81920
EX v41,v41
MM a[40:43],a[144:147],v[72:75],a[40:43]
RD a[224:227],v224 offset:57344
CV v12,v40,v41
MM a[48:51],a[144:147],v[80:83],a[48:51]
EX v42,v42
MM a[48:51],a[148:151],v[84:87],a[48:51]
EX v43,v43
MM a[56:59],a[148:151],v[92:95],a[56:59]
RD a[228:231],v225 offset:57344
CV v13,v42,v43
MM a[56:59],a[144:147],v[88:91],a[56:59]
EX v44,v44
MM a[60:63],a[152:155],v[88:91],a[60:63]
buffer_load_dwordx4 v235, s[12:15], s81 offen offset:0 lds
MM a[60:63],a[156:159],v[92:95],a[60:63]
EX v45,v45
MM a[52:55],a[156:159],v[84:87],a[52:55]
RD a[232:235],v226 offset:57344
CV v14,v44,v45
MM a[52:55],a[152:155],v[80:83],a[52:55]
EX v46,v46
MM a[44:47],a[152:155],v[72:75],a[44:47]
EX v47,v47
MM a[44:47],a[156:159],v[76:79],a[44:47]
RD a[236:239],v227 offset:57344
CV v15,v46,v47
MM a[36:39],a[156:159],v[68:71],a[36:39]
buffer_load_dwordx4 v235, s[12:15], s64 offen offset:1024 lds
MM a[36:39],a[152:155],v[64:67],a[36:39]
EX v48,v48
MM a[64:67],a[160:163],v[64:67],a[64:67]
EX v49,v49
MM a[64:67],a[164:167],v[68:71],a[64:67]
RD a[240:243],v224 offset:61440
CV v20,v48,v49
MM a[72:75],a[164:167],v[76:79],a[72:75]
EX v50,v50
MM a[72:75],a[160:163],v[72:75],a[72:75]
RD a[244:247],v225 offset:61440
RD a[248:251],v226 offset:61440
MM a[80:83],a[160:163],v[80:83],a[80:83]
EX v51,v51
MM a[80:83],a[164:167],v[84:87],a[80:83]
RD a[252:255],v227 offset:61440
CV v21,v50,v51
MM a[88:91],a[164:167],v[92:95],a[88:91]
EX v52,v52
MM a[88:91],a[160:163],v[88:91],a[88:91]
buffer_load_dwordx4 v235, s[12:15], s81 offen offset:2048 lds
MM a[92:95],a[168:171],v[88:91],a[92:95]
EX v53,v53
MM a[92:95],a[172:175],v[92:95],a[92:95]
EX v54,v54
MM a[84:87],a[172:175],v[84:87],a[84:87]
buffer_load_dwordx4 v235, s[12:15], s64 offen offset:3072 lds
MM a[84:87],a[168:171],v[80:83],a[84:87]
EX v55,v55
MM a[76:79],a[168:171],v[72:75],a[76:79]
EX v56,v56
LPG14_%=:
s_cbranch_execz LTR2_%=
MM a[76:79],a[172:175],v[76:79],a[76:79]
EX v57,v57
MM a[68:71],a[172:175],v[68:71],a[68:71]
EX v58,v58
MM a[68:71],a[168:171],v[64:67],a[68:71]
EX v59,v59
MM a[96:99],a[176:179],v[64:67],a[96:99]
EX v60,v60
MM a[96:99],a[180:183],v[68:71],a[96:99]
EX v61,v61
MM a[104:107],a[180:183],v[76:79],a[104:107]
EX v62,v62
MM a[104:107],a[176:179],v[72:75],a[104:107]
EX v63,v63
MM a[112:115],a[176:179],v[80:83],a[112:115]
CV v22,v52,v53
CV v23,v54,v55
MM a[112:115],a[180:183],v[84:87],a[112:115]
CV v28,v56,v57
CV v29,v58,v59
MM a[120:123],a[180:183],v[92:95],a[120:123]
CV v30,v60,v61
CV v31,v62,v63
MM a[120:123],a[176:179],v[88:91],a[120:123]
MM a[124:127],a[184:187],v[88:91],a[124:127]
MM a[124:127],a[188:191],v[92:95],a[124:127]
MM a[116:119],a[188:191],v[84:87],a[116:119]
MM a[116:119],a[184:187],v[80:83],a[116:119]
MM a[108:111],a[184:187],v[72:75],a[108:111]
MM a[108:111],a[188:191],v[76:79],a[108:111]
MM a[100:103],a[188:191],v[68:71],a[100:103]
MM a[100:103],a[184:187],v[64:67],a[100:103]
v_smfmac_f32_16x16x64_bf16 v[236:239], v[144:147], v[0:7], v234
v_smfmac_f32_16x16x64_bf16 v[236:239], v[148:151], v[8:15], v234
v_smfmac_f32_16x16x64_bf16 v[236:239], v[152:155], v[16:23], v234
v_smfmac_f32_16x16x64_bf16 v[236:239], v[156:159], v[24:31], v234
s_waitcnt lgkmcnt(0)
MM v[64:67],a[192:195],v[160:163],v[128:131]
s_add_u32 s64, s64, 16384
TR a[128:129],v232 offset:32768
s_add_u32 s81, s64, 896
MM v[64:67],a[196:199],v[176:179],v[64:67]
TR a[130:131],v232 offset:36864
TR a[132:133],v232 offset:40960
MM v[64:67],a[200:203],v[192:195],v[64:67]
TR a[134:135],v232 offset:45056
TR a[136:137],v233 offset:32768
MM v[64:67],a[204:207],v[208:211],v[64:67]
TR a[138:139],v233 offset:36864
v_add_f32 v246, v236, v237
MM v[68:71],a[220:223],v[208:211],v[128:131]
TR a[140:141],v233 offset:40960
v_cmp_nge_f32 vcc, s71, v246
MM v[68:71],a[216:219],v[192:195],v[68:71]
s_cbranch_vccnz LSITE22_%=
LBACK22_%=:
TR a[142:143],v233 offset:45056
TR a[144:145],v232 offset:33280
MM v[68:71],a[212:215],v[176:179],v[68:71]
s_add_u32 s67, s95, 16384
buffer_store_dwordx4 v[240:243], v248, s[20:23], s67 offen offset:0 sc1
MM v[68:71],a[208:211],v[160:163],v[68:71]
TR a[146:147],v232 offset:37376
TR a[148:149],v232 offset:41472
MM v[76:79],a[208:211],v[164:167],v[132:135]
EX v64,v64
MM v[76:79],a[212:215],v[180:183],v[76:79]
EX v65,v65
MM v[76:79],a[216:219],v[196:199],v[76:79]
TR a[150:151],v232 offset:45568
CV v64,v64,v65
MM v[76:79],a[220:223],v[212:215],v[76:79]
EX v66,v66
MM v[72:75],a[204:207],v[212:215],v[132:135]
EX v67,v67
MM v[72:75],a[200:203],v[196:199],v[72:75]
TR a[152:153],v233 offset:33280
CV v65,v66,v67
MM v[72:75],a[196:199],v[180:183],v[72:75]
EX v68,v68
MM v[72:75],a[192:195],v[164:167],v[72:75]
EX v69,v69
MM v[80:83],a[192:195],v[168:171],v[136:139]
TR a[154:155],v233 offset:37376
CV v66,v68,v69
MM v[80:83],a[196:199],v[184:187],v[80:83]
EX v70,v70
MM v[80:83],a[200:203],v[200:203],v[80:83]
EX v71,v71
MM v[80:83],a[204:207],v[216:219],v[80:83]
TR a[156:157],v233 offset:41472
CV v67,v70,v71
MM v[84:87],a[220:223],v[216:219],v[136:139]
EX v72,v72
MM v[84:87],a[216:219],v[200:203],v[84:87]
TR a[158:159],v233 offset:45568
TR a[160:161],v232 offset:33792
MM v[84:87],a[212:215],v[184:187],v[84:87]
EX v73,v73
MM v[84:87],a[208:211],v[168:171],v[84:87]
TR a[162:163],v232 offset:37888
CV v72,v72,v73
MM v[92:95],a[208:211],v[172:175],v[140:143]
EX v74,v74
MM v[92:95],a[212:215],v[188:191],v[92:95]
TR a[164:165],v232 offset:41984
TR a[166:167],v232 offset:46080
MM v[92:95],a[216:219],v[204:207],v[92:95]
EX v75,v75
MM v[92:95],a[220:223],v[220:223],v[92:95]
TR a[168:169],v233 offset:33792
CV v73,v74,v75
MM v[88:91],a[204:207],v[220:223],v[140:143]
EX v76,v76
MM v[88:91],a[200:203],v[204:207],v[88:91]
EX v77,v77
MM v[88:91],a[196:199],v[188:191],v[88:91]
TR a[170:171],v233 offset:37888
CV v74,v76,v77
MM v[88:91],a[192:195],v[172:175],v[88:91]
EX v78,v78
MM v[96:99],a[224:227],v[160:163],v[128:131]
TR a[172:173],v233 offset:41984
TR a[174:175],v233 offset:46080
MM v[96:99],a[228:231],v[176:179],v[96:99]
EX v79,v79
MM v[96:99],a[232:235],v[192:195],v[96:99]
TR a[176:177],v232 offset:34304
CV v75,v78,v79
MM v[96:99],a[236:239],v[208:211],v[96:99]
EX v80,v80
MM v[100:103],a[252:255],v[208:211],v[128:131]
EX v81,v81
MM v[100:103],a[248:251],v[192:195],v[100:103]
TR a[178:179],v232 offset:38400
CV v80,v80,v81
MM v[100:103],a[244:247],v[176:179],v[100:103]
EX v82,v82
MM v[100:103],a[240:243],v[160:163],v[100:103]
TR a[180:181],v232 offset:42496
TR a[182:183],v232 offset:46592
MM v[108:111],a[240:243],v[164:167],v[132:135]
s_add_u32 s67, s95, 17408
buffer_store_dwordx4 v[240:243], v248, s[20:23], s67 offen offset:0 sc1
MM v[108:111],a[244:247],v[180:183],v[108:111]
EX v83,v83
MM v[108:111],a[248:251],v[196:199],v[108:111]
TR a[184:185],v233 offset:34304
CV v81,v82,v83
MM v[108:111],a[252:255],v[212:215],v[108:111]
EX v84,v84
MM v[104:107],a[236:239],v[212:215],v[132:135]
EX v85,v85
MM v[104:107],a[232:235],v[196:199],v[104:107]
TR a[186:187],v233 offset:38400
CV v82,v84,v85
MM v[104:107],a[228:231],v[180:183],v[104:107]
EX v86,v86
MM v[104:107],a[224:227],v[164:167],v[104:107]
TR a[188:189],v233 offset:42496
TR a[190:191],v233 offset:46592
MM v[112:115],a[224:227],v[168:171],v[136:139]
EX v87,v87
MM v[112:115],a[228:231],v[184:187],v[112:115]
CV v83,v86,v87
MM v[112:115],a[232:235],v[200:203],v[112:115]
EX v88,v88
MM v[112:115],a[236:239],v[216:219],v[112:115]
EX v89,v89
MM v[116:119],a[252:255],v[216:219],v[136:139]
EX v90,v90
MM v[116:119],a[248:251],v[200:203],v[116:119]
EX v91,v91
MM v[116:119],a[244:247],v[184:187],v[116:119]
EX v92,v92
MM v[116:119],a[240:243],v[168:171],v[116:119]
EX v93,v93
MM v[124:127],a[240:243],v[172:175],v[140:143]
EX v94,v94
MM v[124:127],a[244:247],v[188:191],v[124:127]
EX v95,v95
MM v[124:127],a[248:251],v[204:207],v[124:127]
EX v96,v96
MM v[124:127],a[252:255],v[220:223],v[124:127]
EX v97,v97
MM v[120:123],a[236:239],v[220:223],v[140:143]
EX v98,v98
MM v[120:123],a[232:235],v[204:207],v[120:123]
EX v99,v99
MM v[120:123],a[228:231],v[188:191],v[120:123]
EX v100,v100
MM v[120:123],a[224:227],v[172:175],v[120:123]
s_waitcnt vmcnt(17) lgkmcnt(0)
s_barrier
MM a[0:3],a[128:131],v[0:3],a[0:3]
s_lshl_b32 s91, %[swave], 2
CV v88,v88,v89
CV v89,v90,v91
MM a[0:3],a[132:135],v[4:7],a[0:3]
s_add_u32 s91, s91, s48
RD a[192:195],v224 offset:0
CV v90,v92,v93
MM a[8:11],a[132:135],v[12:15],a[8:11]
s_add_u32 s91, s91, 69632
s_add_u32 m0, s65, 32768
CV v91,v94,v95
MM a[8:11],a[128:131],v[8:11],a[8:11]
v_mov_b32 v250, s69
s_mov_b64 exec, 1
buffer_store_dword v250, off, s[20:23], s91 offset:0 sc1
s_mov_b64 exec, -1
MM a[16:19],a[128:131],v[16:19],a[16:19]
buffer_load_dwordx4 v228, s[8:11], s64 offen offset:0 lds
MM a[16:19],a[132:135],v[20:23],a[16:19]
RD a[196:199],v225 offset:0
CV v68,v96,v97
MM a[24:27],a[132:135],v[28:31],a[24:27]
RD a[200:203],v226 offset:0
CV v69,v98,v99
MM a[24:27],a[128:131],v[24:27],a[24:27]
buffer_load_dwordx4 v229, s[8:11], s64 offen offset:1024 lds
MM a[28:31],a[136:139],v[24:27],a[28:31]
RD a[204:207],v227 offset:0
RD a[208:211],v224 offset:4096
MM a[28:31],a[140:143],v[28:31],a[28:31]
RD a[212:215],v225 offset:4096
RD a[216:219],v226 offset:4096
MM a[20:23],a[140:143],v[20:23],a[20:23]
buffer_load_dwordx4 v230, s[8:11], s64 offen offset:2048 lds
MM a[20:23],a[136:139],v[16:19],a[20:23]
EX v101,v101
MM a[12:15],a[136:139],v[8:11],a[12:15]
RD a[220:223],v227 offset:4096
CV v70,v100,v101
MM a[12:15],a[140:143],v[12:15],a[12:15]
EX v102,v102
MM a[4:7],a[140:143],v[4:7],a[4:7]
EX v103,v103
MM a[4:7],a[136:139],v[0:3],a[4:7]
RD a[224:227],v224 offset:8192
CV v71,v102,v103
MM a[32:35],a[144:147],v[0:3],a[32:35]
EX v104,v104
MM a[32:35],a[148:151],v[4:7],a[32:35]
buffer_load_dwordx4 v231, s[8:11], s64 offen offset:3072 lds
MM a[40:43],a[148:151],v[12:15],a[40:43]
s_add_u32 m0, s65, 98304
EX v105,v105
MM a[40:43],a[144:147],v[8:11],a[40:43]
RD a[228:231],v225 offset:8192
CV v76,v104,v105
MM a[48:51],a[144:147],v[16:19],a[48:51]
EX v106,v106
MM a[48:51],a[148:151],v[20:23],a[48:51]
EX v107,v107
MM a[56:59],a[148:151],v[28:31],a[56:59]
RD a[232:235],v226 offset:8192
CV v77,v106,v107
MM a[56:59],a[144:147],v[24:27],a[56:59]
EX v108,v108
MM a[60:63],a[152:155],v[24:27],a[60:63]
buffer_load_dwordx4 v235, s[12:15], s81 offen offset:0 lds
MM a[60:63],a[156:159],v[28:31],a[60:63]
EX v109,v109
MM a[52:55],a[156:159],v[20:23],a[52:55]
RD a[236:239],v227 offset:8192
CV v78,v108,v109
MM a[52:55],a[152:155],v[16:19],a[52:55]
EX v110,v110
MM a[44:47],a[152:155],v[8:11],a[44:47]
EX v111,v111
MM a[44:47],a[156:159],v[12:15],a[44:47]
RD a[240:243],v224 offset:12288
CV v79,v110,v111
MM a[36:39],a[156:159],v[4:7],a[36:39]
buffer_load_dwordx4 v235, s[12:15], s64 offen offset:1024 lds
MM a[36:39],a[152:155],v[0:3],a[36:39]
EX v112,v112
MM a[64:67],a[160:163],v[0:3],a[64:67]
EX v113,v113
MM a[64:67],a[164:167],v[4:7],a[64:67]
RD a[244:247],v225 offset:12288
CV v84,v112,v113
MM a[72:75],a[164:167],v[12:15],a[72:75]
EX v114,v114
MM a[72:75],a[160:163],v[8:11],a[72:75]
EX v115,v115
MM a[80:83],a[160:163],v[16:19],a[80:83]
RD a[248:251],v226 offset:12288
CV v85,v114,v115
MM a[80:83],a[164:167],v[20:23],a[80:83]
buffer_load_dwordx4 v235, s[12:15], s81 offen offset:2048 lds
MM a[88:91],a[164:167],v[28:31],a[88:91]
EX v116,v116
MM a[88:91],a[160:163],v[24:27],a[88:91]
EX v117,v117
MM a[92:95],a[168:171],v[24:27],a[92:95]
RD a[252:255],v227 offset:12288
CV v86,v116,v117
MM a[92:95],a[172:175],v[28:31],a[92:95]
EX v118,v118
MM a[84:87],a[172:175],v[20:23],a[84:87]
buffer_load_dwordx4 v235, s[12:15], s64 offen offset:3072 lds
MM a[84:87],a[168:171],v[16:19],a[84:87]
EX v119,v119
MM a[76:79],a[168:171],v[8:11],a[76:79]
CV v87,v118,v119
MM a[76:79],a[172:175],v[12:15],a[76:79]
EX v120,v120
MM a[68:71],a[172:175],v[4:7],a[68:71]
EX v121,v121
MM a[68:71],a[168:171],v[0:3],a[68:71]
EX v122,v122
MM a[96:99],a[176:179],v[0:3],a[96:99]
)ASM"
R"ASM(
EX v123,v123
MM a[96:99],a[180:183],v[4:7],a[96:99]
EX v124,v124
MM a[104:107],a[180:183],v[12:15],a[104:107]
EX v125,v125
MM a[104:107],a[176:179],v[8:11],a[104:107]
EX v126,v126
MM a[112:115],a[176:179],v[16:19],a[112:115]
EX v127,v127
MM a[112:115],a[180:183],v[20:23],a[112:115]
CV v92,v120,v121
CV v93,v122,v123
MM a[120:123],a[180:183],v[28:31],a[120:123]
CV v94,v124,v125
CV v95,v126,v127
MM a[120:123],a[176:179],v[24:27],a[120:123]
MM a[124:127],a[184:187],v[24:27],a[124:127]
MM a[124:127],a[188:191],v[28:31],a[124:127]
MM a[116:119],a[188:191],v[20:23],a[116:119]
MM a[116:119],a[184:187],v[16:19],a[116:119]
MM a[108:111],a[184:187],v[8:11],a[108:111]
MM a[108:111],a[188:191],v[12:15],a[108:111]
MM a[100:103],a[188:191],v[4:7],a[100:103]
MM a[100:103],a[184:187],v[0:3],a[100:103]
v_smfmac_f32_16x16x64_bf16 v[236:239], v[144:147], v[64:71], v234
v_smfmac_f32_16x16x64_bf16 v[236:239], v[148:151], v[72:79], v234
v_smfmac_f32_16x16x64_bf16 v[236:239], v[152:155], v[80:87], v234
v_smfmac_f32_16x16x64_bf16 v[236:239], v[156:159], v[88:95], v234
s_waitcnt lgkmcnt(0)
MM v[0:3],a[192:195],v[160:163],v[128:131]
s_add_u32 s64, s64, 16384
TR a[128:129],v232 offset:49152
s_add_u32 s81, s64, 896
MM v[0:3],a[196:199],v[176:179],v[0:3]
TR a[130:131],v232 offset:53248
TR a[132:133],v232 offset:57344
MM v[0:3],a[200:203],v[192:195],v[0:3]
TR a[134:135],v232 offset:61440
TR a[136:137],v233 offset:49152
MM v[0:3],a[204:207],v[208:211],v[0:3]
TR a[138:139],v233 offset:53248
v_add_f32 v247, v236, v237
MM v[4:7],a[220:223],v[208:211],v[128:131]
TR a[140:141],v233 offset:57344
v_cmp_nge_f32 vcc, s71, v247
MM v[4:7],a[216:219],v[192:195],v[4:7]
s_cbranch_vccnz LSITE23_%=
LBACK23_%=:
TR a[142:143],v233 offset:61440
TR a[144:145],v232 offset:49664
MM v[4:7],a[212:215],v[176:179],v[4:7]
s_add_u32 s67, s95, 18432
buffer_store_dwordx4 v[240:243], v248, s[20:23], s67 offen offset:0 sc1
MM v[4:7],a[208:211],v[160:163],v[4:7]
TR a[146:147],v232 offset:53760
TR a[148:149],v232 offset:57856
MM v[12:15],a[208:211],v[164:167],v[132:135]
EX v0,v0
MM v[12:15],a[212:215],v[180:183],v[12:15]
EX v1,v1
MM v[12:15],a[216:219],v[196:199],v[12:15]
TR a[150:151],v232 offset:61952
CV v0,v0,v1
MM v[12:15],a[220:223],v[212:215],v[12:15]
EX v2,v2
MM v[8:11],a[204:207],v[212:215],v[132:135]
EX v3,v3
MM v[8:11],a[200:203],v[196:199],v[8:11]
TR a[152:153],v233 offset:49664
CV v1,v2,v3
MM v[8:11],a[196:199],v[180:183],v[8:11]
EX v4,v4
MM v[8:11],a[192:195],v[164:167],v[8:11]
EX v5,v5
MM v[16:19],a[192:195],v[168:171],v[136:139]
TR a[154:155],v233 offset:53760
CV v2,v4,v5
MM v[16:19],a[196:199],v[184:187],v[16:19]
EX v6,v6
MM v[16:19],a[200:203],v[200:203],v[16:19]
EX v7,v7
MM v[16:19],a[204:207],v[216:219],v[16:19]
TR a[156:157],v233 offset:57856
CV v3,v6,v7
MM v[20:23],a[220:223],v[216:219],v[136:139]
EX v8,v8
MM v[20:23],a[216:219],v[200:203],v[20:23]
TR a[158:159],v233 offset:61952
TR a[160:161],v232 offset:50176
MM v[20:23],a[212:215],v[184:187],v[20:23]
EX v9,v9
MM v[20:23],a[208:211],v[168:171],v[20:23]
TR a[162:163],v232 offset:54272
CV v8,v8,v9
MM v[28:31],a[208:211],v[172:175],v[140:143]
s_add_u32 m0, s45, 147456
s_add_u32 s44, s44, 1024
buffer_load_dwordx4 v248, s[24:27], s44 offen offset:0 lds
MM v[28:31],a[212:215],v[188:191],v[28:31]
EX v10,v10
MM v[28:31],a[216:219],v[204:207],v[28:31]
EX v11,v11
MM v[28:31],a[220:223],v[220:223],v[28:31]
TR a[164:165],v232 offset:58368
CV v9,v10,v11
MM v[24:27],a[204:207],v[220:223],v[140:143]
EX v12,v12
MM v[24:27],a[200:203],v[204:207],v[24:27]
TR a[166:167],v232 offset:62464
TR a[168:169],v233 offset:50176
MM v[24:27],a[196:199],v[188:191],v[24:27]
EX v13,v13
MM v[24:27],a[192:195],v[172:175],v[24:27]
TR a[170:171],v233 offset:54272
CV v10,v12,v13
MM v[32:35],a[224:227],v[160:163],v[128:131]
EX v14,v14
MM v[32:35],a[228:231],v[176:179],v[32:35]
EX v15,v15
MM v[32:35],a[232:235],v[192:195],v[32:35]
TR a[172:173],v233 offset:58368
CV v11,v14,v15
MM v[32:35],a[236:239],v[208:211],v[32:35]
TR a[174:175],v233 offset:62464
TR a[176:177],v232 offset:50688
MM v[36:39],a[252:255],v[208:211],v[128:131]
EX v16,v16
MM v[36:39],a[248:251],v[192:195],v[36:39]
EX v17,v17
MM v[36:39],a[244:247],v[176:179],v[36:39]
TR a[178:179],v232 offset:54784
CV v16,v16,v17
MM v[36:39],a[240:243],v[160:163],v[36:39]
EX v18,v18
MM v[44:47],a[240:243],v[164:167],v[132:135]
s_add_u32 s67, s95, 19456
buffer_store_dwordx4 v[240:243], v248, s[20:23], s67 offen offset:0 sc1
MM v[44:47],a[244:247],v[180:183],v[44:47]
EX v19,v19
MM v[44:47],a[248:251],v[196:199],v[44:47]
TR a[180:181],v232 offset:58880
CV v17,v18,v19
MM v[44:47],a[252:255],v[212:215],v[44:47]
EX v20,v20
MM v[40:43],a[236:239],v[212:215],v[132:135]
TR a[182:183],v232 offset:62976
TR a[184:185],v233 offset:50688
MM v[40:43],a[232:235],v[196:199],v[40:43]
EX v21,v21
MM v[40:43],a[228:231],v[180:183],v[40:43]
TR a[186:187],v233 offset:54784
CV v18,v20,v21
MM v[40:43],a[224:227],v[164:167],v[40:43]
EX v22,v22
MM v[48:51],a[224:227],v[168:171],v[136:139]
TR a[188:189],v233 offset:58880
TR a[190:191],v233 offset:62976
MM v[48:51],a[228:231],v[184:187],v[48:51]
EX v23,v23
MM v[48:51],a[232:235],v[200:203],v[48:51]
CV v19,v22,v23
MM v[48:51],a[236:239],v[216:219],v[48:51]
EX v24,v24
MM v[52:55],a[252:255],v[216:219],v[136:139]
EX v25,v25
MM v[52:55],a[248:251],v[200:203],v[52:55]
EX v26,v26
MM v[52:55],a[244:247],v[184:187],v[52:55]
EX v27,v27
MM v[52:55],a[240:243],v[168:171],v[52:55]
EX v28,v28
MM v[60:63],a[240:243],v[172:175],v[140:143]
EX v29,v29
MM v[60:63],a[244:247],v[188:191],v[60:63]
EX v30,v30
MM v[60:63],a[248:251],v[204:207],v[60:63]
EX v31,v31
MM v[60:63],a[252:255],v[220:223],v[60:63]
EX v32,v32
MM v[56:59],a[236:239],v[220:223],v[140:143]
EX v33,v33
MM v[56:59],a[232:235],v[204:207],v[56:59]
EX v34,v34
MM v[56:59],a[228:231],v[188:191],v[56:59]
EX v35,v35
MM v[56:59],a[224:227],v[172:175],v[56:59]
s_waitcnt vmcnt(18) lgkmcnt(0)
s_barrier
MM a[0:3],a[128:131],v[64:67],a[0:3]
s_add_u32 m0, s65, 49152
CV v24,v24,v25
CV v25,v26,v27
MM a[0:3],a[132:135],v[68:71],a[0:3]
RD a[192:195],v224 offset:16384
CV v26,v28,v29
MM a[8:11],a[132:135],v[76:79],a[8:11]
RD a[196:199],v225 offset:16384
CV v27,v30,v31
MM a[8:11],a[128:131],v[72:75],a[8:11]
buffer_load_dwordx4 v228, s[8:11], s64 offen offset:0 lds
MM a[16:19],a[128:131],v[80:83],a[16:19]
RD a[200:203],v226 offset:16384
CV v4,v32,v33
MM a[16:19],a[132:135],v[84:87],a[16:19]
RD a[204:207],v227 offset:16384
CV v5,v34,v35
MM a[24:27],a[132:135],v[92:95],a[24:27]
buffer_load_dwordx4 v229, s[8:11], s64 offen offset:1024 lds
MM a[24:27],a[128:131],v[88:91],a[24:27]
RD a[208:211],v224 offset:20480
RD a[212:215],v225 offset:20480
MM a[28:31],a[136:139],v[88:91],a[28:31]
buffer_load_dwordx4 v230, s[8:11], s64 offen offset:2048 lds
MM a[28:31],a[140:143],v[92:95],a[28:31]
EX v36,v36
MM a[20:23],a[140:143],v[84:87],a[20:23]
EX v37,v37
MM a[20:23],a[136:139],v[80:83],a[20:23]
RD a[216:219],v226 offset:20480
CV v6,v36,v37
MM a[12:15],a[136:139],v[72:75],a[12:15]
EX v38,v38
MM a[12:15],a[140:143],v[76:79],a[12:15]
RD a[220:223],v227 offset:20480
RD a[224:227],v224 offset:24576
MM a[4:7],a[140:143],v[68:71],a[4:7]
EX v39,v39
MM a[4:7],a[136:139],v[64:67],a[4:7]
RD a[228:231],v225 offset:24576
CV v7,v38,v39
MM a[32:35],a[144:147],v[64:67],a[32:35]
EX v40,v40
MM a[32:35],a[148:151],v[68:71],a[32:35]
buffer_load_dwordx4 v231, s[8:11], s64 offen offset:3072 lds
MM a[40:43],a[148:151],v[76:79],a[40:43]
s_add_u32 m0, s65, 114688
EX v41,v41
MM a[40:43],a[144:147],v[72:75],a[40:43]
RD a[232:235],v226 offset:24576
CV v12,v40,v41
MM a[48:51],a[144:147],v[80:83],a[48:51]
EX v42,v42
MM a[48:51],a[148:151],v[84:87],a[48:51]
EX v43,v43
MM a[56:59],a[148:151],v[92:95],a[56:59]
RD a[236:239],v227 offset:24576
CV v13,v42,v43
MM a[56:59],a[144:147],v[88:91],a[56:59]
EX v44,v44
MM a[60:63],a[152:155],v[88:91],a[60:63]
buffer_load_dwordx4 v235, s[12:15], s81 offen offset:0 lds
MM a[60:63],a[156:159],v[92:95],a[60:63]
EX v45,v45
MM a[52:55],a[156:159],v[84:87],a[52:55]
RD a[240:243],v224 offset:28672
CV v14,v44,v45
MM a[52:55],a[152:155],v[80:83],a[52:55]
EX v46,v46
MM a[44:47],a[152:155],v[72:75],a[44:47]
EX v47,v47
MM a[44:47],a[156:159],v[76:79],a[44:47]
RD a[244:247],v225 offset:28672
CV v15,v46,v47
MM a[36:39],a[156:159],v[68:71],a[36:39]
buffer_load_dwordx4 v235, s[12:15], s64 offen offset:1024 lds
MM a[36:39],a[152:155],v[64:67],a[36:39]
EX v48,v48
MM a[64:67],a[160:163],v[64:67],a[64:67]
LPG15_%=:
s_cbranch_execz LTR2_%=
EX v49,v49
MM a[64:67],a[164:167],v[68:71],a[64:67]
RD a[248:251],v226 offset:28672
CV v20,v48,v49
MM a[72:75],a[164:167],v[76:79],a[72:75]
EX v50,v50
MM a[72:75],a[160:163],v[72:75],a[72:75]
EX v51,v51
MM a[80:83],a[160:163],v[80:83],a[80:83]
RD a[252:255],v227 offset:28672
CV v21,v50,v51
MM a[80:83],a[164:167],v[84:87],a[80:83]
buffer_load_dwordx4 v235, s[12:15], s81 offen offset:2048 lds
MM a[88:91],a[164:167],v[92:95],a[88:91]
EX v52,v52
MM a[88:91],a[160:163],v[88:91],a[88:91]
EX v53,v53
MM a[92:95],a[168:171],v[88:91],a[92:95]
EX v54,v54
MM a[92:95],a[172:175],v[92:95],a[92:95]
buffer_load_dwordx4 v235, s[12:15], s64 offen offset:3072 lds
MM a[84:87],a[172:175],v[84:87],a[84:87]
EX v55,v55
MM a[84:87],a[168:171],v[80:83],a[84:87]
EX v56,v56
MM a[76:79],a[168:171],v[72:75],a[76:79]
EX v57,v57
MM a[76:79],a[172:175],v[76:79],a[76:79]
EX v58,v58
MM a[68:71],a[172:175],v[68:71],a[68:71]
EX v59,v59
MM a[68:71],a[168:171],v[64:67],a[68:71]
EX v60,v60
MM a[96:99],a[176:179],v[64:67],a[96:99]
EX v61,v61
MM a[96:99],a[180:183],v[68:71],a[96:99]
EX v62,v62
MM a[104:107],a[180:183],v[76:79],a[104:107]
EX v63,v63
MM a[104:107],a[176:179],v[72:75],a[104:107]
CV v22,v52,v53
CV v23,v54,v55
MM a[112:115],a[176:179],v[80:83],a[112:115]
CV v28,v56,v57
CV v29,v58,v59
MM a[112:115],a[180:183],v[84:87],a[112:115]
CV v30,v60,v61
CV v31,v62,v63
MM a[120:123],a[180:183],v[92:95],a[120:123]
MM a[120:123],a[176:179],v[88:91],a[120:123]
MM a[124:127],a[184:187],v[88:91],a[124:127]
MM a[124:127],a[188:191],v[92:95],a[124:127]
MM a[116:119],a[188:191],v[84:87],a[116:119]
MM a[116:119],a[184:187],v[80:83],a[116:119]
MM a[108:111],a[184:187],v[72:75],a[108:111]
MM a[108:111],a[188:191],v[76:79],a[108:111]
MM a[100:103],a[188:191],v[68:71],a[100:103]
MM a[100:103],a[184:187],v[64:67],a[100:103]
v_smfmac_f32_16x16x64_bf16 v[236:239], v[144:147], v[0:7], v234
v_smfmac_f32_16x16x64_bf16 v[236:239], v[148:151], v[8:15], v234
v_smfmac_f32_16x16x64_bf16 v[236:239], v[152:155], v[16:23], v234
v_smfmac_f32_16x16x64_bf16 v[236:239], v[156:159], v[24:31], v234
s_waitcnt lgkmcnt(0)
MM v[64:67],a[192:195],v[160:163],v[128:131]
s_add_u32 s64, s64, 16384
TR a[128:129],v232 offset:0
s_add_u32 s81, s64, 896
MM v[64:67],a[196:199],v[176:179],v[64:67]
TR a[130:131],v232 offset:4096
TR a[132:133],v232 offset:8192
MM v[64:67],a[200:203],v[192:195],v[64:67]
TR a[134:135],v232 offset:12288
TR a[136:137],v233 offset:0
MM v[64:67],a[204:207],v[208:211],v[64:67]
TR a[138:139],v233 offset:4096
v_add_f32 v246, v236, v237
MM v[68:71],a[220:223],v[208:211],v[128:131]
TR a[140:141],v233 offset:8192
v_cmp_nge_f32 vcc, s71, v246
MM v[68:71],a[216:219],v[192:195],v[68:71]
s_cbranch_vccnz LSITE24_%=
LBACK24_%=:
TR a[142:143],v233 offset:12288
TR a[144:145],v232 offset:512
MM v[68:71],a[212:215],v[176:179],v[68:71]
v_bfe_u32 v242, v248, 6, 3
v_mul_u32_u24 v242, 1280, v242
MM v[68:71],a[208:211],v[160:163],v[68:71]
v_and_b32 v240, 48, v248
v_bfe_u32 v243, v248, 8, 1
MM v[76:79],a[208:211],v[164:167],v[132:135]
v_lshlrev_b32 v243, 5, v243
v_xor_b32 v240, v240, v243
MM v[76:79],a[212:215],v[180:183],v[76:79]
v_add_u32 v240, v240, v242
v_lshrrev_b32 v243, 9, v248
MM v[76:79],a[216:219],v[196:199],v[76:79]
v_mul_u32_u24 v242, 10240, v243
v_add_u32 v241, v240, v242
MM v[76:79],a[220:223],v[212:215],v[76:79]
TR a[146:147],v232 offset:4608
v_lshl_add_u32 v240, v243, 6, v240
MM v[72:75],a[204:207],v[212:215],v[132:135]
s_and_b32 s66, s84, 1
EX v64,v64
MM v[72:75],a[200:203],v[196:199],v[72:75]
s_lshl_b32 s94, s66, 6
TR a[148:149],v232 offset:8704
v_add_u32 v240, s94, v240
)ASM"
R"ASM(
MM v[72:75],a[196:199],v[180:183],v[72:75]
s_xor_b32 s66, s66, 1
EX v65,v65
MM v[72:75],a[192:195],v[164:167],v[72:75]
s_lshl_b32 s66, s66, 8
v_add_u32 v241, s66, v241
CV v64,v64,v65
MM v[80:83],a[192:195],v[168:171],v[136:139]
s_mul_i32 s89, %[swave], 20480
EX v66,v66
MM v[80:83],a[196:199],v[184:187],v[80:83]
s_lshl_b32 s94, %[swave], 10
EX v67,v67
MM v[80:83],a[200:203],v[200:203],v[80:83]
s_add_u32 s94, s94, %[slds]
EX v68,v68
MM v[80:83],a[204:207],v[216:219],v[80:83]
s_xor_b32 s66, s84, 0
EX v69,v69
MM v[84:87],a[220:223],v[216:219],v[136:139]
s_mul_i32 s66, s66, 163840
CV v65,v66,v67
CV v66,v68,v69
MM v[84:87],a[216:219],v[200:203],v[84:87]
s_add_u32 s85, s66, s89
EX v70,v70
MM v[84:87],a[212:215],v[184:187],v[84:87]
s_xor_b32 s66, s84, 1
TR a[150:151],v232 offset:12800
TR a[152:153],v233 offset:512
MM v[84:87],a[208:211],v[168:171],v[84:87]
s_mul_i32 s66, s66, 163840
EX v71,v71
MM v[92:95],a[208:211],v[172:175],v[140:143]
s_add_u32 s86, s66, s89
s_add_u32 m0, s45, 147456
s_add_u32 s44, s44, 1024
buffer_load_dwordx4 v248, s[24:27], s44 offen offset:0 lds
MM v[92:95],a[212:215],v[188:191],v[92:95]
s_xor_b32 s66, s84, 2
EX v72,v72
MM v[92:95],a[216:219],v[204:207],v[92:95]
s_mul_i32 s66, s66, 163840
TR a[154:155],v233 offset:4608
CV v67,v70,v71
MM v[92:95],a[220:223],v[220:223],v[92:95]
s_add_u32 s87, s66, s89
EX v73,v73
MM v[88:91],a[204:207],v[220:223],v[140:143]
s_xor_b32 s66, s84, 3
TR a[156:157],v233 offset:8704
TR a[158:159],v233 offset:12800
MM v[88:91],a[200:203],v[204:207],v[88:91]
s_mul_i32 s66, s66, 163840
EX v74,v74
MM v[88:91],a[196:199],v[188:191],v[88:91]
s_add_u32 s88, s66, s89
EX v75,v75
MM v[88:91],a[192:195],v[172:175],v[88:91]
s_lshl_b32 s89, %[swave], 11
TR a[160:161],v232 offset:1024
CV v72,v72,v73
MM v[96:99],a[224:227],v[160:163],v[128:131]
s_add_u32 s89, s89, %[slds]
EX v76,v76
MM v[96:99],a[228:231],v[176:179],v[96:99]
s_lshr_b32 s96, s49, 2
TR a[162:163],v232 offset:5120
CV v73,v74,v75
MM v[96:99],a[232:235],v[192:195],v[96:99]
s_lshl_b32 s97, s50, 3
EX v77,v77
MM v[96:99],a[236:239],v[208:211],v[96:99]
s_add_u32 s96, s96, s97
TR a[164:165],v232 offset:9216
TR a[166:167],v232 offset:13312
MM v[100:103],a[252:255],v[208:211],v[128:131]
s_lshl_b32 s96, s96, 2
EX v78,v78
MM v[100:103],a[248:251],v[192:195],v[100:103]
s_add_u32 s96, s96, %[swave]
EX v79,v79
MM v[100:103],a[244:247],v[176:179],v[100:103]
s_lshl_b32 s96, s96, 6
TR a[168:169],v233 offset:1024
CV v74,v76,v77
MM v[100:103],a[240:243],v[160:163],v[100:103]
s_add_u32 s96, s96, 0
buffer_load_dword v242, off, s[20:23], s96 sc1
MM v[108:111],a[240:243],v[164:167],v[132:135]
buffer_load_dword v243, off, s[20:23], s19 sc1
MM v[108:111],a[244:247],v[180:183],v[108:111]
EX v80,v80
MM v[108:111],a[248:251],v[196:199],v[108:111]
EX v81,v81
MM v[108:111],a[252:255],v[212:215],v[108:111]
TR a[170:171],v233 offset:5120
TR a[172:173],v233 offset:9216
MM v[104:107],a[236:239],v[212:215],v[132:135]
EX v82,v82
MM v[104:107],a[232:235],v[196:199],v[104:107]
TR a[174:175],v233 offset:13312
CV v75,v78,v79
MM v[104:107],a[228:231],v[180:183],v[104:107]
EX v83,v83
MM v[104:107],a[224:227],v[164:167],v[104:107]
TR a[176:177],v232 offset:1536
CV v80,v80,v81
MM v[112:115],a[224:227],v[168:171],v[136:139]
EX v84,v84
MM v[112:115],a[228:231],v[184:187],v[112:115]
TR a[178:179],v232 offset:5632
TR a[180:181],v232 offset:9728
MM v[112:115],a[232:235],v[200:203],v[112:115]
EX v85,v85
MM v[112:115],a[236:239],v[216:219],v[112:115]
EX v86,v86
MM v[116:119],a[252:255],v[216:219],v[136:139]
TR a[182:183],v232 offset:13824
CV v81,v82,v83
MM v[116:119],a[248:251],v[200:203],v[116:119]
EX v87,v87
MM v[116:119],a[244:247],v[184:187],v[116:119]
TR a[184:185],v233 offset:1536
CV v82,v84,v85
MM v[116:119],a[240:243],v[168:171],v[116:119]
EX v88,v88
MM v[124:127],a[240:243],v[172:175],v[140:143]
TR a[186:187],v233 offset:5632
CV v83,v86,v87
MM v[124:127],a[244:247],v[188:191],v[124:127]
EX v89,v89
MM v[124:127],a[248:251],v[204:207],v[124:127]
TR a[188:189],v233 offset:9728
TR a[190:191],v233 offset:13824
MM v[124:127],a[252:255],v[220:223],v[124:127]
EX v90,v90
MM v[120:123],a[236:239],v[220:223],v[140:143]
EX v91,v91
MM v[120:123],a[232:235],v[204:207],v[120:123]
EX v92,v92
MM v[120:123],a[228:231],v[188:191],v[120:123]
EX v93,v93
MM v[120:123],a[224:227],v[172:175],v[120:123]
s_waitcnt vmcnt(18) lgkmcnt(0)
s_barrier
MM a[0:3],a[128:131],v[0:3],a[0:3]
s_add_u32 m0, s65, 0
CV v88,v88,v89
CV v89,v90,v91
MM a[0:3],a[132:135],v[4:7],a[0:3]
EX v94,v94
MM a[8:11],a[132:135],v[12:15],a[8:11]
RD a[192:195],v224 offset:32768
CV v90,v92,v93
MM a[8:11],a[128:131],v[8:11],a[8:11]
EX v95,v95
MM a[16:19],a[128:131],v[16:19],a[16:19]
RD a[196:199],v225 offset:32768
CV v91,v94,v95
MM a[16:19],a[132:135],v[20:23],a[16:19]
EX v96,v96
MM a[24:27],a[132:135],v[28:31],a[24:27]
buffer_load_dwordx4 v228, s[8:11], s64 offen offset:0 lds
MM a[24:27],a[128:131],v[24:27],a[24:27]
EX v97,v97
MM a[28:31],a[136:139],v[24:27],a[28:31]
EX v98,v98
MM a[28:31],a[140:143],v[28:31],a[28:31]
RD a[200:203],v226 offset:32768
CV v68,v96,v97
MM a[20:23],a[140:143],v[20:23],a[20:23]
EX v99,v99
MM a[20:23],a[136:139],v[16:19],a[20:23]
RD a[204:207],v227 offset:32768
CV v69,v98,v99
MM a[12:15],a[136:139],v[8:11],a[12:15]
EX v100,v100
MM a[12:15],a[140:143],v[12:15],a[12:15]
buffer_load_dwordx4 v229, s[8:11], s64 offen offset:1024 lds
MM a[4:7],a[140:143],v[4:7],a[4:7]
EX v101,v101
MM a[4:7],a[136:139],v[0:3],a[4:7]
RD a[208:211],v224 offset:36864
CV v70,v100,v101
MM a[32:35],a[144:147],v[0:3],a[32:35]
EX v102,v102
MM a[32:35],a[148:151],v[4:7],a[32:35]
EX v103,v103
MM a[40:43],a[148:151],v[12:15],a[40:43]
RD a[212:215],v225 offset:36864
CV v71,v102,v103
MM a[40:43],a[144:147],v[8:11],a[40:43]
EX v104,v104
MM a[48:51],a[144:147],v[16:19],a[48:51]
buffer_load_dwordx4 v230, s[8:11], s64 offen offset:2048 lds
MM a[48:51],a[148:151],v[20:23],a[48:51]
EX v105,v105
MM a[56:59],a[148:151],v[28:31],a[56:59]
RD a[216:219],v226 offset:36864
CV v76,v104,v105
MM a[56:59],a[144:147],v[24:27],a[56:59]
EX v106,v106
MM a[60:63],a[152:155],v[24:27],a[60:63]
EX v107,v107
MM a[60:63],a[156:159],v[28:31],a[60:63]
RD a[220:223],v227 offset:36864
CV v77,v106,v107
MM a[52:55],a[156:159],v[20:23],a[52:55]
EX v108,v108
MM a[52:55],a[152:155],v[16:19],a[52:55]
buffer_load_dwordx4 v231, s[8:11], s64 offen offset:3072 lds
MM a[44:47],a[152:155],v[8:11],a[44:47]
s_add_u32 m0, s65, 65536
EX v109,v109
MM a[44:47],a[156:159],v[12:15],a[44:47]
RD a[224:227],v224 offset:40960
CV v78,v108,v109
MM a[36:39],a[156:159],v[4:7],a[36:39]
EX v110,v110
MM a[36:39],a[152:155],v[0:3],a[36:39]
EX v111,v111
MM a[64:67],a[160:163],v[0:3],a[64:67]
RD a[228:231],v225 offset:40960
CV v79,v110,v111
MM a[64:67],a[164:167],v[4:7],a[64:67]
EX v112,v112
MM a[72:75],a[164:167],v[12:15],a[72:75]
buffer_load_dwordx4 v235, s[12:15], s81 offen offset:0 lds
MM a[72:75],a[160:163],v[8:11],a[72:75]
EX v113,v113
MM a[80:83],a[160:163],v[16:19],a[80:83]
RD a[232:235],v226 offset:40960
CV v84,v112,v113
MM a[80:83],a[164:167],v[20:23],a[80:83]
EX v114,v114
MM a[88:91],a[164:167],v[28:31],a[88:91]
EX v115,v115
MM a[88:91],a[160:163],v[24:27],a[88:91]
RD a[236:239],v227 offset:40960
CV v85,v114,v115
MM a[92:95],a[168:171],v[24:27],a[92:95]
EX v116,v116
MM a[92:95],a[172:175],v[28:31],a[92:95]
buffer_load_dwordx4 v235, s[12:15], s64 offen offset:1024 lds
MM a[84:87],a[172:175],v[20:23],a[84:87]
EX v117,v117
MM a[84:87],a[168:171],v[16:19],a[84:87]
RD a[240:243],v224 offset:45056
CV v86,v116,v117
MM a[76:79],a[168:171],v[8:11],a[76:79]
EX v118,v118
MM a[76:79],a[172:175],v[12:15],a[76:79]
EX v119,v119
MM a[68:71],a[172:175],v[4:7],a[68:71]
RD a[244:247],v225 offset:45056
CV v87,v118,v119
MM a[68:71],a[168:171],v[0:3],a[68:71]
buffer_load_dwordx4 v235, s[12:15], s81 offen offset:2048 lds
MM a[96:99],a[176:179],v[0:3],a[96:99]
EX v120,v120
MM a[96:99],a[180:183],v[4:7],a[96:99]
EX v121,v121
MM a[104:107],a[180:183],v[12:15],a[104:107]
RD a[248:251],v226 offset:45056
CV v92,v120,v121
MM a[104:107],a[176:179],v[8:11],a[104:107]
EX v122,v122
MM a[112:115],a[176:179],v[16:19],a[112:115]
EX v123,v123
MM a[112:115],a[180:183],v[20:23],a[112:115]
RD a[252:255],v227 offset:45056
CV v93,v122,v123
MM a[120:123],a[180:183],v[28:31],a[120:123]
buffer_load_dwordx4 v235, s[12:15], s64 offen offset:3072 lds
MM a[120:123],a[176:179],v[24:27],a[120:123]
EX v124,v124
MM a[124:127],a[184:187],v[24:27],a[124:127]
EX v125,v125
MM a[124:127],a[188:191],v[28:31],a[124:127]
EX v126,v126
MM a[116:119],a[188:191],v[20:23],a[116:119]
EX v127,v127
MM a[116:119],a[184:187],v[16:19],a[116:119]
CV v94,v124,v125
CV v95,v126,v127
MM a[108:111],a[184:187],v[8:11],a[108:111]
MM a[108:111],a[188:191],v[12:15],a[108:111]
MM a[100:103],a[188:191],v[4:7],a[100:103]
MM a[100:103],a[184:187],v[0:3],a[100:103]
v_smfmac_f32_16x16x64_bf16 v[236:239], v[144:147], v[64:71], v234
v_smfmac_f32_16x16x64_bf16 v[236:239], v[148:151], v[72:79], v234
v_smfmac_f32_16x16x64_bf16 v[236:239], v[152:155], v[80:87], v234
v_smfmac_f32_16x16x64_bf16 v[236:239], v[156:159], v[88:95], v234
s_waitcnt lgkmcnt(0)
MM v[0:3],a[192:195],v[160:163],v[128:131]
s_add_u32 s64, s64, 16384
TR a[128:129],v232 offset:16384
s_add_u32 s81, s64, 896
MM v[0:3],a[196:199],v[176:179],v[0:3]
TR a[130:131],v232 offset:20480
TR a[132:133],v232 offset:24576
MM v[0:3],a[200:203],v[192:195],v[0:3]
TR a[134:135],v232 offset:28672
TR a[136:137],v233 offset:16384
MM v[0:3],a[204:207],v[208:211],v[0:3]
TR a[138:139],v233 offset:20480
v_add_f32 v247, v236, v237
MM v[4:7],a[220:223],v[208:211],v[128:131]
TR a[140:141],v233 offset:24576
v_cmp_nge_f32 vcc, s71, v247
MM v[4:7],a[216:219],v[192:195],v[4:7]
s_cbranch_vccnz LSITE25_%=
LBACK25_%=:
TR a[142:143],v233 offset:28672
TR a[144:145],v232 offset:16896
MM v[4:7],a[212:215],v[176:179],v[4:7]
TR a[146:147],v232 offset:20992
TR a[148:149],v232 offset:25088
MM v[4:7],a[208:211],v[160:163],v[4:7]
s_mul_i32 s76, s46, 192
TR a[150:151],v232 offset:29184
TR a[152:153],v233 offset:16896
MM v[12:15],a[208:211],v[164:167],v[132:135]
s_add_u32 s76, s76, 69632
v_lshrrev_b32 v253, 2, v248
v_min_u32 v253, 188, v253
MM v[12:15],a[212:215],v[180:183],v[12:15]
buffer_load_dword v252, v253, s[20:23], s76 offen offset:0 sc1
MM v[12:15],a[216:219],v[196:199],v[12:15]
EX v0,v0
MM v[12:15],a[220:223],v[212:215],v[12:15]
EX v1,v1
MM v[8:11],a[204:207],v[212:215],v[132:135]
EX v2,v2
MM v[8:11],a[200:203],v[196:199],v[8:11]
TR a[154:155],v233 offset:20992
CV v0,v0,v1
MM v[8:11],a[196:199],v[180:183],v[8:11]
EX v3,v3
MM v[8:11],a[192:195],v[164:167],v[8:11]
TR a[156:157],v233 offset:25088
CV v1,v2,v3
MM v[16:19],a[192:195],v[168:171],v[136:139]
EX v4,v4
MM v[16:19],a[196:199],v[184:187],v[16:19]
EX v5,v5
MM v[16:19],a[200:203],v[200:203],v[16:19]
TR a[158:159],v233 offset:29184
CV v2,v4,v5
MM v[16:19],a[204:207],v[216:219],v[16:19]
EX v6,v6
MM v[20:23],a[220:223],v[216:219],v[136:139]
EX v7,v7
MM v[20:23],a[216:219],v[200:203],v[20:23]
TR a[160:161],v232 offset:17408
CV v3,v6,v7
MM v[20:23],a[212:215],v[184:187],v[20:23]
EX v8,v8
MM v[20:23],a[208:211],v[168:171],v[20:23]
EX v9,v9
MM v[28:31],a[208:211],v[172:175],v[140:143]
TR a[162:163],v232 offset:21504
CV v8,v8,v9
MM v[28:31],a[212:215],v[188:191],v[28:31]
EX v10,v10
MM v[28:31],a[216:219],v[204:207],v[28:31]
EX v11,v11
MM v[28:31],a[220:223],v[220:223],v[28:31]
TR a[164:165],v232 offset:25600
CV v9,v10,v11
MM v[24:27],a[204:207],v[220:223],v[140:143]
EX v12,v12
MM v[24:27],a[200:203],v[204:207],v[24:27]
TR a[166:167],v232 offset:29696
TR a[168:169],v233 offset:17408
MM v[24:27],a[196:199],v[188:191],v[24:27]
EX v13,v13
MM v[24:27],a[192:195],v[172:175],v[24:27]
TR a[170:171],v233 offset:21504
CV v10,v12,v13
MM v[32:35],a[224:227],v[160:163],v[128:131]
EX v14,v14
MM v[32:35],a[228:231],v[176:179],v[32:35]
EX v15,v15
MM v[32:35],a[232:235],v[192:195],v[32:35]
TR a[172:173],v233 offset:25600
CV v11,v14,v15
MM v[32:35],a[236:239],v[208:211],v[32:35]
TR a[174:175],v233 offset:29696
TR a[176:177],v232 offset:17920
MM v[36:39],a[252:255],v[208:211],v[128:131]
)ASM"
R"ASM(
EX v16,v16
MM v[36:39],a[248:251],v[192:195],v[36:39]
EX v17,v17
MM v[36:39],a[244:247],v[176:179],v[36:39]
TR a[178:179],v232 offset:22016
CV v16,v16,v17
MM v[36:39],a[240:243],v[160:163],v[36:39]
EX v18,v18
MM v[44:47],a[240:243],v[164:167],v[132:135]
TR a[180:181],v232 offset:26112
TR a[182:183],v232 offset:30208
MM v[44:47],a[244:247],v[180:183],v[44:47]
EX v19,v19
MM v[44:47],a[248:251],v[196:199],v[44:47]
TR a[184:185],v233 offset:17920
CV v17,v18,v19
MM v[44:47],a[252:255],v[212:215],v[44:47]
EX v20,v20
MM v[40:43],a[236:239],v[212:215],v[132:135]
EX v21,v21
MM v[40:43],a[232:235],v[196:199],v[40:43]
TR a[186:187],v233 offset:22016
CV v18,v20,v21
MM v[40:43],a[228:231],v[180:183],v[40:43]
EX v22,v22
MM v[40:43],a[224:227],v[164:167],v[40:43]
TR a[188:189],v233 offset:26112
TR a[190:191],v233 offset:30208
MM v[48:51],a[224:227],v[168:171],v[136:139]
EX v23,v23
MM v[48:51],a[228:231],v[184:187],v[48:51]
CV v19,v22,v23
MM v[48:51],a[232:235],v[200:203],v[48:51]
EX v24,v24
MM v[48:51],a[236:239],v[216:219],v[48:51]
EX v25,v25
MM v[52:55],a[252:255],v[216:219],v[136:139]
EX v26,v26
MM v[52:55],a[248:251],v[200:203],v[52:55]
EX v27,v27
MM v[52:55],a[244:247],v[184:187],v[52:55]
EX v28,v28
MM v[52:55],a[240:243],v[168:171],v[52:55]
EX v29,v29
MM v[60:63],a[240:243],v[172:175],v[140:143]
EX v30,v30
MM v[60:63],a[244:247],v[188:191],v[60:63]
EX v31,v31
MM v[60:63],a[248:251],v[204:207],v[60:63]
EX v32,v32
MM v[60:63],a[252:255],v[220:223],v[60:63]
EX v33,v33
MM v[56:59],a[236:239],v[220:223],v[140:143]
EX v34,v34
MM v[56:59],a[232:235],v[204:207],v[56:59]
EX v35,v35
MM v[56:59],a[228:231],v[188:191],v[56:59]
EX v36,v36
MM v[56:59],a[224:227],v[172:175],v[56:59]
s_waitcnt vmcnt(16) lgkmcnt(0)
s_barrier
MM a[0:3],a[128:131],v[64:67],a[0:3]
s_waitcnt vmcnt(0)
v_cmp_ne_u32 s[90:91], s69, v252
CV v24,v24,v25
MM a[0:3],a[132:135],v[68:71],a[0:3]
s_add_u32 m0, s65, 16384
CV v25,v26,v27
CV v26,v28,v29
MM a[8:11],a[132:135],v[76:79],a[8:11]
s_cmp_lg_u64 s[90:91], 0
s_cbranch_scc1 LRFH_%=
LRFHb_%=:
v_lshrrev_b32 v253, 7, v248
MM a[8:11],a[128:131],v[72:75],a[8:11]
v_mul_u32_u24 v253, 3328, v253
v_add_u32 v253, v253, v248
MM a[16:19],a[128:131],v[80:83],a[16:19]
v_lshrrev_b32 v252, 6, v248
v_mul_u32_u24 v252, 3392, v252
MM a[16:19],a[132:135],v[84:87],a[16:19]
RD a[192:195],v224 offset:49152
v_add_u32 v252, v252, v248
MM a[24:27],a[132:135],v[92:95],a[24:27]
s_add_u32 s76, s76, 21557248
RD a[196:199],v225 offset:49152
CV v27,v30,v31
MM a[24:27],a[128:131],v[88:91],a[24:27]
s_lshl_b32 s90, %[swave], 11
buffer_load_dwordx4 v228, s[8:11], s64 offen offset:0 lds
MM a[28:31],a[136:139],v[88:91],a[28:31]
s_add_u32 s90, s90, %[slds]
s_add_u32 m0, s90, 139264
s_nop 0
buffer_load_dwordx4 v253, s[20:23], s76 offen offset:0 sc1 lds
MM a[28:31],a[140:143],v[92:95],a[28:31]
s_add_u32 m0, s65, 16384
RD a[200:203],v226 offset:49152
CV v4,v32,v33
LPG16_%=:
s_cbranch_execz LTR2_%=
MM a[20:23],a[140:143],v[84:87],a[20:23]
s_add_u32 s76, s76, 128
RD a[204:207],v227 offset:49152
CV v5,v34,v35
MM a[20:23],a[136:139],v[80:83],a[20:23]
s_add_u32 m0, s90, 140288
s_mov_b32 exec_hi, 0
buffer_load_dwordx4 v252, s[20:23], s76 offen offset:0 sc1 lds
s_mov_b64 exec, -1
MM a[12:15],a[136:139],v[72:75],a[12:15]
EX v37,v37
MM a[12:15],a[140:143],v[76:79],a[12:15]
RD a[208:211],v224 offset:53248
CV v6,v36,v37
MM a[4:7],a[140:143],v[68:71],a[4:7]
s_add_u32 m0, s65, 16384
s_nop 0
buffer_load_dwordx4 v229, s[8:11], s64 offen offset:1024 lds
MM a[4:7],a[136:139],v[64:67],a[4:7]
EX v38,v38
MM a[32:35],a[144:147],v[64:67],a[32:35]
EX v39,v39
MM a[32:35],a[148:151],v[68:71],a[32:35]
RD a[212:215],v225 offset:53248
CV v7,v38,v39
MM a[40:43],a[148:151],v[76:79],a[40:43]
buffer_load_dwordx4 v230, s[8:11], s64 offen offset:2048 lds
MM a[40:43],a[144:147],v[72:75],a[40:43]
EX v40,v40
MM a[48:51],a[144:147],v[80:83],a[48:51]
EX v41,v41
MM a[48:51],a[148:151],v[84:87],a[48:51]
RD a[216:219],v226 offset:53248
CV v12,v40,v41
MM a[56:59],a[148:151],v[92:95],a[56:59]
EX v42,v42
MM a[56:59],a[144:147],v[88:91],a[56:59]
EX v43,v43
MM a[60:63],a[152:155],v[88:91],a[60:63]
RD a[220:223],v227 offset:53248
CV v13,v42,v43
MM a[60:63],a[156:159],v[92:95],a[60:63]
buffer_load_dwordx4 v231, s[8:11], s64 offen offset:3072 lds
MM a[52:55],a[156:159],v[84:87],a[52:55]
s_add_u32 m0, s65, 81920
EX v44,v44
MM a[52:55],a[152:155],v[80:83],a[52:55]
EX v45,v45
MM a[44:47],a[152:155],v[72:75],a[44:47]
RD a[224:227],v224 offset:57344
CV v14,v44,v45
MM a[44:47],a[156:159],v[76:79],a[44:47]
EX v46,v46
MM a[36:39],a[156:159],v[68:71],a[36:39]
buffer_load_dwordx4 v235, s[12:15], s81 offen offset:0 lds
MM a[36:39],a[152:155],v[64:67],a[36:39]
EX v47,v47
MM a[64:67],a[160:163],v[64:67],a[64:67]
RD a[228:231],v225 offset:57344
CV v15,v46,v47
MM a[64:67],a[164:167],v[68:71],a[64:67]
EX v48,v48
MM a[72:75],a[164:167],v[76:79],a[72:75]
EX v49,v49
MM a[72:75],a[160:163],v[72:75],a[72:75]
RD a[232:235],v226 offset:57344
CV v20,v48,v49
MM a[80:83],a[160:163],v[80:83],a[80:83]
EX v50,v50
MM a[80:83],a[164:167],v[84:87],a[80:83]
RD a[236:239],v227 offset:57344
RD a[240:243],v224 offset:61440
MM a[88:91],a[164:167],v[92:95],a[88:91]
EX v51,v51
MM a[88:91],a[160:163],v[88:91],a[88:91]
RD a[244:247],v225 offset:61440
CV v21,v50,v51
MM a[92:95],a[168:171],v[88:91],a[92:95]
EX v52,v52
MM a[92:95],a[172:175],v[92:95],a[92:95]
buffer_load_dwordx4 v235, s[12:15], s64 offen offset:1024 lds
MM a[84:87],a[172:175],v[84:87],a[84:87]
EX v53,v53
MM a[84:87],a[168:171],v[80:83],a[84:87]
RD a[248:251],v226 offset:61440
CV v22,v52,v53
MM a[76:79],a[168:171],v[72:75],a[76:79]
EX v54,v54
MM a[76:79],a[172:175],v[76:79],a[76:79]
EX v55,v55
MM a[68:71],a[172:175],v[68:71],a[68:71]
RD a[252:255],v227 offset:61440
CV v23,v54,v55
MM a[68:71],a[168:171],v[64:67],a[68:71]
buffer_load_dwordx4 v235, s[12:15], s81 offen offset:2048 lds
MM a[96:99],a[176:179],v[64:67],a[96:99]
EX v56,v56
MM a[96:99],a[180:183],v[68:71],a[96:99]
EX v57,v57
MM a[104:107],a[180:183],v[76:79],a[104:107]
EX v58,v58
MM a[104:107],a[176:179],v[72:75],a[104:107]
buffer_load_dwordx4 v235, s[12:15], s64 offen offset:3072 lds
MM a[112:115],a[176:179],v[80:83],a[112:115]
EX v59,v59
MM a[112:115],a[180:183],v[84:87],a[112:115]
EX v60,v60
MM a[120:123],a[180:183],v[92:95],a[120:123]
EX v61,v61
MM a[120:123],a[176:179],v[88:91],a[120:123]
EX v62,v62
MM a[124:127],a[184:187],v[88:91],a[124:127]
EX v63,v63
MM a[124:127],a[188:191],v[92:95],a[124:127]
CV v28,v56,v57
CV v29,v58,v59
MM a[116:119],a[188:191],v[84:87],a[116:119]
CV v30,v60,v61
CV v31,v62,v63
MM a[116:119],a[184:187],v[80:83],a[116:119]
MM a[108:111],a[184:187],v[72:75],a[108:111]
MM a[108:111],a[188:191],v[76:79],a[108:111]
MM a[100:103],a[188:191],v[68:71],a[100:103]
MM a[100:103],a[184:187],v[64:67],a[100:103]
v_smfmac_f32_16x16x64_bf16 v[236:239], v[144:147], v[0:7], v234
v_smfmac_f32_16x16x64_bf16 v[236:239], v[148:151], v[8:15], v234
v_smfmac_f32_16x16x64_bf16 v[236:239], v[152:155], v[16:23], v234
v_smfmac_f32_16x16x64_bf16 v[236:239], v[156:159], v[24:31], v234
s_waitcnt lgkmcnt(0)
MM v[64:67],a[192:195],v[160:163],v[128:131]
s_add_u32 s64, s64, 16384
TR a[128:129],v232 offset:32768
s_add_u32 s81, s64, 896
MM v[64:67],a[196:199],v[176:179],v[64:67]
TR a[130:131],v232 offset:36864
TR a[132:133],v232 offset:40960
MM v[64:67],a[200:203],v[192:195],v[64:67]
TR a[134:135],v232 offset:45056
TR a[136:137],v233 offset:32768
MM v[64:67],a[204:207],v[208:211],v[64:67]
TR a[138:139],v233 offset:36864
v_add_f32 v246, v236, v237
MM v[68:71],a[220:223],v[208:211],v[128:131]
TR a[140:141],v233 offset:40960
v_cmp_nge_f32 vcc, s71, v246
MM v[68:71],a[216:219],v[192:195],v[68:71]
s_cbranch_vccnz LSITE26_%=
LBACK26_%=:
TR a[142:143],v233 offset:45056
TR a[144:145],v232 offset:33280
MM v[68:71],a[212:215],v[176:179],v[68:71]
s_add_u32 m0, s94, 131072
s_add_u32 s66, s85, 0
buffer_load_dwordx4 v241, s[24:27], s66 offen offset:0 lds
MM v[68:71],a[208:211],v[160:163],v[68:71]
TR a[146:147],v232 offset:37376
TR a[148:149],v232 offset:41472
MM v[76:79],a[208:211],v[164:167],v[132:135]
EX v64,v64
MM v[76:79],a[212:215],v[180:183],v[76:79]
EX v65,v65
MM v[76:79],a[216:219],v[196:199],v[76:79]
TR a[150:151],v232 offset:45568
CV v64,v64,v65
MM v[76:79],a[220:223],v[212:215],v[76:79]
EX v66,v66
MM v[72:75],a[204:207],v[212:215],v[132:135]
EX v67,v67
MM v[72:75],a[200:203],v[196:199],v[72:75]
TR a[152:153],v233 offset:33280
CV v65,v66,v67
MM v[72:75],a[196:199],v[180:183],v[72:75]
s_add_u32 m0, s94, 135168
s_add_u32 s66, s85, 81920
buffer_load_dwordx4 v241, s[24:27], s66 offen offset:0 lds
MM v[72:75],a[192:195],v[164:167],v[72:75]
EX v68,v68
MM v[80:83],a[192:195],v[168:171],v[136:139]
EX v69,v69
MM v[80:83],a[196:199],v[184:187],v[80:83]
TR a[154:155],v233 offset:37376
CV v66,v68,v69
MM v[80:83],a[200:203],v[200:203],v[80:83]
EX v70,v70
MM v[80:83],a[204:207],v[216:219],v[80:83]
EX v71,v71
MM v[84:87],a[220:223],v[216:219],v[136:139]
TR a[156:157],v233 offset:41472
CV v67,v70,v71
MM v[84:87],a[216:219],v[200:203],v[84:87]
EX v72,v72
MM v[84:87],a[212:215],v[184:187],v[84:87]
s_add_u32 m0, s94, 147456
s_add_u32 s66, s87, 0
buffer_load_dwordx4 v241, s[24:27], s66 offen offset:0 lds
MM v[84:87],a[208:211],v[168:171],v[84:87]
TR a[158:159],v233 offset:45568
TR a[160:161],v232 offset:33792
MM v[92:95],a[208:211],v[172:175],v[140:143]
EX v73,v73
MM v[92:95],a[212:215],v[188:191],v[92:95]
TR a[162:163],v232 offset:37888
CV v72,v72,v73
MM v[92:95],a[216:219],v[204:207],v[92:95]
EX v74,v74
MM v[92:95],a[220:223],v[220:223],v[92:95]
EX v75,v75
MM v[88:91],a[204:207],v[220:223],v[140:143]
TR a[164:165],v232 offset:41984
CV v73,v74,v75
MM v[88:91],a[200:203],v[204:207],v[88:91]
EX v76,v76
MM v[88:91],a[196:199],v[188:191],v[88:91]
TR a[166:167],v232 offset:46080
TR a[168:169],v233 offset:33792
MM v[88:91],a[192:195],v[172:175],v[88:91]
EX v77,v77
MM v[96:99],a[224:227],v[160:163],v[128:131]
TR a[170:171],v233 offset:37888
CV v74,v76,v77
MM v[96:99],a[228:231],v[176:179],v[96:99]
EX v78,v78
MM v[96:99],a[232:235],v[192:195],v[96:99]
EX v79,v79
MM v[96:99],a[236:239],v[208:211],v[96:99]
TR a[172:173],v233 offset:41984
CV v75,v78,v79
MM v[100:103],a[252:255],v[208:211],v[128:131]
EX v80,v80
MM v[100:103],a[248:251],v[192:195],v[100:103]
TR a[174:175],v233 offset:46080
TR a[176:177],v232 offset:34304
MM v[100:103],a[244:247],v[176:179],v[100:103]
EX v81,v81
MM v[100:103],a[240:243],v[160:163],v[100:103]
TR a[178:179],v232 offset:38400
CV v80,v80,v81
MM v[108:111],a[240:243],v[164:167],v[132:135]
EX v82,v82
MM v[108:111],a[244:247],v[180:183],v[108:111]
EX v83,v83
MM v[108:111],a[248:251],v[196:199],v[108:111]
TR a[180:181],v232 offset:42496
CV v81,v82,v83
MM v[108:111],a[252:255],v[212:215],v[108:111]
EX v84,v84
MM v[104:107],a[236:239],v[212:215],v[132:135]
TR a[182:183],v232 offset:46592
TR a[184:185],v233 offset:34304
MM v[104:107],a[232:235],v[196:199],v[104:107]
EX v85,v85
MM v[104:107],a[228:231],v[180:183],v[104:107]
TR a[186:187],v233 offset:38400
CV v82,v84,v85
MM v[104:107],a[224:227],v[164:167],v[104:107]
EX v86,v86
MM v[112:115],a[224:227],v[168:171],v[136:139]
TR a[188:189],v233 offset:42496
TR a[190:191],v233 offset:46592
MM v[112:115],a[228:231],v[184:187],v[112:115]
EX v87,v87
MM v[112:115],a[232:235],v[200:203],v[112:115]
CV v83,v86,v87
MM v[112:115],a[236:239],v[216:219],v[112:115]
EX v88,v88
MM v[116:119],a[252:255],v[216:219],v[136:139]
EX v89,v89
MM v[116:119],a[248:251],v[200:203],v[116:119]
EX v90,v90
MM v[116:119],a[244:247],v[184:187],v[116:119]
EX v91,v91
MM v[116:119],a[240:243],v[168:171],v[116:119]
EX v92,v92
MM v[124:127],a[240:243],v[172:175],v[140:143]
EX v93,v93
MM v[124:127],a[244:247],v[188:191],v[124:127]
EX v94,v94
MM v[124:127],a[248:251],v[204:207],v[124:127]
EX v95,v95
MM v[124:127],a[252:255],v[220:223],v[124:127]
EX v96,v96
MM v[120:123],a[236:239],v[220:223],v[140:143]
EX v97,v97
MM v[120:123],a[232:235],v[204:207],v[120:123]
EX v98,v98
MM v[120:123],a[228:231],v[188:191],v[120:123]
s_getpc_b64 s[98:99]
LTA3_%=:
s_lshl_b32 s73, %[swave], 12
s_add_u32 s73, s73, LPG17_%=-LTA3_%=
)ASM"
R"ASM(
s_add_u32 s98, s98, s73
s_addc_u32 s99, s99, 0
s_mov_b64 exec, 0
s_setpc_b64 s[98:99]
LTR3_%=:
s_mov_b64 exec, -1
s_nop 0
CV v88,v88,v89
MM v[120:123],a[224:227],v[172:175],v[120:123]
s_waitcnt lgkmcnt(0)
s_barrier
MM a[0:3],a[128:131],v[0:3],a[0:3]
s_add_u32 m0, s65, 32768
CV v89,v90,v91
CV v90,v92,v93
MM a[0:3],a[132:135],v[4:7],a[0:3]
RD a[192:195],v224 offset:0
CV v91,v94,v95
MM a[8:11],a[132:135],v[12:15],a[8:11]
buffer_load_dwordx4 v228, s[8:11], s64 offen offset:0 lds
MM a[8:11],a[128:131],v[8:11],a[8:11]
RD a[196:199],v225 offset:0
CV v68,v96,v97
MM a[16:19],a[128:131],v[16:19],a[16:19]
RD a[200:203],v226 offset:0
RD a[204:207],v227 offset:0
MM a[16:19],a[132:135],v[20:23],a[16:19]
buffer_load_dwordx4 v229, s[8:11], s64 offen offset:1024 lds
MM a[24:27],a[132:135],v[28:31],a[24:27]
RD a[208:211],v224 offset:4096
RD a[212:215],v225 offset:4096
MM a[24:27],a[128:131],v[24:27],a[24:27]
EX v99,v99
MM a[28:31],a[136:139],v[24:27],a[28:31]
RD a[216:219],v226 offset:4096
CV v69,v98,v99
MM a[28:31],a[140:143],v[28:31],a[28:31]
EX v100,v100
MM a[20:23],a[140:143],v[20:23],a[20:23]
buffer_load_dwordx4 v230, s[8:11], s64 offen offset:2048 lds
MM a[20:23],a[136:139],v[16:19],a[20:23]
EX v101,v101
MM a[12:15],a[136:139],v[8:11],a[12:15]
RD a[220:223],v227 offset:4096
CV v70,v100,v101
MM a[12:15],a[140:143],v[12:15],a[12:15]
EX v102,v102
MM a[4:7],a[140:143],v[4:7],a[4:7]
EX v103,v103
MM a[4:7],a[136:139],v[0:3],a[4:7]
RD a[224:227],v224 offset:8192
CV v71,v102,v103
MM a[32:35],a[144:147],v[0:3],a[32:35]
EX v104,v104
MM a[32:35],a[148:151],v[4:7],a[32:35]
buffer_load_dwordx4 v231, s[8:11], s64 offen offset:3072 lds
MM a[40:43],a[148:151],v[12:15],a[40:43]
s_add_u32 m0, s65, 98304
EX v105,v105
MM a[40:43],a[144:147],v[8:11],a[40:43]
RD a[228:231],v225 offset:8192
CV v76,v104,v105
MM a[48:51],a[144:147],v[16:19],a[48:51]
EX v106,v106
MM a[48:51],a[148:151],v[20:23],a[48:51]
EX v107,v107
MM a[56:59],a[148:151],v[28:31],a[56:59]
RD a[232:235],v226 offset:8192
CV v77,v106,v107
MM a[56:59],a[144:147],v[24:27],a[56:59]
EX v108,v108
MM a[60:63],a[152:155],v[24:27],a[60:63]
buffer_load_dwordx4 v235, s[12:15], s81 offen offset:0 lds
MM a[60:63],a[156:159],v[28:31],a[60:63]
EX v109,v109
MM a[52:55],a[156:159],v[20:23],a[52:55]
RD a[236:239],v227 offset:8192
CV v78,v108,v109
MM a[52:55],a[152:155],v[16:19],a[52:55]
EX v110,v110
MM a[44:47],a[152:155],v[8:11],a[44:47]
EX v111,v111
MM a[44:47],a[156:159],v[12:15],a[44:47]
RD a[240:243],v224 offset:12288
CV v79,v110,v111
MM a[36:39],a[156:159],v[4:7],a[36:39]
buffer_load_dwordx4 v235, s[12:15], s64 offen offset:1024 lds
MM a[36:39],a[152:155],v[0:3],a[36:39]
EX v112,v112
MM a[64:67],a[160:163],v[0:3],a[64:67]
EX v113,v113
MM a[64:67],a[164:167],v[4:7],a[64:67]
RD a[244:247],v225 offset:12288
CV v84,v112,v113
MM a[72:75],a[164:167],v[12:15],a[72:75]
EX v114,v114
MM a[72:75],a[160:163],v[8:11],a[72:75]
EX v115,v115
MM a[80:83],a[160:163],v[16:19],a[80:83]
RD a[248:251],v226 offset:12288
CV v85,v114,v115
MM a[80:83],a[164:167],v[20:23],a[80:83]
buffer_load_dwordx4 v235, s[12:15], s81 offen offset:2048 lds
MM a[88:91],a[164:167],v[28:31],a[88:91]
EX v116,v116
MM a[88:91],a[160:163],v[24:27],a[88:91]
EX v117,v117
MM a[92:95],a[168:171],v[24:27],a[92:95]
RD a[252:255],v227 offset:12288
CV v86,v116,v117
MM a[92:95],a[172:175],v[28:31],a[92:95]
EX v118,v118
MM a[84:87],a[172:175],v[20:23],a[84:87]
buffer_load_dwordx4 v235, s[12:15], s64 offen offset:3072 lds
MM a[84:87],a[168:171],v[16:19],a[84:87]
EX v119,v119
MM a[76:79],a[168:171],v[8:11],a[76:79]
CV v87,v118,v119
MM a[76:79],a[172:175],v[12:15],a[76:79]
EX v120,v120
MM a[68:71],a[172:175],v[4:7],a[68:71]
EX v121,v121
MM a[68:71],a[168:171],v[0:3],a[68:71]
EX v122,v122
MM a[96:99],a[176:179],v[0:3],a[96:99]
EX v123,v123
MM a[96:99],a[180:183],v[4:7],a[96:99]
EX v124,v124
MM a[104:107],a[180:183],v[12:15],a[104:107]
EX v125,v125
MM a[104:107],a[176:179],v[8:11],a[104:107]
EX v126,v126
MM a[112:115],a[176:179],v[16:19],a[112:115]
EX v127,v127
MM a[112:115],a[180:183],v[20:23],a[112:115]
CV v92,v120,v121
CV v93,v122,v123
MM a[120:123],a[180:183],v[28:31],a[120:123]
CV v94,v124,v125
CV v95,v126,v127
MM a[120:123],a[176:179],v[24:27],a[120:123]
MM a[124:127],a[184:187],v[24:27],a[124:127]
MM a[124:127],a[188:191],v[28:31],a[124:127]
MM a[116:119],a[188:191],v[20:23],a[116:119]
MM a[116:119],a[184:187],v[16:19],a[116:119]
MM a[108:111],a[184:187],v[8:11],a[108:111]
MM a[108:111],a[188:191],v[12:15],a[108:111]
MM a[100:103],a[188:191],v[4:7],a[100:103]
MM a[100:103],a[184:187],v[0:3],a[100:103]
v_smfmac_f32_16x16x64_bf16 v[236:239], v[144:147], v[64:71], v234
v_smfmac_f32_16x16x64_bf16 v[236:239], v[148:151], v[72:79], v234
v_smfmac_f32_16x16x64_bf16 v[236:239], v[152:155], v[80:87], v234
v_smfmac_f32_16x16x64_bf16 v[236:239], v[156:159], v[88:95], v234
s_waitcnt lgkmcnt(0)
MM v[0:3],a[192:195],v[160:163],v[128:131]
s_add_u32 s64, s64, 16384
TR a[128:129],v232 offset:49152
s_add_u32 s81, s64, 896
MM v[0:3],a[196:199],v[176:179],v[0:3]
TR a[130:131],v232 offset:53248
TR a[132:133],v232 offset:57344
MM v[0:3],a[200:203],v[192:195],v[0:3]
TR a[134:135],v232 offset:61440
TR a[136:137],v233 offset:49152
MM v[0:3],a[204:207],v[208:211],v[0:3]
TR a[138:139],v233 offset:53248
v_add_f32 v247, v236, v237
MM v[4:7],a[220:223],v[208:211],v[128:131]
TR a[140:141],v233 offset:57344
v_cmp_nge_f32 vcc, s71, v247
MM v[4:7],a[216:219],v[192:195],v[4:7]
s_cbranch_vccnz LSITE27_%=
LBACK27_%=:
TR a[142:143],v233 offset:61440
TR a[144:145],v232 offset:49664
MM v[4:7],a[212:215],v[176:179],v[4:7]
v_lshrrev_b32 v125, 8, v248
v_lshlrev_b32 v124, 10, v125
MM v[4:7],a[208:211],v[160:163],v[4:7]
v_bfe_u32 v126, v248, 6, 2
v_lshl_add_u32 v124, v126, 7, v124
MM v[12:15],a[208:211],v[164:167],v[132:135]
v_bfe_u32 v126, v248, 4, 2
v_lshl_add_u32 v124, v126, 3, v124
MM v[12:15],a[212:215],v[180:183],v[12:15]
s_lshl_b32 s91, %[swave], 5
EX v0,v0
MM v[12:15],a[216:219],v[196:199],v[12:15]
s_add_u32 s91, s91, %[slds]
EX v1,v1
MM v[12:15],a[220:223],v[212:215],v[12:15]
s_add_u32 s91, s91, 151552
v_add_u32 v124, s91, v124
v_and_b32 v126, 112, v248
MM v[8:11],a[204:207],v[212:215],v[132:135]
s_lshl_b32 s91, %[swave], 11
EX v2,v2
MM v[8:11],a[200:203],v[196:199],v[8:11]
s_add_u32 s91, s91, %[slds]
TR a[146:147],v232 offset:53760
CV v0,v0,v1
MM v[8:11],a[196:199],v[180:183],v[8:11]
s_add_u32 s91, s91, 139264
v_lshl_add_u32 v127, v125, 4, s91
v_lshl_add_u32 v127, v126, 2, v127
MM v[8:11],a[192:195],v[164:167],v[8:11]
TR a[148:149],v232 offset:57856
v_lshl_add_u32 v126, v126, 2, v127
MM v[16:19],a[192:195],v[168:171],v[136:139]
s_waitcnt vmcnt(18)
TR v[96:97],v124 offset:0
TR v[98:99],v124 offset:512
MM v[16:19],a[196:199],v[184:187],v[16:19]
RD v[108:111],v126 offset:0
TR v[100:101],v124 offset:4096
MM v[16:19],a[200:203],v[200:203],v[16:19]
TR v[102:103],v124 offset:4608
RD v[112:115],v126 offset:64
MM v[16:19],a[204:207],v[216:219],v[16:19]
TR v[104:105],v124 offset:8192
TR v[106:107],v124 offset:8704
MM v[20:23],a[220:223],v[216:219],v[136:139]
RD v[116:119],v127 offset:1024
v_bfe_u32 v127, v248, 4, 3
EX v3,v3
MM v[20:23],a[216:219],v[200:203],v[20:23]
v_mul_u32_u24 v127, 2560, v127
v_lshl_add_u32 v127, v125, 4, v127
EX v4,v4
MM v[20:23],a[212:215],v[184:187],v[20:23]
s_lshl_b32 s90, %[swave], 6
EX v5,v5
MM v[20:23],a[208:211],v[168:171],v[20:23]
s_lshl_b32 s91, s47, 8
EX v6,v6
MM v[28:31],a[208:211],v[172:175],v[140:143]
s_add_u32 s90, s90, s91
EX v7,v7
MM v[28:31],a[212:215],v[188:191],v[28:31]
s_mul_i32 s91, s46, 17920
TR a[150:151],v232 offset:61952
CV v1,v2,v3
MM v[28:31],a[216:219],v[204:207],v[28:31]
s_add_u32 s90, s90, s91
EX v8,v8
MM v[28:31],a[220:223],v[220:223],v[28:31]
s_add_u32 s90, s90, 21659648
TR a[152:153],v233 offset:49664
CV v2,v4,v5
MM v[24:27],a[204:207],v[220:223],v[140:143]
EX v9,v9
MM v[24:27],a[200:203],v[204:207],v[24:27]
TR a[154:155],v233 offset:53760
TR a[156:157],v233 offset:57856
MM v[24:27],a[196:199],v[188:191],v[24:27]
EX v10,v10
MM v[24:27],a[192:195],v[172:175],v[24:27]
EX v11,v11
MM v[32:35],a[224:227],v[160:163],v[128:131]
TR a[158:159],v233 offset:61952
CV v3,v6,v7
MM v[32:35],a[228:231],v[176:179],v[32:35]
EX v12,v12
MM v[32:35],a[232:235],v[192:195],v[32:35]
TR a[160:161],v232 offset:50176
CV v8,v8,v9
MM v[32:35],a[236:239],v[208:211],v[32:35]
s_waitcnt lgkmcnt(6)
EX v13,v13
MM v[36:39],a[252:255],v[208:211],v[128:131]
TR a[162:163],v232 offset:54272
TR a[164:165],v232 offset:58368
MM v[120:123],v[96:99],v[108:111],0
MM v[120:123],v[100:103],v[112:115],v[120:123]
MM v[120:123],v[104:107],v[116:119],v[120:123]
MM v[36:39],a[248:251],v[192:195],v[36:39]
EX v14,v14
MM v[36:39],a[244:247],v[176:179],v[36:39]
EX v15,v15
MM v[36:39],a[240:243],v[160:163],v[36:39]
TR a[166:167],v232 offset:62464
CV v9,v10,v11
MM v[44:47],a[240:243],v[164:167],v[132:135]
TR a[168:169],v233 offset:50176
CV v10,v12,v13
MM v[44:47],a[244:247],v[180:183],v[44:47]
EX v16,v16
MM v[44:47],a[248:251],v[196:199],v[44:47]
EX v17,v17
MM v[44:47],a[252:255],v[212:215],v[44:47]
s_mov_b32 exec_lo, 0x00ff00ff
s_mov_b32 exec_hi, 0x00ff00ff
buffer_store_dwordx4 v[120:123], v127, s[20:23], s90 offen offset:0 sc1
s_mov_b64 exec, -1
s_nop 0
MM v[40:43],a[236:239],v[212:215],v[132:135]
TR a[170:171],v233 offset:54272
TR a[172:173],v233 offset:58368
MM v[40:43],a[232:235],v[196:199],v[40:43]
LPG17_%=:
s_cbranch_execz LPG21_%=
EX v18,v18
MM v[40:43],a[228:231],v[180:183],v[40:43]
EX v19,v19
MM v[40:43],a[224:227],v[164:167],v[40:43]
TR a[174:175],v233 offset:62464
CV v11,v14,v15
MM v[48:51],a[224:227],v[168:171],v[136:139]
EX v20,v20
MM v[48:51],a[228:231],v[184:187],v[48:51]
TR a[176:177],v232 offset:50688
TR a[178:179],v232 offset:54784
MM v[48:51],a[232:235],v[200:203],v[48:51]
EX v21,v21
MM v[48:51],a[236:239],v[216:219],v[48:51]
TR a[180:181],v232 offset:58880
CV v16,v16,v17
MM v[52:55],a[252:255],v[216:219],v[136:139]
EX v22,v22
MM v[52:55],a[248:251],v[200:203],v[52:55]
TR a[182:183],v232 offset:62976
CV v17,v18,v19
MM v[52:55],a[244:247],v[184:187],v[52:55]
EX v23,v23
MM v[52:55],a[240:243],v[168:171],v[52:55]
TR a[184:185],v233 offset:50688
CV v18,v20,v21
MM v[60:63],a[240:243],v[172:175],v[140:143]
EX v24,v24
MM v[60:63],a[244:247],v[188:191],v[60:63]
TR a[186:187],v233 offset:54784
TR a[188:189],v233 offset:58880
MM v[60:63],a[248:251],v[204:207],v[60:63]
TR a[190:191],v233 offset:62976
CV v19,v22,v23
MM v[60:63],a[252:255],v[220:223],v[60:63]
EX v25,v25
MM v[56:59],a[236:239],v[220:223],v[140:143]
EX v26,v26
MM v[56:59],a[232:235],v[204:207],v[56:59]
EX v27,v27
MM v[56:59],a[228:231],v[188:191],v[56:59]
EX v28,v28
MM v[56:59],a[224:227],v[172:175],v[56:59]
s_waitcnt vmcnt(16) lgkmcnt(0)
s_barrier
MM a[0:3],a[128:131],v[64:67],a[0:3]
s_add_u32 m0, s65, 49152
CV v24,v24,v25
CV v25,v26,v27
MM a[0:3],a[132:135],v[68:71],a[0:3]
EX v29,v29
MM a[8:11],a[132:135],v[76:79],a[8:11]
EX v30,v30
MM a[8:11],a[128:131],v[72:75],a[8:11]
EX v31,v31
MM a[16:19],a[128:131],v[80:83],a[16:19]
RD a[192:195],v224 offset:16384
CV v26,v28,v29
MM a[16:19],a[132:135],v[84:87],a[16:19]
EX v32,v32
MM a[24:27],a[132:135],v[92:95],a[24:27]
RD a[196:199],v225 offset:16384
CV v27,v30,v31
MM a[24:27],a[128:131],v[88:91],a[24:27]
EX v33,v33
MM a[28:31],a[136:139],v[88:91],a[28:31]
buffer_load_dwordx4 v228, s[8:11], s64 offen offset:0 lds
MM a[28:31],a[140:143],v[92:95],a[28:31]
EX v34,v34
MM a[20:23],a[140:143],v[84:87],a[20:23]
EX v35,v35
MM a[20:23],a[136:139],v[80:83],a[20:23]
RD a[200:203],v226 offset:16384
CV v4,v32,v33
MM a[12:15],a[136:139],v[72:75],a[12:15]
EX v36,v36
MM a[12:15],a[140:143],v[76:79],a[12:15]
RD a[204:207],v227 offset:16384
CV v5,v34,v35
MM a[4:7],a[140:143],v[68:71],a[4:7]
EX v37,v37
MM a[4:7],a[136:139],v[64:67],a[4:7]
buffer_load_dwordx4 v229, s[8:11], s64 offen offset:1024 lds
MM a[32:35],a[144:147],v[64:67],a[32:35]
EX v38,v38
MM a[32:35],a[148:151],v[68:71],a[32:35]
RD a[208:211],v224 offset:20480
CV v6,v36,v37
MM a[40:43],a[148:151],v[76:79],a[40:43]
EX v39,v39
MM a[40:43],a[144:147],v[72:75],a[40:43]
RD a[212:215],v225 offset:20480
CV v7,v38,v39
)ASM"
R"ASM(
MM a[48:51],a[144:147],v[80:83],a[48:51]
EX v40,v40
MM a[48:51],a[148:151],v[84:87],a[48:51]
EX v41,v41
MM a[56:59],a[148:151],v[92:95],a[56:59]
buffer_load_dwordx4 v230, s[8:11], s64 offen offset:2048 lds
MM a[56:59],a[144:147],v[88:91],a[56:59]
EX v42,v42
MM a[60:63],a[152:155],v[88:91],a[60:63]
RD a[216:219],v226 offset:20480
CV v12,v40,v41
MM a[60:63],a[156:159],v[92:95],a[60:63]
EX v43,v43
MM a[52:55],a[156:159],v[84:87],a[52:55]
RD a[220:223],v227 offset:20480
CV v13,v42,v43
MM a[52:55],a[152:155],v[80:83],a[52:55]
EX v44,v44
MM a[44:47],a[152:155],v[72:75],a[44:47]
EX v45,v45
MM a[44:47],a[156:159],v[76:79],a[44:47]
buffer_load_dwordx4 v231, s[8:11], s64 offen offset:3072 lds
MM a[36:39],a[156:159],v[68:71],a[36:39]
s_add_u32 m0, s65, 114688
RD a[224:227],v224 offset:24576
CV v14,v44,v45
MM a[36:39],a[152:155],v[64:67],a[36:39]
EX v46,v46
MM a[64:67],a[160:163],v[64:67],a[64:67]
EX v47,v47
MM a[64:67],a[164:167],v[68:71],a[64:67]
RD a[228:231],v225 offset:24576
CV v15,v46,v47
MM a[72:75],a[164:167],v[76:79],a[72:75]
EX v48,v48
MM a[72:75],a[160:163],v[72:75],a[72:75]
buffer_load_dwordx4 v235, s[12:15], s81 offen offset:0 lds
MM a[80:83],a[160:163],v[80:83],a[80:83]
EX v49,v49
MM a[80:83],a[164:167],v[84:87],a[80:83]
RD a[232:235],v226 offset:24576
CV v20,v48,v49
MM a[88:91],a[164:167],v[92:95],a[88:91]
EX v50,v50
MM a[88:91],a[160:163],v[88:91],a[88:91]
EX v51,v51
MM a[92:95],a[168:171],v[88:91],a[92:95]
RD a[236:239],v227 offset:24576
CV v21,v50,v51
MM a[92:95],a[172:175],v[92:95],a[92:95]
EX v52,v52
MM a[84:87],a[172:175],v[84:87],a[84:87]
buffer_load_dwordx4 v235, s[12:15], s64 offen offset:1024 lds
MM a[84:87],a[168:171],v[80:83],a[84:87]
EX v53,v53
MM a[76:79],a[168:171],v[72:75],a[76:79]
RD a[240:243],v224 offset:28672
CV v22,v52,v53
MM a[76:79],a[172:175],v[76:79],a[76:79]
EX v54,v54
MM a[68:71],a[172:175],v[68:71],a[68:71]
EX v55,v55
MM a[68:71],a[168:171],v[64:67],a[68:71]
RD a[244:247],v225 offset:28672
CV v23,v54,v55
MM a[96:99],a[176:179],v[64:67],a[96:99]
EX v56,v56
MM a[96:99],a[180:183],v[68:71],a[96:99]
buffer_load_dwordx4 v235, s[12:15], s81 offen offset:2048 lds
MM a[104:107],a[180:183],v[76:79],a[104:107]
EX v57,v57
MM a[104:107],a[176:179],v[72:75],a[104:107]
RD a[248:251],v226 offset:28672
CV v28,v56,v57
MM a[112:115],a[176:179],v[80:83],a[112:115]
EX v58,v58
MM a[112:115],a[180:183],v[84:87],a[112:115]
EX v59,v59
MM a[120:123],a[180:183],v[92:95],a[120:123]
RD a[252:255],v227 offset:28672
CV v29,v58,v59
MM a[120:123],a[176:179],v[88:91],a[120:123]
EX v60,v60
MM a[124:127],a[184:187],v[88:91],a[124:127]
buffer_load_dwordx4 v235, s[12:15], s64 offen offset:3072 lds
MM a[124:127],a[188:191],v[92:95],a[124:127]
EX v61,v61
MM a[116:119],a[188:191],v[84:87],a[116:119]
EX v62,v62
MM a[116:119],a[184:187],v[80:83],a[116:119]
EX v63,v63
MM a[108:111],a[184:187],v[72:75],a[108:111]
CV v30,v60,v61
CV v31,v62,v63
MM a[108:111],a[188:191],v[76:79],a[108:111]
MM a[100:103],a[188:191],v[68:71],a[100:103]
MM a[100:103],a[184:187],v[64:67],a[100:103]
v_smfmac_f32_16x16x64_bf16 v[236:239], v[144:147], v[0:7], v234
v_smfmac_f32_16x16x64_bf16 v[236:239], v[148:151], v[8:15], v234
v_smfmac_f32_16x16x64_bf16 v[236:239], v[152:155], v[16:23], v234
v_smfmac_f32_16x16x64_bf16 v[236:239], v[156:159], v[24:31], v234
s_waitcnt lgkmcnt(0)
MM v[64:67],a[192:195],v[160:163],v[128:131]
s_sub_u32 s64, s64, 507904
TR a[128:129],v232 offset:0
s_add_u32 s81, s64, 896
MM v[64:67],a[196:199],v[176:179],v[64:67]
TR a[130:131],v232 offset:4096
TR a[132:133],v232 offset:8192
MM v[64:67],a[200:203],v[192:195],v[64:67]
TR a[134:135],v232 offset:12288
TR a[136:137],v233 offset:0
MM v[64:67],a[204:207],v[208:211],v[64:67]
TR a[138:139],v233 offset:4096
v_add_f32 v246, v236, v237
MM v[68:71],a[220:223],v[208:211],v[128:131]
TR a[140:141],v233 offset:8192
v_cmp_nge_f32 vcc, s71, v246
MM v[68:71],a[216:219],v[192:195],v[68:71]
s_cbranch_vccnz LSITE28_%=
LBACK28_%=:
TR a[142:143],v233 offset:12288
TR a[144:145],v232 offset:512
MM v[68:71],a[212:215],v[176:179],v[68:71]
TR a[146:147],v232 offset:4608
TR a[148:149],v232 offset:8704
MM v[68:71],a[208:211],v[160:163],v[68:71]
TR a[150:151],v232 offset:12800
TR a[152:153],v233 offset:512
MM v[76:79],a[208:211],v[164:167],v[132:135]
EX v64,v64
MM v[76:79],a[212:215],v[180:183],v[76:79]
EX v65,v65
MM v[76:79],a[216:219],v[196:199],v[76:79]
TR a[154:155],v233 offset:4608
CV v64,v64,v65
MM v[76:79],a[220:223],v[212:215],v[76:79]
EX v66,v66
MM v[72:75],a[204:207],v[212:215],v[132:135]
EX v67,v67
MM v[72:75],a[200:203],v[196:199],v[72:75]
TR a[156:157],v233 offset:8704
CV v65,v66,v67
MM v[72:75],a[196:199],v[180:183],v[72:75]
EX v68,v68
MM v[72:75],a[192:195],v[164:167],v[72:75]
EX v69,v69
MM v[80:83],a[192:195],v[168:171],v[136:139]
TR a[158:159],v233 offset:12800
CV v66,v68,v69
MM v[80:83],a[196:199],v[184:187],v[80:83]
EX v70,v70
MM v[80:83],a[200:203],v[200:203],v[80:83]
EX v71,v71
MM v[80:83],a[204:207],v[216:219],v[80:83]
TR a[160:161],v232 offset:1024
CV v67,v70,v71
MM v[84:87],a[220:223],v[216:219],v[136:139]
EX v72,v72
MM v[84:87],a[216:219],v[200:203],v[84:87]
EX v73,v73
MM v[84:87],a[212:215],v[184:187],v[84:87]
TR a[162:163],v232 offset:5120
CV v72,v72,v73
MM v[84:87],a[208:211],v[168:171],v[84:87]
EX v74,v74
MM v[92:95],a[208:211],v[172:175],v[140:143]
TR a[164:165],v232 offset:9216
TR a[166:167],v232 offset:13312
MM v[92:95],a[212:215],v[188:191],v[92:95]
EX v75,v75
MM v[92:95],a[216:219],v[204:207],v[92:95]
TR a[168:169],v233 offset:1024
CV v73,v74,v75
MM v[92:95],a[220:223],v[220:223],v[92:95]
EX v76,v76
MM v[88:91],a[204:207],v[220:223],v[140:143]
EX v77,v77
MM v[88:91],a[200:203],v[204:207],v[88:91]
TR a[170:171],v233 offset:5120
CV v74,v76,v77
MM v[88:91],a[196:199],v[188:191],v[88:91]
EX v78,v78
MM v[88:91],a[192:195],v[172:175],v[88:91]
TR a[172:173],v233 offset:9216
TR a[174:175],v233 offset:13312
MM v[96:99],a[224:227],v[160:163],v[128:131]
EX v79,v79
MM v[96:99],a[228:231],v[176:179],v[96:99]
TR a[176:177],v232 offset:1536
CV v75,v78,v79
MM v[96:99],a[232:235],v[192:195],v[96:99]
EX v80,v80
MM v[96:99],a[236:239],v[208:211],v[96:99]
TR a[178:179],v232 offset:5632
TR a[180:181],v232 offset:9728
MM v[100:103],a[252:255],v[208:211],v[128:131]
EX v81,v81
MM v[100:103],a[248:251],v[192:195],v[100:103]
TR a[182:183],v232 offset:13824
CV v80,v80,v81
MM v[100:103],a[244:247],v[176:179],v[100:103]
EX v82,v82
MM v[100:103],a[240:243],v[160:163],v[100:103]
EX v83,v83
MM v[108:111],a[240:243],v[164:167],v[132:135]
TR a[184:185],v233 offset:1536
CV v81,v82,v83
MM v[108:111],a[244:247],v[180:183],v[108:111]
EX v84,v84
MM v[108:111],a[248:251],v[196:199],v[108:111]
TR a[186:187],v233 offset:5632
TR a[188:189],v233 offset:9728
MM v[108:111],a[252:255],v[212:215],v[108:111]
EX v85,v85
MM v[104:107],a[236:239],v[212:215],v[132:135]
TR a[190:191],v233 offset:13824
CV v82,v84,v85
MM v[104:107],a[232:235],v[196:199],v[104:107]
EX v86,v86
MM v[104:107],a[228:231],v[180:183],v[104:107]
EX v87,v87
MM v[104:107],a[224:227],v[164:167],v[104:107]
CV v83,v86,v87
MM v[112:115],a[224:227],v[168:171],v[136:139]
EX v88,v88
MM v[112:115],a[228:231],v[184:187],v[112:115]
EX v89,v89
MM v[112:115],a[232:235],v[200:203],v[112:115]
EX v90,v90
MM v[112:115],a[236:239],v[216:219],v[112:115]
EX v91,v91
MM v[116:119],a[252:255],v[216:219],v[136:139]
EX v92,v92
MM v[116:119],a[248:251],v[200:203],v[116:119]
EX v93,v93
MM v[116:119],a[244:247],v[184:187],v[116:119]
EX v94,v94
MM v[116:119],a[240:243],v[168:171],v[116:119]
EX v95,v95
MM v[124:127],a[240:243],v[172:175],v[140:143]
EX v96,v96
MM v[124:127],a[244:247],v[188:191],v[124:127]
EX v97,v97
MM v[124:127],a[248:251],v[204:207],v[124:127]
EX v98,v98
MM v[124:127],a[252:255],v[220:223],v[124:127]
EX v99,v99
MM v[120:123],a[236:239],v[220:223],v[140:143]
EX v100,v100
MM v[120:123],a[232:235],v[204:207],v[120:123]
EX v101,v101
MM v[120:123],a[228:231],v[188:191],v[120:123]
EX v102,v102
MM v[120:123],a[224:227],v[172:175],v[120:123]
s_waitcnt vmcnt(13) lgkmcnt(0)
s_barrier
MM a[0:3],a[128:131],v[0:3],a[0:3]
s_add_u32 m0, s65, 0
s_add_u32 m0, s94, 139264
s_add_u32 s66, s86, 0
buffer_load_dwordx4 v241, s[24:27], s66 offen offset:0 lds
MM a[0:3],a[132:135],v[4:7],a[0:3]
CV v88,v88,v89
CV v89,v90,v91
MM a[8:11],a[132:135],v[12:15],a[8:11]
RD a[192:195],v224 offset:32768
CV v90,v92,v93
MM a[8:11],a[128:131],v[8:11],a[8:11]
RD a[196:199],v225 offset:32768
CV v91,v94,v95
MM a[16:19],a[128:131],v[16:19],a[16:19]
s_add_u32 m0, s65, 0
s_nop 0
buffer_load_dwordx4 v228, s[8:11], s64 offen offset:0 lds
MM a[16:19],a[132:135],v[20:23],a[16:19]
RD a[200:203],v226 offset:32768
CV v68,v96,v97
MM a[24:27],a[132:135],v[28:31],a[24:27]
RD a[204:207],v227 offset:32768
CV v69,v98,v99
MM a[24:27],a[128:131],v[24:27],a[24:27]
buffer_load_dwordx4 v229, s[8:11], s64 offen offset:1024 lds
MM a[28:31],a[136:139],v[24:27],a[28:31]
s_add_u32 m0, s94, 143360
s_add_u32 s66, s86, 81920
buffer_load_dwordx4 v241, s[24:27], s66 offen offset:0 lds
MM a[28:31],a[140:143],v[28:31],a[28:31]
RD a[208:211],v224 offset:36864
CV v70,v100,v101
MM a[20:23],a[140:143],v[20:23],a[20:23]
RD a[212:215],v225 offset:36864
RD a[216:219],v226 offset:36864
MM a[20:23],a[136:139],v[16:19],a[20:23]
s_add_u32 m0, s65, 0
s_nop 0
buffer_load_dwordx4 v230, s[8:11], s64 offen offset:2048 lds
MM a[12:15],a[136:139],v[8:11],a[12:15]
RD a[220:223],v227 offset:36864
RD a[224:227],v224 offset:40960
MM a[12:15],a[140:143],v[12:15],a[12:15]
EX v103,v103
MM a[4:7],a[140:143],v[4:7],a[4:7]
RD a[228:231],v225 offset:40960
CV v71,v102,v103
MM a[4:7],a[136:139],v[0:3],a[4:7]
buffer_load_dwordx4 v231, s[8:11], s64 offen offset:3072 lds
MM a[32:35],a[144:147],v[0:3],a[32:35]
s_add_u32 m0, s65, 65536
EX v104,v104
MM a[32:35],a[148:151],v[4:7],a[32:35]
EX v105,v105
MM a[40:43],a[148:151],v[12:15],a[40:43]
RD a[232:235],v226 offset:40960
CV v76,v104,v105
MM a[40:43],a[144:147],v[8:11],a[40:43]
EX v106,v106
MM a[48:51],a[144:147],v[16:19],a[48:51]
buffer_load_dwordx4 v235, s[12:15], s81 offen offset:0 lds
MM a[48:51],a[148:151],v[20:23],a[48:51]
EX v107,v107
MM a[56:59],a[148:151],v[28:31],a[56:59]
RD a[236:239],v227 offset:40960
CV v77,v106,v107
MM a[56:59],a[144:147],v[24:27],a[56:59]
EX v108,v108
MM a[60:63],a[152:155],v[24:27],a[60:63]
EX v109,v109
MM a[60:63],a[156:159],v[28:31],a[60:63]
RD a[240:243],v224 offset:45056
CV v78,v108,v109
MM a[52:55],a[156:159],v[20:23],a[52:55]
EX v110,v110
MM a[52:55],a[152:155],v[16:19],a[52:55]
buffer_load_dwordx4 v235, s[12:15], s64 offen offset:1024 lds
MM a[44:47],a[152:155],v[8:11],a[44:47]
EX v111,v111
MM a[44:47],a[156:159],v[12:15],a[44:47]
RD a[244:247],v225 offset:45056
CV v79,v110,v111
MM a[36:39],a[156:159],v[4:7],a[36:39]
EX v112,v112
MM a[36:39],a[152:155],v[0:3],a[36:39]
EX v113,v113
MM a[64:67],a[160:163],v[0:3],a[64:67]
RD a[248:251],v226 offset:45056
CV v84,v112,v113
MM a[64:67],a[164:167],v[4:7],a[64:67]
EX v114,v114
MM a[72:75],a[164:167],v[12:15],a[72:75]
buffer_load_dwordx4 v235, s[12:15], s81 offen offset:2048 lds
MM a[72:75],a[160:163],v[8:11],a[72:75]
EX v115,v115
MM a[80:83],a[160:163],v[16:19],a[80:83]
RD a[252:255],v227 offset:45056
CV v85,v114,v115
MM a[80:83],a[164:167],v[20:23],a[80:83]
EX v116,v116
MM a[88:91],a[164:167],v[28:31],a[88:91]
EX v117,v117
MM a[88:91],a[160:163],v[24:27],a[88:91]
buffer_load_dwordx4 v235, s[12:15], s64 offen offset:3072 lds
MM a[92:95],a[168:171],v[24:27],a[92:95]
EX v118,v118
MM a[92:95],a[172:175],v[28:31],a[92:95]
EX v119,v119
MM a[84:87],a[172:175],v[20:23],a[84:87]
EX v120,v120
MM a[84:87],a[168:171],v[16:19],a[84:87]
EX v121,v121
MM a[76:79],a[168:171],v[8:11],a[76:79]
EX v122,v122
MM a[76:79],a[172:175],v[12:15],a[76:79]
EX v123,v123
MM a[68:71],a[172:175],v[4:7],a[68:71]
EX v124,v124
MM a[68:71],a[168:171],v[0:3],a[68:71]
EX v125,v125
MM a[96:99],a[176:179],v[0:3],a[96:99]
EX v126,v126
MM a[96:99],a[180:183],v[4:7],a[96:99]
EX v127,v127
MM a[104:107],a[180:183],v[12:15],a[104:107]
CV v86,v116,v117
CV v87,v118,v119
MM a[104:107],a[176:179],v[8:11],a[104:107]
CV v92,v120,v121
CV v93,v122,v123
MM a[112:115],a[176:179],v[16:19],a[112:115]
)ASM"
R"ASM(
CV v94,v124,v125
CV v95,v126,v127
MM a[112:115],a[180:183],v[20:23],a[112:115]
MM a[120:123],a[180:183],v[28:31],a[120:123]
MM a[120:123],a[176:179],v[24:27],a[120:123]
MM a[124:127],a[184:187],v[24:27],a[124:127]
MM a[124:127],a[188:191],v[28:31],a[124:127]
MM a[116:119],a[188:191],v[20:23],a[116:119]
MM a[116:119],a[184:187],v[16:19],a[116:119]
MM a[108:111],a[184:187],v[8:11],a[108:111]
MM a[108:111],a[188:191],v[12:15],a[108:111]
MM a[100:103],a[188:191],v[4:7],a[100:103]
MM a[100:103],a[184:187],v[0:3],a[100:103]
v_smfmac_f32_16x16x64_bf16 v[236:239], v[144:147], v[64:71], v234
v_smfmac_f32_16x16x64_bf16 v[236:239], v[148:151], v[72:79], v234
v_smfmac_f32_16x16x64_bf16 v[236:239], v[152:155], v[80:87], v234
v_smfmac_f32_16x16x64_bf16 v[236:239], v[156:159], v[88:95], v234
s_waitcnt lgkmcnt(0)
MM v[0:3],a[192:195],v[160:163],v[128:131]
s_add_u32 s64, s64, 16384
TR a[128:129],v232 offset:16384
s_add_u32 s81, s64, 896
MM v[0:3],a[196:199],v[176:179],v[0:3]
TR a[130:131],v232 offset:20480
TR a[132:133],v232 offset:24576
MM v[0:3],a[200:203],v[192:195],v[0:3]
TR a[134:135],v232 offset:28672
TR a[136:137],v233 offset:16384
MM v[0:3],a[204:207],v[208:211],v[0:3]
TR a[138:139],v233 offset:20480
v_add_f32 v247, v236, v237
MM v[4:7],a[220:223],v[208:211],v[128:131]
TR a[140:141],v233 offset:24576
v_cmp_nge_f32 vcc, s71, v247
MM v[4:7],a[216:219],v[192:195],v[4:7]
s_cbranch_vccnz LSITE29_%=
LBACK29_%=:
TR a[142:143],v233 offset:28672
TR a[144:145],v232 offset:16896
MM v[4:7],a[212:215],v[176:179],v[4:7]
s_add_u32 m0, s94, 151552
s_add_u32 s66, s87, 81920
buffer_load_dwordx4 v241, s[24:27], s66 offen offset:0 lds
MM v[4:7],a[208:211],v[160:163],v[4:7]
v_bfe_u32 v96, v248, 6, 2
v_mul_u32_u24 v96, 1280, v96
MM v[12:15],a[208:211],v[164:167],v[132:135]
v_bfe_u32 v97, v248, 8, 1
v_mul_u32_u24 v97, 40960, v97
MM v[12:15],a[212:215],v[180:183],v[12:15]
v_add_u32 v96, v96, v97
v_and_b32 v97, 48, v248
MM v[12:15],a[216:219],v[196:199],v[12:15]
v_mul_u32_u24 v97, 6, v97
v_add_u32 v96, v96, v97
MM v[12:15],a[220:223],v[212:215],v[12:15]
s_min_u32 s60, s18, s63
EX v0,v0
MM v[8:11],a[204:207],v[212:215],v[132:135]
s_add_u32 s61, s60, 131072
s_mov_b32 exec_hi, 0
buffer_load_dword v100, v96, s[32:35], s60 offen offset:0
s_mov_b64 exec, -1
MM v[8:11],a[200:203],v[196:199],v[8:11]
EX v1,v1
MM v[8:11],a[196:199],v[180:183],v[8:11]
s_add_u32 m0, s94, 155648
s_add_u32 s66, s88, 0
buffer_load_dwordx4 v241, s[24:27], s66 offen offset:0 lds
MM v[8:11],a[192:195],v[164:167],v[8:11]
s_mov_b32 exec_hi, 0
buffer_load_dword v101, v96, s[20:23], s61 offen offset:0 sc1
s_mov_b64 exec, -1
MM v[16:19],a[192:195],v[168:171],v[136:139]
EX v2,v2
MM v[16:19],a[196:199],v[184:187],v[16:19]
EX v3,v3
MM v[16:19],a[200:203],v[200:203],v[16:19]
EX v4,v4
MM v[16:19],a[204:207],v[216:219],v[16:19]
TR a[146:147],v232 offset:20992
CV v0,v0,v1
MM v[20:23],a[220:223],v[216:219],v[136:139]
EX v5,v5
MM v[20:23],a[216:219],v[200:203],v[20:23]
TR a[148:149],v232 offset:25088
CV v1,v2,v3
MM v[20:23],a[212:215],v[184:187],v[20:23]
s_add_u32 m0, s94, 159744
s_add_u32 s66, s88, 81920
buffer_load_dwordx4 v241, s[24:27], s66 offen offset:0 lds
MM v[20:23],a[208:211],v[168:171],v[20:23]
EX v6,v6
MM v[28:31],a[208:211],v[172:175],v[140:143]
EX v7,v7
MM v[28:31],a[212:215],v[188:191],v[28:31]
TR a[150:151],v232 offset:29184
TR a[152:153],v233 offset:16896
MM v[28:31],a[216:219],v[204:207],v[28:31]
EX v8,v8
MM v[28:31],a[220:223],v[220:223],v[28:31]
EX v9,v9
MM v[24:27],a[204:207],v[220:223],v[140:143]
TR a[154:155],v233 offset:20992
CV v2,v4,v5
s_nop 0
LPG18_%=:
s_cbranch_execz LPG22_%=
MM v[24:27],a[200:203],v[204:207],v[24:27]
EX v10,v10
MM v[24:27],a[196:199],v[188:191],v[24:27]
TR a[156:157],v233 offset:25088
CV v3,v6,v7
MM v[24:27],a[192:195],v[172:175],v[24:27]
EX v11,v11
MM v[32:35],a[224:227],v[160:163],v[128:131]
TR a[158:159],v233 offset:29184
TR a[160:161],v232 offset:17408
MM v[32:35],a[228:231],v[176:179],v[32:35]
EX v12,v12
MM v[32:35],a[232:235],v[192:195],v[32:35]
TR a[162:163],v232 offset:21504
CV v8,v8,v9
MM v[32:35],a[236:239],v[208:211],v[32:35]
EX v13,v13
MM v[36:39],a[252:255],v[208:211],v[128:131]
TR a[164:165],v232 offset:25600
CV v9,v10,v11
MM v[36:39],a[248:251],v[192:195],v[36:39]
EX v14,v14
MM v[36:39],a[244:247],v[176:179],v[36:39]
EX v15,v15
MM v[36:39],a[240:243],v[160:163],v[36:39]
TR a[166:167],v232 offset:29696
TR a[168:169],v233 offset:17408
MM v[44:47],a[240:243],v[164:167],v[132:135]
EX v16,v16
MM v[44:47],a[244:247],v[180:183],v[44:47]
TR a[170:171],v233 offset:21504
CV v10,v12,v13
MM v[44:47],a[248:251],v[196:199],v[44:47]
EX v17,v17
MM v[44:47],a[252:255],v[212:215],v[44:47]
TR a[172:173],v233 offset:25600
CV v11,v14,v15
MM v[40:43],a[236:239],v[212:215],v[132:135]
EX v18,v18
MM v[40:43],a[232:235],v[196:199],v[40:43]
TR a[174:175],v233 offset:29696
TR a[176:177],v232 offset:17920
MM v[40:43],a[228:231],v[180:183],v[40:43]
EX v19,v19
MM v[40:43],a[224:227],v[164:167],v[40:43]
EX v20,v20
MM v[48:51],a[224:227],v[168:171],v[136:139]
TR a[178:179],v232 offset:22016
CV v16,v16,v17
MM v[48:51],a[228:231],v[184:187],v[48:51]
EX v21,v21
MM v[48:51],a[232:235],v[200:203],v[48:51]
TR a[180:181],v232 offset:26112
CV v17,v18,v19
MM v[48:51],a[236:239],v[216:219],v[48:51]
EX v22,v22
MM v[52:55],a[252:255],v[216:219],v[136:139]
TR a[182:183],v232 offset:30208
CV v18,v20,v21
MM v[52:55],a[248:251],v[200:203],v[52:55]
EX v23,v23
MM v[52:55],a[244:247],v[184:187],v[52:55]
TR a[184:185],v233 offset:17920
TR a[186:187],v233 offset:22016
MM v[52:55],a[240:243],v[168:171],v[52:55]
EX v24,v24
MM v[60:63],a[240:243],v[172:175],v[140:143]
EX v25,v25
MM v[60:63],a[244:247],v[188:191],v[60:63]
TR a[188:189],v233 offset:26112
CV v19,v22,v23
MM v[60:63],a[248:251],v[204:207],v[60:63]
TR a[190:191],v233 offset:30208
CV v24,v24,v25
MM v[60:63],a[252:255],v[220:223],v[60:63]
EX v26,v26
MM v[56:59],a[236:239],v[220:223],v[140:143]
EX v27,v27
MM v[56:59],a[232:235],v[204:207],v[56:59]
EX v28,v28
MM v[56:59],a[228:231],v[188:191],v[56:59]
EX v29,v29
MM v[56:59],a[224:227],v[172:175],v[56:59]
s_waitcnt vmcnt(19) lgkmcnt(0)
s_barrier
MM a[0:3],a[128:131],v[64:67],a[0:3]
CV v25,v26,v27
CV v26,v28,v29
MM a[0:3],a[132:135],v[68:71],a[0:3]
s_lshl_b32 s91, s47, 2
EX v30,v30
MM a[8:11],a[132:135],v[76:79],a[8:11]
s_add_u32 s91, s91, %[swave]
EX v31,v31
MM a[8:11],a[128:131],v[72:75],a[8:11]
s_lshl_b32 s91, s91, 2
RD a[192:195],v224 offset:49152
CV v27,v30,v31
MM a[16:19],a[128:131],v[80:83],a[16:19]
s_add_u32 s91, s91, 73728
s_add_u32 m0, s65, 16384
RD a[196:199],v225 offset:49152
MM a[16:19],a[132:135],v[84:87],a[16:19]
v_mov_b32 v250, s69
s_mov_b64 exec, 1
buffer_store_dword v250, off, s[20:23], s91 offset:0 sc1
s_mov_b64 exec, -1
MM a[24:27],a[132:135],v[92:95],a[24:27]
s_lshr_b32 s77, %[sb], 3
EX v32,v32
MM a[24:27],a[128:131],v[88:91],a[24:27]
s_lshl_b32 s77, s77, 2
EX v33,v33
MM a[28:31],a[136:139],v[88:91],a[28:31]
s_add_u32 s77, s77, %[swave]
buffer_load_dwordx4 v228, s[8:11], s64 offen offset:0 lds
MM a[28:31],a[140:143],v[92:95],a[28:31]
s_lshl_b32 s77, s77, 9
EX v34,v34
MM a[20:23],a[140:143],v[84:87],a[20:23]
s_add_u32 s77, s77, 0
v_lshrrev_b32 v253, 1, v248
v_add_u32 v253, s77, v253
MM a[20:23],a[136:139],v[80:83],a[20:23]
s_mov_b32 s77, LKEND_ka-LPD2_%=-16
v_min_u32 v253, s77, v253
CV v4,v32,v33
MM a[12:15],a[136:139],v[72:75],a[12:15]
s_getpc_b64 s[98:99]
LPD2_%=:
global_load_dword v253, v253, s[98:99]
MM a[12:15],a[140:143],v[76:79],a[12:15]
EX v35,v35
MM a[4:7],a[140:143],v[68:71],a[4:7]
EX v36,v36
MM a[4:7],a[136:139],v[64:67],a[4:7]
EX v37,v37
MM a[32:35],a[144:147],v[64:67],a[32:35]
RD a[200:203],v226 offset:49152
CV v5,v34,v35
MM a[32:35],a[148:151],v[68:71],a[32:35]
EX v38,v38
MM a[40:43],a[148:151],v[76:79],a[40:43]
buffer_load_dwordx4 v229, s[8:11], s64 offen offset:1024 lds
MM a[40:43],a[144:147],v[72:75],a[40:43]
EX v39,v39
MM a[48:51],a[144:147],v[80:83],a[48:51]
RD a[204:207],v227 offset:49152
CV v6,v36,v37
MM a[48:51],a[148:151],v[84:87],a[48:51]
EX v40,v40
MM a[56:59],a[148:151],v[92:95],a[56:59]
EX v41,v41
MM a[56:59],a[144:147],v[88:91],a[56:59]
RD a[208:211],v224 offset:53248
CV v7,v38,v39
MM a[60:63],a[152:155],v[88:91],a[60:63]
EX v42,v42
MM a[60:63],a[156:159],v[92:95],a[60:63]
buffer_load_dwordx4 v230, s[8:11], s64 offen offset:2048 lds
MM a[52:55],a[156:159],v[84:87],a[52:55]
EX v43,v43
MM a[52:55],a[152:155],v[80:83],a[52:55]
RD a[212:215],v225 offset:53248
CV v12,v40,v41
MM a[44:47],a[152:155],v[72:75],a[44:47]
EX v44,v44
MM a[44:47],a[156:159],v[76:79],a[44:47]
EX v45,v45
MM a[36:39],a[156:159],v[68:71],a[36:39]
RD a[216:219],v226 offset:53248
CV v13,v42,v43
MM a[36:39],a[152:155],v[64:67],a[36:39]
EX v46,v46
MM a[64:67],a[160:163],v[64:67],a[64:67]
buffer_load_dwordx4 v231, s[8:11], s64 offen offset:3072 lds
MM a[64:67],a[164:167],v[68:71],a[64:67]
s_add_u32 m0, s65, 81920
EX v47,v47
MM a[72:75],a[164:167],v[76:79],a[72:75]
RD a[220:223],v227 offset:53248
CV v14,v44,v45
MM a[72:75],a[160:163],v[72:75],a[72:75]
EX v48,v48
MM a[80:83],a[160:163],v[80:83],a[80:83]
RD a[224:227],v224 offset:57344
CV v15,v46,v47
MM a[80:83],a[164:167],v[84:87],a[80:83]
EX v49,v49
MM a[88:91],a[164:167],v[92:95],a[88:91]
EX v50,v50
MM a[88:91],a[160:163],v[88:91],a[88:91]
buffer_load_dwordx4 v235, s[12:15], s81 offen offset:0 lds
MM a[92:95],a[168:171],v[88:91],a[92:95]
EX v51,v51
MM a[92:95],a[172:175],v[92:95],a[92:95]
RD a[228:231],v225 offset:57344
CV v20,v48,v49
MM a[84:87],a[172:175],v[84:87],a[84:87]
EX v52,v52
MM a[84:87],a[168:171],v[80:83],a[84:87]
RD a[232:235],v226 offset:57344
CV v21,v50,v51
MM a[76:79],a[168:171],v[72:75],a[76:79]
EX v53,v53
MM a[76:79],a[172:175],v[76:79],a[76:79]
RD a[236:239],v227 offset:57344
CV v22,v52,v53
MM a[68:71],a[172:175],v[68:71],a[68:71]
EX v54,v54
MM a[68:71],a[168:171],v[64:67],a[68:71]
buffer_load_dwordx4 v235, s[12:15], s64 offen offset:1024 lds
MM a[96:99],a[176:179],v[64:67],a[96:99]
EX v55,v55
MM a[96:99],a[180:183],v[68:71],a[96:99]
EX v56,v56
MM a[104:107],a[180:183],v[76:79],a[104:107]
RD a[240:243],v224 offset:61440
CV v23,v54,v55
MM a[104:107],a[176:179],v[72:75],a[104:107]
EX v57,v57
MM a[112:115],a[176:179],v[80:83],a[112:115]
RD a[244:247],v225 offset:61440
CV v28,v56,v57
MM a[112:115],a[180:183],v[84:87],a[112:115]
EX v58,v58
MM a[120:123],a[180:183],v[92:95],a[120:123]
s_waitcnt vmcnt(9)
buffer_load_dwordx4 v235, s[12:15], s81 offen offset:2048 lds
MM a[120:123],a[176:179],v[88:91],a[120:123]
EX v59,v59
MM a[124:127],a[184:187],v[88:91],a[124:127]
RD a[248:251],v226 offset:61440
CV v29,v58,v59
MM a[124:127],a[188:191],v[92:95],a[124:127]
EX v60,v60
MM a[116:119],a[188:191],v[84:87],a[116:119]
EX v61,v61
MM a[116:119],a[184:187],v[80:83],a[116:119]
RD a[252:255],v227 offset:61440
CV v30,v60,v61
MM a[108:111],a[184:187],v[72:75],a[108:111]
EX v62,v62
MM a[108:111],a[188:191],v[76:79],a[108:111]
buffer_load_dwordx4 v235, s[12:15], s64 offen offset:3072 lds
MM a[100:103],a[188:191],v[68:71],a[100:103]
EX v63,v63
MM a[100:103],a[184:187],v[64:67],a[100:103]
CV v31,v62,v63
v_smfmac_f32_16x16x64_bf16 v[236:239], v[144:147], v[0:7], v234
v_smfmac_f32_16x16x64_bf16 v[236:239], v[148:151], v[8:15], v234
v_smfmac_f32_16x16x64_bf16 v[236:239], v[152:155], v[16:23], v234
v_smfmac_f32_16x16x64_bf16 v[236:239], v[156:159], v[24:31], v234
s_waitcnt lgkmcnt(0)
MM v[64:67],a[192:195],v[160:163],v[128:131]
s_add_u32 s64, s64, 16384
TR a[128:129],v232 offset:32768
s_add_u32 s81, s64, 896
MM v[64:67],a[196:199],v[176:179],v[64:67]
TR a[130:131],v232 offset:36864
TR a[132:133],v232 offset:40960
MM v[64:67],a[200:203],v[192:195],v[64:67]
TR a[134:135],v232 offset:45056
TR a[136:137],v233 offset:32768
MM v[64:67],a[204:207],v[208:211],v[64:67]
TR a[138:139],v233 offset:36864
v_add_f32 v246, v236, v237
MM v[68:71],a[220:223],v[208:211],v[128:131]
TR a[140:141],v233 offset:40960
v_cmp_nge_f32 vcc, s71, v246
MM v[68:71],a[216:219],v[192:195],v[68:71]
s_cbranch_vccnz LSITE30_%=
LBACK30_%=:
TR a[142:143],v233 offset:45056
TR a[144:145],v232 offset:33280
MM v[68:71],a[212:215],v[176:179],v[68:71]
)ASM"
R"ASM(
TR a[146:147],v232 offset:37376
TR a[148:149],v232 offset:41472
MM v[68:71],a[208:211],v[160:163],v[68:71]
TR a[150:151],v232 offset:45568
TR a[152:153],v233 offset:33280
MM v[76:79],a[208:211],v[164:167],v[132:135]
EX v64,v64
MM v[76:79],a[212:215],v[180:183],v[76:79]
EX v65,v65
MM v[76:79],a[216:219],v[196:199],v[76:79]
TR a[154:155],v233 offset:37376
CV v64,v64,v65
MM v[76:79],a[220:223],v[212:215],v[76:79]
EX v66,v66
MM v[72:75],a[204:207],v[212:215],v[132:135]
EX v67,v67
MM v[72:75],a[200:203],v[196:199],v[72:75]
TR a[156:157],v233 offset:41472
CV v65,v66,v67
MM v[72:75],a[196:199],v[180:183],v[72:75]
EX v68,v68
MM v[72:75],a[192:195],v[164:167],v[72:75]
EX v69,v69
MM v[80:83],a[192:195],v[168:171],v[136:139]
TR a[158:159],v233 offset:45568
CV v66,v68,v69
MM v[80:83],a[196:199],v[184:187],v[80:83]
EX v70,v70
MM v[80:83],a[200:203],v[200:203],v[80:83]
EX v71,v71
MM v[80:83],a[204:207],v[216:219],v[80:83]
TR a[160:161],v232 offset:33792
CV v67,v70,v71
MM v[84:87],a[220:223],v[216:219],v[136:139]
EX v72,v72
MM v[84:87],a[216:219],v[200:203],v[84:87]
EX v73,v73
MM v[84:87],a[212:215],v[184:187],v[84:87]
TR a[162:163],v232 offset:37888
CV v72,v72,v73
MM v[84:87],a[208:211],v[168:171],v[84:87]
EX v74,v74
MM v[92:95],a[208:211],v[172:175],v[140:143]
TR a[164:165],v232 offset:41984
TR a[166:167],v232 offset:46080
MM v[92:95],a[212:215],v[188:191],v[92:95]
EX v75,v75
MM v[92:95],a[216:219],v[204:207],v[92:95]
TR a[168:169],v233 offset:33792
CV v73,v74,v75
MM v[92:95],a[220:223],v[220:223],v[92:95]
EX v76,v76
MM v[88:91],a[204:207],v[220:223],v[140:143]
EX v77,v77
MM v[88:91],a[200:203],v[204:207],v[88:91]
TR a[170:171],v233 offset:37888
CV v74,v76,v77
MM v[88:91],a[196:199],v[188:191],v[88:91]
EX v78,v78
MM v[88:91],a[192:195],v[172:175],v[88:91]
TR a[172:173],v233 offset:41984
TR a[174:175],v233 offset:46080
MM v[96:99],a[224:227],v[160:163],v[128:131]
EX v79,v79
MM v[96:99],a[228:231],v[176:179],v[96:99]
TR a[176:177],v232 offset:34304
CV v75,v78,v79
MM v[96:99],a[232:235],v[192:195],v[96:99]
EX v80,v80
MM v[96:99],a[236:239],v[208:211],v[96:99]
TR a[178:179],v232 offset:38400
TR a[180:181],v232 offset:42496
MM v[100:103],a[252:255],v[208:211],v[128:131]
EX v81,v81
MM v[100:103],a[248:251],v[192:195],v[100:103]
TR a[182:183],v232 offset:46592
CV v80,v80,v81
MM v[100:103],a[244:247],v[176:179],v[100:103]
EX v82,v82
MM v[100:103],a[240:243],v[160:163],v[100:103]
EX v83,v83
MM v[108:111],a[240:243],v[164:167],v[132:135]
TR a[184:185],v233 offset:34304
CV v81,v82,v83
MM v[108:111],a[244:247],v[180:183],v[108:111]
EX v84,v84
MM v[108:111],a[248:251],v[196:199],v[108:111]
TR a[186:187],v233 offset:38400
TR a[188:189],v233 offset:42496
MM v[108:111],a[252:255],v[212:215],v[108:111]
EX v85,v85
MM v[104:107],a[236:239],v[212:215],v[132:135]
TR a[190:191],v233 offset:46592
CV v82,v84,v85
MM v[104:107],a[232:235],v[196:199],v[104:107]
EX v86,v86
MM v[104:107],a[228:231],v[180:183],v[104:107]
EX v87,v87
MM v[104:107],a[224:227],v[164:167],v[104:107]
CV v83,v86,v87
MM v[112:115],a[224:227],v[168:171],v[136:139]
EX v88,v88
MM v[112:115],a[228:231],v[184:187],v[112:115]
EX v89,v89
MM v[112:115],a[232:235],v[200:203],v[112:115]
EX v90,v90
MM v[112:115],a[236:239],v[216:219],v[112:115]
EX v91,v91
MM v[116:119],a[252:255],v[216:219],v[136:139]
EX v92,v92
MM v[116:119],a[248:251],v[200:203],v[116:119]
EX v93,v93
MM v[116:119],a[244:247],v[184:187],v[116:119]
EX v94,v94
MM v[116:119],a[240:243],v[168:171],v[116:119]
EX v95,v95
MM v[124:127],a[240:243],v[172:175],v[140:143]
EX v96,v96
MM v[124:127],a[244:247],v[188:191],v[124:127]
EX v97,v97
MM v[124:127],a[248:251],v[204:207],v[124:127]
EX v98,v98
MM v[124:127],a[252:255],v[220:223],v[124:127]
EX v99,v99
MM v[120:123],a[236:239],v[220:223],v[140:143]
EX v100,v100
MM v[120:123],a[232:235],v[204:207],v[120:123]
EX v101,v101
MM v[120:123],a[228:231],v[188:191],v[120:123]
EX v102,v102
MM v[120:123],a[224:227],v[172:175],v[120:123]
s_waitcnt lgkmcnt(0)
s_barrier
MM a[0:3],a[128:131],v[0:3],a[0:3]
s_add_u32 m0, s65, 32768
CV v88,v88,v89
CV v89,v90,v91
MM a[0:3],a[132:135],v[4:7],a[0:3]
RD a[192:195],v224 offset:0
CV v90,v92,v93
MM a[8:11],a[132:135],v[12:15],a[8:11]
RD a[196:199],v225 offset:0
CV v91,v94,v95
MM a[8:11],a[128:131],v[8:11],a[8:11]
buffer_load_dwordx4 v228, s[8:11], s64 offen offset:0 lds
MM a[16:19],a[128:131],v[16:19],a[16:19]
RD a[200:203],v226 offset:0
CV v68,v96,v97
MM a[16:19],a[132:135],v[20:23],a[16:19]
RD a[204:207],v227 offset:0
CV v69,v98,v99
MM a[24:27],a[132:135],v[28:31],a[24:27]
buffer_load_dwordx4 v229, s[8:11], s64 offen offset:1024 lds
MM a[24:27],a[128:131],v[24:27],a[24:27]
RD a[208:211],v224 offset:4096
CV v70,v100,v101
MM a[28:31],a[136:139],v[24:27],a[28:31]
RD a[212:215],v225 offset:4096
RD a[216:219],v226 offset:4096
MM a[28:31],a[140:143],v[28:31],a[28:31]
buffer_load_dwordx4 v230, s[8:11], s64 offen offset:2048 lds
MM a[20:23],a[140:143],v[20:23],a[20:23]
RD a[220:223],v227 offset:4096
RD a[224:227],v224 offset:8192
MM a[20:23],a[136:139],v[16:19],a[20:23]
buffer_load_dwordx4 v231, s[8:11], s64 offen offset:3072 lds
MM a[12:15],a[136:139],v[8:11],a[12:15]
s_add_u32 m0, s65, 98304
EX v103,v103
MM a[12:15],a[140:143],v[12:15],a[12:15]
RD a[228:231],v225 offset:8192
CV v71,v102,v103
MM a[4:7],a[140:143],v[4:7],a[4:7]
EX v104,v104
MM a[4:7],a[136:139],v[0:3],a[4:7]
buffer_load_dwordx4 v235, s[12:15], s81 offen offset:0 lds
MM a[32:35],a[144:147],v[0:3],a[32:35]
EX v105,v105
MM a[32:35],a[148:151],v[4:7],a[32:35]
RD a[232:235],v226 offset:8192
CV v76,v104,v105
MM a[40:43],a[148:151],v[12:15],a[40:43]
EX v106,v106
MM a[40:43],a[144:147],v[8:11],a[40:43]
EX v107,v107
MM a[48:51],a[144:147],v[16:19],a[48:51]
RD a[236:239],v227 offset:8192
CV v77,v106,v107
MM a[48:51],a[148:151],v[20:23],a[48:51]
EX v108,v108
MM a[56:59],a[148:151],v[28:31],a[56:59]
buffer_load_dwordx4 v235, s[12:15], s64 offen offset:1024 lds
MM a[56:59],a[144:147],v[24:27],a[56:59]
EX v109,v109
MM a[60:63],a[152:155],v[24:27],a[60:63]
RD a[240:243],v224 offset:12288
CV v78,v108,v109
MM a[60:63],a[156:159],v[28:31],a[60:63]
EX v110,v110
MM a[52:55],a[156:159],v[20:23],a[52:55]
EX v111,v111
MM a[52:55],a[152:155],v[16:19],a[52:55]
RD a[244:247],v225 offset:12288
CV v79,v110,v111
MM a[44:47],a[152:155],v[8:11],a[44:47]
EX v112,v112
MM a[44:47],a[156:159],v[12:15],a[44:47]
buffer_load_dwordx4 v235, s[12:15], s81 offen offset:2048 lds
MM a[36:39],a[156:159],v[4:7],a[36:39]
EX v113,v113
MM a[36:39],a[152:155],v[0:3],a[36:39]
RD a[248:251],v226 offset:12288
CV v84,v112,v113
MM a[64:67],a[160:163],v[0:3],a[64:67]
EX v114,v114
MM a[64:67],a[164:167],v[4:7],a[64:67]
EX v115,v115
MM a[72:75],a[164:167],v[12:15],a[72:75]
RD a[252:255],v227 offset:12288
CV v85,v114,v115
MM a[72:75],a[160:163],v[8:11],a[72:75]
EX v116,v116
MM a[80:83],a[160:163],v[16:19],a[80:83]
buffer_load_dwordx4 v235, s[12:15], s64 offen offset:3072 lds
MM a[80:83],a[164:167],v[20:23],a[80:83]
EX v117,v117
MM a[88:91],a[164:167],v[28:31],a[88:91]
EX v118,v118
MM a[88:91],a[160:163],v[24:27],a[88:91]
EX v119,v119
MM a[92:95],a[168:171],v[24:27],a[92:95]
EX v120,v120
MM a[92:95],a[172:175],v[28:31],a[92:95]
EX v121,v121
MM a[84:87],a[172:175],v[20:23],a[84:87]
EX v122,v122
MM a[84:87],a[168:171],v[16:19],a[84:87]
EX v123,v123
MM a[76:79],a[168:171],v[8:11],a[76:79]
EX v124,v124
MM a[76:79],a[172:175],v[12:15],a[76:79]
EX v125,v125
MM a[68:71],a[172:175],v[4:7],a[68:71]
EX v126,v126
MM a[68:71],a[168:171],v[0:3],a[68:71]
EX v127,v127
MM a[96:99],a[176:179],v[0:3],a[96:99]
CV v86,v116,v117
CV v87,v118,v119
MM a[96:99],a[180:183],v[4:7],a[96:99]
CV v92,v120,v121
CV v93,v122,v123
MM a[104:107],a[180:183],v[12:15],a[104:107]
CV v94,v124,v125
CV v95,v126,v127
MM a[104:107],a[176:179],v[8:11],a[104:107]
MM a[112:115],a[176:179],v[16:19],a[112:115]
MM a[112:115],a[180:183],v[20:23],a[112:115]
MM a[120:123],a[180:183],v[28:31],a[120:123]
MM a[120:123],a[176:179],v[24:27],a[120:123]
MM a[124:127],a[184:187],v[24:27],a[124:127]
MM a[124:127],a[188:191],v[28:31],a[124:127]
MM a[116:119],a[188:191],v[20:23],a[116:119]
MM a[116:119],a[184:187],v[16:19],a[116:119]
MM a[108:111],a[184:187],v[8:11],a[108:111]
MM a[108:111],a[188:191],v[12:15],a[108:111]
MM a[100:103],a[188:191],v[4:7],a[100:103]
MM a[100:103],a[184:187],v[0:3],a[100:103]
v_smfmac_f32_16x16x64_bf16 v[236:239], v[144:147], v[64:71], v234
v_smfmac_f32_16x16x64_bf16 v[236:239], v[148:151], v[72:79], v234
v_smfmac_f32_16x16x64_bf16 v[236:239], v[152:155], v[80:87], v234
v_smfmac_f32_16x16x64_bf16 v[236:239], v[156:159], v[88:95], v234
s_waitcnt lgkmcnt(0)
MM v[16:19],a[192:195],v[168:171],v[136:139]
TR a[128:129],v232 offset:49152
TR a[130:131],v232 offset:53248
MM v[16:19],a[196:199],v[184:187],v[16:19]
TR a[132:133],v232 offset:57344
TR a[134:135],v232 offset:61440
MM v[16:19],a[200:203],v[200:203],v[16:19]
TR a[136:137],v233 offset:49152
TR a[138:139],v233 offset:53248
MM v[16:19],a[204:207],v[216:219],v[16:19]
TR a[140:141],v233 offset:57344
v_add_f32 v247, v236, v237
MM v[20:23],a[220:223],v[216:219],v[136:139]
TR a[142:143],v233 offset:61440
v_cmp_nge_f32 vcc, s71, v247
MM v[20:23],a[216:219],v[200:203],v[20:23]
s_cbranch_vccnz LSITE31_%=
LBACK31_%=:
TR a[144:145],v232 offset:49664
TR a[146:147],v232 offset:53760
MM v[20:23],a[212:215],v[184:187],v[20:23]
v_rcp_f32 v145, v247
MM v[20:23],a[208:211],v[168:171],v[20:23]
TR a[148:149],v232 offset:57856
TR a[150:151],v232 offset:61952
MM v[28:31],a[208:211],v[172:175],v[140:143]
EX v16,v16
MM v[28:31],a[212:215],v[188:191],v[28:31]
TR a[152:153],v233 offset:49664
TR a[154:155],v233 offset:53760
MM v[28:31],a[216:219],v[204:207],v[28:31]
EX v17,v17
MM v[28:31],a[220:223],v[220:223],v[28:31]
TR a[156:157],v233 offset:57856
CV v16,v16,v17
MM v[24:27],a[204:207],v[220:223],v[140:143]
TR a[158:159],v233 offset:61952
TR a[160:161],v232 offset:50176
MM v[24:27],a[200:203],v[204:207],v[24:27]
TR a[162:163],v232 offset:54272
LPG19_%=:
s_cbranch_execz LPG23_%=
TR a[164:165],v232 offset:58368
MM v[24:27],a[196:199],v[188:191],v[24:27]
EX v18,v18
MM v[24:27],a[192:195],v[172:175],v[24:27]
TR a[166:167],v232 offset:62464
TR a[168:169],v233 offset:50176
MM v[48:51],a[224:227],v[168:171],v[136:139]
EX v19,v19
MM v[48:51],a[228:231],v[184:187],v[48:51]
TR a[170:171],v233 offset:54272
CV v17,v18,v19
MM v[48:51],a[232:235],v[200:203],v[48:51]
TR a[172:173],v233 offset:58368
TR a[174:175],v233 offset:62464
MM v[48:51],a[236:239],v[216:219],v[48:51]
TR a[176:177],v232 offset:50688
TR a[178:179],v232 offset:54784
MM v[52:55],a[252:255],v[216:219],v[136:139]
EX v20,v20
MM v[52:55],a[248:251],v[200:203],v[52:55]
TR a[180:181],v232 offset:58880
TR a[182:183],v232 offset:62976
MM v[52:55],a[244:247],v[184:187],v[52:55]
EX v21,v21
MM v[52:55],a[240:243],v[168:171],v[52:55]
TR a[184:185],v233 offset:50688
TR a[186:187],v233 offset:54784
MM v[60:63],a[240:243],v[172:175],v[140:143]
TR a[188:189],v233 offset:58880
CV v18,v20,v21
MM v[60:63],a[244:247],v[188:191],v[60:63]
TR a[190:191],v233 offset:62976
v_mov_b32 v144, v145
MM v[60:63],a[248:251],v[204:207],v[60:63]
EX v22,v22
MM v[60:63],a[252:255],v[220:223],v[60:63]
EX v23,v23
MM v[56:59],a[236:239],v[220:223],v[140:143]
CV v19,v22,v23
v_permlane32_swap_b32 v145, v144
MM v[56:59],a[232:235],v[204:207],v[56:59]
EX v24,v24
MM v[56:59],a[228:231],v[188:191],v[56:59]
EX v25,v25
MM v[56:59],a[224:227],v[172:175],v[56:59]
s_waitcnt vmcnt(12) lgkmcnt(0)
s_barrier
CV v24,v24,v25
MM a[0:3],a[128:131],v[64:67],a[0:3]
s_add_u32 m0, s89, 49152
RD a[192:195],v224 offset:16384
RD a[196:199],v225 offset:16384
MM a[0:3],a[132:135],v[68:71],a[0:3]
s_add_u32 s66, s85, 0
buffer_load_dwordx4 v240, s[24:27], s66 offen offset:0 lds
MM a[8:11],a[132:135],v[76:79],a[8:11]
s_add_u32 m0, s89, 50176
EX v26,v26
MM a[8:11],a[128:131],v[72:75],a[8:11]
s_add_u32 s66, s85, 10240
RD a[200:203],v226 offset:16384
RD a[204:207],v227 offset:16384
MM a[12:15],a[136:139],v[72:75],a[12:15]
EX v27,v27
MM a[12:15],a[140:143],v[76:79],a[12:15]
RD a[208:211],v224 offset:20480
CV v25,v26,v27
MM a[4:7],a[140:143],v[68:71],a[4:7]
)ASM"
R"ASM(
buffer_load_dwordx4 v240, s[24:27], s66 offen offset:0 lds
MM a[4:7],a[136:139],v[64:67],a[4:7]
s_add_u32 m0, s89, 57344
EX v28,v28
MM a[32:35],a[144:147],v[64:67],a[32:35]
s_add_u32 s66, s85, 81920
EX v29,v29
MM a[32:35],a[148:151],v[68:71],a[32:35]
CV v26,v28,v29
v_mov_b32 v149, v145
MM a[40:43],a[148:151],v[76:79],a[40:43]
RD a[212:215],v225 offset:20480
v_permlane16_swap_b32 v145, v149
MM a[40:43],a[144:147],v[72:75],a[40:43]
s_mul_i32 s78, s49, 17408
s_add_u32 s78, s83, s78
s_add_u32 s78, s78, 0
MM a[44:47],a[152:155],v[72:75],a[44:47]
s_add_u32 s79, s78, 4096
AR v0,a0
AR v1,a1
MM a[44:47],a[156:159],v[76:79],a[44:47]
AR v2,a2
AR v3,a3
MM a[36:39],a[156:159],v[68:71],a[36:39]
AR v4,a4
AR v5,a5
MM a[36:39],a[152:155],v[64:67],a[36:39]
AR v6,a6
AR v7,a7
MM a[64:67],a[160:163],v[64:67],a[64:67]
EX v30,v30
MM a[64:67],a[164:167],v[68:71],a[64:67]
v_mul_f32 v0, v0, v145
v_mul_f32 v1, v1, v145
MM a[72:75],a[164:167],v[76:79],a[72:75]
v_mul_f32 v2, v2, v145
v_mul_f32 v3, v3, v145
MM a[72:75],a[160:163],v[72:75],a[72:75]
v_mul_f32 v4, v4, v145
v_mul_f32 v5, v5, v145
MM a[76:79],a[168:171],v[72:75],a[76:79]
v_mul_f32 v6, v6, v145
v_mul_f32 v7, v7, v145
MM a[76:79],a[172:175],v[76:79],a[76:79]
EX v31,v31
MM a[68:71],a[172:175],v[68:71],a[68:71]
CV v160,v0,v1
CV v161,v2,v3
MM a[68:71],a[168:171],v[64:67],a[68:71]
CV v162,v4,v5
CV v163,v6,v7
MM a[96:99],a[176:179],v[64:67],a[96:99]
buffer_store_dwordx4 v[160:163], v248, s[20:23], s78 offen offset:0 sc1
EX v48,v48
MM a[96:99],a[180:183],v[68:71],a[96:99]
AR v8,a8
AR v9,a9
MM a[104:107],a[180:183],v[76:79],a[104:107]
AR v10,a10
AR v11,a11
MM a[104:107],a[176:179],v[72:75],a[104:107]
AR v12,a12
AR v13,a13
EX v49,v49
MM a[108:111],a[184:187],v[72:75],a[108:111]
AR v14,a14
AR v15,a15
MM a[108:111],a[188:191],v[76:79],a[108:111]
v_mul_f32 v8, v8, v149
v_mul_f32 v9, v9, v149
EX v50,v50
MM a[100:103],a[188:191],v[68:71],a[100:103]
v_mul_f32 v10, v10, v149
v_mul_f32 v11, v11, v149
MM a[100:103],a[184:187],v[64:67],a[100:103]
buffer_load_dwordx4 v240, s[24:27], s66 offen offset:0 lds
v_mul_f32 v12, v12, v149
v_mul_f32 v13, v13, v149
MM a[16:19],a[128:131],v[80:83],a[16:19]
s_add_u32 m0, s89, 58368
v_mul_f32 v14, v14, v149
v_mul_f32 v15, v15, v149
EX v51,v51
MM a[16:19],a[132:135],v[84:87],a[16:19]
s_add_u32 s66, s85, 92160
CV v164,v8,v9
CV v27,v30,v31
MM a[24:27],a[132:135],v[92:95],a[24:27]
CV v165,v10,v11
CV v166,v12,v13
MM a[24:27],a[128:131],v[88:91],a[24:27]
RD a[216:219],v226 offset:20480
CV v167,v14,v15
EX v52,v52
MM a[28:31],a[136:139],v[88:91],a[28:31]
buffer_store_dwordx4 v[164:167], v248, s[20:23], s79 offen offset:0 sc1
AR v32,a32
MM a[28:31],a[140:143],v[92:95],a[28:31]
AR v33,a33
AR v34,a34
EX v53,v53
MM a[20:23],a[140:143],v[84:87],a[20:23]
AR v35,a35
AR v36,a36
MM a[20:23],a[136:139],v[80:83],a[20:23]
buffer_load_dwordx4 v240, s[24:27], s66 offen offset:0 lds
AR v37,a37
AR v38,a38
MM a[48:51],a[144:147],v[80:83],a[48:51]
s_add_u32 m0, s89, 114688
AR v39,a39
v_mul_f32 v32, v32, v145
EX v54,v54
MM a[48:51],a[148:151],v[84:87],a[48:51]
s_add_u32 s66, s86, 0
v_mul_f32 v33, v33, v145
CV v20,v48,v49
MM a[56:59],a[148:151],v[92:95],a[56:59]
RD a[220:223],v227 offset:20480
v_mul_f32 v34, v34, v145
v_mul_f32 v35, v35, v145
EX v55,v55
MM a[56:59],a[144:147],v[88:91],a[56:59]
v_mul_f32 v36, v36, v145
v_mul_f32 v37, v37, v145
MM a[60:63],a[152:155],v[88:91],a[60:63]
v_mul_f32 v38, v38, v145
v_mul_f32 v39, v39, v145
MM a[60:63],a[156:159],v[92:95],a[60:63]
CV v176,v32,v33
CV v177,v34,v35
EX v56,v56
MM a[52:55],a[156:159],v[84:87],a[52:55]
RD a[224:227],v224 offset:24576
buffer_load_dwordx4 v240, s[24:27], s66 offen offset:0 lds
CV v178,v36,v37
CV v179,v38,v39
CV v21,v50,v51
MM a[52:55],a[152:155],v[80:83],a[52:55]
s_add_u32 m0, s89, 115712
buffer_store_dwordx4 v[176:179], v248, s[20:23], s78 offen offset:1024 sc1
MM a[80:83],a[160:163],v[80:83],a[80:83]
s_add_u32 s66, s86, 10240
AR v40,a40
AR v41,a41
EX v57,v57
MM a[80:83],a[164:167],v[84:87],a[80:83]
AR v42,a42
AR v43,a43
MM a[88:91],a[164:167],v[92:95],a[88:91]
RD a[228:231],v225 offset:24576
AR v44,a44
AR v45,a45
MM a[88:91],a[160:163],v[88:91],a[88:91]
AR v46,a46
CV v22,v52,v53
EX v58,v58
MM a[92:95],a[168:171],v[88:91],a[92:95]
AR v47,a47
v_mul_f32 v40, v40, v149
MM a[92:95],a[172:175],v[92:95],a[92:95]
v_mul_f32 v41, v41, v149
v_mul_f32 v42, v42, v149
EX v59,v59
MM a[84:87],a[172:175],v[84:87],a[84:87]
RD a[232:235],v226 offset:24576
buffer_load_dwordx4 v240, s[24:27], s66 offen offset:0 lds
v_mul_f32 v43, v43, v149
v_mul_f32 v44, v44, v149
MM a[84:87],a[168:171],v[80:83],a[84:87]
s_add_u32 m0, s89, 122880
v_mul_f32 v45, v45, v149
v_mul_f32 v46, v46, v149
MM a[112:115],a[176:179],v[80:83],a[112:115]
s_add_u32 s66, s86, 81920
v_mul_f32 v47, v47, v149
CV v180,v40,v41
EX v60,v60
CV v23,v54,v55
MM a[112:115],a[180:183],v[84:87],a[112:115]
CV v181,v42,v43
CV v182,v44,v45
MM a[120:123],a[180:183],v[92:95],a[120:123]
RD a[236:239],v227 offset:24576
CV v183,v46,v47
EX v61,v61
MM a[120:123],a[176:179],v[88:91],a[120:123]
buffer_store_dwordx4 v[180:183], v248, s[20:23], s79 offen offset:1024 sc1
AR v0,a64
MM a[124:127],a[184:187],v[88:91],a[124:127]
AR v1,a65
AR v2,a66
MM a[124:127],a[188:191],v[92:95],a[124:127]
AR v3,a67
AR v4,a68
EX v62,v62
MM a[116:119],a[188:191],v[84:87],a[116:119]
RD a[240:243],v224 offset:28672
RD a[244:247],v225 offset:28672
RD a[248:251],v226 offset:28672
RD a[252:255],v227 offset:28672
buffer_load_dwordx4 v240, s[24:27], s66 offen offset:0 lds
EX v63,v63
AR v5,a69
CV v28,v56,v57
MM a[116:119],a[184:187],v[80:83],a[116:119]
s_add_u32 m0, s89, 123904
AR v6,a70
AR v7,a71
v_smfmac_f32_16x16x64_bf16 v[236:239], v[152:155], v[16:23], v234
s_add_u32 s66, s86, 92160
buffer_load_dwordx4 v240, s[24:27], s66 offen offset:0 lds
CV v29,v58,v59
CV v30,v60,v61
CV v31,v62,v63
v_mul_f32 v0, v0, v145
v_mul_f32 v1, v1, v145
v_smfmac_f32_16x16x64_bf16 v[236:239], v[156:159], v[24:31], v234
s_waitcnt lgkmcnt(0)
v_mul_f32 v2, v2, v145
v_mul_f32 v3, v3, v145
MM v[80:83],a[192:195],v[168:171],v[136:139]
v_mul_f32 v4, v4, v145
v_mul_f32 v5, v5, v145
MM v[80:83],a[196:199],v[184:187],v[80:83]
v_mul_f32 v6, v6, v145
v_mul_f32 v7, v7, v145
MM v[80:83],a[200:203],v[200:203],v[80:83]
CV v192,v0,v1
CV v193,v2,v3
MM v[80:83],a[204:207],v[216:219],v[80:83]
TR a[128:129],v232 offset:0
v_add_f32 v246, v236, v237
CV v194,v4,v5
CV v195,v6,v7
MM v[84:87],a[220:223],v[216:219],v[136:139]
TR a[130:131],v232 offset:4096
buffer_store_dwordx4 v[192:195], v248, s[20:23], s78 offen offset:2048 sc1
TR a[132:133],v232 offset:8192
v_cmp_nge_f32 vcc, s71, v246
MM v[84:87],a[216:219],v[200:203],v[84:87]
s_cbranch_vccnz LSITE32_%=
LBACK32_%=:
TR a[134:135],v232 offset:12288
AR v8,a72
AR v9,a73
MM v[84:87],a[212:215],v[184:187],v[84:87]
TR a[136:137],v233 offset:0
AR v10,a74
AR v11,a75
MM v[84:87],a[208:211],v[168:171],v[84:87]
TR a[138:139],v233 offset:4096
AR v12,a76
MM v[92:95],a[208:211],v[172:175],v[140:143]
TR a[140:141],v233 offset:8192
TR a[142:143],v233 offset:12288
AR v13,a77
AR v14,a78
MM v[92:95],a[212:215],v[188:191],v[92:95]
TR a[144:145],v232 offset:512
AR v15,a79
v_mul_f32 v8, v8, v149
MM v[92:95],a[216:219],v[204:207],v[92:95]
TR a[146:147],v232 offset:4608
v_mul_f32 v9, v9, v149
v_mul_f32 v10, v10, v149
MM v[92:95],a[220:223],v[220:223],v[92:95]
TR a[148:149],v232 offset:8704
v_mul_f32 v11, v11, v149
v_mul_f32 v12, v12, v149
MM v[88:91],a[204:207],v[220:223],v[140:143]
TR a[150:151],v232 offset:12800
v_mul_f32 v13, v13, v149
EX v80,v80
v_mul_f32 v14, v14, v149
MM v[88:91],a[200:203],v[204:207],v[88:91]
TR a[152:153],v233 offset:512
TR a[154:155],v233 offset:4608
v_mul_f32 v15, v15, v149
CV v196,v8,v9
EX v81,v81
MM v[88:91],a[196:199],v[188:191],v[88:91]
TR a[156:157],v233 offset:8704
CV v197,v10,v11
CV v198,v12,v13
MM v[88:91],a[192:195],v[172:175],v[88:91]
TR a[158:159],v233 offset:12800
CV v199,v14,v15
EX v82,v82
MM v[112:115],a[224:227],v[168:171],v[136:139]
buffer_store_dwordx4 v[196:199], v248, s[20:23], s79 offen offset:2048 sc1
TR a[160:161],v232 offset:1024
AR v32,a96
EX v83,v83
MM v[112:115],a[228:231],v[184:187],v[112:115]
TR a[162:163],v232 offset:5120
TR a[164:165],v232 offset:9216
AR v33,a97
AR v34,a98
MM v[112:115],a[232:235],v[200:203],v[112:115]
TR a[166:167],v232 offset:13312
AR v35,a99
EX v84,v84
MM v[112:115],a[236:239],v[216:219],v[112:115]
TR a[168:169],v233 offset:1024
AR v36,a100
AR v37,a101
MM v[116:119],a[252:255],v[216:219],v[136:139]
TR a[170:171],v233 offset:5120
AR v38,a102
EX v85,v85
AR v39,a103
MM v[116:119],a[248:251],v[200:203],v[116:119]
TR a[172:173],v233 offset:9216
TR a[174:175],v233 offset:13312
v_mul_f32 v32, v32, v145
v_mul_f32 v33, v33, v145
MM v[116:119],a[244:247],v[184:187],v[116:119]
TR a[176:177],v232 offset:1536
v_mul_f32 v34, v34, v145
v_mul_f32 v35, v35, v145
EX v86,v86
MM v[116:119],a[240:243],v[168:171],v[116:119]
TR a[178:179],v232 offset:5632
v_mul_f32 v36, v36, v145
v_mul_f32 v37, v37, v145
EX v87,v87
MM v[124:127],a[240:243],v[172:175],v[140:143]
TR a[180:181],v232 offset:9728
v_mul_f32 v38, v38, v145
v_mul_f32 v39, v39, v145
CV v80,v80,v81
MM v[124:127],a[244:247],v[188:191],v[124:127]
TR a[182:183],v232 offset:13824
CV v208,v32,v33
EX v88,v88
CV v209,v34,v35
MM v[124:127],a[248:251],v[204:207],v[124:127]
TR a[184:185],v233 offset:1536
TR a[186:187],v233 offset:5632
TR a[188:189],v233 offset:9728
TR a[190:191],v233 offset:13824
CV v210,v36,v37
CV v211,v38,v39
EX v89,v89
MM v[124:127],a[252:255],v[220:223],v[124:127]
buffer_store_dwordx4 v[208:211], v248, s[20:23], s78 offen offset:3072 sc1
CV v81,v82,v83
MM v[120:123],a[236:239],v[220:223],v[140:143]
AR v40,a104
AR v41,a105
EX v90,v90
MM v[120:123],a[232:235],v[204:207],v[120:123]
AR v42,a106
AR v43,a107
MM v[120:123],a[228:231],v[188:191],v[120:123]
EX v91,v91
AR v44,a108
CV v82,v84,v85
MM v[120:123],a[224:227],v[172:175],v[120:123]
s_waitcnt vmcnt(19) lgkmcnt(0)
s_barrier
AR v45,a109
AR v46,a110
EX v92,v92
MM a[16:19],a[128:131],v[16:19],a[16:19]
s_add_u32 m0, s89, 0
AR v47,a111
v_mul_f32 v40, v40, v149
MM a[16:19],a[132:135],v[20:23],a[16:19]
s_add_u32 s66, s87, 0
v_mul_f32 v41, v41, v149
v_mul_f32 v42, v42, v149
EX v93,v93
CV v83,v86,v87
MM a[24:27],a[132:135],v[28:31],a[24:27]
v_mul_f32 v43, v43, v149
v_mul_f32 v44, v44, v149
MM a[24:27],a[128:131],v[24:27],a[24:27]
v_mul_f32 v45, v45, v149
v_mul_f32 v46, v46, v149
EX v94,v94
MM a[28:31],a[136:139],v[24:27],a[28:31]
v_mul_f32 v47, v47, v149
CV v212,v40,v41
EX v95,v95
MM a[28:31],a[140:143],v[28:31],a[28:31]
CV v213,v42,v43
CV v88,v88,v89
CV v214,v44,v45
MM a[20:23],a[140:143],v[20:23],a[20:23]
CV v215,v46,v47
CV v89,v90,v91
EX v112,v112
MM a[20:23],a[136:139],v[16:19],a[20:23]
buffer_store_dwordx4 v[212:215], v248, s[20:23], s79 offen offset:3072 sc1
MM a[48:51],a[144:147],v[16:19],a[48:51]
buffer_load_dwordx4 v240, s[24:27], s66 offen offset:0 lds
RD a[192:195],v224 offset:32768
EX v113,v113
MM a[48:51],a[148:151],v[20:23],a[48:51]
s_add_u32 m0, s89, 1024
EX v114,v114
MM a[56:59],a[148:151],v[28:31],a[56:59]
s_add_u32 s66, s87, 10240
RD a[196:199],v225 offset:32768
CV v90,v92,v93
MM a[56:59],a[144:147],v[24:27],a[56:59]
buffer_load_dwordx4 v240, s[24:27], s66 offen offset:0 lds
RD a[200:203],v226 offset:32768
EX v115,v115
MM a[60:63],a[152:155],v[24:27],a[60:63]
s_add_u32 m0, s89, 8192
EX v116,v116
MM a[60:63],a[156:159],v[28:31],a[60:63]
s_add_u32 s66, s87, 81920
RD a[204:207],v227 offset:32768
CV v91,v94,v95
MM a[52:55],a[156:159],v[20:23],a[52:55]
EX v117,v117
MM a[52:55],a[152:155],v[16:19],a[52:55]
RD a[208:211],v224 offset:36864
RD a[212:215],v225 offset:36864
buffer_load_dwordx4 v240, s[24:27], s66 offen offset:0 lds
MM a[80:83],a[160:163],v[16:19],a[80:83]
s_add_u32 m0, s89, 9216
EX v118,v118
MM a[80:83],a[164:167],v[20:23],a[80:83]
s_add_u32 s66, s87, 92160
RD a[216:219],v226 offset:36864
CV v84,v112,v113
MM a[88:91],a[164:167],v[28:31],a[88:91]
EX v119,v119
MM a[88:91],a[160:163],v[24:27],a[88:91]
buffer_load_dwordx4 v240, s[24:27], s66 offen offset:0 lds
EX v120,v120
MM a[92:95],a[168:171],v[24:27],a[92:95]
s_add_u32 m0, s89, 65536
)ASM"
R"ASM(
RD a[220:223],v227 offset:36864
CV v85,v114,v115
MM a[92:95],a[172:175],v[28:31],a[92:95]
s_add_u32 s66, s88, 0
EX v121,v121
MM a[84:87],a[172:175],v[20:23],a[84:87]
RD a[224:227],v224 offset:40960
CV v86,v116,v117
MM a[84:87],a[168:171],v[16:19],a[84:87]
buffer_load_dwordx4 v240, s[24:27], s66 offen offset:0 lds
EX v122,v122
MM a[112:115],a[176:179],v[16:19],a[112:115]
s_add_u32 m0, s89, 66560
RD a[228:231],v225 offset:40960
EX v123,v123
MM a[112:115],a[180:183],v[20:23],a[112:115]
s_add_u32 s66, s88, 10240
RD a[232:235],v226 offset:40960
CV v87,v118,v119
MM a[120:123],a[180:183],v[28:31],a[120:123]
EX v124,v124
MM a[120:123],a[176:179],v[24:27],a[120:123]
buffer_load_dwordx4 v240, s[24:27], s66 offen offset:0 lds
MM a[124:127],a[184:187],v[24:27],a[124:127]
s_add_u32 m0, s89, 73728
RD a[236:239],v227 offset:40960
EX v125,v125
MM a[124:127],a[188:191],v[28:31],a[124:127]
s_add_u32 s66, s88, 81920
EX v126,v126
MM a[116:119],a[188:191],v[20:23],a[116:119]
RD a[240:243],v224 offset:45056
RD a[244:247],v225 offset:45056
RD a[248:251],v226 offset:45056
RD a[252:255],v227 offset:45056
EX v127,v127
MM a[116:119],a[184:187],v[16:19],a[116:119]
buffer_load_dwordx4 v240, s[24:27], s66 offen offset:0 lds
CV v92,v120,v121
v_smfmac_f32_16x16x64_bf16 v[236:239], v[152:155], v[80:87], v234
s_add_u32 m0, s89, 74752
CV v93,v122,v123
CV v94,v124,v125
CV v95,v126,v127
s_nop 1
v_smfmac_f32_16x16x64_bf16 v[236:239], v[156:159], v[88:95], v234
s_waitcnt lgkmcnt(0)
s_add_u32 s66, s88, 92160
buffer_load_dwordx4 v240, s[24:27], s66 offen offset:0 lds
v_lshrrev_b32 v230, 9, v248
v_lshlrev_b32 v230, 9, v230
v_sub_u32 v244, v232, v230
v_sub_u32 v235, v233, v230
v_sub_u32 v244, v244, v230
v_sub_u32 v235, v235, v230
v_sub_u32 v228, v244, v230
v_sub_u32 v229, v235, v230
v_add_u32 v228, 65536, v228
v_add_u32 v229, 65536, v229
v_subrev_u32 v230, 65536, v244
v_subrev_u32 v231, 65536, v235
MM v[16:19],a[192:195],v[168:171],v[136:139]
TR a[128:129],v232 offset:16384
TR a[130:131],v232 offset:20480
MM v[16:19],a[196:199],v[184:187],v[16:19]
TR a[132:133],v232 offset:24576
TR a[134:135],v232 offset:28672
MM v[16:19],a[200:203],v[200:203],v[16:19]
TR a[136:137],v233 offset:16384
TR a[138:139],v233 offset:20480
MM v[16:19],a[204:207],v[216:219],v[16:19]
TR a[140:141],v233 offset:24576
v_add_f32 v247, v236, v237
MM v[20:23],a[220:223],v[216:219],v[136:139]
TR a[142:143],v233 offset:28672
v_cmp_nge_f32 vcc, s71, v247
MM v[20:23],a[216:219],v[200:203],v[20:23]
s_cbranch_vccnz LSITE33_%=
LBACK33_%=:
TR a[144:145],v232 offset:16896
TR a[146:147],v232 offset:20992
MM v[20:23],a[212:215],v[184:187],v[20:23]
TR a[148:149],v232 offset:25088
TR a[150:151],v232 offset:29184
MM v[20:23],a[208:211],v[168:171],v[20:23]
s_nop 0
LPG20_%=:
s_cbranch_execz LPG24_%=
TR a[152:153],v233 offset:16896
TR a[154:155],v233 offset:20992
MM v[28:31],a[208:211],v[172:175],v[140:143]
EX v16,v16
MM v[28:31],a[212:215],v[188:191],v[28:31]
EX v17,v17
MM v[28:31],a[216:219],v[204:207],v[28:31]
s_lshl_b32 s97, s84, 2
s_add_u32 s97, s97, s96
s_add_u32 s97, s97, 0
MM v[28:31],a[220:223],v[220:223],v[28:31]
s_waitcnt vmcnt(8)
v_mov_b32 v242, s69
s_mov_b64 exec, 1
buffer_store_dword v242, off, s[20:23], s97 offset:0 sc1
s_mov_b64 exec, -1
MM v[24:27],a[204:207],v[220:223],v[140:143]
TR a[156:157],v233 offset:25088
TR a[158:159],v233 offset:29184
MM v[24:27],a[200:203],v[204:207],v[24:27]
EX v18,v18
MM v[24:27],a[196:199],v[188:191],v[24:27]
TR a[160:161],v232 offset:17408
TR a[162:163],v232 offset:21504
MM v[24:27],a[192:195],v[172:175],v[24:27]
EX v19,v19
MM v[48:51],a[224:227],v[168:171],v[136:139]
TR a[164:165],v232 offset:25600
TR a[166:167],v232 offset:29696
MM v[48:51],a[228:231],v[184:187],v[48:51]
EX v20,v20
MM v[48:51],a[232:235],v[200:203],v[48:51]
TR a[168:169],v233 offset:17408
TR a[170:171],v233 offset:21504
MM v[48:51],a[236:239],v[216:219],v[48:51]
EX v21,v21
MM v[52:55],a[252:255],v[216:219],v[136:139]
TR a[172:173],v233 offset:25600
CV v16,v16,v17
MM v[52:55],a[248:251],v[200:203],v[52:55]
TR a[174:175],v233 offset:29696
TR a[176:177],v232 offset:17920
MM v[52:55],a[244:247],v[184:187],v[52:55]
EX v22,v22
MM v[52:55],a[240:243],v[168:171],v[52:55]
TR a[178:179],v232 offset:22016
TR a[180:181],v232 offset:26112
EX v23,v23
MM v[60:63],a[240:243],v[172:175],v[140:143]
TR a[182:183],v232 offset:30208
CV v17,v18,v19
MM v[60:63],a[244:247],v[188:191],v[60:63]
EX v24,v24
MM v[60:63],a[248:251],v[204:207],v[60:63]
TR a[184:185],v233 offset:17920
TR a[186:187],v233 offset:22016
TR a[188:189],v233 offset:26112
TR a[190:191],v233 offset:30208
EX v25,v25
MM v[60:63],a[252:255],v[220:223],v[60:63]
EX v26,v26
MM v[56:59],a[236:239],v[220:223],v[140:143]
EX v27,v27
MM v[56:59],a[232:235],v[204:207],v[56:59]
EX v28,v28
MM v[56:59],a[228:231],v[188:191],v[56:59]
EX v29,v29
CV v18,v20,v21
MM v[56:59],a[224:227],v[172:175],v[56:59]
s_waitcnt lgkmcnt(0)
s_barrier
CV v19,v22,v23
MM a[16:19],a[128:131],v[80:83],a[16:19]
s_add_u32 m0, s89, 16384
EX v30,v30
MM a[16:19],a[132:135],v[84:87],a[16:19]
s_add_u32 s66, s85, 128
EX v31,v31
MM a[24:27],a[132:135],v[92:95],a[24:27]
buffer_load_dwordx4 v240, s[24:27], s66 offen offset:0 lds
MM a[24:27],a[128:131],v[88:91],a[24:27]
s_add_u32 m0, s89, 17408
EX v48,v48
MM a[28:31],a[136:139],v[88:91],a[28:31]
s_add_u32 s66, s85, 10368
CV v24,v24,v25
CV v25,v26,v27
MM a[28:31],a[140:143],v[92:95],a[28:31]
CV v26,v28,v29
CV v27,v30,v31
MM a[20:23],a[140:143],v[84:87],a[20:23]
EX v49,v49
MM a[20:23],a[136:139],v[80:83],a[20:23]
EX v50,v50
MM a[48:51],a[144:147],v[80:83],a[48:51]
buffer_load_dwordx4 v240, s[24:27], s66 offen offset:0 lds
MM a[48:51],a[148:151],v[84:87],a[48:51]
s_add_u32 m0, s89, 24576
EX v51,v51
MM a[56:59],a[148:151],v[92:95],a[56:59]
s_add_u32 s66, s85, 82048
TR a[128:129],v232 offset:32768
CV v20,v48,v49
MM a[56:59],a[144:147],v[88:91],a[56:59]
EX v52,v52
MM a[60:63],a[152:155],v[88:91],a[60:63]
TR a[130:131],v232 offset:36864
TR a[132:133],v232 offset:40960
MM a[60:63],a[156:159],v[92:95],a[60:63]
EX v53,v53
MM a[52:55],a[156:159],v[84:87],a[52:55]
TR a[134:135],v232 offset:45056
TR a[136:137],v233 offset:32768
MM a[52:55],a[152:155],v[80:83],a[52:55]
buffer_load_dwordx4 v240, s[24:27], s66 offen offset:0 lds
MM a[80:83],a[160:163],v[80:83],a[80:83]
s_add_u32 m0, s89, 25600
TR a[138:139],v233 offset:36864
TR a[140:141],v233 offset:40960
EX v54,v54
MM a[80:83],a[164:167],v[84:87],a[80:83]
s_add_u32 s66, s85, 92288
TR a[142:143],v233 offset:45056
TR a[144:145],v232 offset:33280
MM a[88:91],a[164:167],v[92:95],a[88:91]
EX v55,v55
MM a[88:91],a[160:163],v[88:91],a[88:91]
TR a[146:147],v232 offset:37376
TR a[148:149],v232 offset:41472
buffer_load_dwordx4 v240, s[24:27], s66 offen offset:0 lds
EX v56,v56
MM a[92:95],a[168:171],v[88:91],a[92:95]
s_add_u32 m0, s89, 81920
TR a[150:151],v232 offset:45568
CV v21,v50,v51
MM a[92:95],a[172:175],v[92:95],a[92:95]
s_add_u32 s66, s86, 128
TR a[152:153],v233 offset:33280
EX v57,v57
MM a[84:87],a[172:175],v[84:87],a[84:87]
TR a[154:155],v233 offset:37376
TR a[156:157],v233 offset:41472
MM a[84:87],a[168:171],v[80:83],a[84:87]
TR a[158:159],v233 offset:45568
buffer_load_dwordx4 v240, s[24:27], s66 offen offset:0 lds
EX v58,v58
MM a[112:115],a[176:179],v[80:83],a[112:115]
s_add_u32 m0, s89, 82944
TR a[160:161],v232 offset:33792
CV v22,v52,v53
EX v59,v59
MM a[112:115],a[180:183],v[84:87],a[112:115]
s_add_u32 s66, s86, 10368
TR a[162:163],v232 offset:37888
TR a[164:165],v232 offset:41984
MM a[120:123],a[180:183],v[92:95],a[120:123]
TR a[166:167],v232 offset:46080
CV v23,v54,v55
EX v60,v60
MM a[120:123],a[176:179],v[88:91],a[120:123]
TR a[168:169],v233 offset:33792
TR a[170:171],v233 offset:37888
buffer_load_dwordx4 v240, s[24:27], s66 offen offset:0 lds
MM a[124:127],a[184:187],v[88:91],a[124:127]
s_add_u32 m0, s89, 90112
TR a[172:173],v233 offset:41984
v_lshrrev_b32 v243, 2, v248
v_min_u32 v243, 12, v243
EX v61,v61
MM a[124:127],a[188:191],v[92:95],a[124:127]
s_add_u32 s66, s86, 82048
buffer_load_dword v242, v243, s[20:23], s96 offen offset:0 sc1
TR a[174:175],v233 offset:46080
EX v62,v62
MM a[116:119],a[188:191],v[84:87],a[116:119]
TR a[176:177],v232 offset:34304
TR a[178:179],v232 offset:38400
EX v63,v63
MM a[116:119],a[184:187],v[80:83],a[116:119]
TR a[180:181],v232 offset:42496
buffer_load_dwordx4 v240, s[24:27], s66 offen offset:0 lds
CV v28,v56,v57
v_smfmac_f32_16x16x64_bf16 v[236:239], v[152:155], v[16:23], v234
s_add_u32 m0, s89, 91136
TR a[182:183],v232 offset:46592
CV v29,v58,v59
CV v30,v60,v61
CV v31,v62,v63
s_nop 1
v_smfmac_f32_16x16x64_bf16 v[236:239], v[156:159], v[24:31], v234
TR a[184:185],v233 offset:34304
TR a[186:187],v233 offset:38400
TR a[188:189],v233 offset:42496
TR a[190:191],v233 offset:46592
s_waitcnt lgkmcnt(0)
s_add_u32 s66, s86, 92288
buffer_load_dwordx4 v240, s[24:27], s66 offen offset:0 lds
s_nop 6
v_add_f32 v246, v236, v237
v_cmp_nge_f32 vcc, s71, v246
s_cbranch_vccnz LSITE34_%=
LBACK34_%=:
s_waitcnt lgkmcnt(0)
MM a[16:19],a[128:131],v[16:19],a[16:19]
s_waitcnt vmcnt(10)
s_barrier
MM a[16:19],a[132:135],v[20:23],a[16:19]
v_rcp_f32 v152, v246
MM a[24:27],a[132:135],v[28:31],a[24:27]
v_mov_b32 v153, v152
MM a[24:27],a[128:131],v[24:27],a[24:27]
s_nop 0
v_permlane32_swap_b32 v152, v153
MM a[28:31],a[136:139],v[24:27],a[28:31]
v_mov_b32 v157, v153
MM a[28:31],a[140:143],v[28:31],a[28:31]
s_nop 0
v_permlane16_swap_b32 v153, v157
MM a[20:23],a[140:143],v[20:23],a[20:23]
s_mul_i32 s78, s49, 17408
s_add_u32 s78, s83, s78
s_add_u32 s78, s78, 8192
MM a[20:23],a[136:139],v[16:19],a[20:23]
s_add_u32 s79, s78, 4096
MM a[48:51],a[144:147],v[16:19],a[48:51]
TR a[128:129],v230 offset:49152
TR a[130:131],v230 offset:51200
MM a[48:51],a[148:151],v[20:23],a[48:51]
MM a[56:59],a[148:151],v[28:31],a[56:59]
TR a[132:133],v230 offset:53248
TR a[134:135],v230 offset:55296
MM a[56:59],a[144:147],v[24:27],a[56:59]
AR v80,a16
AR v81,a17
MM a[60:63],a[152:155],v[24:27],a[60:63]
TR a[136:137],v230 offset:57344
TR a[138:139],v230 offset:59392
MM a[60:63],a[156:159],v[28:31],a[60:63]
AR v82,a18
AR v83,a19
MM a[52:55],a[156:159],v[20:23],a[52:55]
TR a[140:141],v230 offset:61440
TR a[142:143],v230 offset:63488
MM a[52:55],a[152:155],v[16:19],a[52:55]
AR v84,a20
AR v85,a21
MM a[80:83],a[160:163],v[16:19],a[80:83]
TR a[144:145],v231 offset:49152
TR a[146:147],v231 offset:51200
MM a[80:83],a[164:167],v[20:23],a[80:83]
AR v86,a22
AR v87,a23
MM a[88:91],a[164:167],v[28:31],a[88:91]
TR a[148:149],v231 offset:53248
TR a[150:151],v231 offset:55296
MM a[88:91],a[160:163],v[24:27],a[88:91]
s_waitcnt vmcnt(2)
v_cmp_ne_u32 vcc, s69, v242
s_cbranch_vccnz LRPO0_%=
LRPO0b_%=:
v_mul_f32 v80, v80, v153
MM a[92:95],a[168:171],v[24:27],a[92:95]
s_xor_b32 s92, s49, 1
TR a[152:153],v231 offset:57344
TR a[154:155],v231 offset:59392
v_mul_f32 v81, v81, v153
v_mul_f32 v82, v82, v153
MM a[92:95],a[172:175],v[28:31],a[92:95]
s_mul_i32 s92, s92, 17408
s_add_u32 s92, s83, s92
v_mul_f32 v83, v83, v153
MM a[84:87],a[172:175],v[20:23],a[84:87]
TR a[156:157],v231 offset:61440
TR a[158:159],v231 offset:63488
v_mul_f32 v84, v84, v153
v_mul_f32 v85, v85, v153
MM a[84:87],a[168:171],v[16:19],a[84:87]
buffer_load_dwordx4 a[0:3], v248, s[20:23], s92 offen offset:0 sc1
v_mul_f32 v86, v86, v153
v_mul_f32 v87, v87, v153
MM a[112:115],a[176:179],v[16:19],a[112:115]
TR a[160:161],v230 offset:49664
TR a[162:163],v230 offset:51712
CV v168,v80,v81
MM a[112:115],a[180:183],v[20:23],a[112:115]
buffer_load_dwordx4 a[32:35], v248, s[20:23], s92 offen offset:1024 sc1
CV v169,v82,v83
CV v170,v84,v85
MM a[120:123],a[180:183],v[28:31],a[120:123]
TR a[164:165],v230 offset:53760
TR a[166:167],v230 offset:55808
CV v171,v86,v87
MM a[120:123],a[176:179],v[24:27],a[120:123]
buffer_load_dwordx4 a[64:67], v248, s[20:23], s92 offen offset:2048 sc1
MM a[124:127],a[184:187],v[24:27],a[124:127]
TR a[168:169],v230 offset:57856
TR a[170:171],v230 offset:59904
MM a[124:127],a[188:191],v[28:31],a[124:127]
MM a[116:119],a[188:191],v[20:23],a[116:119]
buffer_store_dwordx4 v[168:171], v248, s[20:23], s78 offen offset:0 sc1
TR a[172:173],v230 offset:61952
)ASM"
R"ASM(
TR a[174:175],v230 offset:64000
AR v88,a24
AR v89,a25
AR v90,a26
AR v91,a27
AR v92,a28
MM a[116:119],a[184:187],v[16:19],a[116:119]
AR v93,a29
AR v94,a30
s_waitcnt lgkmcnt(15)
MM v[0:3],a[128:131],v[160:163],0
TR a[176:177],v231 offset:49664
TR a[178:179],v231 offset:51712
AR v95,a31
MM v[0:3],a[132:135],v[176:179],v[0:3]
v_bfe_u32 v249, v248, 6, 2
v_lshlrev_b32 v249, 8, v249
v_mul_f32 v88, v88, v157
v_mul_f32 v89, v89, v157
MM v[0:3],a[136:139],v[192:195],v[0:3]
TR a[180:181],v231 offset:53760
TR a[182:183],v231 offset:55808
v_mul_f32 v90, v90, v157
v_mul_f32 v91, v91, v157
MM v[0:3],a[140:143],v[208:211],v[0:3]
buffer_load_dwordx4 a[96:99], v248, s[20:23], s92 offen offset:3072 sc1
v_lshrrev_b32 v250, 8, v248
v_lshl_add_u32 v249, v250, 6, v249
v_mul_f32 v92, v92, v157
MM v[8:11],a[140:143],v[212:215],0
s_add_u32 s92, s92, 4096
TR a[184:185],v231 offset:57856
TR a[186:187],v231 offset:59904
v_mul_f32 v93, v93, v157
v_mul_f32 v94, v94, v157
MM v[8:11],a[136:139],v[196:199],v[8:11]
v_and_b32 v251, 48, v248
v_add_u32 v249, v249, v251
v_mul_f32 v95, v95, v157
CV v172,v88,v89
MM v[8:11],a[132:135],v[180:183],v[8:11]
s_lshr_b32 s45, s49, 3
buffer_load_dwordx4 a[4:7], v248, s[20:23], s92 offen offset:0 sc1
TR a[188:189],v231 offset:61952
TR a[190:191],v231 offset:64000
CV v173,v90,v91
MM v[8:11],a[128:131],v[164:167],v[8:11]
s_waitcnt lgkmcnt(15)
v_bfe_u32 v245, v248, 6, 2
v_xor_b32 v251, s45, v245
CV v174,v92,v93
CV v175,v94,v95
MM v[4:7],a[144:147],v[160:163],0
buffer_store_dwordx4 v[172:175], v248, s[20:23], s79 offen offset:0 sc1
TR a[128:129],v228 offset:0
TR a[130:131],v228 offset:1024
MM v[4:7],a[148:151],v[176:179],v[4:7]
v_add_u32 v251, -1, v251
v_and_b32 v251, 0x40000000, v251
AR v112,a48
MM v[4:7],a[152:155],v[192:195],v[4:7]
TR a[132:133],v228 offset:2048
TR a[134:135],v228 offset:3072
AR v113,a49
AR v114,a50
MM v[4:7],a[156:159],v[208:211],v[4:7]
v_lshlrev_b32 v245, 6, v245
v_lshl_add_u32 v245, s45, 4, v245
AR v115,a51
MM v[12:15],a[156:159],v[212:215],0
TR a[136:137],v228 offset:4096
TR a[138:139],v228 offset:5120
AR v116,a52
AR v117,a53
MM v[12:15],a[152:155],v[196:199],v[12:15]
v_mov_b32 v234, v249
v_add_u32 v249, v249, v251
AR v118,a54
AR v119,a55
MM v[12:15],a[148:151],v[180:183],v[12:15]
buffer_load_dwordx4 a[36:39], v248, s[20:23], s92 offen offset:1024 sc1
TR a[140:141],v228 offset:6144
TR a[142:143],v228 offset:7168
v_mul_f32 v112, v112, v153
MM v[12:15],a[144:147],v[164:167],v[12:15]
s_waitcnt lgkmcnt(15)
v_bfe_u32 v251, v248, 4, 2
v_lshl_add_u32 v245, v251, 2, v245
v_mul_f32 v113, v113, v153
v_mul_f32 v114, v114, v153
MM v[32:35],a[160:163],v[160:163],0
s_lshl_b32 s47, %[sb], 10
TR a[144:145],v229 offset:0
TR a[146:147],v229 offset:1024
v_mul_f32 v115, v115, v153
v_mul_f32 v116, v116, v153
MM v[32:35],a[164:167],v[176:179],v[32:35]
s_and_b32 s47, s47, 1024
s_add_u32 s46, s95, s47
s_sub_u32 s47, s95, s47
v_mul_f32 v117, v117, v153
MM v[32:35],a[168:171],v[192:195],v[32:35]
TR a[148:149],v229 offset:2048
TR a[150:151],v229 offset:3072
v_mul_f32 v118, v118, v153
v_mul_f32 v119, v119, v153
MM v[32:35],a[172:175],v[208:211],v[32:35]
s_bitcmp1_b32 s49, 4
s_cselect_b32 s44, -1, 1
s_mul_i32 s44, s44, 331776
buffer_load_dwordx4 a[68:71], v248, s[20:23], s92 offen offset:2048 sc1
CV v184,v112,v113
MM v[40:43],a[172:175],v[212:215],0
s_add_u32 s44, s44, s16
TR a[152:153],v229 offset:4096
TR a[154:155],v229 offset:5120
CV v185,v114,v115
CV v186,v116,v117
MM v[40:43],a[168:171],v[196:199],v[40:43]
s_xor_b32 s45, s68, 64
buffer_load_dwordx4 a[100:103], v248, s[20:23], s92 offen offset:3072 sc1
CV v187,v118,v119
MM v[40:43],a[164:167],v[180:183],v[40:43]
s_bfe_u32 s80, s49, 131074
TR a[156:157],v229 offset:6144
TR a[158:159],v229 offset:7168
v_xor_b32 v250, s80, v250
v_mul_u32_u24 v250, 82944, v250
v_and_b32 v252, 240, v248
v_add_u32 v250, v250, v252
v_lshrrev_b32 v251, 8, v248
v_mul_u32_u24 v251, 1280, v251
v_bfe_u32 v252, v248, 4, 3
v_lshl_add_u32 v251, v252, 3, v251
v_bfe_u32 v252, v248, 7, 1
v_mul_u32_u24 v254, 40960, v252
v_add_u32 v254, v254, v251
v_lshl_add_u32 v251, v252, 6, v251
v_lshrrev_b32 v253, 8, v248
MM v[40:43],a[160:163],v[164:167],v[40:43]
s_waitcnt lgkmcnt(15)
v_and_b32 v252, 240, v248
v_lshl_add_u32 v232, v253, 2, v252
s_waitcnt vmcnt(10)
s_barrier
MM v[36:39],a[176:179],v[160:163],0
TR a[160:161],v230 offset:16384
TR a[162:163],v230 offset:18432
MM v[36:39],a[180:183],v[176:179],v[36:39]
buffer_store_dwordx4 v[184:187], v248, s[20:23], s78 offen offset:1024 sc1
AR v120,a56
AR v121,a57
AR v122,a58
AR v123,a59
AR v124,a60
v_xor_b32 v253, s80, v253
MM v[36:39],a[184:187],v[192:195],v[36:39]
TR a[164:165],v230 offset:20480
TR a[166:167],v230 offset:22528
AR v125,a61
AR v126,a62
MM v[36:39],a[188:191],v[208:211],v[36:39]
AR v127,a63
v_lshlrev_b32 v253, 4, v253
v_mul_f32 v120, v120, v157
MM v[44:47],a[188:191],v[212:215],0
TR a[168:169],v230 offset:24576
TR a[170:171],v230 offset:26624
v_mul_f32 v121, v121, v157
v_mov_b32 v233, s69
MM v[44:47],a[184:187],v[196:199],v[44:47]
s_add_u32 s80, s18, 131072
v_mul_f32 v122, v122, v157
v_mul_f32 v123, v123, v157
MM v[44:47],a[180:183],v[180:183],v[44:47]
buffer_load_dwordx2 v[224:225], v251, s[20:23], s80 offen offset:0 sc1
TR a[172:173],v230 offset:28672
TR a[174:175],v230 offset:30720
v_mul_f32 v124, v124, v157
MM v[44:47],a[176:179],v[164:167],v[44:47]
s_waitcnt lgkmcnt(15)
v_mul_f32 v125, v125, v157
v_mul_f32 v126, v126, v157
MM v[128:131],a[128:131],v[160:163],0
s_add_u32 s80, s18, 131200
TR a[176:177],v231 offset:16384
TR a[178:179],v231 offset:18432
v_mul_f32 v127, v127, v157
CV v188,v120,v121
MM v[128:131],a[132:135],v[176:179],v[128:131]
buffer_load_dwordx2 v[226:227], v251, s[20:23], s80 offen offset:0 sc1
CV v189,v122,v123
CV v190,v124,v125
MM v[128:131],a[136:139],v[192:195],v[128:131]
TR a[180:181],v231 offset:20480
TR a[182:183],v231 offset:22528
CV v191,v126,v127
MM v[128:131],a[140:143],v[208:211],v[128:131]
s_add_u32 s80, s18, 172032
buffer_load_dwordx2 v[236:237], v251, s[20:23], s80 offen offset:0 sc1
MM v[136:139],a[140:143],v[212:215],0
TR a[184:185],v231 offset:24576
TR a[186:187],v231 offset:26624
MM v[136:139],a[136:139],v[196:199],v[136:139]
s_add_u32 s80, s18, 172160
buffer_load_dwordx2 v[238:239], v251, s[20:23], s80 offen offset:0 sc1
MM v[136:139],a[132:135],v[180:183],v[136:139]
buffer_store_dwordx4 v[188:191], v248, s[20:23], s79 offen offset:1024 sc1
TR a[188:189],v231 offset:28672
TR a[190:191],v231 offset:30720
AR v80,a80
AR v81,a81
AR v82,a82
AR v83,a83
AR v84,a84
MM v[136:139],a[128:131],v[164:167],v[136:139]
s_waitcnt lgkmcnt(15)
AR v85,a85
AR v86,a86
MM v[132:135],a[144:147],v[160:163],0
s_add_u32 s80, s63, 131072
buffer_load_dwordx2 v[246:247], v254, s[20:23], s80 offen offset:0 sc1
TR a[128:129],v230 offset:16896
TR a[130:131],v230 offset:18944
AR v87,a87
MM v[132:135],a[148:151],v[176:179],v[132:135]
v_mul_f32 v80, v80, v153
v_mul_f32 v81, v81, v153
MM v[132:135],a[152:155],v[192:195],v[132:135]
TR a[132:133],v230 offset:20992
TR a[134:135],v230 offset:23040
v_mul_f32 v82, v82, v153
MM v[132:135],a[156:159],v[208:211],v[132:135]
v_mul_f32 v83, v83, v153
v_mul_f32 v84, v84, v153
MM v[140:143],a[156:159],v[212:215],0
TR a[136:137],v230 offset:25088
TR a[138:139],v230 offset:27136
v_mul_f32 v85, v85, v153
MM v[140:143],a[152:155],v[196:199],v[140:143]
v_mul_f32 v86, v86, v153
v_mul_f32 v87, v87, v153
MM v[140:143],a[148:151],v[180:183],v[140:143]
TR a[140:141],v230 offset:29184
TR a[142:143],v230 offset:31232
CV v200,v80,v81
CV v201,v82,v83
MM v[140:143],a[144:147],v[164:167],v[140:143]
s_waitcnt lgkmcnt(15)
CV v202,v84,v85
CV v203,v86,v87
MM v[64:67],a[160:163],v[160:163],0
TR a[144:145],v231 offset:16896
TR a[146:147],v231 offset:18944
MM v[64:67],a[164:167],v[176:179],v[64:67]
MM v[64:67],a[168:171],v[192:195],v[64:67]
TR a[148:149],v231 offset:20992
TR a[150:151],v231 offset:23040
MM v[64:67],a[172:175],v[208:211],v[64:67]
buffer_store_dwordx4 v[200:203], v248, s[20:23], s78 offen offset:2048 sc1
AR v88,a88
AR v89,a89
AR v90,a90
AR v91,a91
MM v[72:75],a[172:175],v[212:215],0
TR a[152:153],v231 offset:25088
TR a[154:155],v231 offset:27136
AR v92,a92
MM v[72:75],a[168:171],v[196:199],v[72:75]
AR v93,a93
AR v94,a94
MM v[72:75],a[164:167],v[180:183],v[72:75]
TR a[156:157],v231 offset:29184
TR a[158:159],v231 offset:31232
AR v95,a95
v_mul_f32 v88, v88, v157
MM v[72:75],a[160:163],v[164:167],v[72:75]
s_waitcnt lgkmcnt(15)
v_mul_f32 v89, v89, v157
v_mul_f32 v90, v90, v157
MM v[68:71],a[176:179],v[160:163],0
TR a[160:161],v244 offset:49152
TR a[162:163],v244 offset:51200
v_mul_f32 v91, v91, v157
MM v[68:71],a[180:183],v[176:179],v[68:71]
LPG21_%=:
s_cbranch_execz LTR3_%=
v_mul_f32 v92, v92, v157
v_mul_f32 v93, v93, v157
MM v[68:71],a[184:187],v[192:195],v[68:71]
TR a[164:165],v244 offset:53248
TR a[166:167],v244 offset:55296
v_mul_f32 v94, v94, v157
MM v[68:71],a[188:191],v[208:211],v[68:71]
v_mul_f32 v95, v95, v157
CV v204,v88,v89
MM v[76:79],a[188:191],v[212:215],0
TR a[168:169],v244 offset:57344
TR a[170:171],v244 offset:59392
CV v205,v90,v91
CV v206,v92,v93
MM v[76:79],a[184:187],v[196:199],v[76:79]
CV v207,v94,v95
MM v[76:79],a[180:183],v[180:183],v[76:79]
TR a[172:173],v244 offset:61440
TR a[174:175],v244 offset:63488
MM v[76:79],a[176:179],v[164:167],v[76:79]
s_waitcnt lgkmcnt(15)
MM v[96:99],a[128:131],v[160:163],0
s_xor_b32 s92, s49, 2
buffer_store_dwordx4 v[204:207], v248, s[20:23], s79 offen offset:2048 sc1
TR a[176:177],v235 offset:49152
TR a[178:179],v235 offset:51200
AR v112,a112
AR v113,a113
AR v114,a114
MM v[96:99],a[132:135],v[176:179],v[96:99]
s_mul_i32 s92, s92, 17408
s_add_u32 s92, s83, s92
AR v115,a115
AR v116,a116
MM v[96:99],a[136:139],v[192:195],v[96:99]
TR a[180:181],v235 offset:53248
TR a[182:183],v235 offset:55296
AR v117,a117
MM v[96:99],a[140:143],v[208:211],v[96:99]
buffer_load_dwordx4 a[8:11], v248, s[20:23], s92 offen offset:0 sc1
AR v118,a118
AR v119,a119
MM v[104:107],a[140:143],v[212:215],0
TR a[184:185],v235 offset:57344
TR a[186:187],v235 offset:59392
v_mul_f32 v112, v112, v153
v_mul_f32 v113, v113, v153
MM v[104:107],a[136:139],v[196:199],v[104:107]
buffer_load_dwordx4 a[40:43], v248, s[20:23], s92 offen offset:1024 sc1
v_mul_f32 v114, v114, v153
MM v[104:107],a[132:135],v[180:183],v[104:107]
TR a[188:189],v235 offset:61440
TR a[190:191],v235 offset:63488
v_mul_f32 v115, v115, v153
v_mul_f32 v116, v116, v153
MM v[104:107],a[128:131],v[164:167],v[104:107]
s_waitcnt lgkmcnt(15)
buffer_load_dwordx4 a[72:75], v248, s[20:23], s92 offen offset:2048 sc1
v_mul_f32 v117, v117, v153
v_mul_f32 v118, v118, v153
MM v[100:103],a[144:147],v[160:163],0
TR a[128:129],v244 offset:49664
TR a[130:131],v244 offset:51712
v_mul_f32 v119, v119, v153
MM v[100:103],a[148:151],v[176:179],v[100:103]
CV v216,v112,v113
CV v217,v114,v115
MM v[100:103],a[152:155],v[192:195],v[100:103]
TR a[132:133],v244 offset:53760
TR a[134:135],v244 offset:55808
CV v218,v116,v117
MM v[100:103],a[156:159],v[208:211],v[100:103]
buffer_load_dwordx4 a[104:107], v248, s[20:23], s92 offen offset:3072 sc1
CV v219,v118,v119
MM v[108:111],a[156:159],v[212:215],0
s_add_u32 s92, s92, 4096
TR a[136:137],v244 offset:57856
TR a[138:139],v244 offset:59904
MM v[108:111],a[152:155],v[196:199],v[108:111]
buffer_load_dwordx4 a[12:15], v248, s[20:23], s92 offen offset:0 sc1
MM v[108:111],a[148:151],v[180:183],v[108:111]
TR a[140:141],v244 offset:61952
TR a[142:143],v244 offset:64000
MM v[108:111],a[144:147],v[164:167],v[108:111]
s_waitcnt lgkmcnt(15)
s_waitcnt vmcnt(19)
MM v[0:3],a[160:163],a[0:3],v[0:3]
buffer_store_dwordx4 v[216:219], v248, s[20:23], s78 offen offset:3072 sc1
TR a[144:145],v235 offset:49664
TR a[146:147],v235 offset:51712
AR v120,a120
AR v121,a121
AR v122,a122
AR v123,a123
AR v124,a124
AR v125,a125
AR v126,a126
MM v[0:3],a[164:167],a[32:35],v[0:3]
AR v127,a127
v_mul_f32 v120, v120, v157
MM v[0:3],a[168:171],a[64:67],v[0:3]
TR a[148:149],v235 offset:53760
TR a[150:151],v235 offset:55808
v_mul_f32 v121, v121, v157
v_mul_f32 v122, v122, v157
MM v[0:3],a[172:175],a[96:99],v[0:3]
s_waitcnt vmcnt(15)
)ASM"
R"ASM(
buffer_load_dwordx4 a[44:47], v248, s[20:23], s92 offen offset:1024 sc1
v_mul_f32 v123, v123, v157
MM v[8:11],a[172:175],a[100:103],v[8:11]
TR a[152:153],v235 offset:57856
TR a[154:155],v235 offset:59904
v_mul_f32 v124, v124, v157
v_mul_f32 v125, v125, v157
MM v[8:11],a[168:171],a[68:71],v[8:11]
v_mul_f32 v126, v126, v157
v_mul_f32 v127, v127, v157
MM v[8:11],a[164:167],a[36:39],v[8:11]
TR a[156:157],v235 offset:61952
TR a[158:159],v235 offset:64000
CV v220,v120,v121
MM v[8:11],a[160:163],a[4:7],v[8:11]
s_waitcnt lgkmcnt(15)
buffer_load_dwordx4 a[76:79], v248, s[20:23], s92 offen offset:2048 sc1
CV v221,v122,v123
CV v222,v124,v125
MM v[4:7],a[176:179],a[0:3],v[4:7]
TR a[160:161],v228 offset:8192
TR a[162:163],v228 offset:9216
CV v223,v126,v127
MM v[4:7],a[180:183],a[32:35],v[4:7]
buffer_load_dwordx4 a[108:111], v248, s[20:23], s92 offen offset:3072 sc1
MM v[4:7],a[184:187],a[64:67],v[4:7]
TR a[164:165],v228 offset:10240
TR a[166:167],v228 offset:11264
MM v[4:7],a[188:191],a[96:99],v[4:7]
MM v[12:15],a[188:191],a[100:103],v[12:15]
buffer_store_dwordx4 v[220:223], v248, s[20:23], s79 offen offset:3072 sc1
TR a[168:169],v228 offset:12288
TR a[170:171],v228 offset:13312
MM v[12:15],a[184:187],a[68:71],v[12:15]
MM v[12:15],a[180:183],a[36:39],v[12:15]
TR a[172:173],v228 offset:14336
TR a[174:175],v228 offset:15360
MM v[12:15],a[176:179],a[4:7],v[12:15]
s_waitcnt lgkmcnt(15)
MM v[32:35],a[128:131],a[0:3],v[32:35]
TR a[176:177],v229 offset:8192
TR a[178:179],v229 offset:9216
MM v[32:35],a[132:135],a[32:35],v[32:35]
MM v[32:35],a[136:139],a[64:67],v[32:35]
TR a[180:181],v229 offset:10240
TR a[182:183],v229 offset:11264
MM v[32:35],a[140:143],a[96:99],v[32:35]
MM v[40:43],a[140:143],a[100:103],v[40:43]
TR a[184:185],v229 offset:12288
TR a[186:187],v229 offset:13312
MM v[40:43],a[136:139],a[68:71],v[40:43]
MM v[40:43],a[132:135],a[36:39],v[40:43]
TR a[188:189],v229 offset:14336
TR a[190:191],v229 offset:15360
MM v[40:43],a[128:131],a[4:7],v[40:43]
s_waitcnt lgkmcnt(15)
MM v[36:39],a[144:147],a[0:3],v[36:39]
s_waitcnt vmcnt(12)
TR a[128:129],v230 offset:0
TR a[130:131],v230 offset:2048
MM v[36:39],a[148:151],a[32:35],v[36:39]
v_lshlrev_b32 v112, 16, v224
v_and_b32 v113, 0xffff0000, v224
MM v[36:39],a[152:155],a[64:67],v[36:39]
TR a[132:133],v230 offset:4096
TR a[134:135],v230 offset:6144
MM v[36:39],a[156:159],a[96:99],v[36:39]
v_lshlrev_b32 v114, 16, v225
v_and_b32 v115, 0xffff0000, v225
MM v[44:47],a[156:159],a[100:103],v[44:47]
TR a[136:137],v230 offset:8192
TR a[138:139],v230 offset:10240
MM v[44:47],a[152:155],a[68:71],v[44:47]
v_mul_f32 v116, 0xbfb8aa3b, v112
v_mul_f32 v117, 0xbfb8aa3b, v113
MM v[44:47],a[148:151],a[36:39],v[44:47]
TR a[140:141],v230 offset:12288
TR a[142:143],v230 offset:14336
MM v[44:47],a[144:147],a[4:7],v[44:47]
s_waitcnt lgkmcnt(15)
v_mul_f32 v118, 0xbfb8aa3b, v114
v_mul_f32 v119, 0xbfb8aa3b, v115
MM v[128:131],a[160:163],a[0:3],v[128:131]
TR a[144:145],v231 offset:0
TR a[146:147],v231 offset:2048
MM v[128:131],a[164:167],a[32:35],v[128:131]
EX v116,v116
MM v[128:131],a[168:171],a[64:67],v[128:131]
TR a[148:149],v231 offset:4096
TR a[150:151],v231 offset:6144
MM v[128:131],a[172:175],a[96:99],v[128:131]
EX v117,v117
MM v[136:139],a[172:175],a[100:103],v[136:139]
TR a[152:153],v231 offset:8192
TR a[154:155],v231 offset:10240
MM v[136:139],a[168:171],a[68:71],v[136:139]
EX v118,v118
MM v[136:139],a[164:167],a[36:39],v[136:139]
TR a[156:157],v231 offset:12288
TR a[158:159],v231 offset:14336
MM v[136:139],a[160:163],a[4:7],v[136:139]
s_waitcnt lgkmcnt(15)
EX v119,v119
MM v[132:135],a[176:179],a[0:3],v[132:135]
s_xor_b32 s92, s49, 3
TR a[160:161],v230 offset:512
TR a[162:163],v230 offset:2560
MM v[132:135],a[180:183],a[32:35],v[132:135]
s_mul_i32 s92, s92, 17408
s_add_u32 s92, s83, s92
v_add_f32 v116, 1.0, v116
MM v[132:135],a[184:187],a[64:67],v[132:135]
TR a[164:165],v230 offset:4608
TR a[166:167],v230 offset:6656
MM v[132:135],a[188:191],a[96:99],v[132:135]
buffer_load_dwordx4 a[192:195], v248, s[20:23], s92 offen offset:0 sc1
MM v[140:143],a[188:191],a[100:103],v[140:143]
TR a[168:169],v230 offset:8704
TR a[170:171],v230 offset:10752
MM v[140:143],a[184:187],a[68:71],v[140:143]
buffer_load_dwordx4 a[208:211], v248, s[20:23], s92 offen offset:1024 sc1
MM v[140:143],a[180:183],a[36:39],v[140:143]
TR a[172:173],v230 offset:12800
TR a[174:175],v230 offset:14848
MM v[140:143],a[176:179],a[4:7],v[140:143]
s_waitcnt lgkmcnt(15)
buffer_load_dwordx4 a[224:227], v248, s[20:23], s92 offen offset:2048 sc1
s_waitcnt vmcnt(9)
MM v[0:3],a[128:131],a[8:11],v[0:3]
TR a[176:177],v231 offset:512
TR a[178:179],v231 offset:2560
MM v[0:3],a[132:135],a[40:43],v[0:3]
v_add_f32 v117, 1.0, v117
v_add_f32 v118, 1.0, v118
MM v[0:3],a[136:139],a[72:75],v[0:3]
TR a[180:181],v231 offset:4608
TR a[182:183],v231 offset:6656
MM v[0:3],a[140:143],a[104:107],v[0:3]
s_waitcnt vmcnt(4)
buffer_load_dwordx4 a[240:243], v248, s[20:23], s92 offen offset:3072 sc1
MM v[8:11],a[140:143],a[108:111],v[8:11]
s_add_u32 s92, s92, 4096
TR a[184:185],v231 offset:8704
TR a[186:187],v231 offset:10752
MM v[8:11],a[136:139],a[76:79],v[8:11]
buffer_load_dwordx4 a[196:199], v248, s[20:23], s92 offen offset:0 sc1
MM v[8:11],a[132:135],a[44:47],v[8:11]
TR a[188:189],v231 offset:12800
TR a[190:191],v231 offset:14848
MM v[8:11],a[128:131],a[12:15],v[8:11]
s_waitcnt lgkmcnt(15)
v_add_f32 v119, 1.0, v119
MM v[4:7],a[144:147],a[8:11],v[4:7]
TR a[128:129],v228 offset:16384
TR a[130:131],v228 offset:17408
MM v[4:7],a[148:151],a[40:43],v[4:7]
buffer_load_dwordx4 a[212:215], v248, s[20:23], s92 offen offset:1024 sc1
MM v[4:7],a[152:155],a[72:75],v[4:7]
TR a[132:133],v228 offset:18432
TR a[134:135],v228 offset:19456
MM v[4:7],a[156:159],a[104:107],v[4:7]
buffer_load_dwordx4 a[228:231], v248, s[20:23], s92 offen offset:2048 sc1
MM v[12:15],a[156:159],a[108:111],v[12:15]
TR a[136:137],v228 offset:20480
TR a[138:139],v228 offset:21504
MM v[12:15],a[152:155],a[76:79],v[12:15]
v_rcp_f32 v116, v116
MM v[12:15],a[148:151],a[44:47],v[12:15]
TR a[140:141],v228 offset:22528
TR a[142:143],v228 offset:23552
MM v[12:15],a[144:147],a[12:15],v[12:15]
s_waitcnt lgkmcnt(15)
buffer_load_dwordx4 a[244:247], v248, s[20:23], s92 offen offset:3072 sc1
MM v[32:35],a[160:163],a[8:11],v[32:35]
TR a[144:145],v229 offset:16384
TR a[146:147],v229 offset:17408
MM v[32:35],a[164:167],a[40:43],v[32:35]
s_add_u32 m0, s89, 32768
v_rcp_f32 v117, v117
MM v[32:35],a[168:171],a[72:75],v[32:35]
s_add_u32 s66, s87, 128
TR a[148:149],v229 offset:18432
TR a[150:151],v229 offset:19456
MM v[32:35],a[172:175],a[104:107],v[32:35]
buffer_load_dwordx4 v240, s[24:27], s66 offen offset:0 lds
MM v[40:43],a[172:175],a[108:111],v[40:43]
s_add_u32 m0, s89, 33792
TR a[152:153],v229 offset:20480
TR a[154:155],v229 offset:21504
MM v[40:43],a[168:171],a[76:79],v[40:43]
s_add_u32 s66, s87, 10368
buffer_load_dwordx4 v240, s[24:27], s66 offen offset:0 lds
MM v[40:43],a[164:167],a[44:47],v[40:43]
s_add_u32 m0, s89, 40960
TR a[156:157],v229 offset:22528
TR a[158:159],v229 offset:23552
MM v[40:43],a[160:163],a[12:15],v[40:43]
s_waitcnt lgkmcnt(15)
v_rcp_f32 v118, v118
MM v[36:39],a[176:179],a[8:11],v[36:39]
s_add_u32 s66, s87, 82048
TR a[160:161],v244 offset:0
TR a[162:163],v244 offset:2048
MM v[36:39],a[180:183],a[40:43],v[36:39]
buffer_load_dwordx4 v240, s[24:27], s66 offen offset:0 lds
MM v[36:39],a[184:187],a[72:75],v[36:39]
s_add_u32 m0, s89, 41984
TR a[164:165],v244 offset:4096
TR a[166:167],v244 offset:6144
MM v[36:39],a[188:191],a[104:107],v[36:39]
s_add_u32 s66, s87, 92288
buffer_load_dwordx4 v240, s[24:27], s66 offen offset:0 lds
MM v[44:47],a[188:191],a[108:111],v[44:47]
s_add_u32 m0, s89, 98304
TR a[168:169],v244 offset:8192
TR a[170:171],v244 offset:10240
MM v[44:47],a[184:187],a[76:79],v[44:47]
s_add_u32 s66, s88, 128
buffer_load_dwordx4 v240, s[24:27], s66 offen offset:0 lds
MM v[44:47],a[180:183],a[44:47],v[44:47]
s_add_u32 m0, s89, 99328
TR a[172:173],v244 offset:12288
TR a[174:175],v244 offset:14336
MM v[44:47],a[176:179],a[12:15],v[44:47]
s_waitcnt lgkmcnt(15)
v_rcp_f32 v119, v119
MM v[128:131],a[128:131],a[8:11],v[128:131]
s_add_u32 s66, s88, 10368
TR a[176:177],v235 offset:0
TR a[178:179],v235 offset:2048
MM v[128:131],a[132:135],a[40:43],v[128:131]
buffer_load_dwordx4 v240, s[24:27], s66 offen offset:0 lds
MM v[128:131],a[136:139],a[72:75],v[128:131]
s_add_u32 m0, s89, 106496
TR a[180:181],v235 offset:4096
TR a[182:183],v235 offset:6144
MM v[128:131],a[140:143],a[104:107],v[128:131]
s_add_u32 s66, s88, 82048
buffer_load_dwordx4 v240, s[24:27], s66 offen offset:0 lds
MM v[136:139],a[140:143],a[108:111],v[136:139]
s_add_u32 m0, s89, 107520
TR a[184:185],v235 offset:8192
TR a[186:187],v235 offset:10240
MM v[136:139],a[136:139],a[76:79],v[136:139]
s_add_u32 s66, s88, 92288
v_mul_f32 v116, v112, v116
v_mul_f32 v117, v113, v117
MM v[136:139],a[132:135],a[44:47],v[136:139]
TR a[188:189],v235 offset:12288
TR a[190:191],v235 offset:14336
MM v[136:139],a[128:131],a[12:15],v[136:139]
s_waitcnt lgkmcnt(15)
buffer_load_dwordx4 v240, s[24:27], s66 offen offset:0 lds
MM v[132:135],a[144:147],a[8:11],v[132:135]
s_lshl_b32 s97, s84, 2
TR a[128:129],v244 offset:512
TR a[130:131],v244 offset:2560
MM v[132:135],a[148:151],a[40:43],v[132:135]
s_add_u32 s97, s97, s96
s_add_u32 s97, s97, 16
s_waitcnt vmcnt(16)
MM v[132:135],a[152:155],a[72:75],v[132:135]
TR a[132:133],v244 offset:4608
TR a[134:135],v244 offset:6656
MM v[132:135],a[156:159],a[104:107],v[132:135]
v_mov_b32 v242, s69
s_mov_b64 exec, 1
buffer_store_dword v242, off, s[20:23], s97 offset:0 sc1
s_mov_b64 exec, -1
MM v[140:143],a[156:159],a[108:111],v[140:143]
TR a[136:137],v244 offset:8704
TR a[138:139],v244 offset:10752
MM v[140:143],a[152:155],a[76:79],v[140:143]
v_mul_f32 v118, v114, v118
v_mul_f32 v119, v115, v119
MM v[140:143],a[148:151],a[44:47],v[140:143]
TR a[140:141],v244 offset:12800
TR a[142:143],v244 offset:14848
MM v[140:143],a[144:147],a[12:15],v[140:143]
s_waitcnt lgkmcnt(15)
s_waitcnt vmcnt(13)
MM v[0:3],a[160:163],a[192:195],v[0:3]
TR a[144:145],v235 offset:512
TR a[146:147],v235 offset:2560
MM v[0:3],a[164:167],a[208:211],v[0:3]
MM v[0:3],a[168:171],a[224:227],v[0:3]
TR a[148:149],v235 offset:4608
TR a[150:151],v235 offset:6656
MM v[0:3],a[172:175],a[240:243],v[0:3]
s_waitcnt vmcnt(9)
MM v[8:11],a[172:175],a[244:247],v[8:11]
TR a[152:153],v235 offset:8704
TR a[154:155],v235 offset:10752
MM v[8:11],a[168:171],a[228:231],v[8:11]
MM v[8:11],a[164:167],a[212:215],v[8:11]
TR a[156:157],v235 offset:12800
TR a[158:159],v235 offset:14848
MM v[8:11],a[160:163],a[196:199],v[8:11]
s_waitcnt lgkmcnt(15)
MM v[4:7],a[176:179],a[192:195],v[4:7]
TR a[160:161],v228 offset:24576
TR a[162:163],v228 offset:25600
MM v[4:7],a[180:183],a[208:211],v[4:7]
MM v[4:7],a[184:187],a[224:227],v[4:7]
TR a[164:165],v228 offset:26624
TR a[166:167],v228 offset:27648
MM v[4:7],a[188:191],a[240:243],v[4:7]
MM v[12:15],a[188:191],a[244:247],v[12:15]
TR a[168:169],v228 offset:28672
TR a[170:171],v228 offset:29696
MM v[12:15],a[184:187],a[228:231],v[12:15]
MM v[12:15],a[180:183],a[212:215],v[12:15]
TR a[172:173],v228 offset:30720
TR a[174:175],v228 offset:31744
MM v[12:15],a[176:179],a[196:199],v[12:15]
s_waitcnt lgkmcnt(15)
MM v[32:35],a[128:131],a[192:195],v[32:35]
TR a[176:177],v229 offset:24576
TR a[178:179],v229 offset:25600
MM v[32:35],a[132:135],a[208:211],v[32:35]
MM v[32:35],a[136:139],a[224:227],v[32:35]
TR a[180:181],v229 offset:26624
TR a[182:183],v229 offset:27648
MM v[32:35],a[140:143],a[240:243],v[32:35]
s_add_u32 s67, s46, 0
CV v0,v0,v1
CV v1,v2,v3
MM v[40:43],a[140:143],a[244:247],v[40:43]
TR a[184:185],v229 offset:28672
TR a[186:187],v229 offset:29696
MM v[40:43],a[136:139],a[228:231],v[40:43]
CV v2,v4,v5
CV v3,v6,v7
MM v[40:43],a[132:135],a[212:215],v[40:43]
TR a[188:189],v229 offset:30720
TR a[190:191],v229 offset:31744
MM v[40:43],a[128:131],a[196:199],v[40:43]
s_waitcnt lgkmcnt(15)
buffer_store_dwordx4 v[0:3], v249, s[20:23], s67 offen offset:0 sc1
MM v[36:39],a[144:147],a[192:195],v[36:39]
s_add_u32 s67, s47, 0
TR a[128:129],v244 offset:16384
)ASM"
R"ASM(
TR a[130:131],v244 offset:18432
MM v[36:39],a[148:151],a[208:211],v[36:39]
CV v8,v8,v9
CV v9,v10,v11
MM v[36:39],a[152:155],a[224:227],v[36:39]
TR a[132:133],v244 offset:20480
TR a[134:135],v244 offset:22528
MM v[36:39],a[156:159],a[240:243],v[36:39]
CV v10,v12,v13
CV v11,v14,v15
MM v[44:47],a[156:159],a[244:247],v[44:47]
TR a[136:137],v244 offset:24576
TR a[138:139],v244 offset:26624
MM v[44:47],a[152:155],a[228:231],v[44:47]
buffer_store_dwordx4 v[8:11], v234, s[20:23], s67 offen offset:1024 sc1
MM v[44:47],a[148:151],a[212:215],v[44:47]
TR a[140:141],v244 offset:28672
TR a[142:143],v244 offset:30720
MM v[44:47],a[144:147],a[196:199],v[44:47]
s_waitcnt lgkmcnt(15)
ds_bpermute_b32 v160, v245, v0
ds_bpermute_b32 v161, v245, v1
MM v[128:131],a[160:163],a[192:195],v[128:131]
TR a[144:145],v235 offset:16384
TR a[146:147],v235 offset:18432
MM v[128:131],a[164:167],a[208:211],v[128:131]
ds_bpermute_b32 v162, v245, v2
ds_bpermute_b32 v163, v245, v3
MM v[128:131],a[168:171],a[224:227],v[128:131]
TR a[148:149],v235 offset:20480
TR a[150:151],v235 offset:22528
MM v[128:131],a[172:175],a[240:243],v[128:131]
CV v0,v116,v117
CV v1,v118,v119
MM v[136:139],a[172:175],a[244:247],v[136:139]
s_add_u32 s67, s46, 0
TR a[152:153],v235 offset:24576
TR a[154:155],v235 offset:26624
MM v[136:139],a[168:171],a[228:231],v[136:139]
CV v32,v32,v33
CV v33,v34,v35
MM v[136:139],a[164:167],a[212:215],v[136:139]
TR a[156:157],v235 offset:28672
TR a[158:159],v235 offset:30720
MM v[136:139],a[160:163],a[196:199],v[136:139]
s_waitcnt lgkmcnt(15)
CV v34,v36,v37
CV v35,v38,v39
s_waitcnt vmcnt(3)
s_barrier
MM v[132:135],a[176:179],a[192:195],v[132:135]
TR a[160:161],v230 offset:32768
TR a[162:163],v230 offset:34816
MM v[132:135],a[180:183],a[208:211],v[132:135]
buffer_store_dwordx4 v[32:35], v249, s[20:23], s67 offen offset:2048 sc1
MM v[132:135],a[184:187],a[224:227],v[132:135]
s_add_u32 s67, s47, 0
TR a[164:165],v230 offset:36864
TR a[166:167],v230 offset:38912
v_lshrrev_b32 v243, 2, v248
MM v[132:135],a[188:191],a[240:243],v[132:135]
CV v40,v40,v41
CV v41,v42,v43
MM v[140:143],a[188:191],a[244:247],v[140:143]
TR a[168:169],v230 offset:40960
TR a[170:171],v230 offset:43008
MM v[140:143],a[184:187],a[228:231],v[140:143]
CV v42,v44,v45
CV v43,v46,v47
v_min_u32 v243, 12, v243
MM v[140:143],a[180:183],a[212:215],v[140:143]
TR a[172:173],v230 offset:45056
TR a[174:175],v230 offset:47104
MM v[140:143],a[176:179],a[196:199],v[140:143]
s_waitcnt lgkmcnt(15)
buffer_store_dwordx4 v[40:43], v234, s[20:23], s67 offen offset:3072 sc1
MM v[64:67],a[128:131],a[0:3],v[64:67]
buffer_load_dword v242, v243, s[20:23], s96 offen offset:16 sc1
TR a[176:177],v231 offset:32768
TR a[178:179],v231 offset:34816
MM v[64:67],a[132:135],a[32:35],v[64:67]
ds_bpermute_b32 v176, v245, v32
ds_bpermute_b32 v177, v245, v33
MM v[64:67],a[136:139],a[64:67],v[64:67]
TR a[180:181],v231 offset:36864
TR a[182:183],v231 offset:38912
MM v[64:67],a[140:143],a[96:99],v[64:67]
ds_bpermute_b32 v178, v245, v34
ds_bpermute_b32 v179, v245, v35
MM v[72:75],a[140:143],a[100:103],v[72:75]
TR a[184:185],v231 offset:40960
TR a[186:187],v231 offset:43008
MM v[72:75],a[136:139],a[68:71],v[72:75]
v_lshlrev_b32 v112, 16, v226
v_and_b32 v113, 0xffff0000, v226
MM v[72:75],a[132:135],a[36:39],v[72:75]
s_add_u32 s67, s46, 8192
TR a[188:189],v231 offset:45056
TR a[190:191],v231 offset:47104
MM v[72:75],a[128:131],a[4:7],v[72:75]
s_waitcnt lgkmcnt(15)
CV v128,v128,v129
CV v129,v130,v131
MM v[68:71],a[144:147],a[0:3],v[68:71]
TR a[128:129],v244 offset:32768
TR a[130:131],v244 offset:34816
LPG22_%=:
s_cbranch_execz LTR3_%=
MM v[68:71],a[148:151],a[32:35],v[68:71]
CV v130,v132,v133
CV v131,v134,v135
MM v[68:71],a[152:155],a[64:67],v[68:71]
TR a[132:133],v244 offset:36864
TR a[134:135],v244 offset:38912
MM v[68:71],a[156:159],a[96:99],v[68:71]
buffer_store_dwordx4 v[128:131], v249, s[20:23], s67 offen offset:0 sc1
MM v[76:79],a[156:159],a[100:103],v[76:79]
s_add_u32 s67, s47, 8192
TR a[136:137],v244 offset:40960
TR a[138:139],v244 offset:43008
MM v[76:79],a[152:155],a[68:71],v[76:79]
CV v136,v136,v137
CV v137,v138,v139
MM v[76:79],a[148:151],a[36:39],v[76:79]
TR a[140:141],v244 offset:45056
TR a[142:143],v244 offset:47104
MM v[76:79],a[144:147],a[4:7],v[76:79]
s_waitcnt lgkmcnt(15)
CV v138,v140,v141
CV v139,v142,v143
MM v[64:67],a[160:163],a[8:11],v[64:67]
TR a[144:145],v235 offset:32768
TR a[146:147],v235 offset:34816
MM v[64:67],a[164:167],a[40:43],v[64:67]
buffer_store_dwordx4 v[136:139], v234, s[20:23], s67 offen offset:1024 sc1
MM v[64:67],a[168:171],a[72:75],v[64:67]
TR a[148:149],v235 offset:36864
TR a[150:151],v235 offset:38912
MM v[64:67],a[172:175],a[104:107],v[64:67]
ds_bpermute_b32 v164, v245, v128
ds_bpermute_b32 v165, v245, v129
MM v[72:75],a[172:175],a[108:111],v[72:75]
TR a[152:153],v235 offset:40960
TR a[154:155],v235 offset:43008
MM v[72:75],a[168:171],a[76:79],v[72:75]
ds_bpermute_b32 v166, v245, v130
ds_bpermute_b32 v167, v245, v131
MM v[72:75],a[164:167],a[44:47],v[72:75]
TR a[156:157],v235 offset:45056
TR a[158:159],v235 offset:47104
MM v[72:75],a[160:163],a[12:15],v[72:75]
s_waitcnt lgkmcnt(15)
v_lshlrev_b32 v114, 16, v227
v_and_b32 v115, 0xffff0000, v227
MM v[68:71],a[176:179],a[8:11],v[68:71]
TR a[160:161],v244 offset:16896
TR a[162:163],v244 offset:18944
MM v[68:71],a[180:183],a[40:43],v[68:71]
v_mul_f32 v116, 0xbfb8aa3b, v112
v_mul_f32 v117, 0xbfb8aa3b, v113
MM v[68:71],a[184:187],a[72:75],v[68:71]
TR a[164:165],v244 offset:20992
TR a[166:167],v244 offset:23040
MM v[68:71],a[188:191],a[104:107],v[68:71]
v_mul_f32 v118, 0xbfb8aa3b, v114
v_mul_f32 v119, 0xbfb8aa3b, v115
MM v[76:79],a[188:191],a[108:111],v[76:79]
TR a[168:169],v244 offset:25088
TR a[170:171],v244 offset:27136
MM v[76:79],a[184:187],a[76:79],v[76:79]
EX v116,v116
MM v[76:79],a[180:183],a[44:47],v[76:79]
TR a[172:173],v244 offset:29184
TR a[174:175],v244 offset:31232
MM v[76:79],a[176:179],a[12:15],v[76:79]
s_waitcnt lgkmcnt(15)
EX v117,v117
MM v[64:67],a[128:131],a[192:195],v[64:67]
TR a[176:177],v235 offset:16896
TR a[178:179],v235 offset:18944
MM v[64:67],a[132:135],a[208:211],v[64:67]
EX v118,v118
MM v[64:67],a[136:139],a[224:227],v[64:67]
TR a[180:181],v235 offset:20992
TR a[182:183],v235 offset:23040
MM v[64:67],a[140:143],a[240:243],v[64:67]
EX v119,v119
MM v[72:75],a[140:143],a[244:247],v[72:75]
TR a[184:185],v235 offset:25088
TR a[186:187],v235 offset:27136
MM v[72:75],a[136:139],a[228:231],v[72:75]
v_add_f32 v116, 1.0, v116
v_add_f32 v117, 1.0, v117
MM v[72:75],a[132:135],a[212:215],v[72:75]
TR a[188:189],v235 offset:29184
TR a[190:191],v235 offset:31232
MM v[72:75],a[128:131],a[196:199],v[72:75]
s_waitcnt lgkmcnt(15)
v_add_f32 v118, 1.0, v118
v_add_f32 v119, 1.0, v119
MM v[68:71],a[144:147],a[192:195],v[68:71]
TR a[128:129],v230 offset:33280
TR a[130:131],v230 offset:35328
MM v[68:71],a[148:151],a[208:211],v[68:71]
v_rcp_f32 v116, v116
MM v[68:71],a[152:155],a[224:227],v[68:71]
TR a[132:133],v230 offset:37376
TR a[134:135],v230 offset:39424
MM v[68:71],a[156:159],a[240:243],v[68:71]
v_rcp_f32 v117, v117
MM v[76:79],a[156:159],a[244:247],v[76:79]
TR a[136:137],v230 offset:41472
TR a[138:139],v230 offset:43520
MM v[76:79],a[152:155],a[228:231],v[76:79]
v_rcp_f32 v118, v118
MM v[76:79],a[148:151],a[212:215],v[76:79]
TR a[140:141],v230 offset:45568
TR a[142:143],v230 offset:47616
MM v[76:79],a[144:147],a[196:199],v[76:79]
s_waitcnt lgkmcnt(15)
v_rcp_f32 v119, v119
MM v[96:99],a[160:163],a[0:3],v[96:99]
s_waitcnt vmcnt(2)
TR a[144:145],v231 offset:33280
TR a[146:147],v231 offset:35328
MM v[96:99],a[164:167],a[32:35],v[96:99]
v_cmp_ne_u32 vcc, s69, v242
s_cbranch_vccnz LRPO1_%=
LRPO1b_%=:
MM v[96:99],a[168:171],a[64:67],v[96:99]
s_xor_b32 s92, s49, 1
TR a[148:149],v231 offset:37376
TR a[150:151],v231 offset:39424
MM v[96:99],a[172:175],a[96:99],v[96:99]
s_add_u32 s67, s46, 4096
CV v64,v64,v65
CV v65,v66,v67
MM v[104:107],a[172:175],a[100:103],v[104:107]
s_mul_i32 s92, s92, 17408
TR a[152:153],v231 offset:41472
TR a[154:155],v231 offset:43520
MM v[104:107],a[168:171],a[68:71],v[104:107]
s_add_u32 s92, s83, s92
CV v66,v68,v69
CV v67,v70,v71
MM v[104:107],a[164:167],a[36:39],v[104:107]
s_add_u32 s92, s92, 8192
TR a[156:157],v231 offset:45568
TR a[158:159],v231 offset:47616
MM v[104:107],a[160:163],a[4:7],v[104:107]
s_waitcnt lgkmcnt(15)
buffer_store_dwordx4 v[64:67], v249, s[20:23], s67 offen offset:0 sc1
MM v[100:103],a[176:179],a[0:3],v[100:103]
s_add_u32 s67, s47, 4096
TR a[160:161],v244 offset:33280
TR a[162:163],v244 offset:35328
MM v[100:103],a[180:183],a[32:35],v[100:103]
s_cmp_eq_u32 s82, 0
s_cbranch_scc1 LY0_%=
v_lshrrev_b32 v135, 4, v248
v_min_u32 v135, 59, v135
v_mul_u32_u24 v132, 205, v135
v_lshrrev_b32 v132, 11, v132
v_mul_u32_u24 v132, 30, v132
v_add_lshl_u32 v135, v135, v132, 2
s_mul_i32 s78, s84, 40
s_add_u32 s78, s78, 73728
buffer_load_dword v132, v135, s[20:23], s78 offen offset:0 sc1
buffer_load_dword v133, v135, s[20:23], s78 offen offset:960 sc1
buffer_load_dword v134, v135, s[20:23], s78 offen offset:1920 sc1
LY0_%=:
buffer_load_dwordx4 a[16:19], v248, s[20:23], s92 offen offset:0 sc1
CV v72,v72,v73
CV v73,v74,v75
MM v[100:103],a[184:187],a[64:67],v[100:103]
TR a[164:165],v244 offset:37376
TR a[166:167],v244 offset:39424
MM v[100:103],a[188:191],a[96:99],v[100:103]
CV v74,v76,v77
CV v75,v78,v79
MM v[108:111],a[188:191],a[100:103],v[108:111]
TR a[168:169],v244 offset:41472
TR a[170:171],v244 offset:43520
MM v[108:111],a[184:187],a[68:71],v[108:111]
buffer_store_dwordx4 v[72:75], v234, s[20:23], s67 offen offset:1024 sc1
MM v[108:111],a[180:183],a[36:39],v[108:111]
buffer_load_dwordx4 a[48:51], v248, s[20:23], s92 offen offset:1024 sc1
TR a[172:173],v244 offset:45568
TR a[174:175],v244 offset:47616
MM v[108:111],a[176:179],a[4:7],v[108:111]
s_waitcnt lgkmcnt(15)
ds_bpermute_b32 v192, v245, v64
ds_bpermute_b32 v193, v245, v65
MM v[96:99],a[128:131],a[8:11],v[96:99]
TR a[176:177],v235 offset:33280
TR a[178:179],v235 offset:35328
MM v[96:99],a[132:135],a[40:43],v[96:99]
ds_bpermute_b32 v194, v245, v66
ds_bpermute_b32 v195, v245, v67
MM v[96:99],a[136:139],a[72:75],v[96:99]
TR a[180:181],v235 offset:37376
TR a[182:183],v235 offset:39424
MM v[96:99],a[140:143],a[104:107],v[96:99]
buffer_load_dwordx4 a[80:83], v248, s[20:23], s92 offen offset:2048 sc1
MM v[104:107],a[140:143],a[108:111],v[104:107]
TR a[184:185],v235 offset:41472
TR a[186:187],v235 offset:43520
MM v[104:107],a[136:139],a[76:79],v[104:107]
buffer_load_dwordx4 a[112:115], v248, s[20:23], s92 offen offset:3072 sc1
MM v[104:107],a[132:135],a[44:47],v[104:107]
s_add_u32 s92, s92, 4096
TR a[188:189],v235 offset:45568
TR a[190:191],v235 offset:47616
MM v[104:107],a[128:131],a[12:15],v[104:107]
s_waitcnt lgkmcnt(15)
buffer_load_dwordx4 a[20:23], v248, s[20:23], s92 offen offset:0 sc1
MM v[100:103],a[144:147],a[8:11],v[100:103]
TR a[128:129],v230 offset:49152
TR a[130:131],v230 offset:51200
MM v[100:103],a[148:151],a[40:43],v[100:103]
v_mov_b32 v252, s69
s_mov_b64 exec, 1
buffer_store_dword v252, off, s[20:23], s19 offset:0 sc1
s_mov_b64 exec, -1
MM v[100:103],a[152:155],a[72:75],v[100:103]
TR a[132:133],v230 offset:53248
TR a[134:135],v230 offset:55296
MM v[100:103],a[156:159],a[104:107],v[100:103]
buffer_load_dwordx4 a[52:55], v248, s[20:23], s92 offen offset:1024 sc1
MM v[108:111],a[156:159],a[108:111],v[108:111]
TR a[136:137],v230 offset:57344
TR a[138:139],v230 offset:59392
MM v[108:111],a[152:155],a[76:79],v[108:111]
buffer_load_dwordx4 a[84:87], v248, s[20:23], s92 offen offset:2048 sc1
MM v[108:111],a[148:151],a[44:47],v[108:111]
TR a[140:141],v230 offset:61440
TR a[142:143],v230 offset:63488
MM v[108:111],a[144:147],a[12:15],v[108:111]
s_waitcnt lgkmcnt(15)
v_mul_f32 v116, v112, v116
v_mul_f32 v117, v113, v117
MM v[96:99],a[160:163],a[192:195],v[96:99]
TR a[144:145],v231 offset:49152
TR a[146:147],v231 offset:51200
MM v[96:99],a[164:167],a[208:211],v[96:99]
buffer_load_dwordx4 a[116:119], v248, s[20:23], s92 offen offset:3072 sc1
MM v[96:99],a[168:171],a[224:227],v[96:99]
TR a[148:149],v231 offset:53248
TR a[150:151],v231 offset:55296
)ASM"
R"ASM(
MM v[96:99],a[172:175],a[240:243],v[96:99]
v_mul_f32 v118, v114, v118
v_mul_f32 v119, v115, v119
MM v[104:107],a[172:175],a[244:247],v[104:107]
TR a[152:153],v231 offset:57344
TR a[154:155],v231 offset:59392
MM v[104:107],a[168:171],a[228:231],v[104:107]
CV v2,v116,v117
CV v3,v118,v119
MM v[104:107],a[164:167],a[212:215],v[104:107]
TR a[156:157],v231 offset:61440
TR a[158:159],v231 offset:63488
MM v[104:107],a[160:163],a[196:199],v[104:107]
s_waitcnt lgkmcnt(15)
v_lshlrev_b32 v10, 16, v236
v_and_b32 v11, 0xffff0000, v236
MM v[100:103],a[176:179],a[192:195],v[100:103]
TR a[160:161],v230 offset:49664
TR a[162:163],v230 offset:51712
MM v[100:103],a[180:183],a[208:211],v[100:103]
v_lshlrev_b32 v12, 16, v237
v_and_b32 v13, 0xffff0000, v237
MM v[100:103],a[184:187],a[224:227],v[100:103]
TR a[164:165],v230 offset:53760
TR a[166:167],v230 offset:55808
MM v[100:103],a[188:191],a[240:243],v[100:103]
v_mul_f32 v14, 0xbfb8aa3b, v10
v_mul_f32 v15, 0xbfb8aa3b, v11
MM v[108:111],a[188:191],a[244:247],v[108:111]
TR a[168:169],v230 offset:57856
TR a[170:171],v230 offset:59904
MM v[108:111],a[184:187],a[228:231],v[108:111]
v_mul_f32 v42, 0xbfb8aa3b, v12
v_mul_f32 v43, 0xbfb8aa3b, v13
MM v[108:111],a[180:183],a[212:215],v[108:111]
TR a[172:173],v230 offset:61952
TR a[174:175],v230 offset:64000
MM v[108:111],a[176:179],a[196:199],v[108:111]
s_waitcnt lgkmcnt(15)
EX v14,v14
MM v[16:19],a[128:131],v[168:171],0
TR a[176:177],v231 offset:49664
TR a[178:179],v231 offset:51712
MM v[16:19],a[132:135],v[184:187],v[16:19]
AW a3,-1
AW a7,-1
MM v[16:19],a[136:139],v[200:203],v[16:19]
TR a[180:181],v231 offset:53760
TR a[182:183],v231 offset:55808
MM v[16:19],a[140:143],v[216:219],v[16:19]
s_add_u32 s67, s46, 4096
CV v96,v96,v97
CV v97,v98,v99
AW a11,-1
MM v[24:27],a[140:143],v[220:223],0
TR a[184:185],v231 offset:57856
TR a[186:187],v231 offset:59904
MM v[24:27],a[136:139],v[204:207],v[24:27]
CV v98,v100,v101
CV v99,v102,v103
MM v[24:27],a[132:135],v[188:191],v[24:27]
TR a[188:189],v231 offset:61952
TR a[190:191],v231 offset:64000
AW a15,-1
MM v[24:27],a[128:131],v[172:175],v[24:27]
s_waitcnt lgkmcnt(15)
buffer_store_dwordx4 v[96:99], v249, s[20:23], s67 offen offset:2048 sc1
MM v[20:23],a[144:147],v[168:171],0
s_add_u32 s67, s47, 4096
TR a[128:129],v230 offset:16384
TR a[130:131],v230 offset:18432
MM v[20:23],a[148:151],v[184:187],v[20:23]
CV v104,v104,v105
CV v105,v106,v107
AW a35,-1
MM v[20:23],a[152:155],v[200:203],v[20:23]
TR a[132:133],v230 offset:20480
TR a[134:135],v230 offset:22528
MM v[20:23],a[156:159],v[216:219],v[20:23]
CV v106,v108,v109
CV v107,v110,v111
s_mov_b64 exec, 0xffff
AW a0,v160
AW a1,v161
AW a2,v162
AW a3,v163
s_mov_b64 exec, -1
MM v[28:31],a[156:159],v[220:223],0
TR a[136:137],v230 offset:24576
TR a[138:139],v230 offset:26624
AW a39,-1
MM v[28:31],a[152:155],v[204:207],v[28:31]
buffer_store_dwordx4 v[104:107], v234, s[20:23], s67 offen offset:3072 sc1
MM v[28:31],a[148:151],v[188:191],v[28:31]
TR a[140:141],v230 offset:28672
TR a[142:143],v230 offset:30720
s_mov_b64 exec, 0xffff
AW a8,v176
AW a9,v177
AW a10,v178
AW a11,v179
s_mov_b64 exec, -1
MM v[28:31],a[144:147],v[172:175],v[28:31]
s_waitcnt lgkmcnt(15)
ds_bpermute_b32 v208, v245, v96
ds_bpermute_b32 v209, v245, v97
AW a43,-1
MM v[48:51],a[160:163],v[168:171],0
TR a[144:145],v231 offset:16384
TR a[146:147],v231 offset:18432
MM v[48:51],a[164:167],v[184:187],v[48:51]
ds_bpermute_b32 v210, v245, v98
ds_bpermute_b32 v211, v245, v99
MM v[48:51],a[168:171],v[200:203],v[48:51]
TR a[148:149],v231 offset:20480
TR a[150:151],v231 offset:22528
AW a47,-1
MM v[48:51],a[172:175],v[216:219],v[48:51]
buffer_load_dword v240, v253, s[20:23], s68 offen offset:0 sc1
buffer_load_dword v140, v253, s[20:23], s45 offen offset:0 sc1
MM v[56:59],a[172:175],v[220:223],0
TR a[152:153],v231 offset:24576
TR a[154:155],v231 offset:26624
MM v[56:59],a[168:171],v[204:207],v[56:59]
AW a71,-1
AW a79,-1
MM v[56:59],a[164:167],v[188:191],v[56:59]
TR a[156:157],v231 offset:28672
TR a[158:159],v231 offset:30720
MM v[56:59],a[160:163],v[172:175],v[56:59]
s_waitcnt lgkmcnt(15)
AW a103,-1
AW a111,-1
MM v[52:55],a[176:179],v[168:171],0
TR a[160:161],v230 offset:16896
TR a[162:163],v230 offset:18944
MM v[52:55],a[180:183],v[184:187],v[52:55]
AW a195,-1
AW a199,-1
MM v[52:55],a[184:187],v[200:203],v[52:55]
TR a[164:165],v230 offset:20992
TR a[166:167],v230 offset:23040
MM v[52:55],a[188:191],v[216:219],v[52:55]
AW a215,-1
MM v[60:63],a[188:191],v[220:223],0
TR a[168:169],v230 offset:25088
TR a[170:171],v230 offset:27136
MM v[60:63],a[184:187],v[204:207],v[60:63]
s_mov_b64 exec, 0xffff
AW a32,v192
AW a33,v193
AW a34,v194
AW a35,v195
s_mov_b64 exec, -1
MM v[60:63],a[180:183],v[188:191],v[60:63]
s_waitcnt lgkmcnt(12)
TR a[172:173],v230 offset:29184
TR a[174:175],v230 offset:31232
MM v[60:63],a[176:179],v[172:175],v[60:63]
s_mov_b64 exec, 0xffff
AW a40,v208
AW a41,v209
AW a42,v210
AW a43,v211
s_mov_b64 exec, -1
MM v[80:83],a[128:131],v[168:171],0
TR a[176:177],v231 offset:16896
TR a[178:179],v231 offset:18944
MM v[80:83],a[132:135],v[184:187],v[80:83]
s_mov_b64 exec, 0xffff
AW a192,v164
AW a193,v165
AW a194,v166
AW a195,v167
s_mov_b64 exec, -1
MM v[80:83],a[136:139],v[200:203],v[80:83]
TR a[180:181],v231 offset:20992
TR a[182:183],v231 offset:23040
MM v[80:83],a[140:143],v[216:219],v[80:83]
v_lshrrev_b32 v104, 8, v248
v_min_u32 v104, 1, v104
MM v[88:91],a[140:143],v[220:223],0
TR a[184:185],v231 offset:25088
TR a[186:187],v231 offset:27136
MM v[88:91],a[136:139],v[204:207],v[88:91]
EX v15,v15
v_sub_u32 v104, 0, v104
MM v[88:91],a[132:135],v[188:191],v[88:91]
TR a[188:189],v231 offset:29184
TR a[190:191],v231 offset:31232
MM v[88:91],a[128:131],v[172:175],v[88:91]
s_waitcnt lgkmcnt(15)
EX v42,v42
MM v[84:87],a[144:147],v[168:171],0
TR a[128:129],v228 offset:0
TR a[130:131],v228 offset:1024
MM v[84:87],a[148:151],v[184:187],v[84:87]
EX v43,v43
MM v[84:87],a[152:155],v[200:203],v[84:87]
TR a[132:133],v228 offset:2048
TR a[134:135],v228 offset:3072
MM v[84:87],a[156:159],v[216:219],v[84:87]
v_add_f32 v14, 1.0, v14
v_add_f32 v15, 1.0, v15
MM v[92:95],a[156:159],v[220:223],0
TR a[136:137],v228 offset:4096
TR a[138:139],v228 offset:5120
MM v[92:95],a[152:155],v[204:207],v[92:95]
v_add_f32 v42, 1.0, v42
v_add_f32 v43, 1.0, v43
MM v[92:95],a[148:151],v[188:191],v[92:95]
s_xor_b32 s92, s49, 2
TR a[140:141],v228 offset:6144
TR a[142:143],v228 offset:7168
MM v[92:95],a[144:147],v[172:175],v[92:95]
s_waitcnt lgkmcnt(15)
s_mul_i32 s92, s92, 17408
s_add_u32 s92, s83, s92
MM v[112:115],a[160:163],v[168:171],0
s_add_u32 s92, s92, 8192
TR a[144:145],v229 offset:0
TR a[146:147],v229 offset:1024
MM v[112:115],a[164:167],v[184:187],v[112:115]
buffer_load_dwordx4 a[24:27], v248, s[20:23], s92 offen offset:0 sc1
MM v[112:115],a[168:171],v[200:203],v[112:115]
TR a[148:149],v229 offset:2048
TR a[150:151],v229 offset:3072
MM v[112:115],a[172:175],v[216:219],v[112:115]
buffer_load_dwordx4 a[56:59], v248, s[20:23], s92 offen offset:1024 sc1
MM v[120:123],a[172:175],v[220:223],0
TR a[152:153],v229 offset:4096
TR a[154:155],v229 offset:5120
MM v[120:123],a[168:171],v[204:207],v[120:123]
buffer_load_dwordx4 a[88:91], v248, s[20:23], s92 offen offset:2048 sc1
MM v[120:123],a[164:167],v[188:191],v[120:123]
TR a[156:157],v229 offset:6144
TR a[158:159],v229 offset:7168
MM v[120:123],a[160:163],v[172:175],v[120:123]
s_waitcnt lgkmcnt(15)
v_rcp_f32 v14, v14
MM v[116:119],a[176:179],v[168:171],0
TR a[160:161],v244 offset:49152
TR a[162:163],v244 offset:51200
MM v[116:119],a[180:183],v[184:187],v[116:119]
buffer_load_dwordx4 a[120:123], v248, s[20:23], s92 offen offset:3072 sc1
MM v[116:119],a[184:187],v[200:203],v[116:119]
s_add_u32 s92, s92, 4096
TR a[164:165],v244 offset:53248
TR a[166:167],v244 offset:55296
MM v[116:119],a[188:191],v[216:219],v[116:119]
buffer_load_dwordx4 a[28:31], v248, s[20:23], s92 offen offset:0 sc1
MM v[124:127],a[188:191],v[220:223],0
TR a[168:169],v244 offset:57344
TR a[170:171],v244 offset:59392
MM v[124:127],a[184:187],v[204:207],v[124:127]
v_rcp_f32 v15, v15
MM v[124:127],a[180:183],v[188:191],v[124:127]
TR a[172:173],v244 offset:61440
TR a[174:175],v244 offset:63488
MM v[124:127],a[176:179],v[172:175],v[124:127]
s_waitcnt lgkmcnt(15)
buffer_load_dwordx4 a[60:63], v248, s[20:23], s92 offen offset:1024 sc1
MM v[144:147],a[128:131],v[168:171],0
TR a[176:177],v235 offset:49152
TR a[178:179],v235 offset:51200
MM v[144:147],a[132:135],v[184:187],v[144:147]
buffer_load_dwordx4 a[92:95], v248, s[20:23], s92 offen offset:2048 sc1
MM v[144:147],a[136:139],v[200:203],v[144:147]
TR a[180:181],v235 offset:53248
TR a[182:183],v235 offset:55296
MM v[144:147],a[140:143],v[216:219],v[144:147]
v_rcp_f32 v42, v42
MM v[152:155],a[140:143],v[220:223],0
TR a[184:185],v235 offset:57344
TR a[186:187],v235 offset:59392
MM v[152:155],a[136:139],v[204:207],v[152:155]
buffer_load_dwordx4 a[124:127], v248, s[20:23], s92 offen offset:3072 sc1
MM v[152:155],a[132:135],v[188:191],v[152:155]
s_xor_b32 s92, s49, 3
TR a[188:189],v235 offset:61440
TR a[190:191],v235 offset:63488
MM v[152:155],a[128:131],v[172:175],v[152:155]
s_waitcnt lgkmcnt(15)
s_mul_i32 s92, s92, 17408
s_add_u32 s92, s83, s92
MM v[148:151],a[144:147],v[168:171],0
s_add_u32 s92, s92, 8192
TR a[128:129],v244 offset:49664
TR a[130:131],v244 offset:51712
MM v[148:151],a[148:151],v[184:187],v[148:151]
buffer_load_dwordx4 a[200:203], v248, s[20:23], s92 offen offset:0 sc1
MM v[148:151],a[152:155],v[200:203],v[148:151]
TR a[132:133],v244 offset:53760
TR a[134:135],v244 offset:55808
MM v[148:151],a[156:159],v[216:219],v[148:151]
buffer_load_dwordx4 a[216:219], v248, s[20:23], s92 offen offset:1024 sc1
MM v[156:159],a[156:159],v[220:223],0
TR a[136:137],v244 offset:57856
TR a[138:139],v244 offset:59904
MM v[156:159],a[152:155],v[204:207],v[156:159]
buffer_load_dwordx4 a[232:235], v248, s[20:23], s92 offen offset:2048 sc1
MM v[156:159],a[148:151],v[188:191],v[156:159]
TR a[140:141],v244 offset:61952
TR a[142:143],v244 offset:64000
MM v[156:159],a[144:147],v[172:175],v[156:159]
s_waitcnt lgkmcnt(15)
s_waitcnt vmcnt(11)
v_cmp_eq_u32 s[36:37], s69, v240
MM v[16:19],a[160:163],a[16:19],v[16:19]
TR a[144:145],v235 offset:49664
TR a[146:147],v235 offset:51712
MM v[16:19],a[164:167],a[48:51],v[16:19]
LPG23_%=:
s_cbranch_execz LTR3_%=
buffer_load_dwordx4 a[248:251], v248, s[20:23], s92 offen offset:3072 sc1
v_cmp_eq_u32 s[38:39], s69, v140
MM v[16:19],a[168:171],a[80:83],v[16:19]
s_add_u32 s92, s92, 4096
TR a[148:149],v235 offset:53760
TR a[150:151],v235 offset:55808
MM v[16:19],a[172:175],a[112:115],v[16:19]
s_nop 0
buffer_load_dwordx4 a[204:207], v248, s[20:23], s92 offen offset:0 sc1
MM v[24:27],a[172:175],a[116:119],v[24:27]
s_and_b32 s36, s36, 0xffff0000
TR a[152:153],v235 offset:57856
TR a[154:155],v235 offset:59904
MM v[24:27],a[168:171],a[84:87],v[24:27]
v_rcp_f32 v43, v43
MM v[24:27],a[164:167],a[52:55],v[24:27]
TR a[156:157],v235 offset:61952
TR a[158:159],v235 offset:64000
MM v[24:27],a[160:163],a[20:23],v[24:27]
s_waitcnt lgkmcnt(15)
buffer_load_dwordx4 a[220:223], v248, s[20:23], s92 offen offset:1024 sc1
MM v[20:23],a[176:179],a[16:19],v[20:23]
TR a[160:161],v244 offset:16384
TR a[162:163],v244 offset:18432
MM v[20:23],a[180:183],a[48:51],v[20:23]
buffer_load_dwordx4 a[236:239], v248, s[20:23], s92 offen offset:2048 sc1
MM v[20:23],a[184:187],a[80:83],v[20:23]
v_mov_b32 v252, s69
s_mov_b64 exec, 1
buffer_store_dword v252, off, s[20:23], s19 offset:4 sc1
s_mov_b64 exec, -1
TR a[164:165],v244 offset:20480
TR a[166:167],v244 offset:22528
MM v[20:23],a[188:191],a[112:115],v[20:23]
v_mul_f32 v14, v10, v14
v_mul_f32 v15, v11, v15
MM v[28:31],a[188:191],a[116:119],v[28:31]
TR a[168:169],v244 offset:24576
TR a[170:171],v244 offset:26624
MM v[28:31],a[184:187],a[84:87],v[28:31]
buffer_load_dwordx4 a[252:255], v248, s[20:23], s92 offen offset:3072 sc1
MM v[28:31],a[180:183],a[52:55],v[28:31]
TR a[172:173],v244 offset:28672
TR a[174:175],v244 offset:30720
MM v[28:31],a[176:179],a[20:23],v[28:31]
s_waitcnt lgkmcnt(15)
s_add_u32 s66, s16, 0
v_mul_f32 v42, v12, v42
)ASM"
R"ASM(
MM v[48:51],a[128:131],a[16:19],v[48:51]
TR a[176:177],v235 offset:16384
TR a[178:179],v235 offset:18432
MM v[48:51],a[132:135],a[48:51],v[48:51]
s_mov_b64 exec, s[36:37]
buffer_load_dwordx4 a[0:3], v250, s[20:23], s66 offen offset:0 sc1
s_mov_b64 exec, -1
MM v[48:51],a[136:139],a[80:83],v[48:51]
s_add_u32 s66, s44, 0
TR a[180:181],v235 offset:20480
TR a[182:183],v235 offset:22528
MM v[48:51],a[140:143],a[112:115],v[48:51]
s_mov_b64 exec, s[38:39]
buffer_load_dwordx4 a[4:7], v250, s[20:23], s66 offen offset:0 sc1
s_mov_b64 exec, -1
MM v[56:59],a[140:143],a[116:119],v[56:59]
s_add_u32 s66, s16, 2048
TR a[184:185],v235 offset:24576
TR a[186:187],v235 offset:26624
MM v[56:59],a[136:139],a[84:87],v[56:59]
s_mov_b64 exec, s[36:37]
buffer_load_dwordx4 a[8:11], v250, s[20:23], s66 offen offset:0 sc1
s_mov_b64 exec, -1
MM v[56:59],a[132:135],a[52:55],v[56:59]
s_add_u32 s66, s44, 2048
TR a[188:189],v235 offset:28672
TR a[190:191],v235 offset:30720
MM v[56:59],a[128:131],a[20:23],v[56:59]
s_waitcnt lgkmcnt(15)
v_mul_f32 v43, v13, v43
CV v4,v14,v15
MM v[52:55],a[144:147],a[16:19],v[52:55]
TR a[128:129],v244 offset:16896
TR a[130:131],v244 offset:18944
MM v[52:55],a[148:151],a[48:51],v[52:55]
s_mov_b64 exec, s[38:39]
buffer_load_dwordx4 a[12:15], v250, s[20:23], s66 offen offset:0 sc1
s_mov_b64 exec, -1
MM v[52:55],a[152:155],a[80:83],v[52:55]
TR a[132:133],v244 offset:20992
TR a[134:135],v244 offset:23040
MM v[52:55],a[156:159],a[112:115],v[52:55]
CV v5,v42,v43
v_lshlrev_b32 v10, 16, v238
MM v[60:63],a[156:159],a[116:119],v[60:63]
TR a[136:137],v244 offset:25088
TR a[138:139],v244 offset:27136
MM v[60:63],a[152:155],a[84:87],v[60:63]
v_and_b32 v11, 0xffff0000, v238
v_lshlrev_b32 v12, 16, v239
MM v[60:63],a[148:151],a[52:55],v[60:63]
TR a[140:141],v244 offset:29184
TR a[142:143],v244 offset:31232
MM v[60:63],a[144:147],a[20:23],v[60:63]
s_waitcnt lgkmcnt(15)
v_and_b32 v13, 0xffff0000, v239
v_mul_f32 v14, 0xbfb8aa3b, v10
MM v[80:83],a[160:163],a[16:19],v[80:83]
TR a[144:145],v235 offset:16896
TR a[146:147],v235 offset:18944
MM v[80:83],a[164:167],a[48:51],v[80:83]
v_mul_f32 v15, 0xbfb8aa3b, v11
v_mul_f32 v42, 0xbfb8aa3b, v12
MM v[80:83],a[168:171],a[80:83],v[80:83]
TR a[148:149],v235 offset:20992
TR a[150:151],v235 offset:23040
MM v[80:83],a[172:175],a[112:115],v[80:83]
v_mul_f32 v43, 0xbfb8aa3b, v13
MM v[88:91],a[172:175],a[116:119],v[88:91]
TR a[152:153],v235 offset:25088
TR a[154:155],v235 offset:27136
MM v[88:91],a[168:171],a[84:87],v[88:91]
EX v14,v14
MM v[88:91],a[164:167],a[52:55],v[88:91]
TR a[156:157],v235 offset:29184
TR a[158:159],v235 offset:31232
MM v[88:91],a[160:163],a[20:23],v[88:91]
s_waitcnt lgkmcnt(15)
EX v15,v15
MM v[84:87],a[176:179],a[16:19],v[84:87]
TR a[160:161],v228 offset:8192
TR a[162:163],v228 offset:9216
MM v[84:87],a[180:183],a[48:51],v[84:87]
EX v42,v42
MM v[84:87],a[184:187],a[80:83],v[84:87]
TR a[164:165],v228 offset:10240
TR a[166:167],v228 offset:11264
MM v[84:87],a[188:191],a[112:115],v[84:87]
EX v43,v43
MM v[92:95],a[188:191],a[116:119],v[92:95]
TR a[168:169],v228 offset:12288
TR a[170:171],v228 offset:13312
MM v[92:95],a[184:187],a[84:87],v[92:95]
v_add_f32 v14, 1.0, v14
v_add_f32 v15, 1.0, v15
MM v[92:95],a[180:183],a[52:55],v[92:95]
TR a[172:173],v228 offset:14336
TR a[174:175],v228 offset:15360
MM v[92:95],a[176:179],a[20:23],v[92:95]
s_waitcnt lgkmcnt(15)
v_add_f32 v42, 1.0, v42
v_add_f32 v43, 1.0, v43
MM v[112:115],a[128:131],a[16:19],v[112:115]
TR a[176:177],v229 offset:8192
TR a[178:179],v229 offset:9216
MM v[112:115],a[132:135],a[48:51],v[112:115]
v_rcp_f32 v14, v14
MM v[112:115],a[136:139],a[80:83],v[112:115]
TR a[180:181],v229 offset:10240
TR a[182:183],v229 offset:11264
MM v[112:115],a[140:143],a[112:115],v[112:115]
buffer_load_dword v241, v253, s[20:23], s68 offen offset:4 sc1
buffer_load_dword v141, v253, s[20:23], s45 offen offset:4 sc1
MM v[120:123],a[140:143],a[116:119],v[120:123]
TR a[184:185],v229 offset:12288
TR a[186:187],v229 offset:13312
MM v[120:123],a[136:139],a[84:87],v[120:123]
v_rcp_f32 v15, v15
MM v[120:123],a[132:135],a[52:55],v[120:123]
TR a[188:189],v229 offset:14336
TR a[190:191],v229 offset:15360
MM v[120:123],a[128:131],a[20:23],v[120:123]
s_waitcnt lgkmcnt(15)
v_rcp_f32 v42, v42
MM v[116:119],a[144:147],a[16:19],v[116:119]
TR a[128:129],v230 offset:0
TR a[130:131],v230 offset:2048
MM v[116:119],a[148:151],a[48:51],v[116:119]
v_rcp_f32 v43, v43
MM v[116:119],a[152:155],a[80:83],v[116:119]
TR a[132:133],v230 offset:4096
TR a[134:135],v230 offset:6144
MM v[116:119],a[156:159],a[112:115],v[116:119]
v_mul_f32 v14, v10, v14
v_mul_f32 v15, v11, v15
MM v[124:127],a[156:159],a[116:119],v[124:127]
TR a[136:137],v230 offset:8192
TR a[138:139],v230 offset:10240
MM v[124:127],a[152:155],a[84:87],v[124:127]
v_mul_f32 v42, v12, v42
v_mul_f32 v43, v13, v43
MM v[124:127],a[148:151],a[52:55],v[124:127]
TR a[140:141],v230 offset:12288
TR a[142:143],v230 offset:14336
MM v[124:127],a[144:147],a[20:23],v[124:127]
s_waitcnt lgkmcnt(15)
CV v6,v14,v15
CV v7,v42,v43
MM v[144:147],a[160:163],a[16:19],v[144:147]
s_add_u32 s80, s18, 0
TR a[144:145],v244 offset:0
TR a[146:147],v244 offset:2048
MM v[144:147],a[164:167],a[48:51],v[144:147]
buffer_load_dwordx2 v[32:33], v251, s[32:35], s80 offen offset:0
MM v[144:147],a[168:171],a[80:83],v[144:147]
s_add_u32 s80, s18, 128
TR a[148:149],v244 offset:4096
TR a[150:151],v244 offset:6144
MM v[144:147],a[172:175],a[112:115],v[144:147]
buffer_load_dwordx2 v[34:35], v251, s[32:35], s80 offen offset:0
MM v[152:155],a[172:175],a[116:119],v[152:155]
s_add_u32 s80, s18, 40960
TR a[152:153],v244 offset:8192
TR a[154:155],v244 offset:10240
MM v[152:155],a[168:171],a[84:87],v[152:155]
buffer_load_dwordx2 v[36:37], v251, s[32:35], s80 offen offset:0
MM v[152:155],a[164:167],a[52:55],v[152:155]
s_add_u32 s80, s18, 41088
TR a[156:157],v244 offset:12288
TR a[158:159],v244 offset:14336
MM v[152:155],a[160:163],a[20:23],v[152:155]
s_waitcnt lgkmcnt(15)
buffer_load_dwordx2 v[38:39], v251, s[32:35], s80 offen offset:0
MM v[148:151],a[176:179],a[16:19],v[148:151]
s_add_u32 s80, s63, 0
TR a[160:161],v231 offset:0
TR a[162:163],v231 offset:2048
MM v[148:151],a[180:183],a[48:51],v[148:151]
buffer_load_dwordx2 v[40:41], v254, s[32:35], s80 offen offset:0
MM v[148:151],a[184:187],a[80:83],v[148:151]
TR a[164:165],v231 offset:4096
TR a[166:167],v231 offset:6144
MM v[148:151],a[188:191],a[112:115],v[148:151]
v_mov_b32 v77, 0x3f80
v_and_b32 v76, 16, v248
MM v[156:159],a[188:191],a[116:119],v[156:159]
TR a[168:169],v231 offset:8192
TR a[170:171],v231 offset:10240
MM v[156:159],a[184:187],a[84:87],v[156:159]
v_lshlrev_b32 v76, v76, v77
s_mov_b32 s28, 0x30003
MM v[156:159],a[180:183],a[52:55],v[156:159]
s_mov_b32 s29, s28
TR a[172:173],v231 offset:12288
TR a[174:175],v231 offset:14336
MM v[156:159],a[176:179],a[20:23],v[156:159]
s_waitcnt lgkmcnt(15)
v_cndmask_b32 v64, 0, v76, s[28:29]
s_lshl_b64 s[28:29], s[28:29], 2
s_waitcnt vmcnt(24)
MM v[16:19],a[128:131],a[24:27],v[16:19]
TR a[176:177],v235 offset:0
TR a[178:179],v235 offset:2048
MM v[16:19],a[132:135],a[56:59],v[16:19]
v_cndmask_b32 v65, 0, v76, s[28:29]
s_lshl_b64 s[28:29], s[28:29], 2
MM v[16:19],a[136:139],a[88:91],v[16:19]
TR a[180:181],v235 offset:4096
TR a[182:183],v235 offset:6144
MM v[16:19],a[140:143],a[120:123],v[16:19]
s_waitcnt vmcnt(20)
v_cndmask_b32 v66, 0, v76, s[28:29]
s_lshl_b64 s[28:29], s[28:29], 2
MM v[24:27],a[140:143],a[124:127],v[24:27]
TR a[184:185],v235 offset:8192
TR a[186:187],v235 offset:10240
MM v[24:27],a[136:139],a[92:95],v[24:27]
v_cndmask_b32 v67, 0, v76, s[28:29]
s_lshl_b64 s[28:29], s[28:29], 2
MM v[24:27],a[132:135],a[60:63],v[24:27]
TR a[188:189],v235 offset:12288
TR a[190:191],v235 offset:14336
MM v[24:27],a[128:131],a[28:31],v[24:27]
s_waitcnt lgkmcnt(15)
v_cndmask_b32 v68, 0, v76, s[28:29]
s_lshl_b64 s[28:29], s[28:29], 2
s_waitcnt vmcnt(16)
MM v[16:19],a[144:147],a[200:203],v[16:19]
TR a[128:129],v230 offset:512
TR a[130:131],v230 offset:2560
MM v[16:19],a[148:151],a[216:219],v[16:19]
v_cndmask_b32 v69, 0, v76, s[28:29]
s_lshl_b64 s[28:29], s[28:29], 2
MM v[16:19],a[152:155],a[232:235],v[16:19]
TR a[132:133],v230 offset:4608
TR a[134:135],v230 offset:6656
MM v[16:19],a[156:159],a[248:251],v[16:19]
s_waitcnt vmcnt(11)
v_cndmask_b32 v70, 0, v76, s[28:29]
s_lshl_b64 s[28:29], s[28:29], 2
MM v[24:27],a[156:159],a[252:255],v[24:27]
TR a[136:137],v230 offset:8704
TR a[138:139],v230 offset:10752
MM v[24:27],a[152:155],a[236:239],v[24:27]
v_cndmask_b32 v71, 0, v76, s[28:29]
MM v[24:27],a[148:151],a[220:223],v[24:27]
TR a[140:141],v230 offset:12800
TR a[142:143],v230 offset:14848
MM v[24:27],a[144:147],a[204:207],v[24:27]
s_waitcnt lgkmcnt(15)
MM v[20:23],a[160:163],a[24:27],v[20:23]
TR a[144:145],v244 offset:512
TR a[146:147],v244 offset:2560
MM v[20:23],a[164:167],a[56:59],v[20:23]
MM v[20:23],a[168:171],a[88:91],v[20:23]
TR a[148:149],v244 offset:4608
TR a[150:151],v244 offset:6656
MM v[20:23],a[172:175],a[120:123],v[20:23]
MM v[28:31],a[172:175],a[124:127],v[28:31]
TR a[152:153],v244 offset:8704
TR a[154:155],v244 offset:10752
MM v[28:31],a[168:171],a[92:95],v[28:31]
MM v[28:31],a[164:167],a[60:63],v[28:31]
TR a[156:157],v244 offset:12800
TR a[158:159],v244 offset:14848
MM v[28:31],a[160:163],a[28:31],v[28:31]
s_waitcnt lgkmcnt(15)
MM v[28:31],a[188:191],a[252:255],v[28:31]
TR a[160:161],v231 offset:512
TR a[162:163],v231 offset:2560
MM v[28:31],a[184:187],a[236:239],v[28:31]
MM v[28:31],a[180:183],a[220:223],v[28:31]
TR a[164:165],v231 offset:4608
TR a[166:167],v231 offset:6656
MM v[28:31],a[176:179],a[204:207],v[28:31]
MM v[20:23],a[176:179],a[200:203],v[20:23]
TR a[168:169],v231 offset:8704
TR a[170:171],v231 offset:10752
MM v[20:23],a[180:183],a[216:219],v[20:23]
MM v[20:23],a[184:187],a[232:235],v[20:23]
TR a[172:173],v231 offset:12800
TR a[174:175],v231 offset:14848
MM v[20:23],a[188:191],a[248:251],v[20:23]
s_waitcnt lgkmcnt(15)
s_waitcnt vmcnt(5)
v_cmp_eq_u32 s[8:9], s69, v241
MM v[48:51],a[128:131],a[24:27],v[48:51]
TR a[176:177],v235 offset:512
TR a[178:179],v235 offset:2560
MM v[48:51],a[132:135],a[56:59],v[48:51]
v_cmp_eq_u32 s[10:11], s69, v141
MM v[48:51],a[136:139],a[88:91],v[48:51]
TR a[180:181],v235 offset:4608
TR a[182:183],v235 offset:6656
MM v[48:51],a[140:143],a[120:123],v[48:51]
s_add_u32 s67, s46, 8192
CV v16,v16,v17
CV v17,v18,v19
MM v[56:59],a[140:143],a[124:127],v[56:59]
s_nop 0
TR a[184:185],v235 offset:8704
TR a[186:187],v235 offset:10752
MM v[56:59],a[136:139],a[92:95],v[56:59]
s_and_b32 s8, s8, 0xffff0000
CV v18,v20,v21
CV v19,v22,v23
MM v[56:59],a[132:135],a[60:63],v[56:59]
s_add_u32 s66, s16, 4096
TR a[188:189],v235 offset:12800
TR a[190:191],v235 offset:14848
MM v[56:59],a[128:131],a[28:31],v[56:59]
s_waitcnt lgkmcnt(15)
buffer_store_dwordx4 v[16:19], v249, s[20:23], s67 offen offset:2048 sc1
MM v[56:59],a[156:159],a[252:255],v[56:59]
s_add_u32 s67, s47, 8192
TR a[128:129],v230 offset:32768
TR a[130:131],v230 offset:34816
MM v[56:59],a[152:155],a[236:239],v[56:59]
s_mov_b64 exec, s[8:9]
buffer_load_dwordx4 a[32:35], v250, s[20:23], s66 offen offset:0 sc1
s_mov_b64 exec, -1
CV v24,v24,v25
CV v25,v26,v27
MM v[56:59],a[148:151],a[220:223],v[56:59]
s_add_u32 s66, s44, 4096
TR a[132:133],v230 offset:36864
TR a[134:135],v230 offset:38912
MM v[56:59],a[144:147],a[204:207],v[56:59]
CV v26,v28,v29
CV v27,v30,v31
MM v[48:51],a[144:147],a[200:203],v[48:51]
TR a[136:137],v230 offset:40960
TR a[138:139],v230 offset:43008
MM v[48:51],a[148:151],a[216:219],v[48:51]
buffer_store_dwordx4 v[24:27], v234, s[20:23], s67 offen offset:3072 sc1
MM v[48:51],a[152:155],a[232:235],v[48:51]
TR a[140:141],v230 offset:45056
TR a[142:143],v230 offset:47104
MM v[48:51],a[156:159],a[248:251],v[48:51]
s_waitcnt lgkmcnt(15)
ds_bpermute_b32 v16, v245, v16
ds_bpermute_b32 v17, v245, v17
MM v[52:55],a[160:163],a[24:27],v[52:55]
TR a[144:145],v244 offset:32768
TR a[146:147],v244 offset:34816
MM v[52:55],a[164:167],a[56:59],v[52:55]
s_mov_b64 exec, s[10:11]
buffer_load_dwordx4 a[36:39], v250, s[20:23], s66 offen offset:0 sc1
s_mov_b64 exec, -1
)ASM"
R"ASM(
ds_bpermute_b32 v18, v245, v18
MM v[52:55],a[168:171],a[88:91],v[52:55]
s_add_u32 s66, s16, 6144
TR a[148:149],v244 offset:36864
TR a[150:151],v244 offset:38912
MM v[52:55],a[172:175],a[120:123],v[52:55]
ds_bpermute_b32 v19, v245, v19
MM v[60:63],a[172:175],a[124:127],v[60:63]
TR a[152:153],v244 offset:40960
TR a[154:155],v244 offset:43008
MM v[60:63],a[168:171],a[92:95],v[60:63]
MM v[60:63],a[164:167],a[60:63],v[60:63]
TR a[156:157],v244 offset:45056
TR a[158:159],v244 offset:47104
MM v[60:63],a[160:163],a[28:31],v[60:63]
s_waitcnt lgkmcnt(15)
s_mov_b64 exec, s[8:9]
buffer_load_dwordx4 a[40:43], v250, s[20:23], s66 offen offset:0 sc1
s_mov_b64 exec, -1
MM v[60:63],a[188:191],a[252:255],v[60:63]
s_add_u32 s66, s44, 6144
TR a[160:161],v231 offset:32768
TR a[162:163],v231 offset:34816
MM v[60:63],a[184:187],a[236:239],v[60:63]
s_waitcnt lgkmcnt(6)
v_or_b32 v19, v104, v19
MM v[60:63],a[180:183],a[220:223],v[60:63]
TR a[164:165],v231 offset:36864
TR a[166:167],v231 offset:38912
MM v[60:63],a[176:179],a[204:207],v[60:63]
MM v[52:55],a[176:179],a[200:203],v[52:55]
TR a[168:169],v231 offset:40960
TR a[170:171],v231 offset:43008
MM v[52:55],a[180:183],a[216:219],v[52:55]
s_mov_b64 exec, s[10:11]
buffer_load_dwordx4 a[44:47], v250, s[20:23], s66 offen offset:0 sc1
s_mov_b64 exec, -1
MM v[52:55],a[184:187],a[232:235],v[52:55]
s_add_u32 s66, s16, 8192
TR a[172:173],v231 offset:45056
TR a[174:175],v231 offset:47104
MM v[52:55],a[188:191],a[248:251],v[52:55]
MM v[80:83],a[128:131],a[24:27],v[80:83]
TR a[176:177],v235 offset:32768
TR a[178:179],v235 offset:34816
MM v[80:83],a[132:135],a[56:59],v[80:83]
v_mov_b32 v252, s69
s_mov_b64 exec, 1
buffer_store_dword v252, off, s[20:23], s19 offset:8 sc1
s_mov_b64 exec, -1
MM v[80:83],a[136:139],a[88:91],v[80:83]
TR a[180:181],v235 offset:36864
TR a[182:183],v235 offset:38912
MM v[80:83],a[140:143],a[120:123],v[80:83]
s_add_u32 s67, s46, 12288
CV v48,v48,v49
CV v49,v50,v51
MM v[88:91],a[140:143],a[124:127],v[88:91]
TR a[184:185],v235 offset:40960
TR a[186:187],v235 offset:43008
MM v[88:91],a[136:139],a[92:95],v[88:91]
s_mov_b64 exec, s[8:9]
buffer_load_dwordx4 a[192:195], v250, s[20:23], s66 offen offset:0 sc1
s_mov_b64 exec, -1
CV v50,v52,v53
CV v51,v54,v55
MM v[88:91],a[132:135],a[60:63],v[88:91]
s_add_u32 s66, s44, 8192
TR a[188:189],v235 offset:45056
TR a[190:191],v235 offset:47104
MM v[88:91],a[128:131],a[28:31],v[88:91]
s_waitcnt lgkmcnt(15)
buffer_store_dwordx4 v[48:51], v249, s[20:23], s67 offen offset:0 sc1
MM v[88:91],a[156:159],a[252:255],v[88:91]
s_add_u32 s67, s47, 12288
TR a[128:129],v230 offset:33280
TR a[130:131],v230 offset:35328
MM v[88:91],a[152:155],a[236:239],v[88:91]
s_waitcnt vmcnt(9)
CV v56,v56,v57
CV v57,v58,v59
MM v[88:91],a[148:151],a[220:223],v[88:91]
TR a[132:133],v230 offset:37376
TR a[134:135],v230 offset:39424
MM v[72:75],v[64:67],a[0:3],0
MM v[72:75],v[64:67],a[4:7],v[72:75]
MM v[72:75],v[68:71],a[8:11],v[72:75]
MM v[72:75],v[68:71],a[12:15],v[72:75]
MM v[88:91],a[144:147],a[204:207],v[88:91]
CV v58,v60,v61
CV v59,v62,v63
MM v[80:83],a[144:147],a[200:203],v[80:83]
TR a[136:137],v230 offset:41472
TR a[138:139],v230 offset:43520
MM v[80:83],a[148:151],a[216:219],v[80:83]
MM v[80:83],a[152:155],a[232:235],v[80:83]
TR a[140:141],v230 offset:45568
TR a[142:143],v230 offset:47616
MM v[80:83],a[156:159],a[248:251],v[80:83]
s_waitcnt lgkmcnt(15)
MM v[84:87],a[160:163],a[24:27],v[84:87]
TR a[144:145],v244 offset:33280
TR a[146:147],v244 offset:35328
MM v[84:87],a[164:167],a[56:59],v[84:87]
buffer_store_dwordx4 v[56:59], v234, s[20:23], s67 offen offset:1024 sc1
MM v[84:87],a[168:171],a[88:91],v[84:87]
TR a[148:149],v244 offset:37376
TR a[150:151],v244 offset:39424
MM v[84:87],a[172:175],a[120:123],v[84:87]
ds_bpermute_b32 v48, v245, v48
ds_bpermute_b32 v49, v245, v49
MM v[92:95],a[172:175],a[124:127],v[92:95]
TR a[152:153],v244 offset:41472
TR a[154:155],v244 offset:43520
MM v[92:95],a[168:171],a[92:95],v[92:95]
ds_bpermute_b32 v50, v245, v50
ds_bpermute_b32 v51, v245, v51
MM v[92:95],a[164:167],a[60:63],v[92:95]
TR a[156:157],v244 offset:45568
TR a[158:159],v244 offset:47616
MM v[92:95],a[160:163],a[28:31],v[92:95]
s_waitcnt lgkmcnt(15)
v_cmp_u_f32 s[30:31], v72, v72
CV v76,v72,v73
MM v[92:95],a[188:191],a[252:255],v[92:95]
TR a[160:161],v231 offset:33280
TR a[162:163],v231 offset:35328
MM v[92:95],a[184:187],a[236:239],v[92:95]
CV v77,v74,v75
MM v[92:95],a[180:183],a[220:223],v[92:95]
TR a[164:165],v231 offset:37376
TR a[166:167],v231 offset:39424
MM v[92:95],a[176:179],a[204:207],v[92:95]
s_waitcnt lgkmcnt(6)
ds_bpermute_b32 v76, v232, v76
v_or_b32 v51, v104, v51
MM v[84:87],a[176:179],a[200:203],v[84:87]
TR a[168:169],v231 offset:41472
TR a[170:171],v231 offset:43520
MM v[84:87],a[180:183],a[216:219],v[84:87]
ds_bpermute_b32 v77, v232, v77
v_lshlrev_b32 v10, 16, v0
MM v[84:87],a[184:187],a[232:235],v[84:87]
TR a[172:173],v231 offset:45568
TR a[174:175],v231 offset:47616
MM v[84:87],a[188:191],a[248:251],v[84:87]
v_and_b32 v11, 0xffff0000, v0
v_lshlrev_b32 v12, 16, v1
MM v[112:115],a[128:131],a[24:27],v[112:115]
s_cmp_eq_u32 s82, 0
s_cbranch_scc1 LY1_%=
v_xor_b32 v132, s69, v132
v_xor_b32 v133, s69, v133
v_xor_b32 v134, s69, v134
v_or3_b32 v139, v132, v133, v134
v_cmp_ne_u32 vcc, 0, v139
s_cbranch_vccnz LRYF_%=
LRYFb_%=:
s_ff1_i32_b32 s76, s82
s_and_b32 s76, s76, 7
s_mov_b32 s79, s52
s_cmp_eq_u32 s76, 1
s_cselect_b32 s79, s53, s79
s_cmp_eq_u32 s76, 2
s_cselect_b32 s79, s54, s79
s_cmp_eq_u32 s76, 3
s_cselect_b32 s79, s55, s79
s_cmp_eq_u32 s76, 4
s_cselect_b32 s79, s56, s79
s_cmp_eq_u32 s76, 5
s_cselect_b32 s79, s57, s79
s_cmp_eq_u32 s76, 6
s_cselect_b32 s79, s58, s79
s_cmp_eq_u32 s76, 7
s_cselect_b32 s79, s59, s79
LPG24_%=:
s_cbranch_execz LTR3_%=
s_mul_i32 s76, s76, 2560
s_mul_i32 s90, s84, 640
s_add_u32 s76, s76, s90
s_add_u32 s76, s76, 21659648
s_lshl_b32 s90, s79, 2
s_add_u32 s90, s90, 53248
buffer_load_dword v138, off, s[20:23], s90 sc1
s_mul_i32 s90, s79, 1280
s_mul_i32 s77, s84, 320
s_add_u32 s90, s90, s77
s_add_u32 s90, s90, 131072
v_lshrrev_b32 v139, 1, v248
s_mov_b32 exec_lo, -1
s_mov_b32 exec_hi, 255
buffer_load_dwordx2 v[136:137], v139, s[20:23], s90 offen offset:0 sc1
s_mov_b32 exec_lo, -1
s_mov_b32 exec_hi, 255
buffer_load_dwordx4 v[160:163], v248, s[20:23], s76 offen offset:0 sc1
s_add_u32 s76, s76, 20480
buffer_load_dwordx4 v[164:167], v248, s[20:23], s76 offen offset:0 sc1
s_add_u32 s76, s76, 20480
buffer_load_dwordx4 v[168:171], v248, s[20:23], s76 offen offset:0 sc1
s_add_u32 s76, s76, 20480
buffer_load_dwordx4 v[172:175], v248, s[20:23], s76 offen offset:0 sc1
s_add_u32 s76, s76, 20480
buffer_load_dwordx4 v[176:179], v248, s[20:23], s76 offen offset:0 sc1
s_add_u32 s76, s76, 20480
buffer_load_dwordx4 v[180:183], v248, s[20:23], s76 offen offset:0 sc1
s_add_u32 s76, s76, 20480
s_mov_b64 exec, -1
LY1_%=:
TR a[176:177],v235 offset:33280
TR a[178:179],v235 offset:35328
MM v[112:115],a[132:135],a[56:59],v[112:115]
s_mov_b64 exec, s[10:11]
buffer_load_dwordx4 a[196:199], v250, s[20:23], s66 offen offset:0 sc1
s_mov_b64 exec, -1
MM v[112:115],a[136:139],a[88:91],v[112:115]
TR a[180:181],v235 offset:37376
TR a[182:183],v235 offset:39424
MM v[112:115],a[140:143],a[120:123],v[112:115]
s_add_u32 s67, s46, 12288
CV v80,v80,v81
CV v81,v82,v83
MM v[120:123],a[140:143],a[124:127],v[120:123]
TR a[184:185],v235 offset:41472
TR a[186:187],v235 offset:43520
MM v[120:123],a[136:139],a[92:95],v[120:123]
CV v82,v84,v85
CV v83,v86,v87
MM v[120:123],a[132:135],a[60:63],v[120:123]
TR a[188:189],v235 offset:45568
TR a[190:191],v235 offset:47616
MM v[120:123],a[128:131],a[28:31],v[120:123]
s_waitcnt lgkmcnt(15)
buffer_store_dwordx4 v[80:83], v249, s[20:23], s67 offen offset:2048 sc1
MM v[120:123],a[156:159],a[252:255],v[120:123]
s_add_u32 s67, s47, 12288
TR a[128:129],v228 offset:16384
TR a[130:131],v228 offset:17408
MM v[120:123],a[152:155],a[236:239],v[120:123]
CV v88,v88,v89
CV v89,v90,v91
MM v[120:123],a[148:151],a[220:223],v[120:123]
TR a[132:133],v228 offset:18432
TR a[134:135],v228 offset:19456
MM v[120:123],a[144:147],a[204:207],v[120:123]
CV v90,v92,v93
CV v91,v94,v95
MM v[112:115],a[144:147],a[200:203],v[112:115]
TR a[136:137],v228 offset:20480
TR a[138:139],v228 offset:21504
MM v[112:115],a[148:151],a[216:219],v[112:115]
v_and_b32 v13, 0xffff0000, v1
v_lshlrev_b32 v14, 16, v32
MM v[112:115],a[152:155],a[232:235],v[112:115]
TR a[140:141],v228 offset:22528
TR a[142:143],v228 offset:23552
MM v[112:115],a[156:159],a[248:251],v[112:115]
s_waitcnt lgkmcnt(15)
buffer_store_dwordx4 v[88:91], v234, s[20:23], s67 offen offset:3072 sc1
MM v[116:119],a[160:163],a[24:27],v[116:119]
TR a[144:145],v228 offset:24576
TR a[146:147],v228 offset:25600
MM v[116:119],a[164:167],a[56:59],v[116:119]
buffer_load_dword v242, v253, s[20:23], s68 offen offset:8 sc1
buffer_load_dword v142, v253, s[20:23], s45 offen offset:8 sc1
MM v[116:119],a[168:171],a[88:91],v[116:119]
TR a[148:149],v228 offset:26624
TR a[150:151],v228 offset:27648
MM v[116:119],a[172:175],a[120:123],v[116:119]
ds_bpermute_b32 v80, v245, v80
ds_bpermute_b32 v81, v245, v81
MM v[124:127],a[172:175],a[124:127],v[124:127]
TR a[152:153],v228 offset:28672
TR a[154:155],v228 offset:29696
MM v[124:127],a[168:171],a[92:95],v[124:127]
ds_bpermute_b32 v82, v245, v82
ds_bpermute_b32 v83, v245, v83
MM v[124:127],a[164:167],a[60:63],v[124:127]
TR a[156:157],v228 offset:30720
TR a[158:159],v228 offset:31744
MM v[124:127],a[160:163],a[28:31],v[124:127]
s_waitcnt lgkmcnt(15)
v_and_b32 v15, 0xffff0000, v32
v_lshlrev_b32 v42, 16, v33
MM v[124:127],a[188:191],a[252:255],v[124:127]
s_cmp_eq_u32 s82, 0
s_cbranch_scc1 LY2_%=
s_mov_b32 exec_lo, -1
s_mov_b32 exec_hi, 255
buffer_load_dwordx4 v[184:187], v248, s[20:23], s76 offen offset:0 sc1
s_add_u32 s76, s76, 20480
buffer_load_dwordx4 v[188:191], v248, s[20:23], s76 offen offset:0 sc1
s_add_u32 s76, s76, 20480
buffer_load_dwordx4 v[192:195], v248, s[20:23], s76 offen offset:0 sc1
s_add_u32 s76, s76, 20480
buffer_load_dwordx4 v[196:199], v248, s[20:23], s76 offen offset:0 sc1
s_add_u32 s76, s76, 20480
buffer_load_dwordx4 v[200:203], v248, s[20:23], s76 offen offset:0 sc1
s_add_u32 s76, s76, 20480
buffer_load_dwordx4 v[204:207], v248, s[20:23], s76 offen offset:0 sc1
s_add_u32 s76, s76, 20480
s_mov_b64 exec, -1
LY2_%=:
TR a[160:161],v229 offset:16384
TR a[162:163],v229 offset:17408
MM v[124:127],a[184:187],a[236:239],v[124:127]
v_and_b32 v43, 0xffff0000, v33
s_add_u32 s80, s18, 0
MM v[124:127],a[180:183],a[220:223],v[124:127]
TR a[164:165],v229 offset:18432
TR a[166:167],v229 offset:19456
MM v[124:127],a[176:179],a[204:207],v[124:127]
s_waitcnt lgkmcnt(6)
v_or_b32 v83, v104, v83
v_lshlrev_b32 v72, 16, v76
MM v[116:119],a[176:179],a[200:203],v[116:119]
TR a[168:169],v229 offset:20480
TR a[170:171],v229 offset:21504
MM v[116:119],a[180:183],a[216:219],v[116:119]
v_and_b32 v73, 0xffff0000, v76
v_lshlrev_b32 v74, 16, v77
MM v[116:119],a[184:187],a[232:235],v[116:119]
TR a[172:173],v229 offset:22528
TR a[174:175],v229 offset:23552
MM v[116:119],a[188:191],a[248:251],v[116:119]
v_and_b32 v75, 0xffff0000, v77
v_mul_f32 v72, v72, v10
MM v[144:147],a[128:131],a[24:27],v[144:147]
TR a[176:177],v229 offset:24576
TR a[178:179],v229 offset:25600
MM v[144:147],a[132:135],a[56:59],v[144:147]
v_mul_f32 v73, v73, v11
v_mul_f32 v74, v74, v12
MM v[144:147],a[136:139],a[88:91],v[144:147]
TR a[180:181],v229 offset:26624
TR a[182:183],v229 offset:27648
MM v[144:147],a[140:143],a[120:123],v[144:147]
s_add_u32 s67, s46, 16384
CV v112,v112,v113
CV v113,v114,v115
MM v[152:155],a[140:143],a[124:127],v[152:155]
TR a[184:185],v229 offset:28672
TR a[186:187],v229 offset:29696
MM v[152:155],a[136:139],a[92:95],v[152:155]
CV v114,v116,v117
CV v115,v118,v119
MM v[152:155],a[132:135],a[60:63],v[152:155]
TR a[188:189],v229 offset:30720
TR a[190:191],v229 offset:31744
MM v[152:155],a[128:131],a[28:31],v[152:155]
s_waitcnt lgkmcnt(15)
buffer_store_dwordx4 v[112:115], v249, s[20:23], s67 offen offset:0 sc1
MM v[152:155],a[156:159],a[252:255],v[152:155]
s_add_u32 s67, s47, 16384
CV v120,v120,v121
)ASM"
R"ASM(
CV v121,v122,v123
MM v[152:155],a[152:155],a[236:239],v[152:155]
CV v122,v124,v125
CV v123,v126,v127
MM v[152:155],a[148:151],a[220:223],v[152:155]
buffer_store_dwordx4 v[120:123], v234, s[20:23], s67 offen offset:1024 sc1
MM v[152:155],a[144:147],a[204:207],v[152:155]
v_mul_f32 v75, v75, v13
CV v76,v72,v73
MM v[144:147],a[144:147],a[200:203],v[144:147]
ds_bpermute_b32 v112, v245, v112
ds_bpermute_b32 v113, v245, v113
MM v[144:147],a[148:151],a[216:219],v[144:147]
ds_bpermute_b32 v114, v245, v114
ds_bpermute_b32 v115, v245, v115
MM v[144:147],a[152:155],a[232:235],v[144:147]
CV v77,v74,v75
v_lshlrev_b32 v72, 16, v76
MM v[144:147],a[156:159],a[248:251],v[144:147]
s_waitcnt lgkmcnt(12)
v_and_b32 v73, 0xffff0000, v76
v_lshlrev_b32 v74, 16, v77
MM v[148:151],a[160:163],a[24:27],v[148:151]
s_cmp_eq_u32 s82, 0
s_cbranch_scc1 LY3_%=
s_mov_b32 exec_lo, -1
s_mov_b32 exec_hi, 255
buffer_load_dwordx4 v[208:211], v248, s[20:23], s76 offen offset:0 sc1
s_add_u32 s76, s76, 20480
buffer_load_dwordx4 v[212:215], v248, s[20:23], s76 offen offset:0 sc1
s_add_u32 s76, s76, 20480
buffer_load_dwordx4 v[216:219], v248, s[20:23], s76 offen offset:0 sc1
s_add_u32 s76, s76, 20480
buffer_load_dwordx4 v[220:223], v248, s[20:23], s76 offen offset:0 sc1
s_add_u32 s76, s76, 20480
buffer_load_dwordx4 v[96:99], v248, s[20:23], s76 offen offset:0 sc1
s_add_u32 s76, s76, 20480
buffer_load_dwordx4 v[100:103], v248, s[20:23], s76 offen offset:0 sc1
s_mov_b64 exec, -1
LY3_%=:
v_and_b32 v75, 0xffff0000, v77
v_add_f32 v72, v72, v14
MM v[148:151],a[164:167],a[56:59],v[148:151]
v_add_f32 v73, v73, v15
v_add_f32 v74, v74, v42
MM v[148:151],a[168:171],a[88:91],v[148:151]
v_add_f32 v75, v75, v43
CV v44,v72,v73
MM v[148:151],a[172:175],a[120:123],v[148:151]
s_waitcnt lgkmcnt(0)
v_or_b32 v115, v104, v115
CV v45,v74,v75
MM v[156:159],a[172:175],a[124:127],v[156:159]
s_cmp_eq_u64 s[30:31], 0
s_cselect_b32 s94, s51, 0
s_and_b32 s94, s94, 1
MM v[156:159],a[168:171],a[92:95],v[156:159]
s_xor_b32 s17, s17, s94
s_cmp_lg_u32 s94, 0
s_cselect_b64 exec, -1, 0
buffer_store_dwordx2 v[44:45], v251, s[40:43], s80 offen offset:0 nt
s_mov_b64 exec, -1
MM v[156:159],a[164:167],a[60:63],v[156:159]
MM v[156:159],a[160:163],a[28:31],v[156:159]
MM v[156:159],a[188:191],a[252:255],v[156:159]
MM v[156:159],a[184:187],a[236:239],v[156:159]
MM v[156:159],a[180:183],a[220:223],v[156:159]
MM v[156:159],a[176:179],a[204:207],v[156:159]
MM v[148:151],a[176:179],a[200:203],v[148:151]
MM v[148:151],a[180:183],a[216:219],v[148:151]
MM v[148:151],a[184:187],a[232:235],v[148:151]
MM v[148:151],a[188:191],a[248:251],v[148:151]
s_nop 5
s_add_u32 s67, s46, 16384
CV v144,v144,v145
CV v145,v146,v147
CV v146,v148,v149
CV v147,v150,v151
buffer_store_dwordx4 v[144:147], v249, s[20:23], s67 offen offset:2048 sc1
s_add_u32 s67, s47, 16384
CV v152,v152,v153
CV v153,v154,v155
CV v154,v156,v157
CV v155,v158,v159
buffer_store_dwordx4 v[152:155], v234, s[20:23], s67 offen offset:3072 sc1
ds_bpermute_b32 v144, v245, v144
ds_bpermute_b32 v145, v245, v145
ds_bpermute_b32 v146, v245, v146
ds_bpermute_b32 v147, v245, v147
s_waitcnt vmcnt(5)
v_cmp_eq_u32 s[12:13], s69, v242
v_cmp_eq_u32 s[14:15], s69, v142
s_nop 0
s_and_b32 s12, s12, 0xffff0000
s_nop 0
s_add_u32 s66, s16, 10240
s_mov_b64 exec, s[12:13]
buffer_load_dwordx4 v[16:19], v250, s[20:23], s66 offen offset:0 sc1
s_mov_b64 exec, -1
s_add_u32 s66, s44, 10240
s_mov_b64 exec, s[14:15]
buffer_load_dwordx4 a[68:71], v250, s[20:23], s66 offen offset:0 sc1
s_mov_b64 exec, -1
s_add_u32 s66, s16, 12288
s_mov_b64 exec, s[12:13]
buffer_load_dwordx4 v[48:51], v250, s[20:23], s66 offen offset:0 sc1
s_mov_b64 exec, -1
s_add_u32 s66, s44, 12288
s_mov_b64 exec, s[14:15]
buffer_load_dwordx4 a[76:79], v250, s[20:23], s66 offen offset:0 sc1
s_mov_b64 exec, -1
s_add_u32 s66, s16, 14336
s_mov_b64 exec, s[12:13]
buffer_load_dwordx4 v[80:83], v250, s[20:23], s66 offen offset:0 sc1
s_mov_b64 exec, -1
s_add_u32 s66, s44, 14336
s_mov_b64 exec, s[14:15]
buffer_load_dwordx4 a[100:103], v250, s[20:23], s66 offen offset:0 sc1
s_mov_b64 exec, -1
s_add_u32 s66, s16, 16384
s_mov_b64 exec, s[12:13]
buffer_load_dwordx4 v[112:115], v250, s[20:23], s66 offen offset:0 sc1
s_mov_b64 exec, -1
s_add_u32 s66, s44, 16384
s_mov_b64 exec, s[14:15]
buffer_load_dwordx4 a[108:111], v250, s[20:23], s66 offen offset:0 sc1
s_mov_b64 exec, -1
s_waitcnt lgkmcnt(0)
v_or_b32 v147, v104, v147
s_add_u32 s66, s16, 18432
s_mov_b64 exec, s[12:13]
buffer_load_dwordx4 v[144:147], v250, s[20:23], s66 offen offset:0 sc1
s_mov_b64 exec, -1
s_add_u32 s66, s44, 18432
s_mov_b64 exec, s[14:15]
buffer_load_dwordx4 a[212:215], v250, s[20:23], s66 offen offset:0 sc1
s_mov_b64 exec, -1
buffer_load_dwordx4 v[240:243], v253, s[20:23], s68 offen offset:0 sc1
buffer_load_dwordx4 v[140:143], v253, s[20:23], s45 offen offset:0 sc1
v_lshlrev_b32 v10, 16, v246
v_and_b32 v11, 0xffff0000, v246
v_lshlrev_b32 v12, 16, v247
v_and_b32 v13, 0xffff0000, v247
v_mul_f32 v14, 0xbfb8aa3b, v10
v_mul_f32 v15, 0xbfb8aa3b, v11
v_mul_f32 v42, 0xbfb8aa3b, v12
v_mul_f32 v43, 0xbfb8aa3b, v13
EX v14,v14
EX v15,v15
EX v42,v42
EX v43,v43
v_add_f32 v14, 1.0, v14
v_add_f32 v15, 1.0, v15
v_add_f32 v42, 1.0, v42
v_add_f32 v43, 1.0, v43
v_rcp_f32 v14, v14
v_rcp_f32 v15, v15
v_rcp_f32 v42, v42
v_rcp_f32 v43, v43
v_mul_f32 v14, v10, v14
v_mul_f32 v15, v11, v15
v_mul_f32 v42, v12, v42
v_mul_f32 v43, v13, v43
CV v8,v14,v15
CV v9,v42,v43
s_cmp_lg_u32 s82, 0
s_cbranch_scc1 LYS_%=
LYE_%=:
s_mov_b32 s73, 30001
s_mov_b32 s65, 1
s_bitcmp0_b32 s17, 0
s_cbranch_scc1 LSA0_%=
MM v[72:75],v[64:67],a[0:3],0
MM v[72:75],v[64:67],a[4:7],v[72:75]
MM v[72:75],v[68:71],a[8:11],v[72:75]
MM v[72:75],v[68:71],a[12:15],v[72:75]
s_nop 7
v_cmp_u_f32 s[30:31], v72, v72
s_nop 1
s_cmp_lg_u64 s[30:31], 0
s_cbranch_scc1 LSA0_%=
s_bitset0_b32 s17, 0
CV v76,v72,v73
CV v77,v74,v75
ds_bpermute_b32 v76, v232, v76
ds_bpermute_b32 v77, v232, v77
v_lshlrev_b32 v10, 16, v0
v_and_b32 v11, 0xffff0000, v0
v_lshlrev_b32 v12, 16, v1
v_and_b32 v13, 0xffff0000, v1
v_lshlrev_b32 v14, 16, v32
v_and_b32 v15, 0xffff0000, v32
v_lshlrev_b32 v42, 16, v33
v_and_b32 v43, 0xffff0000, v33
s_add_u32 s80, s18, 0
s_waitcnt lgkmcnt(0)
v_lshlrev_b32 v72, 16, v76
v_and_b32 v73, 0xffff0000, v76
v_lshlrev_b32 v74, 16, v77
v_and_b32 v75, 0xffff0000, v77
v_mul_f32 v72, v72, v10
v_mul_f32 v73, v73, v11
v_mul_f32 v74, v74, v12
v_mul_f32 v75, v75, v13
CV v76,v72,v73
CV v77,v74,v75
v_lshlrev_b32 v72, 16, v76
v_and_b32 v73, 0xffff0000, v76
v_lshlrev_b32 v74, 16, v77
v_and_b32 v75, 0xffff0000, v77
v_add_f32 v72, v72, v14
v_add_f32 v73, v73, v15
v_add_f32 v74, v74, v42
v_add_f32 v75, v75, v43
CV v44,v72,v73
CV v45,v74,v75
buffer_store_dwordx2 v[44:45], v251, s[40:43], s80 offen offset:0 nt
s_nop 1
LSA0_%=:
s_bitcmp0_b32 s17, 1
s_cbranch_scc1 LSA1_%=
MM v[72:75],v[64:67],a[32:35],0
MM v[72:75],v[64:67],a[36:39],v[72:75]
MM v[72:75],v[68:71],a[40:43],v[72:75]
MM v[72:75],v[68:71],a[44:47],v[72:75]
s_nop 7
v_cmp_u_f32 s[30:31], v72, v72
s_nop 1
s_cmp_lg_u64 s[30:31], 0
s_cbranch_scc1 LSA1_%=
s_bitset0_b32 s17, 1
CV v76,v72,v73
CV v77,v74,v75
ds_bpermute_b32 v76, v232, v76
ds_bpermute_b32 v77, v232, v77
v_lshlrev_b32 v10, 16, v2
v_and_b32 v11, 0xffff0000, v2
v_lshlrev_b32 v12, 16, v3
v_and_b32 v13, 0xffff0000, v3
v_lshlrev_b32 v14, 16, v34
v_and_b32 v15, 0xffff0000, v34
v_lshlrev_b32 v42, 16, v35
v_and_b32 v43, 0xffff0000, v35
s_add_u32 s80, s18, 128
s_waitcnt lgkmcnt(0)
v_lshlrev_b32 v72, 16, v76
v_and_b32 v73, 0xffff0000, v76
v_lshlrev_b32 v74, 16, v77
v_and_b32 v75, 0xffff0000, v77
v_mul_f32 v72, v72, v10
v_mul_f32 v73, v73, v11
v_mul_f32 v74, v74, v12
v_mul_f32 v75, v75, v13
CV v76,v72,v73
CV v77,v74,v75
v_lshlrev_b32 v72, 16, v76
v_and_b32 v73, 0xffff0000, v76
v_lshlrev_b32 v74, 16, v77
v_and_b32 v75, 0xffff0000, v77
v_add_f32 v72, v72, v14
v_add_f32 v73, v73, v15
v_add_f32 v74, v74, v42
v_add_f32 v75, v75, v43
CV v46,v72,v73
CV v47,v74,v75
buffer_store_dwordx2 v[46:47], v251, s[40:43], s80 offen offset:0 nt
s_nop 1
LSA1_%=:
s_waitcnt vmcnt(0)
s_mov_b64 exec, 1
buffer_store_dword v233, off, s[20:23], s19 offset:12 sc1
s_mov_b64 exec, -1
MM v[72:75],v[64:67],a[192:195],0
MM v[100:103],v[64:67],v[80:83],0
MM v[96:99],v[64:67],v[16:19],0
MM v[72:75],v[64:67],a[196:199],v[72:75]
MM v[100:103],v[64:67],a[100:103],v[100:103]
MM v[96:99],v[64:67],a[68:71],v[96:99]
MM v[72:75],v[68:71],v[144:147],v[72:75]
MM v[100:103],v[68:71],v[112:115],v[100:103]
MM v[96:99],v[68:71],v[48:51],v[96:99]
MM v[72:75],v[68:71],a[212:215],v[72:75]
MM v[100:103],v[68:71],a[108:111],v[100:103]
MM v[96:99],v[68:71],a[76:79],v[96:99]
s_mov_b32 s65, 0
s_mov_b32 s90, 250
s_nop 5
v_cmp_u_f32 vcc, v72, v100
v_cmp_u_f32 s[30:31], v96, v96
s_nop 1
s_or_b64 vcc, vcc, s[30:31]
s_cbranch_scc1 LQS_%=
CV v76,v72,v73
CV v77,v74,v75
CV v108,v100,v101
CV v109,v102,v103
CV v78,v96,v97
CV v79,v98,v99
ds_bpermute_b32 v76, v232, v76
ds_bpermute_b32 v77, v232, v77
ds_bpermute_b32 v108, v232, v108
ds_bpermute_b32 v109, v232, v109
ds_bpermute_b32 v78, v232, v78
ds_bpermute_b32 v79, v232, v79
v_lshlrev_b32 v10, 16, v8
v_and_b32 v11, 0xffff0000, v8
v_lshlrev_b32 v12, 16, v9
v_and_b32 v13, 0xffff0000, v9
v_lshlrev_b32 v14, 16, v40
v_and_b32 v15, 0xffff0000, v40
v_lshlrev_b32 v42, 16, v41
v_and_b32 v43, 0xffff0000, v41
s_add_u32 s80, s63, 0
s_waitcnt lgkmcnt(4)
v_lshlrev_b32 v72, 16, v76
v_and_b32 v73, 0xffff0000, v76
v_lshlrev_b32 v74, 16, v77
v_and_b32 v75, 0xffff0000, v77
v_mul_f32 v72, v72, v10
v_mul_f32 v73, v73, v11
v_mul_f32 v74, v74, v12
v_mul_f32 v75, v75, v13
CV v76,v72,v73
CV v77,v74,v75
v_lshlrev_b32 v72, 16, v76
v_and_b32 v73, 0xffff0000, v76
v_lshlrev_b32 v74, 16, v77
v_and_b32 v75, 0xffff0000, v77
v_add_f32 v72, v72, v14
v_add_f32 v73, v73, v15
v_add_f32 v74, v74, v42
v_add_f32 v75, v75, v43
CV v44,v72,v73
CV v45,v74,v75
buffer_store_dwordx2 v[44:45], v254, s[40:43], s80 offen offset:0 nt
v_lshlrev_b32 v10, 16, v6
v_and_b32 v11, 0xffff0000, v6
v_lshlrev_b32 v12, 16, v7
v_and_b32 v13, 0xffff0000, v7
v_lshlrev_b32 v14, 16, v38
v_and_b32 v15, 0xffff0000, v38
v_lshlrev_b32 v42, 16, v39
v_and_b32 v43, 0xffff0000, v39
s_add_u32 s80, s18, 41088
s_waitcnt lgkmcnt(2)
v_lshlrev_b32 v100, 16, v108
v_and_b32 v101, 0xffff0000, v108
v_lshlrev_b32 v102, 16, v109
v_and_b32 v103, 0xffff0000, v109
v_mul_f32 v100, v100, v10
v_mul_f32 v101, v101, v11
v_mul_f32 v102, v102, v12
v_mul_f32 v103, v103, v13
CV v108,v100,v101
CV v109,v102,v103
v_lshlrev_b32 v100, 16, v108
v_and_b32 v101, 0xffff0000, v108
v_lshlrev_b32 v102, 16, v109
v_and_b32 v103, 0xffff0000, v109
v_add_f32 v100, v100, v14
v_add_f32 v101, v101, v15
v_add_f32 v102, v102, v42
v_add_f32 v103, v103, v43
CV v46,v100,v101
CV v47,v102,v103
buffer_store_dwordx2 v[46:47], v251, s[40:43], s80 offen offset:0 nt
v_lshlrev_b32 v10, 16, v4
v_and_b32 v11, 0xffff0000, v4
v_lshlrev_b32 v12, 16, v5
v_and_b32 v13, 0xffff0000, v5
v_lshlrev_b32 v14, 16, v36
v_and_b32 v15, 0xffff0000, v36
v_lshlrev_b32 v42, 16, v37
v_and_b32 v43, 0xffff0000, v37
s_add_u32 s80, s18, 40960
s_waitcnt lgkmcnt(0)
v_lshlrev_b32 v96, 16, v78
v_and_b32 v97, 0xffff0000, v78
v_lshlrev_b32 v98, 16, v79
v_and_b32 v99, 0xffff0000, v79
v_mul_f32 v96, v96, v10
v_mul_f32 v97, v97, v11
v_mul_f32 v98, v98, v12
v_mul_f32 v99, v99, v13
CV v78,v96,v97
CV v79,v98,v99
v_lshlrev_b32 v96, 16, v78
v_and_b32 v97, 0xffff0000, v78
v_lshlrev_b32 v98, 16, v79
v_and_b32 v99, 0xffff0000, v79
v_add_f32 v96, v96, v14
v_add_f32 v97, v97, v15
v_add_f32 v98, v98, v42
v_add_f32 v99, v99, v43
CV v44,v96,v97
CV v45,v98,v99
buffer_store_dwordx2 v[44:45], v251, s[40:43], s80 offen offset:0 nt
s_and_b32 s17, s17, 3
s_cbranch_scc0 LQE_%=
LQS_%=:
v_cmp_eq_u32 s[12:13], s69, v242
v_cmp_eq_u32 s[14:15], s69, v142
v_cmp_eq_u32 s[8:9], s69, v241
v_cmp_eq_u32 s[10:11], s69, v141
v_cmp_eq_u32 s[36:37], s69, v240
v_cmp_eq_u32 s[38:39], s69, v140
s_mov_b32 s94, 0
s_sub_u32 s90, s90, 1
s_cbranch_scc1 LSW_%=
s_and_b64 vcc, s[12:13], s[14:15]
s_and_b32 s12, s12, 0xffff0000
s_and_b32 s8, s8, 0xffff0000
s_and_b32 s36, s36, 0xffff0000
s_bitcmp0_b32 s17, 4
s_cbranch_scc1 LQG4_%=
AR v108,a195
)ASM"
R"ASM(
AR v109,a199
AR v111,a215
v_cmp_eq_u32 s[24:25], -1, v108
v_cmp_eq_u32 s[26:27], -1, v109
v_cmp_eq_u32 s[28:29], -1, v147
v_cmp_eq_u32 s[46:47], -1, v111
s_nop 0
s_or_b64 s[30:31], s[24:25], s[26:27]
s_or_b64 s[30:31], s[30:31], s[28:29]
s_or_b64 s[30:31], s[30:31], s[46:47]
s_cselect_b32 s76, 256, 16
s_or_b32 s94, s94, s76
s_bitcmp1_b32 s94, 4
s_cbranch_scc1 LQG4_%=
s_and_b64 exec, s[46:47], s[14:15]
s_cbranch_scc0 LQK43_%=
s_add_u32 s66, s44, 18432
buffer_load_dwordx4 a[212:215], v250, s[20:23], s66 offen offset:0 sc1
LQK43_%=:
s_and_b64 exec, s[28:29], s[12:13]
s_cbranch_scc0 LQK42_%=
s_add_u32 s66, s16, 18432
buffer_load_dwordx4 v[144:147], v250, s[20:23], s66 offen offset:0 sc1
LQK42_%=:
s_and_b64 exec, s[26:27], s[10:11]
s_cbranch_scc0 LQK41_%=
s_add_u32 s66, s44, 8192
buffer_load_dwordx4 a[196:199], v250, s[20:23], s66 offen offset:0 sc1
LQK41_%=:
s_and_b64 exec, s[24:25], s[8:9]
s_cbranch_scc0 LQK40_%=
s_add_u32 s66, s16, 8192
buffer_load_dwordx4 a[192:195], v250, s[20:23], s66 offen offset:0 sc1
LQK40_%=:
s_mov_b64 exec, -1
LQG4_%=:
s_bitcmp0_b32 s17, 3
s_cbranch_scc1 LQG3_%=
AR v109,a103
AR v111,a111
v_cmp_eq_u32 s[24:25], -1, v83
v_cmp_eq_u32 s[26:27], -1, v109
v_cmp_eq_u32 s[28:29], -1, v115
v_cmp_eq_u32 s[46:47], -1, v111
s_nop 0
s_or_b64 s[30:31], s[24:25], s[26:27]
s_or_b64 s[30:31], s[30:31], s[28:29]
s_or_b64 s[30:31], s[30:31], s[46:47]
s_cselect_b32 s76, 256, 8
LPG25_%=:
s_nop 0
s_or_b32 s94, s94, s76
s_bitcmp1_b32 s94, 3
s_cbranch_scc1 LQG3_%=
s_and_b64 exec, s[46:47], s[14:15]
s_cbranch_scc0 LQK33_%=
s_add_u32 s66, s44, 16384
buffer_load_dwordx4 a[108:111], v250, s[20:23], s66 offen offset:0 sc1
LQK33_%=:
s_and_b64 exec, s[28:29], s[12:13]
s_cbranch_scc0 LQK32_%=
s_add_u32 s66, s16, 16384
buffer_load_dwordx4 v[112:115], v250, s[20:23], s66 offen offset:0 sc1
LQK32_%=:
s_and_b64 exec, s[26:27], s[14:15]
s_cbranch_scc0 LQK31_%=
s_add_u32 s66, s44, 14336
buffer_load_dwordx4 a[100:103], v250, s[20:23], s66 offen offset:0 sc1
LQK31_%=:
s_and_b64 exec, s[24:25], s[12:13]
s_cbranch_scc0 LQK30_%=
s_add_u32 s66, s16, 14336
buffer_load_dwordx4 v[80:83], v250, s[20:23], s66 offen offset:0 sc1
LQK30_%=:
s_mov_b64 exec, -1
LQG3_%=:
s_bitcmp0_b32 s17, 2
s_cbranch_scc1 LQG2_%=
AR v109,a71
AR v111,a79
v_cmp_eq_u32 s[24:25], -1, v19
v_cmp_eq_u32 s[26:27], -1, v109
v_cmp_eq_u32 s[28:29], -1, v51
v_cmp_eq_u32 s[46:47], -1, v111
s_nop 0
s_or_b64 s[30:31], s[24:25], s[26:27]
s_or_b64 s[30:31], s[30:31], s[28:29]
s_or_b64 s[30:31], s[30:31], s[46:47]
s_cselect_b32 s76, 256, 4
s_or_b32 s94, s94, s76
s_bitcmp1_b32 s94, 2
s_cbranch_scc1 LQG2_%=
s_and_b64 exec, s[46:47], s[14:15]
s_cbranch_scc0 LQK23_%=
s_add_u32 s66, s44, 12288
buffer_load_dwordx4 a[76:79], v250, s[20:23], s66 offen offset:0 sc1
LQK23_%=:
s_and_b64 exec, s[28:29], s[12:13]
s_cbranch_scc0 LQK22_%=
s_add_u32 s66, s16, 12288
buffer_load_dwordx4 v[48:51], v250, s[20:23], s66 offen offset:0 sc1
LQK22_%=:
s_and_b64 exec, s[26:27], s[14:15]
s_cbranch_scc0 LQK21_%=
s_add_u32 s66, s44, 10240
buffer_load_dwordx4 a[68:71], v250, s[20:23], s66 offen offset:0 sc1
LQK21_%=:
s_and_b64 exec, s[24:25], s[12:13]
s_cbranch_scc0 LQK20_%=
s_add_u32 s66, s16, 10240
buffer_load_dwordx4 v[16:19], v250, s[20:23], s66 offen offset:0 sc1
LQK20_%=:
s_mov_b64 exec, -1
LQG2_%=:
s_and_b32 s76, s17, 3
s_cbranch_scc1 LQA_%=
LQB_%=:
s_cmp_eq_u64 vcc, -1
s_cbranch_scc1 LQF_%=
buffer_load_dwordx4 v[240:243], v253, s[20:23], s68 offen offset:0 sc1
buffer_load_dwordx4 v[140:143], v253, s[20:23], s45 offen offset:0 sc1
LQF_%=:
s_and_b32 s76, s94, 3
s_cbranch_scc1 LQC_%=
LQD_%=:
s_bitcmp0_b32 s94, 2
s_cbranch_scc1 LQN2_%=
MM v[72:75],v[64:67],v[16:19],0
MM v[72:75],v[64:67],a[68:71],v[72:75]
MM v[72:75],v[68:71],v[48:51],v[72:75]
MM v[72:75],v[68:71],a[76:79],v[72:75]
s_bitset0_b32 s17, 2
s_nop 6
CV v76,v72,v73
CV v77,v74,v75
ds_bpermute_b32 v76, v232, v76
ds_bpermute_b32 v77, v232, v77
v_lshlrev_b32 v10, 16, v4
v_and_b32 v11, 0xffff0000, v4
v_lshlrev_b32 v12, 16, v5
v_and_b32 v13, 0xffff0000, v5
v_lshlrev_b32 v14, 16, v36
v_and_b32 v15, 0xffff0000, v36
v_lshlrev_b32 v42, 16, v37
v_and_b32 v43, 0xffff0000, v37
s_add_u32 s80, s18, 40960
s_waitcnt lgkmcnt(0)
v_lshlrev_b32 v72, 16, v76
v_and_b32 v73, 0xffff0000, v76
v_lshlrev_b32 v74, 16, v77
v_and_b32 v75, 0xffff0000, v77
v_mul_f32 v72, v72, v10
v_mul_f32 v73, v73, v11
v_mul_f32 v74, v74, v12
v_mul_f32 v75, v75, v13
CV v76,v72,v73
CV v77,v74,v75
v_lshlrev_b32 v72, 16, v76
v_and_b32 v73, 0xffff0000, v76
v_lshlrev_b32 v74, 16, v77
v_and_b32 v75, 0xffff0000, v77
v_add_f32 v72, v72, v14
v_add_f32 v73, v73, v15
v_add_f32 v74, v74, v42
v_add_f32 v75, v75, v43
CV v44,v72,v73
CV v45,v74,v75
buffer_store_dwordx2 v[44:45], v251, s[40:43], s80 offen offset:0 nt
s_bitcmp0_b32 s94, 8
s_cbranch_scc1 LQN2_%=
s_waitcnt vmcnt(1)
s_branch LQS_%=
LQN2_%=:
s_bitcmp0_b32 s94, 3
s_cbranch_scc1 LQN3_%=
MM v[72:75],v[64:67],v[80:83],0
MM v[72:75],v[64:67],a[100:103],v[72:75]
MM v[72:75],v[68:71],v[112:115],v[72:75]
MM v[72:75],v[68:71],a[108:111],v[72:75]
s_bitset0_b32 s17, 3
s_nop 6
CV v76,v72,v73
CV v77,v74,v75
ds_bpermute_b32 v76, v232, v76
ds_bpermute_b32 v77, v232, v77
v_lshlrev_b32 v10, 16, v6
v_and_b32 v11, 0xffff0000, v6
v_lshlrev_b32 v12, 16, v7
v_and_b32 v13, 0xffff0000, v7
v_lshlrev_b32 v14, 16, v38
v_and_b32 v15, 0xffff0000, v38
v_lshlrev_b32 v42, 16, v39
v_and_b32 v43, 0xffff0000, v39
s_add_u32 s80, s18, 41088
s_waitcnt lgkmcnt(0)
v_lshlrev_b32 v72, 16, v76
v_and_b32 v73, 0xffff0000, v76
v_lshlrev_b32 v74, 16, v77
v_and_b32 v75, 0xffff0000, v77
v_mul_f32 v72, v72, v10
v_mul_f32 v73, v73, v11
v_mul_f32 v74, v74, v12
v_mul_f32 v75, v75, v13
CV v76,v72,v73
CV v77,v74,v75
v_lshlrev_b32 v72, 16, v76
v_and_b32 v73, 0xffff0000, v76
v_lshlrev_b32 v74, 16, v77
v_and_b32 v75, 0xffff0000, v77
v_add_f32 v72, v72, v14
v_add_f32 v73, v73, v15
v_add_f32 v74, v74, v42
v_add_f32 v75, v75, v43
CV v46,v72,v73
CV v47,v74,v75
buffer_store_dwordx2 v[46:47], v251, s[40:43], s80 offen offset:0 nt
s_bitcmp0_b32 s94, 8
s_cbranch_scc1 LQN3_%=
s_waitcnt vmcnt(1)
s_branch LQS_%=
LQN3_%=:
s_bitcmp0_b32 s94, 4
s_cbranch_scc1 LQN4_%=
MM v[72:75],v[64:67],a[192:195],0
MM v[72:75],v[64:67],a[196:199],v[72:75]
MM v[72:75],v[68:71],v[144:147],v[72:75]
MM v[72:75],v[68:71],a[212:215],v[72:75]
s_bitset0_b32 s17, 4
s_nop 6
CV v76,v72,v73
CV v77,v74,v75
ds_bpermute_b32 v76, v232, v76
ds_bpermute_b32 v77, v232, v77
v_lshlrev_b32 v10, 16, v8
v_and_b32 v11, 0xffff0000, v8
v_lshlrev_b32 v12, 16, v9
v_and_b32 v13, 0xffff0000, v9
v_lshlrev_b32 v14, 16, v40
v_and_b32 v15, 0xffff0000, v40
v_lshlrev_b32 v42, 16, v41
v_and_b32 v43, 0xffff0000, v41
s_add_u32 s80, s63, 0
s_waitcnt lgkmcnt(0)
v_lshlrev_b32 v72, 16, v76
v_and_b32 v73, 0xffff0000, v76
v_lshlrev_b32 v74, 16, v77
v_and_b32 v75, 0xffff0000, v77
v_mul_f32 v72, v72, v10
v_mul_f32 v73, v73, v11
v_mul_f32 v74, v74, v12
v_mul_f32 v75, v75, v13
CV v76,v72,v73
CV v77,v74,v75
v_lshlrev_b32 v72, 16, v76
v_and_b32 v73, 0xffff0000, v76
v_lshlrev_b32 v74, 16, v77
v_and_b32 v75, 0xffff0000, v77
v_add_f32 v72, v72, v14
v_add_f32 v73, v73, v15
v_add_f32 v74, v74, v42
v_add_f32 v75, v75, v43
CV v44,v72,v73
CV v45,v74,v75
buffer_store_dwordx2 v[44:45], v254, s[40:43], s80 offen offset:0 nt
s_bitcmp0_b32 s94, 8
s_cbranch_scc1 LQN4_%=
s_waitcnt vmcnt(1)
s_branch LQS_%=
LQN4_%=:
s_cmp_eq_u32 s17, 0
s_cbranch_scc1 LQE_%=
s_waitcnt vmcnt(0)
s_branch LQS_%=
LQE_%=:
s_endpgm
LYS_%=:
s_waitcnt vmcnt(15)
LYT_%=:
v_ashrrev_i32 v139, 31, v138
v_not_b32 v139, v139
v_or_b32 v139, 0x80000000, v139
v_xor_b32 v138, v138, v139
v_and_b32 v138, 0xffff0000, v138
v_mul_f32 v138, 0xbfb8aa3b, v138
EX v138,v138
v_pk_add_f32 v[160:161], v[160:161], v[196:197]
v_pk_add_f32 v[162:163], v[162:163], v[198:199]
v_pk_add_f32 v[164:165], v[164:165], v[200:201]
v_pk_add_f32 v[166:167], v[166:167], v[202:203]
v_pk_add_f32 v[168:169], v[168:169], v[204:205]
v_pk_add_f32 v[170:171], v[170:171], v[206:207]
v_pk_add_f32 v[172:173], v[172:173], v[208:209]
v_pk_add_f32 v[174:175], v[174:175], v[210:211]
v_pk_add_f32 v[176:177], v[176:177], v[212:213]
v_pk_add_f32 v[178:179], v[178:179], v[214:215]
v_pk_add_f32 v[180:181], v[180:181], v[216:217]
v_pk_add_f32 v[182:183], v[182:183], v[218:219]
v_pk_add_f32 v[184:185], v[184:185], v[220:221]
v_pk_add_f32 v[186:187], v[186:187], v[222:223]
v_pk_add_f32 v[188:189], v[188:189], v[96:97]
v_pk_add_f32 v[190:191], v[190:191], v[98:99]
v_pk_add_f32 v[192:193], v[192:193], v[100:101]
v_pk_add_f32 v[194:195], v[194:195], v[102:103]
v_pk_add_f32 v[160:161], v[160:161], v[192:193]
v_pk_add_f32 v[162:163], v[162:163], v[194:195]
v_add_f32 v138, 1.0, v138
v_rcp_f32 v138, v138
v_pk_add_f32 v[160:161], v[160:161], v[176:177]
v_pk_add_f32 v[162:163], v[162:163], v[178:179]
v_pk_add_f32 v[164:165], v[164:165], v[180:181]
v_pk_add_f32 v[166:167], v[166:167], v[182:183]
v_pk_add_f32 v[168:169], v[168:169], v[184:185]
v_pk_add_f32 v[170:171], v[170:171], v[186:187]
v_pk_add_f32 v[172:173], v[172:173], v[188:189]
v_pk_add_f32 v[174:175], v[174:175], v[190:191]
CV v138,v138,v138
v_and_b32 v138, 0xffff0000, v138
v_pk_add_f32 v[160:161], v[160:161], v[168:169]
v_pk_add_f32 v[162:163], v[162:163], v[170:171]
v_pk_add_f32 v[164:165], v[164:165], v[172:173]
v_pk_add_f32 v[166:167], v[166:167], v[174:175]
v_pk_add_f32 v[160:161], v[160:161], v[164:165]
v_pk_add_f32 v[162:163], v[162:163], v[166:167]
CV v164,v160,v161
CV v165,v162,v163
v_lshlrev_b32 v160, 16, v164
v_and_b32 v161, 0xffff0000, v164
v_lshlrev_b32 v162, 16, v165
v_and_b32 v163, 0xffff0000, v165
v_mul_f32 v160, v160, v138
v_mul_f32 v161, v161, v138
v_mul_f32 v162, v162, v138
v_mul_f32 v163, v163, v138
CV v164,v160,v161
CV v165,v162,v163
v_lshlrev_b32 v160, 16, v164
v_and_b32 v161, 0xffff0000, v164
v_lshlrev_b32 v162, 16, v165
v_and_b32 v163, 0xffff0000, v165
v_lshlrev_b32 v168, 16, v136
v_and_b32 v169, 0xffff0000, v136
v_lshlrev_b32 v170, 16, v137
v_and_b32 v171, 0xffff0000, v137
v_add_f32 v160, v160, v168
v_add_f32 v161, v161, v169
v_add_f32 v162, v162, v170
v_add_f32 v163, v163, v171
CV v164,v160,v161
CV v165,v162,v163
v_lshlrev_b32 v160, 16, v164
v_and_b32 v161, 0xffff0000, v164
v_lshlrev_b32 v162, 16, v165
v_and_b32 v163, 0xffff0000, v165
v_mul_f32 v168, 0xbfb8aa3b, v160
v_mul_f32 v169, 0xbfb8aa3b, v161
v_mul_f32 v170, 0xbfb8aa3b, v162
v_mul_f32 v171, 0xbfb8aa3b, v163
EX v168,v168
EX v169,v169
EX v170,v170
EX v171,v171
v_bfe_u32 v139, v248, 2, 6
v_and_b32 v132, 28, v139
v_add_f32 v168, 1.0, v168
v_add_f32 v169, 1.0, v169
v_add_f32 v170, 1.0, v170
v_add_f32 v171, 1.0, v171
v_rcp_f32 v168, v168
v_rcp_f32 v169, v169
v_rcp_f32 v170, v170
v_rcp_f32 v171, v171
s_and_b32 s77, s84, 1
s_lshl_b32 s76, s77, 5
v_add_u32 v139, s76, v139
s_xor_b32 s77, s77, 1
s_lshl_b32 s77, s77, 7
v_add_u32 v132, s77, v132
v_mul_f32 v168, v160, v168
v_mul_f32 v169, v161, v169
v_mul_f32 v170, v162, v170
v_mul_f32 v171, v163, v171
CV v172,v168,v169
CV v173,v170,v171
ds_bpermute_b32 v176, v139, v172 offset:0
ds_bpermute_b32 v177, v139, v173 offset:0
ds_bpermute_b32 v180, v139, v172 offset:64
ds_bpermute_b32 v181, v139, v173 offset:64
ds_bpermute_b32 v184, v132, v172 offset:0
ds_bpermute_b32 v185, v132, v173 offset:0
s_ff1_i32_b32 s76, s82
s_bitset0_b32 s82, s76
s_and_b32 s77, s79, 3
s_lshl_b32 s77, s77, 4
s_and_b32 s76, s76, 8
s_waitcnt lgkmcnt(0)
s_lshl_b64 vcc, 0xffff, s77
s_cmp_eq_u32 s76, 0
s_cselect_b64 exec, vcc, 0
v_mov_b32 v0, v176
v_mov_b32 v1, v177
v_mov_b32 v2, v180
v_mov_b32 v3, v181
s_cselect_b64 exec, 0, vcc
v_mov_b32 v4, v176
v_mov_b32 v5, v177
v_mov_b32 v6, v180
v_mov_b32 v7, v181
s_add_u32 s77, s77, s76
s_lshl_b64 exec, 0xff, s77
v_mov_b32 v8, v184
v_mov_b32 v9, v185
s_mov_b64 exec, -1
s_cmp_eq_u32 s82, 0
s_cbranch_scc1 LYE_%=
s_ff1_i32_b32 s76, s82
s_and_b32 s76, s76, 7
s_mov_b32 s79, s52
s_cmp_eq_u32 s76, 1
s_cselect_b32 s79, s53, s79
s_cmp_eq_u32 s76, 2
s_cselect_b32 s79, s54, s79
s_cmp_eq_u32 s76, 3
s_cselect_b32 s79, s55, s79
s_cmp_eq_u32 s76, 4
s_cselect_b32 s79, s56, s79
s_cmp_eq_u32 s76, 5
s_cselect_b32 s79, s57, s79
s_cmp_eq_u32 s76, 6
s_cselect_b32 s79, s58, s79
s_cmp_eq_u32 s76, 7
)ASM"
R"ASM(
s_cselect_b32 s79, s59, s79
s_mul_i32 s76, s76, 2560
s_mul_i32 s90, s84, 640
s_add_u32 s76, s76, s90
s_add_u32 s76, s76, 21659648
s_lshl_b32 s90, s79, 2
s_add_u32 s90, s90, 53248
buffer_load_dword v138, off, s[20:23], s90 sc1
s_mul_i32 s90, s79, 1280
s_mul_i32 s77, s84, 320
s_add_u32 s90, s90, s77
s_add_u32 s90, s90, 131072
v_lshrrev_b32 v139, 1, v248
s_mov_b32 exec_lo, -1
s_mov_b32 exec_hi, 255
buffer_load_dwordx2 v[136:137], v139, s[20:23], s90 offen offset:0 sc1
s_mov_b32 exec_lo, -1
s_mov_b32 exec_hi, 255
buffer_load_dwordx4 v[160:163], v248, s[20:23], s76 offen offset:0 sc1
s_add_u32 s76, s76, 20480
buffer_load_dwordx4 v[164:167], v248, s[20:23], s76 offen offset:0 sc1
s_add_u32 s76, s76, 20480
buffer_load_dwordx4 v[168:171], v248, s[20:23], s76 offen offset:0 sc1
s_add_u32 s76, s76, 20480
buffer_load_dwordx4 v[172:175], v248, s[20:23], s76 offen offset:0 sc1
s_add_u32 s76, s76, 20480
buffer_load_dwordx4 v[176:179], v248, s[20:23], s76 offen offset:0 sc1
s_add_u32 s76, s76, 20480
buffer_load_dwordx4 v[180:183], v248, s[20:23], s76 offen offset:0 sc1
s_add_u32 s76, s76, 20480
buffer_load_dwordx4 v[184:187], v248, s[20:23], s76 offen offset:0 sc1
s_add_u32 s76, s76, 20480
buffer_load_dwordx4 v[188:191], v248, s[20:23], s76 offen offset:0 sc1
s_add_u32 s76, s76, 20480
buffer_load_dwordx4 v[192:195], v248, s[20:23], s76 offen offset:0 sc1
s_add_u32 s76, s76, 20480
buffer_load_dwordx4 v[196:199], v248, s[20:23], s76 offen offset:0 sc1
s_add_u32 s76, s76, 20480
buffer_load_dwordx4 v[200:203], v248, s[20:23], s76 offen offset:0 sc1
s_add_u32 s76, s76, 20480
buffer_load_dwordx4 v[204:207], v248, s[20:23], s76 offen offset:0 sc1
s_add_u32 s76, s76, 20480
buffer_load_dwordx4 v[208:211], v248, s[20:23], s76 offen offset:0 sc1
s_add_u32 s76, s76, 20480
buffer_load_dwordx4 v[212:215], v248, s[20:23], s76 offen offset:0 sc1
s_add_u32 s76, s76, 20480
buffer_load_dwordx4 v[216:219], v248, s[20:23], s76 offen offset:0 sc1
s_add_u32 s76, s76, 20480
buffer_load_dwordx4 v[220:223], v248, s[20:23], s76 offen offset:0 sc1
s_add_u32 s76, s76, 20480
buffer_load_dwordx4 v[96:99], v248, s[20:23], s76 offen offset:0 sc1
s_add_u32 s76, s76, 20480
buffer_load_dwordx4 v[100:103], v248, s[20:23], s76 offen offset:0 sc1
s_mov_b64 exec, -1
s_waitcnt vmcnt(0)
s_branch LYT_%=
.fill 403, 4, 0xbf800000
LXP_%=:
LPG26_%=:
s_cbranch_execz LTR1_%=
v_mov_b32 v250, s69
s_mov_b32 s76, 32768
s_mov_b64 exec, 1
buffer_store_dword v250, off, s[20:23], s76 sc1
s_mov_b64 exec, -1
s_add_u32 s79, %[slds], 154112
s_lshl_b32 s80, %[swave], 2
MM v[64:67],a[192:195],v[160:163],v[128:131]
s_add_u32 s64, s64, 16384
TR a[128:129],v232 offset:0
s_add_u32 s81, s64, 896
MM v[64:67],a[196:199],v[176:179],v[64:67]
TR a[130:131],v232 offset:4096
TR a[132:133],v232 offset:8192
MM v[64:67],a[200:203],v[192:195],v[64:67]
TR a[134:135],v232 offset:12288
TR a[136:137],v233 offset:0
MM v[64:67],a[204:207],v[208:211],v[64:67]
TR a[138:139],v233 offset:4096
v_add_f32 v246, v236, v237
MM v[68:71],a[220:223],v[208:211],v[128:131]
TR a[140:141],v233 offset:8192
v_cmp_nge_f32 vcc, s71, v246
MM v[68:71],a[216:219],v[192:195],v[68:71]
s_cbranch_vccnz LSITE4_%=
LBACK4_%=:
TR a[142:143],v233 offset:12288
TR a[144:145],v232 offset:512
MM v[68:71],a[212:215],v[176:179],v[68:71]
s_mov_b32 m0, s67
TR a[146:147],v232 offset:4608
TR a[148:149],v232 offset:8704
MM v[68:71],a[208:211],v[160:163],v[68:71]
buffer_load_dwordx4 v252, s[36:39], s66 offen offset:0 lds
MM v[76:79],a[208:211],v[164:167],v[132:135]
s_add_u32 s66, s66, 55296
EX v64,v64
MM v[76:79],a[212:215],v[180:183],v[76:79]
s_min_u32 s66, s66, s85
EX v65,v65
MM v[76:79],a[216:219],v[196:199],v[76:79]
s_add_u32 s67, s67, 1024
TR a[150:151],v232 offset:12800
CV v64,v64,v65
MM v[76:79],a[220:223],v[212:215],v[76:79]
s_min_u32 s67, s67, s86
EX v66,v66
MM v[72:75],a[204:207],v[212:215],v[132:135]
EX v67,v67
MM v[72:75],a[200:203],v[196:199],v[72:75]
TR a[152:153],v233 offset:512
CV v65,v66,v67
MM v[72:75],a[196:199],v[180:183],v[72:75]
EX v68,v68
MM v[72:75],a[192:195],v[164:167],v[72:75]
EX v69,v69
MM v[80:83],a[192:195],v[168:171],v[136:139]
TR a[154:155],v233 offset:4608
CV v66,v68,v69
MM v[80:83],a[196:199],v[184:187],v[80:83]
EX v70,v70
MM v[80:83],a[200:203],v[200:203],v[80:83]
EX v71,v71
MM v[80:83],a[204:207],v[216:219],v[80:83]
TR a[156:157],v233 offset:8704
CV v67,v70,v71
MM v[84:87],a[220:223],v[216:219],v[136:139]
EX v72,v72
MM v[84:87],a[216:219],v[200:203],v[84:87]
TR a[158:159],v233 offset:12800
TR a[160:161],v232 offset:1024
MM v[84:87],a[212:215],v[184:187],v[84:87]
EX v73,v73
MM v[84:87],a[208:211],v[168:171],v[84:87]
TR a[162:163],v232 offset:5120
CV v72,v72,v73
MM v[92:95],a[208:211],v[172:175],v[140:143]
EX v74,v74
MM v[92:95],a[212:215],v[188:191],v[92:95]
TR a[164:165],v232 offset:9216
TR a[166:167],v232 offset:13312
MM v[92:95],a[216:219],v[204:207],v[92:95]
EX v75,v75
MM v[92:95],a[220:223],v[220:223],v[92:95]
TR a[168:169],v233 offset:1024
CV v73,v74,v75
MM v[88:91],a[204:207],v[220:223],v[140:143]
EX v76,v76
MM v[88:91],a[200:203],v[204:207],v[88:91]
EX v77,v77
MM v[88:91],a[196:199],v[188:191],v[88:91]
TR a[170:171],v233 offset:5120
CV v74,v76,v77
MM v[88:91],a[192:195],v[172:175],v[88:91]
EX v78,v78
MM v[96:99],a[224:227],v[160:163],v[128:131]
TR a[172:173],v233 offset:9216
TR a[174:175],v233 offset:13312
MM v[96:99],a[228:231],v[176:179],v[96:99]
EX v79,v79
MM v[96:99],a[232:235],v[192:195],v[96:99]
TR a[176:177],v232 offset:1536
CV v75,v78,v79
MM v[96:99],a[236:239],v[208:211],v[96:99]
EX v80,v80
MM v[100:103],a[252:255],v[208:211],v[128:131]
EX v81,v81
MM v[100:103],a[248:251],v[192:195],v[100:103]
TR a[178:179],v232 offset:5632
CV v80,v80,v81
MM v[100:103],a[244:247],v[176:179],v[100:103]
EX v82,v82
MM v[100:103],a[240:243],v[160:163],v[100:103]
TR a[180:181],v232 offset:9728
TR a[182:183],v232 offset:13824
MM v[108:111],a[240:243],v[164:167],v[132:135]
v_add_u32 v48, s78, v248
v_mov_b32 v49, s79
MM v[108:111],a[244:247],v[180:183],v[108:111]
EX v83,v83
MM v[108:111],a[248:251],v[196:199],v[108:111]
TR a[184:185],v233 offset:1536
CV v81,v82,v83
MM v[108:111],a[252:255],v[212:215],v[108:111]
EX v84,v84
MM v[104:107],a[236:239],v[212:215],v[132:135]
EX v85,v85
MM v[104:107],a[232:235],v[196:199],v[104:107]
TR a[186:187],v233 offset:5632
CV v82,v84,v85
MM v[104:107],a[228:231],v[180:183],v[104:107]
EX v86,v86
MM v[104:107],a[224:227],v[164:167],v[104:107]
TR a[188:189],v233 offset:9728
TR a[190:191],v233 offset:13824
MM v[112:115],a[224:227],v[168:171],v[136:139]
EX v87,v87
MM v[112:115],a[228:231],v[184:187],v[112:115]
CV v83,v86,v87
MM v[112:115],a[232:235],v[200:203],v[112:115]
EX v88,v88
MM v[112:115],a[236:239],v[216:219],v[112:115]
EX v89,v89
MM v[116:119],a[252:255],v[216:219],v[136:139]
EX v90,v90
MM v[116:119],a[248:251],v[200:203],v[116:119]
EX v91,v91
MM v[116:119],a[244:247],v[184:187],v[116:119]
EX v92,v92
MM v[116:119],a[240:243],v[168:171],v[116:119]
EX v93,v93
MM v[124:127],a[240:243],v[172:175],v[140:143]
EX v94,v94
MM v[124:127],a[244:247],v[188:191],v[124:127]
EX v95,v95
MM v[124:127],a[248:251],v[204:207],v[124:127]
EX v96,v96
MM v[124:127],a[252:255],v[220:223],v[124:127]
EX v97,v97
MM v[120:123],a[236:239],v[220:223],v[140:143]
EX v98,v98
MM v[120:123],a[232:235],v[204:207],v[120:123]
EX v99,v99
MM v[120:123],a[228:231],v[188:191],v[120:123]
EX v100,v100
MM v[120:123],a[224:227],v[172:175],v[120:123]
s_waitcnt vmcnt(13) lgkmcnt(0)
s_barrier
MM a[0:3],a[128:131],v[0:3],a[0:3]
s_add_u32 m0, s65, 0
RD v[32:35],v48 offset:0
RD v[36:39],v48 offset:1024
MM a[0:3],a[132:135],v[4:7],a[0:3]
s_mov_b32 s92, -1
CV v88,v88,v89
CV v89,v90,v91
MM a[8:11],a[132:135],v[12:15],a[8:11]
RD a[192:195],v224 offset:32768
CV v90,v92,v93
MM a[8:11],a[128:131],v[8:11],a[8:11]
RD a[196:199],v225 offset:32768
CV v91,v94,v95
MM a[16:19],a[128:131],v[16:19],a[16:19]
s_waitcnt lgkmcnt(2)
v_sub_u32 v32, s92, v32
v_sub_u32 v33, s92, v33
MM a[16:19],a[132:135],v[20:23],a[16:19]
v_sub_u32 v34, s92, v34
v_sub_u32 v35, s92, v35
MM a[24:27],a[132:135],v[28:31],a[24:27]
v_sub_u32 v36, s92, v36
v_sub_u32 v37, s92, v37
MM a[24:27],a[128:131],v[24:27],a[24:27]
v_sub_u32 v38, s92, v38
v_sub_u32 v39, s92, v39
MM a[28:31],a[136:139],v[24:27],a[28:31]
v_min3_u32 v52, v32, v33, v34
v_min3_u32 v51, v35, v36, v37
MM a[28:31],a[140:143],v[28:31],a[28:31]
v_min3_u32 v52, v52, v38, v39
v_min_u32 v52, v52, v51
MM a[20:23],a[140:143],v[20:23],a[20:23]
s_nop 0
v_min_u32_dpp v52, v52, v52 row_shr:1 row_mask:0xf bank_mask:0xf
CV v68,v96,v97
MM a[20:23],a[136:139],v[16:19],a[20:23]
RD a[200:203],v226 offset:32768
v_min_u32_dpp v52, v52, v52 row_shr:2 row_mask:0xf bank_mask:0xf
MM a[12:15],a[136:139],v[8:11],a[12:15]
s_nop 0
v_min_u32_dpp v52, v52, v52 row_shr:4 row_mask:0xf bank_mask:0xf
CV v69,v98,v99
MM a[12:15],a[140:143],v[12:15],a[12:15]
RD a[204:207],v227 offset:32768
v_min_u32_dpp v52, v52, v52 row_shr:8 row_mask:0xf bank_mask:0xf
MM a[4:7],a[140:143],v[4:7],a[4:7]
RD a[208:211],v224 offset:36864
v_min_u32_dpp v52, v52, v52 row_bcast:15 row_mask:0xa bank_mask:0xf
MM a[4:7],a[136:139],v[0:3],a[4:7]
RD a[212:215],v225 offset:36864
v_min_u32_dpp v52, v52, v52 row_bcast:31 row_mask:0xc bank_mask:0xf
MM a[32:35],a[144:147],v[0:3],a[32:35]
v_readlane_b32 s76, v52, 63
v_lshl_add_u32 v49, s80, 1, v49
MM a[32:35],a[148:151],v[4:7],a[32:35]
s_add_u32 s76, s76, 1
buffer_load_dwordx4 v228, s[8:11], s64 offen offset:0 lds
v_subrev_u32 v32, s76, v32
v_subrev_u32 v33, s76, v33
EX v101,v101
MM a[40:43],a[148:151],v[12:15],a[40:43]
v_subrev_u32 v34, s76, v34
v_subrev_u32 v35, s76, v35
MM a[40:43],a[144:147],v[8:11],a[40:43]
v_subrev_u32 v36, s76, v36
v_subrev_u32 v37, s76, v37
EX v102,v102
MM a[48:51],a[144:147],v[16:19],a[48:51]
v_subrev_u32 v38, s76, v38
v_subrev_u32 v39, s76, v39
EX v103,v103
MM a[48:51],a[148:151],v[20:23],a[48:51]
v_min3_u32 v53, v32, v33, v34
v_min3_u32 v51, v35, v36, v37
MM a[56:59],a[148:151],v[28:31],a[56:59]
v_min3_u32 v53, v53, v38, v39
v_min_u32 v53, v53, v51
EX v104,v104
MM a[56:59],a[144:147],v[24:27],a[56:59]
v_min_u32_dpp v53, v53, v53 row_shr:1 row_mask:0xf bank_mask:0xf
CV v70,v100,v101
MM a[60:63],a[152:155],v[24:27],a[60:63]
v_min_u32_dpp v53, v53, v53 row_shr:2 row_mask:0xf bank_mask:0xf
CV v71,v102,v103
EX v105,v105
MM a[60:63],a[156:159],v[28:31],a[60:63]
buffer_load_dwordx4 v229, s[8:11], s64 offen offset:1024 lds
v_min_u32_dpp v53, v53, v53 row_shr:4 row_mask:0xf bank_mask:0xf
CV v76,v104,v105
MM a[52:55],a[156:159],v[20:23],a[52:55]
RD a[216:219],v226 offset:36864
v_min_u32_dpp v53, v53, v53 row_shr:8 row_mask:0xf bank_mask:0xf
EX v106,v106
MM a[52:55],a[152:155],v[16:19],a[52:55]
RD a[220:223],v227 offset:36864
v_min_u32_dpp v53, v53, v53 row_bcast:15 row_mask:0xa bank_mask:0xf
MM a[44:47],a[152:155],v[8:11],a[44:47]
RD a[224:227],v224 offset:40960
v_min_u32_dpp v53, v53, v53 row_bcast:31 row_mask:0xc bank_mask:0xf
EX v107,v107
MM a[44:47],a[156:159],v[12:15],a[44:47]
v_add_u32 v53, s76, v53
CV v77,v106,v107
MM a[36:39],a[156:159],v[4:7],a[36:39]
s_mov_b32 exec_lo, 0
s_mov_b32 exec_hi, 0x80000000
ds_write_b64 v49, v[52:53] offset:0
s_mov_b64 exec, -1
EX v108,v108
MM a[36:39],a[152:155],v[0:3],a[36:39]
EX v109,v109
MM a[64:67],a[160:163],v[0:3],a[64:67]
buffer_load_dwordx4 v230, s[8:11], s64 offen offset:2048 lds
MM a[64:67],a[164:167],v[4:7],a[64:67]
EX v110,v110
MM a[72:75],a[164:167],v[12:15],a[72:75]
EX v111,v111
MM a[72:75],a[160:163],v[8:11],a[72:75]
buffer_load_dwordx4 v231, s[8:11], s64 offen offset:3072 lds
MM a[80:83],a[160:163],v[16:19],a[80:83]
s_add_u32 m0, s65, 65536
EX v112,v112
MM a[80:83],a[164:167],v[20:23],a[80:83]
CV v78,v108,v109
CV v79,v110,v111
MM a[88:91],a[164:167],v[28:31],a[88:91]
EX v113,v113
MM a[88:91],a[160:163],v[24:27],a[88:91]
EX v114,v114
MM a[92:95],a[168:171],v[24:27],a[92:95]
EX v115,v115
MM a[92:95],a[172:175],v[28:31],a[92:95]
buffer_load_dwordx4 v235, s[12:15], s81 offen offset:0 lds
MM a[84:87],a[172:175],v[20:23],a[84:87]
RD a[228:231],v225 offset:40960
CV v84,v112,v113
MM a[84:87],a[168:171],v[16:19],a[84:87]
EX v116,v116
MM a[76:79],a[168:171],v[8:11],a[76:79]
)ASM"
R"ASM(
EX v117,v117
MM a[76:79],a[172:175],v[12:15],a[76:79]
RD a[232:235],v226 offset:40960
CV v85,v114,v115
MM a[68:71],a[172:175],v[4:7],a[68:71]
EX v118,v118
MM a[68:71],a[168:171],v[0:3],a[68:71]
RD a[236:239],v227 offset:40960
CV v86,v116,v117
MM a[96:99],a[176:179],v[0:3],a[96:99]
EX v119,v119
MM a[96:99],a[180:183],v[4:7],a[96:99]
buffer_load_dwordx4 v235, s[12:15], s64 offen offset:1024 lds
MM a[104:107],a[180:183],v[12:15],a[104:107]
EX v120,v120
MM a[104:107],a[176:179],v[8:11],a[104:107]
RD a[240:243],v224 offset:45056
CV v87,v118,v119
MM a[112:115],a[176:179],v[16:19],a[112:115]
EX v121,v121
MM a[112:115],a[180:183],v[20:23],a[112:115]
EX v122,v122
MM a[120:123],a[180:183],v[28:31],a[120:123]
RD a[244:247],v225 offset:45056
CV v92,v120,v121
MM a[120:123],a[176:179],v[24:27],a[120:123]
EX v123,v123
MM a[124:127],a[184:187],v[24:27],a[124:127]
buffer_load_dwordx4 v235, s[12:15], s81 offen offset:2048 lds
MM a[124:127],a[188:191],v[28:31],a[124:127]
EX v124,v124
MM a[116:119],a[188:191],v[20:23],a[116:119]
RD a[248:251],v226 offset:45056
CV v93,v122,v123
MM a[116:119],a[184:187],v[16:19],a[116:119]
EX v125,v125
MM a[108:111],a[184:187],v[8:11],a[108:111]
EX v126,v126
MM a[108:111],a[188:191],v[12:15],a[108:111]
RD a[252:255],v227 offset:45056
CV v94,v124,v125
MM a[100:103],a[188:191],v[4:7],a[100:103]
EX v127,v127
MM a[100:103],a[184:187],v[0:3],a[100:103]
buffer_load_dwordx4 v235, s[12:15], s64 offen offset:3072 lds
v_smfmac_f32_16x16x64_bf16 v[236:239], v[144:147], v[64:71], v234
CV v95,v126,v127
v_smfmac_f32_16x16x64_bf16 v[236:239], v[148:151], v[72:79], v234
v_smfmac_f32_16x16x64_bf16 v[236:239], v[152:155], v[80:87], v234
v_smfmac_f32_16x16x64_bf16 v[236:239], v[156:159], v[88:95], v234
s_waitcnt lgkmcnt(0)
MM v[0:3],a[192:195],v[160:163],v[128:131]
s_add_u32 s64, s64, 16384
TR a[128:129],v232 offset:16384
s_add_u32 s81, s64, 896
MM v[0:3],a[196:199],v[176:179],v[0:3]
TR a[130:131],v232 offset:20480
TR a[132:133],v232 offset:24576
MM v[0:3],a[200:203],v[192:195],v[0:3]
TR a[134:135],v232 offset:28672
TR a[136:137],v233 offset:16384
MM v[0:3],a[204:207],v[208:211],v[0:3]
TR a[138:139],v233 offset:20480
v_add_f32 v247, v236, v237
MM v[4:7],a[220:223],v[208:211],v[128:131]
TR a[140:141],v233 offset:24576
v_cmp_nge_f32 vcc, s71, v247
MM v[4:7],a[216:219],v[192:195],v[4:7]
s_cbranch_vccnz LSITE5_%=
LBACK5_%=:
TR a[142:143],v233 offset:28672
TR a[144:145],v232 offset:16896
MM v[4:7],a[212:215],v[176:179],v[4:7]
s_mov_b32 m0, s67
TR a[146:147],v232 offset:20992
TR a[148:149],v232 offset:25088
MM v[4:7],a[208:211],v[160:163],v[4:7]
buffer_load_dwordx4 v252, s[36:39], s66 offen offset:0 lds
MM v[12:15],a[208:211],v[164:167],v[132:135]
s_add_u32 s66, s66, 55296
EX v0,v0
MM v[12:15],a[212:215],v[180:183],v[12:15]
s_min_u32 s66, s66, s85
EX v1,v1
MM v[12:15],a[216:219],v[196:199],v[12:15]
s_add_u32 s67, s67, 1024
TR a[150:151],v232 offset:29184
CV v0,v0,v1
MM v[12:15],a[220:223],v[212:215],v[12:15]
s_min_u32 s67, s67, s86
EX v2,v2
MM v[8:11],a[204:207],v[212:215],v[132:135]
EX v3,v3
MM v[8:11],a[200:203],v[196:199],v[8:11]
TR a[152:153],v233 offset:16896
CV v1,v2,v3
MM v[8:11],a[196:199],v[180:183],v[8:11]
EX v4,v4
MM v[8:11],a[192:195],v[164:167],v[8:11]
EX v5,v5
MM v[16:19],a[192:195],v[168:171],v[136:139]
TR a[154:155],v233 offset:20992
CV v2,v4,v5
MM v[16:19],a[196:199],v[184:187],v[16:19]
EX v6,v6
MM v[16:19],a[200:203],v[200:203],v[16:19]
EX v7,v7
MM v[16:19],a[204:207],v[216:219],v[16:19]
TR a[156:157],v233 offset:25088
CV v3,v6,v7
MM v[20:23],a[220:223],v[216:219],v[136:139]
EX v8,v8
MM v[20:23],a[216:219],v[200:203],v[20:23]
TR a[158:159],v233 offset:29184
TR a[160:161],v232 offset:17408
MM v[20:23],a[212:215],v[184:187],v[20:23]
EX v9,v9
MM v[20:23],a[208:211],v[168:171],v[20:23]
TR a[162:163],v232 offset:21504
CV v8,v8,v9
MM v[28:31],a[208:211],v[172:175],v[140:143]
EX v10,v10
MM v[28:31],a[212:215],v[188:191],v[28:31]
TR a[164:165],v232 offset:25600
TR a[166:167],v232 offset:29696
MM v[28:31],a[216:219],v[204:207],v[28:31]
EX v11,v11
MM v[28:31],a[220:223],v[220:223],v[28:31]
TR a[168:169],v233 offset:17408
CV v9,v10,v11
MM v[24:27],a[204:207],v[220:223],v[140:143]
EX v12,v12
MM v[24:27],a[200:203],v[204:207],v[24:27]
EX v13,v13
MM v[24:27],a[196:199],v[188:191],v[24:27]
TR a[170:171],v233 offset:21504
CV v10,v12,v13
MM v[24:27],a[192:195],v[172:175],v[24:27]
EX v14,v14
MM v[32:35],a[224:227],v[160:163],v[128:131]
TR a[172:173],v233 offset:25600
TR a[174:175],v233 offset:29696
MM v[32:35],a[228:231],v[176:179],v[32:35]
EX v15,v15
MM v[32:35],a[232:235],v[192:195],v[32:35]
TR a[176:177],v232 offset:17920
CV v11,v14,v15
MM v[32:35],a[236:239],v[208:211],v[32:35]
EX v16,v16
MM v[36:39],a[252:255],v[208:211],v[128:131]
EX v17,v17
MM v[36:39],a[248:251],v[192:195],v[36:39]
TR a[178:179],v232 offset:22016
CV v16,v16,v17
MM v[36:39],a[244:247],v[176:179],v[36:39]
EX v18,v18
MM v[36:39],a[240:243],v[160:163],v[36:39]
TR a[180:181],v232 offset:26112
TR a[182:183],v232 offset:30208
MM v[44:47],a[240:243],v[164:167],v[132:135]
v_add_u32 v112, s78, v248
v_mov_b32 v113, s79
MM v[44:47],a[244:247],v[180:183],v[44:47]
EX v19,v19
MM v[44:47],a[248:251],v[196:199],v[44:47]
TR a[184:185],v233 offset:17920
CV v17,v18,v19
MM v[44:47],a[252:255],v[212:215],v[44:47]
EX v20,v20
MM v[40:43],a[236:239],v[212:215],v[132:135]
EX v21,v21
MM v[40:43],a[232:235],v[196:199],v[40:43]
TR a[186:187],v233 offset:22016
CV v18,v20,v21
MM v[40:43],a[228:231],v[180:183],v[40:43]
EX v22,v22
MM v[40:43],a[224:227],v[164:167],v[40:43]
TR a[188:189],v233 offset:26112
TR a[190:191],v233 offset:30208
MM v[48:51],a[224:227],v[168:171],v[136:139]
EX v23,v23
MM v[48:51],a[228:231],v[184:187],v[48:51]
CV v19,v22,v23
MM v[48:51],a[232:235],v[200:203],v[48:51]
EX v24,v24
MM v[48:51],a[236:239],v[216:219],v[48:51]
EX v25,v25
MM v[52:55],a[252:255],v[216:219],v[136:139]
EX v26,v26
MM v[52:55],a[248:251],v[200:203],v[52:55]
EX v27,v27
MM v[52:55],a[244:247],v[184:187],v[52:55]
EX v28,v28
MM v[52:55],a[240:243],v[168:171],v[52:55]
EX v29,v29
MM v[60:63],a[240:243],v[172:175],v[140:143]
EX v30,v30
MM v[60:63],a[244:247],v[188:191],v[60:63]
EX v31,v31
MM v[60:63],a[248:251],v[204:207],v[60:63]
EX v32,v32
MM v[60:63],a[252:255],v[220:223],v[60:63]
EX v33,v33
MM v[56:59],a[236:239],v[220:223],v[140:143]
EX v34,v34
MM v[56:59],a[232:235],v[204:207],v[56:59]
EX v35,v35
MM v[56:59],a[228:231],v[188:191],v[56:59]
EX v36,v36
MM v[56:59],a[224:227],v[172:175],v[56:59]
s_waitcnt vmcnt(13) lgkmcnt(0)
s_barrier
MM a[0:3],a[128:131],v[64:67],a[0:3]
s_add_u32 m0, s65, 16384
RD v[104:107],v113 offset:0
RD v[108:111],v113 offset:16
MM a[0:3],a[132:135],v[68:71],a[0:3]
RD v[96:99],v112 offset:0
RD v[100:103],v112 offset:1024
MM a[8:11],a[132:135],v[76:79],a[8:11]
CV v24,v24,v25
CV v25,v26,v27
MM a[8:11],a[128:131],v[72:75],a[8:11]
RD a[192:195],v224 offset:49152
CV v26,v28,v29
MM a[16:19],a[128:131],v[80:83],a[16:19]
RD a[196:199],v225 offset:49152
CV v27,v30,v31
MM a[16:19],a[132:135],v[84:87],a[16:19]
s_waitcnt lgkmcnt(4)
v_min3_u32 v114, v104, v105, v106
v_min3_u32 v115, v107, v108, v109
MM a[24:27],a[132:135],v[92:95],a[24:27]
v_min3_u32 v114, v114, v110, v111
v_min_u32 v114, v114, v115
MM a[24:27],a[128:131],v[88:91],a[24:27]
v_readfirstlane_b32 s76, v114
CV v4,v32,v33
MM a[28:31],a[136:139],v[88:91],a[28:31]
s_sub_u32 s92, s92, s76
buffer_load_dwordx4 v228, s[8:11], s64 offen offset:0 lds
MM a[28:31],a[140:143],v[92:95],a[28:31]
s_add_u32 s76, s76, 1
v_subrev_u32 v104, s76, v104
v_subrev_u32 v105, s76, v105
MM a[20:23],a[140:143],v[84:87],a[20:23]
v_subrev_u32 v106, s76, v106
v_subrev_u32 v107, s76, v107
MM a[20:23],a[136:139],v[80:83],a[20:23]
v_subrev_u32 v108, s76, v108
v_subrev_u32 v109, s76, v109
MM a[12:15],a[136:139],v[72:75],a[12:15]
v_subrev_u32 v110, s76, v110
v_subrev_u32 v111, s76, v111
MM a[12:15],a[140:143],v[76:79],a[12:15]
s_and_b32 s77, s92, 65535
RD a[200:203],v226 offset:49152
CV v5,v34,v35
MM a[4:7],a[140:143],v[68:71],a[4:7]
s_sub_u32 s52, 65535, s77
v_min3_u32 v114, v104, v105, v106
v_min3_u32 v115, v107, v108, v109
MM a[4:7],a[136:139],v[64:67],a[4:7]
v_min3_u32 v114, v114, v110, v111
v_min_u32 v114, v114, v115
MM a[32:35],a[144:147],v[64:67],a[32:35]
RD a[204:207],v227 offset:49152
v_readfirstlane_b32 s76, v114
MM a[32:35],a[148:151],v[68:71],a[32:35]
s_sub_u32 s92, s92, s76
EX v37,v37
MM a[40:43],a[148:151],v[76:79],a[40:43]
s_sub_u32 s92, s92, 1
EX v38,v38
MM a[40:43],a[144:147],v[72:75],a[40:43]
s_and_b32 s77, s92, 65535
EX v39,v39
MM a[48:51],a[144:147],v[80:83],a[48:51]
s_sub_u32 s53, 65535, s77
buffer_load_dwordx4 v229, s[8:11], s64 offen offset:1024 lds
MM a[48:51],a[148:151],v[84:87],a[48:51]
s_sub_u32 s92, s92, 1
EX v40,v40
MM a[56:59],a[148:151],v[92:95],a[56:59]
s_waitcnt lgkmcnt(4)
v_sub_u32 v96, s92, v96
v_sub_u32 v97, s92, v97
MM a[56:59],a[144:147],v[88:91],a[56:59]
v_sub_u32 v98, s92, v98
v_sub_u32 v99, s92, v99
MM a[60:63],a[152:155],v[88:91],a[60:63]
v_sub_u32 v100, s92, v100
v_sub_u32 v101, s92, v101
EX v41,v41
MM a[60:63],a[156:159],v[92:95],a[60:63]
v_sub_u32 v102, s92, v102
v_sub_u32 v103, s92, v103
LPG27_%=:
s_cbranch_execz LTR1_%=
MM a[52:55],a[156:159],v[84:87],a[52:55]
v_min3_u32 v116, v96, v97, v98
v_min3_u32 v115, v99, v100, v101
EX v42,v42
MM a[52:55],a[152:155],v[80:83],a[52:55]
v_min3_u32 v116, v116, v102, v103
v_min_u32 v116, v116, v115
CV v6,v36,v37
MM a[44:47],a[152:155],v[72:75],a[44:47]
v_min_u32_dpp v116, v116, v116 row_shr:1 row_mask:0xf bank_mask:0xf
CV v7,v38,v39
EX v43,v43
MM a[44:47],a[156:159],v[76:79],a[44:47]
RD a[208:211],v224 offset:53248
v_min_u32_dpp v116, v116, v116 row_shr:2 row_mask:0xf bank_mask:0xf
MM a[36:39],a[156:159],v[68:71],a[36:39]
s_nop 0
v_min_u32_dpp v116, v116, v116 row_shr:4 row_mask:0xf bank_mask:0xf
CV v12,v40,v41
EX v44,v44
MM a[36:39],a[152:155],v[64:67],a[36:39]
RD a[212:215],v225 offset:53248
v_min_u32_dpp v116, v116, v116 row_shr:8 row_mask:0xf bank_mask:0xf
MM a[64:67],a[160:163],v[64:67],a[64:67]
s_nop 0
v_min_u32_dpp v116, v116, v116 row_bcast:15 row_mask:0xa bank_mask:0xf
CV v13,v42,v43
EX v45,v45
MM a[64:67],a[164:167],v[68:71],a[64:67]
RD a[216:219],v226 offset:53248
v_min_u32_dpp v116, v116, v116 row_bcast:31 row_mask:0xc bank_mask:0xf
MM a[72:75],a[164:167],v[76:79],a[72:75]
buffer_load_dwordx4 v230, s[8:11], s64 offen offset:2048 lds
v_readlane_b32 s76, v116, 63
v_lshl_add_u32 v113, s80, 1, v113
EX v46,v46
MM a[72:75],a[160:163],v[72:75],a[72:75]
s_add_u32 s76, s76, 1
v_subrev_u32 v96, s76, v96
v_subrev_u32 v97, s76, v97
EX v47,v47
MM a[80:83],a[160:163],v[80:83],a[80:83]
v_subrev_u32 v98, s76, v98
v_subrev_u32 v99, s76, v99
MM a[80:83],a[164:167],v[84:87],a[80:83]
v_subrev_u32 v100, s76, v100
v_subrev_u32 v101, s76, v101
EX v48,v48
MM a[88:91],a[164:167],v[92:95],a[88:91]
v_subrev_u32 v102, s76, v102
v_subrev_u32 v103, s76, v103
MM a[88:91],a[160:163],v[88:91],a[88:91]
v_min3_u32 v117, v96, v97, v98
v_min3_u32 v115, v99, v100, v101
EX v49,v49
MM a[92:95],a[168:171],v[88:91],a[92:95]
v_min3_u32 v117, v117, v102, v103
v_min_u32 v117, v117, v115
MM a[92:95],a[172:175],v[92:95],a[92:95]
s_nop 0
v_min_u32_dpp v117, v117, v117 row_shr:1 row_mask:0xf bank_mask:0xf
CV v14,v44,v45
EX v50,v50
MM a[84:87],a[172:175],v[84:87],a[84:87]
RD a[220:223],v227 offset:53248
buffer_load_dwordx4 v231, s[8:11], s64 offen offset:3072 lds
v_min_u32_dpp v117, v117, v117 row_shr:2 row_mask:0xf bank_mask:0xf
MM a[84:87],a[168:171],v[80:83],a[84:87]
s_add_u32 m0, s65, 81920
v_min_u32_dpp v117, v117, v117 row_shr:4 row_mask:0xf bank_mask:0xf
CV v15,v46,v47
EX v51,v51
MM a[76:79],a[168:171],v[72:75],a[76:79]
RD a[224:227],v224 offset:57344
v_min_u32_dpp v117, v117, v117 row_shr:8 row_mask:0xf bank_mask:0xf
MM a[76:79],a[172:175],v[76:79],a[76:79]
s_nop 0
v_min_u32_dpp v117, v117, v117 row_bcast:15 row_mask:0xa bank_mask:0xf
CV v20,v48,v49
EX v52,v52
MM a[68:71],a[172:175],v[68:71],a[68:71]
RD a[228:231],v225 offset:57344
v_min_u32_dpp v117, v117, v117 row_bcast:31 row_mask:0xc bank_mask:0xf
EX v53,v53
MM a[68:71],a[168:171],v[64:67],a[68:71]
v_add_u32 v117, s76, v117
)ASM"
R"ASM(
CV v21,v50,v51
MM a[96:99],a[176:179],v[64:67],a[96:99]
s_mov_b32 exec_lo, 0
s_mov_b32 exec_hi, 0x80000000
ds_write_b64 v113, v[116:117] offset:32
s_mov_b64 exec, -1
EX v54,v54
MM a[96:99],a[180:183],v[68:71],a[96:99]
buffer_load_dwordx4 v235, s[12:15], s81 offen offset:0 lds
MM a[104:107],a[180:183],v[76:79],a[104:107]
EX v55,v55
MM a[104:107],a[176:179],v[72:75],a[104:107]
EX v56,v56
MM a[112:115],a[176:179],v[80:83],a[112:115]
RD a[232:235],v226 offset:57344
CV v22,v52,v53
MM a[112:115],a[180:183],v[84:87],a[112:115]
EX v57,v57
MM a[120:123],a[180:183],v[92:95],a[120:123]
buffer_load_dwordx4 v235, s[12:15], s64 offen offset:1024 lds
MM a[120:123],a[176:179],v[88:91],a[120:123]
EX v58,v58
MM a[124:127],a[184:187],v[88:91],a[124:127]
RD a[236:239],v227 offset:57344
CV v23,v54,v55
MM a[124:127],a[188:191],v[92:95],a[124:127]
EX v59,v59
MM a[116:119],a[188:191],v[84:87],a[116:119]
EX v60,v60
MM a[116:119],a[184:187],v[80:83],a[116:119]
RD a[240:243],v224 offset:61440
CV v28,v56,v57
MM a[108:111],a[184:187],v[72:75],a[108:111]
EX v61,v61
MM a[108:111],a[188:191],v[76:79],a[108:111]
buffer_load_dwordx4 v235, s[12:15], s81 offen offset:2048 lds
MM a[100:103],a[188:191],v[68:71],a[100:103]
EX v62,v62
MM a[100:103],a[184:187],v[64:67],a[100:103]
RD a[244:247],v225 offset:61440
CV v29,v58,v59
v_smfmac_f32_16x16x64_bf16 v[236:239], v[144:147], v[0:7], v234
RD a[248:251],v226 offset:61440
RD a[252:255],v227 offset:61440
EX v63,v63
v_smfmac_f32_16x16x64_bf16 v[236:239], v[148:151], v[8:15], v234
CV v30,v60,v61
v_smfmac_f32_16x16x64_bf16 v[236:239], v[152:155], v[16:23], v234
CV v31,v62,v63
s_nop 1
v_smfmac_f32_16x16x64_bf16 v[236:239], v[156:159], v[24:31], v234
s_waitcnt lgkmcnt(0)
buffer_load_dwordx4 v235, s[12:15], s64 offen offset:3072 lds
MM v[64:67],a[192:195],v[160:163],v[128:131]
s_add_u32 s64, s64, 16384
TR a[128:129],v232 offset:32768
s_add_u32 s81, s64, 896
MM v[64:67],a[196:199],v[176:179],v[64:67]
TR a[130:131],v232 offset:36864
TR a[132:133],v232 offset:40960
MM v[64:67],a[200:203],v[192:195],v[64:67]
TR a[134:135],v232 offset:45056
TR a[136:137],v233 offset:32768
MM v[64:67],a[204:207],v[208:211],v[64:67]
TR a[138:139],v233 offset:36864
v_add_f32 v246, v236, v237
MM v[68:71],a[220:223],v[208:211],v[128:131]
TR a[140:141],v233 offset:40960
v_cmp_nge_f32 vcc, s71, v246
MM v[68:71],a[216:219],v[192:195],v[68:71]
s_cbranch_vccnz LSITE6_%=
LBACK6_%=:
TR a[142:143],v233 offset:45056
TR a[144:145],v232 offset:33280
MM v[68:71],a[212:215],v[176:179],v[68:71]
s_mov_b32 m0, s67
TR a[146:147],v232 offset:37376
TR a[148:149],v232 offset:41472
MM v[68:71],a[208:211],v[160:163],v[68:71]
buffer_load_dwordx4 v252, s[36:39], s66 offen offset:0 lds
MM v[76:79],a[208:211],v[164:167],v[132:135]
s_add_u32 s66, s66, 55296
EX v64,v64
MM v[76:79],a[212:215],v[180:183],v[76:79]
s_min_u32 s66, s66, s85
EX v65,v65
MM v[76:79],a[216:219],v[196:199],v[76:79]
s_add_u32 s67, s67, 1024
TR a[150:151],v232 offset:45568
CV v64,v64,v65
MM v[76:79],a[220:223],v[212:215],v[76:79]
s_min_u32 s67, s67, s86
EX v66,v66
MM v[72:75],a[204:207],v[212:215],v[132:135]
EX v67,v67
MM v[72:75],a[200:203],v[196:199],v[72:75]
TR a[152:153],v233 offset:33280
CV v65,v66,v67
MM v[72:75],a[196:199],v[180:183],v[72:75]
EX v68,v68
MM v[72:75],a[192:195],v[164:167],v[72:75]
EX v69,v69
MM v[80:83],a[192:195],v[168:171],v[136:139]
TR a[154:155],v233 offset:37376
CV v66,v68,v69
MM v[80:83],a[196:199],v[184:187],v[80:83]
EX v70,v70
MM v[80:83],a[200:203],v[200:203],v[80:83]
EX v71,v71
MM v[80:83],a[204:207],v[216:219],v[80:83]
TR a[156:157],v233 offset:41472
CV v67,v70,v71
MM v[84:87],a[220:223],v[216:219],v[136:139]
EX v72,v72
MM v[84:87],a[216:219],v[200:203],v[84:87]
TR a[158:159],v233 offset:45568
TR a[160:161],v232 offset:33792
MM v[84:87],a[212:215],v[184:187],v[84:87]
EX v73,v73
MM v[84:87],a[208:211],v[168:171],v[84:87]
TR a[162:163],v232 offset:37888
CV v72,v72,v73
MM v[92:95],a[208:211],v[172:175],v[140:143]
EX v74,v74
MM v[92:95],a[212:215],v[188:191],v[92:95]
TR a[164:165],v232 offset:41984
TR a[166:167],v232 offset:46080
MM v[92:95],a[216:219],v[204:207],v[92:95]
EX v75,v75
MM v[92:95],a[220:223],v[220:223],v[92:95]
TR a[168:169],v233 offset:33792
CV v73,v74,v75
MM v[88:91],a[204:207],v[220:223],v[140:143]
EX v76,v76
MM v[88:91],a[200:203],v[204:207],v[88:91]
EX v77,v77
MM v[88:91],a[196:199],v[188:191],v[88:91]
TR a[170:171],v233 offset:37888
CV v74,v76,v77
MM v[88:91],a[192:195],v[172:175],v[88:91]
EX v78,v78
MM v[96:99],a[224:227],v[160:163],v[128:131]
TR a[172:173],v233 offset:41984
TR a[174:175],v233 offset:46080
MM v[96:99],a[228:231],v[176:179],v[96:99]
EX v79,v79
MM v[96:99],a[232:235],v[192:195],v[96:99]
TR a[176:177],v232 offset:34304
CV v75,v78,v79
MM v[96:99],a[236:239],v[208:211],v[96:99]
EX v80,v80
MM v[100:103],a[252:255],v[208:211],v[128:131]
EX v81,v81
MM v[100:103],a[248:251],v[192:195],v[100:103]
TR a[178:179],v232 offset:38400
CV v80,v80,v81
MM v[100:103],a[244:247],v[176:179],v[100:103]
EX v82,v82
MM v[100:103],a[240:243],v[160:163],v[100:103]
TR a[180:181],v232 offset:42496
TR a[182:183],v232 offset:46592
MM v[108:111],a[240:243],v[164:167],v[132:135]
v_add_u32 v48, s78, v248
v_mov_b32 v49, s79
MM v[108:111],a[244:247],v[180:183],v[108:111]
EX v83,v83
MM v[108:111],a[248:251],v[196:199],v[108:111]
TR a[184:185],v233 offset:34304
CV v81,v82,v83
MM v[108:111],a[252:255],v[212:215],v[108:111]
EX v84,v84
MM v[104:107],a[236:239],v[212:215],v[132:135]
EX v85,v85
MM v[104:107],a[232:235],v[196:199],v[104:107]
TR a[186:187],v233 offset:38400
CV v82,v84,v85
MM v[104:107],a[228:231],v[180:183],v[104:107]
EX v86,v86
MM v[104:107],a[224:227],v[164:167],v[104:107]
TR a[188:189],v233 offset:42496
TR a[190:191],v233 offset:46592
MM v[112:115],a[224:227],v[168:171],v[136:139]
EX v87,v87
MM v[112:115],a[228:231],v[184:187],v[112:115]
CV v83,v86,v87
MM v[112:115],a[232:235],v[200:203],v[112:115]
EX v88,v88
MM v[112:115],a[236:239],v[216:219],v[112:115]
EX v89,v89
MM v[116:119],a[252:255],v[216:219],v[136:139]
EX v90,v90
MM v[116:119],a[248:251],v[200:203],v[116:119]
EX v91,v91
MM v[116:119],a[244:247],v[184:187],v[116:119]
EX v92,v92
MM v[116:119],a[240:243],v[168:171],v[116:119]
EX v93,v93
MM v[124:127],a[240:243],v[172:175],v[140:143]
EX v94,v94
MM v[124:127],a[244:247],v[188:191],v[124:127]
EX v95,v95
MM v[124:127],a[248:251],v[204:207],v[124:127]
EX v96,v96
MM v[124:127],a[252:255],v[220:223],v[124:127]
EX v97,v97
MM v[120:123],a[236:239],v[220:223],v[140:143]
EX v98,v98
MM v[120:123],a[232:235],v[204:207],v[120:123]
EX v99,v99
MM v[120:123],a[228:231],v[188:191],v[120:123]
EX v100,v100
MM v[120:123],a[224:227],v[172:175],v[120:123]
s_waitcnt vmcnt(13) lgkmcnt(0)
s_barrier
MM a[0:3],a[128:131],v[0:3],a[0:3]
s_add_u32 m0, s65, 32768
RD v[40:43],v49 offset:32
RD v[44:47],v49 offset:48
MM a[0:3],a[132:135],v[4:7],a[0:3]
RD v[32:35],v48 offset:0
RD v[36:39],v48 offset:1024
MM a[8:11],a[132:135],v[12:15],a[8:11]
CV v88,v88,v89
CV v89,v90,v91
MM a[8:11],a[128:131],v[8:11],a[8:11]
RD a[192:195],v224 offset:0
CV v90,v92,v93
MM a[16:19],a[128:131],v[16:19],a[16:19]
RD a[196:199],v225 offset:0
CV v91,v94,v95
MM a[16:19],a[132:135],v[20:23],a[16:19]
s_waitcnt lgkmcnt(4)
v_min3_u32 v50, v40, v41, v42
v_min3_u32 v51, v43, v44, v45
MM a[24:27],a[132:135],v[28:31],a[24:27]
v_min3_u32 v50, v50, v46, v47
v_min_u32 v50, v50, v51
MM a[24:27],a[128:131],v[24:27],a[24:27]
v_readfirstlane_b32 s76, v50
CV v68,v96,v97
MM a[28:31],a[136:139],v[24:27],a[28:31]
s_sub_u32 s92, s92, s76
buffer_load_dwordx4 v228, s[8:11], s64 offen offset:0 lds
MM a[28:31],a[140:143],v[28:31],a[28:31]
s_add_u32 s76, s76, 1
v_subrev_u32 v40, s76, v40
v_subrev_u32 v41, s76, v41
MM a[20:23],a[140:143],v[20:23],a[20:23]
v_subrev_u32 v42, s76, v42
v_subrev_u32 v43, s76, v43
MM a[20:23],a[136:139],v[16:19],a[20:23]
v_subrev_u32 v44, s76, v44
v_subrev_u32 v45, s76, v45
MM a[12:15],a[136:139],v[8:11],a[12:15]
v_subrev_u32 v46, s76, v46
v_subrev_u32 v47, s76, v47
MM a[12:15],a[140:143],v[12:15],a[12:15]
s_and_b32 s77, s92, 65535
RD a[200:203],v226 offset:0
CV v69,v98,v99
MM a[4:7],a[140:143],v[4:7],a[4:7]
s_sub_u32 s54, 65535, s77
v_min3_u32 v50, v40, v41, v42
v_min3_u32 v51, v43, v44, v45
MM a[4:7],a[136:139],v[0:3],a[4:7]
v_min3_u32 v50, v50, v46, v47
v_min_u32 v50, v50, v51
MM a[32:35],a[144:147],v[0:3],a[32:35]
RD a[204:207],v227 offset:0
v_readfirstlane_b32 s76, v50
MM a[32:35],a[148:151],v[4:7],a[32:35]
s_sub_u32 s92, s92, s76
EX v101,v101
MM a[40:43],a[148:151],v[12:15],a[40:43]
s_sub_u32 s92, s92, 1
EX v102,v102
MM a[40:43],a[144:147],v[8:11],a[40:43]
s_and_b32 s77, s92, 65535
EX v103,v103
MM a[48:51],a[144:147],v[16:19],a[48:51]
s_sub_u32 s55, 65535, s77
buffer_load_dwordx4 v229, s[8:11], s64 offen offset:1024 lds
MM a[48:51],a[148:151],v[20:23],a[48:51]
s_sub_u32 s92, s92, 1
EX v104,v104
MM a[56:59],a[148:151],v[28:31],a[56:59]
s_waitcnt lgkmcnt(4)
v_sub_u32 v32, s92, v32
v_sub_u32 v33, s92, v33
MM a[56:59],a[144:147],v[24:27],a[56:59]
v_sub_u32 v34, s92, v34
v_sub_u32 v35, s92, v35
MM a[60:63],a[152:155],v[24:27],a[60:63]
v_sub_u32 v36, s92, v36
v_sub_u32 v37, s92, v37
EX v105,v105
MM a[60:63],a[156:159],v[28:31],a[60:63]
v_sub_u32 v38, s92, v38
v_sub_u32 v39, s92, v39
MM a[52:55],a[156:159],v[20:23],a[52:55]
v_min3_u32 v52, v32, v33, v34
v_min3_u32 v51, v35, v36, v37
EX v106,v106
MM a[52:55],a[152:155],v[16:19],a[52:55]
v_min3_u32 v52, v52, v38, v39
v_min_u32 v52, v52, v51
CV v70,v100,v101
MM a[44:47],a[152:155],v[8:11],a[44:47]
v_min_u32_dpp v52, v52, v52 row_shr:1 row_mask:0xf bank_mask:0xf
CV v71,v102,v103
EX v107,v107
MM a[44:47],a[156:159],v[12:15],a[44:47]
RD a[208:211],v224 offset:4096
v_min_u32_dpp v52, v52, v52 row_shr:2 row_mask:0xf bank_mask:0xf
MM a[36:39],a[156:159],v[4:7],a[36:39]
s_nop 0
v_min_u32_dpp v52, v52, v52 row_shr:4 row_mask:0xf bank_mask:0xf
CV v76,v104,v105
EX v108,v108
MM a[36:39],a[152:155],v[0:3],a[36:39]
RD a[212:215],v225 offset:4096
v_min_u32_dpp v52, v52, v52 row_shr:8 row_mask:0xf bank_mask:0xf
MM a[64:67],a[160:163],v[0:3],a[64:67]
s_nop 0
v_min_u32_dpp v52, v52, v52 row_bcast:15 row_mask:0xa bank_mask:0xf
CV v77,v106,v107
EX v109,v109
MM a[64:67],a[164:167],v[4:7],a[64:67]
RD a[216:219],v226 offset:4096
v_min_u32_dpp v52, v52, v52 row_bcast:31 row_mask:0xc bank_mask:0xf
MM a[72:75],a[164:167],v[12:15],a[72:75]
buffer_load_dwordx4 v230, s[8:11], s64 offen offset:2048 lds
v_readlane_b32 s76, v52, 63
v_lshl_add_u32 v49, s80, 1, v49
EX v110,v110
MM a[72:75],a[160:163],v[8:11],a[72:75]
s_add_u32 s76, s76, 1
v_subrev_u32 v32, s76, v32
v_subrev_u32 v33, s76, v33
EX v111,v111
MM a[80:83],a[160:163],v[16:19],a[80:83]
v_subrev_u32 v34, s76, v34
v_subrev_u32 v35, s76, v35
MM a[80:83],a[164:167],v[20:23],a[80:83]
v_subrev_u32 v36, s76, v36
v_subrev_u32 v37, s76, v37
EX v112,v112
MM a[88:91],a[164:167],v[28:31],a[88:91]
v_subrev_u32 v38, s76, v38
v_subrev_u32 v39, s76, v39
MM a[88:91],a[160:163],v[24:27],a[88:91]
v_min3_u32 v53, v32, v33, v34
v_min3_u32 v51, v35, v36, v37
EX v113,v113
MM a[92:95],a[168:171],v[24:27],a[92:95]
v_min3_u32 v53, v53, v38, v39
v_min_u32 v53, v53, v51
MM a[92:95],a[172:175],v[28:31],a[92:95]
s_nop 0
v_min_u32_dpp v53, v53, v53 row_shr:1 row_mask:0xf bank_mask:0xf
CV v78,v108,v109
EX v114,v114
MM a[84:87],a[172:175],v[20:23],a[84:87]
RD a[220:223],v227 offset:4096
buffer_load_dwordx4 v231, s[8:11], s64 offen offset:3072 lds
v_min_u32_dpp v53, v53, v53 row_shr:2 row_mask:0xf bank_mask:0xf
MM a[84:87],a[168:171],v[16:19],a[84:87]
s_add_u32 m0, s65, 98304
v_min_u32_dpp v53, v53, v53 row_shr:4 row_mask:0xf bank_mask:0xf
CV v79,v110,v111
EX v115,v115
MM a[76:79],a[168:171],v[8:11],a[76:79]
RD a[224:227],v224 offset:8192
v_min_u32_dpp v53, v53, v53 row_shr:8 row_mask:0xf bank_mask:0xf
MM a[76:79],a[172:175],v[12:15],a[76:79]
s_nop 0
v_min_u32_dpp v53, v53, v53 row_bcast:15 row_mask:0xa bank_mask:0xf
CV v84,v112,v113
EX v116,v116
MM a[68:71],a[172:175],v[4:7],a[68:71]
RD a[228:231],v225 offset:8192
v_min_u32_dpp v53, v53, v53 row_bcast:31 row_mask:0xc bank_mask:0xf
EX v117,v117
MM a[68:71],a[168:171],v[0:3],a[68:71]
)ASM"
R"ASM(
v_add_u32 v53, s76, v53
CV v85,v114,v115
MM a[96:99],a[176:179],v[0:3],a[96:99]
s_mov_b32 exec_lo, 0
s_mov_b32 exec_hi, 0x80000000
ds_write_b64 v49, v[52:53] offset:0
s_mov_b64 exec, -1
EX v118,v118
MM a[96:99],a[180:183],v[4:7],a[96:99]
buffer_load_dwordx4 v235, s[12:15], s81 offen offset:0 lds
MM a[104:107],a[180:183],v[12:15],a[104:107]
EX v119,v119
MM a[104:107],a[176:179],v[8:11],a[104:107]
EX v120,v120
MM a[112:115],a[176:179],v[16:19],a[112:115]
RD a[232:235],v226 offset:8192
CV v86,v116,v117
MM a[112:115],a[180:183],v[20:23],a[112:115]
EX v121,v121
MM a[120:123],a[180:183],v[28:31],a[120:123]
buffer_load_dwordx4 v235, s[12:15], s64 offen offset:1024 lds
MM a[120:123],a[176:179],v[24:27],a[120:123]
EX v122,v122
MM a[124:127],a[184:187],v[24:27],a[124:127]
RD a[236:239],v227 offset:8192
CV v87,v118,v119
MM a[124:127],a[188:191],v[28:31],a[124:127]
EX v123,v123
MM a[116:119],a[188:191],v[20:23],a[116:119]
EX v124,v124
MM a[116:119],a[184:187],v[16:19],a[116:119]
RD a[240:243],v224 offset:12288
CV v92,v120,v121
MM a[108:111],a[184:187],v[8:11],a[108:111]
EX v125,v125
MM a[108:111],a[188:191],v[12:15],a[108:111]
buffer_load_dwordx4 v235, s[12:15], s81 offen offset:2048 lds
MM a[100:103],a[188:191],v[4:7],a[100:103]
EX v126,v126
MM a[100:103],a[184:187],v[0:3],a[100:103]
RD a[244:247],v225 offset:12288
CV v93,v122,v123
v_smfmac_f32_16x16x64_bf16 v[236:239], v[144:147], v[64:71], v234
RD a[248:251],v226 offset:12288
RD a[252:255],v227 offset:12288
EX v127,v127
v_smfmac_f32_16x16x64_bf16 v[236:239], v[148:151], v[72:79], v234
CV v94,v124,v125
v_smfmac_f32_16x16x64_bf16 v[236:239], v[152:155], v[80:87], v234
CV v95,v126,v127
s_nop 1
v_smfmac_f32_16x16x64_bf16 v[236:239], v[156:159], v[88:95], v234
s_waitcnt lgkmcnt(0)
buffer_load_dwordx4 v235, s[12:15], s64 offen offset:3072 lds
MM v[0:3],a[192:195],v[160:163],v[128:131]
s_add_u32 s64, s64, 16384
TR a[128:129],v232 offset:49152
s_add_u32 s81, s64, 896
MM v[0:3],a[196:199],v[176:179],v[0:3]
TR a[130:131],v232 offset:53248
TR a[132:133],v232 offset:57344
MM v[0:3],a[200:203],v[192:195],v[0:3]
TR a[134:135],v232 offset:61440
TR a[136:137],v233 offset:49152
MM v[0:3],a[204:207],v[208:211],v[0:3]
TR a[138:139],v233 offset:53248
v_add_f32 v247, v236, v237
MM v[4:7],a[220:223],v[208:211],v[128:131]
TR a[140:141],v233 offset:57344
v_cmp_nge_f32 vcc, s71, v247
MM v[4:7],a[216:219],v[192:195],v[4:7]
s_cbranch_vccnz LSITE7_%=
LBACK7_%=:
TR a[142:143],v233 offset:61440
TR a[144:145],v232 offset:49664
MM v[4:7],a[212:215],v[176:179],v[4:7]
s_mov_b32 m0, s67
TR a[146:147],v232 offset:53760
TR a[148:149],v232 offset:57856
MM v[4:7],a[208:211],v[160:163],v[4:7]
buffer_load_dwordx4 v252, s[36:39], s66 offen offset:0 lds
MM v[12:15],a[208:211],v[164:167],v[132:135]
s_add_u32 s66, s66, 55296
EX v0,v0
MM v[12:15],a[212:215],v[180:183],v[12:15]
s_min_u32 s66, s66, s85
EX v1,v1
MM v[12:15],a[216:219],v[196:199],v[12:15]
s_add_u32 s67, s67, 1024
TR a[150:151],v232 offset:61952
CV v0,v0,v1
MM v[12:15],a[220:223],v[212:215],v[12:15]
s_min_u32 s67, s67, s86
EX v2,v2
MM v[8:11],a[204:207],v[212:215],v[132:135]
EX v3,v3
MM v[8:11],a[200:203],v[196:199],v[8:11]
TR a[152:153],v233 offset:49664
CV v1,v2,v3
MM v[8:11],a[196:199],v[180:183],v[8:11]
EX v4,v4
MM v[8:11],a[192:195],v[164:167],v[8:11]
EX v5,v5
MM v[16:19],a[192:195],v[168:171],v[136:139]
TR a[154:155],v233 offset:53760
CV v2,v4,v5
MM v[16:19],a[196:199],v[184:187],v[16:19]
EX v6,v6
MM v[16:19],a[200:203],v[200:203],v[16:19]
EX v7,v7
MM v[16:19],a[204:207],v[216:219],v[16:19]
TR a[156:157],v233 offset:57856
CV v3,v6,v7
MM v[20:23],a[220:223],v[216:219],v[136:139]
EX v8,v8
MM v[20:23],a[216:219],v[200:203],v[20:23]
TR a[158:159],v233 offset:61952
TR a[160:161],v232 offset:50176
MM v[20:23],a[212:215],v[184:187],v[20:23]
EX v9,v9
MM v[20:23],a[208:211],v[168:171],v[20:23]
TR a[162:163],v232 offset:54272
CV v8,v8,v9
MM v[28:31],a[208:211],v[172:175],v[140:143]
EX v10,v10
MM v[28:31],a[212:215],v[188:191],v[28:31]
TR a[164:165],v232 offset:58368
TR a[166:167],v232 offset:62464
MM v[28:31],a[216:219],v[204:207],v[28:31]
EX v11,v11
MM v[28:31],a[220:223],v[220:223],v[28:31]
TR a[168:169],v233 offset:50176
CV v9,v10,v11
MM v[24:27],a[204:207],v[220:223],v[140:143]
LPG28_%=:
s_cbranch_execz LTR1_%=
EX v12,v12
MM v[24:27],a[200:203],v[204:207],v[24:27]
EX v13,v13
MM v[24:27],a[196:199],v[188:191],v[24:27]
TR a[170:171],v233 offset:54272
CV v10,v12,v13
MM v[24:27],a[192:195],v[172:175],v[24:27]
EX v14,v14
MM v[32:35],a[224:227],v[160:163],v[128:131]
TR a[172:173],v233 offset:58368
TR a[174:175],v233 offset:62464
MM v[32:35],a[228:231],v[176:179],v[32:35]
EX v15,v15
MM v[32:35],a[232:235],v[192:195],v[32:35]
TR a[176:177],v232 offset:50688
CV v11,v14,v15
MM v[32:35],a[236:239],v[208:211],v[32:35]
EX v16,v16
MM v[36:39],a[252:255],v[208:211],v[128:131]
EX v17,v17
MM v[36:39],a[248:251],v[192:195],v[36:39]
TR a[178:179],v232 offset:54784
CV v16,v16,v17
MM v[36:39],a[244:247],v[176:179],v[36:39]
EX v18,v18
MM v[36:39],a[240:243],v[160:163],v[36:39]
TR a[180:181],v232 offset:58880
TR a[182:183],v232 offset:62976
MM v[44:47],a[240:243],v[164:167],v[132:135]
v_add_u32 v112, s78, v248
v_mov_b32 v113, s79
MM v[44:47],a[244:247],v[180:183],v[44:47]
EX v19,v19
MM v[44:47],a[248:251],v[196:199],v[44:47]
TR a[184:185],v233 offset:50688
CV v17,v18,v19
MM v[44:47],a[252:255],v[212:215],v[44:47]
EX v20,v20
MM v[40:43],a[236:239],v[212:215],v[132:135]
EX v21,v21
MM v[40:43],a[232:235],v[196:199],v[40:43]
TR a[186:187],v233 offset:54784
CV v18,v20,v21
MM v[40:43],a[228:231],v[180:183],v[40:43]
EX v22,v22
MM v[40:43],a[224:227],v[164:167],v[40:43]
TR a[188:189],v233 offset:58880
TR a[190:191],v233 offset:62976
MM v[48:51],a[224:227],v[168:171],v[136:139]
EX v23,v23
MM v[48:51],a[228:231],v[184:187],v[48:51]
CV v19,v22,v23
MM v[48:51],a[232:235],v[200:203],v[48:51]
EX v24,v24
MM v[48:51],a[236:239],v[216:219],v[48:51]
EX v25,v25
MM v[52:55],a[252:255],v[216:219],v[136:139]
EX v26,v26
MM v[52:55],a[248:251],v[200:203],v[52:55]
EX v27,v27
MM v[52:55],a[244:247],v[184:187],v[52:55]
EX v28,v28
MM v[52:55],a[240:243],v[168:171],v[52:55]
EX v29,v29
MM v[60:63],a[240:243],v[172:175],v[140:143]
EX v30,v30
MM v[60:63],a[244:247],v[188:191],v[60:63]
EX v31,v31
MM v[60:63],a[248:251],v[204:207],v[60:63]
EX v32,v32
MM v[60:63],a[252:255],v[220:223],v[60:63]
EX v33,v33
MM v[56:59],a[236:239],v[220:223],v[140:143]
EX v34,v34
MM v[56:59],a[232:235],v[204:207],v[56:59]
EX v35,v35
MM v[56:59],a[228:231],v[188:191],v[56:59]
EX v36,v36
MM v[56:59],a[224:227],v[172:175],v[56:59]
s_waitcnt vmcnt(13) lgkmcnt(0)
s_barrier
MM a[0:3],a[128:131],v[64:67],a[0:3]
s_add_u32 m0, s65, 49152
RD v[104:107],v113 offset:0
RD v[108:111],v113 offset:16
MM a[0:3],a[132:135],v[68:71],a[0:3]
RD v[96:99],v112 offset:0
RD v[100:103],v112 offset:1024
MM a[8:11],a[132:135],v[76:79],a[8:11]
CV v24,v24,v25
CV v25,v26,v27
MM a[8:11],a[128:131],v[72:75],a[8:11]
RD a[192:195],v224 offset:16384
CV v26,v28,v29
MM a[16:19],a[128:131],v[80:83],a[16:19]
RD a[196:199],v225 offset:16384
CV v27,v30,v31
MM a[16:19],a[132:135],v[84:87],a[16:19]
s_waitcnt lgkmcnt(4)
v_min3_u32 v114, v104, v105, v106
v_min3_u32 v115, v107, v108, v109
MM a[24:27],a[132:135],v[92:95],a[24:27]
v_min3_u32 v114, v114, v110, v111
v_min_u32 v114, v114, v115
MM a[24:27],a[128:131],v[88:91],a[24:27]
v_readfirstlane_b32 s76, v114
CV v4,v32,v33
MM a[28:31],a[136:139],v[88:91],a[28:31]
s_sub_u32 s92, s92, s76
buffer_load_dwordx4 v228, s[8:11], s64 offen offset:0 lds
MM a[28:31],a[140:143],v[92:95],a[28:31]
s_add_u32 s76, s76, 1
v_subrev_u32 v104, s76, v104
v_subrev_u32 v105, s76, v105
MM a[20:23],a[140:143],v[84:87],a[20:23]
v_subrev_u32 v106, s76, v106
v_subrev_u32 v107, s76, v107
MM a[20:23],a[136:139],v[80:83],a[20:23]
v_subrev_u32 v108, s76, v108
v_subrev_u32 v109, s76, v109
MM a[12:15],a[136:139],v[72:75],a[12:15]
v_subrev_u32 v110, s76, v110
v_subrev_u32 v111, s76, v111
MM a[12:15],a[140:143],v[76:79],a[12:15]
s_and_b32 s77, s92, 65535
RD a[200:203],v226 offset:16384
CV v5,v34,v35
MM a[4:7],a[140:143],v[68:71],a[4:7]
s_sub_u32 s56, 65535, s77
v_min3_u32 v114, v104, v105, v106
v_min3_u32 v115, v107, v108, v109
MM a[4:7],a[136:139],v[64:67],a[4:7]
v_min3_u32 v114, v114, v110, v111
v_min_u32 v114, v114, v115
MM a[32:35],a[144:147],v[64:67],a[32:35]
RD a[204:207],v227 offset:16384
v_readfirstlane_b32 s76, v114
MM a[32:35],a[148:151],v[68:71],a[32:35]
s_sub_u32 s92, s92, s76
EX v37,v37
MM a[40:43],a[148:151],v[76:79],a[40:43]
s_sub_u32 s92, s92, 1
EX v38,v38
MM a[40:43],a[144:147],v[72:75],a[40:43]
s_and_b32 s77, s92, 65535
EX v39,v39
MM a[48:51],a[144:147],v[80:83],a[48:51]
s_sub_u32 s57, 65535, s77
buffer_load_dwordx4 v229, s[8:11], s64 offen offset:1024 lds
MM a[48:51],a[148:151],v[84:87],a[48:51]
s_sub_u32 s92, s92, 1
EX v40,v40
MM a[56:59],a[148:151],v[92:95],a[56:59]
s_waitcnt lgkmcnt(4)
v_sub_u32 v96, s92, v96
v_sub_u32 v97, s92, v97
MM a[56:59],a[144:147],v[88:91],a[56:59]
v_sub_u32 v98, s92, v98
v_sub_u32 v99, s92, v99
MM a[60:63],a[152:155],v[88:91],a[60:63]
v_sub_u32 v100, s92, v100
v_sub_u32 v101, s92, v101
EX v41,v41
MM a[60:63],a[156:159],v[92:95],a[60:63]
v_sub_u32 v102, s92, v102
v_sub_u32 v103, s92, v103
MM a[52:55],a[156:159],v[84:87],a[52:55]
v_min3_u32 v116, v96, v97, v98
v_min3_u32 v115, v99, v100, v101
EX v42,v42
MM a[52:55],a[152:155],v[80:83],a[52:55]
v_min3_u32 v116, v116, v102, v103
v_min_u32 v116, v116, v115
CV v6,v36,v37
MM a[44:47],a[152:155],v[72:75],a[44:47]
v_min_u32_dpp v116, v116, v116 row_shr:1 row_mask:0xf bank_mask:0xf
CV v7,v38,v39
EX v43,v43
MM a[44:47],a[156:159],v[76:79],a[44:47]
RD a[208:211],v224 offset:20480
v_min_u32_dpp v116, v116, v116 row_shr:2 row_mask:0xf bank_mask:0xf
MM a[36:39],a[156:159],v[68:71],a[36:39]
s_nop 0
v_min_u32_dpp v116, v116, v116 row_shr:4 row_mask:0xf bank_mask:0xf
CV v12,v40,v41
EX v44,v44
MM a[36:39],a[152:155],v[64:67],a[36:39]
RD a[212:215],v225 offset:20480
v_min_u32_dpp v116, v116, v116 row_shr:8 row_mask:0xf bank_mask:0xf
MM a[64:67],a[160:163],v[64:67],a[64:67]
s_nop 0
v_min_u32_dpp v116, v116, v116 row_bcast:15 row_mask:0xa bank_mask:0xf
CV v13,v42,v43
EX v45,v45
MM a[64:67],a[164:167],v[68:71],a[64:67]
RD a[216:219],v226 offset:20480
v_min_u32_dpp v116, v116, v116 row_bcast:31 row_mask:0xc bank_mask:0xf
MM a[72:75],a[164:167],v[76:79],a[72:75]
buffer_load_dwordx4 v230, s[8:11], s64 offen offset:2048 lds
v_readlane_b32 s76, v116, 63
v_lshl_add_u32 v113, s80, 1, v113
EX v46,v46
MM a[72:75],a[160:163],v[72:75],a[72:75]
s_add_u32 s76, s76, 1
v_subrev_u32 v96, s76, v96
v_subrev_u32 v97, s76, v97
EX v47,v47
MM a[80:83],a[160:163],v[80:83],a[80:83]
v_subrev_u32 v98, s76, v98
v_subrev_u32 v99, s76, v99
MM a[80:83],a[164:167],v[84:87],a[80:83]
v_subrev_u32 v100, s76, v100
v_subrev_u32 v101, s76, v101
EX v48,v48
MM a[88:91],a[164:167],v[92:95],a[88:91]
v_subrev_u32 v102, s76, v102
v_subrev_u32 v103, s76, v103
MM a[88:91],a[160:163],v[88:91],a[88:91]
v_min3_u32 v117, v96, v97, v98
v_min3_u32 v115, v99, v100, v101
EX v49,v49
MM a[92:95],a[168:171],v[88:91],a[92:95]
v_min3_u32 v117, v117, v102, v103
v_min_u32 v117, v117, v115
MM a[92:95],a[172:175],v[92:95],a[92:95]
s_nop 0
v_min_u32_dpp v117, v117, v117 row_shr:1 row_mask:0xf bank_mask:0xf
CV v14,v44,v45
EX v50,v50
MM a[84:87],a[172:175],v[84:87],a[84:87]
RD a[220:223],v227 offset:20480
buffer_load_dwordx4 v231, s[8:11], s64 offen offset:3072 lds
v_min_u32_dpp v117, v117, v117 row_shr:2 row_mask:0xf bank_mask:0xf
MM a[84:87],a[168:171],v[80:83],a[84:87]
s_add_u32 m0, s65, 114688
v_min_u32_dpp v117, v117, v117 row_shr:4 row_mask:0xf bank_mask:0xf
CV v15,v46,v47
EX v51,v51
MM a[76:79],a[168:171],v[72:75],a[76:79]
RD a[224:227],v224 offset:24576
v_min_u32_dpp v117, v117, v117 row_shr:8 row_mask:0xf bank_mask:0xf
MM a[76:79],a[172:175],v[76:79],a[76:79]
s_nop 0
v_min_u32_dpp v117, v117, v117 row_bcast:15 row_mask:0xa bank_mask:0xf
CV v20,v48,v49
EX v52,v52
MM a[68:71],a[172:175],v[68:71],a[68:71]
RD a[228:231],v225 offset:24576
v_min_u32_dpp v117, v117, v117 row_bcast:31 row_mask:0xc bank_mask:0xf
)ASM"
R"ASM(
EX v53,v53
MM a[68:71],a[168:171],v[64:67],a[68:71]
v_add_u32 v117, s76, v117
CV v21,v50,v51
MM a[96:99],a[176:179],v[64:67],a[96:99]
s_mov_b32 exec_lo, 0
s_mov_b32 exec_hi, 0x80000000
ds_write_b64 v113, v[116:117] offset:32
s_mov_b64 exec, -1
EX v54,v54
MM a[96:99],a[180:183],v[68:71],a[96:99]
buffer_load_dwordx4 v235, s[12:15], s81 offen offset:0 lds
MM a[104:107],a[180:183],v[76:79],a[104:107]
EX v55,v55
MM a[104:107],a[176:179],v[72:75],a[104:107]
EX v56,v56
MM a[112:115],a[176:179],v[80:83],a[112:115]
RD a[232:235],v226 offset:24576
CV v22,v52,v53
MM a[112:115],a[180:183],v[84:87],a[112:115]
EX v57,v57
MM a[120:123],a[180:183],v[92:95],a[120:123]
buffer_load_dwordx4 v235, s[12:15], s64 offen offset:1024 lds
MM a[120:123],a[176:179],v[88:91],a[120:123]
EX v58,v58
MM a[124:127],a[184:187],v[88:91],a[124:127]
RD a[236:239],v227 offset:24576
CV v23,v54,v55
MM a[124:127],a[188:191],v[92:95],a[124:127]
EX v59,v59
MM a[116:119],a[188:191],v[84:87],a[116:119]
EX v60,v60
MM a[116:119],a[184:187],v[80:83],a[116:119]
RD a[240:243],v224 offset:28672
CV v28,v56,v57
MM a[108:111],a[184:187],v[72:75],a[108:111]
EX v61,v61
MM a[108:111],a[188:191],v[76:79],a[108:111]
buffer_load_dwordx4 v235, s[12:15], s81 offen offset:2048 lds
MM a[100:103],a[188:191],v[68:71],a[100:103]
EX v62,v62
MM a[100:103],a[184:187],v[64:67],a[100:103]
RD a[244:247],v225 offset:28672
CV v29,v58,v59
v_smfmac_f32_16x16x64_bf16 v[236:239], v[144:147], v[0:7], v234
RD a[248:251],v226 offset:28672
RD a[252:255],v227 offset:28672
EX v63,v63
v_smfmac_f32_16x16x64_bf16 v[236:239], v[148:151], v[8:15], v234
CV v30,v60,v61
v_smfmac_f32_16x16x64_bf16 v[236:239], v[152:155], v[16:23], v234
CV v31,v62,v63
s_nop 1
v_smfmac_f32_16x16x64_bf16 v[236:239], v[156:159], v[24:31], v234
s_waitcnt lgkmcnt(0)
buffer_load_dwordx4 v235, s[12:15], s64 offen offset:3072 lds
s_waitcnt lgkmcnt(0)
s_barrier
v_mov_b32 v113, s79
RD v[104:107],v113 offset:32
RD v[108:111],v113 offset:48
s_waitcnt lgkmcnt(0)
v_min3_u32 v114, v104, v105, v106
v_min3_u32 v115, v107, v108, v109
v_min3_u32 v114, v114, v110, v111
v_min_u32 v114, v114, v115
s_nop 0
v_readfirstlane_b32 s76, v114
s_nop 0
s_sub_u32 s92, s92, s76
s_add_u32 s76, s76, 1
v_subrev_u32 v104, s76, v104
v_subrev_u32 v105, s76, v105
v_subrev_u32 v106, s76, v106
v_subrev_u32 v107, s76, v107
v_subrev_u32 v108, s76, v108
v_subrev_u32 v109, s76, v109
v_subrev_u32 v110, s76, v110
v_subrev_u32 v111, s76, v111
s_and_b32 s77, s92, 65535
s_sub_u32 s58, 65535, s77
v_min3_u32 v114, v104, v105, v106
v_min3_u32 v115, v107, v108, v109
v_min3_u32 v114, v114, v110, v111
v_min_u32 v114, v114, v115
s_nop 0
v_readfirstlane_b32 s76, v114
s_nop 0
s_sub_u32 s92, s92, s76
s_sub_u32 s92, s92, 1
s_and_b32 s77, s92, 65535
s_sub_u32 s59, 65535, s77
s_sub_u32 s92, s92, 1
s_lshl_b32 s90, %[swave], 8
s_add_u32 s90, s90, %[sb]
s_cmp_lg_u32 s90, 0
s_cbranch_scc1 LIX_%=
v_writelane_b32 v112, s52, 0
v_writelane_b32 v112, s53, 1
v_writelane_b32 v112, s54, 2
v_writelane_b32 v112, s55, 3
v_writelane_b32 v112, s56, 4
v_writelane_b32 v112, s57, 5
v_writelane_b32 v112, s58, 6
v_writelane_b32 v112, s59, 7
v_mov_b32 v113, s69
v_lshrrev_b32 v116, 1, v248
s_mov_b32 s90, 86016
s_mov_b64 exec, 0xff
buffer_store_dwordx2 v[112:113], v116, s[20:23], s90 offen offset:0 sc1
s_mov_b64 exec, -1
s_nop 1
LIX_%=:
s_mov_b32 s70, 1
s_branch LLA_%=
.fill 435, 4, 0xbf800000
LPG29_%=:
s_cbranch_execz LTR1_%=
s_nop 0
LRG1o_%=:
s_mov_b32 s70, 30001
LRG1ol_%=:
s_sleep 2
buffer_load_dwordx4 v[200:203], v208, s[20:23], s93 offen offset:0 sc1
buffer_load_dwordx4 v[204:207], v208, s[20:23], s93 offen offset:256 sc1
s_waitcnt vmcnt(0)
v_xor_b32 v201, s69, v201
v_xor_b32 v203, s69, v203
v_xor_b32 v205, s69, v205
v_xor_b32 v207, s69, v207
v_or3_b32 v209, v201, v203, v205
v_or_b32 v209, v209, v207
v_cmp_ne_u32 vcc, 0, v209
s_cbranch_vccz LRG1ob_%=
s_sub_u32 s70, s70, 1
s_cbranch_scc0 LRG1ol_%=
s_branch LRG1ob_%=
LRFR_%=:
s_mov_b32 s91, 30001
LRFRl_%=:
s_sleep 2
buffer_load_dwordx4 v[48:51], v248, s[20:23], s90 offen offset:0 sc1
s_add_u32 s78, s90, 8192
buffer_load_dwordx4 v[56:59], v248, s[20:23], s78 offen offset:0 sc1
s_waitcnt vmcnt(0)
v_xor_b32 v48, s69, v48
v_xor_b32 v49, s69, v49
v_xor_b32 v50, s69, v50
v_xor_b32 v51, s69, v51
v_xor_b32 v56, s69, v56
v_xor_b32 v57, s69, v57
v_xor_b32 v58, s69, v58
v_xor_b32 v59, s69, v59
v_or3_b32 v52, v48, v49, v50
v_or3_b32 v52, v52, v51, v56
v_or3_b32 v52, v52, v57, v58
v_or_b32 v52, v52, v59
v_cmp_ne_u32 vcc, 0, v52
s_cbranch_vccz LRFRb_%=
s_sub_u32 s91, s91, 1
s_cbranch_scc0 LRFRl_%=
s_branch LRFRb_%=
LRIX_%=:
s_mov_b32 s91, 30001
LRIXl_%=:
s_sleep 2
buffer_load_dwordx2 v[48:49], v52, s[20:23], s90 offen offset:0 sc1
s_waitcnt vmcnt(0)
v_cmp_ne_u32 vcc, s69, v49
s_cbranch_vccz LRIXb_%=
s_sub_u32 s91, s91, 1
s_cbranch_scc0 LRIXl_%=
s_branch LRIXb_%=
LRFG_%=:
s_mov_b32 s91, 30001
LRFGl_%=:
s_sleep 2
buffer_load_dword v252, v253, s[20:23], s76 offen offset:0 sc1
s_waitcnt vmcnt(0)
v_cmp_ne_u32 vcc, s69, v252
s_cbranch_vccz LRFGb_%=
s_sub_u32 s91, s91, 1
s_cbranch_scc0 LRFGl_%=
s_branch LRFGb_%=
LRFH_%=:
s_mov_b32 s91, 30001
LRFHl_%=:
s_sleep 2
buffer_load_dword v252, v253, s[20:23], s76 offen offset:0 sc1
s_waitcnt vmcnt(0)
v_cmp_ne_u32 vcc, s69, v252
s_cbranch_vccz LRFHb_%=
s_sub_u32 s91, s91, 1
s_cbranch_scc0 LRFHl_%=
s_branch LRFHb_%=
LRPO0_%=:
s_mov_b32 s97, 30001
LRPO0l_%=:
s_sleep 2
buffer_load_dword v242, v243, s[20:23], s96 offen offset:0 sc1
s_waitcnt vmcnt(0)
v_cmp_ne_u32 vcc, s69, v242
s_cbranch_vccz LRPO0b_%=
s_sub_u32 s97, s97, 1
s_cbranch_scc0 LRPO0l_%=
s_branch LRPO0b_%=
LRPO1_%=:
s_mov_b32 s97, 30001
LRPO1l_%=:
s_sleep 2
buffer_load_dword v242, v243, s[20:23], s96 offen offset:16 sc1
s_waitcnt vmcnt(0)
v_cmp_ne_u32 vcc, s69, v242
s_cbranch_vccz LRPO1b_%=
s_sub_u32 s97, s97, 1
s_cbranch_scc0 LRPO1l_%=
s_branch LRPO1b_%=
LRYF_%=:
s_mov_b32 s91, 30001
LRYFl_%=:
s_sleep 2
buffer_load_dword v132, v135, s[20:23], s78 offen offset:0 sc1
buffer_load_dword v133, v135, s[20:23], s78 offen offset:960 sc1
buffer_load_dword v134, v135, s[20:23], s78 offen offset:1920 sc1
s_waitcnt vmcnt(0)
v_xor_b32 v132, s69, v132
v_xor_b32 v133, s69, v133
v_xor_b32 v134, s69, v134
v_or3_b32 v139, v132, v133, v134
v_cmp_ne_u32 vcc, 0, v139
s_cbranch_vccz LRYFb_%=
s_sub_u32 s91, s91, 1
s_cbranch_scc0 LRYFl_%=
s_branch LRYFb_%=
LQA_%=:
s_bitcmp0_b32 s17, 1
s_cbranch_scc1 LQG1_%=
AR v108,a35
AR v109,a39
AR v110,a43
AR v111,a47
v_cmp_eq_u32 s[24:25], -1, v108
v_cmp_eq_u32 s[26:27], -1, v109
v_cmp_eq_u32 s[28:29], -1, v110
v_cmp_eq_u32 s[46:47], -1, v111
s_nop 0
s_or_b64 s[30:31], s[24:25], s[26:27]
s_or_b64 s[30:31], s[30:31], s[28:29]
s_or_b64 s[30:31], s[30:31], s[46:47]
s_cselect_b32 s76, 256, 2
s_or_b32 s94, s94, s76
s_bitcmp1_b32 s94, 1
s_cbranch_scc1 LQG1_%=
s_and_b64 exec, s[46:47], s[10:11]
s_cbranch_scc0 LQK13_%=
s_add_u32 s66, s44, 6144
buffer_load_dwordx4 a[44:47], v250, s[20:23], s66 offen offset:0 sc1
LQK13_%=:
s_and_b64 exec, s[28:29], s[8:9]
s_cbranch_scc0 LQK12_%=
s_add_u32 s66, s16, 6144
buffer_load_dwordx4 a[40:43], v250, s[20:23], s66 offen offset:0 sc1
LQK12_%=:
s_and_b64 exec, s[26:27], s[10:11]
s_cbranch_scc0 LQK11_%=
s_add_u32 s66, s44, 4096
buffer_load_dwordx4 a[36:39], v250, s[20:23], s66 offen offset:0 sc1
LQK11_%=:
s_and_b64 exec, s[24:25], s[8:9]
s_cbranch_scc0 LQK10_%=
s_add_u32 s66, s16, 4096
buffer_load_dwordx4 a[32:35], v250, s[20:23], s66 offen offset:0 sc1
LQK10_%=:
s_mov_b64 exec, -1
LQG1_%=:
s_bitcmp0_b32 s17, 0
s_cbranch_scc1 LQG0_%=
AR v108,a3
AR v109,a7
AR v110,a11
AR v111,a15
v_cmp_eq_u32 s[24:25], -1, v108
v_cmp_eq_u32 s[26:27], -1, v109
v_cmp_eq_u32 s[28:29], -1, v110
v_cmp_eq_u32 s[46:47], -1, v111
s_nop 0
s_or_b64 s[30:31], s[24:25], s[26:27]
s_or_b64 s[30:31], s[30:31], s[28:29]
s_or_b64 s[30:31], s[30:31], s[46:47]
s_cselect_b32 s76, 256, 1
s_or_b32 s94, s94, s76
s_bitcmp1_b32 s94, 0
s_cbranch_scc1 LQG0_%=
s_and_b64 exec, s[46:47], s[38:39]
s_cbranch_scc0 LQK03_%=
s_add_u32 s66, s44, 2048
buffer_load_dwordx4 a[12:15], v250, s[20:23], s66 offen offset:0 sc1
LQK03_%=:
s_and_b64 exec, s[28:29], s[36:37]
s_cbranch_scc0 LQK02_%=
s_add_u32 s66, s16, 2048
buffer_load_dwordx4 a[8:11], v250, s[20:23], s66 offen offset:0 sc1
LQK02_%=:
s_and_b64 exec, s[26:27], s[38:39]
s_cbranch_scc0 LQK01_%=
s_add_u32 s66, s44, 0
buffer_load_dwordx4 a[4:7], v250, s[20:23], s66 offen offset:0 sc1
LQK01_%=:
s_and_b64 exec, s[24:25], s[36:37]
s_cbranch_scc0 LQK00_%=
s_add_u32 s66, s16, 0
buffer_load_dwordx4 a[0:3], v250, s[20:23], s66 offen offset:0 sc1
LQK00_%=:
s_mov_b64 exec, -1
LQG0_%=:
s_branch LQB_%=
LQC_%=:
s_bitcmp0_b32 s94, 0
s_cbranch_scc1 LQN0_%=
MM v[72:75],v[64:67],a[0:3],0
MM v[72:75],v[64:67],a[4:7],v[72:75]
MM v[72:75],v[68:71],a[8:11],v[72:75]
MM v[72:75],v[68:71],a[12:15],v[72:75]
s_bitset0_b32 s17, 0
s_nop 6
CV v76,v72,v73
CV v77,v74,v75
ds_bpermute_b32 v76, v232, v76
ds_bpermute_b32 v77, v232, v77
v_lshlrev_b32 v10, 16, v0
v_and_b32 v11, 0xffff0000, v0
v_lshlrev_b32 v12, 16, v1
v_and_b32 v13, 0xffff0000, v1
v_lshlrev_b32 v14, 16, v32
v_and_b32 v15, 0xffff0000, v32
v_lshlrev_b32 v42, 16, v33
v_and_b32 v43, 0xffff0000, v33
s_add_u32 s80, s18, 0
s_waitcnt lgkmcnt(0)
v_lshlrev_b32 v72, 16, v76
v_and_b32 v73, 0xffff0000, v76
v_lshlrev_b32 v74, 16, v77
v_and_b32 v75, 0xffff0000, v77
v_mul_f32 v72, v72, v10
v_mul_f32 v73, v73, v11
v_mul_f32 v74, v74, v12
v_mul_f32 v75, v75, v13
CV v76,v72,v73
CV v77,v74,v75
v_lshlrev_b32 v72, 16, v76
v_and_b32 v73, 0xffff0000, v76
v_lshlrev_b32 v74, 16, v77
v_and_b32 v75, 0xffff0000, v77
v_add_f32 v72, v72, v14
v_add_f32 v73, v73, v15
v_add_f32 v74, v74, v42
v_add_f32 v75, v75, v43
CV v44,v72,v73
CV v45,v74,v75
buffer_store_dwordx2 v[44:45], v251, s[40:43], s80 offen offset:0 nt
s_bitcmp0_b32 s94, 8
s_cbranch_scc1 LQN0_%=
s_waitcnt vmcnt(1)
s_branch LQS_%=
LQN0_%=:
s_bitcmp0_b32 s94, 1
s_cbranch_scc1 LQN1_%=
MM v[72:75],v[64:67],a[32:35],0
MM v[72:75],v[64:67],a[36:39],v[72:75]
MM v[72:75],v[68:71],a[40:43],v[72:75]
MM v[72:75],v[68:71],a[44:47],v[72:75]
s_bitset0_b32 s17, 1
s_nop 6
CV v76,v72,v73
CV v77,v74,v75
ds_bpermute_b32 v76, v232, v76
ds_bpermute_b32 v77, v232, v77
v_lshlrev_b32 v10, 16, v2
v_and_b32 v11, 0xffff0000, v2
v_lshlrev_b32 v12, 16, v3
v_and_b32 v13, 0xffff0000, v3
v_lshlrev_b32 v14, 16, v34
v_and_b32 v15, 0xffff0000, v34
v_lshlrev_b32 v42, 16, v35
v_and_b32 v43, 0xffff0000, v35
s_add_u32 s80, s18, 128
s_waitcnt lgkmcnt(0)
v_lshlrev_b32 v72, 16, v76
v_and_b32 v73, 0xffff0000, v76
v_lshlrev_b32 v74, 16, v77
v_and_b32 v75, 0xffff0000, v77
v_mul_f32 v72, v72, v10
v_mul_f32 v73, v73, v11
v_mul_f32 v74, v74, v12
v_mul_f32 v75, v75, v13
CV v76,v72,v73
CV v77,v74,v75
v_lshlrev_b32 v72, 16, v76
v_and_b32 v73, 0xffff0000, v76
v_lshlrev_b32 v74, 16, v77
v_and_b32 v75, 0xffff0000, v77
v_add_f32 v72, v72, v14
v_add_f32 v73, v73, v15
v_add_f32 v74, v74, v42
v_add_f32 v75, v75, v43
CV v46,v72,v73
CV v47,v74,v75
buffer_store_dwordx2 v[46:47], v251, s[40:43], s80 offen offset:0 nt
s_bitcmp0_b32 s94, 8
s_cbranch_scc1 LQN1_%=
s_waitcnt vmcnt(1)
s_branch LQS_%=
LQN1_%=:
s_branch LQD_%=
LSW_%=:
s_sleep 1
s_waitcnt vmcnt(0)
s_cmp_eq_u32 s65, 0
s_cbranch_scc1 LSX_%=
s_mov_b64 exec, 1
buffer_store_dword v233, off, s[20:23], s19 offset:12 sc1
s_mov_b64 exec, -1
s_mov_b32 s65, 0
LSX_%=:
v_cmp_ne_u32 vcc, s69, v243
v_cmp_ne_u32 s[36:37], s69, v143
s_nop 1
s_or_b64 vcc, vcc, s[36:37]
s_cselect_b32 s76, 0, 1
s_add_u32 s72, s72, s76
s_sub_u32 s73, s73, 1
s_cselect_b32 s72, 2, s72
s_bitcmp0_b32 s17, 4
s_cbranch_scc1 LSN4_%=
MM v[72:75],v[64:67],a[192:195],0
MM v[72:75],v[64:67],a[196:199],v[72:75]
MM v[72:75],v[68:71],v[144:147],v[72:75]
MM v[72:75],v[68:71],a[212:215],v[72:75]
AR v76,a195
AR v77,a199
v_mov_b32 v78, v147
AR v79,a215
s_nop 3
v_cmp_u_f32 s[30:31], v72, v72
s_nop 1
s_cmp_eq_u64 s[30:31], 0
s_cbranch_scc1 LSF4_%=
s_cmp_ge_u32 s72, 2
s_cbranch_scc1 LSF4_%=
v_cmp_eq_u32 vcc, s69, v242
v_cmp_eq_u32 s[36:37], -1, v78
s_add_u32 s66, s16, 18432
s_and_b64 vcc, vcc, s[36:37]
s_cmp_eq_u32 s72, 0
s_cselect_b64 exec, vcc, -1
s_and_b32 exec_lo, exec_lo, 0xffff0000
buffer_load_dwordx4 v[144:147], v250, s[20:23], s66 offen offset:0 sc1
)ASM"
R"ASM(
s_mov_b64 exec, -1
v_cmp_eq_u32 vcc, s69, v142
v_cmp_eq_u32 s[36:37], -1, v79
s_add_u32 s66, s44, 18432
s_and_b64 vcc, vcc, s[36:37]
s_cmp_eq_u32 s72, 0
s_cselect_b64 exec, vcc, -1
buffer_load_dwordx4 a[212:215], v250, s[20:23], s66 offen offset:0 sc1
s_mov_b64 exec, -1
v_cmp_eq_u32 vcc, s69, v241
v_cmp_eq_u32 s[36:37], -1, v76
s_add_u32 s66, s16, 8192
s_and_b64 vcc, vcc, s[36:37]
s_cmp_eq_u32 s72, 0
s_cselect_b64 exec, vcc, -1
s_and_b32 exec_lo, exec_lo, 0xffff0000
buffer_load_dwordx4 a[192:195], v250, s[20:23], s66 offen offset:0 sc1
s_mov_b64 exec, -1
v_cmp_eq_u32 vcc, s69, v141
v_cmp_eq_u32 s[36:37], -1, v77
s_add_u32 s66, s44, 8192
s_and_b64 vcc, vcc, s[36:37]
s_cmp_eq_u32 s72, 0
s_cselect_b64 exec, vcc, -1
buffer_load_dwordx4 a[196:199], v250, s[20:23], s66 offen offset:0 sc1
s_mov_b64 exec, -1
s_branch LSN4_%=
LSF4_%=:
s_bitset0_b32 s17, 4
CV v76,v72,v73
CV v77,v74,v75
ds_bpermute_b32 v76, v232, v76
ds_bpermute_b32 v77, v232, v77
v_lshlrev_b32 v10, 16, v8
v_and_b32 v11, 0xffff0000, v8
v_lshlrev_b32 v12, 16, v9
v_and_b32 v13, 0xffff0000, v9
v_lshlrev_b32 v14, 16, v40
v_and_b32 v15, 0xffff0000, v40
v_lshlrev_b32 v42, 16, v41
v_and_b32 v43, 0xffff0000, v41
s_add_u32 s80, s63, 0
s_waitcnt lgkmcnt(0)
v_lshlrev_b32 v72, 16, v76
v_and_b32 v73, 0xffff0000, v76
v_lshlrev_b32 v74, 16, v77
v_and_b32 v75, 0xffff0000, v77
v_mul_f32 v72, v72, v10
v_mul_f32 v73, v73, v11
v_mul_f32 v74, v74, v12
v_mul_f32 v75, v75, v13
CV v76,v72,v73
CV v77,v74,v75
v_lshlrev_b32 v72, 16, v76
v_and_b32 v73, 0xffff0000, v76
v_lshlrev_b32 v74, 16, v77
v_and_b32 v75, 0xffff0000, v77
v_add_f32 v72, v72, v14
v_add_f32 v73, v73, v15
v_add_f32 v74, v74, v42
v_add_f32 v75, v75, v43
CV v44,v72,v73
CV v45,v74,v75
buffer_store_dwordx2 v[44:45], v254, s[40:43], s80 offen offset:0 nt
s_nop 1
LSN4_%=:
s_bitcmp0_b32 s17, 3
s_cbranch_scc1 LSN3_%=
MM v[72:75],v[64:67],v[80:83],0
MM v[72:75],v[64:67],a[100:103],v[72:75]
MM v[72:75],v[68:71],v[112:115],v[72:75]
MM v[72:75],v[68:71],a[108:111],v[72:75]
v_mov_b32 v76, v83
AR v77,a103
v_mov_b32 v78, v115
AR v79,a111
s_nop 3
v_cmp_u_f32 s[30:31], v72, v72
s_nop 1
s_cmp_eq_u64 s[30:31], 0
s_cbranch_scc1 LSF3_%=
s_cmp_ge_u32 s72, 2
s_cbranch_scc1 LSF3_%=
v_cmp_eq_u32 vcc, s69, v242
v_cmp_eq_u32 s[36:37], -1, v78
s_add_u32 s66, s16, 16384
s_and_b64 vcc, vcc, s[36:37]
s_cmp_eq_u32 s72, 0
s_cselect_b64 exec, vcc, -1
s_and_b32 exec_lo, exec_lo, 0xffff0000
buffer_load_dwordx4 v[112:115], v250, s[20:23], s66 offen offset:0 sc1
s_mov_b64 exec, -1
v_cmp_eq_u32 vcc, s69, v142
v_cmp_eq_u32 s[36:37], -1, v79
s_add_u32 s66, s44, 16384
s_and_b64 vcc, vcc, s[36:37]
s_cmp_eq_u32 s72, 0
s_cselect_b64 exec, vcc, -1
buffer_load_dwordx4 a[108:111], v250, s[20:23], s66 offen offset:0 sc1
s_mov_b64 exec, -1
v_cmp_eq_u32 vcc, s69, v242
v_cmp_eq_u32 s[36:37], -1, v76
s_add_u32 s66, s16, 14336
s_and_b64 vcc, vcc, s[36:37]
s_cmp_eq_u32 s72, 0
s_cselect_b64 exec, vcc, -1
s_and_b32 exec_lo, exec_lo, 0xffff0000
buffer_load_dwordx4 v[80:83], v250, s[20:23], s66 offen offset:0 sc1
s_mov_b64 exec, -1
v_cmp_eq_u32 vcc, s69, v142
v_cmp_eq_u32 s[36:37], -1, v77
s_add_u32 s66, s44, 14336
s_and_b64 vcc, vcc, s[36:37]
s_cmp_eq_u32 s72, 0
s_cselect_b64 exec, vcc, -1
buffer_load_dwordx4 a[100:103], v250, s[20:23], s66 offen offset:0 sc1
s_mov_b64 exec, -1
s_branch LSN3_%=
LSF3_%=:
s_bitset0_b32 s17, 3
CV v76,v72,v73
CV v77,v74,v75
ds_bpermute_b32 v76, v232, v76
ds_bpermute_b32 v77, v232, v77
v_lshlrev_b32 v10, 16, v6
v_and_b32 v11, 0xffff0000, v6
v_lshlrev_b32 v12, 16, v7
v_and_b32 v13, 0xffff0000, v7
v_lshlrev_b32 v14, 16, v38
v_and_b32 v15, 0xffff0000, v38
v_lshlrev_b32 v42, 16, v39
v_and_b32 v43, 0xffff0000, v39
s_add_u32 s80, s18, 41088
s_waitcnt lgkmcnt(0)
v_lshlrev_b32 v72, 16, v76
v_and_b32 v73, 0xffff0000, v76
v_lshlrev_b32 v74, 16, v77
v_and_b32 v75, 0xffff0000, v77
v_mul_f32 v72, v72, v10
v_mul_f32 v73, v73, v11
v_mul_f32 v74, v74, v12
v_mul_f32 v75, v75, v13
CV v76,v72,v73
CV v77,v74,v75
v_lshlrev_b32 v72, 16, v76
v_and_b32 v73, 0xffff0000, v76
v_lshlrev_b32 v74, 16, v77
v_and_b32 v75, 0xffff0000, v77
v_add_f32 v72, v72, v14
v_add_f32 v73, v73, v15
v_add_f32 v74, v74, v42
v_add_f32 v75, v75, v43
CV v46,v72,v73
CV v47,v74,v75
buffer_store_dwordx2 v[46:47], v251, s[40:43], s80 offen offset:0 nt
s_nop 1
LSN3_%=:
s_bitcmp0_b32 s17, 2
s_cbranch_scc1 LSN2_%=
MM v[72:75],v[64:67],v[16:19],0
MM v[72:75],v[64:67],a[68:71],v[72:75]
MM v[72:75],v[68:71],v[48:51],v[72:75]
MM v[72:75],v[68:71],a[76:79],v[72:75]
v_mov_b32 v76, v19
AR v77,a71
v_mov_b32 v78, v51
AR v79,a79
s_nop 3
v_cmp_u_f32 s[30:31], v72, v72
s_nop 1
s_cmp_eq_u64 s[30:31], 0
s_cbranch_scc1 LSF2_%=
s_cmp_ge_u32 s72, 2
s_cbranch_scc1 LSF2_%=
v_cmp_eq_u32 vcc, s69, v242
v_cmp_eq_u32 s[36:37], -1, v78
s_add_u32 s66, s16, 12288
s_and_b64 vcc, vcc, s[36:37]
s_cmp_eq_u32 s72, 0
s_cselect_b64 exec, vcc, -1
s_and_b32 exec_lo, exec_lo, 0xffff0000
buffer_load_dwordx4 v[48:51], v250, s[20:23], s66 offen offset:0 sc1
s_mov_b64 exec, -1
v_cmp_eq_u32 vcc, s69, v142
v_cmp_eq_u32 s[36:37], -1, v79
s_add_u32 s66, s44, 12288
s_and_b64 vcc, vcc, s[36:37]
s_cmp_eq_u32 s72, 0
s_cselect_b64 exec, vcc, -1
buffer_load_dwordx4 a[76:79], v250, s[20:23], s66 offen offset:0 sc1
s_mov_b64 exec, -1
v_cmp_eq_u32 vcc, s69, v242
v_cmp_eq_u32 s[36:37], -1, v76
s_add_u32 s66, s16, 10240
s_and_b64 vcc, vcc, s[36:37]
s_cmp_eq_u32 s72, 0
s_cselect_b64 exec, vcc, -1
s_and_b32 exec_lo, exec_lo, 0xffff0000
buffer_load_dwordx4 v[16:19], v250, s[20:23], s66 offen offset:0 sc1
s_mov_b64 exec, -1
v_cmp_eq_u32 vcc, s69, v142
v_cmp_eq_u32 s[36:37], -1, v77
s_add_u32 s66, s44, 10240
s_and_b64 vcc, vcc, s[36:37]
s_cmp_eq_u32 s72, 0
s_cselect_b64 exec, vcc, -1
buffer_load_dwordx4 a[68:71], v250, s[20:23], s66 offen offset:0 sc1
s_mov_b64 exec, -1
s_branch LSN2_%=
LSF2_%=:
s_bitset0_b32 s17, 2
CV v76,v72,v73
CV v77,v74,v75
ds_bpermute_b32 v76, v232, v76
ds_bpermute_b32 v77, v232, v77
v_lshlrev_b32 v10, 16, v4
v_and_b32 v11, 0xffff0000, v4
v_lshlrev_b32 v12, 16, v5
v_and_b32 v13, 0xffff0000, v5
v_lshlrev_b32 v14, 16, v36
v_and_b32 v15, 0xffff0000, v36
v_lshlrev_b32 v42, 16, v37
v_and_b32 v43, 0xffff0000, v37
s_add_u32 s80, s18, 40960
s_waitcnt lgkmcnt(0)
v_lshlrev_b32 v72, 16, v76
v_and_b32 v73, 0xffff0000, v76
v_lshlrev_b32 v74, 16, v77
v_and_b32 v75, 0xffff0000, v77
v_mul_f32 v72, v72, v10
v_mul_f32 v73, v73, v11
v_mul_f32 v74, v74, v12
v_mul_f32 v75, v75, v13
CV v76,v72,v73
CV v77,v74,v75
v_lshlrev_b32 v72, 16, v76
v_and_b32 v73, 0xffff0000, v76
v_lshlrev_b32 v74, 16, v77
v_and_b32 v75, 0xffff0000, v77
v_add_f32 v72, v72, v14
v_add_f32 v73, v73, v15
v_add_f32 v74, v74, v42
v_add_f32 v75, v75, v43
CV v44,v72,v73
CV v45,v74,v75
buffer_store_dwordx2 v[44:45], v251, s[40:43], s80 offen offset:0 nt
s_nop 1
LSN2_%=:
s_bitcmp0_b32 s17, 1
s_cbranch_scc1 LSN1_%=
MM v[72:75],v[64:67],a[32:35],0
MM v[72:75],v[64:67],a[36:39],v[72:75]
MM v[72:75],v[68:71],a[40:43],v[72:75]
MM v[72:75],v[68:71],a[44:47],v[72:75]
AR v76,a35
AR v77,a39
AR v78,a43
AR v79,a47
s_nop 3
v_cmp_u_f32 s[30:31], v72, v72
s_nop 1
s_cmp_eq_u64 s[30:31], 0
s_cbranch_scc1 LSF1_%=
s_cmp_ge_u32 s72, 2
s_cbranch_scc1 LSF1_%=
v_cmp_eq_u32 vcc, s69, v241
v_cmp_eq_u32 s[36:37], -1, v78
s_add_u32 s66, s16, 6144
s_and_b64 vcc, vcc, s[36:37]
s_cmp_eq_u32 s72, 0
s_cselect_b64 exec, vcc, -1
s_and_b32 exec_lo, exec_lo, 0xffff0000
buffer_load_dwordx4 a[40:43], v250, s[20:23], s66 offen offset:0 sc1
s_mov_b64 exec, -1
v_cmp_eq_u32 vcc, s69, v141
v_cmp_eq_u32 s[36:37], -1, v79
s_add_u32 s66, s44, 6144
s_and_b64 vcc, vcc, s[36:37]
s_cmp_eq_u32 s72, 0
s_cselect_b64 exec, vcc, -1
buffer_load_dwordx4 a[44:47], v250, s[20:23], s66 offen offset:0 sc1
s_mov_b64 exec, -1
v_cmp_eq_u32 vcc, s69, v241
v_cmp_eq_u32 s[36:37], -1, v76
s_add_u32 s66, s16, 4096
s_and_b64 vcc, vcc, s[36:37]
s_cmp_eq_u32 s72, 0
s_cselect_b64 exec, vcc, -1
s_and_b32 exec_lo, exec_lo, 0xffff0000
buffer_load_dwordx4 a[32:35], v250, s[20:23], s66 offen offset:0 sc1
s_mov_b64 exec, -1
v_cmp_eq_u32 vcc, s69, v141
v_cmp_eq_u32 s[36:37], -1, v77
s_add_u32 s66, s44, 4096
s_and_b64 vcc, vcc, s[36:37]
s_cmp_eq_u32 s72, 0
s_cselect_b64 exec, vcc, -1
buffer_load_dwordx4 a[36:39], v250, s[20:23], s66 offen offset:0 sc1
s_mov_b64 exec, -1
s_branch LSN1_%=
LSF1_%=:
s_bitset0_b32 s17, 1
CV v76,v72,v73
CV v77,v74,v75
ds_bpermute_b32 v76, v232, v76
ds_bpermute_b32 v77, v232, v77
v_lshlrev_b32 v10, 16, v2
v_and_b32 v11, 0xffff0000, v2
v_lshlrev_b32 v12, 16, v3
v_and_b32 v13, 0xffff0000, v3
v_lshlrev_b32 v14, 16, v34
v_and_b32 v15, 0xffff0000, v34
v_lshlrev_b32 v42, 16, v35
v_and_b32 v43, 0xffff0000, v35
s_add_u32 s80, s18, 128
s_waitcnt lgkmcnt(0)
v_lshlrev_b32 v72, 16, v76
v_and_b32 v73, 0xffff0000, v76
v_lshlrev_b32 v74, 16, v77
v_and_b32 v75, 0xffff0000, v77
v_mul_f32 v72, v72, v10
v_mul_f32 v73, v73, v11
v_mul_f32 v74, v74, v12
v_mul_f32 v75, v75, v13
CV v76,v72,v73
CV v77,v74,v75
v_lshlrev_b32 v72, 16, v76
v_and_b32 v73, 0xffff0000, v76
v_lshlrev_b32 v74, 16, v77
v_and_b32 v75, 0xffff0000, v77
v_add_f32 v72, v72, v14
v_add_f32 v73, v73, v15
v_add_f32 v74, v74, v42
v_add_f32 v75, v75, v43
CV v46,v72,v73
CV v47,v74,v75
buffer_store_dwordx2 v[46:47], v251, s[40:43], s80 offen offset:0 nt
s_nop 1
LSN1_%=:
s_bitcmp0_b32 s17, 0
s_cbranch_scc1 LSN0_%=
MM v[72:75],v[64:67],a[0:3],0
MM v[72:75],v[64:67],a[4:7],v[72:75]
MM v[72:75],v[68:71],a[8:11],v[72:75]
MM v[72:75],v[68:71],a[12:15],v[72:75]
AR v76,a3
AR v77,a7
AR v78,a11
AR v79,a15
s_nop 3
v_cmp_u_f32 s[30:31], v72, v72
s_nop 1
s_cmp_eq_u64 s[30:31], 0
s_cbranch_scc1 LSF0_%=
s_cmp_ge_u32 s72, 2
s_cbranch_scc1 LSF0_%=
v_cmp_eq_u32 vcc, s69, v240
v_cmp_eq_u32 s[36:37], -1, v78
s_add_u32 s66, s16, 2048
s_and_b64 vcc, vcc, s[36:37]
s_cmp_eq_u32 s72, 0
s_cselect_b64 exec, vcc, -1
s_and_b32 exec_lo, exec_lo, 0xffff0000
buffer_load_dwordx4 a[8:11], v250, s[20:23], s66 offen offset:0 sc1
s_mov_b64 exec, -1
v_cmp_eq_u32 vcc, s69, v140
v_cmp_eq_u32 s[36:37], -1, v79
s_add_u32 s66, s44, 2048
s_and_b64 vcc, vcc, s[36:37]
s_cmp_eq_u32 s72, 0
s_cselect_b64 exec, vcc, -1
buffer_load_dwordx4 a[12:15], v250, s[20:23], s66 offen offset:0 sc1
s_mov_b64 exec, -1
v_cmp_eq_u32 vcc, s69, v240
v_cmp_eq_u32 s[36:37], -1, v76
s_add_u32 s66, s16, 0
s_and_b64 vcc, vcc, s[36:37]
s_cmp_eq_u32 s72, 0
s_cselect_b64 exec, vcc, -1
s_and_b32 exec_lo, exec_lo, 0xffff0000
buffer_load_dwordx4 a[0:3], v250, s[20:23], s66 offen offset:0 sc1
s_mov_b64 exec, -1
v_cmp_eq_u32 vcc, s69, v140
v_cmp_eq_u32 s[36:37], -1, v77
s_add_u32 s66, s44, 0
s_and_b64 vcc, vcc, s[36:37]
s_cmp_eq_u32 s72, 0
s_cselect_b64 exec, vcc, -1
buffer_load_dwordx4 a[4:7], v250, s[20:23], s66 offen offset:0 sc1
s_mov_b64 exec, -1
s_branch LSN0_%=
LSF0_%=:
s_bitset0_b32 s17, 0
CV v76,v72,v73
CV v77,v74,v75
ds_bpermute_b32 v76, v232, v76
ds_bpermute_b32 v77, v232, v77
v_lshlrev_b32 v10, 16, v0
v_and_b32 v11, 0xffff0000, v0
v_lshlrev_b32 v12, 16, v1
v_and_b32 v13, 0xffff0000, v1
v_lshlrev_b32 v14, 16, v32
v_and_b32 v15, 0xffff0000, v32
v_lshlrev_b32 v42, 16, v33
v_and_b32 v43, 0xffff0000, v33
s_add_u32 s80, s18, 0
s_waitcnt lgkmcnt(0)
v_lshlrev_b32 v72, 16, v76
v_and_b32 v73, 0xffff0000, v76
v_lshlrev_b32 v74, 16, v77
v_and_b32 v75, 0xffff0000, v77
v_mul_f32 v72, v72, v10
v_mul_f32 v73, v73, v11
v_mul_f32 v74, v74, v12
v_mul_f32 v75, v75, v13
CV v76,v72,v73
CV v77,v74,v75
v_lshlrev_b32 v72, 16, v76
v_and_b32 v73, 0xffff0000, v76
v_lshlrev_b32 v74, 16, v77
v_and_b32 v75, 0xffff0000, v77
v_add_f32 v72, v72, v14
v_add_f32 v73, v73, v15
v_add_f32 v74, v74, v42
v_add_f32 v75, v75, v43
CV v44,v72,v73
CV v45,v74,v75
buffer_store_dwordx2 v[44:45], v251, s[40:43], s80 offen offset:0 nt
s_nop 1
LSN0_%=:
v_cmp_ne_u32 vcc, s69, v242
v_cmp_ne_u32 s[36:37], s69, v142
s_nop 1
s_or_b64 vcc, vcc, s[36:37]
s_cselect_b32 s76, 0, 15
s_and_b32 s76, s76, s73
s_cbranch_scc1 LSU_%=
buffer_load_dwordx4 v[240:243], v253, s[20:23], s68 offen offset:0 sc1
buffer_load_dwordx4 v[140:143], v253, s[20:23], s45 offen offset:0 sc1
LSU_%=:
s_or_b32 s76, s17, s65
s_cbranch_scc1 LSW_%=
s_branch LQE_%=
LKEND_ka:
LSITE0_%=:
s_mov_b32 s73, 0
s_mov_b32 s75, 16384
s_mov_b32 s74, 0
)ASM"
R"ASM(
s_branch LRARE002_%=
LSITE2_%=:
s_mov_b32 s73, 2
s_mov_b32 s75, 49152
s_mov_b32 s74, 32768
s_branch LRARE002_%=
LRARE002_%=:
s_nop 15
s_waitcnt vmcnt(0) lgkmcnt(0)
v_add_u32 v249, s74, v224
RD a[192:195],v249 offset:0
RD a[208:211],v249 offset:4096
RD a[224:227],v249 offset:8192
RD a[240:243],v249 offset:12288
v_add_u32 v250, s74, v225
RD a[196:199],v250 offset:0
RD a[212:215],v250 offset:4096
RD a[228:231],v250 offset:8192
RD a[244:247],v250 offset:12288
v_add_u32 v251, s74, v226
RD a[200:203],v251 offset:0
RD a[216:219],v251 offset:4096
RD a[232:235],v251 offset:8192
RD a[248:251],v251 offset:12288
v_add_u32 v254, s74, v227
RD a[204:207],v254 offset:0
RD a[220:223],v254 offset:4096
RD a[236:239],v254 offset:8192
RD a[252:255],v254 offset:12288
s_waitcnt lgkmcnt(0)
MM v[0:3],a[192:195],v[160:163],v[128:131]
MM v[0:3],a[196:199],v[176:179],v[0:3]
MM v[0:3],a[200:203],v[192:195],v[0:3]
MM v[0:3],a[204:207],v[208:211],v[0:3]
MM v[4:7],a[220:223],v[208:211],v[128:131]
MM v[4:7],a[216:219],v[192:195],v[4:7]
MM v[4:7],a[212:215],v[176:179],v[4:7]
MM v[4:7],a[208:211],v[160:163],v[4:7]
MM v[12:15],a[208:211],v[164:167],v[132:135]
MM v[12:15],a[212:215],v[180:183],v[12:15]
MM v[12:15],a[216:219],v[196:199],v[12:15]
MM v[12:15],a[220:223],v[212:215],v[12:15]
MM v[8:11],a[204:207],v[212:215],v[132:135]
MM v[8:11],a[200:203],v[196:199],v[8:11]
MM v[8:11],a[196:199],v[180:183],v[8:11]
MM v[8:11],a[192:195],v[164:167],v[8:11]
MM v[32:35],a[224:227],v[160:163],v[128:131]
MM v[32:35],a[228:231],v[176:179],v[32:35]
MM v[32:35],a[232:235],v[192:195],v[32:35]
MM v[32:35],a[236:239],v[208:211],v[32:35]
MM v[36:39],a[252:255],v[208:211],v[128:131]
MM v[36:39],a[248:251],v[192:195],v[36:39]
MM v[36:39],a[244:247],v[176:179],v[36:39]
MM v[36:39],a[240:243],v[160:163],v[36:39]
MM v[44:47],a[240:243],v[164:167],v[132:135]
MM v[44:47],a[244:247],v[180:183],v[44:47]
MM v[44:47],a[248:251],v[196:199],v[44:47]
MM v[44:47],a[252:255],v[212:215],v[44:47]
MM v[40:43],a[236:239],v[212:215],v[132:135]
MM v[40:43],a[232:235],v[196:199],v[40:43]
MM v[40:43],a[228:231],v[180:183],v[40:43]
MM v[40:43],a[224:227],v[164:167],v[40:43]
s_nop 15
v_add_u32 v249, s75, v224
RD a[192:195],v249 offset:0
RD a[208:211],v249 offset:4096
RD a[224:227],v249 offset:8192
RD a[240:243],v249 offset:12288
v_add_u32 v250, s75, v225
RD a[196:199],v250 offset:0
RD a[212:215],v250 offset:4096
RD a[228:231],v250 offset:8192
RD a[244:247],v250 offset:12288
v_add_u32 v251, s75, v226
RD a[200:203],v251 offset:0
RD a[216:219],v251 offset:4096
RD a[232:235],v251 offset:8192
RD a[248:251],v251 offset:12288
v_add_u32 v254, s75, v227
RD a[204:207],v254 offset:0
RD a[220:223],v254 offset:4096
RD a[236:239],v254 offset:8192
RD a[252:255],v254 offset:12288
s_waitcnt lgkmcnt(0)
v_mov_b32 v236, v247
v_mov_b32 v237, 0
v_max3_f32 v249, v0, v1, v2
v_max3_f32 v251, v32, v33, v34
v_max3_f32 v249, v249, v3, v4
v_max3_f32 v251, v251, v35, v36
v_max3_f32 v249, v249, v5, v6
v_max3_f32 v251, v251, v37, v38
v_max3_f32 v249, v249, v7, v39
v_max_f32 v249, v249, v251
v_log_f32 v250, v247
s_lshl_b64 exec, 0xffff, 0
v_max_f32 v249, v249, v250
s_mov_b64 exec, -1
v_mov_b32 v251, v249
s_nop 1
v_permlane16_swap_b32 v249, v251
v_max_f32 v249, v249, v251
v_mov_b32 v251, v249
s_nop 1
v_permlane32_swap_b32 v249, v251
v_max_f32 v249, v249, v251
v_max_f32 v249, 0, v249
v_sub_f32 v250, 0, v249
EX v250,v250
v_sub_f32 v128, v128, v249
v_sub_f32 v129, v129, v249
v_sub_f32 v130, v130, v249
v_sub_f32 v131, v131, v249
v_sub_f32 v64, v64, v249
v_sub_f32 v0, v0, v249
v_sub_f32 v65, v65, v249
v_sub_f32 v1, v1, v249
v_sub_f32 v66, v66, v249
v_sub_f32 v2, v2, v249
v_sub_f32 v67, v67, v249
v_sub_f32 v3, v3, v249
v_sub_f32 v68, v68, v249
v_sub_f32 v4, v4, v249
v_sub_f32 v69, v69, v249
v_sub_f32 v5, v5, v249
v_sub_f32 v70, v70, v249
v_sub_f32 v6, v6, v249
v_sub_f32 v71, v71, v249
v_sub_f32 v7, v7, v249
v_sub_f32 v96, v96, v249
v_sub_f32 v32, v32, v249
v_sub_f32 v97, v97, v249
v_sub_f32 v33, v33, v249
v_sub_f32 v98, v98, v249
v_sub_f32 v34, v34, v249
v_sub_f32 v99, v99, v249
v_sub_f32 v35, v35, v249
v_sub_f32 v100, v100, v249
v_sub_f32 v36, v36, v249
v_sub_f32 v101, v101, v249
v_sub_f32 v37, v37, v249
v_sub_f32 v102, v102, v249
v_sub_f32 v38, v38, v249
v_sub_f32 v103, v103, v249
v_sub_f32 v39, v39, v249
s_lshl_b64 exec, 0xffff, 0
v_mul_f32 v236, v236, v250
s_mov_b64 exec, -1
AR v254,a0
v_mul_f32 v254, v254, v250
AW a0,v254
AR v251,a1
v_mul_f32 v251, v251, v250
AW a1,v251
AR v254,a2
v_mul_f32 v254, v254, v250
AW a2,v254
AR v251,a3
v_mul_f32 v251, v251, v250
AW a3,v251
AR v254,a4
v_mul_f32 v254, v254, v250
AW a4,v254
AR v251,a5
v_mul_f32 v251, v251, v250
AW a5,v251
AR v254,a6
v_mul_f32 v254, v254, v250
AW a6,v254
AR v251,a7
v_mul_f32 v251, v251, v250
AW a7,v251
AR v254,a32
v_mul_f32 v254, v254, v250
AW a32,v254
AR v251,a33
v_mul_f32 v251, v251, v250
AW a33,v251
AR v254,a34
v_mul_f32 v254, v254, v250
AW a34,v254
AR v251,a35
v_mul_f32 v251, v251, v250
AW a35,v251
AR v254,a36
v_mul_f32 v254, v254, v250
AW a36,v254
AR v251,a37
v_mul_f32 v251, v251, v250
AW a37,v251
AR v254,a38
v_mul_f32 v254, v254, v250
AW a38,v254
AR v251,a39
v_mul_f32 v251, v251, v250
AW a39,v251
AR v254,a64
v_mul_f32 v254, v254, v250
AW a64,v254
AR v251,a65
v_mul_f32 v251, v251, v250
AW a65,v251
AR v254,a66
v_mul_f32 v254, v254, v250
AW a66,v254
AR v251,a67
v_mul_f32 v251, v251, v250
AW a67,v251
AR v254,a68
v_mul_f32 v254, v254, v250
AW a68,v254
AR v251,a69
v_mul_f32 v251, v251, v250
AW a69,v251
AR v254,a70
v_mul_f32 v254, v254, v250
AW a70,v254
AR v251,a71
v_mul_f32 v251, v251, v250
AW a71,v251
AR v254,a96
v_mul_f32 v254, v254, v250
AW a96,v254
AR v251,a97
v_mul_f32 v251, v251, v250
AW a97,v251
AR v254,a98
v_mul_f32 v254, v254, v250
AW a98,v254
AR v251,a99
v_mul_f32 v251, v251, v250
AW a99,v251
AR v254,a100
v_mul_f32 v254, v254, v250
AW a100,v254
AR v251,a101
v_mul_f32 v251, v251, v250
AW a101,v251
AR v254,a102
v_mul_f32 v254, v254, v250
AW a102,v254
AR v251,a103
v_mul_f32 v251, v251, v250
AW a103,v251
v_max3_f32 v249, v8, v9, v10
v_max3_f32 v251, v40, v41, v42
v_max3_f32 v249, v249, v11, v12
v_max3_f32 v251, v251, v43, v44
v_max3_f32 v249, v249, v13, v14
v_max3_f32 v251, v251, v45, v46
v_max3_f32 v249, v249, v15, v47
v_max_f32 v249, v249, v251
v_log_f32 v250, v247
s_lshl_b64 exec, 0xffff, 16
v_max_f32 v249, v249, v250
s_mov_b64 exec, -1
v_mov_b32 v251, v249
s_nop 1
v_permlane16_swap_b32 v249, v251
v_max_f32 v249, v249, v251
v_mov_b32 v251, v249
s_nop 1
v_permlane32_swap_b32 v249, v251
v_max_f32 v249, v249, v251
v_max_f32 v249, 0, v249
v_sub_f32 v250, 0, v249
EX v250,v250
v_sub_f32 v132, v132, v249
v_sub_f32 v133, v133, v249
v_sub_f32 v134, v134, v249
v_sub_f32 v135, v135, v249
v_sub_f32 v72, v72, v249
v_sub_f32 v8, v8, v249
v_sub_f32 v73, v73, v249
v_sub_f32 v9, v9, v249
v_sub_f32 v74, v74, v249
v_sub_f32 v10, v10, v249
v_sub_f32 v75, v75, v249
v_sub_f32 v11, v11, v249
v_sub_f32 v76, v76, v249
v_sub_f32 v12, v12, v249
v_sub_f32 v77, v77, v249
v_sub_f32 v13, v13, v249
v_sub_f32 v78, v78, v249
v_sub_f32 v14, v14, v249
v_sub_f32 v79, v79, v249
v_sub_f32 v15, v15, v249
v_sub_f32 v104, v104, v249
v_sub_f32 v40, v40, v249
v_sub_f32 v105, v105, v249
v_sub_f32 v41, v41, v249
v_sub_f32 v106, v106, v249
v_sub_f32 v42, v42, v249
v_sub_f32 v107, v107, v249
v_sub_f32 v43, v43, v249
v_sub_f32 v108, v108, v249
v_sub_f32 v44, v44, v249
v_sub_f32 v109, v109, v249
v_sub_f32 v45, v45, v249
v_sub_f32 v110, v110, v249
v_sub_f32 v46, v46, v249
v_sub_f32 v111, v111, v249
v_sub_f32 v47, v47, v249
s_lshl_b64 exec, 0xffff, 16
v_mul_f32 v236, v236, v250
s_mov_b64 exec, -1
AR v254,a8
v_mul_f32 v254, v254, v250
AW a8,v254
AR v251,a9
v_mul_f32 v251, v251, v250
AW a9,v251
AR v254,a10
v_mul_f32 v254, v254, v250
AW a10,v254
AR v251,a11
v_mul_f32 v251, v251, v250
AW a11,v251
AR v254,a12
v_mul_f32 v254, v254, v250
AW a12,v254
AR v251,a13
v_mul_f32 v251, v251, v250
AW a13,v251
AR v254,a14
v_mul_f32 v254, v254, v250
AW a14,v254
AR v251,a15
v_mul_f32 v251, v251, v250
AW a15,v251
AR v254,a40
v_mul_f32 v254, v254, v250
AW a40,v254
AR v251,a41
v_mul_f32 v251, v251, v250
AW a41,v251
AR v254,a42
v_mul_f32 v254, v254, v250
AW a42,v254
AR v251,a43
v_mul_f32 v251, v251, v250
AW a43,v251
AR v254,a44
v_mul_f32 v254, v254, v250
AW a44,v254
AR v251,a45
v_mul_f32 v251, v251, v250
AW a45,v251
AR v254,a46
v_mul_f32 v254, v254, v250
AW a46,v254
AR v251,a47
v_mul_f32 v251, v251, v250
AW a47,v251
AR v254,a72
v_mul_f32 v254, v254, v250
AW a72,v254
AR v251,a73
v_mul_f32 v251, v251, v250
AW a73,v251
AR v254,a74
v_mul_f32 v254, v254, v250
AW a74,v254
AR v251,a75
v_mul_f32 v251, v251, v250
AW a75,v251
AR v254,a76
v_mul_f32 v254, v254, v250
AW a76,v254
AR v251,a77
v_mul_f32 v251, v251, v250
AW a77,v251
AR v254,a78
v_mul_f32 v254, v254, v250
AW a78,v254
AR v251,a79
v_mul_f32 v251, v251, v250
AW a79,v251
AR v254,a104
v_mul_f32 v254, v254, v250
AW a104,v254
AR v251,a105
v_mul_f32 v251, v251, v250
AW a105,v251
AR v254,a106
v_mul_f32 v254, v254, v250
AW a106,v254
AR v251,a107
v_mul_f32 v251, v251, v250
AW a107,v251
AR v254,a108
v_mul_f32 v254, v254, v250
AW a108,v254
AR v251,a109
v_mul_f32 v251, v251, v250
AW a109,v251
AR v254,a110
v_mul_f32 v254, v254, v250
AW a110,v254
AR v251,a111
v_mul_f32 v251, v251, v250
AW a111,v251
EX v0,v0
EX v1,v1
EX v2,v2
EX v3,v3
EX v4,v4
EX v5,v5
EX v6,v6
EX v7,v7
EX v8,v8
EX v9,v9
EX v10,v10
EX v11,v11
EX v12,v12
EX v13,v13
EX v14,v14
EX v15,v15
EX v32,v32
EX v33,v33
EX v34,v34
EX v35,v35
EX v36,v36
EX v37,v37
EX v38,v38
EX v39,v39
EX v40,v40
EX v41,v41
EX v42,v42
EX v43,v43
EX v44,v44
EX v45,v45
EX v46,v46
EX v47,v47
CV v0,v0,v1
CV v1,v2,v3
CV v2,v4,v5
CV v3,v6,v7
CV v8,v8,v9
CV v9,v10,v11
CV v10,v12,v13
CV v11,v14,v15
CV v4,v32,v33
CV v5,v34,v35
CV v6,v36,v37
CV v7,v38,v39
CV v12,v40,v41
CV v13,v42,v43
CV v14,v44,v45
CV v15,v46,v47
v_smfmac_f32_16x16x64_bf16 v[236:239], v[144:147], v[0:7], v234
s_nop 0
v_smfmac_f32_16x16x64_bf16 v[236:239], v[148:151], v[8:15], v234
s_nop 9
v_add_f32 v246, v236, v237
s_cmp_eq_u32 s73, 0
s_cbranch_scc1 LBACK0_%=
s_cmp_eq_u32 s73, 2
s_cbranch_scc1 LBACK2_%=
s_endpgm
LSITE1_%=:
s_mov_b32 s73, 1
s_mov_b32 s75, 32768
s_mov_b32 s74, 16384
s_branch LRARE102_%=
LRARE102_%=:
s_nop 15
s_waitcnt vmcnt(0) lgkmcnt(0)
v_add_u32 v249, s74, v224
RD a[192:195],v249 offset:0
RD a[208:211],v249 offset:4096
RD a[224:227],v249 offset:8192
RD a[240:243],v249 offset:12288
v_add_u32 v250, s74, v225
RD a[196:199],v250 offset:0
RD a[212:215],v250 offset:4096
RD a[228:231],v250 offset:8192
RD a[244:247],v250 offset:12288
v_add_u32 v251, s74, v226
RD a[200:203],v251 offset:0
RD a[216:219],v251 offset:4096
RD a[232:235],v251 offset:8192
RD a[248:251],v251 offset:12288
v_add_u32 v254, s74, v227
RD a[204:207],v254 offset:0
RD a[220:223],v254 offset:4096
RD a[236:239],v254 offset:8192
RD a[252:255],v254 offset:12288
s_waitcnt lgkmcnt(0)
MM v[64:67],a[192:195],v[160:163],v[128:131]
MM v[64:67],a[196:199],v[176:179],v[64:67]
MM v[64:67],a[200:203],v[192:195],v[64:67]
MM v[64:67],a[204:207],v[208:211],v[64:67]
MM v[68:71],a[220:223],v[208:211],v[128:131]
MM v[68:71],a[216:219],v[192:195],v[68:71]
MM v[68:71],a[212:215],v[176:179],v[68:71]
MM v[68:71],a[208:211],v[160:163],v[68:71]
MM v[76:79],a[208:211],v[164:167],v[132:135]
MM v[76:79],a[212:215],v[180:183],v[76:79]
MM v[76:79],a[216:219],v[196:199],v[76:79]
MM v[76:79],a[220:223],v[212:215],v[76:79]
MM v[72:75],a[204:207],v[212:215],v[132:135]
MM v[72:75],a[200:203],v[196:199],v[72:75]
MM v[72:75],a[196:199],v[180:183],v[72:75]
MM v[72:75],a[192:195],v[164:167],v[72:75]
MM v[96:99],a[224:227],v[160:163],v[128:131]
MM v[96:99],a[228:231],v[176:179],v[96:99]
MM v[96:99],a[232:235],v[192:195],v[96:99]
MM v[96:99],a[236:239],v[208:211],v[96:99]
MM v[100:103],a[252:255],v[208:211],v[128:131]
MM v[100:103],a[248:251],v[192:195],v[100:103]
MM v[100:103],a[244:247],v[176:179],v[100:103]
MM v[100:103],a[240:243],v[160:163],v[100:103]
MM v[108:111],a[240:243],v[164:167],v[132:135]
MM v[108:111],a[244:247],v[180:183],v[108:111]
MM v[108:111],a[248:251],v[196:199],v[108:111]
MM v[108:111],a[252:255],v[212:215],v[108:111]
)ASM"
R"ASM(
MM v[104:107],a[236:239],v[212:215],v[132:135]
MM v[104:107],a[232:235],v[196:199],v[104:107]
MM v[104:107],a[228:231],v[180:183],v[104:107]
MM v[104:107],a[224:227],v[164:167],v[104:107]
s_nop 15
v_add_u32 v249, s75, v224
RD a[192:195],v249 offset:0
RD a[208:211],v249 offset:4096
RD a[224:227],v249 offset:8192
RD a[240:243],v249 offset:12288
v_add_u32 v250, s75, v225
RD a[196:199],v250 offset:0
RD a[212:215],v250 offset:4096
RD a[228:231],v250 offset:8192
RD a[244:247],v250 offset:12288
v_add_u32 v251, s75, v226
RD a[200:203],v251 offset:0
RD a[216:219],v251 offset:4096
RD a[232:235],v251 offset:8192
RD a[248:251],v251 offset:12288
v_add_u32 v254, s75, v227
RD a[204:207],v254 offset:0
RD a[220:223],v254 offset:4096
RD a[236:239],v254 offset:8192
RD a[252:255],v254 offset:12288
s_waitcnt lgkmcnt(0)
v_mov_b32 v236, v246
v_mov_b32 v237, 0
v_max3_f32 v249, v64, v65, v66
v_max3_f32 v251, v96, v97, v98
v_max3_f32 v249, v249, v67, v68
v_max3_f32 v251, v251, v99, v100
v_max3_f32 v249, v249, v69, v70
v_max3_f32 v251, v251, v101, v102
v_max3_f32 v249, v249, v71, v103
v_max_f32 v249, v249, v251
v_log_f32 v250, v246
s_lshl_b64 exec, 0xffff, 0
v_max_f32 v249, v249, v250
s_mov_b64 exec, -1
v_mov_b32 v251, v249
s_nop 1
v_permlane16_swap_b32 v249, v251
v_max_f32 v249, v249, v251
v_mov_b32 v251, v249
s_nop 1
v_permlane32_swap_b32 v249, v251
v_max_f32 v249, v249, v251
v_max_f32 v249, 0, v249
v_sub_f32 v250, 0, v249
EX v250,v250
v_sub_f32 v128, v128, v249
v_sub_f32 v129, v129, v249
v_sub_f32 v130, v130, v249
v_sub_f32 v131, v131, v249
v_sub_f32 v0, v0, v249
v_sub_f32 v64, v64, v249
v_sub_f32 v1, v1, v249
v_sub_f32 v65, v65, v249
v_sub_f32 v2, v2, v249
v_sub_f32 v66, v66, v249
v_sub_f32 v3, v3, v249
v_sub_f32 v67, v67, v249
v_sub_f32 v4, v4, v249
v_sub_f32 v68, v68, v249
v_sub_f32 v5, v5, v249
v_sub_f32 v69, v69, v249
v_sub_f32 v6, v6, v249
v_sub_f32 v70, v70, v249
v_sub_f32 v7, v7, v249
v_sub_f32 v71, v71, v249
v_sub_f32 v32, v32, v249
v_sub_f32 v96, v96, v249
v_sub_f32 v33, v33, v249
v_sub_f32 v97, v97, v249
v_sub_f32 v34, v34, v249
v_sub_f32 v98, v98, v249
v_sub_f32 v35, v35, v249
v_sub_f32 v99, v99, v249
v_sub_f32 v36, v36, v249
v_sub_f32 v100, v100, v249
v_sub_f32 v37, v37, v249
v_sub_f32 v101, v101, v249
v_sub_f32 v38, v38, v249
v_sub_f32 v102, v102, v249
v_sub_f32 v39, v39, v249
v_sub_f32 v103, v103, v249
s_lshl_b64 exec, 0xffff, 0
v_mul_f32 v236, v236, v250
s_mov_b64 exec, -1
AR v254,a0
v_mul_f32 v254, v254, v250
AW a0,v254
AR v251,a1
v_mul_f32 v251, v251, v250
AW a1,v251
AR v254,a2
v_mul_f32 v254, v254, v250
AW a2,v254
AR v251,a3
v_mul_f32 v251, v251, v250
AW a3,v251
AR v254,a4
v_mul_f32 v254, v254, v250
AW a4,v254
AR v251,a5
v_mul_f32 v251, v251, v250
AW a5,v251
AR v254,a6
v_mul_f32 v254, v254, v250
AW a6,v254
AR v251,a7
v_mul_f32 v251, v251, v250
AW a7,v251
AR v254,a32
v_mul_f32 v254, v254, v250
AW a32,v254
AR v251,a33
v_mul_f32 v251, v251, v250
AW a33,v251
AR v254,a34
v_mul_f32 v254, v254, v250
AW a34,v254
AR v251,a35
v_mul_f32 v251, v251, v250
AW a35,v251
AR v254,a36
v_mul_f32 v254, v254, v250
AW a36,v254
AR v251,a37
v_mul_f32 v251, v251, v250
AW a37,v251
AR v254,a38
v_mul_f32 v254, v254, v250
AW a38,v254
AR v251,a39
v_mul_f32 v251, v251, v250
AW a39,v251
AR v254,a64
v_mul_f32 v254, v254, v250
AW a64,v254
AR v251,a65
v_mul_f32 v251, v251, v250
AW a65,v251
AR v254,a66
v_mul_f32 v254, v254, v250
AW a66,v254
AR v251,a67
v_mul_f32 v251, v251, v250
AW a67,v251
AR v254,a68
v_mul_f32 v254, v254, v250
AW a68,v254
AR v251,a69
v_mul_f32 v251, v251, v250
AW a69,v251
AR v254,a70
v_mul_f32 v254, v254, v250
AW a70,v254
AR v251,a71
v_mul_f32 v251, v251, v250
AW a71,v251
AR v254,a96
v_mul_f32 v254, v254, v250
AW a96,v254
AR v251,a97
v_mul_f32 v251, v251, v250
AW a97,v251
AR v254,a98
v_mul_f32 v254, v254, v250
AW a98,v254
AR v251,a99
v_mul_f32 v251, v251, v250
AW a99,v251
AR v254,a100
v_mul_f32 v254, v254, v250
AW a100,v254
AR v251,a101
v_mul_f32 v251, v251, v250
AW a101,v251
AR v254,a102
v_mul_f32 v254, v254, v250
AW a102,v254
AR v251,a103
v_mul_f32 v251, v251, v250
AW a103,v251
v_max3_f32 v249, v72, v73, v74
v_max3_f32 v251, v104, v105, v106
v_max3_f32 v249, v249, v75, v76
v_max3_f32 v251, v251, v107, v108
v_max3_f32 v249, v249, v77, v78
v_max3_f32 v251, v251, v109, v110
v_max3_f32 v249, v249, v79, v111
v_max_f32 v249, v249, v251
v_log_f32 v250, v246
s_lshl_b64 exec, 0xffff, 16
v_max_f32 v249, v249, v250
s_mov_b64 exec, -1
v_mov_b32 v251, v249
s_nop 1
v_permlane16_swap_b32 v249, v251
v_max_f32 v249, v249, v251
v_mov_b32 v251, v249
s_nop 1
v_permlane32_swap_b32 v249, v251
v_max_f32 v249, v249, v251
v_max_f32 v249, 0, v249
v_sub_f32 v250, 0, v249
EX v250,v250
v_sub_f32 v132, v132, v249
v_sub_f32 v133, v133, v249
v_sub_f32 v134, v134, v249
v_sub_f32 v135, v135, v249
v_sub_f32 v8, v8, v249
v_sub_f32 v72, v72, v249
v_sub_f32 v9, v9, v249
v_sub_f32 v73, v73, v249
v_sub_f32 v10, v10, v249
v_sub_f32 v74, v74, v249
v_sub_f32 v11, v11, v249
v_sub_f32 v75, v75, v249
v_sub_f32 v12, v12, v249
v_sub_f32 v76, v76, v249
v_sub_f32 v13, v13, v249
v_sub_f32 v77, v77, v249
v_sub_f32 v14, v14, v249
v_sub_f32 v78, v78, v249
v_sub_f32 v15, v15, v249
v_sub_f32 v79, v79, v249
v_sub_f32 v40, v40, v249
v_sub_f32 v104, v104, v249
v_sub_f32 v41, v41, v249
v_sub_f32 v105, v105, v249
v_sub_f32 v42, v42, v249
v_sub_f32 v106, v106, v249
v_sub_f32 v43, v43, v249
v_sub_f32 v107, v107, v249
v_sub_f32 v44, v44, v249
v_sub_f32 v108, v108, v249
v_sub_f32 v45, v45, v249
v_sub_f32 v109, v109, v249
v_sub_f32 v46, v46, v249
v_sub_f32 v110, v110, v249
v_sub_f32 v47, v47, v249
v_sub_f32 v111, v111, v249
s_lshl_b64 exec, 0xffff, 16
v_mul_f32 v236, v236, v250
s_mov_b64 exec, -1
AR v254,a8
v_mul_f32 v254, v254, v250
AW a8,v254
AR v251,a9
v_mul_f32 v251, v251, v250
AW a9,v251
AR v254,a10
v_mul_f32 v254, v254, v250
AW a10,v254
AR v251,a11
v_mul_f32 v251, v251, v250
AW a11,v251
AR v254,a12
v_mul_f32 v254, v254, v250
AW a12,v254
AR v251,a13
v_mul_f32 v251, v251, v250
AW a13,v251
AR v254,a14
v_mul_f32 v254, v254, v250
AW a14,v254
AR v251,a15
v_mul_f32 v251, v251, v250
AW a15,v251
AR v254,a40
v_mul_f32 v254, v254, v250
AW a40,v254
AR v251,a41
v_mul_f32 v251, v251, v250
AW a41,v251
AR v254,a42
v_mul_f32 v254, v254, v250
AW a42,v254
AR v251,a43
v_mul_f32 v251, v251, v250
AW a43,v251
AR v254,a44
v_mul_f32 v254, v254, v250
AW a44,v254
AR v251,a45
v_mul_f32 v251, v251, v250
AW a45,v251
AR v254,a46
v_mul_f32 v254, v254, v250
AW a46,v254
AR v251,a47
v_mul_f32 v251, v251, v250
AW a47,v251
AR v254,a72
v_mul_f32 v254, v254, v250
AW a72,v254
AR v251,a73
v_mul_f32 v251, v251, v250
AW a73,v251
AR v254,a74
v_mul_f32 v254, v254, v250
AW a74,v254
AR v251,a75
v_mul_f32 v251, v251, v250
AW a75,v251
AR v254,a76
v_mul_f32 v254, v254, v250
AW a76,v254
AR v251,a77
v_mul_f32 v251, v251, v250
AW a77,v251
AR v254,a78
v_mul_f32 v254, v254, v250
AW a78,v254
AR v251,a79
v_mul_f32 v251, v251, v250
AW a79,v251
AR v254,a104
v_mul_f32 v254, v254, v250
AW a104,v254
AR v251,a105
v_mul_f32 v251, v251, v250
AW a105,v251
AR v254,a106
v_mul_f32 v254, v254, v250
AW a106,v254
AR v251,a107
v_mul_f32 v251, v251, v250
AW a107,v251
AR v254,a108
v_mul_f32 v254, v254, v250
AW a108,v254
AR v251,a109
v_mul_f32 v251, v251, v250
AW a109,v251
AR v254,a110
v_mul_f32 v254, v254, v250
AW a110,v254
AR v251,a111
v_mul_f32 v251, v251, v250
AW a111,v251
EX v64,v64
EX v65,v65
EX v66,v66
EX v67,v67
EX v68,v68
EX v69,v69
EX v70,v70
EX v71,v71
EX v72,v72
EX v73,v73
EX v74,v74
EX v75,v75
EX v76,v76
EX v77,v77
EX v78,v78
EX v79,v79
EX v96,v96
EX v97,v97
EX v98,v98
EX v99,v99
EX v100,v100
EX v101,v101
EX v102,v102
EX v103,v103
EX v104,v104
EX v105,v105
EX v106,v106
EX v107,v107
EX v108,v108
EX v109,v109
EX v110,v110
EX v111,v111
CV v64,v64,v65
CV v65,v66,v67
CV v66,v68,v69
CV v67,v70,v71
CV v72,v72,v73
CV v73,v74,v75
CV v74,v76,v77
CV v75,v78,v79
CV v68,v96,v97
CV v69,v98,v99
CV v70,v100,v101
CV v71,v102,v103
CV v76,v104,v105
CV v77,v106,v107
CV v78,v108,v109
CV v79,v110,v111
v_smfmac_f32_16x16x64_bf16 v[236:239], v[144:147], v[64:71], v234
s_nop 0
v_smfmac_f32_16x16x64_bf16 v[236:239], v[148:151], v[72:79], v234
s_nop 9
v_add_f32 v247, v236, v237
s_cmp_eq_u32 s73, 1
s_cbranch_scc1 LBACK1_%=
s_endpgm
LSITE3_%=:
s_mov_b32 s73, 3
s_mov_b32 s75, 0
s_mov_b32 s74, 49152
s_branch LRARE104_%=
LSITE5_%=:
s_mov_b32 s73, 5
s_mov_b32 s75, 32768
s_mov_b32 s74, 16384
s_branch LRARE104_%=
LSITE7_%=:
s_mov_b32 s73, 7
s_mov_b32 s75, 0
s_mov_b32 s74, 49152
s_branch LRARE104_%=
LSITE9_%=:
s_mov_b32 s73, 9
s_mov_b32 s75, 32768
s_mov_b32 s74, 16384
s_branch LRARE104_%=
LSITE11_%=:
s_mov_b32 s73, 11
s_mov_b32 s75, 0
s_mov_b32 s74, 49152
s_branch LRARE104_%=
LSITE13_%=:
s_mov_b32 s73, 13
s_mov_b32 s75, 32768
s_mov_b32 s74, 16384
s_branch LRARE104_%=
LSITE15_%=:
s_mov_b32 s73, 15
s_mov_b32 s75, 0
s_mov_b32 s74, 49152
s_branch LRARE104_%=
LSITE17_%=:
s_mov_b32 s73, 17
s_mov_b32 s75, 32768
s_mov_b32 s74, 16384
s_branch LRARE104_%=
LSITE19_%=:
s_mov_b32 s73, 19
s_mov_b32 s75, 0
s_mov_b32 s74, 49152
s_branch LRARE104_%=
LSITE21_%=:
s_mov_b32 s73, 21
s_mov_b32 s75, 32768
s_mov_b32 s74, 16384
s_branch LRARE104_%=
LSITE23_%=:
s_mov_b32 s73, 23
s_mov_b32 s75, 0
s_mov_b32 s74, 49152
s_branch LRARE104_%=
LSITE25_%=:
s_mov_b32 s73, 25
s_mov_b32 s75, 32768
s_mov_b32 s74, 16384
s_branch LRARE104_%=
LSITE27_%=:
s_mov_b32 s73, 27
s_mov_b32 s75, 0
s_mov_b32 s74, 49152
s_branch LRARE104_%=
LSITE29_%=:
s_mov_b32 s73, 29
s_mov_b32 s75, 32768
s_mov_b32 s74, 16384
s_branch LRARE104_%=
LSITE31_%=:
s_mov_b32 s73, 31
s_mov_b32 s75, 0
s_mov_b32 s74, 49152
s_branch LRARE104_%=
LRARE104_%=:
s_nop 15
s_waitcnt vmcnt(0) lgkmcnt(0)
v_add_u32 v249, s74, v224
RD a[192:195],v249 offset:0
RD a[208:211],v249 offset:4096
RD a[224:227],v249 offset:8192
RD a[240:243],v249 offset:12288
v_add_u32 v250, s74, v225
RD a[196:199],v250 offset:0
RD a[212:215],v250 offset:4096
RD a[228:231],v250 offset:8192
RD a[244:247],v250 offset:12288
v_add_u32 v251, s74, v226
RD a[200:203],v251 offset:0
RD a[216:219],v251 offset:4096
RD a[232:235],v251 offset:8192
RD a[248:251],v251 offset:12288
v_add_u32 v254, s74, v227
RD a[204:207],v254 offset:0
RD a[220:223],v254 offset:4096
RD a[236:239],v254 offset:8192
RD a[252:255],v254 offset:12288
s_waitcnt lgkmcnt(0)
MM v[64:67],a[192:195],v[160:163],v[128:131]
MM v[64:67],a[196:199],v[176:179],v[64:67]
MM v[64:67],a[200:203],v[192:195],v[64:67]
MM v[64:67],a[204:207],v[208:211],v[64:67]
MM v[68:71],a[220:223],v[208:211],v[128:131]
MM v[68:71],a[216:219],v[192:195],v[68:71]
MM v[68:71],a[212:215],v[176:179],v[68:71]
MM v[68:71],a[208:211],v[160:163],v[68:71]
MM v[76:79],a[208:211],v[164:167],v[132:135]
MM v[76:79],a[212:215],v[180:183],v[76:79]
MM v[76:79],a[216:219],v[196:199],v[76:79]
MM v[76:79],a[220:223],v[212:215],v[76:79]
MM v[72:75],a[204:207],v[212:215],v[132:135]
MM v[72:75],a[200:203],v[196:199],v[72:75]
MM v[72:75],a[196:199],v[180:183],v[72:75]
MM v[72:75],a[192:195],v[164:167],v[72:75]
MM v[80:83],a[192:195],v[168:171],v[136:139]
MM v[80:83],a[196:199],v[184:187],v[80:83]
MM v[80:83],a[200:203],v[200:203],v[80:83]
MM v[80:83],a[204:207],v[216:219],v[80:83]
MM v[84:87],a[220:223],v[216:219],v[136:139]
MM v[84:87],a[216:219],v[200:203],v[84:87]
MM v[84:87],a[212:215],v[184:187],v[84:87]
MM v[84:87],a[208:211],v[168:171],v[84:87]
MM v[92:95],a[208:211],v[172:175],v[140:143]
MM v[92:95],a[212:215],v[188:191],v[92:95]
MM v[92:95],a[216:219],v[204:207],v[92:95]
MM v[92:95],a[220:223],v[220:223],v[92:95]
MM v[88:91],a[204:207],v[220:223],v[140:143]
MM v[88:91],a[200:203],v[204:207],v[88:91]
MM v[88:91],a[196:199],v[188:191],v[88:91]
MM v[88:91],a[192:195],v[172:175],v[88:91]
MM v[96:99],a[224:227],v[160:163],v[128:131]
MM v[96:99],a[228:231],v[176:179],v[96:99]
MM v[96:99],a[232:235],v[192:195],v[96:99]
MM v[96:99],a[236:239],v[208:211],v[96:99]
MM v[100:103],a[252:255],v[208:211],v[128:131]
MM v[100:103],a[248:251],v[192:195],v[100:103]
MM v[100:103],a[244:247],v[176:179],v[100:103]
MM v[100:103],a[240:243],v[160:163],v[100:103]
MM v[108:111],a[240:243],v[164:167],v[132:135]
MM v[108:111],a[244:247],v[180:183],v[108:111]
MM v[108:111],a[248:251],v[196:199],v[108:111]
)ASM"
R"ASM(
MM v[108:111],a[252:255],v[212:215],v[108:111]
MM v[104:107],a[236:239],v[212:215],v[132:135]
MM v[104:107],a[232:235],v[196:199],v[104:107]
MM v[104:107],a[228:231],v[180:183],v[104:107]
MM v[104:107],a[224:227],v[164:167],v[104:107]
MM v[112:115],a[224:227],v[168:171],v[136:139]
MM v[112:115],a[228:231],v[184:187],v[112:115]
MM v[112:115],a[232:235],v[200:203],v[112:115]
MM v[112:115],a[236:239],v[216:219],v[112:115]
MM v[116:119],a[252:255],v[216:219],v[136:139]
MM v[116:119],a[248:251],v[200:203],v[116:119]
MM v[116:119],a[244:247],v[184:187],v[116:119]
MM v[116:119],a[240:243],v[168:171],v[116:119]
MM v[124:127],a[240:243],v[172:175],v[140:143]
MM v[124:127],a[244:247],v[188:191],v[124:127]
MM v[124:127],a[248:251],v[204:207],v[124:127]
MM v[124:127],a[252:255],v[220:223],v[124:127]
MM v[120:123],a[236:239],v[220:223],v[140:143]
MM v[120:123],a[232:235],v[204:207],v[120:123]
MM v[120:123],a[228:231],v[188:191],v[120:123]
MM v[120:123],a[224:227],v[172:175],v[120:123]
s_nop 15
v_add_u32 v249, s75, v224
RD a[192:195],v249 offset:0
RD a[208:211],v249 offset:4096
RD a[224:227],v249 offset:8192
RD a[240:243],v249 offset:12288
v_add_u32 v250, s75, v225
RD a[196:199],v250 offset:0
RD a[212:215],v250 offset:4096
RD a[228:231],v250 offset:8192
RD a[244:247],v250 offset:12288
v_add_u32 v251, s75, v226
RD a[200:203],v251 offset:0
RD a[216:219],v251 offset:4096
RD a[232:235],v251 offset:8192
RD a[248:251],v251 offset:12288
v_add_u32 v254, s75, v227
RD a[204:207],v254 offset:0
RD a[220:223],v254 offset:4096
RD a[236:239],v254 offset:8192
RD a[252:255],v254 offset:12288
s_waitcnt lgkmcnt(0)
v_mov_b32 v236, v246
v_mov_b32 v237, 0
v_max3_f32 v249, v64, v65, v66
v_max3_f32 v251, v96, v97, v98
v_max3_f32 v249, v249, v67, v68
v_max3_f32 v251, v251, v99, v100
v_max3_f32 v249, v249, v69, v70
v_max3_f32 v251, v251, v101, v102
v_max3_f32 v249, v249, v71, v103
v_max_f32 v249, v249, v251
v_log_f32 v250, v246
s_lshl_b64 exec, 0xffff, 0
v_max_f32 v249, v249, v250
s_mov_b64 exec, -1
v_mov_b32 v251, v249
s_nop 1
v_permlane16_swap_b32 v249, v251
v_max_f32 v249, v249, v251
v_mov_b32 v251, v249
s_nop 1
v_permlane32_swap_b32 v249, v251
v_max_f32 v249, v249, v251
v_max_f32 v249, 0, v249
v_sub_f32 v250, 0, v249
EX v250,v250
v_sub_f32 v128, v128, v249
v_sub_f32 v129, v129, v249
v_sub_f32 v130, v130, v249
v_sub_f32 v131, v131, v249
v_sub_f32 v0, v0, v249
v_sub_f32 v64, v64, v249
v_sub_f32 v1, v1, v249
v_sub_f32 v65, v65, v249
v_sub_f32 v2, v2, v249
v_sub_f32 v66, v66, v249
v_sub_f32 v3, v3, v249
v_sub_f32 v67, v67, v249
v_sub_f32 v4, v4, v249
v_sub_f32 v68, v68, v249
v_sub_f32 v5, v5, v249
v_sub_f32 v69, v69, v249
v_sub_f32 v6, v6, v249
v_sub_f32 v70, v70, v249
v_sub_f32 v7, v7, v249
v_sub_f32 v71, v71, v249
v_sub_f32 v32, v32, v249
v_sub_f32 v96, v96, v249
v_sub_f32 v33, v33, v249
v_sub_f32 v97, v97, v249
v_sub_f32 v34, v34, v249
v_sub_f32 v98, v98, v249
v_sub_f32 v35, v35, v249
v_sub_f32 v99, v99, v249
v_sub_f32 v36, v36, v249
v_sub_f32 v100, v100, v249
v_sub_f32 v37, v37, v249
v_sub_f32 v101, v101, v249
v_sub_f32 v38, v38, v249
v_sub_f32 v102, v102, v249
v_sub_f32 v39, v39, v249
v_sub_f32 v103, v103, v249
s_lshl_b64 exec, 0xffff, 0
v_mul_f32 v236, v236, v250
s_mov_b64 exec, -1
AR v254,a0
v_mul_f32 v254, v254, v250
AW a0,v254
AR v251,a1
v_mul_f32 v251, v251, v250
AW a1,v251
AR v254,a2
v_mul_f32 v254, v254, v250
AW a2,v254
AR v251,a3
v_mul_f32 v251, v251, v250
AW a3,v251
AR v254,a4
v_mul_f32 v254, v254, v250
AW a4,v254
AR v251,a5
v_mul_f32 v251, v251, v250
AW a5,v251
AR v254,a6
v_mul_f32 v254, v254, v250
AW a6,v254
AR v251,a7
v_mul_f32 v251, v251, v250
AW a7,v251
AR v254,a32
v_mul_f32 v254, v254, v250
AW a32,v254
AR v251,a33
v_mul_f32 v251, v251, v250
AW a33,v251
AR v254,a34
v_mul_f32 v254, v254, v250
AW a34,v254
AR v251,a35
v_mul_f32 v251, v251, v250
AW a35,v251
AR v254,a36
v_mul_f32 v254, v254, v250
AW a36,v254
AR v251,a37
v_mul_f32 v251, v251, v250
AW a37,v251
AR v254,a38
v_mul_f32 v254, v254, v250
AW a38,v254
AR v251,a39
v_mul_f32 v251, v251, v250
AW a39,v251
AR v254,a64
v_mul_f32 v254, v254, v250
AW a64,v254
AR v251,a65
v_mul_f32 v251, v251, v250
AW a65,v251
AR v254,a66
v_mul_f32 v254, v254, v250
AW a66,v254
AR v251,a67
v_mul_f32 v251, v251, v250
AW a67,v251
AR v254,a68
v_mul_f32 v254, v254, v250
AW a68,v254
AR v251,a69
v_mul_f32 v251, v251, v250
AW a69,v251
AR v254,a70
v_mul_f32 v254, v254, v250
AW a70,v254
AR v251,a71
v_mul_f32 v251, v251, v250
AW a71,v251
AR v254,a96
v_mul_f32 v254, v254, v250
AW a96,v254
AR v251,a97
v_mul_f32 v251, v251, v250
AW a97,v251
AR v254,a98
v_mul_f32 v254, v254, v250
AW a98,v254
AR v251,a99
v_mul_f32 v251, v251, v250
AW a99,v251
AR v254,a100
v_mul_f32 v254, v254, v250
AW a100,v254
AR v251,a101
v_mul_f32 v251, v251, v250
AW a101,v251
AR v254,a102
v_mul_f32 v254, v254, v250
AW a102,v254
AR v251,a103
v_mul_f32 v251, v251, v250
AW a103,v251
v_max3_f32 v249, v72, v73, v74
v_max3_f32 v251, v104, v105, v106
v_max3_f32 v249, v249, v75, v76
v_max3_f32 v251, v251, v107, v108
v_max3_f32 v249, v249, v77, v78
v_max3_f32 v251, v251, v109, v110
v_max3_f32 v249, v249, v79, v111
v_max_f32 v249, v249, v251
v_log_f32 v250, v246
s_lshl_b64 exec, 0xffff, 16
v_max_f32 v249, v249, v250
s_mov_b64 exec, -1
v_mov_b32 v251, v249
s_nop 1
v_permlane16_swap_b32 v249, v251
v_max_f32 v249, v249, v251
v_mov_b32 v251, v249
s_nop 1
v_permlane32_swap_b32 v249, v251
v_max_f32 v249, v249, v251
v_max_f32 v249, 0, v249
v_sub_f32 v250, 0, v249
EX v250,v250
v_sub_f32 v132, v132, v249
v_sub_f32 v133, v133, v249
v_sub_f32 v134, v134, v249
v_sub_f32 v135, v135, v249
v_sub_f32 v8, v8, v249
v_sub_f32 v72, v72, v249
v_sub_f32 v9, v9, v249
v_sub_f32 v73, v73, v249
v_sub_f32 v10, v10, v249
v_sub_f32 v74, v74, v249
v_sub_f32 v11, v11, v249
v_sub_f32 v75, v75, v249
v_sub_f32 v12, v12, v249
v_sub_f32 v76, v76, v249
v_sub_f32 v13, v13, v249
v_sub_f32 v77, v77, v249
v_sub_f32 v14, v14, v249
v_sub_f32 v78, v78, v249
v_sub_f32 v15, v15, v249
v_sub_f32 v79, v79, v249
v_sub_f32 v40, v40, v249
v_sub_f32 v104, v104, v249
v_sub_f32 v41, v41, v249
v_sub_f32 v105, v105, v249
v_sub_f32 v42, v42, v249
v_sub_f32 v106, v106, v249
v_sub_f32 v43, v43, v249
v_sub_f32 v107, v107, v249
v_sub_f32 v44, v44, v249
v_sub_f32 v108, v108, v249
v_sub_f32 v45, v45, v249
v_sub_f32 v109, v109, v249
v_sub_f32 v46, v46, v249
v_sub_f32 v110, v110, v249
v_sub_f32 v47, v47, v249
v_sub_f32 v111, v111, v249
s_lshl_b64 exec, 0xffff, 16
v_mul_f32 v236, v236, v250
s_mov_b64 exec, -1
AR v254,a8
v_mul_f32 v254, v254, v250
AW a8,v254
AR v251,a9
v_mul_f32 v251, v251, v250
AW a9,v251
AR v254,a10
v_mul_f32 v254, v254, v250
AW a10,v254
AR v251,a11
v_mul_f32 v251, v251, v250
AW a11,v251
AR v254,a12
v_mul_f32 v254, v254, v250
AW a12,v254
AR v251,a13
v_mul_f32 v251, v251, v250
AW a13,v251
AR v254,a14
v_mul_f32 v254, v254, v250
AW a14,v254
AR v251,a15
v_mul_f32 v251, v251, v250
AW a15,v251
AR v254,a40
v_mul_f32 v254, v254, v250
AW a40,v254
AR v251,a41
v_mul_f32 v251, v251, v250
AW a41,v251
AR v254,a42
v_mul_f32 v254, v254, v250
AW a42,v254
AR v251,a43
v_mul_f32 v251, v251, v250
AW a43,v251
AR v254,a44
v_mul_f32 v254, v254, v250
AW a44,v254
AR v251,a45
v_mul_f32 v251, v251, v250
AW a45,v251
AR v254,a46
v_mul_f32 v254, v254, v250
AW a46,v254
AR v251,a47
v_mul_f32 v251, v251, v250
AW a47,v251
AR v254,a72
v_mul_f32 v254, v254, v250
AW a72,v254
AR v251,a73
v_mul_f32 v251, v251, v250
AW a73,v251
AR v254,a74
v_mul_f32 v254, v254, v250
AW a74,v254
AR v251,a75
v_mul_f32 v251, v251, v250
AW a75,v251
AR v254,a76
v_mul_f32 v254, v254, v250
AW a76,v254
AR v251,a77
v_mul_f32 v251, v251, v250
AW a77,v251
AR v254,a78
v_mul_f32 v254, v254, v250
AW a78,v254
AR v251,a79
v_mul_f32 v251, v251, v250
AW a79,v251
AR v254,a104
v_mul_f32 v254, v254, v250
AW a104,v254
AR v251,a105
v_mul_f32 v251, v251, v250
AW a105,v251
AR v254,a106
v_mul_f32 v254, v254, v250
AW a106,v254
AR v251,a107
v_mul_f32 v251, v251, v250
AW a107,v251
AR v254,a108
v_mul_f32 v254, v254, v250
AW a108,v254
AR v251,a109
v_mul_f32 v251, v251, v250
AW a109,v251
AR v254,a110
v_mul_f32 v254, v254, v250
AW a110,v254
AR v251,a111
v_mul_f32 v251, v251, v250
AW a111,v251
v_max3_f32 v249, v80, v81, v82
v_max3_f32 v251, v112, v113, v114
v_max3_f32 v249, v249, v83, v84
v_max3_f32 v251, v251, v115, v116
v_max3_f32 v249, v249, v85, v86
v_max3_f32 v251, v251, v117, v118
v_max3_f32 v249, v249, v87, v119
v_max_f32 v249, v249, v251
v_log_f32 v250, v246
s_lshl_b64 exec, 0xffff, 32
v_max_f32 v249, v249, v250
s_mov_b64 exec, -1
v_mov_b32 v251, v249
s_nop 1
v_permlane16_swap_b32 v249, v251
v_max_f32 v249, v249, v251
v_mov_b32 v251, v249
s_nop 1
v_permlane32_swap_b32 v249, v251
v_max_f32 v249, v249, v251
v_max_f32 v249, 0, v249
v_sub_f32 v250, 0, v249
EX v250,v250
v_sub_f32 v136, v136, v249
v_sub_f32 v137, v137, v249
v_sub_f32 v138, v138, v249
v_sub_f32 v139, v139, v249
v_sub_f32 v16, v16, v249
v_sub_f32 v80, v80, v249
v_sub_f32 v17, v17, v249
v_sub_f32 v81, v81, v249
v_sub_f32 v18, v18, v249
v_sub_f32 v82, v82, v249
v_sub_f32 v19, v19, v249
v_sub_f32 v83, v83, v249
v_sub_f32 v20, v20, v249
v_sub_f32 v84, v84, v249
v_sub_f32 v21, v21, v249
v_sub_f32 v85, v85, v249
v_sub_f32 v22, v22, v249
v_sub_f32 v86, v86, v249
v_sub_f32 v23, v23, v249
v_sub_f32 v87, v87, v249
v_sub_f32 v48, v48, v249
v_sub_f32 v112, v112, v249
v_sub_f32 v49, v49, v249
v_sub_f32 v113, v113, v249
v_sub_f32 v50, v50, v249
v_sub_f32 v114, v114, v249
v_sub_f32 v51, v51, v249
v_sub_f32 v115, v115, v249
v_sub_f32 v52, v52, v249
v_sub_f32 v116, v116, v249
v_sub_f32 v53, v53, v249
v_sub_f32 v117, v117, v249
v_sub_f32 v54, v54, v249
v_sub_f32 v118, v118, v249
v_sub_f32 v55, v55, v249
v_sub_f32 v119, v119, v249
s_lshl_b64 exec, 0xffff, 32
v_mul_f32 v236, v236, v250
s_mov_b64 exec, -1
AR v254,a16
v_mul_f32 v254, v254, v250
AW a16,v254
AR v251,a17
v_mul_f32 v251, v251, v250
AW a17,v251
AR v254,a18
v_mul_f32 v254, v254, v250
AW a18,v254
AR v251,a19
v_mul_f32 v251, v251, v250
AW a19,v251
AR v254,a20
v_mul_f32 v254, v254, v250
AW a20,v254
AR v251,a21
v_mul_f32 v251, v251, v250
AW a21,v251
AR v254,a22
v_mul_f32 v254, v254, v250
AW a22,v254
AR v251,a23
v_mul_f32 v251, v251, v250
AW a23,v251
AR v254,a48
v_mul_f32 v254, v254, v250
AW a48,v254
AR v251,a49
v_mul_f32 v251, v251, v250
AW a49,v251
AR v254,a50
v_mul_f32 v254, v254, v250
AW a50,v254
AR v251,a51
v_mul_f32 v251, v251, v250
AW a51,v251
AR v254,a52
v_mul_f32 v254, v254, v250
AW a52,v254
AR v251,a53
v_mul_f32 v251, v251, v250
AW a53,v251
AR v254,a54
v_mul_f32 v254, v254, v250
AW a54,v254
AR v251,a55
v_mul_f32 v251, v251, v250
AW a55,v251
AR v254,a80
v_mul_f32 v254, v254, v250
AW a80,v254
AR v251,a81
v_mul_f32 v251, v251, v250
AW a81,v251
AR v254,a82
v_mul_f32 v254, v254, v250
AW a82,v254
AR v251,a83
v_mul_f32 v251, v251, v250
AW a83,v251
AR v254,a84
v_mul_f32 v254, v254, v250
AW a84,v254
AR v251,a85
v_mul_f32 v251, v251, v250
AW a85,v251
AR v254,a86
v_mul_f32 v254, v254, v250
AW a86,v254
AR v251,a87
v_mul_f32 v251, v251, v250
AW a87,v251
AR v254,a112
v_mul_f32 v254, v254, v250
AW a112,v254
AR v251,a113
v_mul_f32 v251, v251, v250
AW a113,v251
AR v254,a114
v_mul_f32 v254, v254, v250
AW a114,v254
AR v251,a115
v_mul_f32 v251, v251, v250
AW a115,v251
AR v254,a116
v_mul_f32 v254, v254, v250
AW a116,v254
AR v251,a117
v_mul_f32 v251, v251, v250
AW a117,v251
AR v254,a118
v_mul_f32 v254, v254, v250
AW a118,v254
AR v251,a119
v_mul_f32 v251, v251, v250
AW a119,v251
v_max3_f32 v249, v88, v89, v90
v_max3_f32 v251, v120, v121, v122
v_max3_f32 v249, v249, v91, v92
v_max3_f32 v251, v251, v123, v124
v_max3_f32 v249, v249, v93, v94
v_max3_f32 v251, v251, v125, v126
v_max3_f32 v249, v249, v95, v127
v_max_f32 v249, v249, v251
v_log_f32 v250, v246
s_lshl_b64 exec, 0xffff, 48
v_max_f32 v249, v249, v250
s_mov_b64 exec, -1
v_mov_b32 v251, v249
s_nop 1
v_permlane16_swap_b32 v249, v251
v_max_f32 v249, v249, v251
v_mov_b32 v251, v249
s_nop 1
v_permlane32_swap_b32 v249, v251
v_max_f32 v249, v249, v251
v_max_f32 v249, 0, v249
v_sub_f32 v250, 0, v249
EX v250,v250
v_sub_f32 v140, v140, v249
v_sub_f32 v141, v141, v249
v_sub_f32 v142, v142, v249
v_sub_f32 v143, v143, v249
v_sub_f32 v24, v24, v249
)ASM"
R"ASM(
v_sub_f32 v88, v88, v249
v_sub_f32 v25, v25, v249
v_sub_f32 v89, v89, v249
v_sub_f32 v26, v26, v249
v_sub_f32 v90, v90, v249
v_sub_f32 v27, v27, v249
v_sub_f32 v91, v91, v249
v_sub_f32 v28, v28, v249
v_sub_f32 v92, v92, v249
v_sub_f32 v29, v29, v249
v_sub_f32 v93, v93, v249
v_sub_f32 v30, v30, v249
v_sub_f32 v94, v94, v249
v_sub_f32 v31, v31, v249
v_sub_f32 v95, v95, v249
v_sub_f32 v56, v56, v249
v_sub_f32 v120, v120, v249
v_sub_f32 v57, v57, v249
v_sub_f32 v121, v121, v249
v_sub_f32 v58, v58, v249
v_sub_f32 v122, v122, v249
v_sub_f32 v59, v59, v249
v_sub_f32 v123, v123, v249
v_sub_f32 v60, v60, v249
v_sub_f32 v124, v124, v249
v_sub_f32 v61, v61, v249
v_sub_f32 v125, v125, v249
v_sub_f32 v62, v62, v249
v_sub_f32 v126, v126, v249
v_sub_f32 v63, v63, v249
v_sub_f32 v127, v127, v249
s_lshl_b64 exec, 0xffff, 48
v_mul_f32 v236, v236, v250
s_mov_b64 exec, -1
AR v254,a24
v_mul_f32 v254, v254, v250
AW a24,v254
AR v251,a25
v_mul_f32 v251, v251, v250
AW a25,v251
AR v254,a26
v_mul_f32 v254, v254, v250
AW a26,v254
AR v251,a27
v_mul_f32 v251, v251, v250
AW a27,v251
AR v254,a28
v_mul_f32 v254, v254, v250
AW a28,v254
AR v251,a29
v_mul_f32 v251, v251, v250
AW a29,v251
AR v254,a30
v_mul_f32 v254, v254, v250
AW a30,v254
AR v251,a31
v_mul_f32 v251, v251, v250
AW a31,v251
AR v254,a56
v_mul_f32 v254, v254, v250
AW a56,v254
AR v251,a57
v_mul_f32 v251, v251, v250
AW a57,v251
AR v254,a58
v_mul_f32 v254, v254, v250
AW a58,v254
AR v251,a59
v_mul_f32 v251, v251, v250
AW a59,v251
AR v254,a60
v_mul_f32 v254, v254, v250
AW a60,v254
AR v251,a61
v_mul_f32 v251, v251, v250
AW a61,v251
AR v254,a62
v_mul_f32 v254, v254, v250
AW a62,v254
AR v251,a63
v_mul_f32 v251, v251, v250
AW a63,v251
AR v254,a88
v_mul_f32 v254, v254, v250
AW a88,v254
AR v251,a89
v_mul_f32 v251, v251, v250
AW a89,v251
AR v254,a90
v_mul_f32 v254, v254, v250
AW a90,v254
AR v251,a91
v_mul_f32 v251, v251, v250
AW a91,v251
AR v254,a92
v_mul_f32 v254, v254, v250
AW a92,v254
AR v251,a93
v_mul_f32 v251, v251, v250
AW a93,v251
AR v254,a94
v_mul_f32 v254, v254, v250
AW a94,v254
AR v251,a95
v_mul_f32 v251, v251, v250
AW a95,v251
AR v254,a120
v_mul_f32 v254, v254, v250
AW a120,v254
AR v251,a121
v_mul_f32 v251, v251, v250
AW a121,v251
AR v254,a122
v_mul_f32 v254, v254, v250
AW a122,v254
AR v251,a123
v_mul_f32 v251, v251, v250
AW a123,v251
AR v254,a124
v_mul_f32 v254, v254, v250
AW a124,v254
AR v251,a125
v_mul_f32 v251, v251, v250
AW a125,v251
AR v254,a126
v_mul_f32 v254, v254, v250
AW a126,v254
AR v251,a127
v_mul_f32 v251, v251, v250
AW a127,v251
EX v64,v64
EX v65,v65
EX v66,v66
EX v67,v67
EX v68,v68
EX v69,v69
EX v70,v70
EX v71,v71
EX v72,v72
EX v73,v73
EX v74,v74
EX v75,v75
EX v76,v76
EX v77,v77
EX v78,v78
EX v79,v79
EX v80,v80
EX v81,v81
EX v82,v82
EX v83,v83
EX v84,v84
EX v85,v85
EX v86,v86
EX v87,v87
EX v88,v88
EX v89,v89
EX v90,v90
EX v91,v91
EX v92,v92
EX v93,v93
EX v94,v94
EX v95,v95
EX v96,v96
EX v97,v97
EX v98,v98
EX v99,v99
EX v100,v100
EX v101,v101
EX v102,v102
EX v103,v103
EX v104,v104
EX v105,v105
EX v106,v106
EX v107,v107
EX v108,v108
EX v109,v109
EX v110,v110
EX v111,v111
EX v112,v112
EX v113,v113
EX v114,v114
EX v115,v115
EX v116,v116
EX v117,v117
EX v118,v118
EX v119,v119
EX v120,v120
EX v121,v121
EX v122,v122
EX v123,v123
EX v124,v124
EX v125,v125
EX v126,v126
EX v127,v127
CV v64,v64,v65
CV v65,v66,v67
CV v66,v68,v69
CV v67,v70,v71
CV v72,v72,v73
CV v73,v74,v75
CV v74,v76,v77
CV v75,v78,v79
CV v80,v80,v81
CV v81,v82,v83
CV v82,v84,v85
CV v83,v86,v87
CV v88,v88,v89
CV v89,v90,v91
CV v90,v92,v93
CV v91,v94,v95
CV v68,v96,v97
CV v69,v98,v99
CV v70,v100,v101
CV v71,v102,v103
CV v76,v104,v105
CV v77,v106,v107
CV v78,v108,v109
CV v79,v110,v111
CV v84,v112,v113
CV v85,v114,v115
CV v86,v116,v117
CV v87,v118,v119
CV v92,v120,v121
CV v93,v122,v123
CV v94,v124,v125
CV v95,v126,v127
v_smfmac_f32_16x16x64_bf16 v[236:239], v[144:147], v[64:71], v234
v_smfmac_f32_16x16x64_bf16 v[236:239], v[148:151], v[72:79], v234
v_smfmac_f32_16x16x64_bf16 v[236:239], v[152:155], v[80:87], v234
v_smfmac_f32_16x16x64_bf16 v[236:239], v[156:159], v[88:95], v234
s_nop 9
v_add_f32 v247, v236, v237
s_cmp_eq_u32 s73, 3
s_cbranch_scc1 LBACK3_%=
s_cmp_eq_u32 s73, 5
s_cbranch_scc1 LBACK5_%=
s_cmp_eq_u32 s73, 7
s_cbranch_scc1 LBACK7_%=
s_cmp_eq_u32 s73, 9
s_cbranch_scc1 LBACK9_%=
s_cmp_eq_u32 s73, 11
s_cbranch_scc1 LBACK11_%=
s_cmp_eq_u32 s73, 13
s_cbranch_scc1 LBACK13_%=
s_cmp_eq_u32 s73, 15
s_cbranch_scc1 LBACK15_%=
s_cmp_eq_u32 s73, 17
s_cbranch_scc1 LBACK17_%=
s_cmp_eq_u32 s73, 19
s_cbranch_scc1 LBACK19_%=
s_cmp_eq_u32 s73, 21
s_cbranch_scc1 LBACK21_%=
s_cmp_eq_u32 s73, 23
s_cbranch_scc1 LBACK23_%=
s_cmp_eq_u32 s73, 25
s_cbranch_scc1 LBACK25_%=
s_cmp_eq_u32 s73, 27
s_cbranch_scc1 LBACK27_%=
s_cmp_eq_u32 s73, 29
s_cbranch_scc1 LBACK29_%=
s_cmp_eq_u32 s73, 31
s_cbranch_scc1 LBACK31_%=
s_endpgm
LSITE4_%=:
s_mov_b32 s73, 4
s_mov_b32 s75, 16384
s_mov_b32 s74, 0
s_branch LRARE004_%=
LSITE6_%=:
s_mov_b32 s73, 6
s_mov_b32 s75, 49152
s_mov_b32 s74, 32768
s_branch LRARE004_%=
LSITE8_%=:
s_mov_b32 s73, 8
s_mov_b32 s75, 16384
s_mov_b32 s74, 0
s_branch LRARE004_%=
LSITE10_%=:
s_mov_b32 s73, 10
s_mov_b32 s75, 49152
s_mov_b32 s74, 32768
s_branch LRARE004_%=
LSITE12_%=:
s_mov_b32 s73, 12
s_mov_b32 s75, 16384
s_mov_b32 s74, 0
s_branch LRARE004_%=
LSITE14_%=:
s_mov_b32 s73, 14
s_mov_b32 s75, 49152
s_mov_b32 s74, 32768
s_branch LRARE004_%=
LSITE16_%=:
s_mov_b32 s73, 16
s_mov_b32 s75, 16384
s_mov_b32 s74, 0
s_branch LRARE004_%=
LSITE18_%=:
s_mov_b32 s73, 18
s_mov_b32 s75, 49152
s_mov_b32 s74, 32768
s_branch LRARE004_%=
LSITE20_%=:
s_mov_b32 s73, 20
s_mov_b32 s75, 16384
s_mov_b32 s74, 0
s_branch LRARE004_%=
LSITE22_%=:
s_mov_b32 s73, 22
s_mov_b32 s75, 49152
s_mov_b32 s74, 32768
s_branch LRARE004_%=
LSITE24_%=:
s_mov_b32 s73, 24
s_mov_b32 s75, 16384
s_mov_b32 s74, 0
s_branch LRARE004_%=
LSITE26_%=:
s_mov_b32 s73, 26
s_mov_b32 s75, 49152
s_mov_b32 s74, 32768
s_branch LRARE004_%=
LSITE28_%=:
s_mov_b32 s73, 28
s_mov_b32 s75, 16384
s_mov_b32 s74, 0
s_branch LRARE004_%=
LSITE30_%=:
s_mov_b32 s73, 30
s_mov_b32 s75, 49152
s_mov_b32 s74, 32768
s_branch LRARE004_%=
LRARE004_%=:
s_nop 15
s_waitcnt vmcnt(0) lgkmcnt(0)
v_add_u32 v249, s74, v224
RD a[192:195],v249 offset:0
RD a[208:211],v249 offset:4096
RD a[224:227],v249 offset:8192
RD a[240:243],v249 offset:12288
v_add_u32 v250, s74, v225
RD a[196:199],v250 offset:0
RD a[212:215],v250 offset:4096
RD a[228:231],v250 offset:8192
RD a[244:247],v250 offset:12288
v_add_u32 v251, s74, v226
RD a[200:203],v251 offset:0
RD a[216:219],v251 offset:4096
RD a[232:235],v251 offset:8192
RD a[248:251],v251 offset:12288
v_add_u32 v254, s74, v227
RD a[204:207],v254 offset:0
RD a[220:223],v254 offset:4096
RD a[236:239],v254 offset:8192
RD a[252:255],v254 offset:12288
s_waitcnt lgkmcnt(0)
MM v[0:3],a[192:195],v[160:163],v[128:131]
MM v[0:3],a[196:199],v[176:179],v[0:3]
MM v[0:3],a[200:203],v[192:195],v[0:3]
MM v[0:3],a[204:207],v[208:211],v[0:3]
MM v[4:7],a[220:223],v[208:211],v[128:131]
MM v[4:7],a[216:219],v[192:195],v[4:7]
MM v[4:7],a[212:215],v[176:179],v[4:7]
MM v[4:7],a[208:211],v[160:163],v[4:7]
MM v[12:15],a[208:211],v[164:167],v[132:135]
MM v[12:15],a[212:215],v[180:183],v[12:15]
MM v[12:15],a[216:219],v[196:199],v[12:15]
MM v[12:15],a[220:223],v[212:215],v[12:15]
MM v[8:11],a[204:207],v[212:215],v[132:135]
MM v[8:11],a[200:203],v[196:199],v[8:11]
MM v[8:11],a[196:199],v[180:183],v[8:11]
MM v[8:11],a[192:195],v[164:167],v[8:11]
MM v[16:19],a[192:195],v[168:171],v[136:139]
MM v[16:19],a[196:199],v[184:187],v[16:19]
MM v[16:19],a[200:203],v[200:203],v[16:19]
MM v[16:19],a[204:207],v[216:219],v[16:19]
MM v[20:23],a[220:223],v[216:219],v[136:139]
MM v[20:23],a[216:219],v[200:203],v[20:23]
MM v[20:23],a[212:215],v[184:187],v[20:23]
MM v[20:23],a[208:211],v[168:171],v[20:23]
MM v[28:31],a[208:211],v[172:175],v[140:143]
MM v[28:31],a[212:215],v[188:191],v[28:31]
MM v[28:31],a[216:219],v[204:207],v[28:31]
MM v[28:31],a[220:223],v[220:223],v[28:31]
MM v[24:27],a[204:207],v[220:223],v[140:143]
MM v[24:27],a[200:203],v[204:207],v[24:27]
MM v[24:27],a[196:199],v[188:191],v[24:27]
MM v[24:27],a[192:195],v[172:175],v[24:27]
MM v[32:35],a[224:227],v[160:163],v[128:131]
MM v[32:35],a[228:231],v[176:179],v[32:35]
MM v[32:35],a[232:235],v[192:195],v[32:35]
MM v[32:35],a[236:239],v[208:211],v[32:35]
MM v[36:39],a[252:255],v[208:211],v[128:131]
MM v[36:39],a[248:251],v[192:195],v[36:39]
MM v[36:39],a[244:247],v[176:179],v[36:39]
MM v[36:39],a[240:243],v[160:163],v[36:39]
MM v[44:47],a[240:243],v[164:167],v[132:135]
MM v[44:47],a[244:247],v[180:183],v[44:47]
MM v[44:47],a[248:251],v[196:199],v[44:47]
MM v[44:47],a[252:255],v[212:215],v[44:47]
MM v[40:43],a[236:239],v[212:215],v[132:135]
MM v[40:43],a[232:235],v[196:199],v[40:43]
MM v[40:43],a[228:231],v[180:183],v[40:43]
MM v[40:43],a[224:227],v[164:167],v[40:43]
MM v[48:51],a[224:227],v[168:171],v[136:139]
MM v[48:51],a[228:231],v[184:187],v[48:51]
MM v[48:51],a[232:235],v[200:203],v[48:51]
MM v[48:51],a[236:239],v[216:219],v[48:51]
MM v[52:55],a[252:255],v[216:219],v[136:139]
MM v[52:55],a[248:251],v[200:203],v[52:55]
MM v[52:55],a[244:247],v[184:187],v[52:55]
MM v[52:55],a[240:243],v[168:171],v[52:55]
MM v[60:63],a[240:243],v[172:175],v[140:143]
MM v[60:63],a[244:247],v[188:191],v[60:63]
MM v[60:63],a[248:251],v[204:207],v[60:63]
MM v[60:63],a[252:255],v[220:223],v[60:63]
MM v[56:59],a[236:239],v[220:223],v[140:143]
MM v[56:59],a[232:235],v[204:207],v[56:59]
MM v[56:59],a[228:231],v[188:191],v[56:59]
MM v[56:59],a[224:227],v[172:175],v[56:59]
s_nop 15
v_add_u32 v249, s75, v224
RD a[192:195],v249 offset:0
RD a[208:211],v249 offset:4096
RD a[224:227],v249 offset:8192
RD a[240:243],v249 offset:12288
v_add_u32 v250, s75, v225
RD a[196:199],v250 offset:0
RD a[212:215],v250 offset:4096
RD a[228:231],v250 offset:8192
RD a[244:247],v250 offset:12288
v_add_u32 v251, s75, v226
RD a[200:203],v251 offset:0
RD a[216:219],v251 offset:4096
RD a[232:235],v251 offset:8192
RD a[248:251],v251 offset:12288
v_add_u32 v254, s75, v227
RD a[204:207],v254 offset:0
RD a[220:223],v254 offset:4096
RD a[236:239],v254 offset:8192
RD a[252:255],v254 offset:12288
s_waitcnt lgkmcnt(0)
v_mov_b32 v236, v247
v_mov_b32 v237, 0
v_max3_f32 v249, v0, v1, v2
v_max3_f32 v251, v32, v33, v34
v_max3_f32 v249, v249, v3, v4
v_max3_f32 v251, v251, v35, v36
v_max3_f32 v249, v249, v5, v6
v_max3_f32 v251, v251, v37, v38
v_max3_f32 v249, v249, v7, v39
v_max_f32 v249, v249, v251
v_log_f32 v250, v247
s_lshl_b64 exec, 0xffff, 0
v_max_f32 v249, v249, v250
s_mov_b64 exec, -1
v_mov_b32 v251, v249
s_nop 1
v_permlane16_swap_b32 v249, v251
v_max_f32 v249, v249, v251
v_mov_b32 v251, v249
s_nop 1
v_permlane32_swap_b32 v249, v251
v_max_f32 v249, v249, v251
v_max_f32 v249, 0, v249
v_sub_f32 v250, 0, v249
EX v250,v250
v_sub_f32 v128, v128, v249
v_sub_f32 v129, v129, v249
v_sub_f32 v130, v130, v249
v_sub_f32 v131, v131, v249
v_sub_f32 v64, v64, v249
v_sub_f32 v0, v0, v249
v_sub_f32 v65, v65, v249
v_sub_f32 v1, v1, v249
v_sub_f32 v66, v66, v249
v_sub_f32 v2, v2, v249
v_sub_f32 v67, v67, v249
v_sub_f32 v3, v3, v249
v_sub_f32 v68, v68, v249
v_sub_f32 v4, v4, v249
v_sub_f32 v69, v69, v249
v_sub_f32 v5, v5, v249
v_sub_f32 v70, v70, v249
v_sub_f32 v6, v6, v249
v_sub_f32 v71, v71, v249
v_sub_f32 v7, v7, v249
v_sub_f32 v96, v96, v249
v_sub_f32 v32, v32, v249
v_sub_f32 v97, v97, v249
v_sub_f32 v33, v33, v249
v_sub_f32 v98, v98, v249
v_sub_f32 v34, v34, v249
v_sub_f32 v99, v99, v249
v_sub_f32 v35, v35, v249
v_sub_f32 v100, v100, v249
v_sub_f32 v36, v36, v249
v_sub_f32 v101, v101, v249
v_sub_f32 v37, v37, v249
v_sub_f32 v102, v102, v249
v_sub_f32 v38, v38, v249
v_sub_f32 v103, v103, v249
v_sub_f32 v39, v39, v249
s_lshl_b64 exec, 0xffff, 0
v_mul_f32 v236, v236, v250
s_mov_b64 exec, -1
AR v254,a0
v_mul_f32 v254, v254, v250
AW a0,v254
AR v251,a1
v_mul_f32 v251, v251, v250
AW a1,v251
AR v254,a2
v_mul_f32 v254, v254, v250
AW a2,v254
AR v251,a3
v_mul_f32 v251, v251, v250
AW a3,v251
AR v254,a4
v_mul_f32 v254, v254, v250
AW a4,v254
AR v251,a5
v_mul_f32 v251, v251, v250
AW a5,v251
AR v254,a6
v_mul_f32 v254, v254, v250
)ASM"
R"ASM(
AW a6,v254
AR v251,a7
v_mul_f32 v251, v251, v250
AW a7,v251
AR v254,a32
v_mul_f32 v254, v254, v250
AW a32,v254
AR v251,a33
v_mul_f32 v251, v251, v250
AW a33,v251
AR v254,a34
v_mul_f32 v254, v254, v250
AW a34,v254
AR v251,a35
v_mul_f32 v251, v251, v250
AW a35,v251
AR v254,a36
v_mul_f32 v254, v254, v250
AW a36,v254
AR v251,a37
v_mul_f32 v251, v251, v250
AW a37,v251
AR v254,a38
v_mul_f32 v254, v254, v250
AW a38,v254
AR v251,a39
v_mul_f32 v251, v251, v250
AW a39,v251
AR v254,a64
v_mul_f32 v254, v254, v250
AW a64,v254
AR v251,a65
v_mul_f32 v251, v251, v250
AW a65,v251
AR v254,a66
v_mul_f32 v254, v254, v250
AW a66,v254
AR v251,a67
v_mul_f32 v251, v251, v250
AW a67,v251
AR v254,a68
v_mul_f32 v254, v254, v250
AW a68,v254
AR v251,a69
v_mul_f32 v251, v251, v250
AW a69,v251
AR v254,a70
v_mul_f32 v254, v254, v250
AW a70,v254
AR v251,a71
v_mul_f32 v251, v251, v250
AW a71,v251
AR v254,a96
v_mul_f32 v254, v254, v250
AW a96,v254
AR v251,a97
v_mul_f32 v251, v251, v250
AW a97,v251
AR v254,a98
v_mul_f32 v254, v254, v250
AW a98,v254
AR v251,a99
v_mul_f32 v251, v251, v250
AW a99,v251
AR v254,a100
v_mul_f32 v254, v254, v250
AW a100,v254
AR v251,a101
v_mul_f32 v251, v251, v250
AW a101,v251
AR v254,a102
v_mul_f32 v254, v254, v250
AW a102,v254
AR v251,a103
v_mul_f32 v251, v251, v250
AW a103,v251
v_max3_f32 v249, v8, v9, v10
v_max3_f32 v251, v40, v41, v42
v_max3_f32 v249, v249, v11, v12
v_max3_f32 v251, v251, v43, v44
v_max3_f32 v249, v249, v13, v14
v_max3_f32 v251, v251, v45, v46
v_max3_f32 v249, v249, v15, v47
v_max_f32 v249, v249, v251
v_log_f32 v250, v247
s_lshl_b64 exec, 0xffff, 16
v_max_f32 v249, v249, v250
s_mov_b64 exec, -1
v_mov_b32 v251, v249
s_nop 1
v_permlane16_swap_b32 v249, v251
v_max_f32 v249, v249, v251
v_mov_b32 v251, v249
s_nop 1
v_permlane32_swap_b32 v249, v251
v_max_f32 v249, v249, v251
v_max_f32 v249, 0, v249
v_sub_f32 v250, 0, v249
EX v250,v250
v_sub_f32 v132, v132, v249
v_sub_f32 v133, v133, v249
v_sub_f32 v134, v134, v249
v_sub_f32 v135, v135, v249
v_sub_f32 v72, v72, v249
v_sub_f32 v8, v8, v249
v_sub_f32 v73, v73, v249
v_sub_f32 v9, v9, v249
v_sub_f32 v74, v74, v249
v_sub_f32 v10, v10, v249
v_sub_f32 v75, v75, v249
v_sub_f32 v11, v11, v249
v_sub_f32 v76, v76, v249
v_sub_f32 v12, v12, v249
v_sub_f32 v77, v77, v249
v_sub_f32 v13, v13, v249
v_sub_f32 v78, v78, v249
v_sub_f32 v14, v14, v249
v_sub_f32 v79, v79, v249
v_sub_f32 v15, v15, v249
v_sub_f32 v104, v104, v249
v_sub_f32 v40, v40, v249
v_sub_f32 v105, v105, v249
v_sub_f32 v41, v41, v249
v_sub_f32 v106, v106, v249
v_sub_f32 v42, v42, v249
v_sub_f32 v107, v107, v249
v_sub_f32 v43, v43, v249
v_sub_f32 v108, v108, v249
v_sub_f32 v44, v44, v249
v_sub_f32 v109, v109, v249
v_sub_f32 v45, v45, v249
v_sub_f32 v110, v110, v249
v_sub_f32 v46, v46, v249
v_sub_f32 v111, v111, v249
v_sub_f32 v47, v47, v249
s_lshl_b64 exec, 0xffff, 16
v_mul_f32 v236, v236, v250
s_mov_b64 exec, -1
AR v254,a8
v_mul_f32 v254, v254, v250
AW a8,v254
AR v251,a9
v_mul_f32 v251, v251, v250
AW a9,v251
AR v254,a10
v_mul_f32 v254, v254, v250
AW a10,v254
AR v251,a11
v_mul_f32 v251, v251, v250
AW a11,v251
AR v254,a12
v_mul_f32 v254, v254, v250
AW a12,v254
AR v251,a13
v_mul_f32 v251, v251, v250
AW a13,v251
AR v254,a14
v_mul_f32 v254, v254, v250
AW a14,v254
AR v251,a15
v_mul_f32 v251, v251, v250
AW a15,v251
AR v254,a40
v_mul_f32 v254, v254, v250
AW a40,v254
AR v251,a41
v_mul_f32 v251, v251, v250
AW a41,v251
AR v254,a42
v_mul_f32 v254, v254, v250
AW a42,v254
AR v251,a43
v_mul_f32 v251, v251, v250
AW a43,v251
AR v254,a44
v_mul_f32 v254, v254, v250
AW a44,v254
AR v251,a45
v_mul_f32 v251, v251, v250
AW a45,v251
AR v254,a46
v_mul_f32 v254, v254, v250
AW a46,v254
AR v251,a47
v_mul_f32 v251, v251, v250
AW a47,v251
AR v254,a72
v_mul_f32 v254, v254, v250
AW a72,v254
AR v251,a73
v_mul_f32 v251, v251, v250
AW a73,v251
AR v254,a74
v_mul_f32 v254, v254, v250
AW a74,v254
AR v251,a75
v_mul_f32 v251, v251, v250
AW a75,v251
AR v254,a76
v_mul_f32 v254, v254, v250
AW a76,v254
AR v251,a77
v_mul_f32 v251, v251, v250
AW a77,v251
AR v254,a78
v_mul_f32 v254, v254, v250
AW a78,v254
AR v251,a79
v_mul_f32 v251, v251, v250
AW a79,v251
AR v254,a104
v_mul_f32 v254, v254, v250
AW a104,v254
AR v251,a105
v_mul_f32 v251, v251, v250
AW a105,v251
AR v254,a106
v_mul_f32 v254, v254, v250
AW a106,v254
AR v251,a107
v_mul_f32 v251, v251, v250
AW a107,v251
AR v254,a108
v_mul_f32 v254, v254, v250
AW a108,v254
AR v251,a109
v_mul_f32 v251, v251, v250
AW a109,v251
AR v254,a110
v_mul_f32 v254, v254, v250
AW a110,v254
AR v251,a111
v_mul_f32 v251, v251, v250
AW a111,v251
v_max3_f32 v249, v16, v17, v18
v_max3_f32 v251, v48, v49, v50
v_max3_f32 v249, v249, v19, v20
v_max3_f32 v251, v251, v51, v52
v_max3_f32 v249, v249, v21, v22
v_max3_f32 v251, v251, v53, v54
v_max3_f32 v249, v249, v23, v55
v_max_f32 v249, v249, v251
v_log_f32 v250, v247
s_lshl_b64 exec, 0xffff, 32
v_max_f32 v249, v249, v250
s_mov_b64 exec, -1
v_mov_b32 v251, v249
s_nop 1
v_permlane16_swap_b32 v249, v251
v_max_f32 v249, v249, v251
v_mov_b32 v251, v249
s_nop 1
v_permlane32_swap_b32 v249, v251
v_max_f32 v249, v249, v251
v_max_f32 v249, 0, v249
v_sub_f32 v250, 0, v249
EX v250,v250
v_sub_f32 v136, v136, v249
v_sub_f32 v137, v137, v249
v_sub_f32 v138, v138, v249
v_sub_f32 v139, v139, v249
v_sub_f32 v80, v80, v249
v_sub_f32 v16, v16, v249
v_sub_f32 v81, v81, v249
v_sub_f32 v17, v17, v249
v_sub_f32 v82, v82, v249
v_sub_f32 v18, v18, v249
v_sub_f32 v83, v83, v249
v_sub_f32 v19, v19, v249
v_sub_f32 v84, v84, v249
v_sub_f32 v20, v20, v249
v_sub_f32 v85, v85, v249
v_sub_f32 v21, v21, v249
v_sub_f32 v86, v86, v249
v_sub_f32 v22, v22, v249
v_sub_f32 v87, v87, v249
v_sub_f32 v23, v23, v249
v_sub_f32 v112, v112, v249
v_sub_f32 v48, v48, v249
v_sub_f32 v113, v113, v249
v_sub_f32 v49, v49, v249
v_sub_f32 v114, v114, v249
v_sub_f32 v50, v50, v249
v_sub_f32 v115, v115, v249
v_sub_f32 v51, v51, v249
v_sub_f32 v116, v116, v249
v_sub_f32 v52, v52, v249
v_sub_f32 v117, v117, v249
v_sub_f32 v53, v53, v249
v_sub_f32 v118, v118, v249
v_sub_f32 v54, v54, v249
v_sub_f32 v119, v119, v249
v_sub_f32 v55, v55, v249
s_lshl_b64 exec, 0xffff, 32
v_mul_f32 v236, v236, v250
s_mov_b64 exec, -1
AR v254,a16
v_mul_f32 v254, v254, v250
AW a16,v254
AR v251,a17
v_mul_f32 v251, v251, v250
AW a17,v251
AR v254,a18
v_mul_f32 v254, v254, v250
AW a18,v254
AR v251,a19
v_mul_f32 v251, v251, v250
AW a19,v251
AR v254,a20
v_mul_f32 v254, v254, v250
AW a20,v254
AR v251,a21
v_mul_f32 v251, v251, v250
AW a21,v251
AR v254,a22
v_mul_f32 v254, v254, v250
AW a22,v254
AR v251,a23
v_mul_f32 v251, v251, v250
AW a23,v251
AR v254,a48
v_mul_f32 v254, v254, v250
AW a48,v254
AR v251,a49
v_mul_f32 v251, v251, v250
AW a49,v251
AR v254,a50
v_mul_f32 v254, v254, v250
AW a50,v254
AR v251,a51
v_mul_f32 v251, v251, v250
AW a51,v251
AR v254,a52
v_mul_f32 v254, v254, v250
AW a52,v254
AR v251,a53
v_mul_f32 v251, v251, v250
AW a53,v251
AR v254,a54
v_mul_f32 v254, v254, v250
AW a54,v254
AR v251,a55
v_mul_f32 v251, v251, v250
AW a55,v251
AR v254,a80
v_mul_f32 v254, v254, v250
AW a80,v254
AR v251,a81
v_mul_f32 v251, v251, v250
AW a81,v251
AR v254,a82
v_mul_f32 v254, v254, v250
AW a82,v254
AR v251,a83
v_mul_f32 v251, v251, v250
AW a83,v251
AR v254,a84
v_mul_f32 v254, v254, v250
AW a84,v254
AR v251,a85
v_mul_f32 v251, v251, v250
AW a85,v251
AR v254,a86
v_mul_f32 v254, v254, v250
AW a86,v254
AR v251,a87
v_mul_f32 v251, v251, v250
AW a87,v251
AR v254,a112
v_mul_f32 v254, v254, v250
AW a112,v254
AR v251,a113
v_mul_f32 v251, v251, v250
AW a113,v251
AR v254,a114
v_mul_f32 v254, v254, v250
AW a114,v254
AR v251,a115
v_mul_f32 v251, v251, v250
AW a115,v251
AR v254,a116
v_mul_f32 v254, v254, v250
AW a116,v254
AR v251,a117
v_mul_f32 v251, v251, v250
AW a117,v251
AR v254,a118
v_mul_f32 v254, v254, v250
AW a118,v254
AR v251,a119
v_mul_f32 v251, v251, v250
AW a119,v251
v_max3_f32 v249, v24, v25, v26
v_max3_f32 v251, v56, v57, v58
v_max3_f32 v249, v249, v27, v28
v_max3_f32 v251, v251, v59, v60
v_max3_f32 v249, v249, v29, v30
v_max3_f32 v251, v251, v61, v62
v_max3_f32 v249, v249, v31, v63
v_max_f32 v249, v249, v251
v_log_f32 v250, v247
s_lshl_b64 exec, 0xffff, 48
v_max_f32 v249, v249, v250
s_mov_b64 exec, -1
v_mov_b32 v251, v249
s_nop 1
v_permlane16_swap_b32 v249, v251
v_max_f32 v249, v249, v251
v_mov_b32 v251, v249
s_nop 1
v_permlane32_swap_b32 v249, v251
v_max_f32 v249, v249, v251
v_max_f32 v249, 0, v249
v_sub_f32 v250, 0, v249
EX v250,v250
v_sub_f32 v140, v140, v249
v_sub_f32 v141, v141, v249
v_sub_f32 v142, v142, v249
v_sub_f32 v143, v143, v249
v_sub_f32 v88, v88, v249
v_sub_f32 v24, v24, v249
v_sub_f32 v89, v89, v249
v_sub_f32 v25, v25, v249
v_sub_f32 v90, v90, v249
v_sub_f32 v26, v26, v249
v_sub_f32 v91, v91, v249
v_sub_f32 v27, v27, v249
v_sub_f32 v92, v92, v249
v_sub_f32 v28, v28, v249
v_sub_f32 v93, v93, v249
v_sub_f32 v29, v29, v249
v_sub_f32 v94, v94, v249
v_sub_f32 v30, v30, v249
v_sub_f32 v95, v95, v249
v_sub_f32 v31, v31, v249
v_sub_f32 v120, v120, v249
v_sub_f32 v56, v56, v249
v_sub_f32 v121, v121, v249
v_sub_f32 v57, v57, v249
v_sub_f32 v122, v122, v249
v_sub_f32 v58, v58, v249
v_sub_f32 v123, v123, v249
v_sub_f32 v59, v59, v249
v_sub_f32 v124, v124, v249
v_sub_f32 v60, v60, v249
v_sub_f32 v125, v125, v249
v_sub_f32 v61, v61, v249
v_sub_f32 v126, v126, v249
v_sub_f32 v62, v62, v249
v_sub_f32 v127, v127, v249
v_sub_f32 v63, v63, v249
s_lshl_b64 exec, 0xffff, 48
v_mul_f32 v236, v236, v250
s_mov_b64 exec, -1
AR v254,a24
v_mul_f32 v254, v254, v250
AW a24,v254
AR v251,a25
v_mul_f32 v251, v251, v250
AW a25,v251
AR v254,a26
v_mul_f32 v254, v254, v250
AW a26,v254
AR v251,a27
v_mul_f32 v251, v251, v250
AW a27,v251
AR v254,a28
v_mul_f32 v254, v254, v250
AW a28,v254
AR v251,a29
v_mul_f32 v251, v251, v250
AW a29,v251
AR v254,a30
v_mul_f32 v254, v254, v250
AW a30,v254
AR v251,a31
v_mul_f32 v251, v251, v250
AW a31,v251
AR v254,a56
v_mul_f32 v254, v254, v250
AW a56,v254
AR v251,a57
v_mul_f32 v251, v251, v250
AW a57,v251
AR v254,a58
v_mul_f32 v254, v254, v250
AW a58,v254
AR v251,a59
v_mul_f32 v251, v251, v250
AW a59,v251
AR v254,a60
v_mul_f32 v254, v254, v250
AW a60,v254
AR v251,a61
v_mul_f32 v251, v251, v250
AW a61,v251
AR v254,a62
v_mul_f32 v254, v254, v250
AW a62,v254
AR v251,a63
v_mul_f32 v251, v251, v250
AW a63,v251
AR v254,a88
v_mul_f32 v254, v254, v250
AW a88,v254
AR v251,a89
v_mul_f32 v251, v251, v250
AW a89,v251
AR v254,a90
v_mul_f32 v254, v254, v250
AW a90,v254
AR v251,a91
v_mul_f32 v251, v251, v250
AW a91,v251
AR v254,a92
v_mul_f32 v254, v254, v250
AW a92,v254
AR v251,a93
v_mul_f32 v251, v251, v250
AW a93,v251
AR v254,a94
v_mul_f32 v254, v254, v250
AW a94,v254
AR v251,a95
v_mul_f32 v251, v251, v250
AW a95,v251
AR v254,a120
v_mul_f32 v254, v254, v250
AW a120,v254
AR v251,a121
v_mul_f32 v251, v251, v250
AW a121,v251
AR v254,a122
v_mul_f32 v254, v254, v250
AW a122,v254
AR v251,a123
v_mul_f32 v251, v251, v250
AW a123,v251
AR v254,a124
v_mul_f32 v254, v254, v250
AW a124,v254
AR v251,a125
v_mul_f32 v251, v251, v250
AW a125,v251
AR v254,a126
v_mul_f32 v254, v254, v250
AW a126,v254
AR v251,a127
v_mul_f32 v251, v251, v250
AW a127,v251
EX v0,v0
EX v1,v1
EX v2,v2
EX v3,v3
EX v4,v4
EX v5,v5
EX v6,v6
EX v7,v7
EX v8,v8
EX v9,v9
EX v10,v10
EX v11,v11
EX v12,v12
EX v13,v13
EX v14,v14
EX v15,v15
EX v16,v16
EX v17,v17
EX v18,v18
EX v19,v19
EX v20,v20
EX v21,v21
EX v22,v22
EX v23,v23
EX v24,v24
EX v25,v25
EX v26,v26
EX v27,v27
EX v28,v28
EX v29,v29
EX v30,v30
EX v31,v31
EX v32,v32
EX v33,v33
EX v34,v34
EX v35,v35
EX v36,v36
EX v37,v37
EX v38,v38
EX v39,v39
EX v40,v40
EX v41,v41
EX v42,v42
EX v43,v43
EX v44,v44
EX v45,v45
EX v46,v46
EX v47,v47
EX v48,v48
EX v49,v49
EX v50,v50
EX v51,v51
EX v52,v52
EX v53,v53
EX v54,v54
EX v55,v55
EX v56,v56
EX v57,v57
EX v58,v58
EX v59,v59
EX v60,v60
EX v61,v61
EX v62,v62
EX v63,v63
CV v0,v0,v1
CV v1,v2,v3
CV v2,v4,v5
CV v3,v6,v7
CV v8,v8,v9
CV v9,v10,v11
CV v10,v12,v13
CV v11,v14,v15
CV v16,v16,v17
CV v17,v18,v19
CV v18,v20,v21
CV v19,v22,v23
CV v24,v24,v25
CV v25,v26,v27
CV v26,v28,v29
CV v27,v30,v31
CV v4,v32,v33
CV v5,v34,v35
CV v6,v36,v37
CV v7,v38,v39
CV v12,v40,v41
CV v13,v42,v43
CV v14,v44,v45
CV v15,v46,v47
CV v20,v48,v49
)ASM"
R"ASM(
CV v21,v50,v51
CV v22,v52,v53
CV v23,v54,v55
CV v28,v56,v57
CV v29,v58,v59
CV v30,v60,v61
CV v31,v62,v63
v_smfmac_f32_16x16x64_bf16 v[236:239], v[144:147], v[0:7], v234
v_smfmac_f32_16x16x64_bf16 v[236:239], v[148:151], v[8:15], v234
v_smfmac_f32_16x16x64_bf16 v[236:239], v[152:155], v[16:23], v234
v_smfmac_f32_16x16x64_bf16 v[236:239], v[156:159], v[24:31], v234
s_nop 9
v_add_f32 v246, v236, v237
s_cmp_eq_u32 s73, 4
s_cbranch_scc1 LBACK4_%=
s_cmp_eq_u32 s73, 6
s_cbranch_scc1 LBACK6_%=
s_cmp_eq_u32 s73, 8
s_cbranch_scc1 LBACK8_%=
s_cmp_eq_u32 s73, 10
s_cbranch_scc1 LBACK10_%=
s_cmp_eq_u32 s73, 12
s_cbranch_scc1 LBACK12_%=
s_cmp_eq_u32 s73, 14
s_cbranch_scc1 LBACK14_%=
s_cmp_eq_u32 s73, 16
s_cbranch_scc1 LBACK16_%=
s_cmp_eq_u32 s73, 18
s_cbranch_scc1 LBACK18_%=
s_cmp_eq_u32 s73, 20
s_cbranch_scc1 LBACK20_%=
s_cmp_eq_u32 s73, 22
s_cbranch_scc1 LBACK22_%=
s_cmp_eq_u32 s73, 24
s_cbranch_scc1 LBACK24_%=
s_cmp_eq_u32 s73, 26
s_cbranch_scc1 LBACK26_%=
s_cmp_eq_u32 s73, 28
s_cbranch_scc1 LBACK28_%=
s_cmp_eq_u32 s73, 30
s_cbranch_scc1 LBACK30_%=
s_endpgm
LSITE32_%=:
s_mov_b32 s73, 32
s_mov_b32 s75, 16384
s_mov_b32 s74, 0
s_branch LRARE022_%=
LSITE34_%=:
s_mov_b32 s73, 34
s_mov_b32 s75, 49152
s_mov_b32 s74, 32768
s_branch LRARE022_%=
LRARE022_%=:
s_nop 15
s_waitcnt vmcnt(0) lgkmcnt(0)
v_add_u32 v249, s74, v224
RD a[192:195],v249 offset:0
RD a[208:211],v249 offset:4096
RD a[224:227],v249 offset:8192
RD a[240:243],v249 offset:12288
v_add_u32 v250, s74, v225
RD a[196:199],v250 offset:0
RD a[212:215],v250 offset:4096
RD a[228:231],v250 offset:8192
RD a[244:247],v250 offset:12288
v_add_u32 v251, s74, v226
RD a[200:203],v251 offset:0
RD a[216:219],v251 offset:4096
RD a[232:235],v251 offset:8192
RD a[248:251],v251 offset:12288
v_add_u32 v254, s74, v227
RD a[204:207],v254 offset:0
RD a[220:223],v254 offset:4096
RD a[236:239],v254 offset:8192
RD a[252:255],v254 offset:12288
s_waitcnt lgkmcnt(0)
MM v[16:19],a[192:195],v[168:171],v[136:139]
MM v[16:19],a[196:199],v[184:187],v[16:19]
MM v[16:19],a[200:203],v[200:203],v[16:19]
MM v[16:19],a[204:207],v[216:219],v[16:19]
MM v[20:23],a[220:223],v[216:219],v[136:139]
MM v[20:23],a[216:219],v[200:203],v[20:23]
MM v[20:23],a[212:215],v[184:187],v[20:23]
MM v[20:23],a[208:211],v[168:171],v[20:23]
MM v[28:31],a[208:211],v[172:175],v[140:143]
MM v[28:31],a[212:215],v[188:191],v[28:31]
MM v[28:31],a[216:219],v[204:207],v[28:31]
MM v[28:31],a[220:223],v[220:223],v[28:31]
MM v[24:27],a[204:207],v[220:223],v[140:143]
MM v[24:27],a[200:203],v[204:207],v[24:27]
MM v[24:27],a[196:199],v[188:191],v[24:27]
MM v[24:27],a[192:195],v[172:175],v[24:27]
MM v[48:51],a[224:227],v[168:171],v[136:139]
MM v[48:51],a[228:231],v[184:187],v[48:51]
MM v[48:51],a[232:235],v[200:203],v[48:51]
MM v[48:51],a[236:239],v[216:219],v[48:51]
MM v[52:55],a[252:255],v[216:219],v[136:139]
MM v[52:55],a[248:251],v[200:203],v[52:55]
MM v[52:55],a[244:247],v[184:187],v[52:55]
MM v[52:55],a[240:243],v[168:171],v[52:55]
MM v[60:63],a[240:243],v[172:175],v[140:143]
MM v[60:63],a[244:247],v[188:191],v[60:63]
MM v[60:63],a[248:251],v[204:207],v[60:63]
MM v[60:63],a[252:255],v[220:223],v[60:63]
MM v[56:59],a[236:239],v[220:223],v[140:143]
MM v[56:59],a[232:235],v[204:207],v[56:59]
MM v[56:59],a[228:231],v[188:191],v[56:59]
MM v[56:59],a[224:227],v[172:175],v[56:59]
s_nop 15
v_add_u32 v249, s75, v224
RD a[192:195],v249 offset:0
RD a[208:211],v249 offset:4096
RD a[224:227],v249 offset:8192
RD a[240:243],v249 offset:12288
v_add_u32 v250, s75, v225
RD a[196:199],v250 offset:0
RD a[212:215],v250 offset:4096
RD a[228:231],v250 offset:8192
RD a[244:247],v250 offset:12288
v_add_u32 v251, s75, v226
RD a[200:203],v251 offset:0
RD a[216:219],v251 offset:4096
RD a[232:235],v251 offset:8192
RD a[248:251],v251 offset:12288
v_add_u32 v254, s75, v227
RD a[204:207],v254 offset:0
RD a[220:223],v254 offset:4096
RD a[236:239],v254 offset:8192
RD a[252:255],v254 offset:12288
s_waitcnt lgkmcnt(0)
v_mov_b32 v236, v247
v_mov_b32 v237, 0
v_max3_f32 v249, v16, v17, v18
v_max3_f32 v251, v48, v49, v50
v_max3_f32 v249, v249, v19, v20
v_max3_f32 v251, v251, v51, v52
v_max3_f32 v249, v249, v21, v22
v_max3_f32 v251, v251, v53, v54
v_max3_f32 v249, v249, v23, v55
v_max_f32 v249, v249, v251
v_log_f32 v250, v247
s_lshl_b64 exec, 0xffff, 32
v_max_f32 v249, v249, v250
s_mov_b64 exec, -1
v_mov_b32 v251, v249
s_nop 1
v_permlane16_swap_b32 v249, v251
v_max_f32 v249, v249, v251
v_mov_b32 v251, v249
s_nop 1
v_permlane32_swap_b32 v249, v251
v_max_f32 v249, v249, v251
v_max_f32 v249, 0, v249
v_sub_f32 v250, 0, v249
EX v250,v250
v_sub_f32 v136, v136, v249
v_sub_f32 v137, v137, v249
v_sub_f32 v138, v138, v249
v_sub_f32 v139, v139, v249
v_sub_f32 v80, v80, v249
v_sub_f32 v16, v16, v249
v_sub_f32 v81, v81, v249
v_sub_f32 v17, v17, v249
v_sub_f32 v82, v82, v249
v_sub_f32 v18, v18, v249
v_sub_f32 v83, v83, v249
v_sub_f32 v19, v19, v249
v_sub_f32 v84, v84, v249
v_sub_f32 v20, v20, v249
v_sub_f32 v85, v85, v249
v_sub_f32 v21, v21, v249
v_sub_f32 v86, v86, v249
v_sub_f32 v22, v22, v249
v_sub_f32 v87, v87, v249
v_sub_f32 v23, v23, v249
v_sub_f32 v112, v112, v249
v_sub_f32 v48, v48, v249
v_sub_f32 v113, v113, v249
v_sub_f32 v49, v49, v249
v_sub_f32 v114, v114, v249
v_sub_f32 v50, v50, v249
v_sub_f32 v115, v115, v249
v_sub_f32 v51, v51, v249
v_sub_f32 v116, v116, v249
v_sub_f32 v52, v52, v249
v_sub_f32 v117, v117, v249
v_sub_f32 v53, v53, v249
v_sub_f32 v118, v118, v249
v_sub_f32 v54, v54, v249
v_sub_f32 v119, v119, v249
v_sub_f32 v55, v55, v249
s_lshl_b64 exec, 0xffff, 32
v_mul_f32 v236, v236, v250
s_mov_b64 exec, -1
AR v254,a16
v_mul_f32 v254, v254, v250
AW a16,v254
AR v251,a17
v_mul_f32 v251, v251, v250
AW a17,v251
AR v254,a18
v_mul_f32 v254, v254, v250
AW a18,v254
AR v251,a19
v_mul_f32 v251, v251, v250
AW a19,v251
AR v254,a20
v_mul_f32 v254, v254, v250
AW a20,v254
AR v251,a21
v_mul_f32 v251, v251, v250
AW a21,v251
AR v254,a22
v_mul_f32 v254, v254, v250
AW a22,v254
AR v251,a23
v_mul_f32 v251, v251, v250
AW a23,v251
AR v254,a48
v_mul_f32 v254, v254, v250
AW a48,v254
AR v251,a49
v_mul_f32 v251, v251, v250
AW a49,v251
AR v254,a50
v_mul_f32 v254, v254, v250
AW a50,v254
AR v251,a51
v_mul_f32 v251, v251, v250
AW a51,v251
AR v254,a52
v_mul_f32 v254, v254, v250
AW a52,v254
AR v251,a53
v_mul_f32 v251, v251, v250
AW a53,v251
AR v254,a54
v_mul_f32 v254, v254, v250
AW a54,v254
AR v251,a55
v_mul_f32 v251, v251, v250
AW a55,v251
AR v254,a80
v_mul_f32 v254, v254, v250
AW a80,v254
AR v251,a81
v_mul_f32 v251, v251, v250
AW a81,v251
AR v254,a82
v_mul_f32 v254, v254, v250
AW a82,v254
AR v251,a83
v_mul_f32 v251, v251, v250
AW a83,v251
AR v254,a84
v_mul_f32 v254, v254, v250
AW a84,v254
AR v251,a85
v_mul_f32 v251, v251, v250
AW a85,v251
AR v254,a86
v_mul_f32 v254, v254, v250
AW a86,v254
AR v251,a87
v_mul_f32 v251, v251, v250
AW a87,v251
AR v254,a112
v_mul_f32 v254, v254, v250
AW a112,v254
AR v251,a113
v_mul_f32 v251, v251, v250
AW a113,v251
AR v254,a114
v_mul_f32 v254, v254, v250
AW a114,v254
AR v251,a115
v_mul_f32 v251, v251, v250
AW a115,v251
AR v254,a116
v_mul_f32 v254, v254, v250
AW a116,v254
AR v251,a117
v_mul_f32 v251, v251, v250
AW a117,v251
AR v254,a118
v_mul_f32 v254, v254, v250
AW a118,v254
AR v251,a119
v_mul_f32 v251, v251, v250
AW a119,v251
v_max3_f32 v249, v24, v25, v26
v_max3_f32 v251, v56, v57, v58
v_max3_f32 v249, v249, v27, v28
v_max3_f32 v251, v251, v59, v60
v_max3_f32 v249, v249, v29, v30
v_max3_f32 v251, v251, v61, v62
v_max3_f32 v249, v249, v31, v63
v_max_f32 v249, v249, v251
v_log_f32 v250, v247
s_lshl_b64 exec, 0xffff, 48
v_max_f32 v249, v249, v250
s_mov_b64 exec, -1
v_mov_b32 v251, v249
s_nop 1
v_permlane16_swap_b32 v249, v251
v_max_f32 v249, v249, v251
v_mov_b32 v251, v249
s_nop 1
v_permlane32_swap_b32 v249, v251
v_max_f32 v249, v249, v251
v_max_f32 v249, 0, v249
v_sub_f32 v250, 0, v249
EX v250,v250
v_sub_f32 v140, v140, v249
v_sub_f32 v141, v141, v249
v_sub_f32 v142, v142, v249
v_sub_f32 v143, v143, v249
v_sub_f32 v88, v88, v249
v_sub_f32 v24, v24, v249
v_sub_f32 v89, v89, v249
v_sub_f32 v25, v25, v249
v_sub_f32 v90, v90, v249
v_sub_f32 v26, v26, v249
v_sub_f32 v91, v91, v249
v_sub_f32 v27, v27, v249
v_sub_f32 v92, v92, v249
v_sub_f32 v28, v28, v249
v_sub_f32 v93, v93, v249
v_sub_f32 v29, v29, v249
v_sub_f32 v94, v94, v249
v_sub_f32 v30, v30, v249
v_sub_f32 v95, v95, v249
v_sub_f32 v31, v31, v249
v_sub_f32 v120, v120, v249
v_sub_f32 v56, v56, v249
v_sub_f32 v121, v121, v249
v_sub_f32 v57, v57, v249
v_sub_f32 v122, v122, v249
v_sub_f32 v58, v58, v249
v_sub_f32 v123, v123, v249
v_sub_f32 v59, v59, v249
v_sub_f32 v124, v124, v249
v_sub_f32 v60, v60, v249
v_sub_f32 v125, v125, v249
v_sub_f32 v61, v61, v249
v_sub_f32 v126, v126, v249
v_sub_f32 v62, v62, v249
v_sub_f32 v127, v127, v249
v_sub_f32 v63, v63, v249
s_lshl_b64 exec, 0xffff, 48
v_mul_f32 v236, v236, v250
s_mov_b64 exec, -1
AR v254,a24
v_mul_f32 v254, v254, v250
AW a24,v254
AR v251,a25
v_mul_f32 v251, v251, v250
AW a25,v251
AR v254,a26
v_mul_f32 v254, v254, v250
AW a26,v254
AR v251,a27
v_mul_f32 v251, v251, v250
AW a27,v251
AR v254,a28
v_mul_f32 v254, v254, v250
AW a28,v254
AR v251,a29
v_mul_f32 v251, v251, v250
AW a29,v251
AR v254,a30
v_mul_f32 v254, v254, v250
AW a30,v254
AR v251,a31
v_mul_f32 v251, v251, v250
AW a31,v251
AR v254,a56
v_mul_f32 v254, v254, v250
AW a56,v254
AR v251,a57
v_mul_f32 v251, v251, v250
AW a57,v251
AR v254,a58
v_mul_f32 v254, v254, v250
AW a58,v254
AR v251,a59
v_mul_f32 v251, v251, v250
AW a59,v251
AR v254,a60
v_mul_f32 v254, v254, v250
AW a60,v254
AR v251,a61
v_mul_f32 v251, v251, v250
AW a61,v251
AR v254,a62
v_mul_f32 v254, v254, v250
AW a62,v254
AR v251,a63
v_mul_f32 v251, v251, v250
AW a63,v251
AR v254,a88
v_mul_f32 v254, v254, v250
AW a88,v254
AR v251,a89
v_mul_f32 v251, v251, v250
AW a89,v251
AR v254,a90
v_mul_f32 v254, v254, v250
AW a90,v254
AR v251,a91
v_mul_f32 v251, v251, v250
AW a91,v251
AR v254,a92
v_mul_f32 v254, v254, v250
AW a92,v254
AR v251,a93
v_mul_f32 v251, v251, v250
AW a93,v251
AR v254,a94
v_mul_f32 v254, v254, v250
AW a94,v254
AR v251,a95
v_mul_f32 v251, v251, v250
AW a95,v251
AR v254,a120
v_mul_f32 v254, v254, v250
AW a120,v254
AR v251,a121
v_mul_f32 v251, v251, v250
AW a121,v251
AR v254,a122
v_mul_f32 v254, v254, v250
AW a122,v254
AR v251,a123
v_mul_f32 v251, v251, v250
AW a123,v251
AR v254,a124
v_mul_f32 v254, v254, v250
AW a124,v254
AR v251,a125
v_mul_f32 v251, v251, v250
AW a125,v251
AR v254,a126
v_mul_f32 v254, v254, v250
AW a126,v254
AR v251,a127
v_mul_f32 v251, v251, v250
AW a127,v251
EX v16,v16
EX v17,v17
EX v18,v18
EX v19,v19
EX v20,v20
EX v21,v21
EX v22,v22
EX v23,v23
EX v24,v24
EX v25,v25
EX v26,v26
EX v27,v27
EX v28,v28
EX v29,v29
EX v30,v30
EX v31,v31
EX v48,v48
EX v49,v49
EX v50,v50
EX v51,v51
EX v52,v52
EX v53,v53
EX v54,v54
EX v55,v55
EX v56,v56
EX v57,v57
EX v58,v58
EX v59,v59
EX v60,v60
EX v61,v61
EX v62,v62
EX v63,v63
CV v16,v16,v17
CV v17,v18,v19
CV v18,v20,v21
CV v19,v22,v23
CV v24,v24,v25
CV v25,v26,v27
CV v26,v28,v29
CV v27,v30,v31
CV v20,v48,v49
CV v21,v50,v51
CV v22,v52,v53
CV v23,v54,v55
CV v28,v56,v57
CV v29,v58,v59
CV v30,v60,v61
CV v31,v62,v63
v_smfmac_f32_16x16x64_bf16 v[236:239], v[152:155], v[16:23], v234
s_nop 0
v_smfmac_f32_16x16x64_bf16 v[236:239], v[156:159], v[24:31], v234
s_nop 9
v_add_f32 v246, v236, v237
s_cmp_eq_u32 s73, 32
s_cbranch_scc1 LBACK32_%=
s_cmp_eq_u32 s73, 34
s_cbranch_scc1 LBACK34_%=
s_endpgm
LSITE33_%=:
s_mov_b32 s73, 33
s_mov_b32 s75, 32768
s_mov_b32 s74, 16384
s_branch LRARE122_%=
LRARE122_%=:
s_nop 15
s_waitcnt vmcnt(0) lgkmcnt(0)
v_add_u32 v249, s74, v224
RD a[192:195],v249 offset:0
RD a[208:211],v249 offset:4096
RD a[224:227],v249 offset:8192
RD a[240:243],v249 offset:12288
v_add_u32 v250, s74, v225
RD a[196:199],v250 offset:0
RD a[212:215],v250 offset:4096
RD a[228:231],v250 offset:8192
RD a[244:247],v250 offset:12288
v_add_u32 v251, s74, v226
RD a[200:203],v251 offset:0
RD a[216:219],v251 offset:4096
RD a[232:235],v251 offset:8192
RD a[248:251],v251 offset:12288
v_add_u32 v254, s74, v227
RD a[204:207],v254 offset:0
RD a[220:223],v254 offset:4096
RD a[236:239],v254 offset:8192
RD a[252:255],v254 offset:12288
)ASM"
R"ASM(
s_waitcnt lgkmcnt(0)
MM v[80:83],a[192:195],v[168:171],v[136:139]
MM v[80:83],a[196:199],v[184:187],v[80:83]
MM v[80:83],a[200:203],v[200:203],v[80:83]
MM v[80:83],a[204:207],v[216:219],v[80:83]
MM v[84:87],a[220:223],v[216:219],v[136:139]
MM v[84:87],a[216:219],v[200:203],v[84:87]
MM v[84:87],a[212:215],v[184:187],v[84:87]
MM v[84:87],a[208:211],v[168:171],v[84:87]
MM v[92:95],a[208:211],v[172:175],v[140:143]
MM v[92:95],a[212:215],v[188:191],v[92:95]
MM v[92:95],a[216:219],v[204:207],v[92:95]
MM v[92:95],a[220:223],v[220:223],v[92:95]
MM v[88:91],a[204:207],v[220:223],v[140:143]
MM v[88:91],a[200:203],v[204:207],v[88:91]
MM v[88:91],a[196:199],v[188:191],v[88:91]
MM v[88:91],a[192:195],v[172:175],v[88:91]
MM v[112:115],a[224:227],v[168:171],v[136:139]
MM v[112:115],a[228:231],v[184:187],v[112:115]
MM v[112:115],a[232:235],v[200:203],v[112:115]
MM v[112:115],a[236:239],v[216:219],v[112:115]
MM v[116:119],a[252:255],v[216:219],v[136:139]
MM v[116:119],a[248:251],v[200:203],v[116:119]
MM v[116:119],a[244:247],v[184:187],v[116:119]
MM v[116:119],a[240:243],v[168:171],v[116:119]
MM v[124:127],a[240:243],v[172:175],v[140:143]
MM v[124:127],a[244:247],v[188:191],v[124:127]
MM v[124:127],a[248:251],v[204:207],v[124:127]
MM v[124:127],a[252:255],v[220:223],v[124:127]
MM v[120:123],a[236:239],v[220:223],v[140:143]
MM v[120:123],a[232:235],v[204:207],v[120:123]
MM v[120:123],a[228:231],v[188:191],v[120:123]
MM v[120:123],a[224:227],v[172:175],v[120:123]
s_nop 15
v_add_u32 v249, s75, v224
RD a[192:195],v249 offset:0
RD a[208:211],v249 offset:4096
RD a[224:227],v249 offset:8192
RD a[240:243],v249 offset:12288
v_add_u32 v250, s75, v225
RD a[196:199],v250 offset:0
RD a[212:215],v250 offset:4096
RD a[228:231],v250 offset:8192
RD a[244:247],v250 offset:12288
v_add_u32 v251, s75, v226
RD a[200:203],v251 offset:0
RD a[216:219],v251 offset:4096
RD a[232:235],v251 offset:8192
RD a[248:251],v251 offset:12288
v_add_u32 v254, s75, v227
RD a[204:207],v254 offset:0
RD a[220:223],v254 offset:4096
RD a[236:239],v254 offset:8192
RD a[252:255],v254 offset:12288
s_waitcnt lgkmcnt(0)
v_mov_b32 v236, v246
v_mov_b32 v237, 0
v_max3_f32 v249, v80, v81, v82
v_max3_f32 v251, v112, v113, v114
v_max3_f32 v249, v249, v83, v84
v_max3_f32 v251, v251, v115, v116
v_max3_f32 v249, v249, v85, v86
v_max3_f32 v251, v251, v117, v118
v_max3_f32 v249, v249, v87, v119
v_max_f32 v249, v249, v251
v_log_f32 v250, v246
s_lshl_b64 exec, 0xffff, 32
v_max_f32 v249, v249, v250
s_mov_b64 exec, -1
v_mov_b32 v251, v249
s_nop 1
v_permlane16_swap_b32 v249, v251
v_max_f32 v249, v249, v251
v_mov_b32 v251, v249
s_nop 1
v_permlane32_swap_b32 v249, v251
v_max_f32 v249, v249, v251
v_max_f32 v249, 0, v249
v_sub_f32 v250, 0, v249
EX v250,v250
v_sub_f32 v136, v136, v249
v_sub_f32 v137, v137, v249
v_sub_f32 v138, v138, v249
v_sub_f32 v139, v139, v249
v_sub_f32 v16, v16, v249
v_sub_f32 v80, v80, v249
v_sub_f32 v17, v17, v249
v_sub_f32 v81, v81, v249
v_sub_f32 v18, v18, v249
v_sub_f32 v82, v82, v249
v_sub_f32 v19, v19, v249
v_sub_f32 v83, v83, v249
v_sub_f32 v20, v20, v249
v_sub_f32 v84, v84, v249
v_sub_f32 v21, v21, v249
v_sub_f32 v85, v85, v249
v_sub_f32 v22, v22, v249
v_sub_f32 v86, v86, v249
v_sub_f32 v23, v23, v249
v_sub_f32 v87, v87, v249
v_sub_f32 v48, v48, v249
v_sub_f32 v112, v112, v249
v_sub_f32 v49, v49, v249
v_sub_f32 v113, v113, v249
v_sub_f32 v50, v50, v249
v_sub_f32 v114, v114, v249
v_sub_f32 v51, v51, v249
v_sub_f32 v115, v115, v249
v_sub_f32 v52, v52, v249
v_sub_f32 v116, v116, v249
v_sub_f32 v53, v53, v249
v_sub_f32 v117, v117, v249
v_sub_f32 v54, v54, v249
v_sub_f32 v118, v118, v249
v_sub_f32 v55, v55, v249
v_sub_f32 v119, v119, v249
s_lshl_b64 exec, 0xffff, 32
v_mul_f32 v236, v236, v250
s_mov_b64 exec, -1
AR v254,a16
v_mul_f32 v254, v254, v250
AW a16,v254
AR v251,a17
v_mul_f32 v251, v251, v250
AW a17,v251
AR v254,a18
v_mul_f32 v254, v254, v250
AW a18,v254
AR v251,a19
v_mul_f32 v251, v251, v250
AW a19,v251
AR v254,a20
v_mul_f32 v254, v254, v250
AW a20,v254
AR v251,a21
v_mul_f32 v251, v251, v250
AW a21,v251
AR v254,a22
v_mul_f32 v254, v254, v250
AW a22,v254
AR v251,a23
v_mul_f32 v251, v251, v250
AW a23,v251
AR v254,a48
v_mul_f32 v254, v254, v250
AW a48,v254
AR v251,a49
v_mul_f32 v251, v251, v250
AW a49,v251
AR v254,a50
v_mul_f32 v254, v254, v250
AW a50,v254
AR v251,a51
v_mul_f32 v251, v251, v250
AW a51,v251
AR v254,a52
v_mul_f32 v254, v254, v250
AW a52,v254
AR v251,a53
v_mul_f32 v251, v251, v250
AW a53,v251
AR v254,a54
v_mul_f32 v254, v254, v250
AW a54,v254
AR v251,a55
v_mul_f32 v251, v251, v250
AW a55,v251
AR v254,a80
v_mul_f32 v254, v254, v250
AW a80,v254
AR v251,a81
v_mul_f32 v251, v251, v250
AW a81,v251
AR v254,a82
v_mul_f32 v254, v254, v250
AW a82,v254
AR v251,a83
v_mul_f32 v251, v251, v250
AW a83,v251
AR v254,a84
v_mul_f32 v254, v254, v250
AW a84,v254
AR v251,a85
v_mul_f32 v251, v251, v250
AW a85,v251
AR v254,a86
v_mul_f32 v254, v254, v250
AW a86,v254
AR v251,a87
v_mul_f32 v251, v251, v250
AW a87,v251
AR v254,a112
v_mul_f32 v254, v254, v250
AW a112,v254
AR v251,a113
v_mul_f32 v251, v251, v250
AW a113,v251
AR v254,a114
v_mul_f32 v254, v254, v250
AW a114,v254
AR v251,a115
v_mul_f32 v251, v251, v250
AW a115,v251
AR v254,a116
v_mul_f32 v254, v254, v250
AW a116,v254
AR v251,a117
v_mul_f32 v251, v251, v250
AW a117,v251
AR v254,a118
v_mul_f32 v254, v254, v250
AW a118,v254
AR v251,a119
v_mul_f32 v251, v251, v250
AW a119,v251
v_max3_f32 v249, v88, v89, v90
v_max3_f32 v251, v120, v121, v122
v_max3_f32 v249, v249, v91, v92
v_max3_f32 v251, v251, v123, v124
v_max3_f32 v249, v249, v93, v94
v_max3_f32 v251, v251, v125, v126
v_max3_f32 v249, v249, v95, v127
v_max_f32 v249, v249, v251
v_log_f32 v250, v246
s_lshl_b64 exec, 0xffff, 48
v_max_f32 v249, v249, v250
s_mov_b64 exec, -1
v_mov_b32 v251, v249
s_nop 1
v_permlane16_swap_b32 v249, v251
v_max_f32 v249, v249, v251
v_mov_b32 v251, v249
s_nop 1
v_permlane32_swap_b32 v249, v251
v_max_f32 v249, v249, v251
v_max_f32 v249, 0, v249
v_sub_f32 v250, 0, v249
EX v250,v250
v_sub_f32 v140, v140, v249
v_sub_f32 v141, v141, v249
v_sub_f32 v142, v142, v249
v_sub_f32 v143, v143, v249
v_sub_f32 v24, v24, v249
v_sub_f32 v88, v88, v249
v_sub_f32 v25, v25, v249
v_sub_f32 v89, v89, v249
v_sub_f32 v26, v26, v249
v_sub_f32 v90, v90, v249
v_sub_f32 v27, v27, v249
v_sub_f32 v91, v91, v249
v_sub_f32 v28, v28, v249
v_sub_f32 v92, v92, v249
v_sub_f32 v29, v29, v249
v_sub_f32 v93, v93, v249
v_sub_f32 v30, v30, v249
v_sub_f32 v94, v94, v249
v_sub_f32 v31, v31, v249
v_sub_f32 v95, v95, v249
v_sub_f32 v56, v56, v249
v_sub_f32 v120, v120, v249
v_sub_f32 v57, v57, v249
v_sub_f32 v121, v121, v249
v_sub_f32 v58, v58, v249
v_sub_f32 v122, v122, v249
v_sub_f32 v59, v59, v249
v_sub_f32 v123, v123, v249
v_sub_f32 v60, v60, v249
v_sub_f32 v124, v124, v249
v_sub_f32 v61, v61, v249
v_sub_f32 v125, v125, v249
v_sub_f32 v62, v62, v249
v_sub_f32 v126, v126, v249
v_sub_f32 v63, v63, v249
v_sub_f32 v127, v127, v249
s_lshl_b64 exec, 0xffff, 48
v_mul_f32 v236, v236, v250
s_mov_b64 exec, -1
AR v254,a24
v_mul_f32 v254, v254, v250
AW a24,v254
AR v251,a25
v_mul_f32 v251, v251, v250
AW a25,v251
AR v254,a26
v_mul_f32 v254, v254, v250
AW a26,v254
AR v251,a27
v_mul_f32 v251, v251, v250
AW a27,v251
AR v254,a28
v_mul_f32 v254, v254, v250
AW a28,v254
AR v251,a29
v_mul_f32 v251, v251, v250
AW a29,v251
AR v254,a30
v_mul_f32 v254, v254, v250
AW a30,v254
AR v251,a31
v_mul_f32 v251, v251, v250
AW a31,v251
AR v254,a56
v_mul_f32 v254, v254, v250
AW a56,v254
AR v251,a57
v_mul_f32 v251, v251, v250
AW a57,v251
AR v254,a58
v_mul_f32 v254, v254, v250
AW a58,v254
AR v251,a59
v_mul_f32 v251, v251, v250
AW a59,v251
AR v254,a60
v_mul_f32 v254, v254, v250
AW a60,v254
AR v251,a61
v_mul_f32 v251, v251, v250
AW a61,v251
AR v254,a62
v_mul_f32 v254, v254, v250
AW a62,v254
AR v251,a63
v_mul_f32 v251, v251, v250
AW a63,v251
AR v254,a88
v_mul_f32 v254, v254, v250
AW a88,v254
AR v251,a89
v_mul_f32 v251, v251, v250
AW a89,v251
AR v254,a90
v_mul_f32 v254, v254, v250
AW a90,v254
AR v251,a91
v_mul_f32 v251, v251, v250
AW a91,v251
AR v254,a92
v_mul_f32 v254, v254, v250
AW a92,v254
AR v251,a93
v_mul_f32 v251, v251, v250
AW a93,v251
AR v254,a94
v_mul_f32 v254, v254, v250
AW a94,v254
AR v251,a95
v_mul_f32 v251, v251, v250
AW a95,v251
AR v254,a120
v_mul_f32 v254, v254, v250
AW a120,v254
AR v251,a121
v_mul_f32 v251, v251, v250
AW a121,v251
AR v254,a122
v_mul_f32 v254, v254, v250
AW a122,v254
AR v251,a123
v_mul_f32 v251, v251, v250
AW a123,v251
AR v254,a124
v_mul_f32 v254, v254, v250
AW a124,v254
AR v251,a125
v_mul_f32 v251, v251, v250
AW a125,v251
AR v254,a126
v_mul_f32 v254, v254, v250
AW a126,v254
AR v251,a127
v_mul_f32 v251, v251, v250
AW a127,v251
EX v80,v80
EX v81,v81
EX v82,v82
EX v83,v83
EX v84,v84
EX v85,v85
EX v86,v86
EX v87,v87
EX v88,v88
EX v89,v89
EX v90,v90
EX v91,v91
EX v92,v92
EX v93,v93
EX v94,v94
EX v95,v95
EX v112,v112
EX v113,v113
EX v114,v114
EX v115,v115
EX v116,v116
EX v117,v117
EX v118,v118
EX v119,v119
EX v120,v120
EX v121,v121
EX v122,v122
EX v123,v123
EX v124,v124
EX v125,v125
EX v126,v126
EX v127,v127
CV v80,v80,v81
CV v81,v82,v83
CV v82,v84,v85
CV v83,v86,v87
CV v88,v88,v89
CV v89,v90,v91
CV v90,v92,v93
CV v91,v94,v95
CV v84,v112,v113
CV v85,v114,v115
CV v86,v116,v117
CV v87,v118,v119
CV v92,v120,v121
CV v93,v122,v123
CV v94,v124,v125
CV v95,v126,v127
v_smfmac_f32_16x16x64_bf16 v[236:239], v[152:155], v[80:87], v234
s_nop 0
v_smfmac_f32_16x16x64_bf16 v[236:239], v[156:159], v[88:95], v234
s_nop 9
v_add_f32 v247, v236, v237
s_cmp_eq_u32 s73, 33
s_cbranch_scc1 LBACK33_%=
s_endpgm
LEND_%=:
)ASM"

    :: [ka] "s"(__builtin_amdgcn_kernarg_segment_ptr()), [slds] "s"(slds), [swave] "s"(w), [sb] "s"(blockIdx.x)
    : "memory", "vcc", "scc", CLOB_V, CLOB_A, CLOB_S);
}

extern "C" void kernel_launch(void* const* d_in, const int* in_sizes, int n_in, void* d_out, int out_size, void* d_ws, size_t ws_size, hipStream_t stream) {
  hipFuncSetAttribute((const void*)ka_fused, hipFuncAttributeMaxDynamicSharedMemorySize, 163840);
  ka_fused<<<256, 256, 163840, stream>>>((const unsigned short*)d_in[0], (const unsigned short*)d_in[1], (const unsigned short*)d_in[2], (const unsigned short*)d_in[3],
                                         (const unsigned short*)d_in[4], (const unsigned short*)d_in[6], (const unsigned short*)d_in[7], (const unsigned short*)d_in[8],
                                         (const unsigned short*)d_in[9], (char*)d_ws, (unsigned short*)d_out
                                         );
}
```

```cpp
#include <hip/hip_runtime.h>
#include <stdint.h>

using f32x4  = __attribute__((ext_vector_type(4))) float;
using u32x4  = __attribute__((ext_vector_type(4))) unsigned;
using u32x2  = __attribute__((ext_vector_type(2))) unsigned;

constexpr int SEQ = 2048, DM = 640, NH = 32, DH = 128, HD = NH * DH, FF = 1728, TOPC = 8;

constexpr unsigned WS_FO  = 0;
constexpr unsigned WS_EP  = 32768;
constexpr unsigned WS_G1  = 36864;
constexpr unsigned WS_G2  = 53248;
constexpr unsigned WS_FH  = 69632;
constexpr unsigned WS_FY  = 73728;
constexpr unsigned WS_FG  = 77824;
constexpr unsigned WS_FR  = 81920;
constexpr unsigned WS_IX  = 86016;
constexpr unsigned WS_XN  = 131072;
constexpr unsigned WS_OF  = WS_XN + SEQ * DM * 2;
constexpr unsigned WS_H   = WS_OF + (18u << 20);
constexpr unsigned WS_Y   = WS_H + 32768;
constexpr unsigned WS_P   = WS_Y + 1048576;
constexpr unsigned WS_SPAN = 64u << 20;
constexpr int DN_FIRST = 76, YP = 18, SPIN = 30000;

#define CLOB_V "v0","v1","v2","v3","v4","v5","v6","v7","v8","v9","v10","v11","v12","v13","v14","v15","v16","v17","v18","v19","v20","v21","v22","v23","v24","v25","v26","v27","v28","v29","v30","v31","v32","v33","v34","v35","v36","v37","v38","v39","v40","v41","v42","v43","v44","v45","v46","v47","v48","v49","v50","v51","v52","v53","v54","v55","v56","v57","v58","v59","v60","v61","v62","v63","v64","v65","v66","v67","v68","v69","v70","v71","v72","v73","v74","v75","v76","v77","v78","v79","v80","v81","v82","v83","v84","v85","v86","v87","v88","v89","v90","v91","v92","v93","v94","v95","v96","v97","v98","v99","v100","v101","v102","v103","v104","v105","v106","v107","v108","v109","v110","v111","v112","v113","v114","v115","v116","v117","v118","v119","v120","v121","v122","v123","v124","v125","v126","v127","v128","v129","v130","v131","v132","v133","v134","v135","v136","v137","v138","v139","v140","v141","v142","v143","v144","v145","v146","v147","v148","v149","v150","v151","v152","v153","v154","v155","v156","v157","v158","v159","v160","v161","v162","v163","v164","v165","v166","v167","v168","v169","v170","v171","v172","v173","v174","v175","v176","v177","v178","v179","v180","v181","v182","v183","v184","v185","v186","v187","v188","v189","v190","v191","v192","v193","v194","v195","v196","v197","v198","v199","v200","v201","v202","v203","v204","v205","v206","v207","v208","v209","v210","v211","v212","v213","v214","v215","v216","v217","v218","v219","v220","v221","v222","v223","v224","v225","v226","v227","v228","v229","v230","v231","v232","v233","v234","v235","v236","v237","v238","v239","v240","v241","v242","v243","v244","v245","v246","v247","v248","v249","v250","v251","v252","v253","v254","v255"
#define CLOB_A "a0","a1","a2","a3","a4","a5","a6","a7","a8","a9","a10","a11","a12","a13","a14","a15","a16","a17","a18","a19","a20","a21","a22","a23","a24","a25","a26","a27","a28","a29","a30","a31","a32","a33","a34","a35","a36","a37","a38","a39","a40","a41","a42","a43","a44","a45","a46","a47","a48","a49","a50","a51","a52","a53","a54","a55","a56","a57","a58","a59","a60","a61","a62","a63","a64","a65","a66","a67","a68","a69","a70","a71","a72","a73","a74","a75","a76","a77","a78","a79","a80","a81","a82","a83","a84","a85","a86","a87","a88","a89","a90","a91","a92","a93","a94","a95","a96","a97","a98","a99","a100","a101","a102","a103","a104","a105","a106","a107","a108","a109","a110","a111","a112","a113","a114","a115","a116","a117","a118","a119","a120","a121","a122","a123","a124","a125","a126","a127","a128","a129","a130","a131","a132","a133","a134","a135","a136","a137","a138","a139","a140","a141","a142","a143","a144","a145","a146","a147","a148","a149","a150","a151","a152","a153","a154","a155","a156","a157","a158","a159","a160","a161","a162","a163","a164","a165","a166","a167","a168","a169","a170","a171","a172","a173","a174","a175","a176","a177","a178","a179","a180","a181","a182","a183","a184","a185","a186","a187","a188","a189","a190","a191","a192","a193","a194","a195","a196","a197","a198","a199","a200","a201","a202","a203","a204","a205","a206","a207","a208","a209","a210","a211","a212","a213","a214","a215","a216","a217","a218","a219","a220","a221","a222","a223","a224","a225","a226","a227","a228","a229","a230","a231","a232","a233","a234","a235","a236","a237","a238","a239","a240","a241","a242","a243","a244","a245","a246","a247","a248","a249","a250","a251","a252","a253","a254","a255"
#define CLOB_S "s8","s9","s10","s11","s12","s13","s14","s15","s16","s17","s18","s19","s20","s21","s22","s23","s24","s25","s26","s27","s28","s29","s30","s31","s32","s33","s34","s35","s36","s37","s38","s39","s40","s41","s42","s43","s44","s45","s46","s47","s48","s49","s50","s51","s52","s53","s54","s55","s56","s57","s58","s59","s60","s61","s62","s63","s64","s65","s66","s67","s68","s69","s70","s71","s72","s73","s74","s75","s76","s77","s78","s79","s80","s81","s82","s83","s84","s85","s86","s87","s88","s89","s90","s91","s92","s93","s94","s95","s96","s97","s98","s99"
__global__ __launch_bounds__(256, 1) __attribute__((aligned(4096))) void ka_fused(const unsigned short* __restrict__ x, const unsigned short* __restrict__ Kc, const unsigned short* __restrict__ Vc,
                                                   const unsigned short* __restrict__ wq, const unsigned short* __restrict__ wo,
                                                   const unsigned short* __restrict__ wr, const unsigned short* __restrict__ wg, const unsigned short* __restrict__ wu,
                                                   const unsigned short* __restrict__ wd,
                                                   char* __restrict__ ws, unsigned short* __restrict__ out
                                                   ) {
  extern __shared__ __attribute__((aligned(16))) char lds[];
  const unsigned w = __builtin_amdgcn_readfirstlane(threadIdx.x >> 6);
  const unsigned slds = __builtin_amdgcn_readfirstlane((unsigned)(size_t)lds);
  asm volatile(
R"ASM(
.macro PM a, b, c, d:vararg
v_perm_b32 \a, \b, \c, \d
.endm
.macro MM a, b, c, d:vararg
v_mfma_f32_16x16x32_bf16 \a, \b, \c, \d
.endm
.macro TR a, b:vararg
ds_read_b64_tr_b16 \a, \b
.endm
.macro CV a, b, c:vararg
v_cvt_pk_bf16_f32 \a, \b, \c
.endm
.macro AR a, b:vararg
v_accvgpr_read_b32 \a, \b
.endm
.macro AW a, b:vararg
v_accvgpr_write_b32 \a, \b
.endm
.macro EX a, b:vararg
v_exp_f32 \a, \b
.endm
.macro RD a, b:vararg
ds_read_b128 \a, \b
.endm
s_branch LPG0_%=
.p2align 6
LPG0_%=:
s_load_dwordx2 s[16:17], %[ka], 24
s_load_dwordx2 s[32:33], %[ka], 0
s_load_dwordx2 s[20:21], %[ka], 72
s_load_dwordx2 s[8:9], %[ka], 8
s_load_dwordx2 s[12:13], %[ka], 16
s_load_dwordx2 s[24:25], %[ka], 32
s_load_dwordx2 s[44:45], %[ka], 40
s_load_dwordx2 s[28:29], %[ka], 64
s_load_dwordx2 s[40:41], %[ka], 80
s_lshl_b32 s77, %[swave], 2
s_and_b32 s77, s77, 8
s_add_u32 s77, s77, 48
s_load_dwordx2 s[36:37], %[ka], s77
v_mbcnt_lo_u32_b32 v127, -1, 0
v_mbcnt_hi_u32_b32 v127, -1, v127
v_lshlrev_b32 v248, 4, v127
s_lshr_b32 s77, %[sb], 3
s_lshl_b32 s77, s77, 2
s_add_u32 s77, s77, %[swave]
s_lshl_b32 s77, s77, 10
v_add_u32 v118, s77, v248
s_mov_b32 s77, LKEND_ka-LPC_%=-16
v_min_u32 v118, s77, v118
s_getpc_b64 s[98:99]
LPC_%=:
global_load_dwordx4 a[252:255], v118, s[98:99]
s_and_b32 s77, %[sb], 7
s_lshl_b32 s77, s77, 2
s_bfe_u32 s84, %[sb], 131075
s_add_u32 s49, s77, s84
s_lshr_b32 s50, %[sb], 5
s_lshr_b32 s76, %[sb], 8
s_lshl_b32 s76, s76, 7
s_and_b32 s77, %[sb], 127
s_or_b32 s76, s76, s77
s_lshl_b32 s76, s76, 2
s_add_u32 s76, s76, %[swave]
s_lshl_b32 s76, s76, 1
s_bitcmp1_b32 %[sb], 7
s_cselect_b64 s[52:53], -1, 0
s_and_b32 s54, s52, 1
s_mov_b32 s55, s54
s_and_b32 s54, s52, 0x10001
s_mov_b32 s55, s54
v_lshrrev_b32 v180, 5, v127
s_mul_i32 s82, s76, 2560
v_and_b32 v182, 240, v248
v_lshrrev_b32 v181, 4, v127
v_mul_u32_u24 v168, 1280, v181
v_add_u32 v168, v168, v182
s_lshl_b32 s77, %[swave], 12
s_add_u32 s65, s77, %[slds]
s_lshl_b32 s89, %[swave], 17
v_bfe_u32 v210, v127, 2, 3
v_and_b32 v212, 3, v127
v_lshlrev_b32 v213, 4, v212
v_lshl_add_u32 v213, v180, 6, v213
v_lshl_add_u32 v253, v210, 13, v213
v_add_u32 v254, 128, v253
s_mul_i32 s96, %[swave], 4112
s_add_u32 s96, s96, %[slds]
s_lshl_b32 s77, s50, 2
s_add_u32 s77, s77, %[swave]
s_mul_i32 s68, s77, 81920
s_mul_i32 s83, s77, 560128
s_add_u32 s83, s83, 2752512
s_lshl_b32 s93, s77, 9
s_lshl_b32 s94, %[swave], 13
s_add_u32 s94, s94, %[slds]
v_lshrrev_b32 v211, 3, v127
v_mul_u32_u24 v220, 1280, v211
v_lshrrev_b32 v212, 4, v127
v_and_b32 v211, 7, v127
v_xor_b32 v211, v211, v212
v_lshl_add_u32 v220, v211, 4, v220
v_xor_b32 v221, 64, v220
s_bitcmp1_b32 s84, 0
s_mov_b32 s77, 0x77552200
s_cselect_b32 s77, 0x77552200, s77
s_lshl_b32 s91, s50, 2
s_lshr_b32 s77, s77, s91
s_and_b32 s77, s77, 15
s_lshl_b32 s86, s77, 7
s_lshl_b32 s85, s77, 19
s_lshl_b32 s90, s49, 8
s_waitcnt lgkmcnt(0)
s_add_u32 s16, s16, s90
s_addc_u32 s17, s17, 0
s_sub_u32 s18, 5242880, s90
s_mov_b32 s19, 131072
s_mov_b32 s34, 2621440
s_mov_b32 s35, 131072
s_mov_b32 s22, 67108864
s_mov_b32 s23, 131072
s_bitcmp1_b32 %[sb], 7
s_cbranch_scc1 LCA_%=
s_add_u32 s87, s89, s85
s_add_u32 s85, s85, 524288
s_cmp_ge_u32 s85, 5242880
s_cselect_b32 s85, 0, s85
s_add_u32 m0, s96, 65536
s_nop 0
buffer_load_dwordx4 v253, s[16:19], s87 offen offset:0 lds
s_add_u32 m0, s96, 66560
s_nop 0
buffer_load_dwordx4 v254, s[16:19], s87 offen offset:0 lds
s_add_u32 s87, s87, 65536
s_add_u32 m0, s96, 67592
s_nop 0
buffer_load_dwordx4 v253, s[16:19], s87 offen offset:0 lds
s_add_u32 m0, s96, 68616
s_nop 0
buffer_load_dwordx4 v254, s[16:19], s87 offen offset:0 lds
s_add_u32 s88, s68, s86
s_add_u32 s86, s86, 128
s_cmp_ge_u32 s86, 1280
s_cselect_b32 s86, 0, s86
s_add_u32 m0, s94, 0
s_add_u32 s95, s88, 0
buffer_load_dwordx4 v220, s[32:35], s95 offen offset:0 lds
s_add_u32 s95, s88, 9216
buffer_load_dwordx4 v221, s[32:35], s95 offen offset:1024 lds
s_add_u32 s95, s88, 18432
buffer_load_dwordx4 v220, s[32:35], s95 offen offset:2048 lds
s_add_u32 s95, s88, 27648
buffer_load_dwordx4 v221, s[32:35], s95 offen offset:3072 lds
s_add_u32 m0, s94, 4096
s_add_u32 s95, s88, 40960
buffer_load_dwordx4 v220, s[32:35], s95 offen offset:0 lds
s_add_u32 s95, s88, 50176
buffer_load_dwordx4 v221, s[32:35], s95 offen offset:1024 lds
s_add_u32 s95, s88, 59392
buffer_load_dwordx4 v220, s[32:35], s95 offen offset:2048 lds
s_add_u32 s95, s88, 68608
buffer_load_dwordx4 v221, s[32:35], s95 offen offset:3072 lds
s_add_u32 s87, s89, s85
s_add_u32 s85, s85, 524288
s_cmp_ge_u32 s85, 5242880
s_cselect_b32 s85, 0, s85
s_add_u32 m0, s96, 81984
s_nop 0
buffer_load_dwordx4 v253, s[16:19], s87 offen offset:0 lds
s_add_u32 m0, s96, 83008
s_nop 0
buffer_load_dwordx4 v254, s[16:19], s87 offen offset:0 lds
s_add_u32 s87, s87, 65536
s_add_u32 m0, s96, 84040
s_nop 0
buffer_load_dwordx4 v253, s[16:19], s87 offen offset:0 lds
s_add_u32 m0, s96, 85064
s_nop 0
buffer_load_dwordx4 v254, s[16:19], s87 offen offset:0 lds
s_branch LCT_%=
LCA_%=:
buffer_load_dwordx4 v[128:131], v168, s[32:35], s82 offen offset:0
buffer_load_dwordx4 v[132:135], v168, s[32:35], s82 offen offset:256
buffer_load_dwordx4 v[136:139], v168, s[32:35], s82 offen offset:512
buffer_load_dwordx4 v[140:143], v168, s[32:35], s82 offen offset:768
buffer_load_dwordx4 v[144:147], v168, s[32:35], s82 offen offset:1024
global_load_dwordx4 v[100:103], v182, s[44:45] offset:0
global_load_dwordx4 v[104:107], v182, s[44:45] offset:256
global_load_dwordx4 v[108:111], v182, s[44:45] offset:512
global_load_dwordx4 v[112:115], v182, s[44:45] offset:768
global_load_dwordx4 v[116:119], v182, s[44:45] offset:1024
LCT_%=:
v_and_b32 v211, 15, v127
v_bfe_u32 v222, v127, 1, 3
v_xor_b32 v222, v222, v212
v_lshlrev_b32 v222, 4, v222
v_lshl_add_u32 v222, v211, 7, v222
s_lshl_b32 s77, %[sb], 11
s_and_b32 s77, s77, 2048
v_add_u32 v222, s77, v222
v_xor_b32 v223, 64, v222
v_xor_b32 v217, 2048, v222
v_add_u32 v217, s94, v217
v_xor_b32 v218, 2048, v223
v_add_u32 v218, s94, v218
v_add_u32 v222, s94, v222
v_add_u32 v223, s94, v223
v_and_b32 v214, 1, v212
v_lshlrev_b32 v215, 5, v214
v_xor_b32 v215, v215, v213
v_lshl_add_u32 v235, v210, 8, v215
v_add_u32 v235, 128, v235
v_and_b32 v234, 1, v127
v_mul_u32_u24 v234, 43690, v234
v_add_u32 v234, 17476, v234
v_bfe_u32 v215, v127, 2, 2
v_and_b32 v216, 3, v127
v_mul_u32_u24 v252, 2056, v212
v_lshl_add_u32 v252, v215, 6, v252
v_lshl_add_u32 v252, v216, 4, v252
s_xor_b32 s77, %[swave], 0
s_lshl_b32 s77, s77, 9
s_add_u32 s77, s77, %[slds]
s_add_u32 s77, s77, 65536
v_add_u32 v192, s77, v252
v_add_u32 v196, 49344, v192
s_xor_b32 s77, %[swave], 1
s_lshl_b32 s77, s77, 9
s_add_u32 s77, s77, %[slds]
s_add_u32 s77, s77, 65536
v_add_u32 v193, s77, v252
v_add_u32 v197, 49344, v193
s_xor_b32 s77, %[swave], 2
s_lshl_b32 s77, s77, 9
s_add_u32 s77, s77, %[slds]
s_add_u32 s77, s77, 65536
v_add_u32 v194, s77, v252
v_add_u32 v198, 49344, v194
s_xor_b32 s77, %[swave], 3
s_lshl_b32 s77, s77, 9
s_add_u32 s77, s77, %[slds]
s_add_u32 s77, s77, 65536
v_add_u32 v195, s77, v252
v_add_u32 v199, 49344, v195
v_mov_b32 v236, 0x3f803f80
v_mov_b32 v184, 0
v_mov_b32 v237, 0x3f803f80
v_mov_b32 v185, 0
v_mov_b32 v238, 0x3f803f80
v_mov_b32 v188, 0
v_mov_b32 v239, 0x3f803f80
v_mov_b32 v189, 0
s_getpc_b64 s[98:99]
LTA0_%=:
s_lshl_b32 s73, %[swave], 12
s_add_u32 s73, s73, LPG1_%=-LTA0_%=
s_add_u32 s98, s98, s73
s_addc_u32 s99, s99, 0
s_mov_b64 exec, 0
s_setpc_b64 s[98:99]
LTR0_%=:
s_mov_b64 exec, -1
s_nop 0
s_bitcmp1_b32 %[sb], 7
s_cbranch_scc1 LCB_%=
s_add_u32 s88, s68, s86
s_add_u32 s86, s86, 128
s_cmp_ge_u32 s86, 1280
s_cselect_b32 s86, 0, s86
s_add_u32 m0, s94, 32768
s_add_u32 s95, s88, 0
buffer_load_dwordx4 v220, s[32:35], s95 offen offset:0 lds
s_add_u32 s95, s88, 9216
buffer_load_dwordx4 v221, s[32:35], s95 offen offset:1024 lds
s_add_u32 s95, s88, 18432
buffer_load_dwordx4 v220, s[32:35], s95 offen offset:2048 lds
s_add_u32 s95, s88, 27648
buffer_load_dwordx4 v221, s[32:35], s95 offen offset:3072 lds
s_add_u32 m0, s94, 36864
s_add_u32 s95, s88, 40960
buffer_load_dwordx4 v220, s[32:35], s95 offen offset:0 lds
s_add_u32 s95, s88, 50176
buffer_load_dwordx4 v221, s[32:35], s95 offen offset:1024 lds
s_add_u32 s95, s88, 59392
buffer_load_dwordx4 v220, s[32:35], s95 offen offset:2048 lds
s_add_u32 s95, s88, 68608
buffer_load_dwordx4 v221, s[32:35], s95 offen offset:3072 lds
s_branch LCM_%=
LCB_%=:
s_waitcnt vmcnt(5)
s_mov_b32 s61, 0x3acccccd
s_add_u32 s87, s89, s85
s_add_u32 s85, s85, 524288
s_cmp_ge_u32 s85, 5242880
s_cselect_b32 s85, 0, s85
s_add_u32 m0, s96, 65536
s_nop 0
buffer_load_dwordx4 v253, s[16:19], s87 offen offset:0 lds
s_lshl_b32 s60, s76, 1
s_sub_u32 s60, 65535, s60
v_lshlrev_b32 v0, 16, v128
v_and_b32 v1, 0xffff0000, v128
v_lshlrev_b32 v2, 16, v129
v_and_b32 v3, 0xffff0000, v129
v_lshlrev_b32 v4, 16, v130
v_and_b32 v5, 0xffff0000, v130
s_add_u32 m0, s96, 66560
s_nop 0
buffer_load_dwordx4 v254, s[16:19], s87 offen offset:0 lds
v_lshlrev_b32 v6, 16, v131
v_and_b32 v7, 0xffff0000, v131
v_lshlrev_b32 v8, 16, v132
v_and_b32 v9, 0xffff0000, v132
v_lshlrev_b32 v10, 16, v133
v_and_b32 v11, 0xffff0000, v133
v_lshlrev_b32 v12, 16, v134
v_and_b32 v13, 0xffff0000, v134
s_add_u32 s87, s87, 65536
s_add_u32 m0, s96, 67592
s_nop 0
buffer_load_dwordx4 v253, s[16:19], s87 offen offset:0 lds
v_lshlrev_b32 v14, 16, v135
v_and_b32 v15, 0xffff0000, v135
v_lshlrev_b32 v16, 16, v136
v_and_b32 v17, 0xffff0000, v136
v_lshlrev_b32 v18, 16, v137
v_and_b32 v19, 0xffff0000, v137
v_lshlrev_b32 v20, 16, v138
v_and_b32 v21, 0xffff0000, v138
s_add_u32 m0, s96, 68616
s_nop 0
buffer_load_dwordx4 v254, s[16:19], s87 offen offset:0 lds
v_lshlrev_b32 v22, 16, v139
v_and_b32 v23, 0xffff0000, v139
v_lshlrev_b32 v24, 16, v140
v_and_b32 v25, 0xffff0000, v140
v_lshlrev_b32 v26, 16, v141
v_and_b32 v27, 0xffff0000, v141
v_lshlrev_b32 v28, 16, v142
v_and_b32 v29, 0xffff0000, v142
s_add_u32 s88, s68, s86
s_add_u32 s86, s86, 128
s_cmp_ge_u32 s86, 1280
s_cselect_b32 s86, 0, s86
s_add_u32 m0, s94, 0
s_add_u32 s95, s88, 0
buffer_load_dwordx4 v220, s[32:35], s95 offen offset:0 lds
v_lshlrev_b32 v30, 16, v143
v_and_b32 v31, 0xffff0000, v143
v_lshlrev_b32 v32, 16, v144
v_and_b32 v33, 0xffff0000, v144
v_lshlrev_b32 v34, 16, v145
v_and_b32 v35, 0xffff0000, v145
v_lshlrev_b32 v36, 16, v146
v_and_b32 v37, 0xffff0000, v146
s_add_u32 s95, s88, 9216
buffer_load_dwordx4 v221, s[32:35], s95 offen offset:1024 lds
v_lshlrev_b32 v38, 16, v147
v_and_b32 v39, 0xffff0000, v147
v_pk_add_f32 v[40:41], v[0:1], v[2:3]
v_pk_add_f32 v[42:43], v[4:5], v[6:7]
v_pk_add_f32 v[40:41], v[40:41], v[8:9]
v_pk_add_f32 v[42:43], v[42:43], v[10:11]
v_pk_add_f32 v[40:41], v[40:41], v[12:13]
v_pk_add_f32 v[42:43], v[42:43], v[14:15]
s_add_u32 s95, s88, 18432
buffer_load_dwordx4 v220, s[32:35], s95 offen offset:2048 lds
v_pk_add_f32 v[40:41], v[40:41], v[16:17]
v_pk_add_f32 v[42:43], v[42:43], v[18:19]
v_pk_add_f32 v[40:41], v[40:41], v[20:21]
v_pk_add_f32 v[42:43], v[42:43], v[22:23]
v_pk_add_f32 v[40:41], v[40:41], v[24:25]
v_pk_add_f32 v[42:43], v[42:43], v[26:27]
v_pk_add_f32 v[40:41], v[40:41], v[28:29]
v_pk_add_f32 v[42:43], v[42:43], v[30:31]
s_add_u32 s95, s88, 27648
buffer_load_dwordx4 v221, s[32:35], s95 offen offset:3072 lds
v_pk_add_f32 v[40:41], v[40:41], v[32:33]
v_pk_add_f32 v[42:43], v[42:43], v[34:35]
v_pk_add_f32 v[40:41], v[40:41], v[36:37]
v_pk_add_f32 v[42:43], v[42:43], v[38:39]
v_pk_add_f32 v[40:41], v[40:41], v[42:43]
v_add_f32 v48, v40, v41
s_nop 1
v_add_f32_dpp v48, v48, v48 row_ror:8 row_mask:0xf bank_mask:0xf
s_nop 1
v_add_f32_dpp v48, v48, v48 row_ror:4 row_mask:0xf bank_mask:0xf
s_add_u32 m0, s94, 4096
s_add_u32 s95, s88, 40960
buffer_load_dwordx4 v220, s[32:35], s95 offen offset:0 lds
v_add_f32_dpp v48, v48, v48 row_ror:2 row_mask:0xf bank_mask:0xf
s_nop 1
v_add_f32_dpp v48, v48, v48 row_ror:1 row_mask:0xf bank_mask:0xf
v_mul_f32 v48, 0x3acccccd, v48
CV v44,v48,v48
v_and_b32 v44, 0xffff0000, v44
v_sub_f32 v48, v48, v44
v_pk_add_f32 v[0:1], v[0:1], v[44:45] op_sel_hi:[1,0] neg_lo:[0,1] neg_hi:[0,1]
v_pk_add_f32 v[2:3], v[2:3], v[44:45] op_sel_hi:[1,0] neg_lo:[0,1] neg_hi:[0,1]
s_add_u32 s95, s88, 50176
buffer_load_dwordx4 v221, s[32:35], s95 offen offset:1024 lds
v_pk_add_f32 v[4:5], v[4:5], v[44:45] op_sel_hi:[1,0] neg_lo:[0,1] neg_hi:[0,1]
v_pk_add_f32 v[6:7], v[6:7], v[44:45] op_sel_hi:[1,0] neg_lo:[0,1] neg_hi:[0,1]
v_pk_add_f32 v[8:9], v[8:9], v[44:45] op_sel_hi:[1,0] neg_lo:[0,1] neg_hi:[0,1]
v_pk_add_f32 v[10:11], v[10:11], v[44:45] op_sel_hi:[1,0] neg_lo:[0,1] neg_hi:[0,1]
v_pk_add_f32 v[12:13], v[12:13], v[44:45] op_sel_hi:[1,0] neg_lo:[0,1] neg_hi:[0,1]
)ASM"
R"ASM(
v_pk_add_f32 v[14:15], v[14:15], v[44:45] op_sel_hi:[1,0] neg_lo:[0,1] neg_hi:[0,1]
v_pk_add_f32 v[16:17], v[16:17], v[44:45] op_sel_hi:[1,0] neg_lo:[0,1] neg_hi:[0,1]
v_pk_add_f32 v[18:19], v[18:19], v[44:45] op_sel_hi:[1,0] neg_lo:[0,1] neg_hi:[0,1]
s_add_u32 s95, s88, 59392
buffer_load_dwordx4 v220, s[32:35], s95 offen offset:2048 lds
v_pk_add_f32 v[20:21], v[20:21], v[44:45] op_sel_hi:[1,0] neg_lo:[0,1] neg_hi:[0,1]
v_pk_add_f32 v[22:23], v[22:23], v[44:45] op_sel_hi:[1,0] neg_lo:[0,1] neg_hi:[0,1]
v_pk_add_f32 v[24:25], v[24:25], v[44:45] op_sel_hi:[1,0] neg_lo:[0,1] neg_hi:[0,1]
v_pk_add_f32 v[26:27], v[26:27], v[44:45] op_sel_hi:[1,0] neg_lo:[0,1] neg_hi:[0,1]
v_pk_add_f32 v[28:29], v[28:29], v[44:45] op_sel_hi:[1,0] neg_lo:[0,1] neg_hi:[0,1]
v_pk_add_f32 v[30:31], v[30:31], v[44:45] op_sel_hi:[1,0] neg_lo:[0,1] neg_hi:[0,1]
v_pk_add_f32 v[32:33], v[32:33], v[44:45] op_sel_hi:[1,0] neg_lo:[0,1] neg_hi:[0,1]
v_pk_add_f32 v[34:35], v[34:35], v[44:45] op_sel_hi:[1,0] neg_lo:[0,1] neg_hi:[0,1]
s_add_u32 s95, s88, 68608
buffer_load_dwordx4 v221, s[32:35], s95 offen offset:3072 lds
v_pk_add_f32 v[36:37], v[36:37], v[44:45] op_sel_hi:[1,0] neg_lo:[0,1] neg_hi:[0,1]
v_pk_add_f32 v[38:39], v[38:39], v[44:45] op_sel_hi:[1,0] neg_lo:[0,1] neg_hi:[0,1]
v_pk_mul_f32 v[40:41], v[0:1], v[0:1]
v_pk_mul_f32 v[42:43], v[2:3], v[2:3]
v_pk_fma_f32 v[40:41], v[4:5], v[4:5], v[40:41]
v_pk_fma_f32 v[42:43], v[6:7], v[6:7], v[42:43]
v_pk_fma_f32 v[40:41], v[8:9], v[8:9], v[40:41]
v_pk_fma_f32 v[42:43], v[10:11], v[10:11], v[42:43]
s_add_u32 s87, s89, s85
s_add_u32 s85, s85, 524288
s_cmp_ge_u32 s85, 5242880
s_cselect_b32 s85, 0, s85
s_add_u32 m0, s96, 81984
s_nop 0
buffer_load_dwordx4 v253, s[16:19], s87 offen offset:0 lds
v_pk_fma_f32 v[40:41], v[12:13], v[12:13], v[40:41]
v_pk_fma_f32 v[42:43], v[14:15], v[14:15], v[42:43]
v_pk_fma_f32 v[40:41], v[16:17], v[16:17], v[40:41]
v_pk_fma_f32 v[42:43], v[18:19], v[18:19], v[42:43]
v_pk_fma_f32 v[40:41], v[20:21], v[20:21], v[40:41]
v_pk_fma_f32 v[42:43], v[22:23], v[22:23], v[42:43]
v_pk_fma_f32 v[40:41], v[24:25], v[24:25], v[40:41]
v_pk_fma_f32 v[42:43], v[26:27], v[26:27], v[42:43]
s_add_u32 m0, s96, 83008
s_nop 0
buffer_load_dwordx4 v254, s[16:19], s87 offen offset:0 lds
v_pk_fma_f32 v[40:41], v[28:29], v[28:29], v[40:41]
v_pk_fma_f32 v[42:43], v[30:31], v[30:31], v[42:43]
v_pk_fma_f32 v[40:41], v[32:33], v[32:33], v[40:41]
v_pk_fma_f32 v[42:43], v[34:35], v[34:35], v[42:43]
v_pk_fma_f32 v[40:41], v[36:37], v[36:37], v[40:41]
v_pk_fma_f32 v[42:43], v[38:39], v[38:39], v[42:43]
v_mul_f32 v48, v48, v48
v_pk_add_f32 v[40:41], v[40:41], v[42:43]
s_add_u32 s87, s87, 65536
s_add_u32 m0, s96, 84040
s_nop 0
buffer_load_dwordx4 v253, s[16:19], s87 offen offset:0 lds
v_add_f32 v49, v40, v41
CV v128,v0,v1
CV v129,v2,v3
v_add_f32_dpp v49, v49, v49 row_ror:8 row_mask:0xf bank_mask:0xf
CV v130,v4,v5
CV v131,v6,v7
v_add_f32_dpp v49, v49, v49 row_ror:4 row_mask:0xf bank_mask:0xf
CV v132,v8,v9
s_add_u32 m0, s96, 85064
s_nop 0
buffer_load_dwordx4 v254, s[16:19], s87 offen offset:0 lds
CV v133,v10,v11
v_add_f32_dpp v49, v49, v49 row_ror:2 row_mask:0xf bank_mask:0xf
CV v134,v12,v13
CV v135,v14,v15
v_add_f32_dpp v49, v49, v49 row_ror:1 row_mask:0xf bank_mask:0xf
v_fma_f32 v49, v49, s61, -v48
CV v136,v16,v17
v_max_f32 v49, 0, v49
s_add_u32 s88, s68, s86
s_add_u32 s86, s86, 128
s_cmp_ge_u32 s86, 1280
s_cselect_b32 s86, 0, s86
s_add_u32 m0, s94, 32768
s_add_u32 s95, s88, 0
buffer_load_dwordx4 v220, s[32:35], s95 offen offset:0 lds
CV v137,v18,v19
CV v49,v49,v49
CV v138,v20,v21
v_and_b32 v49, 0xffff0000, v49
CV v139,v22,v23
v_add_f32 v49, 0x3727c5ac, v49
CV v140,v24,v25
CV v49,v49,v49
s_add_u32 s95, s88, 9216
buffer_load_dwordx4 v221, s[32:35], s95 offen offset:1024 lds
CV v141,v26,v27
v_and_b32 v49, 0xffff0000, v49
CV v142,v28,v29
v_sqrt_f32 v49, v49
CV v143,v30,v31
CV v178,v44,v49
CV v144,v32,v33
v_and_b32 v49, 0xffff0000, v178
s_add_u32 s95, s88, 18432
buffer_load_dwordx4 v220, s[32:35], s95 offen offset:2048 lds
CV v145,v34,v35
v_rcp_f32 v46, v49
CV v146,v36,v37
CV v147,v38,v39
v_lshlrev_b32 v0, 16, v128
v_and_b32 v1, 0xffff0000, v128
v_lshlrev_b32 v2, 16, v129
v_and_b32 v3, 0xffff0000, v129
s_add_u32 s95, s88, 27648
buffer_load_dwordx4 v221, s[32:35], s95 offen offset:3072 lds
v_lshlrev_b32 v4, 16, v130
v_and_b32 v5, 0xffff0000, v130
v_lshlrev_b32 v6, 16, v131
v_and_b32 v7, 0xffff0000, v131
v_lshlrev_b32 v8, 16, v132
v_and_b32 v9, 0xffff0000, v132
v_lshlrev_b32 v10, 16, v133
v_and_b32 v11, 0xffff0000, v133
s_add_u32 m0, s94, 36864
s_add_u32 s95, s88, 40960
buffer_load_dwordx4 v220, s[32:35], s95 offen offset:0 lds
v_lshlrev_b32 v12, 16, v134
v_and_b32 v13, 0xffff0000, v134
v_lshlrev_b32 v14, 16, v135
v_and_b32 v15, 0xffff0000, v135
v_lshlrev_b32 v16, 16, v136
v_and_b32 v17, 0xffff0000, v136
v_lshlrev_b32 v18, 16, v137
v_and_b32 v19, 0xffff0000, v137
s_add_u32 s95, s88, 50176
buffer_load_dwordx4 v221, s[32:35], s95 offen offset:1024 lds
v_lshlrev_b32 v20, 16, v138
v_and_b32 v21, 0xffff0000, v138
v_lshlrev_b32 v22, 16, v139
v_and_b32 v23, 0xffff0000, v139
v_lshlrev_b32 v24, 16, v140
v_and_b32 v25, 0xffff0000, v140
v_lshlrev_b32 v26, 16, v141
v_and_b32 v27, 0xffff0000, v141
s_add_u32 s95, s88, 59392
buffer_load_dwordx4 v220, s[32:35], s95 offen offset:2048 lds
v_lshlrev_b32 v28, 16, v142
v_and_b32 v29, 0xffff0000, v142
v_lshlrev_b32 v30, 16, v143
v_and_b32 v31, 0xffff0000, v143
v_lshlrev_b32 v32, 16, v144
v_and_b32 v33, 0xffff0000, v144
v_lshlrev_b32 v34, 16, v145
v_and_b32 v35, 0xffff0000, v145
s_add_u32 s95, s88, 68608
buffer_load_dwordx4 v221, s[32:35], s95 offen offset:3072 lds
v_lshlrev_b32 v36, 16, v146
v_and_b32 v37, 0xffff0000, v146
v_lshlrev_b32 v38, 16, v147
v_and_b32 v39, 0xffff0000, v147
v_pk_mul_f32 v[0:1], v[0:1], v[46:47] op_sel_hi:[1,0]
v_pk_mul_f32 v[2:3], v[2:3], v[46:47] op_sel_hi:[1,0]
v_pk_mul_f32 v[4:5], v[4:5], v[46:47] op_sel_hi:[1,0]
v_pk_mul_f32 v[6:7], v[6:7], v[46:47] op_sel_hi:[1,0]
v_pk_mul_f32 v[8:9], v[8:9], v[46:47] op_sel_hi:[1,0]
v_pk_mul_f32 v[10:11], v[10:11], v[46:47] op_sel_hi:[1,0]
v_pk_mul_f32 v[12:13], v[12:13], v[46:47] op_sel_hi:[1,0]
v_pk_mul_f32 v[14:15], v[14:15], v[46:47] op_sel_hi:[1,0]
v_pk_mul_f32 v[16:17], v[16:17], v[46:47] op_sel_hi:[1,0]
v_pk_mul_f32 v[18:19], v[18:19], v[46:47] op_sel_hi:[1,0]
v_pk_mul_f32 v[20:21], v[20:21], v[46:47] op_sel_hi:[1,0]
v_pk_mul_f32 v[22:23], v[22:23], v[46:47] op_sel_hi:[1,0]
v_pk_mul_f32 v[24:25], v[24:25], v[46:47] op_sel_hi:[1,0]
v_pk_mul_f32 v[26:27], v[26:27], v[46:47] op_sel_hi:[1,0]
v_pk_mul_f32 v[28:29], v[28:29], v[46:47] op_sel_hi:[1,0]
v_pk_mul_f32 v[30:31], v[30:31], v[46:47] op_sel_hi:[1,0]
v_pk_mul_f32 v[32:33], v[32:33], v[46:47] op_sel_hi:[1,0]
v_pk_mul_f32 v[34:35], v[34:35], v[46:47] op_sel_hi:[1,0]
v_pk_mul_f32 v[36:37], v[36:37], v[46:47] op_sel_hi:[1,0]
v_pk_mul_f32 v[38:39], v[38:39], v[46:47] op_sel_hi:[1,0]
CV v128,v0,v1
CV v129,v2,v3
CV v130,v4,v5
CV v131,v6,v7
CV v132,v8,v9
CV v133,v10,v11
CV v134,v12,v13
CV v135,v14,v15
CV v136,v16,v17
CV v137,v18,v19
CV v138,v20,v21
CV v139,v22,v23
CV v140,v24,v25
CV v141,v26,v27
CV v142,v28,v29
CV v143,v30,v31
CV v144,v32,v33
CV v145,v34,v35
CV v146,v36,v37
CV v147,v38,v39
s_waitcnt vmcnt(24)
v_dot2_f32_bf16 v40, v128, v100, 0
v_dot2_f32_bf16 v41, v129, v101, 0
v_dot2_f32_bf16 v42, v130, v102, 0
v_dot2_f32_bf16 v43, v131, v103, 0
v_dot2_f32_bf16 v40, v132, v104, v40
v_dot2_f32_bf16 v41, v133, v105, v41
v_dot2_f32_bf16 v42, v134, v106, v42
v_dot2_f32_bf16 v43, v135, v107, v43
v_dot2_f32_bf16 v40, v136, v108, v40
v_dot2_f32_bf16 v41, v137, v109, v41
v_dot2_f32_bf16 v42, v138, v110, v42
v_dot2_f32_bf16 v43, v139, v111, v43
v_dot2_f32_bf16 v40, v140, v112, v40
v_dot2_f32_bf16 v41, v141, v113, v41
v_dot2_f32_bf16 v42, v142, v114, v42
v_dot2_f32_bf16 v43, v143, v115, v43
v_dot2_f32_bf16 v40, v144, v116, v40
v_dot2_f32_bf16 v41, v145, v117, v41
v_dot2_f32_bf16 v42, v146, v118, v42
v_dot2_f32_bf16 v43, v147, v119, v43
s_nop 2
v_pk_add_f32 v[40:41], v[40:41], v[42:43]
v_add_f32 v160, v40, v41
s_nop 1
v_add_f32_dpp v160, v160, v160 row_ror:8 row_mask:0xf bank_mask:0xf
s_nop 1
v_add_f32_dpp v160, v160, v160 row_ror:4 row_mask:0xf bank_mask:0xf
s_nop 1
v_add_f32_dpp v160, v160, v160 row_ror:2 row_mask:0xf bank_mask:0xf
s_nop 1
v_add_f32_dpp v160, v160, v160 row_ror:1 row_mask:0xf bank_mask:0xf
CV v160,v160,v160
v_ashrrev_i32 v50, 31, v160
v_or_b32 v50, 0x80000000, v50
v_xor_b32 v160, v160, v50
v_and_b32 v160, 0xffff0000, v160
v_sub_u32 v50, s60, v181
v_or_b32 v160, v160, v50
LCM_%=:
s_waitcnt vmcnt(12)
LPG1_%=:
s_cbranch_execz LPG5_%=
RD a[128:131],v222 offset:0
RD a[132:135],v223 offset:0
RD a[136:139],v217 offset:0
RD a[140:143],v218 offset:0
RD a[144:147],v222 offset:4096
RD a[148:151],v223 offset:4096
RD a[152:155],v217 offset:4096
RD a[156:159],v218 offset:4096
s_barrier
TR a[0:1],v192 offset:0
TR a[2:3],v192 offset:256
TR a[4:5],v192 offset:8224
TR a[6:7],v192 offset:8480
TR a[8:9],v192 offset:8
TR a[10:11],v192 offset:264
TR a[12:13],v192 offset:8232
TR a[14:15],v192 offset:8488
s_waitcnt lgkmcnt(8)
s_waitcnt lgkmcnt(4)
MM v[0:3],a[0:3],a[128:131],0
s_mov_b32 s66, 32768
TR a[16:17],v193 offset:0
TR a[18:19],v193 offset:256
MM v[0:3],a[4:7],a[132:135],v[0:3]
TR a[20:21],v193 offset:8224
TR a[22:23],v193 offset:8480
MM v[8:11],a[4:7],a[140:143],0
buffer_load_dword v169, off, s[20:23], s66 sc1
MM v[8:11],a[0:3],a[136:139],v[8:11]
s_add_u32 s82, s82, 131072
TR a[24:25],v193 offset:8
TR a[26:27],v193 offset:264
MM v[16:19],a[0:3],a[144:147],0
s_add_u32 s87, s89, s85
TR a[28:29],v193 offset:8232
TR a[30:31],v193 offset:8488
MM v[16:19],a[4:7],a[148:151],v[16:19]
s_add_u32 s85, s85, 524288
TR a[32:33],v194 offset:0
TR a[34:35],v194 offset:256
MM v[24:27],a[4:7],a[156:159],0
s_lshl_b32 s90, s76, 1
TR a[36:37],v194 offset:8224
TR a[38:39],v194 offset:8480
MM v[24:27],a[0:3],a[152:155],v[24:27]
s_waitcnt lgkmcnt(12)
TR a[40:41],v194 offset:8
TR a[42:43],v194 offset:264
s_cmp_ge_u32 s85, 5242880
s_cselect_b32 s85, 0, s85
MM v[4:7],a[8:11],a[128:131],0
s_add_u32 m0, s96, 98432
TR a[44:45],v194 offset:8232
TR a[46:47],v194 offset:8488
MM v[4:7],a[12:15],a[132:135],v[4:7]
TR a[48:49],v195 offset:0
TR a[50:51],v195 offset:256
MM v[12:15],a[12:15],a[140:143],0
buffer_load_dwordx4 v253, s[16:19], s87 offen offset:0 lds
MM v[12:15],a[8:11],a[136:139],v[12:15]
s_add_u32 m0, s96, 99456
TR a[52:53],v195 offset:8224
TR a[54:55],v195 offset:8480
MM v[20:23],a[8:11],a[144:147],0
TR a[56:57],v195 offset:8
TR a[58:59],v195 offset:264
MM v[20:23],a[12:15],a[148:151],v[20:23]
buffer_load_dwordx4 v254, s[16:19], s87 offen offset:0 lds
MM v[28:31],a[12:15],a[156:159],0
s_add_u32 s87, s87, 65536
TR a[60:61],v195 offset:8232
TR a[62:63],v195 offset:8488
MM v[28:31],a[8:11],a[152:155],v[28:31]
s_add_u32 m0, s96, 100488
s_and_b32 s77, s90, 15
s_lshl_b32 s77, s77, 1
v_smfmac_f32_16x16x64_bf16 v[184:187], v[236:239], a[0:7], v234
s_bfe_u32 s91, s90, 65540
s_add_u32 s77, s77, s91
v_smfmac_f32_16x16x64_bf16 v[188:191], v[236:239], a[8:15], v234
s_waitcnt lgkmcnt(15)
s_and_b32 s91, s90, 0xffffffe0
MM v[32:35],a[16:19],a[128:131],0
buffer_load_dwordx4 v253, s[16:19], s87 offen offset:0 lds
MM v[32:35],a[20:23],a[132:135],v[32:35]
s_add_u32 m0, s96, 101512
s_add_u32 s77, s77, s91
s_lshl_b32 s77, s77, 3
MM v[40:43],a[20:23],a[140:143],0
s_add_u32 s97, s77, 36864
s_sub_u32 s79, 65535, s90
s_waitcnt vmcnt(12)
s_barrier
MM v[40:43],a[16:19],a[136:139],v[40:43]
TR a[0:1],v192 offset:16448
TR a[2:3],v192 offset:16704
MM v[48:51],a[16:19],a[144:147],0
buffer_load_dwordx4 v254, s[16:19], s87 offen offset:0 lds
MM v[48:51],a[20:23],a[148:151],v[48:51]
TR a[4:5],v192 offset:24672
TR a[6:7],v192 offset:24928
MM v[56:59],a[20:23],a[156:159],0
s_add_u32 s88, s68, s86
TR a[8:9],v192 offset:16456
TR a[10:11],v192 offset:16712
MM v[56:59],a[16:19],a[152:155],v[56:59]
s_add_u32 s86, s86, 128
TR a[12:13],v192 offset:24680
TR a[14:15],v192 offset:24936
MM v[36:39],a[24:27],a[128:131],0
s_lshl_b32 s78, s90, 2
TR a[16:17],v193 offset:16448
TR a[18:19],v193 offset:16704
MM v[36:39],a[28:31],a[132:135],v[36:39]
)ASM"
R"ASM(
s_add_u32 s78, s78, 53248
TR a[20:21],v193 offset:24672
TR a[22:23],v193 offset:24928
s_cmp_ge_u32 s86, 1280
s_cselect_b32 s86, 0, s86
MM v[44:47],a[28:31],a[140:143],0
s_add_u32 m0, s94, 0
s_add_u32 s95, s88, 0
s_lshl_b32 s80, s76, 2
MM v[44:47],a[24:27],a[136:139],v[44:47]
buffer_load_dwordx4 v220, s[32:35], s95 offen offset:0 lds
MM v[52:55],a[24:27],a[144:147],0
s_add_u32 s95, s88, 9216
s_andn2_b32 s91, 8192, s52
s_add_u32 s80, s80, s91
MM v[52:55],a[28:31],a[148:151],v[52:55]
buffer_load_dwordx4 v221, s[32:35], s95 offen offset:1024 lds
MM v[60:63],a[28:31],a[156:159],0
s_add_u32 s95, s88, 18432
s_add_u32 s80, s80, 81920
v_lshlrev_b32 v232, 11, v180
MM v[60:63],a[24:27],a[152:155],v[60:63]
s_waitcnt lgkmcnt(15)
v_lshl_add_u32 v232, v214, 8, v232
v_lshl_add_u32 v232, v215, 6, v232
MM v[64:67],a[32:35],a[128:131],0
TR a[24:25],v193 offset:16456
TR a[26:27],v193 offset:16712
MM v[64:67],a[36:39],a[132:135],v[64:67]
buffer_load_dwordx4 v220, s[32:35], s95 offen offset:2048 lds
MM v[72:75],a[36:39],a[140:143],0
s_add_u32 s95, s88, 27648
TR a[28:29],v193 offset:24680
TR a[30:31],v193 offset:24936
MM v[72:75],a[32:35],a[136:139],v[72:75]
v_lshl_add_u32 v232, v216, 3, v232
v_lshl_add_u32 v232, v214, 5, v232
MM v[80:83],a[32:35],a[144:147],0
buffer_load_dwordx4 v221, s[32:35], s95 offen offset:3072 lds
MM v[80:83],a[36:39],a[148:151],v[80:83]
s_waitcnt vmcnt(9)
RD a[160:163],v222 offset:32768
v_xor_b32 v233, 32, v232
MM v[88:91],a[36:39],a[156:159],0
s_add_u32 s77, %[slds], 65536
RD a[164:167],v223 offset:32768
v_add_u32 v232, s77, v232
MM v[88:91],a[32:35],a[152:155],v[88:91]
RD a[168:171],v217 offset:32768
v_add_u32 v233, s77, v233
MM v[68:71],a[40:43],a[128:131],0
TR a[32:33],v194 offset:16448
TR a[34:35],v194 offset:16704
MM v[68:71],a[44:47],a[132:135],v[68:71]
s_waitcnt vmcnt(8)
TR a[36:37],v194 offset:24672
TR a[38:39],v194 offset:24928
MM v[76:79],a[44:47],a[140:143],0
RD a[172:175],v218 offset:32768
v_readfirstlane_b32 s69, v169
MM v[76:79],a[40:43],a[136:139],v[76:79]
RD a[176:179],v222 offset:36864
v_lshlrev_b32 v173, 4, v181
MM v[84:87],a[40:43],a[144:147],0
s_add_u32 s69, s69, 1
RD a[180:183],v223 offset:36864
v_mov_b32 v179, s69
MM v[84:87],a[44:47],a[148:151],v[84:87]
RD a[184:187],v217 offset:36864
v_lshlrev_b32 v214, 8, v211
MM v[92:95],a[44:47],a[156:159],0
RD a[188:191],v218 offset:36864
v_add_u32 v214, %[slds], v214
MM v[92:95],a[40:43],a[152:155],v[92:95]
s_add_u32 m0, s94, 4096
s_add_u32 s95, s88, 40960
s_mov_b64 exec, s[54:55]
buffer_store_dwordx2 v[178:179], v173, s[20:23], s97 offen offset:0 sc1
s_mov_b64 exec, -1
v_xor_b32 v215, v211, v212
MM v[96:99],a[48:51],a[128:131],0
TR a[40:41],v194 offset:16456
TR a[42:43],v194 offset:16712
MM v[96:99],a[52:55],a[132:135],v[96:99]
buffer_load_dwordx4 v220, s[32:35], s95 offen offset:0 lds
MM v[104:107],a[52:55],a[140:143],0
s_add_u32 s95, s88, 50176
TR a[44:45],v194 offset:24680
TR a[46:47],v194 offset:24936
MM v[104:107],a[48:51],a[136:139],v[104:107]
buffer_load_dwordx4 v221, s[32:35], s95 offen offset:1024 lds
MM v[112:115],a[48:51],a[144:147],0
s_add_u32 s95, s88, 59392
s_lshl_b32 s77, %[swave], 2
v_xor_b32 v213, s77, v215
MM v[112:115],a[52:55],a[148:151],v[112:115]
v_xor_b32 v216, 0, v213
v_lshl_add_u32 v224, v216, 4, v214
MM v[120:123],a[52:55],a[156:159],0
buffer_load_dwordx4 v220, s[32:35], s95 offen offset:2048 lds
MM v[120:123],a[48:51],a[152:155],v[120:123]
s_waitcnt lgkmcnt(15)
s_add_u32 s95, s88, 68608
v_xor_b32 v216, 4, v213
MM v[100:103],a[56:59],a[128:131],0
TR a[48:49],v195 offset:16448
TR a[50:51],v195 offset:16704
MM v[100:103],a[60:63],a[132:135],v[100:103]
TR a[52:53],v195 offset:24672
TR a[54:55],v195 offset:24928
MM v[108:111],a[60:63],a[140:143],0
buffer_load_dwordx4 v221, s[32:35], s95 offen offset:3072 lds
MM v[108:111],a[56:59],a[136:139],v[108:111]
s_add_u32 s87, s89, s85
s_add_u32 s85, s85, 524288
v_lshl_add_u32 v225, v216, 4, v214
MM v[116:119],a[56:59],a[144:147],0
s_cmp_ge_u32 s85, 5242880
s_cselect_b32 s85, 0, s85
s_add_u32 m0, s96, 114880
MM v[116:119],a[60:63],a[148:151],v[116:119]
v_xor_b32 v216, 8, v213
v_lshl_add_u32 v226, v216, 4, v214
MM v[124:127],a[60:63],a[156:159],0
buffer_load_dwordx4 v253, s[16:19], s87 offen offset:0 lds
MM v[124:127],a[56:59],a[152:155],v[124:127]
s_add_u32 m0, s96, 115904
v_xor_b32 v216, 12, v213
v_lshl_add_u32 v227, v216, 4, v214
s_waitcnt vmcnt(10) lgkmcnt(8)
s_barrier
MM v[0:3],a[0:3],a[160:163],v[0:3]
TR a[56:57],v195 offset:16456
TR a[58:59],v195 offset:16712
MM v[0:3],a[4:7],a[164:167],v[0:3]
TR a[60:61],v195 offset:24680
TR a[62:63],v195 offset:24936
MM v[8:11],a[4:7],a[172:175],v[8:11]
buffer_load_dwordx4 v254, s[16:19], s87 offen offset:0 lds
MM v[8:11],a[0:3],a[168:171],v[8:11]
s_add_u32 s87, s87, 65536
s_add_u32 m0, s96, 116936
v_lshlrev_b32 v214, 8, v212
MM v[16:19],a[0:3],a[176:179],v[16:19]
v_xor_b32 v216, 0, v215
v_lshl_add_u32 v228, v216, 4, v214
MM v[16:19],a[4:7],a[180:183],v[16:19]
v_xor_b32 v216, 4, v215
v_lshl_add_u32 v229, v216, 4, v214
MM v[24:27],a[4:7],a[188:191],v[24:27]
buffer_load_dwordx4 v253, s[16:19], s87 offen offset:0 lds
MM v[24:27],a[0:3],a[184:187],v[24:27]
s_add_u32 m0, s96, 117960
v_xor_b32 v216, 8, v215
v_lshl_add_u32 v230, v216, 4, v214
MM v[4:7],a[8:11],a[160:163],v[4:7]
v_xor_b32 v216, 12, v215
v_lshl_add_u32 v231, v216, 4, v214
MM v[4:7],a[12:15],a[164:167],v[4:7]
s_lshl_b32 s90, s49, 19
s_add_u32 s8, s8, s90
s_addc_u32 s9, s9, 0
MM v[12:15],a[12:15],a[172:175],v[12:15]
buffer_load_dwordx4 v254, s[16:19], s87 offen offset:0 lds
MM v[12:15],a[8:11],a[168:171],v[12:15]
s_mov_b32 s10, 524288
s_mov_b32 s11, 131072
MM v[20:23],a[8:11],a[176:179],v[20:23]
s_add_u32 s12, s12, s90
s_addc_u32 s13, s13, 0
s_mov_b32 s14, 525312
MM v[20:23],a[12:15],a[180:183],v[20:23]
s_add_u32 s88, s68, s86
s_add_u32 s86, s86, 128
MM v[28:31],a[12:15],a[188:191],v[28:31]
s_mov_b32 s15, 131072
s_cmp_ge_u32 s86, 1280
s_cselect_b32 s86, 0, s86
MM v[28:31],a[8:11],a[184:187],v[28:31]
s_add_u32 m0, s94, 32768
s_mov_b64 exec, s[52:53]
buffer_store_dwordx4 v[128:131], v168, s[20:23], s82 offen offset:0 sc1
s_mov_b64 exec, -1
v_smfmac_f32_16x16x64_bf16 v[184:187], v[236:239], a[0:7], v234
s_add_u32 s95, s88, 0
v_smfmac_f32_16x16x64_bf16 v[188:191], v[236:239], a[8:15], v234
s_sub_u32 s12, s12, 1024
s_subb_u32 s13, s13, 0
MM v[32:35],a[16:19],a[160:163],v[32:35]
TR a[0:1],v192 offset:32896
TR a[2:3],v192 offset:33152
MM v[32:35],a[20:23],a[164:167],v[32:35]
buffer_load_dwordx4 v220, s[32:35], s95 offen offset:0 lds
MM v[40:43],a[20:23],a[172:175],v[40:43]
s_add_u32 s95, s88, 9216
TR a[4:5],v192 offset:41120
TR a[6:7],v192 offset:41376
MM v[40:43],a[16:19],a[168:171],v[40:43]
s_lshr_b32 s90, s49, 2
TR a[8:9],v192 offset:32904
TR a[10:11],v192 offset:33160
MM v[48:51],a[16:19],a[176:179],v[48:51]
TR a[12:13],v192 offset:41128
TR a[14:15],v192 offset:41384
MM v[48:51],a[20:23],a[180:183],v[48:51]
buffer_load_dwordx4 v221, s[32:35], s95 offen offset:1024 lds
MM v[56:59],a[20:23],a[188:191],v[56:59]
s_add_u32 s95, s88, 18432
s_mul_i32 s90, s90, 655360
s_mul_i32 s77, s84, 320
MM v[56:59],a[16:19],a[184:187],v[56:59]
s_add_u32 s90, s90, s77
s_add_u32 s24, s24, s90
s_addc_u32 s25, s25, 0
MM v[36:39],a[24:27],a[160:163],v[36:39]
TR a[16:17],v193 offset:32896
TR a[18:19],v193 offset:33152
MM v[36:39],a[28:31],a[164:167],v[36:39]
buffer_load_dwordx4 v220, s[32:35], s95 offen offset:2048 lds
MM v[44:47],a[28:31],a[172:175],v[44:47]
s_add_u32 s95, s88, 27648
TR a[20:21],v193 offset:41120
TR a[22:23],v193 offset:41376
MM v[44:47],a[24:27],a[168:171],v[44:47]
s_sub_u32 s26, 5242880, s90
s_mov_b32 s27, 131072
s_sub_u32 s77, %[sb], 76
MM v[52:55],a[24:27],a[176:179],v[52:55]
buffer_load_dwordx4 v221, s[32:35], s95 offen offset:3072 lds
MM v[52:55],a[28:31],a[180:183],v[52:55]
s_add_u32 m0, s94, 36864
s_add_u32 s95, s88, 40960
s_min_u32 s47, s77, %[sb]
MM v[60:63],a[28:31],a[188:191],v[60:63]
s_mul_i32 s77, s47, 205
s_lshr_b32 s46, s77, 11
s_mul_i32 s90, s46, 121600
MM v[60:63],a[24:27],a[184:187],v[60:63]
s_lshl_b32 s77, s47, 7
s_add_u32 s90, s90, s77
MM v[64:67],a[32:35],a[160:163],v[64:67]
TR a[24:25],v193 offset:32904
TR a[26:27],v193 offset:33160
s_mov_b64 exec, s[52:53]
buffer_store_dwordx4 v[132:135], v168, s[20:23], s82 offen offset:256 sc1
s_mov_b64 exec, -1
MM v[64:67],a[36:39],a[164:167],v[64:67]
TR a[28:29],v193 offset:41128
TR a[30:31],v193 offset:41384
MM v[72:75],a[36:39],a[172:175],v[72:75]
s_add_u32 s28, s28, s90
s_addc_u32 s29, s29, 0
s_sub_u32 s30, 2211840, s90
MM v[72:75],a[32:35],a[168:171],v[72:75]
s_mov_b32 s31, 131072
s_mov_b32 s38, 2211840
s_mov_b32 s39, 131072
MM v[80:83],a[32:35],a[176:179],v[80:83]
buffer_load_dwordx4 v220, s[32:35], s95 offen offset:0 lds
MM v[80:83],a[36:39],a[180:183],v[80:83]
s_add_u32 s95, s88, 50176
s_mov_b32 s42, 2621440
s_mov_b32 s43, 131072
MM v[88:91],a[36:39],a[188:191],v[88:91]
s_lshr_b32 s77, %[sb], 6
s_lshl_b32 s77, s77, 3
s_and_b32 s90, %[sb], 7
MM v[88:91],a[32:35],a[184:187],v[88:91]
s_waitcnt lgkmcnt(15)
s_add_u32 s77, s77, s90
s_sub_u32 s90, s77, 8
MM v[68:71],a[40:43],a[160:163],v[68:71]
TR a[32:33],v194 offset:32896
TR a[34:35],v194 offset:33152
MM v[68:71],a[44:47],a[164:167],v[68:71]
buffer_load_dwordx4 v221, s[32:35], s95 offen offset:1024 lds
MM v[76:79],a[44:47],a[172:175],v[76:79]
s_add_u32 s95, s88, 59392
TR a[36:37],v194 offset:41120
TR a[38:39],v194 offset:41376
MM v[76:79],a[40:43],a[168:171],v[76:79]
s_cmp_ge_u32 s77, 27
s_cselect_b32 s77, s90, s77
s_lshl_b32 s77, s77, 7
MM v[84:87],a[40:43],a[176:179],v[84:87]
buffer_load_dwordx4 v220, s[32:35], s95 offen offset:2048 lds
MM v[84:87],a[44:47],a[180:183],v[84:87]
s_add_u32 s95, s88, 68608
s_lshl_b32 s90, %[sb], 1
s_and_b32 s90, s90, 112
MM v[92:95],a[44:47],a[188:191],v[92:95]
s_add_u32 s48, s77, s90
s_mov_b32 s71, 0x4e800000
s_mov_b32 s72, 0x3e0293ee
MM v[92:95],a[40:43],a[184:187],v[92:95]
s_lshl_b32 s64, %[swave], 12
MM v[96:99],a[48:51],a[160:163],v[96:99]
TR a[40:41],v194 offset:32904
TR a[42:43],v194 offset:33160
MM v[96:99],a[52:55],a[164:167],v[96:99]
buffer_load_dwordx4 v221, s[32:35], s95 offen offset:3072 lds
MM v[104:107],a[52:55],a[172:175],v[104:107]
s_add_u32 s87, s89, s85
TR a[44:45],v194 offset:41128
TR a[46:47],v194 offset:41384
MM v[104:107],a[48:51],a[168:171],v[104:107]
s_add_u32 s85, s85, 524288
s_cmp_ge_u32 s85, 5242880
s_cselect_b32 s85, 0, s85
MM v[112:115],a[48:51],a[176:179],v[112:115]
s_add_u32 m0, s96, 131328
s_mov_b64 exec, s[52:53]
buffer_store_dwordx4 v[136:139], v168, s[20:23], s82 offen offset:512 sc1
s_mov_b64 exec, -1
MM v[112:115],a[52:55],a[180:183],v[112:115]
MM v[120:123],a[52:55],a[188:191],v[120:123]
MM v[120:123],a[48:51],a[184:187],v[120:123]
MM v[100:103],a[56:59],a[160:163],v[100:103]
TR a[48:49],v195 offset:32896
TR a[50:51],v195 offset:33152
MM v[100:103],a[60:63],a[164:167],v[100:103]
buffer_load_dwordx4 v253, s[16:19], s87 offen offset:0 lds
MM v[108:111],a[60:63],a[172:175],v[108:111]
s_add_u32 m0, s96, 132352
TR a[52:53],v195 offset:41120
TR a[54:55],v195 offset:41376
MM v[108:111],a[56:59],a[168:171],v[108:111]
MM v[116:119],a[56:59],a[176:179],v[116:119]
buffer_load_dwordx4 v254, s[16:19], s87 offen offset:0 lds
MM v[116:119],a[60:63],a[180:183],v[116:119]
s_add_u32 s87, s87, 65536
s_add_u32 m0, s96, 133384
MM v[124:127],a[60:63],a[188:191],v[124:127]
MM v[124:127],a[56:59],a[184:187],v[124:127]
buffer_load_dwordx4 v253, s[16:19], s87 offen offset:0 lds
s_add_u32 m0, s96, 134408
s_nop 0
buffer_load_dwordx4 v254, s[16:19], s87 offen offset:0 lds
s_waitcnt vmcnt(19)
RD a[192:195],v222 offset:0
RD a[196:199],v223 offset:0
RD a[200:203],v217 offset:0
RD a[204:207],v218 offset:0
RD a[208:211],v222 offset:4096
RD a[212:215],v223 offset:4096
RD a[216:219],v217 offset:4096
RD a[220:223],v218 offset:4096
s_waitcnt vmcnt(15) lgkmcnt(0)
s_barrier
MM v[0:3],a[0:3],a[192:195],v[0:3]
TR a[56:57],v195 offset:32904
TR a[58:59],v195 offset:33160
MM v[0:3],a[4:7],a[196:199],v[0:3]
TR a[60:61],v195 offset:41128
TR a[62:63],v195 offset:41384
MM v[8:11],a[4:7],a[204:207],v[8:11]
s_mov_b64 exec, s[52:53]
)ASM"
R"ASM(
buffer_store_dwordx4 v[140:143], v168, s[20:23], s82 offen offset:768 sc1
s_mov_b64 exec, -1
MM v[8:11],a[0:3],a[200:203],v[8:11]
s_add_u32 s88, s68, s86
s_add_u32 s86, s86, 128
MM v[16:19],a[0:3],a[208:211],v[16:19]
s_cmp_ge_u32 s86, 1280
s_cselect_b32 s86, 0, s86
s_add_u32 m0, s94, 0
MM v[16:19],a[4:7],a[212:215],v[16:19]
s_add_u32 s95, s88, 0
MM v[24:27],a[4:7],a[220:223],v[24:27]
buffer_load_dwordx4 v220, s[32:35], s95 offen offset:0 lds
MM v[24:27],a[0:3],a[216:219],v[24:27]
s_add_u32 s95, s88, 9216
MM v[4:7],a[8:11],a[192:195],v[4:7]
buffer_load_dwordx4 v221, s[32:35], s95 offen offset:1024 lds
MM v[4:7],a[12:15],a[196:199],v[4:7]
s_add_u32 s95, s88, 18432
MM v[12:15],a[12:15],a[204:207],v[12:15]
MM v[12:15],a[8:11],a[200:203],v[12:15]
MM v[20:23],a[8:11],a[208:211],v[20:23]
buffer_load_dwordx4 v220, s[32:35], s95 offen offset:2048 lds
MM v[20:23],a[12:15],a[212:215],v[20:23]
s_add_u32 s95, s88, 27648
MM v[28:31],a[12:15],a[220:223],v[28:31]
MM v[28:31],a[8:11],a[216:219],v[28:31]
s_nop 0
v_smfmac_f32_16x16x64_bf16 v[184:187], v[236:239], a[0:7], v234
s_nop 0
v_smfmac_f32_16x16x64_bf16 v[188:191], v[236:239], a[8:15], v234
s_nop 0
MM v[32:35],a[16:19],a[192:195],v[32:35]
buffer_load_dwordx4 v221, s[32:35], s95 offen offset:3072 lds
MM v[32:35],a[20:23],a[196:199],v[32:35]
s_add_u32 m0, s94, 4096
TR a[0:1],v196 offset:0
TR a[2:3],v196 offset:256
MM v[40:43],a[20:23],a[204:207],v[40:43]
s_add_u32 s95, s88, 40960
TR a[4:5],v196 offset:8224
TR a[6:7],v196 offset:8480
s_mov_b64 exec, s[52:53]
buffer_store_dwordx4 v[144:147], v168, s[20:23], s82 offen offset:1024 sc1
s_mov_b64 exec, -1
MM v[40:43],a[16:19],a[200:203],v[40:43]
s_nop 0
TR a[8:9],v196 offset:8
TR a[10:11],v196 offset:264
MM v[48:51],a[16:19],a[208:211],v[48:51]
s_nop 0
TR a[12:13],v196 offset:8232
TR a[14:15],v196 offset:8488
MM v[48:51],a[20:23],a[212:215],v[48:51]
s_nop 0
MM v[56:59],a[20:23],a[220:223],v[56:59]
buffer_load_dwordx4 v220, s[32:35], s95 offen offset:0 lds
MM v[56:59],a[16:19],a[216:219],v[56:59]
s_add_u32 s95, s88, 50176
MM v[36:39],a[24:27],a[192:195],v[36:39]
s_nop 0
TR a[16:17],v197 offset:0
TR a[18:19],v197 offset:256
MM v[36:39],a[28:31],a[196:199],v[36:39]
s_nop 0
TR a[20:21],v197 offset:8224
TR a[22:23],v197 offset:8480
MM v[44:47],a[28:31],a[204:207],v[44:47]
buffer_load_dwordx4 v221, s[32:35], s95 offen offset:1024 lds
MM v[44:47],a[24:27],a[200:203],v[44:47]
s_add_u32 s95, s88, 59392
v_lshlrev_b32 v173, 2, v181
MM v[52:55],a[24:27],a[208:211],v[52:55]
s_mov_b64 exec, s[54:55]
buffer_store_dword v160, v173, s[20:23], s78 offen offset:0 sc1
s_mov_b64 exec, -1
MM v[52:55],a[28:31],a[212:215],v[52:55]
s_nop 0
v_mov_b32 v170, s69
v_mov_b32 v171, s69
MM v[60:63],a[28:31],a[220:223],v[60:63]
buffer_load_dwordx4 v220, s[32:35], s95 offen offset:2048 lds
MM v[60:63],a[24:27],a[216:219],v[60:63]
s_add_u32 s95, s88, 68608
MM v[64:67],a[32:35],a[192:195],v[64:67]
s_nop 0
TR a[24:25],v197 offset:8
TR a[26:27],v197 offset:264
MM v[64:67],a[36:39],a[196:199],v[64:67]
s_nop 0
TR a[28:29],v197 offset:8232
TR a[30:31],v197 offset:8488
MM v[72:75],a[36:39],a[204:207],v[72:75]
buffer_load_dwordx4 v221, s[32:35], s95 offen offset:3072 lds
MM v[72:75],a[32:35],a[200:203],v[72:75]
s_add_u32 s87, s89, s85
s_add_u32 s85, s85, 524288
MM v[80:83],a[32:35],a[208:211],v[80:83]
s_cmp_ge_u32 s85, 5242880
s_cselect_b32 s85, 0, s85
s_add_u32 m0, s96, 65536
MM v[80:83],a[36:39],a[212:215],v[80:83]
LPG2_%=:
s_cbranch_execz LPG6_%=
MM v[88:91],a[36:39],a[220:223],v[88:91]
buffer_load_dwordx4 v253, s[16:19], s87 offen offset:0 lds
MM v[88:91],a[32:35],a[216:219],v[88:91]
s_add_u32 m0, s96, 66560
MM v[68:71],a[40:43],a[192:195],v[68:71]
TR a[32:33],v198 offset:0
TR a[34:35],v198 offset:256
MM v[68:71],a[44:47],a[196:199],v[68:71]
TR a[36:37],v198 offset:8224
TR a[38:39],v198 offset:8480
MM v[76:79],a[44:47],a[204:207],v[76:79]
buffer_load_dwordx4 v254, s[16:19], s87 offen offset:0 lds
MM v[76:79],a[40:43],a[200:203],v[76:79]
s_add_u32 s87, s87, 65536
s_add_u32 m0, s96, 67592
MM v[84:87],a[40:43],a[208:211],v[84:87]
MM v[84:87],a[44:47],a[212:215],v[84:87]
MM v[92:95],a[44:47],a[220:223],v[92:95]
buffer_load_dwordx4 v253, s[16:19], s87 offen offset:0 lds
MM v[92:95],a[40:43],a[216:219],v[92:95]
s_add_u32 m0, s96, 68616
MM v[96:99],a[48:51],a[192:195],v[96:99]
TR a[40:41],v198 offset:8
TR a[42:43],v198 offset:264
MM v[96:99],a[52:55],a[196:199],v[96:99]
TR a[44:45],v198 offset:8232
TR a[46:47],v198 offset:8488
MM v[104:107],a[52:55],a[204:207],v[104:107]
buffer_load_dwordx4 v254, s[16:19], s87 offen offset:0 lds
MM v[104:107],a[48:51],a[200:203],v[104:107]
s_waitcnt vmcnt(20)
RD a[224:227],v222 offset:32768
MM v[112:115],a[48:51],a[208:211],v[112:115]
RD a[228:231],v223 offset:32768
MM v[112:115],a[52:55],a[212:215],v[112:115]
RD a[232:235],v217 offset:32768
MM v[120:123],a[52:55],a[220:223],v[120:123]
RD a[236:239],v218 offset:32768
MM v[120:123],a[48:51],a[216:219],v[120:123]
s_waitcnt lgkmcnt(15)
RD a[240:243],v222 offset:36864
MM v[100:103],a[56:59],a[192:195],v[100:103]
TR a[48:49],v199 offset:0
TR a[50:51],v199 offset:256
MM v[100:103],a[60:63],a[196:199],v[100:103]
TR a[52:53],v199 offset:8224
TR a[54:55],v199 offset:8480
MM v[108:111],a[60:63],a[204:207],v[108:111]
RD a[244:247],v223 offset:36864
MM v[108:111],a[56:59],a[200:203],v[108:111]
RD a[248:251],v217 offset:36864
MM v[116:119],a[56:59],a[208:211],v[116:119]
RD a[252:255],v218 offset:36864
MM v[116:119],a[60:63],a[212:215],v[116:119]
MM v[124:127],a[60:63],a[220:223],v[124:127]
MM v[124:127],a[56:59],a[216:219],v[124:127]
s_waitcnt vmcnt(15) lgkmcnt(0)
s_barrier
MM v[0:3],a[0:3],a[224:227],v[0:3]
TR a[56:57],v199 offset:8
TR a[58:59],v199 offset:264
MM v[0:3],a[4:7],a[228:231],v[0:3]
s_add_u32 s88, s68, s86
TR a[60:61],v199 offset:8232
TR a[62:63],v199 offset:8488
MM v[8:11],a[4:7],a[236:239],v[8:11]
s_add_u32 s86, s86, 128
s_cmp_ge_u32 s86, 1280
s_cselect_b32 s86, 0, s86
MM v[8:11],a[0:3],a[232:235],v[8:11]
s_add_u32 m0, s94, 32768
s_add_u32 s95, s88, 0
MM v[16:19],a[0:3],a[240:243],v[16:19]
buffer_load_dwordx4 v220, s[32:35], s95 offen offset:0 lds
MM v[16:19],a[4:7],a[244:247],v[16:19]
s_add_u32 s95, s88, 9216
MM v[24:27],a[4:7],a[252:255],v[24:27]
buffer_load_dwordx4 v221, s[32:35], s95 offen offset:1024 lds
MM v[24:27],a[0:3],a[248:251],v[24:27]
s_add_u32 s95, s88, 18432
MM v[4:7],a[8:11],a[224:227],v[4:7]
MM v[4:7],a[12:15],a[228:231],v[4:7]
buffer_load_dwordx4 v220, s[32:35], s95 offen offset:2048 lds
MM v[12:15],a[12:15],a[236:239],v[12:15]
s_add_u32 s95, s88, 27648
MM v[12:15],a[8:11],a[232:235],v[12:15]
MM v[20:23],a[8:11],a[240:243],v[20:23]
MM v[20:23],a[12:15],a[244:247],v[20:23]
buffer_load_dwordx4 v221, s[32:35], s95 offen offset:3072 lds
MM v[28:31],a[12:15],a[252:255],v[28:31]
s_add_u32 m0, s94, 36864
s_add_u32 s95, s88, 40960
MM v[28:31],a[8:11],a[248:251],v[28:31]
v_smfmac_f32_16x16x64_bf16 v[184:187], v[236:239], a[0:7], v234
v_smfmac_f32_16x16x64_bf16 v[188:191], v[236:239], a[8:15], v234
MM v[32:35],a[16:19],a[224:227],v[32:35]
buffer_load_dwordx4 v220, s[32:35], s95 offen offset:0 lds
MM v[32:35],a[20:23],a[228:231],v[32:35]
s_add_u32 s95, s88, 50176
TR a[0:1],v196 offset:16448
TR a[2:3],v196 offset:16704
MM v[40:43],a[20:23],a[236:239],v[40:43]
TR a[4:5],v196 offset:24672
TR a[6:7],v196 offset:24928
MM v[40:43],a[16:19],a[232:235],v[40:43]
TR a[8:9],v196 offset:16456
TR a[10:11],v196 offset:16712
MM v[48:51],a[16:19],a[240:243],v[48:51]
buffer_load_dwordx4 v221, s[32:35], s95 offen offset:1024 lds
MM v[48:51],a[20:23],a[244:247],v[48:51]
s_add_u32 s95, s88, 59392
TR a[12:13],v196 offset:24680
TR a[14:15],v196 offset:24936
MM v[56:59],a[20:23],a[252:255],v[56:59]
MM v[56:59],a[16:19],a[248:251],v[56:59]
buffer_load_dwordx4 v220, s[32:35], s95 offen offset:2048 lds
MM v[36:39],a[24:27],a[224:227],v[36:39]
s_add_u32 s95, s88, 68608
TR a[16:17],v197 offset:16448
TR a[18:19],v197 offset:16704
MM v[36:39],a[28:31],a[228:231],v[36:39]
TR a[20:21],v197 offset:24672
TR a[22:23],v197 offset:24928
MM v[44:47],a[28:31],a[236:239],v[44:47]
MM v[44:47],a[24:27],a[232:235],v[44:47]
buffer_load_dwordx4 v221, s[32:35], s95 offen offset:3072 lds
MM v[52:55],a[24:27],a[240:243],v[52:55]
s_add_u32 s87, s89, s85
s_add_u32 s85, s85, 524288
MM v[52:55],a[28:31],a[244:247],v[52:55]
s_cmp_ge_u32 s85, 5242880
s_cselect_b32 s85, 0, s85
s_add_u32 m0, s96, 81984
MM v[60:63],a[28:31],a[252:255],v[60:63]
MM v[60:63],a[24:27],a[248:251],v[60:63]
buffer_load_dwordx4 v253, s[16:19], s87 offen offset:0 lds
MM v[64:67],a[32:35],a[224:227],v[64:67]
s_add_u32 m0, s96, 83008
TR a[24:25],v197 offset:16456
TR a[26:27],v197 offset:16712
MM v[64:67],a[36:39],a[228:231],v[64:67]
TR a[28:29],v197 offset:24680
TR a[30:31],v197 offset:24936
MM v[72:75],a[36:39],a[236:239],v[72:75]
MM v[72:75],a[32:35],a[232:235],v[72:75]
buffer_load_dwordx4 v254, s[16:19], s87 offen offset:0 lds
MM v[80:83],a[32:35],a[240:243],v[80:83]
s_add_u32 s87, s87, 65536
s_add_u32 m0, s96, 84040
MM v[80:83],a[36:39],a[244:247],v[80:83]
MM v[88:91],a[36:39],a[252:255],v[88:91]
MM v[88:91],a[32:35],a[248:251],v[88:91]
buffer_load_dwordx4 v253, s[16:19], s87 offen offset:0 lds
MM v[68:71],a[40:43],a[224:227],v[68:71]
s_add_u32 m0, s96, 85064
TR a[32:33],v198 offset:16448
TR a[34:35],v198 offset:16704
MM v[68:71],a[44:47],a[228:231],v[68:71]
TR a[36:37],v198 offset:24672
TR a[38:39],v198 offset:24928
MM v[76:79],a[44:47],a[236:239],v[76:79]
MM v[76:79],a[40:43],a[232:235],v[76:79]
buffer_load_dwordx4 v254, s[16:19], s87 offen offset:0 lds
MM v[84:87],a[40:43],a[240:243],v[84:87]
s_waitcnt vmcnt(16)
RD a[128:131],v222 offset:0
MM v[84:87],a[44:47],a[244:247],v[84:87]
RD a[132:135],v223 offset:0
MM v[92:95],a[44:47],a[252:255],v[92:95]
RD a[136:139],v217 offset:0
MM v[92:95],a[40:43],a[248:251],v[92:95]
RD a[140:143],v218 offset:0
MM v[96:99],a[48:51],a[224:227],v[96:99]
TR a[40:41],v198 offset:16456
TR a[42:43],v198 offset:16712
MM v[96:99],a[52:55],a[228:231],v[96:99]
TR a[44:45],v198 offset:24680
TR a[46:47],v198 offset:24936
MM v[104:107],a[52:55],a[236:239],v[104:107]
RD a[144:147],v222 offset:4096
MM v[104:107],a[48:51],a[232:235],v[104:107]
RD a[148:151],v223 offset:4096
MM v[112:115],a[48:51],a[240:243],v[112:115]
RD a[152:155],v217 offset:4096
MM v[112:115],a[52:55],a[244:247],v[112:115]
RD a[156:159],v218 offset:4096
MM v[120:123],a[52:55],a[252:255],v[120:123]
MM v[120:123],a[48:51],a[248:251],v[120:123]
s_waitcnt lgkmcnt(15)
MM v[100:103],a[56:59],a[224:227],v[100:103]
TR a[48:49],v199 offset:16448
TR a[50:51],v199 offset:16704
MM v[100:103],a[60:63],a[228:231],v[100:103]
s_waitcnt lgkmcnt(2)
TR a[52:53],v199 offset:24672
TR a[54:55],v199 offset:24928
MM v[108:111],a[60:63],a[236:239],v[108:111]
s_add_u32 s88, s68, s86
s_add_u32 s86, s86, 128
MM v[108:111],a[56:59],a[232:235],v[108:111]
s_cmp_ge_u32 s86, 1280
s_cselect_b32 s86, 0, s86
s_add_u32 m0, s94, 0
MM v[116:119],a[56:59],a[240:243],v[116:119]
s_add_u32 s95, s88, 0
MM v[116:119],a[60:63],a[244:247],v[116:119]
buffer_load_dwordx4 v220, s[32:35], s95 offen offset:0 lds
MM v[124:127],a[60:63],a[252:255],v[124:127]
s_add_u32 s95, s88, 9216
MM v[124:127],a[56:59],a[248:251],v[124:127]
buffer_load_dwordx4 v221, s[32:35], s95 offen offset:1024 lds
s_waitcnt vmcnt(14)
s_barrier
MM v[0:3],a[0:3],a[128:131],v[0:3]
s_add_u32 s95, s88, 18432
TR a[56:57],v199 offset:16456
TR a[58:59],v199 offset:16712
MM v[0:3],a[4:7],a[132:135],v[0:3]
TR a[60:61],v199 offset:24680
TR a[62:63],v199 offset:24936
MM v[8:11],a[4:7],a[140:143],v[8:11]
buffer_load_dwordx4 v220, s[32:35], s95 offen offset:2048 lds
MM v[8:11],a[0:3],a[136:139],v[8:11]
s_add_u32 s95, s88, 27648
MM v[16:19],a[0:3],a[144:147],v[16:19]
MM v[16:19],a[4:7],a[148:151],v[16:19]
MM v[24:27],a[4:7],a[156:159],v[24:27]
buffer_load_dwordx4 v221, s[32:35], s95 offen offset:3072 lds
MM v[24:27],a[0:3],a[152:155],v[24:27]
s_add_u32 m0, s94, 4096
s_add_u32 s95, s88, 40960
MM v[4:7],a[8:11],a[128:131],v[4:7]
MM v[4:7],a[12:15],a[132:135],v[4:7]
MM v[12:15],a[12:15],a[140:143],v[12:15]
buffer_load_dwordx4 v220, s[32:35], s95 offen offset:0 lds
)ASM"
R"ASM(
MM v[12:15],a[8:11],a[136:139],v[12:15]
s_add_u32 s95, s88, 50176
MM v[20:23],a[8:11],a[144:147],v[20:23]
MM v[20:23],a[12:15],a[148:151],v[20:23]
MM v[28:31],a[12:15],a[156:159],v[28:31]
buffer_load_dwordx4 v221, s[32:35], s95 offen offset:1024 lds
MM v[28:31],a[8:11],a[152:155],v[28:31]
s_add_u32 s95, s88, 59392
v_smfmac_f32_16x16x64_bf16 v[184:187], v[236:239], a[0:7], v234
v_smfmac_f32_16x16x64_bf16 v[188:191], v[236:239], a[8:15], v234
MM v[32:35],a[16:19],a[128:131],v[32:35]
TR a[0:1],v192 offset:0
TR a[2:3],v192 offset:256
MM v[32:35],a[20:23],a[132:135],v[32:35]
buffer_load_dwordx4 v220, s[32:35], s95 offen offset:2048 lds
MM v[40:43],a[20:23],a[140:143],v[40:43]
s_add_u32 s95, s88, 68608
TR a[4:5],v192 offset:8224
TR a[6:7],v192 offset:8480
MM v[40:43],a[16:19],a[136:139],v[40:43]
TR a[8:9],v192 offset:8
TR a[10:11],v192 offset:264
MM v[48:51],a[16:19],a[144:147],v[48:51]
TR a[12:13],v192 offset:8232
TR a[14:15],v192 offset:8488
MM v[48:51],a[20:23],a[148:151],v[48:51]
buffer_load_dwordx4 v221, s[32:35], s95 offen offset:3072 lds
MM v[56:59],a[20:23],a[156:159],v[56:59]
s_add_u32 s87, s89, s85
s_add_u32 s85, s85, 524288
MM v[56:59],a[16:19],a[152:155],v[56:59]
s_cmp_ge_u32 s85, 5242880
s_cselect_b32 s85, 0, s85
s_add_u32 m0, s96, 98432
MM v[36:39],a[24:27],a[128:131],v[36:39]
TR a[16:17],v193 offset:0
TR a[18:19],v193 offset:256
MM v[36:39],a[28:31],a[132:135],v[36:39]
buffer_load_dwordx4 v253, s[16:19], s87 offen offset:0 lds
MM v[44:47],a[28:31],a[140:143],v[44:47]
s_add_u32 m0, s96, 99456
TR a[20:21],v193 offset:8224
TR a[22:23],v193 offset:8480
MM v[44:47],a[24:27],a[136:139],v[44:47]
MM v[52:55],a[24:27],a[144:147],v[52:55]
buffer_load_dwordx4 v254, s[16:19], s87 offen offset:0 lds
MM v[52:55],a[28:31],a[148:151],v[52:55]
s_add_u32 s87, s87, 65536
s_add_u32 m0, s96, 100488
MM v[60:63],a[28:31],a[156:159],v[60:63]
MM v[60:63],a[24:27],a[152:155],v[60:63]
MM v[64:67],a[32:35],a[128:131],v[64:67]
TR a[24:25],v193 offset:8
TR a[26:27],v193 offset:264
MM v[64:67],a[36:39],a[132:135],v[64:67]
buffer_load_dwordx4 v253, s[16:19], s87 offen offset:0 lds
MM v[72:75],a[36:39],a[140:143],v[72:75]
s_add_u32 m0, s96, 101512
TR a[28:29],v193 offset:8232
TR a[30:31],v193 offset:8488
MM v[72:75],a[32:35],a[136:139],v[72:75]
MM v[80:83],a[32:35],a[144:147],v[80:83]
buffer_load_dwordx4 v254, s[16:19], s87 offen offset:0 lds
MM v[80:83],a[36:39],a[148:151],v[80:83]
s_waitcnt vmcnt(16)
RD a[160:163],v222 offset:32768
MM v[88:91],a[36:39],a[156:159],v[88:91]
RD a[164:167],v223 offset:32768
MM v[88:91],a[32:35],a[152:155],v[88:91]
RD a[168:171],v217 offset:32768
MM v[68:71],a[40:43],a[128:131],v[68:71]
TR a[32:33],v194 offset:0
TR a[34:35],v194 offset:256
MM v[68:71],a[44:47],a[132:135],v[68:71]
TR a[36:37],v194 offset:8224
TR a[38:39],v194 offset:8480
MM v[76:79],a[44:47],a[140:143],v[76:79]
RD a[172:175],v218 offset:32768
MM v[76:79],a[40:43],a[136:139],v[76:79]
RD a[176:179],v222 offset:36864
MM v[84:87],a[40:43],a[144:147],v[84:87]
RD a[180:183],v223 offset:36864
MM v[84:87],a[44:47],a[148:151],v[84:87]
RD a[184:187],v217 offset:36864
MM v[92:95],a[44:47],a[156:159],v[92:95]
RD a[188:191],v218 offset:36864
MM v[92:95],a[40:43],a[152:155],v[92:95]
s_waitcnt lgkmcnt(15)
MM v[96:99],a[48:51],a[128:131],v[96:99]
TR a[40:41],v194 offset:8
TR a[42:43],v194 offset:264
MM v[96:99],a[52:55],a[132:135],v[96:99]
TR a[44:45],v194 offset:8232
TR a[46:47],v194 offset:8488
MM v[104:107],a[52:55],a[140:143],v[104:107]
s_waitcnt lgkmcnt(4)
s_add_u32 s88, s68, s86
s_add_u32 s86, s86, 128
MM v[104:107],a[48:51],a[136:139],v[104:107]
s_cmp_ge_u32 s86, 1280
s_cselect_b32 s86, 0, s86
s_add_u32 m0, s94, 32768
MM v[112:115],a[48:51],a[144:147],v[112:115]
s_add_u32 s95, s88, 0
MM v[112:115],a[52:55],a[148:151],v[112:115]
buffer_load_dwordx4 v220, s[32:35], s95 offen offset:0 lds
MM v[120:123],a[52:55],a[156:159],v[120:123]
s_add_u32 s95, s88, 9216
MM v[120:123],a[48:51],a[152:155],v[120:123]
buffer_load_dwordx4 v221, s[32:35], s95 offen offset:1024 lds
MM v[100:103],a[56:59],a[128:131],v[100:103]
s_add_u32 s95, s88, 18432
TR a[48:49],v195 offset:0
TR a[50:51],v195 offset:256
MM v[100:103],a[60:63],a[132:135],v[100:103]
TR a[52:53],v195 offset:8224
TR a[54:55],v195 offset:8480
MM v[108:111],a[60:63],a[140:143],v[108:111]
buffer_load_dwordx4 v220, s[32:35], s95 offen offset:2048 lds
MM v[108:111],a[56:59],a[136:139],v[108:111]
s_add_u32 s95, s88, 27648
MM v[116:119],a[56:59],a[144:147],v[116:119]
MM v[116:119],a[60:63],a[148:151],v[116:119]
MM v[124:127],a[60:63],a[156:159],v[124:127]
buffer_load_dwordx4 v221, s[32:35], s95 offen offset:3072 lds
MM v[124:127],a[56:59],a[152:155],v[124:127]
s_add_u32 m0, s94, 36864
s_add_u32 s95, s88, 40960
s_waitcnt vmcnt(16)
s_barrier
MM v[0:3],a[0:3],a[160:163],v[0:3]
TR a[56:57],v195 offset:8
TR a[58:59],v195 offset:264
MM v[0:3],a[4:7],a[164:167],v[0:3]
TR a[60:61],v195 offset:8232
TR a[62:63],v195 offset:8488
MM v[8:11],a[4:7],a[172:175],v[8:11]
buffer_load_dwordx4 v220, s[32:35], s95 offen offset:0 lds
MM v[8:11],a[0:3],a[168:171],v[8:11]
s_add_u32 s95, s88, 50176
MM v[16:19],a[0:3],a[176:179],v[16:19]
MM v[16:19],a[4:7],a[180:183],v[16:19]
MM v[24:27],a[4:7],a[188:191],v[24:27]
buffer_load_dwordx4 v221, s[32:35], s95 offen offset:1024 lds
MM v[24:27],a[0:3],a[184:187],v[24:27]
s_add_u32 s95, s88, 59392
MM v[4:7],a[8:11],a[160:163],v[4:7]
MM v[4:7],a[12:15],a[164:167],v[4:7]
MM v[12:15],a[12:15],a[172:175],v[12:15]
buffer_load_dwordx4 v220, s[32:35], s95 offen offset:2048 lds
MM v[12:15],a[8:11],a[168:171],v[12:15]
s_add_u32 s95, s88, 68608
MM v[20:23],a[8:11],a[176:179],v[20:23]
MM v[20:23],a[12:15],a[180:183],v[20:23]
MM v[28:31],a[12:15],a[188:191],v[28:31]
buffer_load_dwordx4 v221, s[32:35], s95 offen offset:3072 lds
MM v[28:31],a[8:11],a[184:187],v[28:31]
s_add_u32 s87, s89, s85
s_add_u32 s85, s85, 524288
v_smfmac_f32_16x16x64_bf16 v[184:187], v[236:239], a[0:7], v234
s_cmp_ge_u32 s85, 5242880
s_cselect_b32 s85, 0, s85
v_smfmac_f32_16x16x64_bf16 v[188:191], v[236:239], a[8:15], v234
s_add_u32 m0, s96, 114880
MM v[32:35],a[16:19],a[160:163],v[32:35]
TR a[0:1],v192 offset:16448
TR a[2:3],v192 offset:16704
MM v[32:35],a[20:23],a[164:167],v[32:35]
buffer_load_dwordx4 v253, s[16:19], s87 offen offset:0 lds
MM v[40:43],a[20:23],a[172:175],v[40:43]
s_add_u32 m0, s96, 115904
TR a[4:5],v192 offset:24672
TR a[6:7],v192 offset:24928
MM v[40:43],a[16:19],a[168:171],v[40:43]
TR a[8:9],v192 offset:16456
TR a[10:11],v192 offset:16712
MM v[48:51],a[16:19],a[176:179],v[48:51]
TR a[12:13],v192 offset:24680
TR a[14:15],v192 offset:24936
MM v[48:51],a[20:23],a[180:183],v[48:51]
buffer_load_dwordx4 v254, s[16:19], s87 offen offset:0 lds
MM v[56:59],a[20:23],a[188:191],v[56:59]
s_add_u32 s87, s87, 65536
s_add_u32 m0, s96, 116936
MM v[56:59],a[16:19],a[184:187],v[56:59]
MM v[36:39],a[24:27],a[160:163],v[36:39]
TR a[16:17],v193 offset:16448
TR a[18:19],v193 offset:16704
MM v[36:39],a[28:31],a[164:167],v[36:39]
buffer_load_dwordx4 v253, s[16:19], s87 offen offset:0 lds
MM v[44:47],a[28:31],a[172:175],v[44:47]
s_add_u32 m0, s96, 117960
TR a[20:21],v193 offset:24672
TR a[22:23],v193 offset:24928
MM v[44:47],a[24:27],a[168:171],v[44:47]
MM v[52:55],a[24:27],a[176:179],v[52:55]
buffer_load_dwordx4 v254, s[16:19], s87 offen offset:0 lds
MM v[52:55],a[28:31],a[180:183],v[52:55]
s_waitcnt vmcnt(16)
RD a[192:195],v222 offset:0
MM v[60:63],a[28:31],a[188:191],v[60:63]
RD a[196:199],v223 offset:0
MM v[60:63],a[24:27],a[184:187],v[60:63]
RD a[200:203],v217 offset:0
MM v[64:67],a[32:35],a[160:163],v[64:67]
TR a[24:25],v193 offset:16456
TR a[26:27],v193 offset:16712
MM v[64:67],a[36:39],a[164:167],v[64:67]
TR a[28:29],v193 offset:24680
TR a[30:31],v193 offset:24936
MM v[72:75],a[36:39],a[172:175],v[72:75]
RD a[204:207],v218 offset:0
MM v[72:75],a[32:35],a[168:171],v[72:75]
RD a[208:211],v222 offset:4096
MM v[80:83],a[32:35],a[176:179],v[80:83]
RD a[212:215],v223 offset:4096
MM v[80:83],a[36:39],a[180:183],v[80:83]
RD a[216:219],v217 offset:4096
MM v[88:91],a[36:39],a[188:191],v[88:91]
RD a[220:223],v218 offset:4096
MM v[88:91],a[32:35],a[184:187],v[88:91]
s_waitcnt lgkmcnt(15)
MM v[68:71],a[40:43],a[160:163],v[68:71]
TR a[32:33],v194 offset:16448
TR a[34:35],v194 offset:16704
MM v[68:71],a[44:47],a[164:167],v[68:71]
TR a[36:37],v194 offset:24672
TR a[38:39],v194 offset:24928
MM v[76:79],a[44:47],a[172:175],v[76:79]
s_waitcnt lgkmcnt(4)
s_add_u32 s88, s68, s86
s_add_u32 s86, s86, 128
MM v[76:79],a[40:43],a[168:171],v[76:79]
s_cmp_ge_u32 s86, 1280
s_cselect_b32 s86, 0, s86
s_add_u32 m0, s94, 0
MM v[84:87],a[40:43],a[176:179],v[84:87]
s_add_u32 s95, s88, 0
MM v[84:87],a[44:47],a[180:183],v[84:87]
buffer_load_dwordx4 v220, s[32:35], s95 offen offset:0 lds
MM v[92:95],a[44:47],a[188:191],v[92:95]
s_add_u32 s95, s88, 9216
MM v[92:95],a[40:43],a[184:187],v[92:95]
buffer_load_dwordx4 v221, s[32:35], s95 offen offset:1024 lds
MM v[96:99],a[48:51],a[160:163],v[96:99]
s_add_u32 s95, s88, 18432
TR a[40:41],v194 offset:16456
TR a[42:43],v194 offset:16712
MM v[96:99],a[52:55],a[164:167],v[96:99]
TR a[44:45],v194 offset:24680
TR a[46:47],v194 offset:24936
MM v[104:107],a[52:55],a[172:175],v[104:107]
buffer_load_dwordx4 v220, s[32:35], s95 offen offset:2048 lds
MM v[104:107],a[48:51],a[168:171],v[104:107]
s_add_u32 s95, s88, 27648
MM v[112:115],a[48:51],a[176:179],v[112:115]
MM v[112:115],a[52:55],a[180:183],v[112:115]
MM v[120:123],a[52:55],a[188:191],v[120:123]
buffer_load_dwordx4 v221, s[32:35], s95 offen offset:3072 lds
MM v[120:123],a[48:51],a[184:187],v[120:123]
s_add_u32 m0, s94, 4096
s_add_u32 s95, s88, 40960
MM v[100:103],a[56:59],a[160:163],v[100:103]
TR a[48:49],v195 offset:16448
TR a[50:51],v195 offset:16704
MM v[100:103],a[60:63],a[164:167],v[100:103]
TR a[52:53],v195 offset:24672
TR a[54:55],v195 offset:24928
MM v[108:111],a[60:63],a[172:175],v[108:111]
buffer_load_dwordx4 v220, s[32:35], s95 offen offset:0 lds
MM v[108:111],a[56:59],a[168:171],v[108:111]
s_add_u32 s95, s88, 50176
MM v[116:119],a[56:59],a[176:179],v[116:119]
MM v[116:119],a[60:63],a[180:183],v[116:119]
s_nop 0
MM v[124:127],a[60:63],a[188:191],v[124:127]
buffer_load_dwordx4 v221, s[32:35], s95 offen offset:1024 lds
MM v[124:127],a[56:59],a[184:187],v[124:127]
s_add_u32 s95, s88, 59392
s_waitcnt vmcnt(18)
s_barrier
MM v[0:3],a[0:3],a[192:195],v[0:3]
s_nop 0
TR a[56:57],v195 offset:16456
TR a[58:59],v195 offset:16712
MM v[0:3],a[4:7],a[196:199],v[0:3]
s_nop 0
TR a[60:61],v195 offset:24680
TR a[62:63],v195 offset:24936
MM v[8:11],a[4:7],a[204:207],v[8:11]
buffer_load_dwordx4 v220, s[32:35], s95 offen offset:2048 lds
MM v[8:11],a[0:3],a[200:203],v[8:11]
s_add_u32 s95, s88, 68608
v_and_b32 v208, 240, v248
s_add_u32 s93, s93, 36864
MM v[16:19],a[0:3],a[208:211],v[16:19]
s_nop 0
MM v[16:19],a[4:7],a[212:215],v[16:19]
LPG3_%=:
s_cbranch_execz LPG7_%=
MM v[24:27],a[4:7],a[220:223],v[24:27]
buffer_load_dwordx4 v221, s[32:35], s95 offen offset:3072 lds
MM v[24:27],a[0:3],a[216:219],v[24:27]
s_add_u32 s87, s89, s85
s_add_u32 s85, s85, 524288
MM v[4:7],a[8:11],a[192:195],v[4:7]
s_cmp_ge_u32 s85, 5242880
s_cselect_b32 s85, 0, s85
s_add_u32 m0, s96, 131328
MM v[4:7],a[12:15],a[196:199],v[4:7]
MM v[12:15],a[12:15],a[204:207],v[12:15]
buffer_load_dwordx4 v253, s[16:19], s87 offen offset:0 lds
MM v[12:15],a[8:11],a[200:203],v[12:15]
s_add_u32 m0, s96, 132352
MM v[20:23],a[8:11],a[208:211],v[20:23]
MM v[20:23],a[12:15],a[212:215],v[20:23]
MM v[28:31],a[12:15],a[220:223],v[28:31]
buffer_load_dwordx4 v254, s[16:19], s87 offen offset:0 lds
MM v[28:31],a[8:11],a[216:219],v[28:31]
s_add_u32 s87, s87, 65536
s_add_u32 m0, s96, 133384
v_smfmac_f32_16x16x64_bf16 v[184:187], v[236:239], a[0:7], v234
v_smfmac_f32_16x16x64_bf16 v[188:191], v[236:239], a[8:15], v234
MM v[32:35],a[16:19],a[192:195],v[32:35]
TR a[0:1],v192 offset:32896
TR a[2:3],v192 offset:33152
MM v[32:35],a[20:23],a[196:199],v[32:35]
buffer_load_dwordx4 v253, s[16:19], s87 offen offset:0 lds
MM v[40:43],a[20:23],a[204:207],v[40:43]
s_add_u32 m0, s96, 134408
TR a[4:5],v192 offset:41120
TR a[6:7],v192 offset:41376
)ASM"
R"ASM(
MM v[40:43],a[16:19],a[200:203],v[40:43]
TR a[8:9],v192 offset:32904
TR a[10:11],v192 offset:33160
MM v[48:51],a[16:19],a[208:211],v[48:51]
buffer_load_dwordx4 v[200:203], v208, s[20:23], s93 offen offset:0 sc1
MM v[48:51],a[20:23],a[212:215],v[48:51]
TR a[12:13],v192 offset:41128
TR a[14:15],v192 offset:41384
MM v[56:59],a[20:23],a[220:223],v[56:59]
MM v[56:59],a[16:19],a[216:219],v[56:59]
MM v[36:39],a[24:27],a[192:195],v[36:39]
TR a[16:17],v193 offset:32896
TR a[18:19],v193 offset:33152
MM v[36:39],a[28:31],a[196:199],v[36:39]
buffer_load_dwordx4 v254, s[16:19], s87 offen offset:0 lds
MM v[44:47],a[28:31],a[204:207],v[44:47]
s_waitcnt vmcnt(17)
TR a[20:21],v193 offset:41120
TR a[22:23],v193 offset:41376
MM v[44:47],a[24:27],a[200:203],v[44:47]
RD a[224:227],v222 offset:32768
MM v[52:55],a[24:27],a[208:211],v[52:55]
buffer_load_dwordx4 v[204:207], v208, s[20:23], s93 offen offset:256 sc1
MM v[52:55],a[28:31],a[212:215],v[52:55]
RD a[228:231],v223 offset:32768
MM v[60:63],a[28:31],a[220:223],v[60:63]
RD a[232:235],v217 offset:32768
MM v[60:63],a[24:27],a[216:219],v[60:63]
s_waitcnt lgkmcnt(15)
RD a[236:239],v218 offset:32768
MM v[64:67],a[32:35],a[192:195],v[64:67]
TR a[24:25],v193 offset:32904
TR a[26:27],v193 offset:33160
MM v[64:67],a[36:39],a[196:199],v[64:67]
TR a[28:29],v193 offset:41128
TR a[30:31],v193 offset:41384
MM v[72:75],a[36:39],a[204:207],v[72:75]
RD a[240:243],v222 offset:36864
MM v[72:75],a[32:35],a[200:203],v[72:75]
RD a[244:247],v223 offset:36864
MM v[80:83],a[32:35],a[208:211],v[80:83]
RD a[248:251],v217 offset:36864
MM v[80:83],a[36:39],a[212:215],v[80:83]
RD a[252:255],v218 offset:36864
MM v[88:91],a[36:39],a[220:223],v[88:91]
MM v[88:91],a[32:35],a[216:219],v[88:91]
MM v[68:71],a[40:43],a[192:195],v[68:71]
TR a[32:33],v194 offset:32896
TR a[34:35],v194 offset:33152
MM v[68:71],a[44:47],a[196:199],v[68:71]
s_waitcnt lgkmcnt(2)
TR a[36:37],v194 offset:41120
TR a[38:39],v194 offset:41376
MM v[76:79],a[44:47],a[204:207],v[76:79]
s_add_u32 s88, s68, s86
s_add_u32 s86, s86, 128
MM v[76:79],a[40:43],a[200:203],v[76:79]
s_cmp_ge_u32 s86, 1280
s_cselect_b32 s86, 0, s86
s_add_u32 m0, s94, 32768
MM v[84:87],a[40:43],a[208:211],v[84:87]
s_add_u32 s95, s88, 0
MM v[84:87],a[44:47],a[212:215],v[84:87]
buffer_load_dwordx4 v220, s[32:35], s95 offen offset:0 lds
MM v[92:95],a[44:47],a[220:223],v[92:95]
s_add_u32 s95, s88, 9216
MM v[92:95],a[40:43],a[216:219],v[92:95]
buffer_load_dwordx4 v221, s[32:35], s95 offen offset:1024 lds
MM v[96:99],a[48:51],a[192:195],v[96:99]
s_add_u32 s95, s88, 18432
TR a[40:41],v194 offset:32904
TR a[42:43],v194 offset:33160
MM v[96:99],a[52:55],a[196:199],v[96:99]
TR a[44:45],v194 offset:41128
TR a[46:47],v194 offset:41384
MM v[104:107],a[52:55],a[204:207],v[104:107]
buffer_load_dwordx4 v220, s[32:35], s95 offen offset:2048 lds
MM v[104:107],a[48:51],a[200:203],v[104:107]
s_add_u32 s95, s88, 27648
MM v[112:115],a[48:51],a[208:211],v[112:115]
MM v[112:115],a[52:55],a[212:215],v[112:115]
MM v[120:123],a[52:55],a[220:223],v[120:123]
buffer_load_dwordx4 v221, s[32:35], s95 offen offset:3072 lds
MM v[120:123],a[48:51],a[216:219],v[120:123]
s_waitcnt vmcnt(10)
RD a[128:131],v222 offset:0
MM v[100:103],a[56:59],a[192:195],v[100:103]
TR a[48:49],v195 offset:32896
TR a[50:51],v195 offset:33152
MM v[100:103],a[60:63],a[196:199],v[100:103]
TR a[52:53],v195 offset:41120
TR a[54:55],v195 offset:41376
MM v[108:111],a[60:63],a[204:207],v[108:111]
RD a[132:135],v223 offset:0
MM v[108:111],a[56:59],a[200:203],v[108:111]
RD a[136:139],v217 offset:0
MM v[116:119],a[56:59],a[208:211],v[116:119]
RD a[140:143],v218 offset:0
MM v[116:119],a[60:63],a[212:215],v[116:119]
RD a[144:147],v222 offset:4096
MM v[124:127],a[60:63],a[220:223],v[124:127]
RD a[148:151],v223 offset:4096
MM v[124:127],a[56:59],a[216:219],v[124:127]
RD a[152:155],v217 offset:4096
RD a[156:159],v218 offset:4096
s_add_u32 m0, s94, 36864
s_add_u32 s95, s88, 40960
buffer_load_dwordx4 v220, s[32:35], s95 offen offset:0 lds
s_add_u32 s95, s88, 50176
buffer_load_dwordx4 v221, s[32:35], s95 offen offset:1024 lds
s_add_u32 s95, s88, 59392
buffer_load_dwordx4 v220, s[32:35], s95 offen offset:2048 lds
s_add_u32 s95, s88, 68608
buffer_load_dwordx4 v221, s[32:35], s95 offen offset:3072 lds
s_waitcnt lgkmcnt(0)
s_waitcnt vmcnt(9)
s_barrier
MM v[0:3],a[0:3],a[224:227],v[0:3]
s_add_u32 s77, s64, 0
TR a[56:57],v195 offset:32904
TR a[58:59],v195 offset:33160
MM v[0:3],a[4:7],a[228:231],v[0:3]
s_add_u32 m0, s65, 0
TR a[60:61],v195 offset:41128
TR a[62:63],v195 offset:41384
MM v[8:11],a[4:7],a[236:239],v[8:11]
TR a[64:65],v196 offset:0
TR a[66:67],v196 offset:256
s_waitcnt vmcnt(8)
MM v[8:11],a[0:3],a[232:235],v[8:11]
s_lshl_b32 s76, %[swave], 7
v_lshrrev_b32 v253, 8, v248
v_lshlrev_b32 v253, 5, v253
MM v[16:19],a[0:3],a[240:243],v[16:19]
s_add_u32 s76, s76, %[slds]
TR a[68:69],v196 offset:8224
TR a[70:71],v196 offset:8480
MM v[16:19],a[4:7],a[244:247],v[16:19]
s_mov_b64 exec, 1
buffer_store_dwordx2 v[170:171], off, s[20:23], s80 sc1
s_mov_b64 exec, -1
v_add_u32 v253, s76, v253
v_add_u32 v253, 151552, v253
v_xor_b32 v201, s69, v201
MM v[24:27],a[4:7],a[252:255],v[24:27]
buffer_load_dwordx4 v228, s[8:11], s77 offen offset:0 lds
MM v[24:27],a[0:3],a[248:251],v[24:27]
TR a[72:73],v196 offset:16448
TR a[74:75],v196 offset:16704
MM v[4:7],a[8:11],a[224:227],v[4:7]
buffer_load_dwordx4 v229, s[8:11], s77 offen offset:1024 lds
MM v[4:7],a[12:15],a[228:231],v[4:7]
TR a[76:77],v196 offset:24672
TR a[78:79],v196 offset:24928
v_xor_b32 v203, s69, v203
MM v[12:15],a[12:15],a[236:239],v[12:15]
TR a[80:81],v196 offset:8
TR a[82:83],v196 offset:264
MM v[12:15],a[8:11],a[232:235],v[12:15]
TR a[84:85],v196 offset:8232
TR a[86:87],v196 offset:8488
MM v[20:23],a[8:11],a[240:243],v[20:23]
buffer_load_dwordx4 v230, s[8:11], s77 offen offset:2048 lds
MM v[20:23],a[12:15],a[244:247],v[20:23]
TR a[88:89],v196 offset:16456
TR a[90:91],v196 offset:16712
v_xor_b32 v205, s69, v205
MM v[28:31],a[12:15],a[252:255],v[28:31]
TR a[92:93],v196 offset:24680
TR a[94:95],v196 offset:24936
MM v[28:31],a[8:11],a[248:251],v[28:31]
TR a[96:97],v197 offset:0
TR a[98:99],v197 offset:256
v_smfmac_f32_16x16x64_bf16 v[184:187], v[236:239], a[0:7], v234
TR a[100:101],v197 offset:8224
v_xor_b32 v207, s69, v207
v_smfmac_f32_16x16x64_bf16 v[188:191], v[236:239], a[8:15], v234
TR a[102:103],v197 offset:8480
MM v[32:35],a[16:19],a[224:227],v[32:35]
buffer_load_dwordx4 v231, s[8:11], s77 offen offset:3072 lds
MM v[32:35],a[20:23],a[228:231],v[32:35]
s_add_u32 s77, s64, 16384
TR a[104:105],v197 offset:16448
TR a[106:107],v197 offset:16704
v_or3_b32 v209, v201, v203, v205
MM v[40:43],a[20:23],a[236:239],v[40:43]
s_add_u32 m0, s65, 16384
TR a[108:109],v197 offset:24672
TR a[110:111],v197 offset:24928
MM v[40:43],a[16:19],a[232:235],v[40:43]
TR a[112:113],v197 offset:8
TR a[114:115],v197 offset:264
MM v[48:51],a[16:19],a[240:243],v[48:51]
TR a[116:117],v197 offset:8232
TR a[118:119],v197 offset:8488
v_or_b32 v209, v209, v207
MM v[48:51],a[20:23],a[244:247],v[48:51]
buffer_load_dwordx4 v228, s[8:11], s77 offen offset:0 lds
MM v[56:59],a[20:23],a[252:255],v[56:59]
TR a[120:121],v197 offset:16456
TR a[122:123],v197 offset:16712
MM v[56:59],a[16:19],a[248:251],v[56:59]
TR a[124:125],v197 offset:24680
TR a[126:127],v197 offset:24936
v_cmp_ne_u32 s[74:75], 0, v209
MM v[36:39],a[24:27],a[224:227],v[36:39]
TR a[0:1],v198 offset:0
TR a[2:3],v198 offset:256
MM v[36:39],a[28:31],a[228:231],v[36:39]
TR a[4:5],v198 offset:8224
TR a[6:7],v198 offset:8480
MM v[44:47],a[28:31],a[236:239],v[44:47]
TR a[8:9],v198 offset:16448
TR a[10:11],v198 offset:16704
MM v[44:47],a[24:27],a[232:235],v[44:47]
TR a[12:13],v198 offset:24672
TR a[14:15],v198 offset:24928
s_cmp_lg_u64 s[74:75], 0
s_cbranch_scc1 LRG1o_%=
LRG1ob_%=:
MM v[52:55],a[24:27],a[240:243],v[52:55]
s_bitcmp1_b32 %[sb], 0
s_cselect_b64 exec, -1, 0
v_swap_b32 v200, v202
v_swap_b32 v204, v206
s_mov_b64 exec, -1
MM v[52:55],a[28:31],a[244:247],v[52:55]
v_lshlrev_b32 v240, 16, v200
v_and_b32 v200, 0xffff0000, v200
MM v[60:63],a[28:31],a[252:255],v[60:63]
buffer_load_dwordx4 v229, s[8:11], s77 offen offset:1024 lds
MM v[60:63],a[24:27],a[248:251],v[60:63]
v_rcp_f32 v246, v200
MM v[64:67],a[32:35],a[224:227],v[64:67]
TR a[16:17],v198 offset:8
TR a[18:19],v198 offset:264
MM v[64:67],a[36:39],a[228:231],v[64:67]
TR a[20:21],v198 offset:8232
TR a[22:23],v198 offset:8488
v_xor_b32 v240, 0x80000000, v240
MM v[72:75],a[36:39],a[236:239],v[72:75]
TR a[24:25],v198 offset:16456
TR a[26:27],v198 offset:16712
MM v[72:75],a[32:35],a[232:235],v[72:75]
TR a[28:29],v198 offset:24680
TR a[30:31],v198 offset:24936
MM v[80:83],a[32:35],a[240:243],v[80:83]
v_mul_f32 v246, s72, v246
v_lshlrev_b32 v241, 16, v202
MM v[80:83],a[36:39],a[244:247],v[80:83]
v_and_b32 v202, 0xffff0000, v202
MM v[88:91],a[36:39],a[252:255],v[88:91]
v_rcp_f32 v247, v202
MM v[88:91],a[32:35],a[248:251],v[88:91]
s_waitcnt lgkmcnt(15)
v_xor_b32 v241, 0x80000000, v241
v_mul_f32 v247, s72, v247
v_smfmac_f32_16x16x64_bf16 v[184:187], v[236:239], a[64:71], v234
v_lshlrev_b32 v242, 16, v204
v_smfmac_f32_16x16x64_bf16 v[184:187], v[236:239], a[72:79], v234
v_and_b32 v204, 0xffff0000, v204
v_smfmac_f32_16x16x64_bf16 v[188:191], v[236:239], a[80:87], v234
v_smfmac_f32_16x16x64_bf16 v[188:191], v[236:239], a[88:95], v234
v_rcp_f32 v244, v204
MM v[68:71],a[40:43],a[224:227],v[68:71]
v_bfe_u32 v183, v248, 6, 2
v_lshlrev_b32 v183, 5, v183
MM v[68:71],a[44:47],a[228:231],v[68:71]
buffer_load_dwordx4 v230, s[8:11], s77 offen offset:2048 lds
v_bfe_u32 v182, v248, 4, 2
v_lshl_add_u32 v183, v182, 2, v183
MM v[76:79],a[44:47],a[236:239],v[76:79]
v_add_u32 v183, s76, v183
v_add_u32 v183, 151552, v183
v_xor_b32 v242, 0x80000000, v242
MM v[76:79],a[40:43],a[232:235],v[76:79]
v_mul_f32 v244, s72, v244
v_lshlrev_b32 v243, 16, v206
MM v[84:87],a[40:43],a[240:243],v[84:87]
v_and_b32 v206, 0xffff0000, v206
MM v[84:87],a[44:47],a[244:247],v[84:87]
v_rcp_f32 v245, v206
MM v[92:95],a[44:47],a[252:255],v[92:95]
buffer_load_dwordx4 v231, s[8:11], s77 offen offset:3072 lds
MM v[92:95],a[40:43],a[248:251],v[92:95]
s_waitcnt vmcnt(9)
v_add_f32 v184, v184, v185
v_add_f32 v188, v188, v189
v_xor_b32 v243, 0x80000000, v243
MM v[96:99],a[48:51],a[224:227],v[96:99]
TR a[32:33],v199 offset:0
TR a[34:35],v199 offset:256
MM v[96:99],a[52:55],a[228:231],v[96:99]
RD a[160:163],v222 offset:32768
ds_write_b32 v183, v184 offset:0
ds_write_b32 v183, v188 offset:16
MM v[104:107],a[52:55],a[236:239],v[104:107]
TR a[36:37],v199 offset:8224
TR a[38:39],v199 offset:8480
v_mul_f32 v245, s72, v245
MM v[104:107],a[48:51],a[232:235],v[104:107]
TR a[40:41],v199 offset:16448
TR a[42:43],v199 offset:16704
MM v[112:115],a[48:51],a[240:243],v[112:115]
TR a[44:45],v199 offset:24672
TR a[46:47],v199 offset:24928
RD a[164:167],v223 offset:32768
MM v[112:115],a[52:55],a[244:247],v[112:115]
RD a[168:171],v217 offset:32768
MM v[120:123],a[52:55],a[252:255],v[120:123]
RD a[172:175],v218 offset:32768
MM v[120:123],a[48:51],a[248:251],v[120:123]
RD a[176:179],v222 offset:36864
MM v[100:103],a[56:59],a[224:227],v[100:103]
RD a[180:183],v223 offset:36864
MM v[100:103],a[60:63],a[228:231],v[100:103]
RD a[184:187],v217 offset:36864
MM v[108:111],a[60:63],a[236:239],v[108:111]
RD a[188:191],v218 offset:36864
MM v[108:111],a[56:59],a[232:235],v[108:111]
s_add_u32 s77, s64, 0
s_add_u32 s81, s77, 896
s_add_u32 m0, s65, 65536
MM v[116:119],a[56:59],a[240:243],v[116:119]
buffer_load_dwordx4 v235, s[12:15], s81 offen offset:0 lds
MM v[116:119],a[60:63],a[244:247],v[116:119]
MM v[124:127],a[60:63],a[252:255],v[124:127]
MM v[124:127],a[56:59],a[248:251],v[124:127]
s_waitcnt lgkmcnt(0)
s_barrier
MM v[0:3],a[64:67],a[128:131],v[0:3]
s_mov_b32 s56, 0x000f000f
TR a[48:49],v199 offset:8
TR a[50:51],v199 offset:264
MM v[0:3],a[68:71],a[132:135],v[0:3]
s_mov_b32 s57, s56
TR a[52:53],v199 offset:8232
TR a[54:55],v199 offset:8488
MM v[0:3],a[72:75],a[160:163],v[0:3]
TR a[56:57],v199 offset:16456
TR a[58:59],v199 offset:16712
MM v[0:3],a[76:79],a[164:167],v[0:3]
buffer_load_dwordx4 v235, s[12:15], s77 offen offset:1024 lds
)ASM"
R"ASM(
MM v[8:11],a[76:79],a[172:175],v[8:11]
s_lshl_b64 s[58:59], s[56:57], 4
TR a[60:61],v199 offset:24680
TR a[62:63],v199 offset:24936
MM v[8:11],a[72:75],a[168:171],v[8:11]
v_xor_b32 v151, 0, v253
v_xor_b32 v143, 0, v253
MM v[8:11],a[68:71],a[140:143],v[8:11]
RD v[144:147],v151 offset:0
RD v[148:151],v151 offset:16
MM v[8:11],a[64:67],a[136:139],v[8:11]
RD v[136:139],v143 offset:0
RD v[140:143],v143 offset:16
MM v[4:7],a[80:83],a[128:131],v[4:7]
MM v[4:7],a[84:87],a[132:135],v[4:7]
MM v[4:7],a[88:91],a[160:163],v[4:7]
s_waitcnt lgkmcnt(2)
MM v[4:7],a[92:95],a[164:167],v[4:7]
s_waitcnt lgkmcnt(0)
MM v[12:15],a[92:95],a[172:175],v[12:15]
buffer_load_dwordx4 v235, s[12:15], s81 offen offset:2048 lds
MM v[12:15],a[88:91],a[168:171],v[12:15]
MM v[12:15],a[84:87],a[140:143],v[12:15]
MM v[12:15],a[80:83],a[136:139],v[12:15]
MM v[32:35],a[96:99],a[128:131],v[32:35]
MM v[32:35],a[100:103],a[132:135],v[32:35]
MM v[32:35],a[104:107],a[160:163],v[32:35]
MM v[32:35],a[108:111],a[164:167],v[32:35]
v_pk_fma_f32 v[0:1], v[240:241], v[144:145], v[0:1] op_sel:[0,0,0] op_sel_hi:[0,1,1]
v_pk_fma_f32 v[2:3], v[240:241], v[146:147], v[2:3] op_sel:[0,0,0] op_sel_hi:[0,1,1]
v_pk_fma_f32 v[4:5], v[240:241], v[148:149], v[4:5] op_sel:[0,0,0] op_sel_hi:[0,1,1]
v_pk_fma_f32 v[6:7], v[240:241], v[150:151], v[6:7] op_sel:[0,0,0] op_sel_hi:[0,1,1]
v_pk_mul_f32 v[0:1], v[246:247], v[0:1] op_sel:[0,0] op_sel_hi:[0,1]
v_pk_mul_f32 v[2:3], v[246:247], v[2:3] op_sel:[0,0] op_sel_hi:[0,1]
v_pk_mul_f32 v[4:5], v[246:247], v[4:5] op_sel:[0,0] op_sel_hi:[0,1]
v_pk_mul_f32 v[6:7], v[246:247], v[6:7] op_sel:[0,0] op_sel_hi:[0,1]
MM v[40:43],a[108:111],a[172:175],v[40:43]
CV v160,v0,v1
CV v161,v2,v3
MM v[40:43],a[104:107],a[168:171],v[40:43]
CV v162,v4,v5
CV v163,v6,v7
MM v[40:43],a[100:103],a[140:143],v[40:43]
buffer_load_dwordx4 v235, s[12:15], s77 offen offset:3072 lds
MM v[40:43],a[96:99],a[136:139],v[40:43]
s_add_u32 s77, s64, 32768
v_pk_fma_f32 v[8:9], v[240:241], v[144:145], v[8:9] op_sel:[1,0,0] op_sel_hi:[1,1,1]
v_pk_fma_f32 v[10:11], v[240:241], v[146:147], v[10:11] op_sel:[1,0,0] op_sel_hi:[1,1,1]
v_pk_fma_f32 v[12:13], v[240:241], v[148:149], v[12:13] op_sel:[1,0,0] op_sel_hi:[1,1,1]
v_pk_fma_f32 v[14:15], v[240:241], v[150:151], v[14:15] op_sel:[1,0,0] op_sel_hi:[1,1,1]
v_pk_mul_f32 v[8:9], v[246:247], v[8:9] op_sel:[1,0] op_sel_hi:[1,1]
v_pk_mul_f32 v[10:11], v[246:247], v[10:11] op_sel:[1,0] op_sel_hi:[1,1]
v_pk_mul_f32 v[12:13], v[246:247], v[12:13] op_sel:[1,0] op_sel_hi:[1,1]
v_pk_mul_f32 v[14:15], v[246:247], v[14:15] op_sel:[1,0] op_sel_hi:[1,1]
MM v[36:39],a[112:115],a[128:131],v[36:39]
s_add_u32 m0, s65, 32768
CV v164,v8,v9
CV v165,v10,v11
MM v[36:39],a[116:119],a[132:135],v[36:39]
CV v166,v12,v13
CV v167,v14,v15
MM v[36:39],a[120:123],a[160:163],v[36:39]
v_xor_b32 v159, 128, v253
MM v[36:39],a[124:127],a[164:167],v[36:39]
RD v[152:155],v159 offset:0
RD v[156:159],v159 offset:16
MM v[44:47],a[124:127],a[172:175],v[44:47]
MM v[44:47],a[120:123],a[168:171],v[44:47]
MM v[44:47],a[116:119],a[140:143],v[44:47]
MM v[44:47],a[112:115],a[136:139],v[44:47]
s_waitcnt lgkmcnt(0)
MM v[64:67],a[0:3],a[128:131],v[64:67]
buffer_load_dwordx4 v228, s[8:11], s77 offen offset:0 lds
MM v[64:67],a[4:7],a[132:135],v[64:67]
MM v[64:67],a[8:11],a[160:163],v[64:67]
MM v[64:67],a[12:15],a[164:167],v[64:67]
v_pk_fma_f32 v[32:33], v[240:241], v[152:153], v[32:33] op_sel:[0,0,0] op_sel_hi:[0,1,1]
v_pk_fma_f32 v[34:35], v[240:241], v[154:155], v[34:35] op_sel:[0,0,0] op_sel_hi:[0,1,1]
v_pk_fma_f32 v[36:37], v[240:241], v[156:157], v[36:37] op_sel:[0,0,0] op_sel_hi:[0,1,1]
v_pk_fma_f32 v[38:39], v[240:241], v[158:159], v[38:39] op_sel:[0,0,0] op_sel_hi:[0,1,1]
v_pk_mul_f32 v[32:33], v[246:247], v[32:33] op_sel:[0,0] op_sel_hi:[0,1]
v_pk_mul_f32 v[34:35], v[246:247], v[34:35] op_sel:[0,0] op_sel_hi:[0,1]
v_pk_mul_f32 v[36:37], v[246:247], v[36:37] op_sel:[0,0] op_sel_hi:[0,1]
v_pk_mul_f32 v[38:39], v[246:247], v[38:39] op_sel:[0,0] op_sel_hi:[0,1]
MM v[72:75],a[12:15],a[172:175],v[72:75]
CV v176,v32,v33
CV v177,v34,v35
MM v[72:75],a[8:11],a[168:171],v[72:75]
CV v178,v36,v37
CV v179,v38,v39
MM v[72:75],a[4:7],a[140:143],v[72:75]
v_pk_fma_f32 v[40:41], v[240:241], v[152:153], v[40:41] op_sel:[1,0,0] op_sel_hi:[1,1,1]
v_pk_fma_f32 v[42:43], v[240:241], v[154:155], v[42:43] op_sel:[1,0,0] op_sel_hi:[1,1,1]
v_pk_fma_f32 v[44:45], v[240:241], v[156:157], v[44:45] op_sel:[1,0,0] op_sel_hi:[1,1,1]
v_pk_fma_f32 v[46:47], v[240:241], v[158:159], v[46:47] op_sel:[1,0,0] op_sel_hi:[1,1,1]
v_pk_mul_f32 v[40:41], v[246:247], v[40:41] op_sel:[1,0] op_sel_hi:[1,1]
v_pk_mul_f32 v[42:43], v[246:247], v[42:43] op_sel:[1,0] op_sel_hi:[1,1]
v_pk_mul_f32 v[44:45], v[246:247], v[44:45] op_sel:[1,0] op_sel_hi:[1,1]
v_pk_mul_f32 v[46:47], v[246:247], v[46:47] op_sel:[1,0] op_sel_hi:[1,1]
MM v[72:75],a[0:3],a[136:139],v[72:75]
CV v180,v40,v41
CV v181,v42,v43
MM v[68:71],a[16:19],a[128:131],v[68:71]
CV v182,v44,v45
CV v183,v46,v47
MM v[68:71],a[20:23],a[132:135],v[68:71]
v_xor_b32 v151, 256, v253
MM v[68:71],a[24:27],a[160:163],v[68:71]
buffer_load_dwordx4 v229, s[8:11], s77 offen offset:1024 lds
MM v[68:71],a[28:31],a[164:167],v[68:71]
RD v[144:147],v151 offset:0
RD v[148:151],v151 offset:16
MM v[76:79],a[28:31],a[172:175],v[76:79]
MM v[76:79],a[24:27],a[168:171],v[76:79]
MM v[76:79],a[20:23],a[140:143],v[76:79]
MM v[76:79],a[16:19],a[136:139],v[76:79]
s_waitcnt lgkmcnt(0)
MM v[96:99],a[32:35],a[128:131],v[96:99]
MM v[96:99],a[36:39],a[132:135],v[96:99]
MM v[96:99],a[40:43],a[160:163],v[96:99]
MM v[96:99],a[44:47],a[164:167],v[96:99]
v_pk_fma_f32 v[64:65], v[240:241], v[144:145], v[64:65] op_sel:[0,0,0] op_sel_hi:[0,1,1]
v_pk_fma_f32 v[66:67], v[240:241], v[146:147], v[66:67] op_sel:[0,0,0] op_sel_hi:[0,1,1]
v_pk_fma_f32 v[68:69], v[240:241], v[148:149], v[68:69] op_sel:[0,0,0] op_sel_hi:[0,1,1]
v_pk_fma_f32 v[70:71], v[240:241], v[150:151], v[70:71] op_sel:[0,0,0] op_sel_hi:[0,1,1]
v_pk_mul_f32 v[64:65], v[246:247], v[64:65] op_sel:[0,0] op_sel_hi:[0,1]
v_pk_mul_f32 v[66:67], v[246:247], v[66:67] op_sel:[0,0] op_sel_hi:[0,1]
v_pk_mul_f32 v[68:69], v[246:247], v[68:69] op_sel:[0,0] op_sel_hi:[0,1]
v_pk_mul_f32 v[70:71], v[246:247], v[70:71] op_sel:[0,0] op_sel_hi:[0,1]
MM v[104:107],a[44:47],a[172:175],v[104:107]
buffer_load_dwordx4 v230, s[8:11], s77 offen offset:2048 lds
MM v[104:107],a[40:43],a[168:171],v[104:107]
CV v192,v64,v65
CV v193,v66,v67
MM v[104:107],a[36:39],a[140:143],v[104:107]
CV v194,v68,v69
CV v195,v70,v71
MM v[104:107],a[32:35],a[136:139],v[104:107]
v_pk_fma_f32 v[72:73], v[240:241], v[144:145], v[72:73] op_sel:[1,0,0] op_sel_hi:[1,1,1]
v_pk_fma_f32 v[74:75], v[240:241], v[146:147], v[74:75] op_sel:[1,0,0] op_sel_hi:[1,1,1]
v_pk_fma_f32 v[76:77], v[240:241], v[148:149], v[76:77] op_sel:[1,0,0] op_sel_hi:[1,1,1]
v_pk_fma_f32 v[78:79], v[240:241], v[150:151], v[78:79] op_sel:[1,0,0] op_sel_hi:[1,1,1]
v_pk_mul_f32 v[72:73], v[246:247], v[72:73] op_sel:[1,0] op_sel_hi:[1,1]
v_pk_mul_f32 v[74:75], v[246:247], v[74:75] op_sel:[1,0] op_sel_hi:[1,1]
v_pk_mul_f32 v[76:77], v[246:247], v[76:77] op_sel:[1,0] op_sel_hi:[1,1]
v_pk_mul_f32 v[78:79], v[246:247], v[78:79] op_sel:[1,0] op_sel_hi:[1,1]
MM v[100:103],a[48:51],a[128:131],v[100:103]
CV v196,v72,v73
CV v197,v74,v75
MM v[100:103],a[52:55],a[132:135],v[100:103]
CV v198,v76,v77
CV v199,v78,v79
MM v[100:103],a[56:59],a[160:163],v[100:103]
v_xor_b32 v159, 384, v253
MM v[100:103],a[60:63],a[164:167],v[100:103]
RD v[152:155],v159 offset:0
RD v[156:159],v159 offset:16
MM v[108:111],a[60:63],a[172:175],v[108:111]
MM v[108:111],a[56:59],a[168:171],v[108:111]
MM v[108:111],a[52:55],a[140:143],v[108:111]
buffer_load_dwordx4 v231, s[8:11], s77 offen offset:3072 lds
MM v[108:111],a[48:51],a[136:139],v[108:111]
s_add_u32 s77, s64, 16384
s_add_u32 s81, s77, 896
s_add_u32 m0, s65, 81920
MM v[16:19],a[64:67],a[144:147],v[16:19]
s_waitcnt lgkmcnt(0)
MM v[16:19],a[68:71],a[148:151],v[16:19]
s_nop 0
MM v[16:19],a[72:75],a[176:179],v[16:19]
LPG4_%=:
s_cbranch_execz LPG8_%=
MM v[16:19],a[76:79],a[180:183],v[16:19]
v_pk_fma_f32 v[96:97], v[240:241], v[152:153], v[96:97] op_sel:[0,0,0] op_sel_hi:[0,1,1]
v_pk_fma_f32 v[98:99], v[240:241], v[154:155], v[98:99] op_sel:[0,0,0] op_sel_hi:[0,1,1]
v_pk_fma_f32 v[100:101], v[240:241], v[156:157], v[100:101] op_sel:[0,0,0] op_sel_hi:[0,1,1]
v_pk_fma_f32 v[102:103], v[240:241], v[158:159], v[102:103] op_sel:[0,0,0] op_sel_hi:[0,1,1]
v_pk_mul_f32 v[96:97], v[246:247], v[96:97] op_sel:[0,0] op_sel_hi:[0,1]
v_pk_mul_f32 v[98:99], v[246:247], v[98:99] op_sel:[0,0] op_sel_hi:[0,1]
v_pk_mul_f32 v[100:101], v[246:247], v[100:101] op_sel:[0,0] op_sel_hi:[0,1]
v_pk_mul_f32 v[102:103], v[246:247], v[102:103] op_sel:[0,0] op_sel_hi:[0,1]
MM v[24:27],a[76:79],a[188:191],v[24:27]
CV v208,v96,v97
CV v209,v98,v99
MM v[24:27],a[72:75],a[184:187],v[24:27]
CV v210,v100,v101
CV v211,v102,v103
MM v[24:27],a[68:71],a[156:159],v[24:27]
s_waitcnt vmcnt(12)
s_barrier
RD a[192:195],v224 offset:0
MM v[24:27],a[64:67],a[152:155],v[24:27]
RD a[196:199],v225 offset:0
RD a[200:203],v226 offset:0
MM v[20:23],a[80:83],a[144:147],v[20:23]
buffer_load_dwordx4 v235, s[12:15], s81 offen offset:0 lds
MM v[20:23],a[84:87],a[148:151],v[20:23]
RD a[204:207],v227 offset:0
RD a[208:211],v224 offset:4096
MM v[20:23],a[88:91],a[176:179],v[20:23]
RD a[212:215],v225 offset:4096
RD a[216:219],v226 offset:4096
MM v[20:23],a[92:95],a[180:183],v[20:23]
RD a[220:223],v227 offset:4096
RD a[224:227],v224 offset:8192
MM v[28:31],a[92:95],a[188:191],v[28:31]
RD a[228:231],v225 offset:8192
RD a[232:235],v226 offset:8192
MM v[28:31],a[88:91],a[184:187],v[28:31]
RD a[236:239],v227 offset:8192
RD a[240:243],v224 offset:12288
MM v[28:31],a[84:87],a[156:159],v[28:31]
RD a[244:247],v225 offset:12288
RD a[248:251],v226 offset:12288
MM v[28:31],a[80:83],a[152:155],v[28:31]
RD a[252:255],v227 offset:12288
MM v[48:51],a[96:99],a[144:147],v[48:51]
v_pk_fma_f32 v[104:105], v[240:241], v[152:153], v[104:105] op_sel:[1,0,0] op_sel_hi:[1,1,1]
v_pk_fma_f32 v[106:107], v[240:241], v[154:155], v[106:107] op_sel:[1,0,0] op_sel_hi:[1,1,1]
v_pk_fma_f32 v[108:109], v[240:241], v[156:157], v[108:109] op_sel:[1,0,0] op_sel_hi:[1,1,1]
v_pk_fma_f32 v[110:111], v[240:241], v[158:159], v[110:111] op_sel:[1,0,0] op_sel_hi:[1,1,1]
v_pk_mul_f32 v[104:105], v[246:247], v[104:105] op_sel:[1,0] op_sel_hi:[1,1]
v_pk_mul_f32 v[106:107], v[246:247], v[106:107] op_sel:[1,0] op_sel_hi:[1,1]
v_pk_mul_f32 v[108:109], v[246:247], v[108:109] op_sel:[1,0] op_sel_hi:[1,1]
v_pk_mul_f32 v[110:111], v[246:247], v[110:111] op_sel:[1,0] op_sel_hi:[1,1]
MM v[48:51],a[100:103],a[148:151],v[48:51]
CV v212,v104,v105
CV v213,v106,v107
MM v[48:51],a[104:107],a[176:179],v[48:51]
buffer_load_dwordx4 v235, s[12:15], s77 offen offset:1024 lds
MM v[48:51],a[108:111],a[180:183],v[48:51]
CV v214,v108,v109
CV v215,v110,v111
MM v[56:59],a[108:111],a[188:191],v[56:59]
v_mov_b32 v247, 0
v_mov_b32 v236, 0
MM v[56:59],a[104:107],a[184:187],v[56:59]
v_mov_b32 v237, 0
v_cndmask_b32 v144, 0, v238, s[56:57]
MM v[56:59],a[100:103],a[156:159],v[56:59]
v_cndmask_b32 v145, 0, v238, s[56:57]
v_cndmask_b32 v146, 0, v238, s[56:57]
MM v[56:59],a[96:99],a[152:155],v[56:59]
v_cndmask_b32 v147, 0, v238, s[56:57]
v_cndmask_b32 v148, 0, v238, s[58:59]
MM v[52:55],a[112:115],a[144:147],v[52:55]
v_cndmask_b32 v149, 0, v238, s[58:59]
v_cndmask_b32 v150, 0, v238, s[58:59]
MM v[52:55],a[116:119],a[148:151],v[52:55]
v_pk_fma_f32 v[16:17], v[242:243], v[136:137], v[16:17] op_sel:[0,0,0] op_sel_hi:[0,1,1]
v_pk_fma_f32 v[18:19], v[242:243], v[138:139], v[18:19] op_sel:[0,0,0] op_sel_hi:[0,1,1]
v_pk_fma_f32 v[20:21], v[242:243], v[140:141], v[20:21] op_sel:[0,0,0] op_sel_hi:[0,1,1]
v_pk_fma_f32 v[22:23], v[242:243], v[142:143], v[22:23] op_sel:[0,0,0] op_sel_hi:[0,1,1]
v_pk_mul_f32 v[16:17], v[244:245], v[16:17] op_sel:[0,0] op_sel_hi:[0,1]
v_pk_mul_f32 v[18:19], v[244:245], v[18:19] op_sel:[0,0] op_sel_hi:[0,1]
v_pk_mul_f32 v[20:21], v[244:245], v[20:21] op_sel:[0,0] op_sel_hi:[0,1]
v_pk_mul_f32 v[22:23], v[244:245], v[22:23] op_sel:[0,0] op_sel_hi:[0,1]
)ASM"
R"ASM(
MM v[52:55],a[120:123],a[176:179],v[52:55]
v_cndmask_b32 v151, 0, v238, s[58:59]
CV v168,v16,v17
MM v[52:55],a[124:127],a[180:183],v[52:55]
CV v169,v18,v19
CV v170,v20,v21
s_waitcnt lgkmcnt(0)
MM v[0:3],a[192:195],v[160:163],0
buffer_load_dwordx4 v235, s[12:15], s81 offen offset:2048 lds
MM v[0:3],a[196:199],v[176:179],v[0:3]
CV v171,v22,v23
MM v[0:3],a[200:203],v[192:195],v[0:3]
v_pk_fma_f32 v[24:25], v[242:243], v[136:137], v[24:25] op_sel:[1,0,0] op_sel_hi:[1,1,1]
v_pk_fma_f32 v[26:27], v[242:243], v[138:139], v[26:27] op_sel:[1,0,0] op_sel_hi:[1,1,1]
v_pk_fma_f32 v[28:29], v[242:243], v[140:141], v[28:29] op_sel:[1,0,0] op_sel_hi:[1,1,1]
v_pk_fma_f32 v[30:31], v[242:243], v[142:143], v[30:31] op_sel:[1,0,0] op_sel_hi:[1,1,1]
v_pk_mul_f32 v[24:25], v[244:245], v[24:25] op_sel:[1,0] op_sel_hi:[1,1]
v_pk_mul_f32 v[26:27], v[244:245], v[26:27] op_sel:[1,0] op_sel_hi:[1,1]
v_pk_mul_f32 v[28:29], v[244:245], v[28:29] op_sel:[1,0] op_sel_hi:[1,1]
v_pk_mul_f32 v[30:31], v[244:245], v[30:31] op_sel:[1,0] op_sel_hi:[1,1]
MM v[0:3],a[204:207],v[208:211],v[0:3]
CV v172,v24,v25
CV v173,v26,v27
MM v[8:11],a[192:195],v[164:167],0
CV v174,v28,v29
CV v175,v30,v31
MM v[8:11],a[196:199],v[180:183],v[8:11]
v_xor_b32 v143, 128, v253
MM v[8:11],a[200:203],v[196:199],v[8:11]
RD v[136:139],v143 offset:0
RD v[140:143],v143 offset:16
MM v[8:11],a[204:207],v[212:215],v[8:11]
MM v[4:7],a[208:211],v[160:163],0
v_max3_f32 v152, v0, v1, v2
v_max_f32 v152, v152, v3
MM v[4:7],a[212:215],v[176:179],v[4:7]
v_mov_b32 v153, v152
MM v[4:7],a[216:219],v[192:195],v[4:7]
buffer_load_dwordx4 v235, s[12:15], s77 offen offset:3072 lds
MM v[4:7],a[220:223],v[208:211],v[4:7]
s_add_u32 s77, s64, 32768
s_add_u32 s81, s77, 896
s_add_u32 m0, s65, 98304
v_permlane16_swap_b32 v152, v153
v_max_f32 v152, v152, v153
MM v[12:15],a[208:211],v[164:167],0
s_waitcnt vmcnt(8)
s_barrier
RD a[192:195],v224 offset:16384
v_mov_b32 v153, v152
MM v[12:15],a[212:215],v[180:183],v[12:15]
s_waitcnt lgkmcnt(1)
RD a[196:199],v225 offset:16384
RD a[200:203],v226 offset:16384
v_permlane32_swap_b32 v152, v153
v_max_f32 v152, v152, v153
v_sub_f32 v128, 0, v152
MM v[12:15],a[216:219],v[196:199],v[12:15]
RD a[204:207],v227 offset:16384
v_sub_f32 v129, 0, v152
v_sub_f32 v130, 0, v152
MM v[12:15],a[220:223],v[212:215],v[12:15]
v_sub_f32 v131, 0, v152
v_max3_f32 v154, v8, v9, v10
MM v[32:35],a[224:227],v[160:163],0
RD a[208:211],v224 offset:20480
RD a[212:215],v225 offset:20480
v_max_f32 v154, v154, v11
v_mov_b32 v155, v154
MM v[32:35],a[228:231],v[176:179],v[32:35]
RD a[216:219],v226 offset:20480
RD a[220:223],v227 offset:20480
v_permlane16_swap_b32 v154, v155
v_max_f32 v154, v154, v155
MM v[32:35],a[232:235],v[192:195],v[32:35]
v_mov_b32 v155, v154
v_sub_f32 v0, v0, v152
MM v[32:35],a[236:239],v[208:211],v[32:35]
v_permlane32_swap_b32 v154, v155
v_max_f32 v154, v154, v155
v_sub_f32 v132, 0, v154
MM v[40:43],a[224:227],v[164:167],0
buffer_load_dwordx4 v235, s[12:15], s81 offen offset:0 lds
v_sub_f32 v133, 0, v154
v_sub_f32 v134, 0, v154
v_sub_f32 v135, 0, v154
MM v[40:43],a[228:231],v[180:183],v[40:43]
v_sub_f32 v1, v1, v152
EX v0,v0
EX v1,v1
MM v[40:43],a[232:235],v[196:199],v[40:43]
v_sub_f32 v2, v2, v152
v_sub_f32 v3, v3, v152
MM v[40:43],a[236:239],v[212:215],v[40:43]
EX v2,v2
MM v[36:39],a[240:243],v[160:163],v[128:131]
RD a[224:227],v224 offset:24576
RD a[228:231],v225 offset:24576
EX v3,v3
MM v[36:39],a[244:247],v[176:179],v[36:39]
RD a[232:235],v226 offset:24576
RD a[236:239],v227 offset:24576
CV v0,v0,v1
v_sub_f32 v4, v4, v152
MM v[36:39],a[248:251],v[192:195],v[36:39]
v_sub_f32 v5, v5, v152
EX v4,v4
MM v[36:39],a[252:255],v[208:211],v[36:39]
EX v5,v5
MM v[44:47],a[240:243],v[164:167],v[132:135]
CV v1,v2,v3
v_sub_f32 v6, v6, v152
MM v[44:47],a[244:247],v[180:183],v[44:47]
v_sub_f32 v7, v7, v152
MM v[44:47],a[248:251],v[196:199],v[44:47]
buffer_load_dwordx4 v235, s[12:15], s77 offen offset:1024 lds
MM v[44:47],a[252:255],v[212:215],v[44:47]
EX v6,v6
EX v7,v7
CV v2,v4,v5
MM v[60:63],a[124:127],a[188:191],v[60:63]
RD a[240:243],v224 offset:28672
RD a[244:247],v225 offset:28672
v_sub_f32 v8, v8, v154
v_sub_f32 v9, v9, v154
MM v[60:63],a[120:123],a[184:187],v[60:63]
RD a[248:251],v226 offset:28672
RD a[252:255],v227 offset:28672
EX v8,v8
MM v[60:63],a[116:119],a[156:159],v[60:63]
EX v9,v9
MM v[60:63],a[112:115],a[152:155],v[60:63]
CV v3,v6,v7
v_sub_f32 v10, v10, v154
MM v[80:83],a[0:3],a[144:147],v[80:83]
v_sub_f32 v11, v11, v154
EX v10,v10
MM v[80:83],a[4:7],a[148:151],v[80:83]
EX v11,v11
MM v[80:83],a[8:11],a[176:179],v[80:83]
CV v8,v8,v9
v_sub_f32 v12, v12, v154
MM v[80:83],a[12:15],a[180:183],v[80:83]
v_pk_fma_f32 v[48:49], v[242:243], v[136:137], v[48:49] op_sel:[0,0,0] op_sel_hi:[0,1,1]
v_pk_fma_f32 v[50:51], v[242:243], v[138:139], v[50:51] op_sel:[0,0,0] op_sel_hi:[0,1,1]
v_pk_fma_f32 v[52:53], v[242:243], v[140:141], v[52:53] op_sel:[0,0,0] op_sel_hi:[0,1,1]
v_pk_fma_f32 v[54:55], v[242:243], v[142:143], v[54:55] op_sel:[0,0,0] op_sel_hi:[0,1,1]
v_pk_mul_f32 v[48:49], v[244:245], v[48:49] op_sel:[0,0] op_sel_hi:[0,1]
v_pk_mul_f32 v[50:51], v[244:245], v[50:51] op_sel:[0,0] op_sel_hi:[0,1]
v_pk_mul_f32 v[52:53], v[244:245], v[52:53] op_sel:[0,0] op_sel_hi:[0,1]
v_pk_mul_f32 v[54:55], v[244:245], v[54:55] op_sel:[0,0] op_sel_hi:[0,1]
v_sub_f32 v13, v13, v154
EX v12,v12
MM v[88:91],a[12:15],a[188:191],v[88:91]
buffer_load_dwordx4 v235, s[12:15], s81 offen offset:2048 lds
MM v[88:91],a[8:11],a[184:187],v[88:91]
EX v13,v13
CV v9,v10,v11
MM v[88:91],a[4:7],a[156:159],v[88:91]
v_sub_f32 v14, v14, v154
v_sub_f32 v15, v15, v154
EX v14,v14
MM v[88:91],a[0:3],a[152:155],v[88:91]
EX v15,v15
MM v[84:87],a[16:19],a[144:147],v[84:87]
CV v10,v12,v13
v_sub_f32 v32, v32, v152
MM v[84:87],a[20:23],a[148:151],v[84:87]
v_sub_f32 v33, v33, v152
CV v184,v48,v49
MM v[84:87],a[24:27],a[176:179],v[84:87]
EX v32,v32
MM v[84:87],a[28:31],a[180:183],v[84:87]
EX v33,v33
MM v[92:95],a[28:31],a[188:191],v[92:95]
CV v11,v14,v15
v_sub_f32 v34, v34, v152
v_sub_f32 v35, v35, v152
MM v[92:95],a[24:27],a[184:187],v[92:95]
EX v34,v34
MM v[92:95],a[20:23],a[156:159],v[92:95]
buffer_load_dwordx4 v235, s[12:15], s77 offen offset:3072 lds
MM v[92:95],a[16:19],a[152:155],v[92:95]
s_add_u32 s77, s64, 49152
s_add_u32 m0, s65, 49152
CV v185,v50,v51
EX v35,v35
CV v4,v32,v33
EX v36,v36
MM v[112:115],a[32:35],a[144:147],v[112:115]
buffer_load_dwordx4 v228, s[8:11], s77 offen offset:0 lds
MM v[112:115],a[36:39],a[148:151],v[112:115]
EX v37,v37
CV v5,v34,v35
MM v[112:115],a[40:43],a[176:179],v[112:115]
EX v38,v38
EX v39,v39
MM v[112:115],a[44:47],a[180:183],v[112:115]
buffer_load_dwordx4 v229, s[8:11], s77 offen offset:1024 lds
MM v[120:123],a[44:47],a[188:191],v[120:123]
CV v6,v36,v37
v_sub_f32 v40, v40, v154
v_sub_f32 v41, v41, v154
MM v[120:123],a[40:43],a[184:187],v[120:123]
EX v40,v40
MM v[120:123],a[36:39],a[156:159],v[120:123]
EX v41,v41
MM v[120:123],a[32:35],a[152:155],v[120:123]
buffer_load_dwordx4 v230, s[8:11], s77 offen offset:2048 lds
MM v[116:119],a[48:51],a[144:147],v[116:119]
CV v7,v38,v39
v_sub_f32 v42, v42, v154
v_sub_f32 v43, v43, v154
EX v42,v42
MM v[116:119],a[52:55],a[148:151],v[116:119]
EX v43,v43
MM v[116:119],a[56:59],a[176:179],v[116:119]
CV v12,v40,v41
CV v186,v52,v53
EX v44,v44
MM v[116:119],a[60:63],a[180:183],v[116:119]
buffer_load_dwordx4 v231, s[8:11], s77 offen offset:3072 lds
MM v[124:127],a[60:63],a[188:191],v[124:127]
s_add_u32 s77, s64, 49152
s_add_u32 s81, s77, 896
s_add_u32 m0, s65, 114688
EX v45,v45
CV v13,v42,v43
MM v[124:127],a[56:59],a[184:187],v[124:127]
EX v46,v46
MM v[124:127],a[52:55],a[156:159],v[124:127]
EX v47,v47
CV v14,v44,v45
CV v15,v46,v47
MM v[124:127],a[48:51],a[152:155],v[124:127]
buffer_load_dwordx4 v235, s[12:15], s81 offen offset:0 lds
v_smfmac_f32_16x16x64_bf16 v[236:239], v[144:147], v[0:7], v234
CV v187,v54,v55
v_smfmac_f32_16x16x64_bf16 v[236:239], v[148:151], v[8:15], v234
s_waitcnt lgkmcnt(0)
buffer_load_dwordx4 v235, s[12:15], s77 offen offset:1024 lds
buffer_load_dwordx4 v235, s[12:15], s81 offen offset:2048 lds
buffer_load_dwordx4 v235, s[12:15], s77 offen offset:3072 lds
s_add_u32 s64, s64, 49152
MM v[64:67],a[192:195],v[160:163],v[128:131]
s_add_u32 s64, s64, 16384
TR a[128:129],v232 offset:0
TR a[130:131],v232 offset:4096
MM v[64:67],a[196:199],v[176:179],v[64:67]
s_add_u32 s81, s64, 896
v_pk_fma_f32 v[56:57], v[242:243], v[136:137], v[56:57] op_sel:[1,0,0] op_sel_hi:[1,1,1]
v_pk_fma_f32 v[58:59], v[242:243], v[138:139], v[58:59] op_sel:[1,0,0] op_sel_hi:[1,1,1]
v_pk_fma_f32 v[60:61], v[242:243], v[140:141], v[60:61] op_sel:[1,0,0] op_sel_hi:[1,1,1]
v_pk_fma_f32 v[62:63], v[242:243], v[142:143], v[62:63] op_sel:[1,0,0] op_sel_hi:[1,1,1]
v_pk_mul_f32 v[56:57], v[244:245], v[56:57] op_sel:[1,0] op_sel_hi:[1,1]
v_pk_mul_f32 v[58:59], v[244:245], v[58:59] op_sel:[1,0] op_sel_hi:[1,1]
v_pk_mul_f32 v[60:61], v[244:245], v[60:61] op_sel:[1,0] op_sel_hi:[1,1]
v_pk_mul_f32 v[62:63], v[244:245], v[62:63] op_sel:[1,0] op_sel_hi:[1,1]
MM v[64:67],a[200:203],v[192:195],v[64:67]
TR a[132:133],v232 offset:8192
TR a[134:135],v232 offset:12288
MM v[64:67],a[204:207],v[208:211],v[64:67]
TR a[136:137],v233 offset:0
v_add_f32 v246, v236, v237
MM v[68:71],a[220:223],v[208:211],v[128:131]
TR a[138:139],v233 offset:4096
v_cmp_nge_f32 vcc, s71, v246
MM v[68:71],a[216:219],v[192:195],v[68:71]
s_cbranch_vccnz LSITE0_%=
LBACK0_%=:
TR a[140:141],v233 offset:8192
TR a[142:143],v233 offset:12288
MM v[68:71],a[212:215],v[176:179],v[68:71]
TR a[144:145],v232 offset:512
TR a[146:147],v232 offset:4608
MM v[68:71],a[208:211],v[160:163],v[68:71]
TR a[148:149],v232 offset:8704
CV v188,v56,v57
MM v[76:79],a[208:211],v[164:167],v[132:135]
TR a[150:151],v232 offset:12800
CV v189,v58,v59
MM v[76:79],a[212:215],v[180:183],v[76:79]
EX v64,v64
MM v[76:79],a[216:219],v[196:199],v[76:79]
TR a[152:153],v233 offset:512
CV v190,v60,v61
MM v[76:79],a[220:223],v[212:215],v[76:79]
TR a[154:155],v233 offset:4608
CV v191,v62,v63
MM v[72:75],a[204:207],v[212:215],v[132:135]
EX v65,v65
MM v[72:75],a[200:203],v[196:199],v[72:75]
TR a[156:157],v233 offset:8704
v_xor_b32 v143, 256, v253
MM v[72:75],a[196:199],v[180:183],v[72:75]
TR a[158:159],v233 offset:12800
RD v[136:139],v143 offset:0
MM v[72:75],a[192:195],v[164:167],v[72:75]
EX v66,v66
MM v[96:99],a[224:227],v[160:163],v[128:131]
TR a[160:161],v232 offset:1024
RD v[140:143],v143 offset:16
EX v67,v67
MM v[96:99],a[228:231],v[176:179],v[96:99]
TR a[162:163],v232 offset:5120
TR a[164:165],v232 offset:9216
MM v[96:99],a[232:235],v[192:195],v[96:99]
TR a[166:167],v232 offset:13312
TR a[168:169],v233 offset:1024
EX v68,v68
MM v[96:99],a[236:239],v[208:211],v[96:99]
TR a[170:171],v233 offset:5120
TR a[172:173],v233 offset:9216
MM v[100:103],a[252:255],v[208:211],v[128:131]
s_waitcnt lgkmcnt(6)
v_pk_fma_f32 v[80:81], v[242:243], v[136:137], v[80:81] op_sel:[0,0,0] op_sel_hi:[0,1,1]
v_pk_fma_f32 v[82:83], v[242:243], v[138:139], v[82:83] op_sel:[0,0,0] op_sel_hi:[0,1,1]
v_pk_fma_f32 v[84:85], v[242:243], v[140:141], v[84:85] op_sel:[0,0,0] op_sel_hi:[0,1,1]
v_pk_fma_f32 v[86:87], v[242:243], v[142:143], v[86:87] op_sel:[0,0,0] op_sel_hi:[0,1,1]
v_pk_mul_f32 v[80:81], v[244:245], v[80:81] op_sel:[0,0] op_sel_hi:[0,1]
v_pk_mul_f32 v[82:83], v[244:245], v[82:83] op_sel:[0,0] op_sel_hi:[0,1]
v_pk_mul_f32 v[84:85], v[244:245], v[84:85] op_sel:[0,0] op_sel_hi:[0,1]
v_pk_mul_f32 v[86:87], v[244:245], v[86:87] op_sel:[0,0] op_sel_hi:[0,1]
EX v69,v69
MM v[100:103],a[248:251],v[192:195],v[100:103]
TR a[174:175],v233 offset:13312
CV v64,v64,v65
MM v[100:103],a[244:247],v[176:179],v[100:103]
TR a[176:177],v232 offset:1536
EX v70,v70
MM v[100:103],a[240:243],v[160:163],v[100:103]
TR a[178:179],v232 offset:5632
TR a[180:181],v232 offset:9728
EX v71,v71
MM v[108:111],a[240:243],v[164:167],v[132:135]
TR a[182:183],v232 offset:13824
CV v200,v80,v81
MM v[108:111],a[244:247],v[180:183],v[108:111]
EX v72,v72
MM v[108:111],a[248:251],v[196:199],v[108:111]
TR a[184:185],v233 offset:1536
TR a[186:187],v233 offset:5632
TR a[188:189],v233 offset:9728
)ASM"
R"ASM(
TR a[190:191],v233 offset:13824
CV v201,v82,v83
EX v73,v73
MM v[108:111],a[252:255],v[212:215],v[108:111]
CV v202,v84,v85
CV v65,v66,v67
MM v[104:107],a[236:239],v[212:215],v[132:135]
CV v203,v86,v87
CV v66,v68,v69
EX v74,v74
MM v[104:107],a[232:235],v[196:199],v[104:107]
v_pk_fma_f32 v[88:89], v[242:243], v[136:137], v[88:89] op_sel:[1,0,0] op_sel_hi:[1,1,1]
v_pk_fma_f32 v[90:91], v[242:243], v[138:139], v[90:91] op_sel:[1,0,0] op_sel_hi:[1,1,1]
v_pk_fma_f32 v[92:93], v[242:243], v[140:141], v[92:93] op_sel:[1,0,0] op_sel_hi:[1,1,1]
v_pk_fma_f32 v[94:95], v[242:243], v[142:143], v[94:95] op_sel:[1,0,0] op_sel_hi:[1,1,1]
v_pk_mul_f32 v[88:89], v[244:245], v[88:89] op_sel:[1,0] op_sel_hi:[1,1]
v_pk_mul_f32 v[90:91], v[244:245], v[90:91] op_sel:[1,0] op_sel_hi:[1,1]
v_pk_mul_f32 v[92:93], v[244:245], v[92:93] op_sel:[1,0] op_sel_hi:[1,1]
v_pk_mul_f32 v[94:95], v[244:245], v[94:95] op_sel:[1,0] op_sel_hi:[1,1]
MM v[104:107],a[228:231],v[180:183],v[104:107]
EX v75,v75
MM v[104:107],a[224:227],v[164:167],v[104:107]
s_waitcnt vmcnt(12) lgkmcnt(0)
s_barrier
CV v67,v70,v71
EX v76,v76
MM a[0:3],a[128:131],v[0:3],0
s_add_u32 m0, s65, 0
EX v77,v77
MM a[0:3],a[132:135],v[4:7],a[0:3]
CV v72,v72,v73
CV v73,v74,v75
MM a[8:11],a[132:135],v[12:15],0
EX v78,v78
MM a[8:11],a[128:131],v[8:11],a[8:11]
CV v204,v88,v89
CV v205,v90,v91
MM a[12:15],a[136:139],v[8:11],0
EX v79,v79
MM a[12:15],a[140:143],v[12:15],a[12:15]
RD a[192:195],v224 offset:32768
CV v206,v92,v93
MM a[4:7],a[140:143],v[4:7],0
EX v96,v96
MM a[4:7],a[136:139],v[0:3],a[4:7]
CV v207,v94,v95
v_xor_b32 v143, 384, v253
MM a[32:35],a[144:147],v[0:3],0
EX v97,v97
MM a[32:35],a[148:151],v[4:7],a[32:35]
RD v[136:139],v143 offset:0
RD a[196:199],v225 offset:32768
EX v98,v98
MM a[40:43],a[148:151],v[12:15],0
buffer_load_dwordx4 v228, s[8:11], s64 offen offset:0 lds
MM a[40:43],a[144:147],v[8:11],a[40:43]
RD v[140:143],v143 offset:16
RD a[200:203],v226 offset:32768
CV v74,v76,v77
EX v99,v99
MM a[44:47],a[152:155],v[8:11],0
buffer_load_dwordx4 v229, s[8:11], s64 offen offset:1024 lds
MM a[44:47],a[156:159],v[12:15],a[44:47]
RD a[204:207],v227 offset:32768
EX v100,v100
MM a[36:39],a[156:159],v[4:7],0
EX v101,v101
CV v75,v78,v79
MM a[36:39],a[152:155],v[0:3],a[36:39]
s_waitcnt lgkmcnt(2)
RD a[208:211],v224 offset:36864
v_pk_fma_f32 v[112:113], v[242:243], v[136:137], v[112:113] op_sel:[0,0,0] op_sel_hi:[0,1,1]
v_pk_fma_f32 v[114:115], v[242:243], v[138:139], v[114:115] op_sel:[0,0,0] op_sel_hi:[0,1,1]
v_pk_fma_f32 v[116:117], v[242:243], v[140:141], v[116:117] op_sel:[0,0,0] op_sel_hi:[0,1,1]
v_pk_fma_f32 v[118:119], v[242:243], v[142:143], v[118:119] op_sel:[0,0,0] op_sel_hi:[0,1,1]
v_pk_mul_f32 v[112:113], v[244:245], v[112:113] op_sel:[0,0] op_sel_hi:[0,1]
v_pk_mul_f32 v[114:115], v[244:245], v[114:115] op_sel:[0,0] op_sel_hi:[0,1]
v_pk_mul_f32 v[116:117], v[244:245], v[116:117] op_sel:[0,0] op_sel_hi:[0,1]
v_pk_mul_f32 v[118:119], v[244:245], v[118:119] op_sel:[0,0] op_sel_hi:[0,1]
MM a[64:67],a[160:163],v[0:3],0
EX v102,v102
MM a[64:67],a[164:167],v[4:7],a[64:67]
buffer_load_dwordx4 v230, s[8:11], s64 offen offset:2048 lds
MM a[72:75],a[164:167],v[12:15],0
RD a[212:215],v225 offset:36864
EX v103,v103
CV v216,v112,v113
CV v68,v96,v97
MM a[72:75],a[160:163],v[8:11],a[72:75]
RD a[216:219],v226 offset:36864
CV v217,v114,v115
EX v104,v104
MM a[76:79],a[168:171],v[8:11],0
RD a[220:223],v227 offset:36864
CV v218,v116,v117
MM a[76:79],a[172:175],v[12:15],a[76:79]
EX v105,v105
CV v219,v118,v119
CV v69,v98,v99
MM a[68:71],a[172:175],v[4:7],0
buffer_load_dwordx4 v231, s[8:11], s64 offen offset:3072 lds
MM a[68:71],a[168:171],v[0:3],a[68:71]
s_add_u32 m0, s65, 65536
RD a[224:227],v224 offset:40960
v_pk_fma_f32 v[120:121], v[242:243], v[136:137], v[120:121] op_sel:[1,0,0] op_sel_hi:[1,1,1]
v_pk_fma_f32 v[122:123], v[242:243], v[138:139], v[122:123] op_sel:[1,0,0] op_sel_hi:[1,1,1]
v_pk_fma_f32 v[124:125], v[242:243], v[140:141], v[124:125] op_sel:[1,0,0] op_sel_hi:[1,1,1]
v_pk_fma_f32 v[126:127], v[242:243], v[142:143], v[126:127] op_sel:[1,0,0] op_sel_hi:[1,1,1]
v_pk_mul_f32 v[120:121], v[244:245], v[120:121] op_sel:[1,0] op_sel_hi:[1,1]
v_pk_mul_f32 v[122:123], v[244:245], v[122:123] op_sel:[1,0] op_sel_hi:[1,1]
v_pk_mul_f32 v[124:125], v[244:245], v[124:125] op_sel:[1,0] op_sel_hi:[1,1]
v_pk_mul_f32 v[126:127], v[244:245], v[126:127] op_sel:[1,0] op_sel_hi:[1,1]
EX v106,v106
MM a[96:99],a[176:179],v[0:3],0
RD a[228:231],v225 offset:40960
EX v107,v107
CV v70,v100,v101
MM a[96:99],a[180:183],v[4:7],a[96:99]
buffer_load_dwordx4 v235, s[12:15], s81 offen offset:0 lds
MM a[104:107],a[180:183],v[12:15],0
RD a[232:235],v226 offset:40960
EX v108,v108
MM a[104:107],a[176:179],v[8:11],a[104:107]
RD a[236:239],v227 offset:40960
CV v71,v102,v103
MM a[108:111],a[184:187],v[8:11],0
EX v109,v109
MM a[108:111],a[188:191],v[12:15],a[108:111]
buffer_load_dwordx4 v235, s[12:15], s64 offen offset:1024 lds
MM a[100:103],a[188:191],v[4:7],0
RD a[240:243],v224 offset:45056
RD a[244:247],v225 offset:45056
RD a[248:251],v226 offset:45056
RD a[252:255],v227 offset:45056
EX v110,v110
CV v220,v120,v121
CV v221,v122,v123
EX v111,v111
CV v222,v124,v125
CV v223,v126,v127
MM a[100:103],a[184:187],v[0:3],a[100:103]
buffer_load_dwordx4 v235, s[12:15], s81 offen offset:2048 lds
v_smfmac_f32_16x16x64_bf16 v[236:239], v[144:147], v[64:71], v234
CV v76,v104,v105
CV v77,v106,v107
CV v78,v108,v109
CV v79,v110,v111
s_nop 1
v_smfmac_f32_16x16x64_bf16 v[236:239], v[148:151], v[72:79], v234
LPG5_%=:
s_cbranch_execz LPG9_%=
s_waitcnt lgkmcnt(0)
buffer_load_dwordx4 v235, s[12:15], s64 offen offset:3072 lds
MM v[0:3],a[192:195],v[160:163],v[128:131]
s_add_u32 s64, s64, 16384
TR a[128:129],v232 offset:16384
TR a[130:131],v232 offset:20480
MM v[0:3],a[196:199],v[176:179],v[0:3]
s_add_u32 s81, s64, 896
TR a[132:133],v232 offset:24576
TR a[134:135],v232 offset:28672
MM v[0:3],a[200:203],v[192:195],v[0:3]
TR a[136:137],v233 offset:16384
TR a[138:139],v233 offset:20480
MM v[0:3],a[204:207],v[208:211],v[0:3]
TR a[140:141],v233 offset:24576
v_add_f32 v247, v236, v237
MM v[4:7],a[220:223],v[208:211],v[128:131]
TR a[142:143],v233 offset:28672
v_cmp_nge_f32 vcc, s71, v247
MM v[4:7],a[216:219],v[192:195],v[4:7]
s_cbranch_vccnz LSITE1_%=
LBACK1_%=:
TR a[144:145],v232 offset:16896
TR a[146:147],v232 offset:20992
MM v[4:7],a[212:215],v[176:179],v[4:7]
s_cmp_lg_u32 %[sb], 0
s_cbranch_scc1 LKA_%=
s_lshl_b32 s90, %[swave], 10
s_add_u32 s90, s90, 81920
buffer_load_dwordx4 v[48:51], v248, s[20:23], s90 offen offset:0 sc1
s_add_u32 s78, s90, 8192
buffer_load_dwordx4 v[56:59], v248, s[20:23], s78 offen offset:0 sc1
LKA_%=:
MM v[4:7],a[208:211],v[160:163],v[4:7]
TR a[148:149],v232 offset:25088
TR a[150:151],v232 offset:29184
MM v[12:15],a[208:211],v[164:167],v[132:135]
EX v0,v0
MM v[12:15],a[212:215],v[180:183],v[12:15]
EX v1,v1
MM v[12:15],a[216:219],v[196:199],v[12:15]
TR a[152:153],v233 offset:16896
CV v0,v0,v1
MM v[12:15],a[220:223],v[212:215],v[12:15]
EX v2,v2
MM v[8:11],a[204:207],v[212:215],v[132:135]
TR a[154:155],v233 offset:20992
TR a[156:157],v233 offset:25088
MM v[8:11],a[200:203],v[196:199],v[8:11]
EX v3,v3
MM v[8:11],a[196:199],v[180:183],v[8:11]
TR a[158:159],v233 offset:29184
CV v1,v2,v3
MM v[8:11],a[192:195],v[164:167],v[8:11]
EX v4,v4
MM v[32:35],a[224:227],v[160:163],v[128:131]
TR a[160:161],v232 offset:17408
TR a[162:163],v232 offset:21504
MM v[32:35],a[228:231],v[176:179],v[32:35]
EX v5,v5
MM v[32:35],a[232:235],v[192:195],v[32:35]
TR a[164:165],v232 offset:25600
CV v2,v4,v5
MM v[32:35],a[236:239],v[208:211],v[32:35]
TR a[166:167],v232 offset:29696
TR a[168:169],v233 offset:17408
MM v[36:39],a[252:255],v[208:211],v[128:131]
EX v6,v6
MM v[36:39],a[248:251],v[192:195],v[36:39]
EX v7,v7
MM v[36:39],a[244:247],v[176:179],v[36:39]
TR a[170:171],v233 offset:21504
CV v3,v6,v7
MM v[36:39],a[240:243],v[160:163],v[36:39]
TR a[172:173],v233 offset:25600
TR a[174:175],v233 offset:29696
MM v[44:47],a[240:243],v[164:167],v[132:135]
EX v8,v8
MM v[44:47],a[244:247],v[180:183],v[44:47]
TR a[176:177],v232 offset:17920
TR a[178:179],v232 offset:22016
MM v[44:47],a[248:251],v[196:199],v[44:47]
EX v9,v9
MM v[44:47],a[252:255],v[212:215],v[44:47]
TR a[180:181],v232 offset:26112
CV v8,v8,v9
MM v[40:43],a[236:239],v[212:215],v[132:135]
EX v10,v10
MM v[40:43],a[232:235],v[196:199],v[40:43]
TR a[182:183],v232 offset:30208
TR a[184:185],v233 offset:17920
MM v[40:43],a[228:231],v[180:183],v[40:43]
EX v11,v11
MM v[40:43],a[224:227],v[164:167],v[40:43]
TR a[186:187],v233 offset:22016
CV v9,v10,v11
MM v[16:19],a[192:195],v[168:171],0
EX v12,v12
MM v[16:19],a[196:199],v[184:187],v[16:19]
TR a[188:189],v233 offset:26112
TR a[190:191],v233 offset:30208
MM v[16:19],a[200:203],v[200:203],v[16:19]
EX v13,v13
MM v[16:19],a[204:207],v[216:219],v[16:19]
EX v14,v14
MM v[24:27],a[192:195],v[172:175],0
EX v15,v15
MM v[24:27],a[196:199],v[188:191],v[24:27]
EX v32,v32
MM v[24:27],a[200:203],v[204:207],v[24:27]
EX v33,v33
MM v[24:27],a[204:207],v[220:223],v[24:27]
s_waitcnt vmcnt(12) lgkmcnt(0)
s_barrier
CV v10,v12,v13
MM a[0:3],a[128:131],v[64:67],a[0:3]
s_add_u32 m0, s65, 16384
RD a[192:195],v224 offset:49152
CV v11,v14,v15
MM a[0:3],a[132:135],v[68:71],a[0:3]
CV v4,v32,v33
v_max3_f32 v154, v16, v17, v18
MM a[8:11],a[132:135],v[76:79],a[8:11]
buffer_load_dwordx4 v228, s[8:11], s64 offen offset:0 lds
MM a[8:11],a[128:131],v[72:75],a[8:11]
RD a[196:199],v225 offset:49152
v_max_f32 v154, v154, v19
MM a[12:15],a[136:139],v[72:75],a[12:15]
EX v34,v34
MM a[12:15],a[140:143],v[76:79],a[12:15]
RD a[200:203],v226 offset:49152
v_mov_b32 v155, v154
MM a[4:7],a[140:143],v[68:71],a[4:7]
s_nop 0
v_permlane16_swap_b32 v154, v155
v_max_f32 v154, v154, v155
MM a[4:7],a[136:139],v[64:67],a[4:7]
EX v35,v35
MM a[32:35],a[144:147],v[64:67],a[32:35]
RD a[204:207],v227 offset:49152
v_mov_b32 v155, v154
MM a[32:35],a[148:151],v[68:71],a[32:35]
s_nop 0
v_permlane32_swap_b32 v154, v155
v_max_f32 v154, v154, v155
MM a[40:43],a[148:151],v[76:79],a[40:43]
EX v36,v36
MM a[40:43],a[144:147],v[72:75],a[40:43]
v_sub_f32 v136, 0, v154
v_sub_f32 v137, 0, v154
MM a[44:47],a[152:155],v[72:75],a[44:47]
EX v37,v37
MM a[44:47],a[156:159],v[76:79],a[44:47]
v_sub_f32 v138, 0, v154
v_sub_f32 v139, 0, v154
MM a[36:39],a[156:159],v[68:71],a[36:39]
buffer_load_dwordx4 v229, s[8:11], s64 offen offset:1024 lds
MM a[36:39],a[152:155],v[64:67],a[36:39]
RD a[208:211],v224 offset:53248
EX v38,v38
v_mov_b32_dpp v152, v144 row_ror:8 row_mask:0xf bank_mask:0xf
v_mov_b32_dpp v153, v145 row_ror:8 row_mask:0xf bank_mask:0xf
MM a[64:67],a[160:163],v[64:67],a[64:67]
EX v39,v39
v_mov_b32_dpp v154, v146 row_ror:8 row_mask:0xf bank_mask:0xf
MM a[64:67],a[164:167],v[68:71],a[64:67]
RD a[212:215],v225 offset:53248
v_mov_b32_dpp v155, v147 row_ror:8 row_mask:0xf bank_mask:0xf
MM a[72:75],a[164:167],v[76:79],a[72:75]
buffer_load_dwordx4 v230, s[8:11], s64 offen offset:2048 lds
MM a[72:75],a[160:163],v[72:75],a[72:75]
RD a[216:219],v226 offset:53248
v_max3_f32 v158, v24, v25, v26
v_max_f32 v158, v158, v27
EX v40,v40
v_mov_b32 v159, v158
CV v5,v34,v35
MM a[76:79],a[168:171],v[72:75],a[76:79]
RD a[220:223],v227 offset:53248
EX v41,v41
v_permlane16_swap_b32 v158, v159
MM a[76:79],a[172:175],v[76:79],a[76:79]
buffer_load_dwordx4 v231, s[8:11], s64 offen offset:3072 lds
MM a[68:71],a[172:175],v[68:71],a[68:71]
s_add_u32 m0, s65, 81920
RD a[224:227],v224 offset:57344
v_max_f32 v158, v158, v159
v_mov_b32 v159, v158
EX v42,v42
MM a[68:71],a[168:171],v[64:67],a[68:71]
v_permlane32_swap_b32 v158, v159
CV v6,v36,v37
v_max_f32 v158, v158, v159
EX v43,v43
MM a[96:99],a[176:179],v[64:67],a[96:99]
RD a[228:231],v225 offset:57344
v_sub_f32 v140, 0, v158
MM a[96:99],a[180:183],v[68:71],a[96:99]
buffer_load_dwordx4 v235, s[12:15], s81 offen offset:0 lds
MM a[104:107],a[180:183],v[76:79],a[104:107]
RD a[232:235],v226 offset:57344
v_sub_f32 v141, 0, v158
v_sub_f32 v142, 0, v158
EX v44,v44
CV v7,v38,v39
MM a[104:107],a[176:179],v[72:75],a[104:107]
v_sub_f32 v143, 0, v158
v_mov_b32_dpp v156, v148 row_ror:8 row_mask:0xf bank_mask:0xf
)ASM"
R"ASM(
EX v45,v45
MM a[108:111],a[184:187],v[72:75],a[108:111]
RD a[236:239],v227 offset:57344
v_mov_b32_dpp v157, v149 row_ror:8 row_mask:0xf bank_mask:0xf
v_mov_b32_dpp v158, v150 row_ror:8 row_mask:0xf bank_mask:0xf
v_mov_b32_dpp v159, v151 row_ror:8 row_mask:0xf bank_mask:0xf
MM a[108:111],a[188:191],v[76:79],a[108:111]
buffer_load_dwordx4 v235, s[12:15], s64 offen offset:1024 lds
MM a[100:103],a[188:191],v[68:71],a[100:103]
RD a[240:243],v224 offset:61440
RD a[244:247],v225 offset:61440
RD a[248:251],v226 offset:61440
RD a[252:255],v227 offset:61440
EX v46,v46
EX v47,v47
MM a[100:103],a[184:187],v[64:67],a[100:103]
buffer_load_dwordx4 v235, s[12:15], s81 offen offset:2048 lds
v_smfmac_f32_16x16x64_bf16 v[236:239], v[144:147], v[0:7], v234
CV v12,v40,v41
CV v13,v42,v43
CV v14,v44,v45
CV v15,v46,v47
s_nop 1
v_smfmac_f32_16x16x64_bf16 v[236:239], v[148:151], v[8:15], v234
s_waitcnt lgkmcnt(0)
buffer_load_dwordx4 v235, s[12:15], s64 offen offset:3072 lds
MM v[64:67],a[192:195],v[160:163],v[128:131]
s_add_u32 s64, s64, 16384
TR a[128:129],v232 offset:32768
s_add_u32 s81, s64, 896
MM v[64:67],a[196:199],v[176:179],v[64:67]
TR a[130:131],v232 offset:36864
TR a[132:133],v232 offset:40960
MM v[64:67],a[200:203],v[192:195],v[64:67]
TR a[134:135],v232 offset:45056
TR a[136:137],v233 offset:32768
MM v[64:67],a[204:207],v[208:211],v[64:67]
TR a[138:139],v233 offset:36864
v_add_f32 v246, v236, v237
MM v[68:71],a[220:223],v[208:211],v[128:131]
TR a[140:141],v233 offset:40960
v_cmp_nge_f32 vcc, s71, v246
MM v[68:71],a[216:219],v[192:195],v[68:71]
s_cbranch_vccnz LSITE2_%=
LBACK2_%=:
TR a[142:143],v233 offset:45056
TR a[144:145],v232 offset:33280
MM v[68:71],a[212:215],v[176:179],v[68:71]
TR a[146:147],v232 offset:37376
TR a[148:149],v232 offset:41472
MM v[68:71],a[208:211],v[160:163],v[68:71]
TR a[150:151],v232 offset:45568
TR a[152:153],v233 offset:33280
MM v[76:79],a[208:211],v[164:167],v[132:135]
EX v64,v64
MM v[76:79],a[212:215],v[180:183],v[76:79]
EX v65,v65
MM v[76:79],a[216:219],v[196:199],v[76:79]
TR a[154:155],v233 offset:37376
CV v64,v64,v65
MM v[76:79],a[220:223],v[212:215],v[76:79]
EX v66,v66
MM v[72:75],a[204:207],v[212:215],v[132:135]
EX v67,v67
MM v[72:75],a[200:203],v[196:199],v[72:75]
TR a[156:157],v233 offset:41472
CV v65,v66,v67
MM v[72:75],a[196:199],v[180:183],v[72:75]
EX v68,v68
MM v[72:75],a[192:195],v[164:167],v[72:75]
EX v69,v69
MM v[80:83],a[192:195],v[168:171],v[136:139]
TR a[158:159],v233 offset:45568
CV v66,v68,v69
MM v[80:83],a[196:199],v[184:187],v[80:83]
EX v70,v70
MM v[80:83],a[200:203],v[200:203],v[80:83]
EX v71,v71
MM v[80:83],a[204:207],v[216:219],v[80:83]
TR a[160:161],v232 offset:33792
CV v67,v70,v71
MM v[84:87],a[220:223],v[216:219],v[136:139]
s_cmp_lg_u32 %[sb], 0
s_cbranch_scc1 LKB_%=
s_waitcnt vmcnt(8)
v_xor_b32 v48, s69, v48
v_xor_b32 v49, s69, v49
v_xor_b32 v50, s69, v50
v_xor_b32 v51, s69, v51
v_xor_b32 v56, s69, v56
v_xor_b32 v57, s69, v57
v_xor_b32 v58, s69, v58
v_xor_b32 v59, s69, v59
v_or3_b32 v52, v48, v49, v50
v_or3_b32 v52, v52, v51, v56
v_or3_b32 v52, v52, v57, v58
v_or_b32 v52, v52, v59
v_cmp_ne_u32 vcc, 0, v52
s_cbranch_vccnz LRFR_%=
LRFRb_%=:
s_lshl_b32 s90, %[swave], 11
s_add_u32 s78, s90, %[slds]
s_add_u32 s78, s78, 155648
s_add_u32 s90, s90, 53248
s_mov_b32 m0, s78
s_nop 0
buffer_load_dwordx4 v248, s[20:23], s90 offen offset:0 sc1 lds
buffer_load_dwordx4 v248, s[20:23], s90 offen offset:1024 sc1 lds
LKB_%=:
MM v[84:87],a[216:219],v[200:203],v[84:87]
EX v72,v72
MM v[84:87],a[212:215],v[184:187],v[84:87]
EX v73,v73
MM v[84:87],a[208:211],v[168:171],v[84:87]
EX v74,v74
MM v[92:95],a[208:211],v[172:175],v[140:143]
EX v75,v75
MM v[92:95],a[212:215],v[188:191],v[92:95]
CV v72,v72,v73
CV v73,v74,v75
MM v[92:95],a[216:219],v[204:207],v[92:95]
EX v76,v76
MM v[92:95],a[220:223],v[220:223],v[92:95]
EX v77,v77
MM v[88:91],a[204:207],v[220:223],v[140:143]
EX v78,v78
MM v[88:91],a[200:203],v[204:207],v[88:91]
EX v79,v79
MM v[88:91],a[196:199],v[188:191],v[88:91]
CV v74,v76,v77
CV v75,v78,v79
MM v[88:91],a[192:195],v[172:175],v[88:91]
EX v80,v80
MM v[96:99],a[224:227],v[160:163],v[128:131]
EX v81,v81
MM v[96:99],a[228:231],v[176:179],v[96:99]
TR a[162:163],v232 offset:37888
TR a[164:165],v232 offset:41984
MM v[96:99],a[232:235],v[192:195],v[96:99]
EX v82,v82
MM v[96:99],a[236:239],v[208:211],v[96:99]
EX v83,v83
MM v[100:103],a[252:255],v[208:211],v[128:131]
TR a[166:167],v232 offset:46080
CV v80,v80,v81
MM v[100:103],a[248:251],v[192:195],v[100:103]
EX v84,v84
MM v[100:103],a[244:247],v[176:179],v[100:103]
EX v85,v85
MM v[100:103],a[240:243],v[160:163],v[100:103]
TR a[168:169],v233 offset:33792
CV v81,v82,v83
MM v[108:111],a[240:243],v[164:167],v[132:135]
EX v86,v86
MM v[108:111],a[244:247],v[180:183],v[108:111]
EX v87,v87
MM v[108:111],a[248:251],v[196:199],v[108:111]
TR a[170:171],v233 offset:37888
CV v82,v84,v85
MM v[108:111],a[252:255],v[212:215],v[108:111]
EX v88,v88
MM v[104:107],a[236:239],v[212:215],v[132:135]
EX v89,v89
MM v[104:107],a[232:235],v[196:199],v[104:107]
TR a[172:173],v233 offset:41984
TR a[174:175],v233 offset:46080
MM v[104:107],a[228:231],v[180:183],v[104:107]
EX v90,v90
MM v[104:107],a[224:227],v[164:167],v[104:107]
EX v91,v91
MM v[112:115],a[224:227],v[168:171],v[136:139]
TR a[176:177],v232 offset:34304
CV v83,v86,v87
MM v[112:115],a[228:231],v[184:187],v[112:115]
EX v92,v92
MM v[112:115],a[232:235],v[200:203],v[112:115]
EX v93,v93
MM v[112:115],a[236:239],v[216:219],v[112:115]
TR a[178:179],v232 offset:38400
CV v88,v88,v89
MM v[116:119],a[252:255],v[216:219],v[136:139]
EX v94,v94
MM v[116:119],a[248:251],v[200:203],v[116:119]
EX v95,v95
MM v[116:119],a[244:247],v[184:187],v[116:119]
TR a[180:181],v232 offset:42496
TR a[182:183],v232 offset:46592
MM v[116:119],a[240:243],v[168:171],v[116:119]
EX v96,v96
MM v[124:127],a[240:243],v[172:175],v[140:143]
EX v97,v97
CV v89,v90,v91
MM v[124:127],a[244:247],v[188:191],v[124:127]
EX v98,v98
MM v[124:127],a[248:251],v[204:207],v[124:127]
TR a[184:185],v233 offset:34304
TR a[186:187],v233 offset:38400
TR a[188:189],v233 offset:42496
TR a[190:191],v233 offset:46592
MM v[124:127],a[252:255],v[220:223],v[124:127]
EX v99,v99
CV v90,v92,v93
MM v[120:123],a[236:239],v[220:223],v[140:143]
EX v100,v100
MM v[120:123],a[232:235],v[204:207],v[120:123]
EX v101,v101
CV v91,v94,v95
MM v[120:123],a[228:231],v[188:191],v[120:123]
EX v102,v102
MM v[120:123],a[224:227],v[172:175],v[120:123]
s_waitcnt vmcnt(12) lgkmcnt(0)
s_barrier
CV v68,v96,v97
MM a[0:3],a[128:131],v[0:3],a[0:3]
s_add_u32 m0, s65, 32768
EX v103,v103
MM a[0:3],a[132:135],v[4:7],a[0:3]
EX v104,v104
MM a[8:11],a[132:135],v[12:15],a[8:11]
CV v69,v98,v99
CV v70,v100,v101
MM a[8:11],a[128:131],v[8:11],a[8:11]
EX v105,v105
MM a[12:15],a[136:139],v[8:11],a[12:15]
EX v106,v106
MM a[12:15],a[140:143],v[12:15],a[12:15]
EX v107,v107
MM a[4:7],a[140:143],v[4:7],a[4:7]
EX v108,v108
MM a[4:7],a[136:139],v[0:3],a[4:7]
RD a[192:195],v224 offset:0
CV v71,v102,v103
MM a[32:35],a[144:147],v[0:3],a[32:35]
EX v109,v109
MM a[32:35],a[148:151],v[4:7],a[32:35]
buffer_load_dwordx4 v228, s[8:11], s64 offen offset:0 lds
MM a[40:43],a[148:151],v[12:15],a[40:43]
RD a[196:199],v225 offset:0
EX v110,v110
MM a[40:43],a[144:147],v[8:11],a[40:43]
EX v111,v111
CV v76,v104,v105
MM a[44:47],a[152:155],v[8:11],a[44:47]
RD a[200:203],v226 offset:0
EX v112,v112
MM a[44:47],a[156:159],v[12:15],a[44:47]
buffer_load_dwordx4 v229, s[8:11], s64 offen offset:1024 lds
MM a[36:39],a[156:159],v[4:7],a[36:39]
RD a[204:207],v227 offset:0
EX v113,v113
CV v77,v106,v107
MM a[36:39],a[152:155],v[0:3],a[36:39]
EX v114,v114
MM a[64:67],a[160:163],v[0:3],a[64:67]
RD a[208:211],v224 offset:4096
EX v115,v115
CV v78,v108,v109
MM a[64:67],a[164:167],v[4:7],a[64:67]
buffer_load_dwordx4 v230, s[8:11], s64 offen offset:2048 lds
MM a[72:75],a[164:167],v[12:15],a[72:75]
RD a[212:215],v225 offset:4096
EX v116,v116
MM a[72:75],a[160:163],v[8:11],a[72:75]
EX v117,v117
CV v79,v110,v111
MM a[76:79],a[168:171],v[8:11],a[76:79]
RD a[216:219],v226 offset:4096
CV v84,v112,v113
EX v118,v118
MM a[76:79],a[172:175],v[12:15],a[76:79]
buffer_load_dwordx4 v231, s[8:11], s64 offen offset:3072 lds
MM a[68:71],a[172:175],v[4:7],a[68:71]
s_add_u32 m0, s65, 98304
RD a[220:223],v227 offset:4096
EX v119,v119
MM a[68:71],a[168:171],v[0:3],a[68:71]
EX v120,v120
MM a[96:99],a[176:179],v[0:3],a[96:99]
RD a[224:227],v224 offset:8192
CV v85,v114,v115
EX v121,v121
MM a[96:99],a[180:183],v[4:7],a[96:99]
RD a[228:231],v225 offset:8192
CV v86,v116,v117
MM a[104:107],a[180:183],v[12:15],a[104:107]
EX v122,v122
MM a[104:107],a[176:179],v[8:11],a[104:107]
buffer_load_dwordx4 v235, s[12:15], s81 offen offset:0 lds
MM a[108:111],a[184:187],v[8:11],a[108:111]
RD a[232:235],v226 offset:8192
EX v123,v123
MM a[108:111],a[188:191],v[12:15],a[108:111]
EX v124,v124
MM a[100:103],a[188:191],v[4:7],a[100:103]
RD a[236:239],v227 offset:8192
EX v125,v125
CV v87,v118,v119
MM a[100:103],a[184:187],v[0:3],a[100:103]
buffer_load_dwordx4 v235, s[12:15], s64 offen offset:1024 lds
v_smfmac_f32_16x16x64_bf16 v[236:239], v[144:147], v[64:71], v234
RD a[240:243],v224 offset:12288
RD a[244:247],v225 offset:12288
RD a[248:251],v226 offset:12288
RD a[252:255],v227 offset:12288
EX v126,v126
EX v127,v127
v_smfmac_f32_16x16x64_bf16 v[236:239], v[148:151], v[72:79], v234
CV v92,v120,v121
v_smfmac_f32_16x16x64_bf16 v[236:239], v[152:155], v[80:87], v234
CV v93,v122,v123
CV v94,v124,v125
CV v95,v126,v127
s_nop 1
v_smfmac_f32_16x16x64_bf16 v[236:239], v[156:159], v[88:95], v234
s_waitcnt lgkmcnt(0)
buffer_load_dwordx4 v235, s[12:15], s81 offen offset:2048 lds
buffer_load_dwordx4 v235, s[12:15], s64 offen offset:3072 lds
MM v[0:3],a[192:195],v[160:163],v[128:131]
s_add_u32 s64, s64, 16384
TR a[128:129],v232 offset:49152
s_add_u32 s81, s64, 896
MM v[0:3],a[196:199],v[176:179],v[0:3]
TR a[130:131],v232 offset:53248
TR a[132:133],v232 offset:57344
MM v[0:3],a[200:203],v[192:195],v[0:3]
TR a[134:135],v232 offset:61440
TR a[136:137],v233 offset:49152
MM v[0:3],a[204:207],v[208:211],v[0:3]
TR a[138:139],v233 offset:53248
v_add_f32 v247, v236, v237
MM v[4:7],a[220:223],v[208:211],v[128:131]
TR a[140:141],v233 offset:57344
v_cmp_nge_f32 vcc, s71, v247
MM v[4:7],a[216:219],v[192:195],v[4:7]
s_cbranch_vccnz LSITE3_%=
LBACK3_%=:
MM v[4:7],a[212:215],v[176:179],v[4:7]
s_nop 0
v_lshrrev_b32 v252, 6, v248
v_mul_u32_u24 v252, 3456, v252
MM v[4:7],a[208:211],v[160:163],v[4:7]
s_nop 0
v_and_b32 v251, 48, v248
v_add_u32 v252, v252, v251
MM v[12:15],a[208:211],v[164:167],v[132:135]
s_and_b32 s90, %[swave], 1
EX v0,v0
MM v[12:15],a[212:215],v[180:183],v[12:15]
s_mul_i32 s90, s90, 276480
EX v1,v1
MM v[12:15],a[216:219],v[196:199],v[12:15]
s_bfe_u32 s91, s48, 131077
TR a[142:143],v233 offset:61440
CV v0,v0,v1
MM v[12:15],a[220:223],v[212:215],v[12:15]
s_mul_i32 s91, s91, 552960
EX v2,v2
MM v[8:11],a[204:207],v[212:215],v[132:135]
s_add_u32 s90, s90, s91
TR a[144:145],v232 offset:49664
TR a[146:147],v232 offset:53760
MM v[8:11],a[200:203],v[196:199],v[8:11]
s_and_b32 s91, s48, 0xffffff80
EX v3,v3
MM v[8:11],a[196:199],v[180:183],v[8:11]
s_add_u32 s90, s90, s91
TR a[148:149],v232 offset:57856
CV v1,v2,v3
MM v[8:11],a[192:195],v[164:167],v[8:11]
s_and_b32 s91, s48, 16
EX v4,v4
MM v[16:19],a[192:195],v[168:171],v[136:139]
s_lshl_b32 s91, s91, 2
EX v5,v5
MM v[16:19],a[196:199],v[184:187],v[16:19]
s_add_u32 s66, s90, s91
TR a[150:151],v232 offset:61952
CV v2,v4,v5
MM v[16:19],a[200:203],v[200:203],v[16:19]
s_mul_i32 s91, %[swave], 5120
EX v6,v6
MM v[16:19],a[204:207],v[216:219],v[16:19]
s_add_u32 s91, s91, %[slds]
TR a[152:153],v233 offset:49664
TR a[154:155],v233 offset:53760
MM v[20:23],a[220:223],v[216:219],v[136:139]
s_add_u32 s67, s91, 131072
EX v7,v7
MM v[20:23],a[216:219],v[200:203],v[20:23]
s_add_u32 s85, s66, 221184
TR a[156:157],v233 offset:57856
CV v3,v6,v7
MM v[20:23],a[212:215],v[184:187],v[20:23]
s_add_u32 s86, s67, 4096
EX v8,v8
MM v[20:23],a[208:211],v[168:171],v[20:23]
s_nop 0
TR a[158:159],v233 offset:61952
TR a[160:161],v232 offset:50176
MM v[28:31],a[208:211],v[172:175],v[140:143]
)ASM"
R"ASM(
LPG6_%=:
s_cbranch_execz LPG10_%=
EX v9,v9
MM v[28:31],a[212:215],v[188:191],v[28:31]
TR a[162:163],v232 offset:54272
CV v8,v8,v9
MM v[28:31],a[216:219],v[204:207],v[28:31]
EX v10,v10
MM v[28:31],a[220:223],v[220:223],v[28:31]
EX v11,v11
MM v[24:27],a[204:207],v[220:223],v[140:143]
TR a[164:165],v232 offset:58368
CV v9,v10,v11
MM v[24:27],a[200:203],v[204:207],v[24:27]
EX v12,v12
MM v[24:27],a[196:199],v[188:191],v[24:27]
TR a[166:167],v232 offset:62464
TR a[168:169],v233 offset:50176
MM v[24:27],a[192:195],v[172:175],v[24:27]
EX v13,v13
MM v[32:35],a[224:227],v[160:163],v[128:131]
TR a[170:171],v233 offset:54272
CV v10,v12,v13
MM v[32:35],a[228:231],v[176:179],v[32:35]
EX v14,v14
MM v[32:35],a[232:235],v[192:195],v[32:35]
EX v15,v15
MM v[32:35],a[236:239],v[208:211],v[32:35]
TR a[172:173],v233 offset:58368
CV v11,v14,v15
MM v[36:39],a[252:255],v[208:211],v[128:131]
EX v16,v16
MM v[36:39],a[248:251],v[192:195],v[36:39]
TR a[174:175],v233 offset:62464
TR a[176:177],v232 offset:50688
MM v[36:39],a[244:247],v[176:179],v[36:39]
EX v17,v17
MM v[36:39],a[240:243],v[160:163],v[36:39]
TR a[178:179],v232 offset:54784
CV v16,v16,v17
MM v[44:47],a[240:243],v[164:167],v[132:135]
EX v18,v18
MM v[44:47],a[244:247],v[180:183],v[44:47]
EX v19,v19
MM v[44:47],a[248:251],v[196:199],v[44:47]
TR a[180:181],v232 offset:58880
CV v17,v18,v19
MM v[44:47],a[252:255],v[212:215],v[44:47]
EX v20,v20
MM v[40:43],a[236:239],v[212:215],v[132:135]
TR a[182:183],v232 offset:62976
TR a[184:185],v233 offset:50688
MM v[40:43],a[232:235],v[196:199],v[40:43]
EX v21,v21
MM v[40:43],a[228:231],v[180:183],v[40:43]
TR a[186:187],v233 offset:54784
CV v18,v20,v21
MM v[40:43],a[224:227],v[164:167],v[40:43]
EX v22,v22
MM v[48:51],a[224:227],v[168:171],v[136:139]
TR a[188:189],v233 offset:58880
TR a[190:191],v233 offset:62976
MM v[48:51],a[228:231],v[184:187],v[48:51]
EX v23,v23
MM v[48:51],a[232:235],v[200:203],v[48:51]
CV v19,v22,v23
MM v[48:51],a[236:239],v[216:219],v[48:51]
EX v24,v24
MM v[52:55],a[252:255],v[216:219],v[136:139]
EX v25,v25
MM v[52:55],a[248:251],v[200:203],v[52:55]
EX v26,v26
MM v[52:55],a[244:247],v[184:187],v[52:55]
EX v27,v27
MM v[52:55],a[240:243],v[168:171],v[52:55]
EX v28,v28
MM v[60:63],a[240:243],v[172:175],v[140:143]
EX v29,v29
MM v[60:63],a[244:247],v[188:191],v[60:63]
EX v30,v30
MM v[60:63],a[248:251],v[204:207],v[60:63]
EX v31,v31
MM v[60:63],a[252:255],v[220:223],v[60:63]
EX v32,v32
MM v[56:59],a[236:239],v[220:223],v[140:143]
EX v33,v33
MM v[56:59],a[232:235],v[204:207],v[56:59]
EX v34,v34
MM v[56:59],a[228:231],v[188:191],v[56:59]
EX v35,v35
MM v[56:59],a[224:227],v[172:175],v[56:59]
s_waitcnt vmcnt(8) lgkmcnt(0)
s_barrier
MM a[0:3],a[128:131],v[64:67],a[0:3]
s_add_u32 m0, s65, 49152
CV v24,v24,v25
CV v25,v26,v27
MM a[0:3],a[132:135],v[68:71],a[0:3]
RD a[192:195],v224 offset:16384
CV v26,v28,v29
MM a[8:11],a[132:135],v[76:79],a[8:11]
RD a[196:199],v225 offset:16384
CV v27,v30,v31
MM a[8:11],a[128:131],v[72:75],a[8:11]
buffer_load_dwordx4 v228, s[8:11], s64 offen offset:0 lds
MM a[16:19],a[128:131],v[80:83],0
RD a[200:203],v226 offset:16384
CV v4,v32,v33
MM a[16:19],a[132:135],v[84:87],a[16:19]
RD a[204:207],v227 offset:16384
CV v5,v34,v35
MM a[24:27],a[132:135],v[92:95],0
buffer_load_dwordx4 v229, s[8:11], s64 offen offset:1024 lds
MM a[24:27],a[128:131],v[88:91],a[24:27]
RD a[208:211],v224 offset:20480
RD a[212:215],v225 offset:20480
MM a[28:31],a[136:139],v[88:91],0
buffer_load_dwordx4 v230, s[8:11], s64 offen offset:2048 lds
MM a[28:31],a[140:143],v[92:95],a[28:31]
EX v36,v36
MM a[20:23],a[140:143],v[84:87],0
EX v37,v37
MM a[20:23],a[136:139],v[80:83],a[20:23]
RD a[216:219],v226 offset:20480
CV v6,v36,v37
MM a[12:15],a[136:139],v[72:75],a[12:15]
EX v38,v38
MM a[12:15],a[140:143],v[76:79],a[12:15]
RD a[220:223],v227 offset:20480
RD a[224:227],v224 offset:24576
MM a[4:7],a[140:143],v[68:71],a[4:7]
EX v39,v39
MM a[4:7],a[136:139],v[64:67],a[4:7]
RD a[228:231],v225 offset:24576
CV v7,v38,v39
MM a[32:35],a[144:147],v[64:67],a[32:35]
EX v40,v40
MM a[32:35],a[148:151],v[68:71],a[32:35]
buffer_load_dwordx4 v231, s[8:11], s64 offen offset:3072 lds
MM a[40:43],a[148:151],v[76:79],a[40:43]
s_add_u32 m0, s65, 114688
EX v41,v41
MM a[40:43],a[144:147],v[72:75],a[40:43]
RD a[232:235],v226 offset:24576
CV v12,v40,v41
MM a[48:51],a[144:147],v[80:83],0
EX v42,v42
MM a[48:51],a[148:151],v[84:87],a[48:51]
EX v43,v43
MM a[56:59],a[148:151],v[92:95],0
RD a[236:239],v227 offset:24576
CV v13,v42,v43
MM a[56:59],a[144:147],v[88:91],a[56:59]
EX v44,v44
MM a[60:63],a[152:155],v[88:91],0
buffer_load_dwordx4 v235, s[12:15], s81 offen offset:0 lds
MM a[60:63],a[156:159],v[92:95],a[60:63]
EX v45,v45
MM a[52:55],a[156:159],v[84:87],0
RD a[240:243],v224 offset:28672
CV v14,v44,v45
MM a[52:55],a[152:155],v[80:83],a[52:55]
EX v46,v46
MM a[44:47],a[152:155],v[72:75],a[44:47]
EX v47,v47
MM a[44:47],a[156:159],v[76:79],a[44:47]
RD a[244:247],v225 offset:28672
CV v15,v46,v47
MM a[36:39],a[156:159],v[68:71],a[36:39]
buffer_load_dwordx4 v235, s[12:15], s64 offen offset:1024 lds
MM a[36:39],a[152:155],v[64:67],a[36:39]
EX v48,v48
MM a[64:67],a[160:163],v[64:67],a[64:67]
EX v49,v49
MM a[64:67],a[164:167],v[68:71],a[64:67]
RD a[248:251],v226 offset:28672
CV v20,v48,v49
MM a[72:75],a[164:167],v[76:79],a[72:75]
EX v50,v50
MM a[72:75],a[160:163],v[72:75],a[72:75]
EX v51,v51
MM a[80:83],a[160:163],v[80:83],0
RD a[252:255],v227 offset:28672
CV v21,v50,v51
MM a[80:83],a[164:167],v[84:87],a[80:83]
buffer_load_dwordx4 v235, s[12:15], s81 offen offset:2048 lds
MM a[88:91],a[164:167],v[92:95],0
EX v52,v52
MM a[88:91],a[160:163],v[88:91],a[88:91]
EX v53,v53
MM a[92:95],a[168:171],v[88:91],0
EX v54,v54
MM a[92:95],a[172:175],v[92:95],a[92:95]
buffer_load_dwordx4 v235, s[12:15], s64 offen offset:3072 lds
MM a[84:87],a[172:175],v[84:87],0
EX v55,v55
MM a[84:87],a[168:171],v[80:83],a[84:87]
EX v56,v56
MM a[76:79],a[168:171],v[72:75],a[76:79]
EX v57,v57
MM a[76:79],a[172:175],v[76:79],a[76:79]
EX v58,v58
MM a[68:71],a[172:175],v[68:71],a[68:71]
EX v59,v59
MM a[68:71],a[168:171],v[64:67],a[68:71]
EX v60,v60
MM a[96:99],a[176:179],v[64:67],a[96:99]
EX v61,v61
MM a[96:99],a[180:183],v[68:71],a[96:99]
EX v62,v62
MM a[104:107],a[180:183],v[76:79],a[104:107]
EX v63,v63
MM a[104:107],a[176:179],v[72:75],a[104:107]
CV v22,v52,v53
CV v23,v54,v55
MM a[112:115],a[176:179],v[80:83],0
CV v28,v56,v57
CV v29,v58,v59
MM a[112:115],a[180:183],v[84:87],a[112:115]
CV v30,v60,v61
CV v31,v62,v63
MM a[120:123],a[180:183],v[92:95],0
MM a[120:123],a[176:179],v[88:91],a[120:123]
MM a[124:127],a[184:187],v[88:91],0
MM a[124:127],a[188:191],v[92:95],a[124:127]
MM a[116:119],a[188:191],v[84:87],0
MM a[116:119],a[184:187],v[80:83],a[116:119]
MM a[108:111],a[184:187],v[72:75],a[108:111]
MM a[108:111],a[188:191],v[76:79],a[108:111]
MM a[100:103],a[188:191],v[68:71],a[100:103]
MM a[100:103],a[184:187],v[64:67],a[100:103]
v_smfmac_f32_16x16x64_bf16 v[236:239], v[144:147], v[0:7], v234
v_smfmac_f32_16x16x64_bf16 v[236:239], v[148:151], v[8:15], v234
v_smfmac_f32_16x16x64_bf16 v[236:239], v[152:155], v[16:23], v234
v_smfmac_f32_16x16x64_bf16 v[236:239], v[156:159], v[24:31], v234
s_waitcnt lgkmcnt(0)
s_cmp_lg_u32 %[sb], 0
s_cbranch_scc1 LXS_%=
s_getpc_b64 s[98:99]
LTA1_%=:
s_lshl_b32 s73, %[swave], 12
s_add_u32 s73, s73, LPG26_%=-LTA1_%=
s_ashr_i32 s74, s73, 31
s_add_u32 s98, s98, s73
s_addc_u32 s99, s99, s74
s_mov_b64 exec, 0
s_setpc_b64 s[98:99]
LTR1_%=:
s_mov_b64 exec, -1
s_branch LXP_%=
LXS_%=:
s_mov_b32 s70, 2
LLA_%=:
MM v[64:67],a[192:195],v[160:163],v[128:131]
s_add_u32 s64, s64, 16384
TR a[128:129],v232 offset:0
s_add_u32 s81, s64, 896
MM v[64:67],a[196:199],v[176:179],v[64:67]
TR a[130:131],v232 offset:4096
TR a[132:133],v232 offset:8192
MM v[64:67],a[200:203],v[192:195],v[64:67]
TR a[134:135],v232 offset:12288
TR a[136:137],v233 offset:0
MM v[64:67],a[204:207],v[208:211],v[64:67]
TR a[138:139],v233 offset:4096
v_add_f32 v246, v236, v237
MM v[68:71],a[220:223],v[208:211],v[128:131]
TR a[140:141],v233 offset:8192
v_cmp_nge_f32 vcc, s71, v246
MM v[68:71],a[216:219],v[192:195],v[68:71]
s_cbranch_vccnz LSITE8_%=
LBACK8_%=:
MM v[68:71],a[212:215],v[176:179],v[68:71]
s_mov_b32 m0, s67
TR a[142:143],v233 offset:12288
TR a[144:145],v232 offset:512
MM v[68:71],a[208:211],v[160:163],v[68:71]
buffer_load_dwordx4 v252, s[36:39], s66 offen offset:0 lds
MM v[76:79],a[208:211],v[164:167],v[132:135]
s_add_u32 s66, s66, 55296
EX v64,v64
MM v[76:79],a[212:215],v[180:183],v[76:79]
s_min_u32 s66, s66, s85
EX v65,v65
MM v[76:79],a[216:219],v[196:199],v[76:79]
s_add_u32 s67, s67, 1024
TR a[146:147],v232 offset:4608
CV v64,v64,v65
MM v[76:79],a[220:223],v[212:215],v[76:79]
s_min_u32 s67, s67, s86
EX v66,v66
MM v[72:75],a[204:207],v[212:215],v[132:135]
EX v67,v67
MM v[72:75],a[200:203],v[196:199],v[72:75]
TR a[148:149],v232 offset:8704
CV v65,v66,v67
MM v[72:75],a[196:199],v[180:183],v[72:75]
EX v68,v68
MM v[72:75],a[192:195],v[164:167],v[72:75]
TR a[150:151],v232 offset:12800
TR a[152:153],v233 offset:512
MM v[80:83],a[192:195],v[168:171],v[136:139]
EX v69,v69
MM v[80:83],a[196:199],v[184:187],v[80:83]
TR a[154:155],v233 offset:4608
CV v66,v68,v69
MM v[80:83],a[200:203],v[200:203],v[80:83]
EX v70,v70
MM v[80:83],a[204:207],v[216:219],v[80:83]
EX v71,v71
MM v[84:87],a[220:223],v[216:219],v[136:139]
TR a[156:157],v233 offset:8704
CV v67,v70,v71
MM v[84:87],a[216:219],v[200:203],v[84:87]
EX v72,v72
MM v[84:87],a[212:215],v[184:187],v[84:87]
TR a[158:159],v233 offset:12800
TR a[160:161],v232 offset:1024
MM v[84:87],a[208:211],v[168:171],v[84:87]
EX v73,v73
MM v[92:95],a[208:211],v[172:175],v[140:143]
TR a[162:163],v232 offset:5120
CV v72,v72,v73
MM v[92:95],a[212:215],v[188:191],v[92:95]
EX v74,v74
MM v[92:95],a[216:219],v[204:207],v[92:95]
EX v75,v75
MM v[92:95],a[220:223],v[220:223],v[92:95]
TR a[164:165],v232 offset:9216
CV v73,v74,v75
MM v[88:91],a[204:207],v[220:223],v[140:143]
EX v76,v76
MM v[88:91],a[200:203],v[204:207],v[88:91]
TR a[166:167],v232 offset:13312
TR a[168:169],v233 offset:1024
MM v[88:91],a[196:199],v[188:191],v[88:91]
EX v77,v77
MM v[88:91],a[192:195],v[172:175],v[88:91]
TR a[170:171],v233 offset:5120
CV v74,v76,v77
MM v[96:99],a[224:227],v[160:163],v[128:131]
EX v78,v78
MM v[96:99],a[228:231],v[176:179],v[96:99]
EX v79,v79
MM v[96:99],a[232:235],v[192:195],v[96:99]
TR a[172:173],v233 offset:9216
CV v75,v78,v79
MM v[96:99],a[236:239],v[208:211],v[96:99]
TR a[174:175],v233 offset:13312
TR a[176:177],v232 offset:1536
MM v[100:103],a[252:255],v[208:211],v[128:131]
EX v80,v80
MM v[100:103],a[248:251],v[192:195],v[100:103]
EX v81,v81
MM v[100:103],a[244:247],v[176:179],v[100:103]
TR a[178:179],v232 offset:5632
CV v80,v80,v81
MM v[100:103],a[240:243],v[160:163],v[100:103]
EX v82,v82
MM v[108:111],a[240:243],v[164:167],v[132:135]
TR a[180:181],v232 offset:9728
TR a[182:183],v232 offset:13824
MM v[108:111],a[244:247],v[180:183],v[108:111]
EX v83,v83
MM v[108:111],a[248:251],v[196:199],v[108:111]
TR a[184:185],v233 offset:1536
CV v81,v82,v83
MM v[108:111],a[252:255],v[212:215],v[108:111]
EX v84,v84
MM v[104:107],a[236:239],v[212:215],v[132:135]
EX v85,v85
MM v[104:107],a[232:235],v[196:199],v[104:107]
TR a[186:187],v233 offset:5632
CV v82,v84,v85
MM v[104:107],a[228:231],v[180:183],v[104:107]
EX v86,v86
MM v[104:107],a[224:227],v[164:167],v[104:107]
TR a[188:189],v233 offset:9728
TR a[190:191],v233 offset:13824
MM v[112:115],a[224:227],v[168:171],v[136:139]
EX v87,v87
MM v[112:115],a[228:231],v[184:187],v[112:115]
CV v83,v86,v87
MM v[112:115],a[232:235],v[200:203],v[112:115]
EX v88,v88
MM v[112:115],a[236:239],v[216:219],v[112:115]
EX v89,v89
MM v[116:119],a[252:255],v[216:219],v[136:139]
EX v90,v90
MM v[116:119],a[248:251],v[200:203],v[116:119]
EX v91,v91
MM v[116:119],a[244:247],v[184:187],v[116:119]
EX v92,v92
MM v[116:119],a[240:243],v[168:171],v[116:119]
EX v93,v93
MM v[124:127],a[240:243],v[172:175],v[140:143]
EX v94,v94
MM v[124:127],a[244:247],v[188:191],v[124:127]
EX v95,v95
MM v[124:127],a[248:251],v[204:207],v[124:127]
EX v96,v96
MM v[124:127],a[252:255],v[220:223],v[124:127]
)ASM"
R"ASM(
EX v97,v97
MM v[120:123],a[236:239],v[220:223],v[140:143]
EX v98,v98
MM v[120:123],a[232:235],v[204:207],v[120:123]
EX v99,v99
MM v[120:123],a[228:231],v[188:191],v[120:123]
EX v100,v100
MM v[120:123],a[224:227],v[172:175],v[120:123]
s_waitcnt vmcnt(13) lgkmcnt(0)
s_barrier
MM a[0:3],a[128:131],v[0:3],a[0:3]
s_add_u32 m0, s65, 0
CV v88,v88,v89
CV v89,v90,v91
MM a[0:3],a[132:135],v[4:7],a[0:3]
RD a[192:195],v224 offset:32768
CV v90,v92,v93
MM a[8:11],a[132:135],v[12:15],a[8:11]
RD a[196:199],v225 offset:32768
CV v91,v94,v95
MM a[8:11],a[128:131],v[8:11],a[8:11]
buffer_load_dwordx4 v228, s[8:11], s64 offen offset:0 lds
MM a[16:19],a[128:131],v[16:19],a[16:19]
RD a[200:203],v226 offset:32768
CV v68,v96,v97
MM a[16:19],a[132:135],v[20:23],a[16:19]
RD a[204:207],v227 offset:32768
CV v69,v98,v99
MM a[24:27],a[132:135],v[28:31],a[24:27]
buffer_load_dwordx4 v229, s[8:11], s64 offen offset:1024 lds
MM a[24:27],a[128:131],v[24:27],a[24:27]
RD a[208:211],v224 offset:36864
RD a[212:215],v225 offset:36864
MM a[28:31],a[136:139],v[24:27],a[28:31]
buffer_load_dwordx4 v230, s[8:11], s64 offen offset:2048 lds
MM a[28:31],a[140:143],v[28:31],a[28:31]
RD a[216:219],v226 offset:36864
RD a[220:223],v227 offset:36864
MM a[20:23],a[140:143],v[20:23],a[20:23]
EX v101,v101
MM a[20:23],a[136:139],v[16:19],a[20:23]
RD a[224:227],v224 offset:40960
CV v70,v100,v101
MM a[12:15],a[136:139],v[8:11],a[12:15]
EX v102,v102
MM a[12:15],a[140:143],v[12:15],a[12:15]
buffer_load_dwordx4 v231, s[8:11], s64 offen offset:3072 lds
MM a[4:7],a[140:143],v[4:7],a[4:7]
s_add_u32 m0, s65, 65536
EX v103,v103
MM a[4:7],a[136:139],v[0:3],a[4:7]
RD a[228:231],v225 offset:40960
CV v71,v102,v103
MM a[32:35],a[144:147],v[0:3],a[32:35]
EX v104,v104
MM a[32:35],a[148:151],v[4:7],a[32:35]
EX v105,v105
MM a[40:43],a[148:151],v[12:15],a[40:43]
RD a[232:235],v226 offset:40960
CV v76,v104,v105
MM a[40:43],a[144:147],v[8:11],a[40:43]
EX v106,v106
MM a[48:51],a[144:147],v[16:19],a[48:51]
buffer_load_dwordx4 v235, s[12:15], s81 offen offset:0 lds
MM a[48:51],a[148:151],v[20:23],a[48:51]
EX v107,v107
MM a[56:59],a[148:151],v[28:31],a[56:59]
RD a[236:239],v227 offset:40960
CV v77,v106,v107
MM a[56:59],a[144:147],v[24:27],a[56:59]
EX v108,v108
MM a[60:63],a[152:155],v[24:27],a[60:63]
EX v109,v109
MM a[60:63],a[156:159],v[28:31],a[60:63]
RD a[240:243],v224 offset:45056
CV v78,v108,v109
MM a[52:55],a[156:159],v[20:23],a[52:55]
EX v110,v110
MM a[52:55],a[152:155],v[16:19],a[52:55]
buffer_load_dwordx4 v235, s[12:15], s64 offen offset:1024 lds
MM a[44:47],a[152:155],v[8:11],a[44:47]
EX v111,v111
MM a[44:47],a[156:159],v[12:15],a[44:47]
RD a[244:247],v225 offset:45056
CV v79,v110,v111
MM a[36:39],a[156:159],v[4:7],a[36:39]
EX v112,v112
MM a[36:39],a[152:155],v[0:3],a[36:39]
EX v113,v113
MM a[64:67],a[160:163],v[0:3],a[64:67]
RD a[248:251],v226 offset:45056
CV v84,v112,v113
MM a[64:67],a[164:167],v[4:7],a[64:67]
EX v114,v114
MM a[72:75],a[164:167],v[12:15],a[72:75]
buffer_load_dwordx4 v235, s[12:15], s81 offen offset:2048 lds
MM a[72:75],a[160:163],v[8:11],a[72:75]
EX v115,v115
MM a[80:83],a[160:163],v[16:19],a[80:83]
RD a[252:255],v227 offset:45056
CV v85,v114,v115
MM a[80:83],a[164:167],v[20:23],a[80:83]
EX v116,v116
MM a[88:91],a[164:167],v[28:31],a[88:91]
EX v117,v117
MM a[88:91],a[160:163],v[24:27],a[88:91]
buffer_load_dwordx4 v235, s[12:15], s64 offen offset:3072 lds
MM a[92:95],a[168:171],v[24:27],a[92:95]
EX v118,v118
MM a[92:95],a[172:175],v[28:31],a[92:95]
EX v119,v119
MM a[84:87],a[172:175],v[20:23],a[84:87]
EX v120,v120
MM a[84:87],a[168:171],v[16:19],a[84:87]
EX v121,v121
MM a[76:79],a[168:171],v[8:11],a[76:79]
EX v122,v122
MM a[76:79],a[172:175],v[12:15],a[76:79]
EX v123,v123
MM a[68:71],a[172:175],v[4:7],a[68:71]
EX v124,v124
MM a[68:71],a[168:171],v[0:3],a[68:71]
EX v125,v125
MM a[96:99],a[176:179],v[0:3],a[96:99]
EX v126,v126
MM a[96:99],a[180:183],v[4:7],a[96:99]
EX v127,v127
MM a[104:107],a[180:183],v[12:15],a[104:107]
CV v86,v116,v117
CV v87,v118,v119
MM a[104:107],a[176:179],v[8:11],a[104:107]
CV v92,v120,v121
CV v93,v122,v123
MM a[112:115],a[176:179],v[16:19],a[112:115]
CV v94,v124,v125
CV v95,v126,v127
MM a[112:115],a[180:183],v[20:23],a[112:115]
MM a[120:123],a[180:183],v[28:31],a[120:123]
MM a[120:123],a[176:179],v[24:27],a[120:123]
MM a[124:127],a[184:187],v[24:27],a[124:127]
MM a[124:127],a[188:191],v[28:31],a[124:127]
MM a[116:119],a[188:191],v[20:23],a[116:119]
MM a[116:119],a[184:187],v[16:19],a[116:119]
MM a[108:111],a[184:187],v[8:11],a[108:111]
MM a[108:111],a[188:191],v[12:15],a[108:111]
MM a[100:103],a[188:191],v[4:7],a[100:103]
MM a[100:103],a[184:187],v[0:3],a[100:103]
v_smfmac_f32_16x16x64_bf16 v[236:239], v[144:147], v[64:71], v234
v_smfmac_f32_16x16x64_bf16 v[236:239], v[148:151], v[72:79], v234
s_nop 0
v_smfmac_f32_16x16x64_bf16 v[236:239], v[152:155], v[80:87], v234
s_nop 0
v_smfmac_f32_16x16x64_bf16 v[236:239], v[156:159], v[88:95], v234
s_waitcnt lgkmcnt(0)
MM v[0:3],a[192:195],v[160:163],v[128:131]
s_add_u32 s64, s64, 16384
TR a[128:129],v232 offset:16384
s_add_u32 s81, s64, 896
MM v[0:3],a[196:199],v[176:179],v[0:3]
s_nop 0
TR a[130:131],v232 offset:20480
TR a[132:133],v232 offset:24576
MM v[0:3],a[200:203],v[192:195],v[0:3]
s_nop 0
TR a[134:135],v232 offset:28672
TR a[136:137],v233 offset:16384
MM v[0:3],a[204:207],v[208:211],v[0:3]
s_nop 0
TR a[138:139],v233 offset:20480
v_add_f32 v247, v236, v237
MM v[4:7],a[220:223],v[208:211],v[128:131]
s_nop 0
TR a[140:141],v233 offset:24576
v_cmp_nge_f32 vcc, s71, v247
MM v[4:7],a[216:219],v[192:195],v[4:7]
s_cbranch_vccnz LSITE9_%=
LBACK9_%=:
MM v[4:7],a[212:215],v[176:179],v[4:7]
s_mov_b32 m0, s67
TR a[142:143],v233 offset:28672
TR a[144:145],v232 offset:16896
MM v[4:7],a[208:211],v[160:163],v[4:7]
buffer_load_dwordx4 v252, s[36:39], s66 offen offset:0 lds
MM v[12:15],a[208:211],v[164:167],v[132:135]
s_add_u32 s66, s66, 55296
EX v0,v0
MM v[12:15],a[212:215],v[180:183],v[12:15]
s_min_u32 s66, s66, s85
EX v1,v1
MM v[12:15],a[216:219],v[196:199],v[12:15]
s_add_u32 s67, s67, 1024
TR a[146:147],v232 offset:20992
CV v0,v0,v1
MM v[12:15],a[220:223],v[212:215],v[12:15]
s_min_u32 s67, s67, s86
EX v2,v2
MM v[8:11],a[204:207],v[212:215],v[132:135]
LPG7_%=:
s_cbranch_execz LPG11_%=
EX v3,v3
MM v[8:11],a[200:203],v[196:199],v[8:11]
TR a[148:149],v232 offset:25088
CV v1,v2,v3
MM v[8:11],a[196:199],v[180:183],v[8:11]
EX v4,v4
MM v[8:11],a[192:195],v[164:167],v[8:11]
TR a[150:151],v232 offset:29184
TR a[152:153],v233 offset:16896
MM v[16:19],a[192:195],v[168:171],v[136:139]
EX v5,v5
MM v[16:19],a[196:199],v[184:187],v[16:19]
TR a[154:155],v233 offset:20992
CV v2,v4,v5
MM v[16:19],a[200:203],v[200:203],v[16:19]
EX v6,v6
MM v[16:19],a[204:207],v[216:219],v[16:19]
EX v7,v7
MM v[20:23],a[220:223],v[216:219],v[136:139]
TR a[156:157],v233 offset:25088
CV v3,v6,v7
MM v[20:23],a[216:219],v[200:203],v[20:23]
EX v8,v8
MM v[20:23],a[212:215],v[184:187],v[20:23]
TR a[158:159],v233 offset:29184
TR a[160:161],v232 offset:17408
MM v[20:23],a[208:211],v[168:171],v[20:23]
EX v9,v9
MM v[28:31],a[208:211],v[172:175],v[140:143]
TR a[162:163],v232 offset:21504
CV v8,v8,v9
MM v[28:31],a[212:215],v[188:191],v[28:31]
EX v10,v10
MM v[28:31],a[216:219],v[204:207],v[28:31]
EX v11,v11
MM v[28:31],a[220:223],v[220:223],v[28:31]
TR a[164:165],v232 offset:25600
CV v9,v10,v11
MM v[24:27],a[204:207],v[220:223],v[140:143]
EX v12,v12
MM v[24:27],a[200:203],v[204:207],v[24:27]
TR a[166:167],v232 offset:29696
TR a[168:169],v233 offset:17408
MM v[24:27],a[196:199],v[188:191],v[24:27]
EX v13,v13
MM v[24:27],a[192:195],v[172:175],v[24:27]
TR a[170:171],v233 offset:21504
CV v10,v12,v13
MM v[32:35],a[224:227],v[160:163],v[128:131]
EX v14,v14
MM v[32:35],a[228:231],v[176:179],v[32:35]
EX v15,v15
MM v[32:35],a[232:235],v[192:195],v[32:35]
TR a[172:173],v233 offset:25600
CV v11,v14,v15
MM v[32:35],a[236:239],v[208:211],v[32:35]
TR a[174:175],v233 offset:29696
TR a[176:177],v232 offset:17920
MM v[36:39],a[252:255],v[208:211],v[128:131]
EX v16,v16
MM v[36:39],a[248:251],v[192:195],v[36:39]
EX v17,v17
MM v[36:39],a[244:247],v[176:179],v[36:39]
TR a[178:179],v232 offset:22016
CV v16,v16,v17
MM v[36:39],a[240:243],v[160:163],v[36:39]
EX v18,v18
MM v[44:47],a[240:243],v[164:167],v[132:135]
TR a[180:181],v232 offset:26112
TR a[182:183],v232 offset:30208
MM v[44:47],a[244:247],v[180:183],v[44:47]
EX v19,v19
MM v[44:47],a[248:251],v[196:199],v[44:47]
TR a[184:185],v233 offset:17920
CV v17,v18,v19
MM v[44:47],a[252:255],v[212:215],v[44:47]
EX v20,v20
MM v[40:43],a[236:239],v[212:215],v[132:135]
EX v21,v21
MM v[40:43],a[232:235],v[196:199],v[40:43]
TR a[186:187],v233 offset:22016
CV v18,v20,v21
MM v[40:43],a[228:231],v[180:183],v[40:43]
EX v22,v22
MM v[40:43],a[224:227],v[164:167],v[40:43]
TR a[188:189],v233 offset:26112
TR a[190:191],v233 offset:30208
MM v[48:51],a[224:227],v[168:171],v[136:139]
EX v23,v23
MM v[48:51],a[228:231],v[184:187],v[48:51]
CV v19,v22,v23
MM v[48:51],a[232:235],v[200:203],v[48:51]
EX v24,v24
MM v[48:51],a[236:239],v[216:219],v[48:51]
EX v25,v25
MM v[52:55],a[252:255],v[216:219],v[136:139]
EX v26,v26
MM v[52:55],a[248:251],v[200:203],v[52:55]
EX v27,v27
MM v[52:55],a[244:247],v[184:187],v[52:55]
EX v28,v28
MM v[52:55],a[240:243],v[168:171],v[52:55]
EX v29,v29
MM v[60:63],a[240:243],v[172:175],v[140:143]
EX v30,v30
MM v[60:63],a[244:247],v[188:191],v[60:63]
EX v31,v31
MM v[60:63],a[248:251],v[204:207],v[60:63]
EX v32,v32
MM v[60:63],a[252:255],v[220:223],v[60:63]
EX v33,v33
MM v[56:59],a[236:239],v[220:223],v[140:143]
EX v34,v34
MM v[56:59],a[232:235],v[204:207],v[56:59]
EX v35,v35
MM v[56:59],a[228:231],v[188:191],v[56:59]
EX v36,v36
MM v[56:59],a[224:227],v[172:175],v[56:59]
s_waitcnt vmcnt(13) lgkmcnt(0)
s_barrier
MM a[0:3],a[128:131],v[64:67],a[0:3]
s_add_u32 m0, s65, 16384
CV v24,v24,v25
CV v25,v26,v27
MM a[0:3],a[132:135],v[68:71],a[0:3]
RD a[192:195],v224 offset:49152
CV v26,v28,v29
MM a[8:11],a[132:135],v[76:79],a[8:11]
RD a[196:199],v225 offset:49152
CV v27,v30,v31
MM a[8:11],a[128:131],v[72:75],a[8:11]
buffer_load_dwordx4 v228, s[8:11], s64 offen offset:0 lds
MM a[16:19],a[128:131],v[80:83],a[16:19]
RD a[200:203],v226 offset:49152
CV v4,v32,v33
MM a[16:19],a[132:135],v[84:87],a[16:19]
RD a[204:207],v227 offset:49152
CV v5,v34,v35
MM a[24:27],a[132:135],v[92:95],a[24:27]
buffer_load_dwordx4 v229, s[8:11], s64 offen offset:1024 lds
MM a[24:27],a[128:131],v[88:91],a[24:27]
RD a[208:211],v224 offset:53248
RD a[212:215],v225 offset:53248
MM a[28:31],a[136:139],v[88:91],a[28:31]
buffer_load_dwordx4 v230, s[8:11], s64 offen offset:2048 lds
MM a[28:31],a[140:143],v[92:95],a[28:31]
RD a[216:219],v226 offset:53248
RD a[220:223],v227 offset:53248
MM a[20:23],a[140:143],v[84:87],a[20:23]
EX v37,v37
MM a[20:23],a[136:139],v[80:83],a[20:23]
RD a[224:227],v224 offset:57344
CV v6,v36,v37
MM a[12:15],a[136:139],v[72:75],a[12:15]
EX v38,v38
MM a[12:15],a[140:143],v[76:79],a[12:15]
buffer_load_dwordx4 v231, s[8:11], s64 offen offset:3072 lds
MM a[4:7],a[140:143],v[68:71],a[4:7]
s_add_u32 m0, s65, 81920
EX v39,v39
MM a[4:7],a[136:139],v[64:67],a[4:7]
RD a[228:231],v225 offset:57344
CV v7,v38,v39
MM a[32:35],a[144:147],v[64:67],a[32:35]
EX v40,v40
MM a[32:35],a[148:151],v[68:71],a[32:35]
EX v41,v41
MM a[40:43],a[148:151],v[76:79],a[40:43]
RD a[232:235],v226 offset:57344
CV v12,v40,v41
MM a[40:43],a[144:147],v[72:75],a[40:43]
EX v42,v42
MM a[48:51],a[144:147],v[80:83],a[48:51]
buffer_load_dwordx4 v235, s[12:15], s81 offen offset:0 lds
MM a[48:51],a[148:151],v[84:87],a[48:51]
EX v43,v43
MM a[56:59],a[148:151],v[92:95],a[56:59]
RD a[236:239],v227 offset:57344
CV v13,v42,v43
MM a[56:59],a[144:147],v[88:91],a[56:59]
EX v44,v44
MM a[60:63],a[152:155],v[88:91],a[60:63]
EX v45,v45
MM a[60:63],a[156:159],v[92:95],a[60:63]
RD a[240:243],v224 offset:61440
CV v14,v44,v45
MM a[52:55],a[156:159],v[84:87],a[52:55]
EX v46,v46
MM a[52:55],a[152:155],v[80:83],a[52:55]
buffer_load_dwordx4 v235, s[12:15], s64 offen offset:1024 lds
MM a[44:47],a[152:155],v[72:75],a[44:47]
EX v47,v47
MM a[44:47],a[156:159],v[76:79],a[44:47]
RD a[244:247],v225 offset:61440
)ASM"
R"ASM(
CV v15,v46,v47
MM a[36:39],a[156:159],v[68:71],a[36:39]
EX v48,v48
MM a[36:39],a[152:155],v[64:67],a[36:39]
EX v49,v49
MM a[64:67],a[160:163],v[64:67],a[64:67]
RD a[248:251],v226 offset:61440
CV v20,v48,v49
MM a[64:67],a[164:167],v[68:71],a[64:67]
EX v50,v50
MM a[72:75],a[164:167],v[76:79],a[72:75]
buffer_load_dwordx4 v235, s[12:15], s81 offen offset:2048 lds
MM a[72:75],a[160:163],v[72:75],a[72:75]
EX v51,v51
MM a[80:83],a[160:163],v[80:83],a[80:83]
RD a[252:255],v227 offset:61440
CV v21,v50,v51
MM a[80:83],a[164:167],v[84:87],a[80:83]
EX v52,v52
MM a[88:91],a[164:167],v[92:95],a[88:91]
EX v53,v53
MM a[88:91],a[160:163],v[88:91],a[88:91]
buffer_load_dwordx4 v235, s[12:15], s64 offen offset:3072 lds
MM a[92:95],a[168:171],v[88:91],a[92:95]
EX v54,v54
MM a[92:95],a[172:175],v[92:95],a[92:95]
EX v55,v55
MM a[84:87],a[172:175],v[84:87],a[84:87]
EX v56,v56
MM a[84:87],a[168:171],v[80:83],a[84:87]
EX v57,v57
MM a[76:79],a[168:171],v[72:75],a[76:79]
EX v58,v58
MM a[76:79],a[172:175],v[76:79],a[76:79]
EX v59,v59
MM a[68:71],a[172:175],v[68:71],a[68:71]
EX v60,v60
MM a[68:71],a[168:171],v[64:67],a[68:71]
EX v61,v61
MM a[96:99],a[176:179],v[64:67],a[96:99]
EX v62,v62
MM a[96:99],a[180:183],v[68:71],a[96:99]
EX v63,v63
MM a[104:107],a[180:183],v[76:79],a[104:107]
CV v22,v52,v53
CV v23,v54,v55
MM a[104:107],a[176:179],v[72:75],a[104:107]
CV v28,v56,v57
CV v29,v58,v59
MM a[112:115],a[176:179],v[80:83],a[112:115]
CV v30,v60,v61
CV v31,v62,v63
MM a[112:115],a[180:183],v[84:87],a[112:115]
MM a[120:123],a[180:183],v[92:95],a[120:123]
MM a[120:123],a[176:179],v[88:91],a[120:123]
MM a[124:127],a[184:187],v[88:91],a[124:127]
MM a[124:127],a[188:191],v[92:95],a[124:127]
MM a[116:119],a[188:191],v[84:87],a[116:119]
MM a[116:119],a[184:187],v[80:83],a[116:119]
MM a[108:111],a[184:187],v[72:75],a[108:111]
MM a[108:111],a[188:191],v[76:79],a[108:111]
MM a[100:103],a[188:191],v[68:71],a[100:103]
MM a[100:103],a[184:187],v[64:67],a[100:103]
v_smfmac_f32_16x16x64_bf16 v[236:239], v[144:147], v[0:7], v234
v_smfmac_f32_16x16x64_bf16 v[236:239], v[148:151], v[8:15], v234
v_smfmac_f32_16x16x64_bf16 v[236:239], v[152:155], v[16:23], v234
v_smfmac_f32_16x16x64_bf16 v[236:239], v[156:159], v[24:31], v234
s_waitcnt lgkmcnt(0)
MM v[64:67],a[192:195],v[160:163],v[128:131]
s_add_u32 s64, s64, 16384
TR a[128:129],v232 offset:32768
s_add_u32 s81, s64, 896
MM v[64:67],a[196:199],v[176:179],v[64:67]
TR a[130:131],v232 offset:36864
TR a[132:133],v232 offset:40960
MM v[64:67],a[200:203],v[192:195],v[64:67]
TR a[134:135],v232 offset:45056
TR a[136:137],v233 offset:32768
MM v[64:67],a[204:207],v[208:211],v[64:67]
TR a[138:139],v233 offset:36864
v_add_f32 v246, v236, v237
MM v[68:71],a[220:223],v[208:211],v[128:131]
TR a[140:141],v233 offset:40960
v_cmp_nge_f32 vcc, s71, v246
MM v[68:71],a[216:219],v[192:195],v[68:71]
s_cbranch_vccnz LSITE10_%=
LBACK10_%=:
MM v[68:71],a[212:215],v[176:179],v[68:71]
s_mov_b32 m0, s67
TR a[142:143],v233 offset:45056
TR a[144:145],v232 offset:33280
MM v[68:71],a[208:211],v[160:163],v[68:71]
buffer_load_dwordx4 v252, s[36:39], s66 offen offset:0 lds
MM v[76:79],a[208:211],v[164:167],v[132:135]
s_add_u32 s66, s66, 55296
EX v64,v64
MM v[76:79],a[212:215],v[180:183],v[76:79]
s_min_u32 s66, s66, s85
EX v65,v65
MM v[76:79],a[216:219],v[196:199],v[76:79]
s_add_u32 s67, s67, 1024
TR a[146:147],v232 offset:37376
CV v64,v64,v65
MM v[76:79],a[220:223],v[212:215],v[76:79]
s_min_u32 s67, s67, s86
EX v66,v66
MM v[72:75],a[204:207],v[212:215],v[132:135]
EX v67,v67
MM v[72:75],a[200:203],v[196:199],v[72:75]
TR a[148:149],v232 offset:41472
CV v65,v66,v67
MM v[72:75],a[196:199],v[180:183],v[72:75]
EX v68,v68
MM v[72:75],a[192:195],v[164:167],v[72:75]
TR a[150:151],v232 offset:45568
TR a[152:153],v233 offset:33280
MM v[80:83],a[192:195],v[168:171],v[136:139]
EX v69,v69
MM v[80:83],a[196:199],v[184:187],v[80:83]
TR a[154:155],v233 offset:37376
CV v66,v68,v69
MM v[80:83],a[200:203],v[200:203],v[80:83]
EX v70,v70
MM v[80:83],a[204:207],v[216:219],v[80:83]
EX v71,v71
MM v[84:87],a[220:223],v[216:219],v[136:139]
TR a[156:157],v233 offset:41472
CV v67,v70,v71
MM v[84:87],a[216:219],v[200:203],v[84:87]
EX v72,v72
MM v[84:87],a[212:215],v[184:187],v[84:87]
TR a[158:159],v233 offset:45568
TR a[160:161],v232 offset:33792
MM v[84:87],a[208:211],v[168:171],v[84:87]
EX v73,v73
MM v[92:95],a[208:211],v[172:175],v[140:143]
TR a[162:163],v232 offset:37888
CV v72,v72,v73
MM v[92:95],a[212:215],v[188:191],v[92:95]
EX v74,v74
MM v[92:95],a[216:219],v[204:207],v[92:95]
EX v75,v75
MM v[92:95],a[220:223],v[220:223],v[92:95]
TR a[164:165],v232 offset:41984
CV v73,v74,v75
MM v[88:91],a[204:207],v[220:223],v[140:143]
EX v76,v76
MM v[88:91],a[200:203],v[204:207],v[88:91]
TR a[166:167],v232 offset:46080
TR a[168:169],v233 offset:33792
MM v[88:91],a[196:199],v[188:191],v[88:91]
EX v77,v77
MM v[88:91],a[192:195],v[172:175],v[88:91]
TR a[170:171],v233 offset:37888
CV v74,v76,v77
MM v[96:99],a[224:227],v[160:163],v[128:131]
EX v78,v78
MM v[96:99],a[228:231],v[176:179],v[96:99]
EX v79,v79
MM v[96:99],a[232:235],v[192:195],v[96:99]
TR a[172:173],v233 offset:41984
CV v75,v78,v79
MM v[96:99],a[236:239],v[208:211],v[96:99]
TR a[174:175],v233 offset:46080
TR a[176:177],v232 offset:34304
MM v[100:103],a[252:255],v[208:211],v[128:131]
EX v80,v80
MM v[100:103],a[248:251],v[192:195],v[100:103]
EX v81,v81
MM v[100:103],a[244:247],v[176:179],v[100:103]
TR a[178:179],v232 offset:38400
CV v80,v80,v81
MM v[100:103],a[240:243],v[160:163],v[100:103]
EX v82,v82
MM v[108:111],a[240:243],v[164:167],v[132:135]
TR a[180:181],v232 offset:42496
TR a[182:183],v232 offset:46592
MM v[108:111],a[244:247],v[180:183],v[108:111]
EX v83,v83
MM v[108:111],a[248:251],v[196:199],v[108:111]
TR a[184:185],v233 offset:34304
CV v81,v82,v83
MM v[108:111],a[252:255],v[212:215],v[108:111]
EX v84,v84
MM v[104:107],a[236:239],v[212:215],v[132:135]
EX v85,v85
MM v[104:107],a[232:235],v[196:199],v[104:107]
TR a[186:187],v233 offset:38400
CV v82,v84,v85
MM v[104:107],a[228:231],v[180:183],v[104:107]
EX v86,v86
MM v[104:107],a[224:227],v[164:167],v[104:107]
TR a[188:189],v233 offset:42496
TR a[190:191],v233 offset:46592
MM v[112:115],a[224:227],v[168:171],v[136:139]
EX v87,v87
MM v[112:115],a[228:231],v[184:187],v[112:115]
CV v83,v86,v87
MM v[112:115],a[232:235],v[200:203],v[112:115]
EX v88,v88
MM v[112:115],a[236:239],v[216:219],v[112:115]
EX v89,v89
MM v[116:119],a[252:255],v[216:219],v[136:139]
EX v90,v90
MM v[116:119],a[248:251],v[200:203],v[116:119]
EX v91,v91
MM v[116:119],a[244:247],v[184:187],v[116:119]
EX v92,v92
MM v[116:119],a[240:243],v[168:171],v[116:119]
EX v93,v93
MM v[124:127],a[240:243],v[172:175],v[140:143]
EX v94,v94
MM v[124:127],a[244:247],v[188:191],v[124:127]
EX v95,v95
MM v[124:127],a[248:251],v[204:207],v[124:127]
EX v96,v96
MM v[124:127],a[252:255],v[220:223],v[124:127]
EX v97,v97
MM v[120:123],a[236:239],v[220:223],v[140:143]
EX v98,v98
MM v[120:123],a[232:235],v[204:207],v[120:123]
EX v99,v99
MM v[120:123],a[228:231],v[188:191],v[120:123]
EX v100,v100
MM v[120:123],a[224:227],v[172:175],v[120:123]
s_waitcnt vmcnt(13) lgkmcnt(0)
s_barrier
MM a[0:3],a[128:131],v[0:3],a[0:3]
s_add_u32 m0, s65, 32768
CV v88,v88,v89
CV v89,v90,v91
MM a[0:3],a[132:135],v[4:7],a[0:3]
RD a[192:195],v224 offset:0
CV v90,v92,v93
MM a[8:11],a[132:135],v[12:15],a[8:11]
RD a[196:199],v225 offset:0
CV v91,v94,v95
MM a[8:11],a[128:131],v[8:11],a[8:11]
buffer_load_dwordx4 v228, s[8:11], s64 offen offset:0 lds
MM a[16:19],a[128:131],v[16:19],a[16:19]
RD a[200:203],v226 offset:0
CV v68,v96,v97
MM a[16:19],a[132:135],v[20:23],a[16:19]
RD a[204:207],v227 offset:0
CV v69,v98,v99
MM a[24:27],a[132:135],v[28:31],a[24:27]
buffer_load_dwordx4 v229, s[8:11], s64 offen offset:1024 lds
MM a[24:27],a[128:131],v[24:27],a[24:27]
RD a[208:211],v224 offset:4096
RD a[212:215],v225 offset:4096
MM a[28:31],a[136:139],v[24:27],a[28:31]
buffer_load_dwordx4 v230, s[8:11], s64 offen offset:2048 lds
MM a[28:31],a[140:143],v[28:31],a[28:31]
RD a[216:219],v226 offset:4096
RD a[220:223],v227 offset:4096
MM a[20:23],a[140:143],v[20:23],a[20:23]
EX v101,v101
MM a[20:23],a[136:139],v[16:19],a[20:23]
RD a[224:227],v224 offset:8192
CV v70,v100,v101
MM a[12:15],a[136:139],v[8:11],a[12:15]
EX v102,v102
MM a[12:15],a[140:143],v[12:15],a[12:15]
buffer_load_dwordx4 v231, s[8:11], s64 offen offset:3072 lds
MM a[4:7],a[140:143],v[4:7],a[4:7]
s_add_u32 m0, s65, 98304
EX v103,v103
MM a[4:7],a[136:139],v[0:3],a[4:7]
RD a[228:231],v225 offset:8192
CV v71,v102,v103
MM a[32:35],a[144:147],v[0:3],a[32:35]
EX v104,v104
MM a[32:35],a[148:151],v[4:7],a[32:35]
EX v105,v105
MM a[40:43],a[148:151],v[12:15],a[40:43]
RD a[232:235],v226 offset:8192
CV v76,v104,v105
MM a[40:43],a[144:147],v[8:11],a[40:43]
EX v106,v106
MM a[48:51],a[144:147],v[16:19],a[48:51]
buffer_load_dwordx4 v235, s[12:15], s81 offen offset:0 lds
MM a[48:51],a[148:151],v[20:23],a[48:51]
EX v107,v107
MM a[56:59],a[148:151],v[28:31],a[56:59]
RD a[236:239],v227 offset:8192
CV v77,v106,v107
MM a[56:59],a[144:147],v[24:27],a[56:59]
EX v108,v108
MM a[60:63],a[152:155],v[24:27],a[60:63]
EX v109,v109
MM a[60:63],a[156:159],v[28:31],a[60:63]
RD a[240:243],v224 offset:12288
CV v78,v108,v109
MM a[52:55],a[156:159],v[20:23],a[52:55]
EX v110,v110
MM a[52:55],a[152:155],v[16:19],a[52:55]
buffer_load_dwordx4 v235, s[12:15], s64 offen offset:1024 lds
MM a[44:47],a[152:155],v[8:11],a[44:47]
EX v111,v111
MM a[44:47],a[156:159],v[12:15],a[44:47]
RD a[244:247],v225 offset:12288
CV v79,v110,v111
MM a[36:39],a[156:159],v[4:7],a[36:39]
EX v112,v112
MM a[36:39],a[152:155],v[0:3],a[36:39]
EX v113,v113
MM a[64:67],a[160:163],v[0:3],a[64:67]
RD a[248:251],v226 offset:12288
CV v84,v112,v113
MM a[64:67],a[164:167],v[4:7],a[64:67]
EX v114,v114
MM a[72:75],a[164:167],v[12:15],a[72:75]
buffer_load_dwordx4 v235, s[12:15], s81 offen offset:2048 lds
MM a[72:75],a[160:163],v[8:11],a[72:75]
EX v115,v115
MM a[80:83],a[160:163],v[16:19],a[80:83]
RD a[252:255],v227 offset:12288
CV v85,v114,v115
MM a[80:83],a[164:167],v[20:23],a[80:83]
EX v116,v116
MM a[88:91],a[164:167],v[28:31],a[88:91]
EX v117,v117
MM a[88:91],a[160:163],v[24:27],a[88:91]
buffer_load_dwordx4 v235, s[12:15], s64 offen offset:3072 lds
MM a[92:95],a[168:171],v[24:27],a[92:95]
EX v118,v118
MM a[92:95],a[172:175],v[28:31],a[92:95]
EX v119,v119
MM a[84:87],a[172:175],v[20:23],a[84:87]
EX v120,v120
MM a[84:87],a[168:171],v[16:19],a[84:87]
EX v121,v121
MM a[76:79],a[168:171],v[8:11],a[76:79]
EX v122,v122
MM a[76:79],a[172:175],v[12:15],a[76:79]
EX v123,v123
MM a[68:71],a[172:175],v[4:7],a[68:71]
EX v124,v124
MM a[68:71],a[168:171],v[0:3],a[68:71]
EX v125,v125
MM a[96:99],a[176:179],v[0:3],a[96:99]
EX v126,v126
MM a[96:99],a[180:183],v[4:7],a[96:99]
EX v127,v127
MM a[104:107],a[180:183],v[12:15],a[104:107]
CV v86,v116,v117
CV v87,v118,v119
MM a[104:107],a[176:179],v[8:11],a[104:107]
CV v92,v120,v121
CV v93,v122,v123
MM a[112:115],a[176:179],v[16:19],a[112:115]
CV v94,v124,v125
CV v95,v126,v127
MM a[112:115],a[180:183],v[20:23],a[112:115]
MM a[120:123],a[180:183],v[28:31],a[120:123]
MM a[120:123],a[176:179],v[24:27],a[120:123]
MM a[124:127],a[184:187],v[24:27],a[124:127]
MM a[124:127],a[188:191],v[28:31],a[124:127]
MM a[116:119],a[188:191],v[20:23],a[116:119]
MM a[116:119],a[184:187],v[16:19],a[116:119]
MM a[108:111],a[184:187],v[8:11],a[108:111]
MM a[108:111],a[188:191],v[12:15],a[108:111]
s_nop 0
MM a[100:103],a[188:191],v[4:7],a[100:103]
s_nop 0
MM a[100:103],a[184:187],v[0:3],a[100:103]
s_nop 0
v_smfmac_f32_16x16x64_bf16 v[236:239], v[144:147], v[64:71], v234
s_nop 0
v_smfmac_f32_16x16x64_bf16 v[236:239], v[148:151], v[72:79], v234
s_nop 0
v_smfmac_f32_16x16x64_bf16 v[236:239], v[152:155], v[80:87], v234
s_nop 0
v_smfmac_f32_16x16x64_bf16 v[236:239], v[156:159], v[88:95], v234
s_waitcnt lgkmcnt(0)
MM v[0:3],a[192:195],v[160:163],v[128:131]
s_add_u32 s64, s64, 16384
TR a[128:129],v232 offset:49152
s_add_u32 s81, s64, 896
MM v[0:3],a[196:199],v[176:179],v[0:3]
s_nop 0
TR a[130:131],v232 offset:53248
TR a[132:133],v232 offset:57344
MM v[0:3],a[200:203],v[192:195],v[0:3]
)ASM"
R"ASM(
s_nop 0
TR a[134:135],v232 offset:61440
TR a[136:137],v233 offset:49152
MM v[0:3],a[204:207],v[208:211],v[0:3]
s_nop 0
TR a[138:139],v233 offset:53248
v_add_f32 v247, v236, v237
MM v[4:7],a[220:223],v[208:211],v[128:131]
LPG8_%=:
s_cbranch_execz LPG12_%=
TR a[140:141],v233 offset:57344
v_cmp_nge_f32 vcc, s71, v247
MM v[4:7],a[216:219],v[192:195],v[4:7]
s_cbranch_vccnz LSITE11_%=
LBACK11_%=:
MM v[4:7],a[212:215],v[176:179],v[4:7]
s_mov_b32 m0, s67
TR a[142:143],v233 offset:61440
TR a[144:145],v232 offset:49664
MM v[4:7],a[208:211],v[160:163],v[4:7]
buffer_load_dwordx4 v252, s[36:39], s66 offen offset:0 lds
MM v[12:15],a[208:211],v[164:167],v[132:135]
s_add_u32 s66, s66, 55296
EX v0,v0
MM v[12:15],a[212:215],v[180:183],v[12:15]
s_min_u32 s66, s66, s85
EX v1,v1
MM v[12:15],a[216:219],v[196:199],v[12:15]
s_add_u32 s67, s67, 1024
TR a[146:147],v232 offset:53760
CV v0,v0,v1
MM v[12:15],a[220:223],v[212:215],v[12:15]
s_min_u32 s67, s67, s86
EX v2,v2
MM v[8:11],a[204:207],v[212:215],v[132:135]
EX v3,v3
MM v[8:11],a[200:203],v[196:199],v[8:11]
TR a[148:149],v232 offset:57856
CV v1,v2,v3
MM v[8:11],a[196:199],v[180:183],v[8:11]
EX v4,v4
MM v[8:11],a[192:195],v[164:167],v[8:11]
TR a[150:151],v232 offset:61952
TR a[152:153],v233 offset:49664
MM v[16:19],a[192:195],v[168:171],v[136:139]
EX v5,v5
MM v[16:19],a[196:199],v[184:187],v[16:19]
TR a[154:155],v233 offset:53760
CV v2,v4,v5
MM v[16:19],a[200:203],v[200:203],v[16:19]
EX v6,v6
MM v[16:19],a[204:207],v[216:219],v[16:19]
EX v7,v7
MM v[20:23],a[220:223],v[216:219],v[136:139]
TR a[156:157],v233 offset:57856
CV v3,v6,v7
MM v[20:23],a[216:219],v[200:203],v[20:23]
EX v8,v8
MM v[20:23],a[212:215],v[184:187],v[20:23]
TR a[158:159],v233 offset:61952
TR a[160:161],v232 offset:50176
MM v[20:23],a[208:211],v[168:171],v[20:23]
EX v9,v9
MM v[28:31],a[208:211],v[172:175],v[140:143]
TR a[162:163],v232 offset:54272
CV v8,v8,v9
MM v[28:31],a[212:215],v[188:191],v[28:31]
EX v10,v10
MM v[28:31],a[216:219],v[204:207],v[28:31]
EX v11,v11
MM v[28:31],a[220:223],v[220:223],v[28:31]
TR a[164:165],v232 offset:58368
CV v9,v10,v11
MM v[24:27],a[204:207],v[220:223],v[140:143]
EX v12,v12
MM v[24:27],a[200:203],v[204:207],v[24:27]
TR a[166:167],v232 offset:62464
TR a[168:169],v233 offset:50176
MM v[24:27],a[196:199],v[188:191],v[24:27]
EX v13,v13
MM v[24:27],a[192:195],v[172:175],v[24:27]
TR a[170:171],v233 offset:54272
CV v10,v12,v13
MM v[32:35],a[224:227],v[160:163],v[128:131]
EX v14,v14
MM v[32:35],a[228:231],v[176:179],v[32:35]
EX v15,v15
MM v[32:35],a[232:235],v[192:195],v[32:35]
TR a[172:173],v233 offset:58368
CV v11,v14,v15
MM v[32:35],a[236:239],v[208:211],v[32:35]
TR a[174:175],v233 offset:62464
TR a[176:177],v232 offset:50688
MM v[36:39],a[252:255],v[208:211],v[128:131]
EX v16,v16
MM v[36:39],a[248:251],v[192:195],v[36:39]
EX v17,v17
MM v[36:39],a[244:247],v[176:179],v[36:39]
TR a[178:179],v232 offset:54784
CV v16,v16,v17
MM v[36:39],a[240:243],v[160:163],v[36:39]
EX v18,v18
MM v[44:47],a[240:243],v[164:167],v[132:135]
TR a[180:181],v232 offset:58880
TR a[182:183],v232 offset:62976
MM v[44:47],a[244:247],v[180:183],v[44:47]
EX v19,v19
MM v[44:47],a[248:251],v[196:199],v[44:47]
TR a[184:185],v233 offset:50688
CV v17,v18,v19
MM v[44:47],a[252:255],v[212:215],v[44:47]
EX v20,v20
MM v[40:43],a[236:239],v[212:215],v[132:135]
EX v21,v21
MM v[40:43],a[232:235],v[196:199],v[40:43]
TR a[186:187],v233 offset:54784
CV v18,v20,v21
MM v[40:43],a[228:231],v[180:183],v[40:43]
EX v22,v22
MM v[40:43],a[224:227],v[164:167],v[40:43]
TR a[188:189],v233 offset:58880
TR a[190:191],v233 offset:62976
MM v[48:51],a[224:227],v[168:171],v[136:139]
EX v23,v23
MM v[48:51],a[228:231],v[184:187],v[48:51]
CV v19,v22,v23
MM v[48:51],a[232:235],v[200:203],v[48:51]
EX v24,v24
MM v[48:51],a[236:239],v[216:219],v[48:51]
EX v25,v25
MM v[52:55],a[252:255],v[216:219],v[136:139]
EX v26,v26
MM v[52:55],a[248:251],v[200:203],v[52:55]
EX v27,v27
MM v[52:55],a[244:247],v[184:187],v[52:55]
EX v28,v28
MM v[52:55],a[240:243],v[168:171],v[52:55]
EX v29,v29
MM v[60:63],a[240:243],v[172:175],v[140:143]
EX v30,v30
MM v[60:63],a[244:247],v[188:191],v[60:63]
EX v31,v31
MM v[60:63],a[248:251],v[204:207],v[60:63]
EX v32,v32
MM v[60:63],a[252:255],v[220:223],v[60:63]
EX v33,v33
MM v[56:59],a[236:239],v[220:223],v[140:143]
EX v34,v34
MM v[56:59],a[232:235],v[204:207],v[56:59]
EX v35,v35
MM v[56:59],a[228:231],v[188:191],v[56:59]
EX v36,v36
MM v[56:59],a[224:227],v[172:175],v[56:59]
s_waitcnt vmcnt(13) lgkmcnt(0)
s_barrier
MM a[0:3],a[128:131],v[64:67],a[0:3]
s_add_u32 m0, s65, 49152
CV v24,v24,v25
CV v25,v26,v27
MM a[0:3],a[132:135],v[68:71],a[0:3]
RD a[192:195],v224 offset:16384
CV v26,v28,v29
MM a[8:11],a[132:135],v[76:79],a[8:11]
RD a[196:199],v225 offset:16384
CV v27,v30,v31
MM a[8:11],a[128:131],v[72:75],a[8:11]
buffer_load_dwordx4 v228, s[8:11], s64 offen offset:0 lds
MM a[16:19],a[128:131],v[80:83],a[16:19]
RD a[200:203],v226 offset:16384
CV v4,v32,v33
MM a[16:19],a[132:135],v[84:87],a[16:19]
RD a[204:207],v227 offset:16384
CV v5,v34,v35
MM a[24:27],a[132:135],v[92:95],a[24:27]
buffer_load_dwordx4 v229, s[8:11], s64 offen offset:1024 lds
MM a[24:27],a[128:131],v[88:91],a[24:27]
RD a[208:211],v224 offset:20480
RD a[212:215],v225 offset:20480
MM a[28:31],a[136:139],v[88:91],a[28:31]
buffer_load_dwordx4 v230, s[8:11], s64 offen offset:2048 lds
MM a[28:31],a[140:143],v[92:95],a[28:31]
RD a[216:219],v226 offset:20480
RD a[220:223],v227 offset:20480
MM a[20:23],a[140:143],v[84:87],a[20:23]
EX v37,v37
MM a[20:23],a[136:139],v[80:83],a[20:23]
RD a[224:227],v224 offset:24576
CV v6,v36,v37
MM a[12:15],a[136:139],v[72:75],a[12:15]
EX v38,v38
MM a[12:15],a[140:143],v[76:79],a[12:15]
buffer_load_dwordx4 v231, s[8:11], s64 offen offset:3072 lds
MM a[4:7],a[140:143],v[68:71],a[4:7]
s_add_u32 m0, s65, 114688
EX v39,v39
MM a[4:7],a[136:139],v[64:67],a[4:7]
RD a[228:231],v225 offset:24576
CV v7,v38,v39
MM a[32:35],a[144:147],v[64:67],a[32:35]
EX v40,v40
MM a[32:35],a[148:151],v[68:71],a[32:35]
EX v41,v41
MM a[40:43],a[148:151],v[76:79],a[40:43]
RD a[232:235],v226 offset:24576
CV v12,v40,v41
MM a[40:43],a[144:147],v[72:75],a[40:43]
EX v42,v42
MM a[48:51],a[144:147],v[80:83],a[48:51]
buffer_load_dwordx4 v235, s[12:15], s81 offen offset:0 lds
MM a[48:51],a[148:151],v[84:87],a[48:51]
EX v43,v43
MM a[56:59],a[148:151],v[92:95],a[56:59]
RD a[236:239],v227 offset:24576
CV v13,v42,v43
MM a[56:59],a[144:147],v[88:91],a[56:59]
EX v44,v44
MM a[60:63],a[152:155],v[88:91],a[60:63]
EX v45,v45
MM a[60:63],a[156:159],v[92:95],a[60:63]
RD a[240:243],v224 offset:28672
CV v14,v44,v45
MM a[52:55],a[156:159],v[84:87],a[52:55]
EX v46,v46
MM a[52:55],a[152:155],v[80:83],a[52:55]
buffer_load_dwordx4 v235, s[12:15], s64 offen offset:1024 lds
MM a[44:47],a[152:155],v[72:75],a[44:47]
EX v47,v47
MM a[44:47],a[156:159],v[76:79],a[44:47]
RD a[244:247],v225 offset:28672
CV v15,v46,v47
MM a[36:39],a[156:159],v[68:71],a[36:39]
EX v48,v48
MM a[36:39],a[152:155],v[64:67],a[36:39]
EX v49,v49
MM a[64:67],a[160:163],v[64:67],a[64:67]
RD a[248:251],v226 offset:28672
CV v20,v48,v49
MM a[64:67],a[164:167],v[68:71],a[64:67]
EX v50,v50
MM a[72:75],a[164:167],v[76:79],a[72:75]
buffer_load_dwordx4 v235, s[12:15], s81 offen offset:2048 lds
MM a[72:75],a[160:163],v[72:75],a[72:75]
EX v51,v51
MM a[80:83],a[160:163],v[80:83],a[80:83]
RD a[252:255],v227 offset:28672
CV v21,v50,v51
MM a[80:83],a[164:167],v[84:87],a[80:83]
EX v52,v52
MM a[88:91],a[164:167],v[92:95],a[88:91]
EX v53,v53
MM a[88:91],a[160:163],v[88:91],a[88:91]
buffer_load_dwordx4 v235, s[12:15], s64 offen offset:3072 lds
MM a[92:95],a[168:171],v[88:91],a[92:95]
EX v54,v54
MM a[92:95],a[172:175],v[92:95],a[92:95]
EX v55,v55
MM a[84:87],a[172:175],v[84:87],a[84:87]
EX v56,v56
MM a[84:87],a[168:171],v[80:83],a[84:87]
EX v57,v57
MM a[76:79],a[168:171],v[72:75],a[76:79]
EX v58,v58
MM a[76:79],a[172:175],v[76:79],a[76:79]
EX v59,v59
MM a[68:71],a[172:175],v[68:71],a[68:71]
EX v60,v60
MM a[68:71],a[168:171],v[64:67],a[68:71]
EX v61,v61
MM a[96:99],a[176:179],v[64:67],a[96:99]
EX v62,v62
MM a[96:99],a[180:183],v[68:71],a[96:99]
EX v63,v63
MM a[104:107],a[180:183],v[76:79],a[104:107]
CV v22,v52,v53
CV v23,v54,v55
MM a[104:107],a[176:179],v[72:75],a[104:107]
CV v28,v56,v57
CV v29,v58,v59
MM a[112:115],a[176:179],v[80:83],a[112:115]
CV v30,v60,v61
CV v31,v62,v63
MM a[112:115],a[180:183],v[84:87],a[112:115]
MM a[120:123],a[180:183],v[92:95],a[120:123]
MM a[120:123],a[176:179],v[88:91],a[120:123]
MM a[124:127],a[184:187],v[88:91],a[124:127]
MM a[124:127],a[188:191],v[92:95],a[124:127]
MM a[116:119],a[188:191],v[84:87],a[116:119]
MM a[116:119],a[184:187],v[80:83],a[116:119]
MM a[108:111],a[184:187],v[72:75],a[108:111]
MM a[108:111],a[188:191],v[76:79],a[108:111]
MM a[100:103],a[188:191],v[68:71],a[100:103]
MM a[100:103],a[184:187],v[64:67],a[100:103]
v_smfmac_f32_16x16x64_bf16 v[236:239], v[144:147], v[0:7], v234
v_smfmac_f32_16x16x64_bf16 v[236:239], v[148:151], v[8:15], v234
v_smfmac_f32_16x16x64_bf16 v[236:239], v[152:155], v[16:23], v234
v_smfmac_f32_16x16x64_bf16 v[236:239], v[156:159], v[24:31], v234
s_waitcnt lgkmcnt(0)
s_sub_u32 s70, s70, 1
s_cmp_lg_u32 s70, 0
s_cbranch_scc1 LLA_%=
MM v[64:67],a[192:195],v[160:163],v[128:131]
s_add_u32 s64, s64, 16384
TR a[128:129],v232 offset:0
s_add_u32 s81, s64, 896
MM v[64:67],a[196:199],v[176:179],v[64:67]
TR a[130:131],v232 offset:4096
TR a[132:133],v232 offset:8192
MM v[64:67],a[200:203],v[192:195],v[64:67]
TR a[134:135],v232 offset:12288
TR a[136:137],v233 offset:0
MM v[64:67],a[204:207],v[208:211],v[64:67]
TR a[138:139],v233 offset:4096
v_add_f32 v246, v236, v237
MM v[68:71],a[220:223],v[208:211],v[128:131]
TR a[140:141],v233 offset:8192
v_cmp_nge_f32 vcc, s71, v246
MM v[68:71],a[216:219],v[192:195],v[68:71]
s_cbranch_vccnz LSITE12_%=
LBACK12_%=:
MM v[68:71],a[212:215],v[176:179],v[68:71]
v_lshrrev_b32 v52, 1, v248
v_and_b32 v52, 56, v52
MM v[68:71],a[208:211],v[160:163],v[68:71]
s_mov_b32 s90, 86016
TR a[142:143],v233 offset:12288
TR a[144:145],v232 offset:512
MM v[76:79],a[208:211],v[164:167],v[132:135]
buffer_load_dwordx2 v[48:49], v52, s[20:23], s90 offen offset:0 sc1
MM v[76:79],a[212:215],v[180:183],v[76:79]
EX v64,v64
MM v[76:79],a[216:219],v[196:199],v[76:79]
EX v65,v65
MM v[76:79],a[220:223],v[212:215],v[76:79]
TR a[146:147],v232 offset:4608
CV v64,v64,v65
MM v[72:75],a[204:207],v[212:215],v[132:135]
EX v66,v66
MM v[72:75],a[200:203],v[196:199],v[72:75]
EX v67,v67
MM v[72:75],a[196:199],v[180:183],v[72:75]
TR a[148:149],v232 offset:8704
CV v65,v66,v67
MM v[72:75],a[192:195],v[164:167],v[72:75]
EX v68,v68
MM v[80:83],a[192:195],v[168:171],v[136:139]
EX v69,v69
MM v[80:83],a[196:199],v[184:187],v[80:83]
TR a[150:151],v232 offset:12800
CV v66,v68,v69
MM v[80:83],a[200:203],v[200:203],v[80:83]
EX v70,v70
MM v[80:83],a[204:207],v[216:219],v[80:83]
TR a[152:153],v233 offset:512
TR a[154:155],v233 offset:4608
MM v[84:87],a[220:223],v[216:219],v[136:139]
EX v71,v71
MM v[84:87],a[216:219],v[200:203],v[84:87]
TR a[156:157],v233 offset:8704
CV v67,v70,v71
MM v[84:87],a[212:215],v[184:187],v[84:87]
EX v72,v72
MM v[84:87],a[208:211],v[168:171],v[84:87]
TR a[158:159],v233 offset:12800
TR a[160:161],v232 offset:1024
MM v[92:95],a[208:211],v[172:175],v[140:143]
EX v73,v73
MM v[92:95],a[212:215],v[188:191],v[92:95]
TR a[162:163],v232 offset:5120
CV v72,v72,v73
MM v[92:95],a[216:219],v[204:207],v[92:95]
EX v74,v74
MM v[92:95],a[220:223],v[220:223],v[92:95]
EX v75,v75
MM v[88:91],a[204:207],v[220:223],v[140:143]
TR a[164:165],v232 offset:9216
CV v73,v74,v75
MM v[88:91],a[200:203],v[204:207],v[88:91]
EX v76,v76
MM v[88:91],a[196:199],v[188:191],v[88:91]
TR a[166:167],v232 offset:13312
TR a[168:169],v233 offset:1024
MM v[88:91],a[192:195],v[172:175],v[88:91]
EX v77,v77
MM v[96:99],a[224:227],v[160:163],v[128:131]
TR a[170:171],v233 offset:5120
CV v74,v76,v77
MM v[96:99],a[228:231],v[176:179],v[96:99]
EX v78,v78
MM v[96:99],a[232:235],v[192:195],v[96:99]
EX v79,v79
MM v[96:99],a[236:239],v[208:211],v[96:99]
TR a[172:173],v233 offset:9216
CV v75,v78,v79
MM v[100:103],a[252:255],v[208:211],v[128:131]
)ASM"
R"ASM(
EX v80,v80
MM v[100:103],a[248:251],v[192:195],v[100:103]
TR a[174:175],v233 offset:13312
TR a[176:177],v232 offset:1536
MM v[100:103],a[244:247],v[176:179],v[100:103]
EX v81,v81
MM v[100:103],a[240:243],v[160:163],v[100:103]
TR a[178:179],v232 offset:5632
CV v80,v80,v81
MM v[108:111],a[240:243],v[164:167],v[132:135]
EX v82,v82
MM v[108:111],a[244:247],v[180:183],v[108:111]
EX v83,v83
MM v[108:111],a[248:251],v[196:199],v[108:111]
TR a[180:181],v232 offset:9728
CV v81,v82,v83
MM v[108:111],a[252:255],v[212:215],v[108:111]
EX v84,v84
MM v[104:107],a[236:239],v[212:215],v[132:135]
TR a[182:183],v232 offset:13824
TR a[184:185],v233 offset:1536
MM v[104:107],a[232:235],v[196:199],v[104:107]
EX v85,v85
MM v[104:107],a[228:231],v[180:183],v[104:107]
TR a[186:187],v233 offset:5632
CV v82,v84,v85
MM v[104:107],a[224:227],v[164:167],v[104:107]
EX v86,v86
MM v[112:115],a[224:227],v[168:171],v[136:139]
TR a[188:189],v233 offset:9728
TR a[190:191],v233 offset:13824
MM v[112:115],a[228:231],v[184:187],v[112:115]
EX v87,v87
MM v[112:115],a[232:235],v[200:203],v[112:115]
CV v83,v86,v87
MM v[112:115],a[236:239],v[216:219],v[112:115]
EX v88,v88
MM v[116:119],a[252:255],v[216:219],v[136:139]
EX v89,v89
MM v[116:119],a[248:251],v[200:203],v[116:119]
EX v90,v90
MM v[116:119],a[244:247],v[184:187],v[116:119]
EX v91,v91
MM v[116:119],a[240:243],v[168:171],v[116:119]
EX v92,v92
MM v[124:127],a[240:243],v[172:175],v[140:143]
EX v93,v93
MM v[124:127],a[244:247],v[188:191],v[124:127]
EX v94,v94
MM v[124:127],a[248:251],v[204:207],v[124:127]
EX v95,v95
MM v[124:127],a[252:255],v[220:223],v[124:127]
EX v96,v96
MM v[120:123],a[236:239],v[220:223],v[140:143]
EX v97,v97
MM v[120:123],a[232:235],v[204:207],v[120:123]
EX v98,v98
MM v[120:123],a[228:231],v[188:191],v[120:123]
EX v99,v99
MM v[120:123],a[224:227],v[172:175],v[120:123]
s_waitcnt vmcnt(14) lgkmcnt(0)
s_barrier
MM a[0:3],a[128:131],v[0:3],a[0:3]
s_waitcnt vmcnt(0)
v_cmp_ne_u32 s[76:77], s69, v49
CV v88,v88,v89
MM a[0:3],a[132:135],v[4:7],a[0:3]
s_add_u32 m0, s65, 0
CV v89,v90,v91
CV v90,v92,v93
MM a[8:11],a[132:135],v[12:15],a[8:11]
s_cmp_lg_u64 s[76:77], 0
s_cbranch_scc1 LRIX_%=
LRIXb_%=:
v_readlane_b32 s52, v48, 0
MM a[8:11],a[128:131],v[8:11],a[8:11]
v_readlane_b32 s53, v48, 1
v_readlane_b32 s54, v48, 2
MM a[16:19],a[128:131],v[16:19],a[16:19]
v_readlane_b32 s55, v48, 3
v_readlane_b32 s56, v48, 4
MM a[16:19],a[132:135],v[20:23],a[16:19]
v_readlane_b32 s57, v48, 5
v_readlane_b32 s58, v48, 6
MM a[24:27],a[132:135],v[28:31],a[24:27]
RD a[192:195],v224 offset:32768
v_readlane_b32 s59, v48, 7
MM a[24:27],a[128:131],v[24:27],a[24:27]
RD a[196:199],v225 offset:32768
CV v91,v94,v95
MM a[28:31],a[136:139],v[24:27],a[28:31]
buffer_load_dwordx4 v228, s[8:11], s64 offen offset:0 lds
MM a[28:31],a[140:143],v[28:31],a[28:31]
s_lshl_b32 s90, %[swave], 1
RD a[200:203],v226 offset:32768
CV v68,v96,v97
MM a[20:23],a[140:143],v[20:23],a[20:23]
s_add_u32 s91, s90, 1
v_readlane_b32 s66, v48, s90
v_readlane_b32 s67, v48, s91
MM a[20:23],a[136:139],v[16:19],a[20:23]
s_mul_i32 s91, %[swave], 640
CV v69,v98,v99
MM a[12:15],a[136:139],v[8:11],a[12:15]
EX v100,v100
MM a[12:15],a[140:143],v[12:15],a[12:15]
buffer_load_dwordx4 v229, s[8:11], s64 offen offset:1024 lds
MM a[4:7],a[140:143],v[4:7],a[4:7]
s_add_u32 s91, s91, %[slds]
EX v101,v101
MM a[4:7],a[136:139],v[0:3],a[4:7]
s_bfe_u32 s90, s48, 131077
RD a[204:207],v227 offset:32768
CV v70,v100,v101
MM a[32:35],a[144:147],v[0:3],a[32:35]
s_mul_i32 s90, s90, 320
EX v102,v102
MM a[32:35],a[148:151],v[4:7],a[32:35]
s_add_u32 s90, s90, 131072
EX v103,v103
MM a[40:43],a[148:151],v[12:15],a[40:43]
s_mul_i32 s66, s66, 1280
RD a[208:211],v224 offset:36864
CV v71,v102,v103
MM a[40:43],a[144:147],v[8:11],a[40:43]
s_add_u32 s66, s66, s90
s_add_u32 m0, s91, 151552
s_mov_b64 exec, 0xfffff
buffer_load_dwordx4 v248, s[20:23], s66 offen offset:0 sc1 lds
s_mov_b64 exec, -1
MM a[48:51],a[144:147],v[16:19],a[48:51]
s_mul_i32 s67, s67, 1280
EX v104,v104
MM a[48:51],a[148:151],v[20:23],a[48:51]
s_add_u32 s67, s67, s90
s_add_u32 m0, s91, 151872
s_mov_b64 exec, 0xfffff
buffer_load_dwordx4 v248, s[20:23], s67 offen offset:0 sc1 lds
s_mov_b64 exec, -1
MM a[56:59],a[148:151],v[28:31],a[56:59]
s_add_u32 m0, s65, 0
EX v105,v105
MM a[56:59],a[144:147],v[24:27],a[56:59]
buffer_load_dwordx4 v230, s[8:11], s64 offen offset:2048 lds
MM a[60:63],a[152:155],v[24:27],a[60:63]
EX v106,v106
MM a[60:63],a[156:159],v[28:31],a[60:63]
RD a[212:215],v225 offset:36864
CV v76,v104,v105
MM a[52:55],a[156:159],v[20:23],a[52:55]
EX v107,v107
MM a[52:55],a[152:155],v[16:19],a[52:55]
EX v108,v108
MM a[44:47],a[152:155],v[8:11],a[44:47]
RD a[216:219],v226 offset:36864
CV v77,v106,v107
MM a[44:47],a[156:159],v[12:15],a[44:47]
EX v109,v109
MM a[36:39],a[156:159],v[4:7],a[36:39]
buffer_load_dwordx4 v231, s[8:11], s64 offen offset:3072 lds
MM a[36:39],a[152:155],v[0:3],a[36:39]
s_add_u32 m0, s65, 65536
EX v110,v110
MM a[64:67],a[160:163],v[0:3],a[64:67]
RD a[220:223],v227 offset:36864
CV v78,v108,v109
MM a[64:67],a[164:167],v[4:7],a[64:67]
EX v111,v111
MM a[72:75],a[164:167],v[12:15],a[72:75]
EX v112,v112
MM a[72:75],a[160:163],v[8:11],a[72:75]
RD a[224:227],v224 offset:40960
CV v79,v110,v111
MM a[80:83],a[160:163],v[16:19],a[80:83]
EX v113,v113
MM a[80:83],a[164:167],v[20:23],a[80:83]
buffer_load_dwordx4 v235, s[12:15], s81 offen offset:0 lds
MM a[88:91],a[164:167],v[28:31],a[88:91]
EX v114,v114
MM a[88:91],a[160:163],v[24:27],a[88:91]
RD a[228:231],v225 offset:40960
CV v84,v112,v113
MM a[92:95],a[168:171],v[24:27],a[92:95]
EX v115,v115
MM a[92:95],a[172:175],v[28:31],a[92:95]
RD a[232:235],v226 offset:40960
CV v85,v114,v115
MM a[84:87],a[172:175],v[20:23],a[84:87]
EX v116,v116
MM a[84:87],a[168:171],v[16:19],a[84:87]
EX v117,v117
MM a[76:79],a[168:171],v[8:11],a[76:79]
RD a[236:239],v227 offset:40960
CV v86,v116,v117
MM a[76:79],a[172:175],v[12:15],a[76:79]
EX v118,v118
MM a[68:71],a[172:175],v[4:7],a[68:71]
buffer_load_dwordx4 v235, s[12:15], s64 offen offset:1024 lds
MM a[68:71],a[168:171],v[0:3],a[68:71]
EX v119,v119
MM a[96:99],a[176:179],v[0:3],a[96:99]
RD a[240:243],v224 offset:45056
CV v87,v118,v119
MM a[96:99],a[180:183],v[4:7],a[96:99]
EX v120,v120
MM a[104:107],a[180:183],v[12:15],a[104:107]
LPG9_%=:
s_cbranch_execz LTR0_%=
EX v121,v121
MM a[104:107],a[176:179],v[8:11],a[104:107]
RD a[244:247],v225 offset:45056
CV v92,v120,v121
MM a[112:115],a[176:179],v[16:19],a[112:115]
buffer_load_dwordx4 v235, s[12:15], s81 offen offset:2048 lds
MM a[112:115],a[180:183],v[20:23],a[112:115]
EX v122,v122
MM a[120:123],a[180:183],v[28:31],a[120:123]
EX v123,v123
MM a[120:123],a[176:179],v[24:27],a[120:123]
RD a[248:251],v226 offset:45056
CV v93,v122,v123
MM a[124:127],a[184:187],v[24:27],a[124:127]
EX v124,v124
MM a[124:127],a[188:191],v[28:31],a[124:127]
EX v125,v125
MM a[116:119],a[188:191],v[20:23],a[116:119]
RD a[252:255],v227 offset:45056
CV v94,v124,v125
MM a[116:119],a[184:187],v[16:19],a[116:119]
buffer_load_dwordx4 v235, s[12:15], s64 offen offset:3072 lds
MM a[108:111],a[184:187],v[8:11],a[108:111]
EX v126,v126
MM a[108:111],a[188:191],v[12:15],a[108:111]
EX v127,v127
MM a[100:103],a[188:191],v[4:7],a[100:103]
CV v95,v126,v127
MM a[100:103],a[184:187],v[0:3],a[100:103]
v_smfmac_f32_16x16x64_bf16 v[236:239], v[144:147], v[64:71], v234
v_smfmac_f32_16x16x64_bf16 v[236:239], v[148:151], v[72:79], v234
v_smfmac_f32_16x16x64_bf16 v[236:239], v[152:155], v[80:87], v234
v_smfmac_f32_16x16x64_bf16 v[236:239], v[156:159], v[88:95], v234
s_waitcnt lgkmcnt(0)
MM v[0:3],a[192:195],v[160:163],v[128:131]
s_add_u32 s64, s64, 16384
TR a[128:129],v232 offset:16384
s_add_u32 s81, s64, 896
MM v[0:3],a[196:199],v[176:179],v[0:3]
TR a[130:131],v232 offset:20480
TR a[132:133],v232 offset:24576
MM v[0:3],a[200:203],v[192:195],v[0:3]
TR a[134:135],v232 offset:28672
TR a[136:137],v233 offset:16384
MM v[0:3],a[204:207],v[208:211],v[0:3]
TR a[138:139],v233 offset:20480
v_add_f32 v247, v236, v237
MM v[4:7],a[220:223],v[208:211],v[128:131]
TR a[140:141],v233 offset:24576
v_cmp_nge_f32 vcc, s71, v247
MM v[4:7],a[216:219],v[192:195],v[4:7]
s_cbranch_vccnz LSITE13_%=
LBACK13_%=:
MM v[4:7],a[212:215],v[176:179],v[4:7]
v_lshrrev_b32 v127, 8, v248
v_lshlrev_b32 v124, 9, v127
MM v[4:7],a[208:211],v[160:163],v[4:7]
v_and_b32 v126, 192, v248
v_add_u32 v124, v124, v126
MM v[12:15],a[208:211],v[164:167],v[132:135]
v_bfe_u32 v126, v248, 4, 2
v_lshl_add_u32 v124, v126, 3, v124
MM v[12:15],a[212:215],v[180:183],v[12:15]
s_lshr_b32 s76, %[swave], 1
EX v0,v0
MM v[12:15],a[216:219],v[196:199],v[12:15]
s_mul_i32 s76, s76, 10240
EX v1,v1
MM v[12:15],a[220:223],v[212:215],v[12:15]
s_and_b32 s77, %[swave], 1
TR a[142:143],v233 offset:28672
CV v0,v0,v1
MM v[8:11],a[204:207],v[212:215],v[132:135]
s_lshl_b32 s77, s77, 5
EX v2,v2
MM v[8:11],a[200:203],v[196:199],v[8:11]
s_add_u32 s76, s76, s77
TR a[144:145],v232 offset:16896
TR a[146:147],v232 offset:20992
MM v[8:11],a[196:199],v[180:183],v[8:11]
s_add_u32 s76, s76, %[slds]
EX v3,v3
MM v[8:11],a[192:195],v[164:167],v[8:11]
s_add_u32 s76, s76, 131072
v_add_u32 v124, s76, v124
v_bfe_u32 v126, v248, 4, 3
MM v[16:19],a[192:195],v[168:171],v[136:139]
v_mul_u32_u24 v125, 320, v126
v_lshl_add_u32 v125, v127, 4, v125
MM v[16:19],a[196:199],v[184:187],v[16:19]
s_add_u32 s76, %[slds], 151552
v_add_u32 v125, s76, v125
v_lshl_add_u32 v126, v127, 3, v126
MM v[16:19],a[200:203],v[200:203],v[16:19]
v_lshlrev_b32 v126, 4, v126
CV v1,v2,v3
MM v[16:19],a[204:207],v[216:219],v[16:19]
s_lshr_b32 s76, s48, 7
EX v4,v4
MM v[20:23],a[220:223],v[216:219],v[136:139]
s_lshl_b32 s76, s76, 5
EX v5,v5
MM v[20:23],a[216:219],v[200:203],v[20:23]
s_and_b32 s77, s48, 16
TR a[148:149],v232 offset:25088
TR a[150:151],v232 offset:29184
MM v[20:23],a[212:215],v[184:187],v[20:23]
s_add_u32 s76, s76, s77
EX v6,v6
MM v[20:23],a[208:211],v[168:171],v[20:23]
s_bfe_u32 s77, s48, 131077
EX v7,v7
MM v[28:31],a[208:211],v[172:175],v[140:143]
s_lshl_b32 s77, s77, 2
TR a[152:153],v233 offset:16896
CV v2,v4,v5
MM v[28:31],a[212:215],v[188:191],v[28:31]
s_add_u32 s76, s76, s77
EX v8,v8
MM v[28:31],a[216:219],v[204:207],v[28:31]
s_add_u32 s76, s76, %[swave]
TR a[154:155],v233 offset:20992
CV v3,v6,v7
MM v[28:31],a[220:223],v[220:223],v[28:31]
s_lshl_b32 s76, s76, 9
EX v9,v9
MM v[24:27],a[204:207],v[220:223],v[140:143]
s_add_u32 s76, s76, 22183936
TR a[156:157],v233 offset:25088
TR a[158:159],v233 offset:29184
MM v[24:27],a[200:203],v[204:207],v[24:27]
EX v10,v10
MM v[24:27],a[196:199],v[188:191],v[24:27]
EX v11,v11
MM v[24:27],a[192:195],v[172:175],v[24:27]
TR a[160:161],v232 offset:17408
CV v8,v8,v9
MM v[32:35],a[224:227],v[160:163],v[128:131]
EX v12,v12
MM v[32:35],a[228:231],v[176:179],v[32:35]
TR a[162:163],v232 offset:21504
CV v9,v10,v11
MM v[32:35],a[232:235],v[192:195],v[32:35]
EX v13,v13
MM v[32:35],a[236:239],v[208:211],v[32:35]
TR a[164:165],v232 offset:25600
TR a[166:167],v232 offset:29696
MM v[36:39],a[252:255],v[208:211],v[128:131]
EX v14,v14
MM v[36:39],a[248:251],v[192:195],v[36:39]
EX v15,v15
MM v[36:39],a[244:247],v[176:179],v[36:39]
TR a[168:169],v233 offset:17408
CV v10,v12,v13
MM v[36:39],a[240:243],v[160:163],v[36:39]
EX v16,v16
MM v[44:47],a[240:243],v[164:167],v[132:135]
TR a[170:171],v233 offset:21504
CV v11,v14,v15
MM v[44:47],a[244:247],v[180:183],v[44:47]
EX v17,v17
MM v[44:47],a[248:251],v[196:199],v[44:47]
TR a[172:173],v233 offset:25600
TR a[174:175],v233 offset:29696
MM v[44:47],a[252:255],v[212:215],v[44:47]
EX v18,v18
MM v[40:43],a[236:239],v[212:215],v[132:135]
TR a[176:177],v232 offset:17920
CV v16,v16,v17
MM v[40:43],a[232:235],v[196:199],v[40:43]
EX v19,v19
MM v[40:43],a[228:231],v[180:183],v[40:43]
TR a[178:179],v232 offset:22016
CV v17,v18,v19
MM v[40:43],a[224:227],v[164:167],v[40:43]
EX v20,v20
MM v[48:51],a[224:227],v[168:171],v[136:139]
EX v21,v21
MM v[48:51],a[228:231],v[184:187],v[48:51]
TR a[180:181],v232 offset:26112
TR a[182:183],v232 offset:30208
MM v[48:51],a[232:235],v[200:203],v[48:51]
EX v22,v22
MM v[48:51],a[236:239],v[216:219],v[48:51]
TR a[184:185],v233 offset:17920
)ASM"
R"ASM(
CV v18,v20,v21
MM v[52:55],a[252:255],v[216:219],v[136:139]
EX v23,v23
MM v[52:55],a[248:251],v[200:203],v[52:55]
TR a[186:187],v233 offset:22016
CV v19,v22,v23
MM v[52:55],a[244:247],v[184:187],v[52:55]
EX v24,v24
MM v[52:55],a[240:243],v[168:171],v[52:55]
TR a[188:189],v233 offset:26112
TR a[190:191],v233 offset:30208
MM v[60:63],a[240:243],v[172:175],v[140:143]
EX v25,v25
MM v[60:63],a[244:247],v[188:191],v[60:63]
EX v26,v26
MM v[60:63],a[248:251],v[204:207],v[60:63]
EX v27,v27
MM v[60:63],a[252:255],v[220:223],v[60:63]
EX v28,v28
MM v[56:59],a[236:239],v[220:223],v[140:143]
EX v29,v29
MM v[56:59],a[232:235],v[204:207],v[56:59]
EX v30,v30
MM v[56:59],a[228:231],v[188:191],v[56:59]
EX v31,v31
MM v[56:59],a[224:227],v[172:175],v[56:59]
s_waitcnt vmcnt(6) lgkmcnt(0)
s_barrier
MM a[0:3],a[128:131],v[64:67],a[0:3]
s_add_u32 m0, s65, 16384
CV v24,v24,v25
CV v25,v26,v27
MM a[0:3],a[132:135],v[68:71],a[0:3]
RD a[192:195],v224 offset:49152
CV v26,v28,v29
MM a[8:11],a[132:135],v[76:79],a[8:11]
RD a[196:199],v225 offset:49152
CV v27,v30,v31
MM a[8:11],a[128:131],v[72:75],a[8:11]
buffer_load_dwordx4 v228, s[8:11], s64 offen offset:0 lds
MM a[16:19],a[128:131],v[80:83],a[16:19]
EX v32,v32
MM a[16:19],a[132:135],v[84:87],a[16:19]
EX v33,v33
MM a[24:27],a[132:135],v[92:95],a[24:27]
RD a[200:203],v226 offset:49152
CV v4,v32,v33
MM a[24:27],a[128:131],v[88:91],a[24:27]
EX v34,v34
MM a[28:31],a[136:139],v[88:91],a[28:31]
buffer_load_dwordx4 v229, s[8:11], s64 offen offset:1024 lds
MM a[28:31],a[140:143],v[92:95],a[28:31]
EX v35,v35
MM a[20:23],a[140:143],v[84:87],a[20:23]
RD a[204:207],v227 offset:49152
CV v5,v34,v35
MM a[20:23],a[136:139],v[80:83],a[20:23]
EX v36,v36
MM a[12:15],a[136:139],v[72:75],a[12:15]
TR v[96:97],v124 offset:0
TR v[98:99],v124 offset:256
MM a[12:15],a[140:143],v[76:79],a[12:15]
RD v[108:111],v125 offset:0
TR v[100:101],v124 offset:2048
MM a[4:7],a[140:143],v[68:71],a[4:7]
TR v[102:103],v124 offset:2304
RD v[112:115],v125 offset:64
MM a[4:7],a[136:139],v[64:67],a[4:7]
TR v[104:105],v124 offset:4096
TR v[106:107],v124 offset:4352
MM a[32:35],a[144:147],v[64:67],a[32:35]
RD v[116:119],v125 offset:128
RD a[208:211],v224 offset:53248
MM a[32:35],a[148:151],v[68:71],a[32:35]
EX v37,v37
MM a[40:43],a[148:151],v[76:79],a[40:43]
EX v38,v38
MM a[40:43],a[144:147],v[72:75],a[40:43]
EX v39,v39
MM a[48:51],a[144:147],v[80:83],a[48:51]
CV v6,v36,v37
CV v7,v38,v39
MM a[48:51],a[148:151],v[84:87],a[48:51]
EX v40,v40
MM a[56:59],a[148:151],v[92:95],a[56:59]
EX v41,v41
MM a[56:59],a[144:147],v[88:91],a[56:59]
buffer_load_dwordx4 v230, s[8:11], s64 offen offset:2048 lds
MM a[60:63],a[152:155],v[88:91],a[60:63]
s_waitcnt lgkmcnt(1)
EX v42,v42
MM a[60:63],a[156:159],v[92:95],a[60:63]
RD a[212:215],v225 offset:53248
CV v12,v40,v41
MM v[120:123],v[96:99],v[108:111],0
MM v[120:123],v[100:103],v[112:115],v[120:123]
MM v[120:123],v[104:107],v[116:119],v[120:123]
MM a[52:55],a[156:159],v[84:87],a[52:55]
TR v[96:97],v124 offset:6144
TR v[98:99],v124 offset:6400
MM a[52:55],a[152:155],v[80:83],a[52:55]
RD v[108:111],v125 offset:192
TR v[100:101],v124 offset:8192
MM a[44:47],a[152:155],v[72:75],a[44:47]
TR v[102:103],v124 offset:8448
RD v[112:115],v125 offset:256
EX v43,v43
MM a[44:47],a[156:159],v[76:79],a[44:47]
EX v44,v44
MM a[36:39],a[156:159],v[68:71],a[36:39]
EX v45,v45
MM a[36:39],a[152:155],v[64:67],a[36:39]
RD a[216:219],v226 offset:53248
CV v13,v42,v43
MM a[64:67],a[160:163],v[64:67],a[64:67]
EX v46,v46
MM a[64:67],a[164:167],v[68:71],a[64:67]
EX v47,v47
MM a[72:75],a[164:167],v[76:79],a[72:75]
buffer_load_dwordx4 v231, s[8:11], s64 offen offset:3072 lds
MM a[72:75],a[160:163],v[72:75],a[72:75]
s_add_u32 m0, s65, 81920
RD a[220:223],v227 offset:53248
CV v14,v44,v45
MM a[80:83],a[160:163],v[80:83],a[80:83]
s_waitcnt lgkmcnt(2)
EX v48,v48
MM a[80:83],a[164:167],v[84:87],a[80:83]
EX v49,v49
MM v[120:123],v[96:99],v[108:111],v[120:123]
MM v[120:123],v[100:103],v[112:115],v[120:123]
MM a[88:91],a[164:167],v[92:95],a[88:91]
RD a[224:227],v224 offset:57344
CV v15,v46,v47
MM a[88:91],a[160:163],v[88:91],a[88:91]
EX v50,v50
MM a[92:95],a[168:171],v[88:91],a[92:95]
EX v51,v51
MM a[92:95],a[172:175],v[92:95],a[92:95]
buffer_load_dwordx4 v235, s[12:15], s81 offen offset:0 lds
MM a[84:87],a[172:175],v[84:87],a[84:87]
RD a[228:231],v225 offset:57344
CV v20,v48,v49
MM a[84:87],a[168:171],v[80:83],a[84:87]
EX v52,v52
MM a[76:79],a[168:171],v[72:75],a[76:79]
s_mov_b32 exec_lo, 0x00ff00ff
s_mov_b32 exec_hi, 0x00ff00ff
buffer_store_dwordx4 v[120:123], v126, s[20:23], s76 offen offset:0 sc1
s_mov_b64 exec, -1
s_nop 0
MM a[76:79],a[172:175],v[76:79],a[76:79]
s_lshl_b32 s76, s50, 2
EX v53,v53
MM a[68:71],a[172:175],v[68:71],a[68:71]
s_add_u32 s76, s76, s84
RD a[232:235],v226 offset:57344
CV v21,v50,v51
MM a[68:71],a[168:171],v[64:67],a[68:71]
s_lshl_b32 s76, s76, 3
EX v54,v54
MM a[96:99],a[176:179],v[64:67],a[96:99]
s_lshr_b32 s77, s49, 2
EX v55,v55
MM a[96:99],a[180:183],v[68:71],a[96:99]
RD a[236:239],v227 offset:57344
CV v22,v52,v53
MM a[104:107],a[180:183],v[76:79],a[104:107]
EX v56,v56
MM a[104:107],a[176:179],v[72:75],a[104:107]
buffer_load_dwordx4 v235, s[12:15], s64 offen offset:1024 lds
MM a[112:115],a[176:179],v[80:83],a[112:115]
s_add_u32 s76, s76, s77
EX v57,v57
MM a[112:115],a[180:183],v[84:87],a[112:115]
s_mul_i32 s95, s76, 82944
RD a[240:243],v224 offset:61440
CV v23,v54,v55
MM a[120:123],a[180:183],v[92:95],a[120:123]
s_mul_i32 s76, %[swave], 20480
EX v58,v58
MM a[120:123],a[176:179],v[88:91],a[120:123]
s_add_u32 s95, s95, s76
EX v59,v59
MM a[124:127],a[184:187],v[88:91],a[124:127]
s_add_u32 s95, s95, 22708224
v_mov_b32 v240, -1
v_mov_b32 v241, -1
MM a[124:127],a[188:191],v[92:95],a[124:127]
v_mov_b32 v242, -1
v_mov_b32 v243, -1
MM a[116:119],a[188:191],v[84:87],a[116:119]
EX v60,v60
MM a[116:119],a[184:187],v[80:83],a[116:119]
RD a[244:247],v225 offset:61440
CV v28,v56,v57
MM a[108:111],a[184:187],v[72:75],a[108:111]
EX v61,v61
MM a[108:111],a[188:191],v[76:79],a[108:111]
buffer_load_dwordx4 v235, s[12:15], s81 offen offset:2048 lds
MM a[100:103],a[188:191],v[68:71],a[100:103]
EX v62,v62
MM a[100:103],a[184:187],v[64:67],a[100:103]
RD a[248:251],v226 offset:61440
CV v29,v58,v59
v_smfmac_f32_16x16x64_bf16 v[236:239], v[144:147], v[0:7], v234
RD a[252:255],v227 offset:61440
EX v63,v63
v_smfmac_f32_16x16x64_bf16 v[236:239], v[148:151], v[8:15], v234
CV v30,v60,v61
v_smfmac_f32_16x16x64_bf16 v[236:239], v[152:155], v[16:23], v234
CV v31,v62,v63
s_nop 1
v_smfmac_f32_16x16x64_bf16 v[236:239], v[156:159], v[24:31], v234
s_waitcnt lgkmcnt(0)
buffer_load_dwordx4 v235, s[12:15], s64 offen offset:3072 lds
MM v[64:67],a[192:195],v[160:163],v[128:131]
s_add_u32 s64, s64, 16384
TR a[128:129],v232 offset:32768
s_add_u32 s81, s64, 896
MM v[64:67],a[196:199],v[176:179],v[64:67]
TR a[130:131],v232 offset:36864
TR a[132:133],v232 offset:40960
MM v[64:67],a[200:203],v[192:195],v[64:67]
TR a[134:135],v232 offset:45056
TR a[136:137],v233 offset:32768
MM v[64:67],a[204:207],v[208:211],v[64:67]
TR a[138:139],v233 offset:36864
v_add_f32 v246, v236, v237
MM v[68:71],a[220:223],v[208:211],v[128:131]
TR a[140:141],v233 offset:40960
v_cmp_nge_f32 vcc, s71, v246
MM v[68:71],a[216:219],v[192:195],v[68:71]
s_cbranch_vccnz LSITE14_%=
LBACK14_%=:
MM v[68:71],a[212:215],v[176:179],v[68:71]
s_lshr_b32 s77, %[sb], 3
TR a[142:143],v233 offset:45056
TR a[144:145],v232 offset:33280
MM v[68:71],a[208:211],v[160:163],v[68:71]
s_lshl_b32 s77, s77, 2
TR a[146:147],v232 offset:37376
TR a[148:149],v232 offset:41472
MM v[76:79],a[208:211],v[164:167],v[132:135]
s_add_u32 s77, s77, %[swave]
EX v64,v64
MM v[76:79],a[212:215],v[180:183],v[76:79]
s_lshl_b32 s77, s77, 9
EX v65,v65
MM v[76:79],a[216:219],v[196:199],v[76:79]
s_add_u32 s77, s77, 0
v_lshrrev_b32 v253, 1, v248
v_add_u32 v253, s77, v253
MM v[76:79],a[220:223],v[212:215],v[76:79]
s_mov_b32 s77, LKEND_ka-LPD0_%=-16
v_min_u32 v253, s77, v253
CV v64,v64,v65
MM v[72:75],a[204:207],v[212:215],v[132:135]
s_getpc_b64 s[98:99]
LPD0_%=:
global_load_dword v253, v253, s[98:99]
MM v[72:75],a[200:203],v[196:199],v[72:75]
EX v66,v66
MM v[72:75],a[196:199],v[180:183],v[72:75]
EX v67,v67
MM v[72:75],a[192:195],v[164:167],v[72:75]
TR a[150:151],v232 offset:45568
CV v65,v66,v67
MM v[80:83],a[192:195],v[168:171],v[136:139]
EX v68,v68
MM v[80:83],a[196:199],v[184:187],v[80:83]
EX v69,v69
MM v[80:83],a[200:203],v[200:203],v[80:83]
TR a[152:153],v233 offset:33280
CV v66,v68,v69
MM v[80:83],a[204:207],v[216:219],v[80:83]
EX v70,v70
MM v[84:87],a[220:223],v[216:219],v[136:139]
EX v71,v71
MM v[84:87],a[216:219],v[200:203],v[84:87]
TR a[154:155],v233 offset:37376
CV v67,v70,v71
MM v[84:87],a[212:215],v[184:187],v[84:87]
EX v72,v72
MM v[84:87],a[208:211],v[168:171],v[84:87]
TR a[156:157],v233 offset:41472
TR a[158:159],v233 offset:45568
MM v[92:95],a[208:211],v[172:175],v[140:143]
EX v73,v73
MM v[92:95],a[212:215],v[188:191],v[92:95]
TR a[160:161],v232 offset:33792
CV v72,v72,v73
MM v[92:95],a[216:219],v[204:207],v[92:95]
EX v74,v74
MM v[92:95],a[220:223],v[220:223],v[92:95]
EX v75,v75
MM v[88:91],a[204:207],v[220:223],v[140:143]
TR a[162:163],v232 offset:37888
CV v73,v74,v75
MM v[88:91],a[200:203],v[204:207],v[88:91]
TR a[164:165],v232 offset:41984
TR a[166:167],v232 offset:46080
MM v[88:91],a[196:199],v[188:191],v[88:91]
EX v76,v76
MM v[88:91],a[192:195],v[172:175],v[88:91]
EX v77,v77
MM v[96:99],a[224:227],v[160:163],v[128:131]
TR a[168:169],v233 offset:33792
CV v74,v76,v77
MM v[96:99],a[228:231],v[176:179],v[96:99]
EX v78,v78
MM v[96:99],a[232:235],v[192:195],v[96:99]
TR a[170:171],v233 offset:37888
TR a[172:173],v233 offset:41984
MM v[96:99],a[236:239],v[208:211],v[96:99]
EX v79,v79
MM v[100:103],a[252:255],v[208:211],v[128:131]
TR a[174:175],v233 offset:46080
CV v75,v78,v79
MM v[100:103],a[248:251],v[192:195],v[100:103]
EX v80,v80
MM v[100:103],a[244:247],v[176:179],v[100:103]
EX v81,v81
MM v[100:103],a[240:243],v[160:163],v[100:103]
TR a[176:177],v232 offset:34304
CV v80,v80,v81
MM v[108:111],a[240:243],v[164:167],v[132:135]
s_add_u32 s67, s95, 10240
EX v82,v82
MM v[108:111],a[244:247],v[180:183],v[108:111]
buffer_store_dwordx4 v[240:243], v248, s[20:23], s67 offen offset:0 sc1
MM v[108:111],a[248:251],v[196:199],v[108:111]
TR a[178:179],v232 offset:38400
TR a[180:181],v232 offset:42496
MM v[108:111],a[252:255],v[212:215],v[108:111]
EX v83,v83
MM v[104:107],a[236:239],v[212:215],v[132:135]
TR a[182:183],v232 offset:46592
CV v81,v82,v83
MM v[104:107],a[232:235],v[196:199],v[104:107]
EX v84,v84
MM v[104:107],a[228:231],v[180:183],v[104:107]
EX v85,v85
MM v[104:107],a[224:227],v[164:167],v[104:107]
TR a[184:185],v233 offset:34304
CV v82,v84,v85
MM v[112:115],a[224:227],v[168:171],v[136:139]
EX v86,v86
MM v[112:115],a[228:231],v[184:187],v[112:115]
TR a[186:187],v233 offset:38400
TR a[188:189],v233 offset:42496
MM v[112:115],a[232:235],v[200:203],v[112:115]
EX v87,v87
MM v[112:115],a[236:239],v[216:219],v[112:115]
TR a[190:191],v233 offset:46592
CV v83,v86,v87
MM v[116:119],a[252:255],v[216:219],v[136:139]
EX v88,v88
MM v[116:119],a[248:251],v[200:203],v[116:119]
EX v89,v89
MM v[116:119],a[244:247],v[184:187],v[116:119]
EX v90,v90
MM v[116:119],a[240:243],v[168:171],v[116:119]
EX v91,v91
MM v[124:127],a[240:243],v[172:175],v[140:143]
EX v92,v92
MM v[124:127],a[244:247],v[188:191],v[124:127]
EX v93,v93
MM v[124:127],a[248:251],v[204:207],v[124:127]
EX v94,v94
MM v[124:127],a[252:255],v[220:223],v[124:127]
EX v95,v95
MM v[120:123],a[236:239],v[220:223],v[140:143]
EX v96,v96
MM v[120:123],a[232:235],v[204:207],v[120:123]
EX v97,v97
MM v[120:123],a[228:231],v[188:191],v[120:123]
EX v98,v98
MM v[120:123],a[224:227],v[172:175],v[120:123]
s_waitcnt vmcnt(15) lgkmcnt(0)
s_barrier
MM a[0:3],a[128:131],v[0:3],a[0:3]
s_add_u32 m0, s65, 32768
CV v88,v88,v89
CV v89,v90,v91
MM a[0:3],a[132:135],v[4:7],a[0:3]
RD a[192:195],v224 offset:0
CV v90,v92,v93
MM a[8:11],a[132:135],v[12:15],a[8:11]
RD a[196:199],v225 offset:0
CV v91,v94,v95
MM a[8:11],a[128:131],v[8:11],a[8:11]
buffer_load_dwordx4 v228, s[8:11], s64 offen offset:0 lds
)ASM"
R"ASM(
MM a[16:19],a[128:131],v[16:19],a[16:19]
RD a[200:203],v226 offset:0
CV v68,v96,v97
MM a[16:19],a[132:135],v[20:23],a[16:19]
buffer_load_dwordx4 v229, s[8:11], s64 offen offset:1024 lds
MM a[24:27],a[132:135],v[28:31],a[24:27]
RD a[204:207],v227 offset:0
RD a[208:211],v224 offset:4096
MM a[24:27],a[128:131],v[24:27],a[24:27]
EX v99,v99
MM a[28:31],a[136:139],v[24:27],a[28:31]
RD a[212:215],v225 offset:4096
CV v69,v98,v99
MM a[28:31],a[140:143],v[28:31],a[28:31]
EX v100,v100
MM a[20:23],a[140:143],v[20:23],a[20:23]
buffer_load_dwordx4 v230, s[8:11], s64 offen offset:2048 lds
MM a[20:23],a[136:139],v[16:19],a[20:23]
LPG10_%=:
s_cbranch_execz LTR0_%=
EX v101,v101
MM a[12:15],a[136:139],v[8:11],a[12:15]
RD a[216:219],v226 offset:4096
CV v70,v100,v101
MM a[12:15],a[140:143],v[12:15],a[12:15]
EX v102,v102
MM a[4:7],a[140:143],v[4:7],a[4:7]
EX v103,v103
MM a[4:7],a[136:139],v[0:3],a[4:7]
RD a[220:223],v227 offset:4096
CV v71,v102,v103
MM a[32:35],a[144:147],v[0:3],a[32:35]
EX v104,v104
MM a[32:35],a[148:151],v[4:7],a[32:35]
buffer_load_dwordx4 v231, s[8:11], s64 offen offset:3072 lds
MM a[40:43],a[148:151],v[12:15],a[40:43]
s_add_u32 m0, s65, 98304
EX v105,v105
MM a[40:43],a[144:147],v[8:11],a[40:43]
RD a[224:227],v224 offset:8192
CV v76,v104,v105
MM a[48:51],a[144:147],v[16:19],a[48:51]
EX v106,v106
MM a[48:51],a[148:151],v[20:23],a[48:51]
EX v107,v107
MM a[56:59],a[148:151],v[28:31],a[56:59]
RD a[228:231],v225 offset:8192
CV v77,v106,v107
MM a[56:59],a[144:147],v[24:27],a[56:59]
EX v108,v108
MM a[60:63],a[152:155],v[24:27],a[60:63]
buffer_load_dwordx4 v235, s[12:15], s81 offen offset:0 lds
MM a[60:63],a[156:159],v[28:31],a[60:63]
EX v109,v109
MM a[52:55],a[156:159],v[20:23],a[52:55]
RD a[232:235],v226 offset:8192
CV v78,v108,v109
MM a[52:55],a[152:155],v[16:19],a[52:55]
EX v110,v110
MM a[44:47],a[152:155],v[8:11],a[44:47]
EX v111,v111
MM a[44:47],a[156:159],v[12:15],a[44:47]
RD a[236:239],v227 offset:8192
CV v79,v110,v111
MM a[36:39],a[156:159],v[4:7],a[36:39]
buffer_load_dwordx4 v235, s[12:15], s64 offen offset:1024 lds
MM a[36:39],a[152:155],v[0:3],a[36:39]
EX v112,v112
MM a[64:67],a[160:163],v[0:3],a[64:67]
EX v113,v113
MM a[64:67],a[164:167],v[4:7],a[64:67]
RD a[240:243],v224 offset:12288
CV v84,v112,v113
MM a[72:75],a[164:167],v[12:15],a[72:75]
EX v114,v114
MM a[72:75],a[160:163],v[8:11],a[72:75]
RD a[244:247],v225 offset:12288
RD a[248:251],v226 offset:12288
MM a[80:83],a[160:163],v[16:19],a[80:83]
EX v115,v115
MM a[80:83],a[164:167],v[20:23],a[80:83]
RD a[252:255],v227 offset:12288
CV v85,v114,v115
MM a[88:91],a[164:167],v[28:31],a[88:91]
EX v116,v116
MM a[88:91],a[160:163],v[24:27],a[88:91]
buffer_load_dwordx4 v235, s[12:15], s81 offen offset:2048 lds
MM a[92:95],a[168:171],v[24:27],a[92:95]
EX v117,v117
MM a[92:95],a[172:175],v[28:31],a[92:95]
EX v118,v118
MM a[84:87],a[172:175],v[20:23],a[84:87]
buffer_load_dwordx4 v235, s[12:15], s64 offen offset:3072 lds
MM a[84:87],a[168:171],v[16:19],a[84:87]
EX v119,v119
MM a[76:79],a[168:171],v[8:11],a[76:79]
EX v120,v120
MM a[76:79],a[172:175],v[12:15],a[76:79]
EX v121,v121
MM a[68:71],a[172:175],v[4:7],a[68:71]
EX v122,v122
MM a[68:71],a[168:171],v[0:3],a[68:71]
EX v123,v123
MM a[96:99],a[176:179],v[0:3],a[96:99]
EX v124,v124
MM a[96:99],a[180:183],v[4:7],a[96:99]
EX v125,v125
MM a[104:107],a[180:183],v[12:15],a[104:107]
EX v126,v126
MM a[104:107],a[176:179],v[8:11],a[104:107]
EX v127,v127
MM a[112:115],a[176:179],v[16:19],a[112:115]
CV v86,v116,v117
CV v87,v118,v119
MM a[112:115],a[180:183],v[20:23],a[112:115]
CV v92,v120,v121
CV v93,v122,v123
MM a[120:123],a[180:183],v[28:31],a[120:123]
CV v94,v124,v125
CV v95,v126,v127
MM a[120:123],a[176:179],v[24:27],a[120:123]
MM a[124:127],a[184:187],v[24:27],a[124:127]
MM a[124:127],a[188:191],v[28:31],a[124:127]
MM a[116:119],a[188:191],v[20:23],a[116:119]
MM a[116:119],a[184:187],v[16:19],a[116:119]
MM a[108:111],a[184:187],v[8:11],a[108:111]
MM a[108:111],a[188:191],v[12:15],a[108:111]
MM a[100:103],a[188:191],v[4:7],a[100:103]
MM a[100:103],a[184:187],v[0:3],a[100:103]
v_smfmac_f32_16x16x64_bf16 v[236:239], v[144:147], v[64:71], v234
v_smfmac_f32_16x16x64_bf16 v[236:239], v[148:151], v[72:79], v234
v_smfmac_f32_16x16x64_bf16 v[236:239], v[152:155], v[80:87], v234
v_smfmac_f32_16x16x64_bf16 v[236:239], v[156:159], v[88:95], v234
s_waitcnt lgkmcnt(0)
MM v[0:3],a[192:195],v[160:163],v[128:131]
s_add_u32 s64, s64, 16384
TR a[128:129],v232 offset:49152
s_add_u32 s81, s64, 896
MM v[0:3],a[196:199],v[176:179],v[0:3]
TR a[130:131],v232 offset:53248
TR a[132:133],v232 offset:57344
MM v[0:3],a[200:203],v[192:195],v[0:3]
TR a[134:135],v232 offset:61440
TR a[136:137],v233 offset:49152
MM v[0:3],a[204:207],v[208:211],v[0:3]
TR a[138:139],v233 offset:53248
v_add_f32 v247, v236, v237
MM v[4:7],a[220:223],v[208:211],v[128:131]
TR a[140:141],v233 offset:57344
v_cmp_nge_f32 vcc, s71, v247
MM v[4:7],a[216:219],v[192:195],v[4:7]
s_cbranch_vccnz LSITE15_%=
LBACK15_%=:
MM v[4:7],a[212:215],v[176:179],v[4:7]
s_add_u32 s67, s95, 11264
TR a[142:143],v233 offset:61440
TR a[144:145],v232 offset:49664
MM v[4:7],a[208:211],v[160:163],v[4:7]
buffer_store_dwordx4 v[240:243], v248, s[20:23], s67 offen offset:0 sc1
MM v[12:15],a[208:211],v[164:167],v[132:135]
EX v0,v0
MM v[12:15],a[212:215],v[180:183],v[12:15]
EX v1,v1
MM v[12:15],a[216:219],v[196:199],v[12:15]
TR a[146:147],v232 offset:53760
CV v0,v0,v1
MM v[12:15],a[220:223],v[212:215],v[12:15]
EX v2,v2
MM v[8:11],a[204:207],v[212:215],v[132:135]
EX v3,v3
MM v[8:11],a[200:203],v[196:199],v[8:11]
TR a[148:149],v232 offset:57856
CV v1,v2,v3
MM v[8:11],a[196:199],v[180:183],v[8:11]
EX v4,v4
MM v[8:11],a[192:195],v[164:167],v[8:11]
TR a[150:151],v232 offset:61952
TR a[152:153],v233 offset:49664
MM v[16:19],a[192:195],v[168:171],v[136:139]
EX v5,v5
MM v[16:19],a[196:199],v[184:187],v[16:19]
TR a[154:155],v233 offset:53760
CV v2,v4,v5
MM v[16:19],a[200:203],v[200:203],v[16:19]
EX v6,v6
MM v[16:19],a[204:207],v[216:219],v[16:19]
EX v7,v7
MM v[20:23],a[220:223],v[216:219],v[136:139]
TR a[156:157],v233 offset:57856
CV v3,v6,v7
MM v[20:23],a[216:219],v[200:203],v[20:23]
EX v8,v8
MM v[20:23],a[212:215],v[184:187],v[20:23]
TR a[158:159],v233 offset:61952
TR a[160:161],v232 offset:50176
MM v[20:23],a[208:211],v[168:171],v[20:23]
EX v9,v9
MM v[28:31],a[208:211],v[172:175],v[140:143]
TR a[162:163],v232 offset:54272
CV v8,v8,v9
MM v[28:31],a[212:215],v[188:191],v[28:31]
EX v10,v10
MM v[28:31],a[216:219],v[204:207],v[28:31]
EX v11,v11
MM v[28:31],a[220:223],v[220:223],v[28:31]
TR a[164:165],v232 offset:58368
CV v9,v10,v11
MM v[24:27],a[204:207],v[220:223],v[140:143]
EX v12,v12
MM v[24:27],a[200:203],v[204:207],v[24:27]
TR a[166:167],v232 offset:62464
TR a[168:169],v233 offset:50176
MM v[24:27],a[196:199],v[188:191],v[24:27]
EX v13,v13
MM v[24:27],a[192:195],v[172:175],v[24:27]
TR a[170:171],v233 offset:54272
CV v10,v12,v13
MM v[32:35],a[224:227],v[160:163],v[128:131]
EX v14,v14
MM v[32:35],a[228:231],v[176:179],v[32:35]
EX v15,v15
MM v[32:35],a[232:235],v[192:195],v[32:35]
TR a[172:173],v233 offset:58368
CV v11,v14,v15
MM v[32:35],a[236:239],v[208:211],v[32:35]
TR a[174:175],v233 offset:62464
TR a[176:177],v232 offset:50688
MM v[36:39],a[252:255],v[208:211],v[128:131]
EX v16,v16
MM v[36:39],a[248:251],v[192:195],v[36:39]
EX v17,v17
MM v[36:39],a[244:247],v[176:179],v[36:39]
TR a[178:179],v232 offset:54784
CV v16,v16,v17
MM v[36:39],a[240:243],v[160:163],v[36:39]
EX v18,v18
MM v[44:47],a[240:243],v[164:167],v[132:135]
s_add_u32 s67, s95, 12288
TR a[180:181],v232 offset:58880
TR a[182:183],v232 offset:62976
MM v[44:47],a[244:247],v[180:183],v[44:47]
buffer_store_dwordx4 v[240:243], v248, s[20:23], s67 offen offset:0 sc1
MM v[44:47],a[248:251],v[196:199],v[44:47]
EX v19,v19
MM v[44:47],a[252:255],v[212:215],v[44:47]
TR a[184:185],v233 offset:50688
CV v17,v18,v19
MM v[40:43],a[236:239],v[212:215],v[132:135]
EX v20,v20
MM v[40:43],a[232:235],v[196:199],v[40:43]
EX v21,v21
MM v[40:43],a[228:231],v[180:183],v[40:43]
TR a[186:187],v233 offset:54784
CV v18,v20,v21
MM v[40:43],a[224:227],v[164:167],v[40:43]
EX v22,v22
MM v[48:51],a[224:227],v[168:171],v[136:139]
TR a[188:189],v233 offset:58880
TR a[190:191],v233 offset:62976
MM v[48:51],a[228:231],v[184:187],v[48:51]
EX v23,v23
MM v[48:51],a[232:235],v[200:203],v[48:51]
CV v19,v22,v23
MM v[48:51],a[236:239],v[216:219],v[48:51]
EX v24,v24
MM v[52:55],a[252:255],v[216:219],v[136:139]
EX v25,v25
MM v[52:55],a[248:251],v[200:203],v[52:55]
EX v26,v26
MM v[52:55],a[244:247],v[184:187],v[52:55]
EX v27,v27
MM v[52:55],a[240:243],v[168:171],v[52:55]
EX v28,v28
MM v[60:63],a[240:243],v[172:175],v[140:143]
EX v29,v29
MM v[60:63],a[244:247],v[188:191],v[60:63]
EX v30,v30
MM v[60:63],a[248:251],v[204:207],v[60:63]
EX v31,v31
MM v[60:63],a[252:255],v[220:223],v[60:63]
EX v32,v32
MM v[56:59],a[236:239],v[220:223],v[140:143]
EX v33,v33
MM v[56:59],a[232:235],v[204:207],v[56:59]
EX v34,v34
MM v[56:59],a[228:231],v[188:191],v[56:59]
EX v35,v35
MM v[56:59],a[224:227],v[172:175],v[56:59]
s_waitcnt vmcnt(17) lgkmcnt(0)
s_barrier
MM a[0:3],a[128:131],v[64:67],a[0:3]
s_lshr_b32 s91, s48, 7
CV v24,v24,v25
CV v25,v26,v27
MM a[0:3],a[132:135],v[68:71],a[0:3]
s_lshl_b32 s91, s91, 5
RD a[192:195],v224 offset:16384
CV v26,v28,v29
MM a[8:11],a[132:135],v[76:79],a[8:11]
s_and_b32 s77, s48, 16
s_add_u32 m0, s65, 49152
CV v27,v30,v31
MM a[8:11],a[128:131],v[72:75],a[8:11]
buffer_load_dwordx4 v228, s[8:11], s64 offen offset:0 lds
MM a[16:19],a[128:131],v[80:83],a[16:19]
s_add_u32 s91, s91, s77
RD a[196:199],v225 offset:16384
CV v4,v32,v33
MM a[16:19],a[132:135],v[84:87],a[16:19]
s_bfe_u32 s77, s48, 131077
RD a[200:203],v226 offset:16384
CV v5,v34,v35
MM a[24:27],a[132:135],v[92:95],a[24:27]
s_lshl_b32 s77, s77, 2
RD a[204:207],v227 offset:16384
MM a[24:27],a[128:131],v[88:91],a[24:27]
buffer_load_dwordx4 v229, s[8:11], s64 offen offset:1024 lds
MM a[28:31],a[136:139],v[88:91],a[28:31]
s_add_u32 s91, s91, s77
RD a[208:211],v224 offset:20480
MM a[28:31],a[140:143],v[92:95],a[28:31]
s_add_u32 s91, s91, %[swave]
EX v36,v36
MM a[20:23],a[140:143],v[84:87],a[20:23]
buffer_load_dwordx4 v230, s[8:11], s64 offen offset:2048 lds
MM a[20:23],a[136:139],v[80:83],a[20:23]
s_lshl_b32 s91, s91, 2
EX v37,v37
MM a[12:15],a[136:139],v[72:75],a[12:15]
s_add_u32 s91, s91, 77824
RD a[212:215],v225 offset:20480
CV v6,v36,v37
MM a[12:15],a[140:143],v[76:79],a[12:15]
s_waitcnt vmcnt(18)
v_mov_b32 v250, s69
s_mov_b64 exec, 1
buffer_store_dword v250, off, s[20:23], s91 offset:0 sc1
s_mov_b64 exec, -1
MM a[4:7],a[140:143],v[68:71],a[4:7]
EX v38,v38
MM a[4:7],a[136:139],v[64:67],a[4:7]
EX v39,v39
MM a[32:35],a[144:147],v[64:67],a[32:35]
RD a[216:219],v226 offset:20480
CV v7,v38,v39
MM a[32:35],a[148:151],v[68:71],a[32:35]
EX v40,v40
MM a[40:43],a[148:151],v[76:79],a[40:43]
RD a[220:223],v227 offset:20480
RD a[224:227],v224 offset:24576
MM a[40:43],a[144:147],v[72:75],a[40:43]
EX v41,v41
MM a[48:51],a[144:147],v[80:83],a[48:51]
RD a[228:231],v225 offset:24576
CV v12,v40,v41
MM a[48:51],a[148:151],v[84:87],a[48:51]
EX v42,v42
MM a[56:59],a[148:151],v[92:95],a[56:59]
buffer_load_dwordx4 v231, s[8:11], s64 offen offset:3072 lds
MM a[56:59],a[144:147],v[88:91],a[56:59]
s_add_u32 m0, s65, 114688
EX v43,v43
MM a[60:63],a[152:155],v[88:91],a[60:63]
RD a[232:235],v226 offset:24576
CV v13,v42,v43
MM a[60:63],a[156:159],v[92:95],a[60:63]
EX v44,v44
MM a[52:55],a[156:159],v[84:87],a[52:55]
EX v45,v45
MM a[52:55],a[152:155],v[80:83],a[52:55]
RD a[236:239],v227 offset:24576
CV v14,v44,v45
MM a[44:47],a[152:155],v[72:75],a[44:47]
EX v46,v46
MM a[44:47],a[156:159],v[76:79],a[44:47]
buffer_load_dwordx4 v235, s[12:15], s81 offen offset:0 lds
MM a[36:39],a[156:159],v[68:71],a[36:39]
EX v47,v47
MM a[36:39],a[152:155],v[64:67],a[36:39]
RD a[240:243],v224 offset:28672
CV v15,v46,v47
MM a[64:67],a[160:163],v[64:67],a[64:67]
EX v48,v48
MM a[64:67],a[164:167],v[68:71],a[64:67]
EX v49,v49
MM a[72:75],a[164:167],v[76:79],a[72:75]
RD a[244:247],v225 offset:28672
CV v20,v48,v49
MM a[72:75],a[160:163],v[72:75],a[72:75]
buffer_load_dwordx4 v235, s[12:15], s64 offen offset:1024 lds
)ASM"
R"ASM(
MM a[80:83],a[160:163],v[80:83],a[80:83]
EX v50,v50
MM a[80:83],a[164:167],v[84:87],a[80:83]
EX v51,v51
MM a[88:91],a[164:167],v[92:95],a[88:91]
RD a[248:251],v226 offset:28672
CV v21,v50,v51
MM a[88:91],a[160:163],v[88:91],a[88:91]
EX v52,v52
MM a[92:95],a[168:171],v[88:91],a[92:95]
EX v53,v53
MM a[92:95],a[172:175],v[92:95],a[92:95]
RD a[252:255],v227 offset:28672
CV v22,v52,v53
MM a[84:87],a[172:175],v[84:87],a[84:87]
buffer_load_dwordx4 v235, s[12:15], s81 offen offset:2048 lds
MM a[84:87],a[168:171],v[80:83],a[84:87]
EX v54,v54
MM a[76:79],a[168:171],v[72:75],a[76:79]
EX v55,v55
MM a[76:79],a[172:175],v[76:79],a[76:79]
CV v23,v54,v55
MM a[68:71],a[172:175],v[68:71],a[68:71]
EX v56,v56
MM a[68:71],a[168:171],v[64:67],a[68:71]
EX v57,v57
MM a[96:99],a[176:179],v[64:67],a[96:99]
buffer_load_dwordx4 v235, s[12:15], s64 offen offset:3072 lds
MM a[96:99],a[180:183],v[68:71],a[96:99]
EX v58,v58
MM a[104:107],a[180:183],v[76:79],a[104:107]
EX v59,v59
MM a[104:107],a[176:179],v[72:75],a[104:107]
EX v60,v60
MM a[112:115],a[176:179],v[80:83],a[112:115]
EX v61,v61
MM a[112:115],a[180:183],v[84:87],a[112:115]
EX v62,v62
MM a[120:123],a[180:183],v[92:95],a[120:123]
EX v63,v63
MM a[120:123],a[176:179],v[88:91],a[120:123]
CV v28,v56,v57
CV v29,v58,v59
MM a[124:127],a[184:187],v[88:91],a[124:127]
CV v30,v60,v61
CV v31,v62,v63
MM a[124:127],a[188:191],v[92:95],a[124:127]
MM a[116:119],a[188:191],v[84:87],a[116:119]
MM a[116:119],a[184:187],v[80:83],a[116:119]
MM a[108:111],a[184:187],v[72:75],a[108:111]
MM a[108:111],a[188:191],v[76:79],a[108:111]
MM a[100:103],a[188:191],v[68:71],a[100:103]
MM a[100:103],a[184:187],v[64:67],a[100:103]
v_smfmac_f32_16x16x64_bf16 v[236:239], v[144:147], v[0:7], v234
v_smfmac_f32_16x16x64_bf16 v[236:239], v[148:151], v[8:15], v234
v_smfmac_f32_16x16x64_bf16 v[236:239], v[152:155], v[16:23], v234
v_smfmac_f32_16x16x64_bf16 v[236:239], v[156:159], v[24:31], v234
s_waitcnt lgkmcnt(0)
MM v[64:67],a[192:195],v[160:163],v[128:131]
s_add_u32 s64, s64, 16384
TR a[128:129],v232 offset:0
s_add_u32 s81, s64, 896
MM v[64:67],a[196:199],v[176:179],v[64:67]
TR a[130:131],v232 offset:4096
TR a[132:133],v232 offset:8192
MM v[64:67],a[200:203],v[192:195],v[64:67]
TR a[134:135],v232 offset:12288
TR a[136:137],v233 offset:0
MM v[64:67],a[204:207],v[208:211],v[64:67]
TR a[138:139],v233 offset:4096
v_add_f32 v246, v236, v237
MM v[68:71],a[220:223],v[208:211],v[128:131]
TR a[140:141],v233 offset:8192
v_cmp_nge_f32 vcc, s71, v246
MM v[68:71],a[216:219],v[192:195],v[68:71]
s_cbranch_vccnz LSITE16_%=
LBACK16_%=:
MM v[68:71],a[212:215],v[176:179],v[68:71]
s_add_u32 s67, s95, 13312
TR a[142:143],v233 offset:12288
TR a[144:145],v232 offset:512
MM v[68:71],a[208:211],v[160:163],v[68:71]
buffer_store_dwordx4 v[240:243], v248, s[20:23], s67 offen offset:0 sc1
MM v[76:79],a[208:211],v[164:167],v[132:135]
EX v64,v64
MM v[76:79],a[212:215],v[180:183],v[76:79]
EX v65,v65
MM v[76:79],a[216:219],v[196:199],v[76:79]
TR a[146:147],v232 offset:4608
CV v64,v64,v65
MM v[76:79],a[220:223],v[212:215],v[76:79]
EX v66,v66
MM v[72:75],a[204:207],v[212:215],v[132:135]
EX v67,v67
MM v[72:75],a[200:203],v[196:199],v[72:75]
TR a[148:149],v232 offset:8704
CV v65,v66,v67
MM v[72:75],a[196:199],v[180:183],v[72:75]
EX v68,v68
MM v[72:75],a[192:195],v[164:167],v[72:75]
TR a[150:151],v232 offset:12800
TR a[152:153],v233 offset:512
MM v[80:83],a[192:195],v[168:171],v[136:139]
EX v69,v69
MM v[80:83],a[196:199],v[184:187],v[80:83]
TR a[154:155],v233 offset:4608
CV v66,v68,v69
MM v[80:83],a[200:203],v[200:203],v[80:83]
EX v70,v70
MM v[80:83],a[204:207],v[216:219],v[80:83]
EX v71,v71
MM v[84:87],a[220:223],v[216:219],v[136:139]
TR a[156:157],v233 offset:8704
CV v67,v70,v71
MM v[84:87],a[216:219],v[200:203],v[84:87]
EX v72,v72
MM v[84:87],a[212:215],v[184:187],v[84:87]
TR a[158:159],v233 offset:12800
TR a[160:161],v232 offset:1024
MM v[84:87],a[208:211],v[168:171],v[84:87]
EX v73,v73
MM v[92:95],a[208:211],v[172:175],v[140:143]
s_and_b32 s44, s50, 7
TR a[162:163],v232 offset:5120
CV v72,v72,v73
MM v[92:95],a[212:215],v[188:191],v[92:95]
s_lshl_b32 s44, s44, 2
EX v74,v74
MM v[92:95],a[216:219],v[204:207],v[92:95]
s_add_u32 s44, s44, s84
EX v75,v75
MM v[92:95],a[220:223],v[220:223],v[92:95]
s_lshl_b32 s44, s44, 2
TR a[164:165],v232 offset:9216
CV v73,v74,v75
MM v[88:91],a[204:207],v[220:223],v[140:143]
s_add_u32 s44, s44, %[swave]
EX v76,v76
MM v[88:91],a[200:203],v[204:207],v[88:91]
s_mul_i32 s44, s44, 5120
TR a[166:167],v232 offset:13312
TR a[168:169],v233 offset:1024
MM v[88:91],a[196:199],v[188:191],v[88:91]
s_mul_i32 s45, s84, 320
EX v77,v77
MM v[88:91],a[192:195],v[172:175],v[88:91]
s_sub_u32 s44, s44, s45
TR a[170:171],v233 offset:5120
CV v74,v76,v77
MM v[96:99],a[224:227],v[160:163],v[128:131]
s_lshl_b32 s45, %[swave], 10
EX v78,v78
MM v[96:99],a[228:231],v[176:179],v[96:99]
s_add_u32 s45, s45, %[slds]
s_add_u32 m0, s45, 147456
s_add_u32 s44, s44, 0
buffer_load_dwordx4 v248, s[24:27], s44 offen offset:0 lds
MM v[96:99],a[232:235],v[192:195],v[96:99]
EX v79,v79
MM v[96:99],a[236:239],v[208:211],v[96:99]
TR a[172:173],v233 offset:9216
CV v75,v78,v79
MM v[100:103],a[252:255],v[208:211],v[128:131]
EX v80,v80
MM v[100:103],a[248:251],v[192:195],v[100:103]
TR a[174:175],v233 offset:13312
TR a[176:177],v232 offset:1536
MM v[100:103],a[244:247],v[176:179],v[100:103]
EX v81,v81
MM v[100:103],a[240:243],v[160:163],v[100:103]
TR a[178:179],v232 offset:5632
CV v80,v80,v81
MM v[108:111],a[240:243],v[164:167],v[132:135]
s_add_u32 s67, s95, 14336
EX v82,v82
MM v[108:111],a[244:247],v[180:183],v[108:111]
buffer_store_dwordx4 v[240:243], v248, s[20:23], s67 offen offset:0 sc1
MM v[108:111],a[248:251],v[196:199],v[108:111]
EX v83,v83
MM v[108:111],a[252:255],v[212:215],v[108:111]
TR a[180:181],v232 offset:9728
CV v81,v82,v83
MM v[104:107],a[236:239],v[212:215],v[132:135]
EX v84,v84
MM v[104:107],a[232:235],v[196:199],v[104:107]
TR a[182:183],v232 offset:13824
TR a[184:185],v233 offset:1536
MM v[104:107],a[228:231],v[180:183],v[104:107]
EX v85,v85
MM v[104:107],a[224:227],v[164:167],v[104:107]
TR a[186:187],v233 offset:5632
CV v82,v84,v85
MM v[112:115],a[224:227],v[168:171],v[136:139]
EX v86,v86
MM v[112:115],a[228:231],v[184:187],v[112:115]
EX v87,v87
MM v[112:115],a[232:235],v[200:203],v[112:115]
TR a[188:189],v233 offset:9728
CV v83,v86,v87
MM v[112:115],a[236:239],v[216:219],v[112:115]
EX v88,v88
MM v[116:119],a[252:255],v[216:219],v[136:139]
EX v89,v89
MM v[116:119],a[248:251],v[200:203],v[116:119]
TR a[190:191],v233 offset:13824
CV v88,v88,v89
MM v[116:119],a[244:247],v[184:187],v[116:119]
EX v90,v90
MM v[116:119],a[240:243],v[168:171],v[116:119]
EX v91,v91
MM v[124:127],a[240:243],v[172:175],v[140:143]
EX v92,v92
MM v[124:127],a[244:247],v[188:191],v[124:127]
EX v93,v93
MM v[124:127],a[248:251],v[204:207],v[124:127]
EX v94,v94
MM v[124:127],a[252:255],v[220:223],v[124:127]
EX v95,v95
MM v[120:123],a[236:239],v[220:223],v[140:143]
EX v96,v96
MM v[120:123],a[232:235],v[204:207],v[120:123]
LPG11_%=:
s_cbranch_execz LTR0_%=
EX v97,v97
MM v[120:123],a[228:231],v[188:191],v[120:123]
EX v98,v98
MM v[120:123],a[224:227],v[172:175],v[120:123]
s_waitcnt vmcnt(18) lgkmcnt(0)
s_barrier
MM a[0:3],a[128:131],v[0:3],a[0:3]
s_add_u32 m0, s65, 0
CV v89,v90,v91
CV v90,v92,v93
MM a[0:3],a[132:135],v[4:7],a[0:3]
RD a[192:195],v224 offset:32768
CV v91,v94,v95
MM a[8:11],a[132:135],v[12:15],a[8:11]
buffer_load_dwordx4 v228, s[8:11], s64 offen offset:0 lds
MM a[8:11],a[128:131],v[8:11],a[8:11]
RD a[196:199],v225 offset:32768
CV v68,v96,v97
MM a[16:19],a[128:131],v[16:19],a[16:19]
RD a[200:203],v226 offset:32768
RD a[204:207],v227 offset:32768
MM a[16:19],a[132:135],v[20:23],a[16:19]
buffer_load_dwordx4 v229, s[8:11], s64 offen offset:1024 lds
MM a[24:27],a[132:135],v[28:31],a[24:27]
RD a[208:211],v224 offset:36864
RD a[212:215],v225 offset:36864
MM a[24:27],a[128:131],v[24:27],a[24:27]
EX v99,v99
MM a[28:31],a[136:139],v[24:27],a[28:31]
RD a[216:219],v226 offset:36864
CV v69,v98,v99
MM a[28:31],a[140:143],v[28:31],a[28:31]
EX v100,v100
MM a[20:23],a[140:143],v[20:23],a[20:23]
buffer_load_dwordx4 v230, s[8:11], s64 offen offset:2048 lds
MM a[20:23],a[136:139],v[16:19],a[20:23]
EX v101,v101
MM a[12:15],a[136:139],v[8:11],a[12:15]
RD a[220:223],v227 offset:36864
CV v70,v100,v101
MM a[12:15],a[140:143],v[12:15],a[12:15]
EX v102,v102
MM a[4:7],a[140:143],v[4:7],a[4:7]
EX v103,v103
MM a[4:7],a[136:139],v[0:3],a[4:7]
RD a[224:227],v224 offset:40960
CV v71,v102,v103
MM a[32:35],a[144:147],v[0:3],a[32:35]
EX v104,v104
MM a[32:35],a[148:151],v[4:7],a[32:35]
buffer_load_dwordx4 v231, s[8:11], s64 offen offset:3072 lds
MM a[40:43],a[148:151],v[12:15],a[40:43]
s_add_u32 m0, s65, 65536
EX v105,v105
MM a[40:43],a[144:147],v[8:11],a[40:43]
RD a[228:231],v225 offset:40960
CV v76,v104,v105
MM a[48:51],a[144:147],v[16:19],a[48:51]
EX v106,v106
MM a[48:51],a[148:151],v[20:23],a[48:51]
EX v107,v107
MM a[56:59],a[148:151],v[28:31],a[56:59]
RD a[232:235],v226 offset:40960
CV v77,v106,v107
MM a[56:59],a[144:147],v[24:27],a[56:59]
EX v108,v108
MM a[60:63],a[152:155],v[24:27],a[60:63]
buffer_load_dwordx4 v235, s[12:15], s81 offen offset:0 lds
MM a[60:63],a[156:159],v[28:31],a[60:63]
EX v109,v109
MM a[52:55],a[156:159],v[20:23],a[52:55]
RD a[236:239],v227 offset:40960
CV v78,v108,v109
MM a[52:55],a[152:155],v[16:19],a[52:55]
EX v110,v110
MM a[44:47],a[152:155],v[8:11],a[44:47]
EX v111,v111
MM a[44:47],a[156:159],v[12:15],a[44:47]
RD a[240:243],v224 offset:45056
CV v79,v110,v111
MM a[36:39],a[156:159],v[4:7],a[36:39]
buffer_load_dwordx4 v235, s[12:15], s64 offen offset:1024 lds
MM a[36:39],a[152:155],v[0:3],a[36:39]
EX v112,v112
MM a[64:67],a[160:163],v[0:3],a[64:67]
EX v113,v113
MM a[64:67],a[164:167],v[4:7],a[64:67]
RD a[244:247],v225 offset:45056
CV v84,v112,v113
MM a[72:75],a[164:167],v[12:15],a[72:75]
EX v114,v114
MM a[72:75],a[160:163],v[8:11],a[72:75]
EX v115,v115
MM a[80:83],a[160:163],v[16:19],a[80:83]
RD a[248:251],v226 offset:45056
CV v85,v114,v115
MM a[80:83],a[164:167],v[20:23],a[80:83]
buffer_load_dwordx4 v235, s[12:15], s81 offen offset:2048 lds
MM a[88:91],a[164:167],v[28:31],a[88:91]
EX v116,v116
MM a[88:91],a[160:163],v[24:27],a[88:91]
EX v117,v117
MM a[92:95],a[168:171],v[24:27],a[92:95]
RD a[252:255],v227 offset:45056
CV v86,v116,v117
MM a[92:95],a[172:175],v[28:31],a[92:95]
EX v118,v118
MM a[84:87],a[172:175],v[20:23],a[84:87]
buffer_load_dwordx4 v235, s[12:15], s64 offen offset:3072 lds
MM a[84:87],a[168:171],v[16:19],a[84:87]
EX v119,v119
MM a[76:79],a[168:171],v[8:11],a[76:79]
CV v87,v118,v119
MM a[76:79],a[172:175],v[12:15],a[76:79]
EX v120,v120
MM a[68:71],a[172:175],v[4:7],a[68:71]
EX v121,v121
MM a[68:71],a[168:171],v[0:3],a[68:71]
EX v122,v122
MM a[96:99],a[176:179],v[0:3],a[96:99]
EX v123,v123
MM a[96:99],a[180:183],v[4:7],a[96:99]
EX v124,v124
MM a[104:107],a[180:183],v[12:15],a[104:107]
EX v125,v125
MM a[104:107],a[176:179],v[8:11],a[104:107]
EX v126,v126
MM a[112:115],a[176:179],v[16:19],a[112:115]
EX v127,v127
MM a[112:115],a[180:183],v[20:23],a[112:115]
CV v92,v120,v121
CV v93,v122,v123
MM a[120:123],a[180:183],v[28:31],a[120:123]
CV v94,v124,v125
CV v95,v126,v127
MM a[120:123],a[176:179],v[24:27],a[120:123]
MM a[124:127],a[184:187],v[24:27],a[124:127]
MM a[124:127],a[188:191],v[28:31],a[124:127]
MM a[116:119],a[188:191],v[20:23],a[116:119]
MM a[116:119],a[184:187],v[16:19],a[116:119]
MM a[108:111],a[184:187],v[8:11],a[108:111]
MM a[108:111],a[188:191],v[12:15],a[108:111]
MM a[100:103],a[188:191],v[4:7],a[100:103]
MM a[100:103],a[184:187],v[0:3],a[100:103]
v_smfmac_f32_16x16x64_bf16 v[236:239], v[144:147], v[64:71], v234
v_smfmac_f32_16x16x64_bf16 v[236:239], v[148:151], v[72:79], v234
v_smfmac_f32_16x16x64_bf16 v[236:239], v[152:155], v[80:87], v234
v_smfmac_f32_16x16x64_bf16 v[236:239], v[156:159], v[88:95], v234
s_waitcnt lgkmcnt(0)
MM v[0:3],a[192:195],v[160:163],v[128:131]
s_add_u32 s64, s64, 16384
TR a[128:129],v232 offset:16384
s_add_u32 s81, s64, 896
MM v[0:3],a[196:199],v[176:179],v[0:3]
TR a[130:131],v232 offset:20480
TR a[132:133],v232 offset:24576
)ASM"
R"ASM(
MM v[0:3],a[200:203],v[192:195],v[0:3]
TR a[134:135],v232 offset:28672
TR a[136:137],v233 offset:16384
MM v[0:3],a[204:207],v[208:211],v[0:3]
TR a[138:139],v233 offset:20480
v_add_f32 v247, v236, v237
MM v[4:7],a[220:223],v[208:211],v[128:131]
TR a[140:141],v233 offset:24576
v_cmp_nge_f32 vcc, s71, v247
MM v[4:7],a[216:219],v[192:195],v[4:7]
s_cbranch_vccnz LSITE17_%=
LBACK17_%=:
MM v[4:7],a[212:215],v[176:179],v[4:7]
TR a[142:143],v233 offset:28672
TR a[144:145],v232 offset:16896
MM v[4:7],a[208:211],v[160:163],v[4:7]
TR a[146:147],v232 offset:20992
TR a[148:149],v232 offset:25088
MM v[12:15],a[208:211],v[164:167],v[132:135]
EX v0,v0
MM v[12:15],a[212:215],v[180:183],v[12:15]
EX v1,v1
MM v[12:15],a[216:219],v[196:199],v[12:15]
TR a[150:151],v232 offset:29184
CV v0,v0,v1
MM v[12:15],a[220:223],v[212:215],v[12:15]
EX v2,v2
MM v[8:11],a[204:207],v[212:215],v[132:135]
EX v3,v3
MM v[8:11],a[200:203],v[196:199],v[8:11]
TR a[152:153],v233 offset:16896
CV v1,v2,v3
MM v[8:11],a[196:199],v[180:183],v[8:11]
EX v4,v4
MM v[8:11],a[192:195],v[164:167],v[8:11]
EX v5,v5
MM v[16:19],a[192:195],v[168:171],v[136:139]
TR a[154:155],v233 offset:20992
CV v2,v4,v5
MM v[16:19],a[196:199],v[184:187],v[16:19]
EX v6,v6
MM v[16:19],a[200:203],v[200:203],v[16:19]
EX v7,v7
MM v[16:19],a[204:207],v[216:219],v[16:19]
TR a[156:157],v233 offset:25088
CV v3,v6,v7
MM v[20:23],a[220:223],v[216:219],v[136:139]
EX v8,v8
MM v[20:23],a[216:219],v[200:203],v[20:23]
TR a[158:159],v233 offset:29184
TR a[160:161],v232 offset:17408
MM v[20:23],a[212:215],v[184:187],v[20:23]
EX v9,v9
MM v[20:23],a[208:211],v[168:171],v[20:23]
TR a[162:163],v232 offset:21504
CV v8,v8,v9
MM v[28:31],a[208:211],v[172:175],v[140:143]
s_add_u32 m0, s45, 147456
s_add_u32 s44, s44, 1024
buffer_load_dwordx4 v248, s[24:27], s44 offen offset:0 lds
MM v[28:31],a[212:215],v[188:191],v[28:31]
EX v10,v10
MM v[28:31],a[216:219],v[204:207],v[28:31]
EX v11,v11
MM v[28:31],a[220:223],v[220:223],v[28:31]
TR a[164:165],v232 offset:25600
CV v9,v10,v11
MM v[24:27],a[204:207],v[220:223],v[140:143]
EX v12,v12
MM v[24:27],a[200:203],v[204:207],v[24:27]
TR a[166:167],v232 offset:29696
TR a[168:169],v233 offset:17408
MM v[24:27],a[196:199],v[188:191],v[24:27]
EX v13,v13
MM v[24:27],a[192:195],v[172:175],v[24:27]
TR a[170:171],v233 offset:21504
CV v10,v12,v13
MM v[32:35],a[224:227],v[160:163],v[128:131]
EX v14,v14
MM v[32:35],a[228:231],v[176:179],v[32:35]
EX v15,v15
MM v[32:35],a[232:235],v[192:195],v[32:35]
TR a[172:173],v233 offset:25600
CV v11,v14,v15
MM v[32:35],a[236:239],v[208:211],v[32:35]
TR a[174:175],v233 offset:29696
TR a[176:177],v232 offset:17920
MM v[36:39],a[252:255],v[208:211],v[128:131]
EX v16,v16
MM v[36:39],a[248:251],v[192:195],v[36:39]
EX v17,v17
MM v[36:39],a[244:247],v[176:179],v[36:39]
TR a[178:179],v232 offset:22016
CV v16,v16,v17
MM v[36:39],a[240:243],v[160:163],v[36:39]
EX v18,v18
MM v[44:47],a[240:243],v[164:167],v[132:135]
TR a[180:181],v232 offset:26112
TR a[182:183],v232 offset:30208
MM v[44:47],a[244:247],v[180:183],v[44:47]
EX v19,v19
MM v[44:47],a[248:251],v[196:199],v[44:47]
TR a[184:185],v233 offset:17920
CV v17,v18,v19
MM v[44:47],a[252:255],v[212:215],v[44:47]
EX v20,v20
MM v[40:43],a[236:239],v[212:215],v[132:135]
EX v21,v21
MM v[40:43],a[232:235],v[196:199],v[40:43]
TR a[186:187],v233 offset:22016
CV v18,v20,v21
MM v[40:43],a[228:231],v[180:183],v[40:43]
EX v22,v22
MM v[40:43],a[224:227],v[164:167],v[40:43]
TR a[188:189],v233 offset:26112
TR a[190:191],v233 offset:30208
MM v[48:51],a[224:227],v[168:171],v[136:139]
EX v23,v23
MM v[48:51],a[228:231],v[184:187],v[48:51]
CV v19,v22,v23
MM v[48:51],a[232:235],v[200:203],v[48:51]
EX v24,v24
MM v[48:51],a[236:239],v[216:219],v[48:51]
EX v25,v25
MM v[52:55],a[252:255],v[216:219],v[136:139]
EX v26,v26
MM v[52:55],a[248:251],v[200:203],v[52:55]
EX v27,v27
MM v[52:55],a[244:247],v[184:187],v[52:55]
EX v28,v28
MM v[52:55],a[240:243],v[168:171],v[52:55]
EX v29,v29
MM v[60:63],a[240:243],v[172:175],v[140:143]
EX v30,v30
MM v[60:63],a[244:247],v[188:191],v[60:63]
EX v31,v31
MM v[60:63],a[248:251],v[204:207],v[60:63]
EX v32,v32
MM v[60:63],a[252:255],v[220:223],v[60:63]
EX v33,v33
MM v[56:59],a[236:239],v[220:223],v[140:143]
EX v34,v34
MM v[56:59],a[232:235],v[204:207],v[56:59]
EX v35,v35
MM v[56:59],a[228:231],v[188:191],v[56:59]
EX v36,v36
MM v[56:59],a[224:227],v[172:175],v[56:59]
s_waitcnt vmcnt(16) lgkmcnt(0)
s_barrier
MM a[0:3],a[128:131],v[64:67],a[0:3]
s_lshl_b32 s60, s50, 2
CV v24,v24,v25
CV v25,v26,v27
MM a[0:3],a[132:135],v[68:71],a[0:3]
s_add_u32 s60, s60, %[swave]
RD a[192:195],v224 offset:49152
CV v26,v28,v29
MM a[8:11],a[132:135],v[76:79],a[8:11]
s_lshl_b32 s60, s60, 2
s_add_u32 m0, s65, 16384
CV v27,v30,v31
MM a[8:11],a[128:131],v[72:75],a[8:11]
buffer_load_dwordx4 v228, s[8:11], s64 offen offset:0 lds
MM a[16:19],a[128:131],v[80:83],a[16:19]
s_bfe_u32 s62, s49, 65538
RD a[196:199],v225 offset:49152
CV v4,v32,v33
MM a[16:19],a[132:135],v[84:87],a[16:19]
s_add_u32 s93, s60, s62
RD a[200:203],v226 offset:49152
CV v5,v34,v35
MM a[24:27],a[132:135],v[92:95],a[24:27]
s_lshl_b32 s93, s93, 2
RD a[204:207],v227 offset:49152
MM a[24:27],a[128:131],v[88:91],a[24:27]
buffer_load_dwordx4 v229, s[8:11], s64 offen offset:1024 lds
MM a[28:31],a[136:139],v[88:91],a[28:31]
s_lshr_b32 s61, s49, 3
RD a[208:211],v224 offset:53248
MM a[28:31],a[140:143],v[92:95],a[28:31]
RD a[212:215],v225 offset:53248
RD a[216:219],v226 offset:53248
MM a[20:23],a[140:143],v[84:87],a[20:23]
buffer_load_dwordx4 v230, s[8:11], s64 offen offset:2048 lds
MM a[20:23],a[136:139],v[80:83],a[20:23]
s_add_u32 s93, s93, s61
EX v37,v37
MM a[12:15],a[136:139],v[72:75],a[12:15]
s_mul_i32 s18, s93, 5120
CV v6,v36,v37
MM a[12:15],a[140:143],v[76:79],a[12:15]
s_mul_i32 s60, s84, 320
EX v38,v38
MM a[4:7],a[140:143],v[68:71],a[4:7]
s_add_u32 s18, s18, s60
EX v39,v39
MM a[4:7],a[136:139],v[64:67],a[4:7]
s_and_b32 s60, s84, 1
RD a[220:223],v227 offset:53248
CV v7,v38,v39
MM a[32:35],a[144:147],v[64:67],a[32:35]
EX v40,v40
MM a[32:35],a[148:151],v[68:71],a[32:35]
buffer_load_dwordx4 v231, s[8:11], s64 offen offset:3072 lds
MM a[40:43],a[148:151],v[76:79],a[40:43]
s_xor_b32 s60, s60, 1
EX v41,v41
MM a[40:43],a[144:147],v[72:75],a[40:43]
s_lshl_b32 s60, s60, 8
RD a[224:227],v224 offset:57344
CV v12,v40,v41
MM a[48:51],a[144:147],v[80:83],a[48:51]
s_add_u32 s63, s18, s60
EX v42,v42
MM a[48:51],a[148:151],v[84:87],a[48:51]
s_and_b32 s60, s84, 1
EX v43,v43
MM a[56:59],a[148:151],v[92:95],a[56:59]
s_lshl_b32 s60, s60, 6
s_add_u32 m0, s65, 81920
CV v13,v42,v43
MM a[56:59],a[144:147],v[88:91],a[56:59]
s_add_u32 s18, s18, s60
RD a[228:231],v225 offset:57344
RD a[232:235],v226 offset:57344
MM a[60:63],a[152:155],v[88:91],a[60:63]
EX v44,v44
MM a[60:63],a[156:159],v[92:95],a[60:63]
buffer_load_dwordx4 v235, s[12:15], s81 offen offset:0 lds
MM a[52:55],a[156:159],v[84:87],a[52:55]
s_lshl_b32 s60, s50, 2
EX v45,v45
MM a[52:55],a[152:155],v[80:83],a[52:55]
s_add_u32 s60, s60, s84
RD a[236:239],v227 offset:57344
CV v14,v44,v45
MM a[44:47],a[152:155],v[72:75],a[44:47]
s_mul_i32 s16, s60, 663552
EX v46,v46
MM a[44:47],a[156:159],v[76:79],a[44:47]
s_lshl_b32 s62, s62, 2
EX v47,v47
MM a[36:39],a[156:159],v[68:71],a[36:39]
s_add_u32 s62, s62, s61
CV v15,v46,v47
MM a[36:39],a[152:155],v[64:67],a[36:39]
EX v48,v48
MM a[64:67],a[160:163],v[64:67],a[64:67]
buffer_load_dwordx4 v235, s[12:15], s64 offen offset:1024 lds
MM a[64:67],a[164:167],v[68:71],a[64:67]
s_lshl_b32 s62, s62, 8
EX v49,v49
MM a[72:75],a[164:167],v[76:79],a[72:75]
s_add_u32 s16, s16, s62
RD a[240:243],v224 offset:61440
CV v20,v48,v49
MM a[72:75],a[160:163],v[72:75],a[72:75]
s_mul_i32 s60, %[swave], 20480
EX v50,v50
MM a[80:83],a[160:163],v[80:83],a[80:83]
s_add_u32 s16, s16, s60
EX v51,v51
MM a[80:83],a[164:167],v[84:87],a[80:83]
s_lshr_b32 s61, s49, 4
RD a[244:247],v225 offset:61440
CV v21,v50,v51
MM a[88:91],a[164:167],v[92:95],a[88:91]
s_mul_i32 s61, s61, 331776
EX v52,v52
MM a[88:91],a[160:163],v[88:91],a[88:91]
s_add_u32 s16, s16, s61
RD a[248:251],v226 offset:61440
MM a[92:95],a[168:171],v[88:91],a[92:95]
EX v53,v53
MM a[92:95],a[172:175],v[92:95],a[92:95]
RD a[252:255],v227 offset:61440
CV v22,v52,v53
MM a[84:87],a[172:175],v[84:87],a[84:87]
buffer_load_dwordx4 v235, s[12:15], s81 offen offset:2048 lds
MM a[84:87],a[168:171],v[80:83],a[84:87]
s_add_u32 s16, s16, 22708224
EX v54,v54
MM a[76:79],a[168:171],v[72:75],a[76:79]
s_mov_b32 s17, 31
EX v55,v55
MM a[76:79],a[172:175],v[76:79],a[76:79]
s_mov_b32 s72, 0
CV v23,v54,v55
MM a[68:71],a[172:175],v[68:71],a[68:71]
EX v56,v56
MM a[68:71],a[168:171],v[64:67],a[68:71]
EX v57,v57
MM a[96:99],a[176:179],v[64:67],a[96:99]
buffer_load_dwordx4 v235, s[12:15], s64 offen offset:3072 lds
MM a[96:99],a[180:183],v[68:71],a[96:99]
s_lshl_b32 s60, s50, 2
EX v58,v58
MM a[104:107],a[180:183],v[76:79],a[104:107]
s_add_u32 s60, s60, s84
EX v59,v59
MM a[104:107],a[176:179],v[72:75],a[104:107]
s_lshl_b32 s60, s60, 2
EX v60,v60
MM a[112:115],a[176:179],v[80:83],a[112:115]
s_add_u32 s60, s60, %[swave]
EX v61,v61
MM a[112:115],a[180:183],v[84:87],a[112:115]
s_lshl_b32 s60, s60, 5
EX v62,v62
MM a[120:123],a[180:183],v[92:95],a[120:123]
s_and_b32 s61, s49, 28
EX v63,v63
MM a[120:123],a[176:179],v[88:91],a[120:123]
s_add_u32 s19, s60, s61
CV v28,v56,v57
CV v29,v58,v59
MM a[124:127],a[184:187],v[88:91],a[124:127]
s_lshl_b32 s19, s19, 2
CV v30,v60,v61
CV v31,v62,v63
MM a[124:127],a[188:191],v[92:95],a[124:127]
s_add_u32 s19, s19, 16384
s_and_b32 s68, s19, 0xffffffc0
s_mov_b32 s82, 0
MM a[116:119],a[188:191],v[84:87],a[116:119]
s_lshr_b32 s61, s52, 2
s_sub_u32 s61, s61, s93
MM a[116:119],a[184:187],v[80:83],a[116:119]
s_cmp_eq_u32 s61, 0
s_cselect_b32 s60, 1, 0
s_or_b32 s82, s82, s60
MM a[108:111],a[184:187],v[72:75],a[108:111]
s_cmp_eq_u32 s61, 8
s_cselect_b32 s60, 256, 0
s_or_b32 s82, s82, s60
MM a[108:111],a[188:191],v[76:79],a[108:111]
s_lshr_b32 s61, s53, 2
s_sub_u32 s61, s61, s93
MM a[100:103],a[188:191],v[68:71],a[100:103]
s_cmp_eq_u32 s61, 0
s_cselect_b32 s60, 2, 0
s_or_b32 s82, s82, s60
MM a[100:103],a[184:187],v[64:67],a[100:103]
s_cmp_eq_u32 s61, 8
s_cselect_b32 s60, 512, 0
s_or_b32 s82, s82, s60
v_smfmac_f32_16x16x64_bf16 v[236:239], v[144:147], v[0:7], v234
s_lshr_b32 s61, s54, 2
s_sub_u32 s61, s61, s93
v_smfmac_f32_16x16x64_bf16 v[236:239], v[148:151], v[8:15], v234
s_cmp_eq_u32 s61, 0
s_cselect_b32 s60, 4, 0
v_smfmac_f32_16x16x64_bf16 v[236:239], v[152:155], v[16:23], v234
s_or_b32 s82, s82, s60
v_smfmac_f32_16x16x64_bf16 v[236:239], v[156:159], v[24:31], v234
s_waitcnt lgkmcnt(0)
MM v[64:67],a[192:195],v[160:163],v[128:131]
s_add_u32 s64, s64, 16384
s_cmp_eq_u32 s61, 8
s_cselect_b32 s60, 1024, 0
MM v[64:67],a[196:199],v[176:179],v[64:67]
s_or_b32 s82, s82, s60
TR a[128:129],v232 offset:32768
TR a[130:131],v232 offset:36864
MM v[64:67],a[200:203],v[192:195],v[64:67]
s_lshr_b32 s61, s55, 2
s_add_u32 s81, s64, 896
TR a[132:133],v232 offset:40960
MM v[64:67],a[204:207],v[208:211],v[64:67]
s_sub_u32 s61, s61, s93
TR a[134:135],v232 offset:45056
v_add_f32 v246, v236, v237
MM v[68:71],a[220:223],v[208:211],v[128:131]
s_cmp_eq_u32 s61, 0
s_cselect_b32 s60, 8, 0
v_cmp_nge_f32 vcc, s71, v246
MM v[68:71],a[216:219],v[192:195],v[68:71]
s_cbranch_vccnz LSITE18_%=
LBACK18_%=:
MM v[68:71],a[212:215],v[176:179],v[68:71]
v_bfe_u32 v253, v248, 4, 4
v_lshlrev_b32 v253, 2, v253
MM v[68:71],a[208:211],v[160:163],v[68:71]
s_and_b32 s76, s48, 0xffffffc0
TR a[136:137],v233 offset:32768
TR a[138:139],v233 offset:36864
MM v[76:79],a[208:211],v[164:167],v[132:135]
s_add_u32 s76, s76, 77824
EX v64,v64
MM v[76:79],a[212:215],v[180:183],v[76:79]
buffer_load_dword v252, v253, s[20:23], s76 offen offset:0 sc1
MM v[76:79],a[216:219],v[196:199],v[76:79]
s_or_b32 s82, s82, s60
s_cmp_eq_u32 s61, 8
s_cselect_b32 s60, 2048, 0
MM v[76:79],a[220:223],v[212:215],v[76:79]
s_or_b32 s82, s82, s60
EX v65,v65
MM v[72:75],a[204:207],v[212:215],v[132:135]
s_lshr_b32 s61, s56, 2
TR a[140:141],v233 offset:40960
CV v64,v64,v65
MM v[72:75],a[200:203],v[196:199],v[72:75]
s_sub_u32 s61, s61, s93
)ASM"
R"ASM(
s_cmp_eq_u32 s61, 0
s_cselect_b32 s60, 16, 0
MM v[72:75],a[196:199],v[180:183],v[72:75]
s_or_b32 s82, s82, s60
s_cmp_eq_u32 s61, 8
s_cselect_b32 s60, 4096, 0
MM v[72:75],a[192:195],v[164:167],v[72:75]
s_or_b32 s82, s82, s60
EX v66,v66
MM v[80:83],a[192:195],v[168:171],v[136:139]
s_lshr_b32 s61, s57, 2
EX v67,v67
MM v[80:83],a[196:199],v[184:187],v[80:83]
s_sub_u32 s61, s61, s93
s_cmp_eq_u32 s61, 0
s_cselect_b32 s60, 32, 0
MM v[80:83],a[200:203],v[200:203],v[80:83]
s_or_b32 s82, s82, s60
s_cmp_eq_u32 s61, 8
s_cselect_b32 s60, 8192, 0
MM v[80:83],a[204:207],v[216:219],v[80:83]
s_or_b32 s82, s82, s60
EX v68,v68
MM v[84:87],a[220:223],v[216:219],v[136:139]
s_lshr_b32 s61, s58, 2
TR a[142:143],v233 offset:45056
TR a[144:145],v232 offset:33280
MM v[84:87],a[216:219],v[200:203],v[84:87]
s_sub_u32 s61, s61, s93
s_cmp_eq_u32 s61, 0
s_cselect_b32 s60, 64, 0
EX v69,v69
MM v[84:87],a[212:215],v[184:187],v[84:87]
s_or_b32 s82, s82, s60
s_cmp_eq_u32 s61, 8
s_cselect_b32 s60, 16384, 0
MM v[84:87],a[208:211],v[168:171],v[84:87]
s_or_b32 s82, s82, s60
TR a[146:147],v232 offset:37376
EX v70,v70
MM v[92:95],a[208:211],v[172:175],v[140:143]
s_lshr_b32 s61, s59, 2
TR a[148:149],v232 offset:41472
TR a[150:151],v232 offset:45568
MM v[92:95],a[212:215],v[188:191],v[92:95]
s_sub_u32 s61, s61, s93
s_cmp_eq_u32 s61, 0
s_cselect_b32 s60, 128, 0
EX v71,v71
MM v[92:95],a[216:219],v[204:207],v[92:95]
s_or_b32 s82, s82, s60
s_cmp_eq_u32 s61, 8
s_cselect_b32 s60, 32768, 0
MM v[92:95],a[220:223],v[220:223],v[92:95]
s_or_b32 s82, s82, s60
s_and_b32 s60, s82, 255
s_cselect_b32 s51, 0, 31
EX v72,v72
LPG12_%=:
s_cbranch_execz LTR0_%=
MM v[88:91],a[204:207],v[220:223],v[140:143]
TR a[152:153],v233 offset:33280
CV v65,v66,v67
MM v[88:91],a[200:203],v[204:207],v[88:91]
EX v73,v73
MM v[88:91],a[196:199],v[188:191],v[88:91]
TR a[154:155],v233 offset:37376
CV v66,v68,v69
MM v[88:91],a[192:195],v[172:175],v[88:91]
EX v74,v74
MM v[96:99],a[224:227],v[160:163],v[128:131]
TR a[156:157],v233 offset:41472
TR a[158:159],v233 offset:45568
EX v75,v75
MM v[96:99],a[228:231],v[176:179],v[96:99]
TR a[160:161],v232 offset:33792
CV v67,v70,v71
MM v[96:99],a[232:235],v[192:195],v[96:99]
EX v76,v76
MM v[96:99],a[236:239],v[208:211],v[96:99]
EX v77,v77
MM v[100:103],a[252:255],v[208:211],v[128:131]
TR a[162:163],v232 offset:37888
CV v72,v72,v73
MM v[100:103],a[248:251],v[192:195],v[100:103]
EX v78,v78
MM v[100:103],a[244:247],v[176:179],v[100:103]
TR a[164:165],v232 offset:41984
CV v73,v74,v75
MM v[100:103],a[240:243],v[160:163],v[100:103]
EX v79,v79
MM v[108:111],a[240:243],v[164:167],v[132:135]
TR a[166:167],v232 offset:46080
TR a[168:169],v233 offset:33792
MM v[108:111],a[244:247],v[180:183],v[108:111]
EX v80,v80
MM v[108:111],a[248:251],v[196:199],v[108:111]
EX v81,v81
MM v[108:111],a[252:255],v[212:215],v[108:111]
TR a[170:171],v233 offset:37888
CV v74,v76,v77
MM v[104:107],a[236:239],v[212:215],v[132:135]
EX v82,v82
MM v[104:107],a[232:235],v[196:199],v[104:107]
TR a[172:173],v233 offset:41984
CV v75,v78,v79
MM v[104:107],a[228:231],v[180:183],v[104:107]
EX v83,v83
MM v[104:107],a[224:227],v[164:167],v[104:107]
TR a[174:175],v233 offset:46080
TR a[176:177],v232 offset:34304
MM v[112:115],a[224:227],v[168:171],v[136:139]
EX v84,v84
MM v[112:115],a[228:231],v[184:187],v[112:115]
TR a[178:179],v232 offset:38400
CV v80,v80,v81
MM v[112:115],a[232:235],v[200:203],v[112:115]
EX v85,v85
MM v[112:115],a[236:239],v[216:219],v[112:115]
TR a[180:181],v232 offset:42496
CV v81,v82,v83
MM v[116:119],a[252:255],v[216:219],v[136:139]
EX v86,v86
MM v[116:119],a[248:251],v[200:203],v[116:119]
TR a[182:183],v232 offset:46592
TR a[184:185],v233 offset:34304
MM v[116:119],a[244:247],v[184:187],v[116:119]
EX v87,v87
MM v[116:119],a[240:243],v[168:171],v[116:119]
EX v88,v88
MM v[124:127],a[240:243],v[172:175],v[140:143]
TR a[186:187],v233 offset:38400
CV v82,v84,v85
MM v[124:127],a[244:247],v[188:191],v[124:127]
EX v89,v89
MM v[124:127],a[248:251],v[204:207],v[124:127]
TR a[188:189],v233 offset:42496
TR a[190:191],v233 offset:46592
MM v[124:127],a[252:255],v[220:223],v[124:127]
EX v90,v90
MM v[120:123],a[236:239],v[220:223],v[140:143]
CV v83,v86,v87
CV v88,v88,v89
MM v[120:123],a[232:235],v[204:207],v[120:123]
EX v91,v91
MM v[120:123],a[228:231],v[188:191],v[120:123]
s_getpc_b64 s[98:99]
LTA2_%=:
s_lshl_b32 s73, %[swave], 12
s_add_u32 s73, s73, LPG13_%=-LTA2_%=
s_add_u32 s98, s98, s73
s_addc_u32 s99, s99, 0
s_mov_b64 exec, 0
s_setpc_b64 s[98:99]
LTR2_%=:
s_mov_b64 exec, -1
s_nop 0
CV v89,v90,v91
MM v[120:123],a[224:227],v[172:175],v[120:123]
s_waitcnt vmcnt(14) lgkmcnt(0)
s_barrier
MM a[0:3],a[128:131],v[0:3],a[0:3]
s_waitcnt vmcnt(0)
RD a[192:195],v224 offset:0
v_cmp_ne_u32 s[90:91], s69, v252
MM a[0:3],a[132:135],v[4:7],a[0:3]
s_add_u32 m0, s65, 32768
EX v92,v92
MM a[8:11],a[132:135],v[12:15],a[8:11]
s_cmp_lg_u64 s[90:91], 0
s_cbranch_scc1 LRFG_%=
LRFGb_%=:
v_lshrrev_b32 v253, 8, v248
MM a[8:11],a[128:131],v[8:11],a[8:11]
v_lshlrev_b32 v253, 10, v253
v_and_b32 v252, 240, v248
EX v93,v93
MM a[16:19],a[128:131],v[16:19],a[16:19]
v_add_u32 v253, v253, v252
CV v90,v92,v93
EX v94,v94
MM a[16:19],a[132:135],v[20:23],a[16:19]
EX v95,v95
MM a[24:27],a[132:135],v[28:31],a[24:27]
s_lshr_b32 s76, s48, 6
EX v96,v96
MM a[24:27],a[128:131],v[24:27],a[24:27]
s_lshl_b32 s76, s76, 13
EX v97,v97
MM a[28:31],a[136:139],v[24:27],a[28:31]
RD a[196:199],v225 offset:0
CV v91,v94,v95
MM a[28:31],a[140:143],v[28:31],a[28:31]
EX v98,v98
MM a[20:23],a[140:143],v[20:23],a[20:23]
buffer_load_dwordx4 v228, s[8:11], s64 offen offset:0 lds
MM a[20:23],a[136:139],v[16:19],a[20:23]
s_bfe_u32 s90, s48, 131076
EX v99,v99
MM a[12:15],a[136:139],v[8:11],a[12:15]
s_lshl_b32 s90, s90, 8
RD a[200:203],v226 offset:0
CV v68,v96,v97
MM a[12:15],a[140:143],v[12:15],a[12:15]
s_add_u32 s76, s76, s90
EX v100,v100
MM a[4:7],a[140:143],v[4:7],a[4:7]
s_add_u32 s76, s76, 22183936
EX v101,v101
MM a[4:7],a[136:139],v[0:3],a[4:7]
s_lshl_b32 s90, %[swave], 11
CV v69,v98,v99
MM a[32:35],a[144:147],v[0:3],a[32:35]
EX v102,v102
MM a[32:35],a[148:151],v[4:7],a[32:35]
buffer_load_dwordx4 v229, s[8:11], s64 offen offset:1024 lds
MM a[40:43],a[148:151],v[12:15],a[40:43]
s_add_u32 s90, s90, %[slds]
s_add_u32 m0, s90, 131072
s_nop 0
buffer_load_dwordx4 v253, s[20:23], s76 offen offset:0 sc1 lds
MM a[40:43],a[144:147],v[8:11],a[40:43]
s_add_u32 m0, s65, 32768
EX v103,v103
MM a[48:51],a[144:147],v[16:19],a[48:51]
s_add_u32 m0, s90, 132096
s_add_u32 s76, s76, 4096
buffer_load_dwordx4 v253, s[20:23], s76 offen offset:0 sc1 lds
MM a[48:51],a[148:151],v[20:23],a[48:51]
EX v104,v104
MM a[56:59],a[148:151],v[28:31],a[56:59]
RD a[204:207],v227 offset:0
CV v70,v100,v101
MM a[56:59],a[144:147],v[24:27],a[56:59]
EX v105,v105
MM a[60:63],a[152:155],v[24:27],a[60:63]
RD a[208:211],v224 offset:4096
CV v71,v102,v103
MM a[60:63],a[156:159],v[28:31],a[60:63]
EX v106,v106
MM a[52:55],a[156:159],v[20:23],a[52:55]
EX v107,v107
MM a[52:55],a[152:155],v[16:19],a[52:55]
s_add_u32 m0, s65, 32768
s_nop 0
buffer_load_dwordx4 v230, s[8:11], s64 offen offset:2048 lds
MM a[44:47],a[152:155],v[8:11],a[44:47]
EX v108,v108
MM a[44:47],a[156:159],v[12:15],a[44:47]
RD a[212:215],v225 offset:4096
CV v76,v104,v105
MM a[36:39],a[156:159],v[4:7],a[36:39]
EX v109,v109
MM a[36:39],a[152:155],v[0:3],a[36:39]
RD a[216:219],v226 offset:4096
CV v77,v106,v107
MM a[64:67],a[160:163],v[0:3],a[64:67]
EX v110,v110
MM a[64:67],a[164:167],v[4:7],a[64:67]
EX v111,v111
MM a[72:75],a[164:167],v[12:15],a[72:75]
buffer_load_dwordx4 v231, s[8:11], s64 offen offset:3072 lds
MM a[72:75],a[160:163],v[8:11],a[72:75]
s_add_u32 m0, s65, 98304
RD a[220:223],v227 offset:4096
CV v78,v108,v109
MM a[80:83],a[160:163],v[16:19],a[80:83]
EX v112,v112
MM a[80:83],a[164:167],v[20:23],a[80:83]
EX v113,v113
MM a[88:91],a[164:167],v[28:31],a[88:91]
RD a[224:227],v224 offset:8192
CV v79,v110,v111
MM a[88:91],a[160:163],v[24:27],a[88:91]
EX v114,v114
MM a[92:95],a[168:171],v[24:27],a[92:95]
EX v115,v115
MM a[92:95],a[172:175],v[28:31],a[92:95]
buffer_load_dwordx4 v235, s[12:15], s81 offen offset:0 lds
MM a[84:87],a[172:175],v[20:23],a[84:87]
RD a[228:231],v225 offset:8192
CV v84,v112,v113
MM a[84:87],a[168:171],v[16:19],a[84:87]
EX v116,v116
MM a[76:79],a[168:171],v[8:11],a[76:79]
EX v117,v117
MM a[76:79],a[172:175],v[12:15],a[76:79]
RD a[232:235],v226 offset:8192
CV v85,v114,v115
MM a[68:71],a[172:175],v[4:7],a[68:71]
EX v118,v118
MM a[68:71],a[168:171],v[0:3],a[68:71]
RD a[236:239],v227 offset:8192
CV v86,v116,v117
MM a[96:99],a[176:179],v[0:3],a[96:99]
EX v119,v119
MM a[96:99],a[180:183],v[4:7],a[96:99]
buffer_load_dwordx4 v235, s[12:15], s64 offen offset:1024 lds
MM a[104:107],a[180:183],v[12:15],a[104:107]
EX v120,v120
MM a[104:107],a[176:179],v[8:11],a[104:107]
RD a[240:243],v224 offset:12288
CV v87,v118,v119
MM a[112:115],a[176:179],v[16:19],a[112:115]
EX v121,v121
MM a[112:115],a[180:183],v[20:23],a[112:115]
EX v122,v122
MM a[120:123],a[180:183],v[28:31],a[120:123]
RD a[244:247],v225 offset:12288
CV v92,v120,v121
MM a[120:123],a[176:179],v[24:27],a[120:123]
EX v123,v123
MM a[124:127],a[184:187],v[24:27],a[124:127]
buffer_load_dwordx4 v235, s[12:15], s81 offen offset:2048 lds
MM a[124:127],a[188:191],v[28:31],a[124:127]
EX v124,v124
MM a[116:119],a[188:191],v[20:23],a[116:119]
RD a[248:251],v226 offset:12288
CV v93,v122,v123
MM a[116:119],a[184:187],v[16:19],a[116:119]
EX v125,v125
MM a[108:111],a[184:187],v[8:11],a[108:111]
EX v126,v126
MM a[108:111],a[188:191],v[12:15],a[108:111]
RD a[252:255],v227 offset:12288
CV v94,v124,v125
MM a[100:103],a[188:191],v[4:7],a[100:103]
EX v127,v127
MM a[100:103],a[184:187],v[0:3],a[100:103]
buffer_load_dwordx4 v235, s[12:15], s64 offen offset:3072 lds
v_smfmac_f32_16x16x64_bf16 v[236:239], v[144:147], v[64:71], v234
CV v95,v126,v127
v_smfmac_f32_16x16x64_bf16 v[236:239], v[148:151], v[72:79], v234
v_smfmac_f32_16x16x64_bf16 v[236:239], v[152:155], v[80:87], v234
v_smfmac_f32_16x16x64_bf16 v[236:239], v[156:159], v[88:95], v234
s_waitcnt lgkmcnt(0)
MM v[0:3],a[192:195],v[160:163],v[128:131]
s_add_u32 s64, s64, 16384
TR a[128:129],v232 offset:49152
s_add_u32 s81, s64, 896
MM v[0:3],a[196:199],v[176:179],v[0:3]
TR a[130:131],v232 offset:53248
TR a[132:133],v232 offset:57344
MM v[0:3],a[200:203],v[192:195],v[0:3]
TR a[134:135],v232 offset:61440
TR a[136:137],v233 offset:49152
MM v[0:3],a[204:207],v[208:211],v[0:3]
TR a[138:139],v233 offset:53248
v_add_f32 v247, v236, v237
MM v[4:7],a[220:223],v[208:211],v[128:131]
TR a[140:141],v233 offset:57344
v_cmp_nge_f32 vcc, s71, v247
MM v[4:7],a[216:219],v[192:195],v[4:7]
s_cbranch_vccnz LSITE19_%=
LBACK19_%=:
MM v[4:7],a[212:215],v[176:179],v[4:7]
v_lshrrev_b32 v253, 7, v248
v_mul_u32_u24 v253, 1280, v253
MM v[4:7],a[208:211],v[160:163],v[4:7]
v_and_b32 v252, 112, v248
v_add_u32 v253, v253, v252
MM v[12:15],a[208:211],v[164:167],v[132:135]
s_mul_i32 s90, %[swave], 30720
EX v0,v0
MM v[12:15],a[212:215],v[180:183],v[12:15]
s_mul_i32 s91, %[swave], 3072
EX v1,v1
MM v[12:15],a[216:219],v[196:199],v[12:15]
s_add_u32 s91, s91, %[slds]
s_add_u32 m0, s91, 151552
s_add_u32 s77, s90, 0
buffer_load_dwordx4 v253, s[28:31], s77 offen offset:0 lds
MM v[12:15],a[220:223],v[212:215],v[12:15]
TR a[142:143],v233 offset:61440
CV v0,v0,v1
MM v[8:11],a[204:207],v[212:215],v[132:135]
EX v2,v2
MM v[8:11],a[200:203],v[196:199],v[8:11]
s_add_u32 m0, s91, 152576
s_add_u32 s77, s90, 10240
buffer_load_dwordx4 v253, s[28:31], s77 offen offset:0 lds
MM v[8:11],a[196:199],v[180:183],v[8:11]
EX v3,v3
MM v[8:11],a[192:195],v[164:167],v[8:11]
TR a[144:145],v232 offset:49664
CV v1,v2,v3
MM v[16:19],a[192:195],v[168:171],v[136:139]
s_add_u32 m0, s91, 153600
s_add_u32 s77, s90, 20480
buffer_load_dwordx4 v253, s[28:31], s77 offen offset:0 lds
MM v[16:19],a[196:199],v[184:187],v[16:19]
EX v4,v4
MM v[16:19],a[200:203],v[200:203],v[16:19]
TR a[146:147],v232 offset:53760
TR a[148:149],v232 offset:57856
MM v[16:19],a[204:207],v[216:219],v[16:19]
EX v5,v5
MM v[20:23],a[220:223],v[216:219],v[136:139]
EX v6,v6
MM v[20:23],a[216:219],v[200:203],v[20:23]
)ASM"
R"ASM(
TR a[150:151],v232 offset:61952
CV v2,v4,v5
MM v[20:23],a[212:215],v[184:187],v[20:23]
EX v7,v7
MM v[20:23],a[208:211],v[168:171],v[20:23]
TR a[152:153],v233 offset:49664
CV v3,v6,v7
MM v[28:31],a[208:211],v[172:175],v[140:143]
EX v8,v8
MM v[28:31],a[212:215],v[188:191],v[28:31]
TR a[154:155],v233 offset:53760
TR a[156:157],v233 offset:57856
MM v[28:31],a[216:219],v[204:207],v[28:31]
EX v9,v9
MM v[28:31],a[220:223],v[220:223],v[28:31]
EX v10,v10
MM v[24:27],a[204:207],v[220:223],v[140:143]
TR a[158:159],v233 offset:61952
CV v8,v8,v9
MM v[24:27],a[200:203],v[204:207],v[24:27]
EX v11,v11
MM v[24:27],a[196:199],v[188:191],v[24:27]
TR a[160:161],v232 offset:50176
CV v9,v10,v11
MM v[24:27],a[192:195],v[172:175],v[24:27]
EX v12,v12
MM v[32:35],a[224:227],v[160:163],v[128:131]
TR a[162:163],v232 offset:54272
TR a[164:165],v232 offset:58368
MM v[32:35],a[228:231],v[176:179],v[32:35]
EX v13,v13
MM v[32:35],a[232:235],v[192:195],v[32:35]
TR a[166:167],v232 offset:62464
CV v10,v12,v13
MM v[32:35],a[236:239],v[208:211],v[32:35]
EX v14,v14
MM v[36:39],a[252:255],v[208:211],v[128:131]
TR a[168:169],v233 offset:50176
TR a[170:171],v233 offset:54272
MM v[36:39],a[248:251],v[192:195],v[36:39]
EX v15,v15
MM v[36:39],a[244:247],v[176:179],v[36:39]
TR a[172:173],v233 offset:58368
CV v11,v14,v15
MM v[36:39],a[240:243],v[160:163],v[36:39]
EX v16,v16
MM v[44:47],a[240:243],v[164:167],v[132:135]
EX v17,v17
MM v[44:47],a[244:247],v[180:183],v[44:47]
TR a[174:175],v233 offset:62464
CV v16,v16,v17
MM v[44:47],a[248:251],v[196:199],v[44:47]
EX v18,v18
MM v[44:47],a[252:255],v[212:215],v[44:47]
TR a[176:177],v232 offset:50688
TR a[178:179],v232 offset:54784
MM v[40:43],a[236:239],v[212:215],v[132:135]
EX v19,v19
MM v[40:43],a[232:235],v[196:199],v[40:43]
TR a[180:181],v232 offset:58880
CV v17,v18,v19
MM v[40:43],a[228:231],v[180:183],v[40:43]
EX v20,v20
MM v[40:43],a[224:227],v[164:167],v[40:43]
EX v21,v21
MM v[48:51],a[224:227],v[168:171],v[136:139]
TR a[182:183],v232 offset:62976
CV v18,v20,v21
MM v[48:51],a[228:231],v[184:187],v[48:51]
EX v22,v22
MM v[48:51],a[232:235],v[200:203],v[48:51]
TR a[184:185],v233 offset:50688
TR a[186:187],v233 offset:54784
MM v[48:51],a[236:239],v[216:219],v[48:51]
EX v23,v23
MM v[52:55],a[252:255],v[216:219],v[136:139]
TR a[188:189],v233 offset:58880
CV v19,v22,v23
MM v[52:55],a[248:251],v[200:203],v[52:55]
EX v24,v24
MM v[52:55],a[244:247],v[184:187],v[52:55]
EX v25,v25
MM v[52:55],a[240:243],v[168:171],v[52:55]
TR a[190:191],v233 offset:62976
CV v24,v24,v25
MM v[60:63],a[240:243],v[172:175],v[140:143]
EX v26,v26
MM v[60:63],a[244:247],v[188:191],v[60:63]
EX v27,v27
MM v[60:63],a[248:251],v[204:207],v[60:63]
EX v28,v28
MM v[60:63],a[252:255],v[220:223],v[60:63]
EX v29,v29
MM v[56:59],a[236:239],v[220:223],v[140:143]
EX v30,v30
MM v[56:59],a[232:235],v[204:207],v[56:59]
EX v31,v31
MM v[56:59],a[228:231],v[188:191],v[56:59]
EX v32,v32
MM v[56:59],a[224:227],v[172:175],v[56:59]
s_waitcnt lgkmcnt(0)
s_barrier
MM a[0:3],a[128:131],v[64:67],a[0:3]
s_add_u32 m0, s65, 49152
CV v25,v26,v27
CV v26,v28,v29
MM a[0:3],a[132:135],v[68:71],a[0:3]
RD a[192:195],v224 offset:16384
CV v27,v30,v31
MM a[8:11],a[132:135],v[76:79],a[8:11]
buffer_load_dwordx4 v228, s[8:11], s64 offen offset:0 lds
MM a[8:11],a[128:131],v[72:75],a[8:11]
RD a[196:199],v225 offset:16384
RD a[200:203],v226 offset:16384
MM a[16:19],a[128:131],v[80:83],a[16:19]
EX v33,v33
MM a[16:19],a[132:135],v[84:87],a[16:19]
RD a[204:207],v227 offset:16384
CV v4,v32,v33
MM a[24:27],a[132:135],v[92:95],a[24:27]
buffer_load_dwordx4 v229, s[8:11], s64 offen offset:1024 lds
MM a[24:27],a[128:131],v[88:91],a[24:27]
EX v34,v34
MM a[28:31],a[136:139],v[88:91],a[28:31]
EX v35,v35
MM a[28:31],a[140:143],v[92:95],a[28:31]
RD a[208:211],v224 offset:20480
CV v5,v34,v35
MM a[20:23],a[140:143],v[84:87],a[20:23]
EX v36,v36
MM a[20:23],a[136:139],v[80:83],a[20:23]
EX v37,v37
MM a[12:15],a[136:139],v[72:75],a[12:15]
RD a[212:215],v225 offset:20480
CV v6,v36,v37
MM a[12:15],a[140:143],v[76:79],a[12:15]
buffer_load_dwordx4 v230, s[8:11], s64 offen offset:2048 lds
MM a[4:7],a[140:143],v[68:71],a[4:7]
EX v38,v38
MM a[4:7],a[136:139],v[64:67],a[4:7]
EX v39,v39
MM a[32:35],a[144:147],v[64:67],a[32:35]
RD a[216:219],v226 offset:20480
CV v7,v38,v39
MM a[32:35],a[148:151],v[68:71],a[32:35]
EX v40,v40
MM a[40:43],a[148:151],v[76:79],a[40:43]
RD a[220:223],v227 offset:20480
RD a[224:227],v224 offset:24576
MM a[40:43],a[144:147],v[72:75],a[40:43]
EX v41,v41
MM a[48:51],a[144:147],v[80:83],a[48:51]
RD a[228:231],v225 offset:24576
CV v12,v40,v41
MM a[48:51],a[148:151],v[84:87],a[48:51]
EX v42,v42
MM a[56:59],a[148:151],v[92:95],a[56:59]
buffer_load_dwordx4 v231, s[8:11], s64 offen offset:3072 lds
MM a[56:59],a[144:147],v[88:91],a[56:59]
s_add_u32 m0, s65, 114688
EX v43,v43
MM a[60:63],a[152:155],v[88:91],a[60:63]
RD a[232:235],v226 offset:24576
CV v13,v42,v43
MM a[60:63],a[156:159],v[92:95],a[60:63]
EX v44,v44
MM a[52:55],a[156:159],v[84:87],a[52:55]
EX v45,v45
MM a[52:55],a[152:155],v[80:83],a[52:55]
RD a[236:239],v227 offset:24576
CV v14,v44,v45
MM a[44:47],a[152:155],v[72:75],a[44:47]
EX v46,v46
MM a[44:47],a[156:159],v[76:79],a[44:47]
buffer_load_dwordx4 v235, s[12:15], s81 offen offset:0 lds
MM a[36:39],a[156:159],v[68:71],a[36:39]
EX v47,v47
MM a[36:39],a[152:155],v[64:67],a[36:39]
RD a[240:243],v224 offset:28672
CV v15,v46,v47
MM a[64:67],a[160:163],v[64:67],a[64:67]
EX v48,v48
MM a[64:67],a[164:167],v[68:71],a[64:67]
EX v49,v49
MM a[72:75],a[164:167],v[76:79],a[72:75]
RD a[244:247],v225 offset:28672
CV v20,v48,v49
MM a[72:75],a[160:163],v[72:75],a[72:75]
buffer_load_dwordx4 v235, s[12:15], s64 offen offset:1024 lds
MM a[80:83],a[160:163],v[80:83],a[80:83]
EX v50,v50
MM a[80:83],a[164:167],v[84:87],a[80:83]
EX v51,v51
MM a[88:91],a[164:167],v[92:95],a[88:91]
RD a[248:251],v226 offset:28672
CV v21,v50,v51
MM a[88:91],a[160:163],v[88:91],a[88:91]
EX v52,v52
MM a[92:95],a[168:171],v[88:91],a[92:95]
EX v53,v53
MM a[92:95],a[172:175],v[92:95],a[92:95]
s_nop 0
RD a[252:255],v227 offset:28672
CV v22,v52,v53
MM a[84:87],a[172:175],v[84:87],a[84:87]
buffer_load_dwordx4 v235, s[12:15], s81 offen offset:2048 lds
MM a[84:87],a[168:171],v[80:83],a[84:87]
s_nop 0
EX v54,v54
MM a[76:79],a[168:171],v[72:75],a[76:79]
s_nop 0
EX v55,v55
MM a[76:79],a[172:175],v[76:79],a[76:79]
s_nop 0
CV v23,v54,v55
MM a[68:71],a[172:175],v[68:71],a[68:71]
s_nop 0
EX v56,v56
MM a[68:71],a[168:171],v[64:67],a[68:71]
s_nop 0
EX v57,v57
MM a[96:99],a[176:179],v[64:67],a[96:99]
buffer_load_dwordx4 v235, s[12:15], s64 offen offset:3072 lds
MM a[96:99],a[180:183],v[68:71],a[96:99]
s_nop 0
EX v58,v58
MM a[104:107],a[180:183],v[76:79],a[104:107]
s_nop 0
EX v59,v59
MM a[104:107],a[176:179],v[72:75],a[104:107]
s_nop 0
EX v60,v60
MM a[112:115],a[176:179],v[80:83],a[112:115]
s_nop 0
EX v61,v61
MM a[112:115],a[180:183],v[84:87],a[112:115]
s_nop 0
EX v62,v62
MM a[120:123],a[180:183],v[92:95],a[120:123]
s_nop 0
EX v63,v63
MM a[120:123],a[176:179],v[88:91],a[120:123]
s_nop 0
CV v28,v56,v57
CV v29,v58,v59
MM a[124:127],a[184:187],v[88:91],a[124:127]
s_nop 0
CV v30,v60,v61
CV v31,v62,v63
MM a[124:127],a[188:191],v[92:95],a[124:127]
s_nop 0
MM a[116:119],a[188:191],v[84:87],a[116:119]
s_nop 0
MM a[116:119],a[184:187],v[80:83],a[116:119]
s_nop 0
MM a[108:111],a[184:187],v[72:75],a[108:111]
s_nop 0
MM a[108:111],a[188:191],v[76:79],a[108:111]
s_nop 0
MM a[100:103],a[188:191],v[68:71],a[100:103]
s_nop 0
MM a[100:103],a[184:187],v[64:67],a[100:103]
LPG13_%=:
s_cbranch_execz LTR2_%=
v_smfmac_f32_16x16x64_bf16 v[236:239], v[144:147], v[0:7], v234
v_smfmac_f32_16x16x64_bf16 v[236:239], v[148:151], v[8:15], v234
v_smfmac_f32_16x16x64_bf16 v[236:239], v[152:155], v[16:23], v234
v_smfmac_f32_16x16x64_bf16 v[236:239], v[156:159], v[24:31], v234
s_waitcnt lgkmcnt(0)
MM v[64:67],a[192:195],v[160:163],v[128:131]
s_add_u32 s64, s64, 16384
TR a[128:129],v232 offset:0
s_add_u32 s81, s64, 896
MM v[64:67],a[196:199],v[176:179],v[64:67]
TR a[130:131],v232 offset:4096
TR a[132:133],v232 offset:8192
MM v[64:67],a[200:203],v[192:195],v[64:67]
TR a[134:135],v232 offset:12288
TR a[136:137],v233 offset:0
MM v[64:67],a[204:207],v[208:211],v[64:67]
TR a[138:139],v233 offset:4096
v_add_f32 v246, v236, v237
MM v[68:71],a[220:223],v[208:211],v[128:131]
TR a[140:141],v233 offset:8192
v_cmp_nge_f32 vcc, s71, v246
MM v[68:71],a[216:219],v[192:195],v[68:71]
s_cbranch_vccnz LSITE20_%=
LBACK20_%=:
MM v[68:71],a[212:215],v[176:179],v[68:71]
v_lshrrev_b32 v43, 7, v248
v_bfe_u32 v42, v248, 6, 1
MM v[68:71],a[208:211],v[160:163],v[68:71]
v_lshl_add_u32 v40, v42, 3, v43
v_lshlrev_b32 v40, 4, v40
MM v[76:79],a[208:211],v[164:167],v[132:135]
v_bfe_u32 v42, v248, 4, 2
v_lshl_add_u32 v40, v42, 2, v40
MM v[76:79],a[212:215],v[180:183],v[76:79]
s_lshl_b32 s76, %[swave], 11
EX v64,v64
MM v[76:79],a[216:219],v[196:199],v[76:79]
s_add_u32 s76, s76, %[slds]
EX v65,v65
MM v[76:79],a[220:223],v[212:215],v[76:79]
s_add_u32 s76, s76, 131072
v_add_u32 v40, s76, v40
CV v64,v64,v65
MM v[72:75],a[204:207],v[212:215],v[132:135]
s_waitcnt vmcnt(17)
ds_read_b32 v32, v40 offset:0
ds_read_b32 v36, v40 offset:256
MM v[72:75],a[200:203],v[196:199],v[72:75]
ds_read_b32 v33, v40 offset:512
ds_read_b32 v37, v40 offset:768
MM v[72:75],a[196:199],v[180:183],v[72:75]
ds_read_b32 v34, v40 offset:1024
ds_read_b32 v38, v40 offset:1280
MM v[72:75],a[192:195],v[164:167],v[72:75]
ds_read_b32 v35, v40 offset:1536
ds_read_b32 v39, v40 offset:1792
MM v[80:83],a[192:195],v[168:171],v[136:139]
v_mul_u32_u24 v41, 3456, v43
v_bfe_u32 v42, v248, 4, 3
MM v[80:83],a[196:199],v[184:187],v[80:83]
TR a[142:143],v233 offset:12288
v_lshl_add_u32 v41, v42, 1, v41
MM v[80:83],a[200:203],v[200:203],v[80:83]
s_add_u32 s76, s48, 21626880
EX v66,v66
MM v[80:83],a[204:207],v[216:219],v[80:83]
s_lshl_b32 s77, %[swave], 4
EX v67,v67
MM v[84:87],a[220:223],v[216:219],v[136:139]
EX v68,v68
MM v[84:87],a[216:219],v[200:203],v[84:87]
EX v69,v69
MM v[84:87],a[212:215],v[184:187],v[84:87]
TR a[144:145],v232 offset:512
TR a[146:147],v232 offset:4608
MM v[84:87],a[208:211],v[168:171],v[84:87]
EX v70,v70
MM v[92:95],a[208:211],v[172:175],v[140:143]
TR a[148:149],v232 offset:8704
TR a[150:151],v232 offset:12800
MM v[92:95],a[212:215],v[188:191],v[92:95]
EX v71,v71
MM v[92:95],a[216:219],v[204:207],v[92:95]
EX v72,v72
MM v[92:95],a[220:223],v[220:223],v[92:95]
s_waitcnt lgkmcnt(5)
v_add_f32 v32, v32, v33
v_add_f32 v36, v36, v37
MM v[88:91],a[204:207],v[220:223],v[140:143]
v_add_f32 v34, v34, v35
v_add_f32 v38, v38, v39
MM v[88:91],a[200:203],v[204:207],v[88:91]
TR a[152:153],v233 offset:512
v_add_f32 v32, v32, v34
v_add_f32 v36, v36, v38
EX v73,v73
CV v65,v66,v67
MM v[88:91],a[196:199],v[188:191],v[88:91]
TR a[154:155],v233 offset:4608
CV v42,v32,v36
v_lshlrev_b32 v32, 16, v42
MM v[88:91],a[192:195],v[172:175],v[88:91]
v_and_b32 v36, 0xffff0000, v42
v_mul_f32 v42, 0xbfb8aa3b, v32
EX v74,v74
MM v[96:99],a[224:227],v[160:163],v[128:131]
TR a[156:157],v233 offset:8704
EX v42,v42
EX v75,v75
CV v66,v68,v69
MM v[96:99],a[228:231],v[176:179],v[96:99]
TR a[158:159],v233 offset:12800
v_add_f32 v42, 1.0, v42
MM v[96:99],a[232:235],v[192:195],v[96:99]
v_rcp_f32 v42, v42
EX v76,v76
MM v[96:99],a[236:239],v[208:211],v[96:99]
v_mul_f32 v42, v32, v42
CV v42,v42,v42
MM v[100:103],a[252:255],v[208:211],v[128:131]
TR a[160:161],v232 offset:1024
v_lshlrev_b32 v42, 16, v42
v_mul_f32 v32, v42, v36
EX v77,v77
CV v67,v70,v71
MM v[100:103],a[248:251],v[192:195],v[100:103]
TR a[162:163],v232 offset:5120
CV v43,v32,v32
MM v[100:103],a[244:247],v[176:179],v[100:103]
s_lshl_b64 exec, 0xffff, s77
buffer_store_short v43, v41, s[20:23], s76 offen offset:0 sc1
s_mov_b64 exec, -1
s_nop 0
EX v78,v78
MM v[100:103],a[240:243],v[160:163],v[100:103]
TR a[164:165],v232 offset:9216
CV v72,v72,v73
MM v[108:111],a[240:243],v[164:167],v[132:135]
EX v79,v79
MM v[108:111],a[244:247],v[180:183],v[108:111]
TR a[166:167],v232 offset:13312
CV v73,v74,v75
MM v[108:111],a[248:251],v[196:199],v[108:111]
EX v80,v80
MM v[108:111],a[252:255],v[212:215],v[108:111]
TR a[168:169],v233 offset:1024
)ASM"
R"ASM(
TR a[170:171],v233 offset:5120
EX v81,v81
MM v[104:107],a[236:239],v[212:215],v[132:135]
TR a[172:173],v233 offset:9216
CV v74,v76,v77
MM v[104:107],a[232:235],v[196:199],v[104:107]
EX v82,v82
MM v[104:107],a[228:231],v[180:183],v[104:107]
EX v83,v83
MM v[104:107],a[224:227],v[164:167],v[104:107]
TR a[174:175],v233 offset:13312
CV v75,v78,v79
MM v[112:115],a[224:227],v[168:171],v[136:139]
EX v84,v84
MM v[112:115],a[228:231],v[184:187],v[112:115]
TR a[176:177],v232 offset:1536
TR a[178:179],v232 offset:5632
MM v[112:115],a[232:235],v[200:203],v[112:115]
EX v85,v85
MM v[112:115],a[236:239],v[216:219],v[112:115]
TR a[180:181],v232 offset:9728
CV v80,v80,v81
MM v[116:119],a[252:255],v[216:219],v[136:139]
EX v86,v86
MM v[116:119],a[248:251],v[200:203],v[116:119]
TR a[182:183],v232 offset:13824
CV v81,v82,v83
MM v[116:119],a[244:247],v[184:187],v[116:119]
EX v87,v87
MM v[116:119],a[240:243],v[168:171],v[116:119]
TR a[184:185],v233 offset:1536
CV v82,v84,v85
MM v[124:127],a[240:243],v[172:175],v[140:143]
EX v88,v88
MM v[124:127],a[244:247],v[188:191],v[124:127]
TR a[186:187],v233 offset:5632
TR a[188:189],v233 offset:9728
MM v[124:127],a[248:251],v[204:207],v[124:127]
TR a[190:191],v233 offset:13824
CV v83,v86,v87
MM v[124:127],a[252:255],v[220:223],v[124:127]
EX v89,v89
MM v[120:123],a[236:239],v[220:223],v[140:143]
EX v90,v90
MM v[120:123],a[232:235],v[204:207],v[120:123]
EX v91,v91
MM v[120:123],a[228:231],v[188:191],v[120:123]
EX v92,v92
MM v[120:123],a[224:227],v[172:175],v[120:123]
s_waitcnt vmcnt(16) lgkmcnt(0)
s_barrier
MM a[0:3],a[128:131],v[0:3],a[0:3]
s_add_u32 m0, s65, 0
CV v88,v88,v89
CV v89,v90,v91
MM a[0:3],a[132:135],v[4:7],a[0:3]
EX v93,v93
MM a[8:11],a[132:135],v[12:15],a[8:11]
EX v94,v94
MM a[8:11],a[128:131],v[8:11],a[8:11]
EX v95,v95
MM a[16:19],a[128:131],v[16:19],a[16:19]
RD a[192:195],v224 offset:32768
CV v90,v92,v93
MM a[16:19],a[132:135],v[20:23],a[16:19]
EX v96,v96
MM a[24:27],a[132:135],v[28:31],a[24:27]
RD a[196:199],v225 offset:32768
CV v91,v94,v95
MM a[24:27],a[128:131],v[24:27],a[24:27]
EX v97,v97
MM a[28:31],a[136:139],v[24:27],a[28:31]
buffer_load_dwordx4 v228, s[8:11], s64 offen offset:0 lds
MM a[28:31],a[140:143],v[28:31],a[28:31]
EX v98,v98
MM a[20:23],a[140:143],v[20:23],a[20:23]
EX v99,v99
MM a[20:23],a[136:139],v[16:19],a[20:23]
RD a[200:203],v226 offset:32768
CV v68,v96,v97
MM a[12:15],a[136:139],v[8:11],a[12:15]
EX v100,v100
MM a[12:15],a[140:143],v[12:15],a[12:15]
RD a[204:207],v227 offset:32768
CV v69,v98,v99
MM a[4:7],a[140:143],v[4:7],a[4:7]
EX v101,v101
MM a[4:7],a[136:139],v[0:3],a[4:7]
buffer_load_dwordx4 v229, s[8:11], s64 offen offset:1024 lds
MM a[32:35],a[144:147],v[0:3],a[32:35]
EX v102,v102
MM a[32:35],a[148:151],v[4:7],a[32:35]
RD a[208:211],v224 offset:36864
CV v70,v100,v101
MM a[40:43],a[148:151],v[12:15],a[40:43]
EX v103,v103
MM a[40:43],a[144:147],v[8:11],a[40:43]
RD a[212:215],v225 offset:36864
CV v71,v102,v103
MM a[48:51],a[144:147],v[16:19],a[48:51]
EX v104,v104
MM a[48:51],a[148:151],v[20:23],a[48:51]
EX v105,v105
MM a[56:59],a[148:151],v[28:31],a[56:59]
buffer_load_dwordx4 v230, s[8:11], s64 offen offset:2048 lds
MM a[56:59],a[144:147],v[24:27],a[56:59]
EX v106,v106
MM a[60:63],a[152:155],v[24:27],a[60:63]
RD a[216:219],v226 offset:36864
CV v76,v104,v105
MM a[60:63],a[156:159],v[28:31],a[60:63]
EX v107,v107
MM a[52:55],a[156:159],v[20:23],a[52:55]
RD a[220:223],v227 offset:36864
CV v77,v106,v107
MM a[52:55],a[152:155],v[16:19],a[52:55]
EX v108,v108
MM a[44:47],a[152:155],v[8:11],a[44:47]
EX v109,v109
MM a[44:47],a[156:159],v[12:15],a[44:47]
buffer_load_dwordx4 v231, s[8:11], s64 offen offset:3072 lds
MM a[36:39],a[156:159],v[4:7],a[36:39]
s_add_u32 m0, s65, 65536
RD a[224:227],v224 offset:40960
CV v78,v108,v109
MM a[36:39],a[152:155],v[0:3],a[36:39]
EX v110,v110
MM a[64:67],a[160:163],v[0:3],a[64:67]
EX v111,v111
MM a[64:67],a[164:167],v[4:7],a[64:67]
RD a[228:231],v225 offset:40960
CV v79,v110,v111
MM a[72:75],a[164:167],v[12:15],a[72:75]
EX v112,v112
MM a[72:75],a[160:163],v[8:11],a[72:75]
buffer_load_dwordx4 v235, s[12:15], s81 offen offset:0 lds
MM a[80:83],a[160:163],v[16:19],a[80:83]
EX v113,v113
MM a[80:83],a[164:167],v[20:23],a[80:83]
RD a[232:235],v226 offset:40960
CV v84,v112,v113
MM a[88:91],a[164:167],v[28:31],a[88:91]
EX v114,v114
MM a[88:91],a[160:163],v[24:27],a[88:91]
EX v115,v115
MM a[92:95],a[168:171],v[24:27],a[92:95]
RD a[236:239],v227 offset:40960
CV v85,v114,v115
MM a[92:95],a[172:175],v[28:31],a[92:95]
EX v116,v116
MM a[84:87],a[172:175],v[20:23],a[84:87]
buffer_load_dwordx4 v235, s[12:15], s64 offen offset:1024 lds
MM a[84:87],a[168:171],v[16:19],a[84:87]
EX v117,v117
MM a[76:79],a[168:171],v[8:11],a[76:79]
RD a[240:243],v224 offset:45056
CV v86,v116,v117
MM a[76:79],a[172:175],v[12:15],a[76:79]
EX v118,v118
MM a[68:71],a[172:175],v[4:7],a[68:71]
EX v119,v119
MM a[68:71],a[168:171],v[0:3],a[68:71]
RD a[244:247],v225 offset:45056
CV v87,v118,v119
MM a[96:99],a[176:179],v[0:3],a[96:99]
EX v120,v120
MM a[96:99],a[180:183],v[4:7],a[96:99]
buffer_load_dwordx4 v235, s[12:15], s81 offen offset:2048 lds
MM a[104:107],a[180:183],v[12:15],a[104:107]
EX v121,v121
MM a[104:107],a[176:179],v[8:11],a[104:107]
RD a[248:251],v226 offset:45056
CV v92,v120,v121
MM a[112:115],a[176:179],v[16:19],a[112:115]
EX v122,v122
MM a[112:115],a[180:183],v[20:23],a[112:115]
EX v123,v123
MM a[120:123],a[180:183],v[28:31],a[120:123]
RD a[252:255],v227 offset:45056
CV v93,v122,v123
MM a[120:123],a[176:179],v[24:27],a[120:123]
EX v124,v124
MM a[124:127],a[184:187],v[24:27],a[124:127]
buffer_load_dwordx4 v235, s[12:15], s64 offen offset:3072 lds
MM a[124:127],a[188:191],v[28:31],a[124:127]
EX v125,v125
MM a[116:119],a[188:191],v[20:23],a[116:119]
EX v126,v126
MM a[116:119],a[184:187],v[16:19],a[116:119]
EX v127,v127
MM a[108:111],a[184:187],v[8:11],a[108:111]
CV v94,v124,v125
CV v95,v126,v127
MM a[108:111],a[188:191],v[12:15],a[108:111]
MM a[100:103],a[188:191],v[4:7],a[100:103]
MM a[100:103],a[184:187],v[0:3],a[100:103]
v_smfmac_f32_16x16x64_bf16 v[236:239], v[144:147], v[64:71], v234
v_smfmac_f32_16x16x64_bf16 v[236:239], v[148:151], v[72:79], v234
v_smfmac_f32_16x16x64_bf16 v[236:239], v[152:155], v[80:87], v234
v_smfmac_f32_16x16x64_bf16 v[236:239], v[156:159], v[88:95], v234
s_waitcnt lgkmcnt(0)
MM v[0:3],a[192:195],v[160:163],v[128:131]
s_add_u32 s64, s64, 16384
TR a[128:129],v232 offset:16384
s_add_u32 s81, s64, 896
MM v[0:3],a[196:199],v[176:179],v[0:3]
TR a[130:131],v232 offset:20480
TR a[132:133],v232 offset:24576
MM v[0:3],a[200:203],v[192:195],v[0:3]
TR a[134:135],v232 offset:28672
TR a[136:137],v233 offset:16384
MM v[0:3],a[204:207],v[208:211],v[0:3]
TR a[138:139],v233 offset:20480
v_add_f32 v247, v236, v237
MM v[4:7],a[220:223],v[208:211],v[128:131]
TR a[140:141],v233 offset:24576
v_cmp_nge_f32 vcc, s71, v247
MM v[4:7],a[216:219],v[192:195],v[4:7]
s_cbranch_vccnz LSITE21_%=
LBACK21_%=:
MM v[4:7],a[212:215],v[176:179],v[4:7]
s_lshr_b32 s77, %[sb], 3
TR a[142:143],v233 offset:28672
TR a[144:145],v232 offset:16896
MM v[4:7],a[208:211],v[160:163],v[4:7]
s_lshl_b32 s77, s77, 2
TR a[146:147],v232 offset:20992
TR a[148:149],v232 offset:25088
MM v[12:15],a[208:211],v[164:167],v[132:135]
s_add_u32 s77, s77, %[swave]
EX v0,v0
MM v[12:15],a[212:215],v[180:183],v[12:15]
s_lshl_b32 s77, s77, 9
EX v1,v1
MM v[12:15],a[216:219],v[196:199],v[12:15]
s_add_u32 s77, s77, 0
v_lshrrev_b32 v253, 1, v248
v_add_u32 v253, s77, v253
MM v[12:15],a[220:223],v[212:215],v[12:15]
s_mov_b32 s77, LKEND_ka-LPD1_%=-16
v_min_u32 v253, s77, v253
CV v0,v0,v1
MM v[8:11],a[204:207],v[212:215],v[132:135]
s_getpc_b64 s[98:99]
LPD1_%=:
global_load_dword v253, v253, s[98:99]
MM v[8:11],a[200:203],v[196:199],v[8:11]
EX v2,v2
MM v[8:11],a[196:199],v[180:183],v[8:11]
EX v3,v3
MM v[8:11],a[192:195],v[164:167],v[8:11]
TR a[150:151],v232 offset:29184
CV v1,v2,v3
MM v[16:19],a[192:195],v[168:171],v[136:139]
EX v4,v4
MM v[16:19],a[196:199],v[184:187],v[16:19]
EX v5,v5
MM v[16:19],a[200:203],v[200:203],v[16:19]
TR a[152:153],v233 offset:16896
CV v2,v4,v5
MM v[16:19],a[204:207],v[216:219],v[16:19]
EX v6,v6
MM v[20:23],a[220:223],v[216:219],v[136:139]
EX v7,v7
MM v[20:23],a[216:219],v[200:203],v[20:23]
TR a[154:155],v233 offset:20992
CV v3,v6,v7
MM v[20:23],a[212:215],v[184:187],v[20:23]
EX v8,v8
MM v[20:23],a[208:211],v[168:171],v[20:23]
TR a[156:157],v233 offset:25088
TR a[158:159],v233 offset:29184
MM v[28:31],a[208:211],v[172:175],v[140:143]
s_add_u32 m0, s45, 147456
s_add_u32 s44, s44, 1024
buffer_load_dwordx4 v248, s[24:27], s44 offen offset:0 lds
MM v[28:31],a[212:215],v[188:191],v[28:31]
EX v9,v9
MM v[28:31],a[216:219],v[204:207],v[28:31]
TR a[160:161],v232 offset:17408
CV v8,v8,v9
MM v[28:31],a[220:223],v[220:223],v[28:31]
EX v10,v10
MM v[24:27],a[204:207],v[220:223],v[140:143]
EX v11,v11
MM v[24:27],a[200:203],v[204:207],v[24:27]
TR a[162:163],v232 offset:21504
CV v9,v10,v11
MM v[24:27],a[196:199],v[188:191],v[24:27]
EX v12,v12
MM v[24:27],a[192:195],v[172:175],v[24:27]
TR a[164:165],v232 offset:25600
TR a[166:167],v232 offset:29696
MM v[32:35],a[224:227],v[160:163],v[128:131]
EX v13,v13
MM v[32:35],a[228:231],v[176:179],v[32:35]
TR a[168:169],v233 offset:17408
CV v10,v12,v13
MM v[32:35],a[232:235],v[192:195],v[32:35]
EX v14,v14
MM v[32:35],a[236:239],v[208:211],v[32:35]
EX v15,v15
MM v[36:39],a[252:255],v[208:211],v[128:131]
TR a[170:171],v233 offset:21504
CV v11,v14,v15
MM v[36:39],a[248:251],v[192:195],v[36:39]
EX v16,v16
MM v[36:39],a[244:247],v[176:179],v[36:39]
TR a[172:173],v233 offset:25600
TR a[174:175],v233 offset:29696
MM v[36:39],a[240:243],v[160:163],v[36:39]
EX v17,v17
MM v[44:47],a[240:243],v[164:167],v[132:135]
s_add_u32 s67, s95, 15360
TR a[176:177],v232 offset:17920
CV v16,v16,v17
MM v[44:47],a[244:247],v[180:183],v[44:47]
buffer_store_dwordx4 v[240:243], v248, s[20:23], s67 offen offset:0 sc1
MM v[44:47],a[248:251],v[196:199],v[44:47]
EX v18,v18
MM v[44:47],a[252:255],v[212:215],v[44:47]
EX v19,v19
MM v[40:43],a[236:239],v[212:215],v[132:135]
TR a[178:179],v232 offset:22016
CV v17,v18,v19
MM v[40:43],a[232:235],v[196:199],v[40:43]
EX v20,v20
MM v[40:43],a[228:231],v[180:183],v[40:43]
TR a[180:181],v232 offset:26112
TR a[182:183],v232 offset:30208
MM v[40:43],a[224:227],v[164:167],v[40:43]
EX v21,v21
MM v[48:51],a[224:227],v[168:171],v[136:139]
TR a[184:185],v233 offset:17920
CV v18,v20,v21
MM v[48:51],a[228:231],v[184:187],v[48:51]
EX v22,v22
MM v[48:51],a[232:235],v[200:203],v[48:51]
EX v23,v23
MM v[48:51],a[236:239],v[216:219],v[48:51]
TR a[186:187],v233 offset:22016
CV v19,v22,v23
MM v[52:55],a[252:255],v[216:219],v[136:139]
EX v24,v24
MM v[52:55],a[248:251],v[200:203],v[52:55]
TR a[188:189],v233 offset:26112
TR a[190:191],v233 offset:30208
MM v[52:55],a[244:247],v[184:187],v[52:55]
EX v25,v25
MM v[52:55],a[240:243],v[168:171],v[52:55]
EX v26,v26
MM v[60:63],a[240:243],v[172:175],v[140:143]
EX v27,v27
MM v[60:63],a[244:247],v[188:191],v[60:63]
EX v28,v28
MM v[60:63],a[248:251],v[204:207],v[60:63]
EX v29,v29
MM v[60:63],a[252:255],v[220:223],v[60:63]
EX v30,v30
MM v[56:59],a[236:239],v[220:223],v[140:143]
EX v31,v31
MM v[56:59],a[232:235],v[204:207],v[56:59]
EX v32,v32
MM v[56:59],a[228:231],v[188:191],v[56:59]
EX v33,v33
MM v[56:59],a[224:227],v[172:175],v[56:59]
s_waitcnt vmcnt(16) lgkmcnt(0)
s_barrier
MM a[0:3],a[128:131],v[64:67],a[0:3]
s_add_u32 m0, s65, 16384
CV v24,v24,v25
CV v25,v26,v27
MM a[0:3],a[132:135],v[68:71],a[0:3]
RD a[192:195],v224 offset:49152
CV v26,v28,v29
MM a[8:11],a[132:135],v[76:79],a[8:11]
RD a[196:199],v225 offset:49152
CV v27,v30,v31
MM a[8:11],a[128:131],v[72:75],a[8:11]
buffer_load_dwordx4 v228, s[8:11], s64 offen offset:0 lds
MM a[16:19],a[128:131],v[80:83],a[16:19]
RD a[200:203],v226 offset:49152
CV v4,v32,v33
MM a[16:19],a[132:135],v[84:87],a[16:19]
buffer_load_dwordx4 v229, s[8:11], s64 offen offset:1024 lds
MM a[24:27],a[132:135],v[92:95],a[24:27]
EX v34,v34
MM a[24:27],a[128:131],v[88:91],a[24:27]
RD a[204:207],v227 offset:49152
RD a[208:211],v224 offset:53248
MM a[28:31],a[136:139],v[88:91],a[28:31]
)ASM"
R"ASM(
EX v35,v35
MM a[28:31],a[140:143],v[92:95],a[28:31]
RD a[212:215],v225 offset:53248
CV v5,v34,v35
MM a[20:23],a[140:143],v[84:87],a[20:23]
EX v36,v36
MM a[20:23],a[136:139],v[80:83],a[20:23]
EX v37,v37
MM a[12:15],a[136:139],v[72:75],a[12:15]
RD a[216:219],v226 offset:53248
CV v6,v36,v37
MM a[12:15],a[140:143],v[76:79],a[12:15]
buffer_load_dwordx4 v230, s[8:11], s64 offen offset:2048 lds
MM a[4:7],a[140:143],v[68:71],a[4:7]
EX v38,v38
MM a[4:7],a[136:139],v[64:67],a[4:7]
EX v39,v39
MM a[32:35],a[144:147],v[64:67],a[32:35]
RD a[220:223],v227 offset:53248
CV v7,v38,v39
MM a[32:35],a[148:151],v[68:71],a[32:35]
EX v40,v40
MM a[40:43],a[148:151],v[76:79],a[40:43]
EX v41,v41
MM a[40:43],a[144:147],v[72:75],a[40:43]
RD a[224:227],v224 offset:57344
CV v12,v40,v41
MM a[48:51],a[144:147],v[80:83],a[48:51]
buffer_load_dwordx4 v231, s[8:11], s64 offen offset:3072 lds
MM a[48:51],a[148:151],v[84:87],a[48:51]
s_add_u32 m0, s65, 81920
EX v42,v42
MM a[56:59],a[148:151],v[92:95],a[56:59]
EX v43,v43
MM a[56:59],a[144:147],v[88:91],a[56:59]
RD a[228:231],v225 offset:57344
CV v13,v42,v43
MM a[60:63],a[152:155],v[88:91],a[60:63]
EX v44,v44
MM a[60:63],a[156:159],v[92:95],a[60:63]
buffer_load_dwordx4 v235, s[12:15], s81 offen offset:0 lds
MM a[52:55],a[156:159],v[84:87],a[52:55]
EX v45,v45
MM a[52:55],a[152:155],v[80:83],a[52:55]
s_nop 0
RD a[232:235],v226 offset:57344
CV v14,v44,v45
MM a[44:47],a[152:155],v[72:75],a[44:47]
s_nop 0
EX v46,v46
MM a[44:47],a[156:159],v[76:79],a[44:47]
s_nop 0
EX v47,v47
MM a[36:39],a[156:159],v[68:71],a[36:39]
s_nop 0
RD a[236:239],v227 offset:57344
CV v15,v46,v47
MM a[36:39],a[152:155],v[64:67],a[36:39]
LPG14_%=:
s_cbranch_execz LTR2_%=
EX v48,v48
MM a[64:67],a[160:163],v[64:67],a[64:67]
buffer_load_dwordx4 v235, s[12:15], s64 offen offset:1024 lds
MM a[64:67],a[164:167],v[68:71],a[64:67]
EX v49,v49
MM a[72:75],a[164:167],v[76:79],a[72:75]
RD a[240:243],v224 offset:61440
CV v20,v48,v49
MM a[72:75],a[160:163],v[72:75],a[72:75]
EX v50,v50
MM a[80:83],a[160:163],v[80:83],a[80:83]
EX v51,v51
MM a[80:83],a[164:167],v[84:87],a[80:83]
RD a[244:247],v225 offset:61440
CV v21,v50,v51
MM a[88:91],a[164:167],v[92:95],a[88:91]
EX v52,v52
MM a[88:91],a[160:163],v[88:91],a[88:91]
buffer_load_dwordx4 v235, s[12:15], s81 offen offset:2048 lds
MM a[92:95],a[168:171],v[88:91],a[92:95]
EX v53,v53
MM a[92:95],a[172:175],v[92:95],a[92:95]
RD a[248:251],v226 offset:61440
CV v22,v52,v53
MM a[84:87],a[172:175],v[84:87],a[84:87]
EX v54,v54
MM a[84:87],a[168:171],v[80:83],a[84:87]
EX v55,v55
MM a[76:79],a[168:171],v[72:75],a[76:79]
RD a[252:255],v227 offset:61440
CV v23,v54,v55
MM a[76:79],a[172:175],v[76:79],a[76:79]
EX v56,v56
MM a[68:71],a[172:175],v[68:71],a[68:71]
buffer_load_dwordx4 v235, s[12:15], s64 offen offset:3072 lds
MM a[68:71],a[168:171],v[64:67],a[68:71]
EX v57,v57
MM a[96:99],a[176:179],v[64:67],a[96:99]
EX v58,v58
MM a[96:99],a[180:183],v[68:71],a[96:99]
EX v59,v59
MM a[104:107],a[180:183],v[76:79],a[104:107]
EX v60,v60
MM a[104:107],a[176:179],v[72:75],a[104:107]
EX v61,v61
MM a[112:115],a[176:179],v[80:83],a[112:115]
EX v62,v62
MM a[112:115],a[180:183],v[84:87],a[112:115]
EX v63,v63
MM a[120:123],a[180:183],v[92:95],a[120:123]
CV v28,v56,v57
CV v29,v58,v59
MM a[120:123],a[176:179],v[88:91],a[120:123]
CV v30,v60,v61
CV v31,v62,v63
MM a[124:127],a[184:187],v[88:91],a[124:127]
MM a[124:127],a[188:191],v[92:95],a[124:127]
MM a[116:119],a[188:191],v[84:87],a[116:119]
MM a[116:119],a[184:187],v[80:83],a[116:119]
MM a[108:111],a[184:187],v[72:75],a[108:111]
MM a[108:111],a[188:191],v[76:79],a[108:111]
MM a[100:103],a[188:191],v[68:71],a[100:103]
MM a[100:103],a[184:187],v[64:67],a[100:103]
v_smfmac_f32_16x16x64_bf16 v[236:239], v[144:147], v[0:7], v234
v_smfmac_f32_16x16x64_bf16 v[236:239], v[148:151], v[8:15], v234
v_smfmac_f32_16x16x64_bf16 v[236:239], v[152:155], v[16:23], v234
v_smfmac_f32_16x16x64_bf16 v[236:239], v[156:159], v[24:31], v234
s_waitcnt lgkmcnt(0)
MM v[64:67],a[192:195],v[160:163],v[128:131]
s_add_u32 s64, s64, 16384
TR a[128:129],v232 offset:32768
s_add_u32 s81, s64, 896
MM v[64:67],a[196:199],v[176:179],v[64:67]
TR a[130:131],v232 offset:36864
TR a[132:133],v232 offset:40960
MM v[64:67],a[200:203],v[192:195],v[64:67]
TR a[134:135],v232 offset:45056
TR a[136:137],v233 offset:32768
MM v[64:67],a[204:207],v[208:211],v[64:67]
TR a[138:139],v233 offset:36864
v_add_f32 v246, v236, v237
MM v[68:71],a[220:223],v[208:211],v[128:131]
TR a[140:141],v233 offset:40960
v_cmp_nge_f32 vcc, s71, v246
MM v[68:71],a[216:219],v[192:195],v[68:71]
s_cbranch_vccnz LSITE22_%=
LBACK22_%=:
MM v[68:71],a[212:215],v[176:179],v[68:71]
s_add_u32 s67, s95, 16384
TR a[142:143],v233 offset:45056
TR a[144:145],v232 offset:33280
MM v[68:71],a[208:211],v[160:163],v[68:71]
buffer_store_dwordx4 v[240:243], v248, s[20:23], s67 offen offset:0 sc1
MM v[76:79],a[208:211],v[164:167],v[132:135]
EX v64,v64
MM v[76:79],a[212:215],v[180:183],v[76:79]
EX v65,v65
MM v[76:79],a[216:219],v[196:199],v[76:79]
TR a[146:147],v232 offset:37376
CV v64,v64,v65
MM v[76:79],a[220:223],v[212:215],v[76:79]
EX v66,v66
MM v[72:75],a[204:207],v[212:215],v[132:135]
EX v67,v67
MM v[72:75],a[200:203],v[196:199],v[72:75]
TR a[148:149],v232 offset:41472
CV v65,v66,v67
MM v[72:75],a[196:199],v[180:183],v[72:75]
EX v68,v68
MM v[72:75],a[192:195],v[164:167],v[72:75]
TR a[150:151],v232 offset:45568
TR a[152:153],v233 offset:33280
MM v[80:83],a[192:195],v[168:171],v[136:139]
EX v69,v69
MM v[80:83],a[196:199],v[184:187],v[80:83]
TR a[154:155],v233 offset:37376
CV v66,v68,v69
MM v[80:83],a[200:203],v[200:203],v[80:83]
EX v70,v70
MM v[80:83],a[204:207],v[216:219],v[80:83]
EX v71,v71
MM v[84:87],a[220:223],v[216:219],v[136:139]
TR a[156:157],v233 offset:41472
CV v67,v70,v71
MM v[84:87],a[216:219],v[200:203],v[84:87]
EX v72,v72
MM v[84:87],a[212:215],v[184:187],v[84:87]
TR a[158:159],v233 offset:45568
TR a[160:161],v232 offset:33792
MM v[84:87],a[208:211],v[168:171],v[84:87]
EX v73,v73
MM v[92:95],a[208:211],v[172:175],v[140:143]
TR a[162:163],v232 offset:37888
CV v72,v72,v73
MM v[92:95],a[212:215],v[188:191],v[92:95]
EX v74,v74
MM v[92:95],a[216:219],v[204:207],v[92:95]
EX v75,v75
MM v[92:95],a[220:223],v[220:223],v[92:95]
TR a[164:165],v232 offset:41984
CV v73,v74,v75
MM v[88:91],a[204:207],v[220:223],v[140:143]
EX v76,v76
MM v[88:91],a[200:203],v[204:207],v[88:91]
TR a[166:167],v232 offset:46080
TR a[168:169],v233 offset:33792
MM v[88:91],a[196:199],v[188:191],v[88:91]
EX v77,v77
MM v[88:91],a[192:195],v[172:175],v[88:91]
TR a[170:171],v233 offset:37888
CV v74,v76,v77
MM v[96:99],a[224:227],v[160:163],v[128:131]
EX v78,v78
MM v[96:99],a[228:231],v[176:179],v[96:99]
EX v79,v79
MM v[96:99],a[232:235],v[192:195],v[96:99]
TR a[172:173],v233 offset:41984
CV v75,v78,v79
MM v[96:99],a[236:239],v[208:211],v[96:99]
TR a[174:175],v233 offset:46080
TR a[176:177],v232 offset:34304
MM v[100:103],a[252:255],v[208:211],v[128:131]
EX v80,v80
MM v[100:103],a[248:251],v[192:195],v[100:103]
EX v81,v81
MM v[100:103],a[244:247],v[176:179],v[100:103]
TR a[178:179],v232 offset:38400
CV v80,v80,v81
MM v[100:103],a[240:243],v[160:163],v[100:103]
EX v82,v82
MM v[108:111],a[240:243],v[164:167],v[132:135]
s_add_u32 s67, s95, 17408
TR a[180:181],v232 offset:42496
TR a[182:183],v232 offset:46592
MM v[108:111],a[244:247],v[180:183],v[108:111]
buffer_store_dwordx4 v[240:243], v248, s[20:23], s67 offen offset:0 sc1
MM v[108:111],a[248:251],v[196:199],v[108:111]
EX v83,v83
MM v[108:111],a[252:255],v[212:215],v[108:111]
TR a[184:185],v233 offset:34304
CV v81,v82,v83
MM v[104:107],a[236:239],v[212:215],v[132:135]
EX v84,v84
MM v[104:107],a[232:235],v[196:199],v[104:107]
EX v85,v85
MM v[104:107],a[228:231],v[180:183],v[104:107]
TR a[186:187],v233 offset:38400
CV v82,v84,v85
MM v[104:107],a[224:227],v[164:167],v[104:107]
EX v86,v86
MM v[112:115],a[224:227],v[168:171],v[136:139]
TR a[188:189],v233 offset:42496
TR a[190:191],v233 offset:46592
MM v[112:115],a[228:231],v[184:187],v[112:115]
EX v87,v87
MM v[112:115],a[232:235],v[200:203],v[112:115]
CV v83,v86,v87
MM v[112:115],a[236:239],v[216:219],v[112:115]
EX v88,v88
MM v[116:119],a[252:255],v[216:219],v[136:139]
EX v89,v89
MM v[116:119],a[248:251],v[200:203],v[116:119]
EX v90,v90
MM v[116:119],a[244:247],v[184:187],v[116:119]
EX v91,v91
MM v[116:119],a[240:243],v[168:171],v[116:119]
EX v92,v92
MM v[124:127],a[240:243],v[172:175],v[140:143]
EX v93,v93
MM v[124:127],a[244:247],v[188:191],v[124:127]
EX v94,v94
MM v[124:127],a[248:251],v[204:207],v[124:127]
EX v95,v95
MM v[124:127],a[252:255],v[220:223],v[124:127]
EX v96,v96
MM v[120:123],a[236:239],v[220:223],v[140:143]
EX v97,v97
MM v[120:123],a[232:235],v[204:207],v[120:123]
EX v98,v98
MM v[120:123],a[228:231],v[188:191],v[120:123]
EX v99,v99
MM v[120:123],a[224:227],v[172:175],v[120:123]
s_waitcnt vmcnt(17) lgkmcnt(0)
s_barrier
MM a[0:3],a[128:131],v[0:3],a[0:3]
s_lshl_b32 s91, %[swave], 2
CV v88,v88,v89
CV v89,v90,v91
MM a[0:3],a[132:135],v[4:7],a[0:3]
s_add_u32 s91, s91, s48
RD a[192:195],v224 offset:0
CV v90,v92,v93
MM a[8:11],a[132:135],v[12:15],a[8:11]
s_add_u32 s91, s91, 69632
s_add_u32 m0, s65, 32768
CV v91,v94,v95
MM a[8:11],a[128:131],v[8:11],a[8:11]
buffer_load_dwordx4 v228, s[8:11], s64 offen offset:0 lds
MM a[16:19],a[128:131],v[16:19],a[16:19]
v_mov_b32 v250, s69
s_mov_b64 exec, 1
buffer_store_dword v250, off, s[20:23], s91 offset:0 sc1
s_mov_b64 exec, -1
MM a[16:19],a[132:135],v[20:23],a[16:19]
RD a[196:199],v225 offset:0
CV v68,v96,v97
MM a[24:27],a[132:135],v[28:31],a[24:27]
RD a[200:203],v226 offset:0
CV v69,v98,v99
MM a[24:27],a[128:131],v[24:27],a[24:27]
buffer_load_dwordx4 v229, s[8:11], s64 offen offset:1024 lds
MM a[28:31],a[136:139],v[24:27],a[28:31]
RD a[204:207],v227 offset:0
RD a[208:211],v224 offset:4096
MM a[28:31],a[140:143],v[28:31],a[28:31]
EX v100,v100
MM a[20:23],a[140:143],v[20:23],a[20:23]
buffer_load_dwordx4 v230, s[8:11], s64 offen offset:2048 lds
MM a[20:23],a[136:139],v[16:19],a[20:23]
EX v101,v101
MM a[12:15],a[136:139],v[8:11],a[12:15]
RD a[212:215],v225 offset:4096
CV v70,v100,v101
MM a[12:15],a[140:143],v[12:15],a[12:15]
EX v102,v102
MM a[4:7],a[140:143],v[4:7],a[4:7]
RD a[216:219],v226 offset:4096
RD a[220:223],v227 offset:4096
MM a[4:7],a[136:139],v[0:3],a[4:7]
EX v103,v103
MM a[32:35],a[144:147],v[0:3],a[32:35]
RD a[224:227],v224 offset:8192
CV v71,v102,v103
MM a[32:35],a[148:151],v[4:7],a[32:35]
EX v104,v104
MM a[40:43],a[148:151],v[12:15],a[40:43]
EX v105,v105
MM a[40:43],a[144:147],v[8:11],a[40:43]
RD a[228:231],v225 offset:8192
CV v76,v104,v105
MM a[48:51],a[144:147],v[16:19],a[48:51]
buffer_load_dwordx4 v231, s[8:11], s64 offen offset:3072 lds
MM a[48:51],a[148:151],v[20:23],a[48:51]
s_add_u32 m0, s65, 98304
EX v106,v106
MM a[56:59],a[148:151],v[28:31],a[56:59]
EX v107,v107
MM a[56:59],a[144:147],v[24:27],a[56:59]
RD a[232:235],v226 offset:8192
CV v77,v106,v107
MM a[60:63],a[152:155],v[24:27],a[60:63]
EX v108,v108
MM a[60:63],a[156:159],v[28:31],a[60:63]
buffer_load_dwordx4 v235, s[12:15], s81 offen offset:0 lds
MM a[52:55],a[156:159],v[20:23],a[52:55]
EX v109,v109
MM a[52:55],a[152:155],v[16:19],a[52:55]
RD a[236:239],v227 offset:8192
CV v78,v108,v109
MM a[44:47],a[152:155],v[8:11],a[44:47]
EX v110,v110
MM a[44:47],a[156:159],v[12:15],a[44:47]
EX v111,v111
MM a[36:39],a[156:159],v[4:7],a[36:39]
RD a[240:243],v224 offset:12288
CV v79,v110,v111
MM a[36:39],a[152:155],v[0:3],a[36:39]
EX v112,v112
MM a[64:67],a[160:163],v[0:3],a[64:67]
buffer_load_dwordx4 v235, s[12:15], s64 offen offset:1024 lds
MM a[64:67],a[164:167],v[4:7],a[64:67]
EX v113,v113
MM a[72:75],a[164:167],v[12:15],a[72:75]
RD a[244:247],v225 offset:12288
CV v84,v112,v113
MM a[72:75],a[160:163],v[8:11],a[72:75]
EX v114,v114
MM a[80:83],a[160:163],v[16:19],a[80:83]
EX v115,v115
MM a[80:83],a[164:167],v[20:23],a[80:83]
RD a[248:251],v226 offset:12288
CV v85,v114,v115
MM a[88:91],a[164:167],v[28:31],a[88:91]
EX v116,v116
MM a[88:91],a[160:163],v[24:27],a[88:91]
buffer_load_dwordx4 v235, s[12:15], s81 offen offset:2048 lds
MM a[92:95],a[168:171],v[24:27],a[92:95]
)ASM"
R"ASM(
EX v117,v117
MM a[92:95],a[172:175],v[28:31],a[92:95]
RD a[252:255],v227 offset:12288
CV v86,v116,v117
MM a[84:87],a[172:175],v[20:23],a[84:87]
EX v118,v118
MM a[84:87],a[168:171],v[16:19],a[84:87]
EX v119,v119
MM a[76:79],a[168:171],v[8:11],a[76:79]
CV v87,v118,v119
MM a[76:79],a[172:175],v[12:15],a[76:79]
EX v120,v120
MM a[68:71],a[172:175],v[4:7],a[68:71]
buffer_load_dwordx4 v235, s[12:15], s64 offen offset:3072 lds
MM a[68:71],a[168:171],v[0:3],a[68:71]
EX v121,v121
MM a[96:99],a[176:179],v[0:3],a[96:99]
EX v122,v122
MM a[96:99],a[180:183],v[4:7],a[96:99]
EX v123,v123
MM a[104:107],a[180:183],v[12:15],a[104:107]
EX v124,v124
MM a[104:107],a[176:179],v[8:11],a[104:107]
EX v125,v125
MM a[112:115],a[176:179],v[16:19],a[112:115]
EX v126,v126
MM a[112:115],a[180:183],v[20:23],a[112:115]
EX v127,v127
MM a[120:123],a[180:183],v[28:31],a[120:123]
CV v92,v120,v121
CV v93,v122,v123
MM a[120:123],a[176:179],v[24:27],a[120:123]
CV v94,v124,v125
CV v95,v126,v127
MM a[124:127],a[184:187],v[24:27],a[124:127]
MM a[124:127],a[188:191],v[28:31],a[124:127]
MM a[116:119],a[188:191],v[20:23],a[116:119]
MM a[116:119],a[184:187],v[16:19],a[116:119]
MM a[108:111],a[184:187],v[8:11],a[108:111]
MM a[108:111],a[188:191],v[12:15],a[108:111]
MM a[100:103],a[188:191],v[4:7],a[100:103]
MM a[100:103],a[184:187],v[0:3],a[100:103]
v_smfmac_f32_16x16x64_bf16 v[236:239], v[144:147], v[64:71], v234
v_smfmac_f32_16x16x64_bf16 v[236:239], v[148:151], v[72:79], v234
v_smfmac_f32_16x16x64_bf16 v[236:239], v[152:155], v[80:87], v234
v_smfmac_f32_16x16x64_bf16 v[236:239], v[156:159], v[88:95], v234
s_waitcnt lgkmcnt(0)
MM v[0:3],a[192:195],v[160:163],v[128:131]
s_add_u32 s64, s64, 16384
TR a[128:129],v232 offset:49152
s_add_u32 s81, s64, 896
MM v[0:3],a[196:199],v[176:179],v[0:3]
TR a[130:131],v232 offset:53248
TR a[132:133],v232 offset:57344
MM v[0:3],a[200:203],v[192:195],v[0:3]
TR a[134:135],v232 offset:61440
TR a[136:137],v233 offset:49152
MM v[0:3],a[204:207],v[208:211],v[0:3]
TR a[138:139],v233 offset:53248
v_add_f32 v247, v236, v237
MM v[4:7],a[220:223],v[208:211],v[128:131]
TR a[140:141],v233 offset:57344
v_cmp_nge_f32 vcc, s71, v247
MM v[4:7],a[216:219],v[192:195],v[4:7]
s_cbranch_vccnz LSITE23_%=
LBACK23_%=:
MM v[4:7],a[212:215],v[176:179],v[4:7]
s_add_u32 s67, s95, 18432
TR a[142:143],v233 offset:61440
TR a[144:145],v232 offset:49664
MM v[4:7],a[208:211],v[160:163],v[4:7]
buffer_store_dwordx4 v[240:243], v248, s[20:23], s67 offen offset:0 sc1
MM v[12:15],a[208:211],v[164:167],v[132:135]
EX v0,v0
MM v[12:15],a[212:215],v[180:183],v[12:15]
EX v1,v1
MM v[12:15],a[216:219],v[196:199],v[12:15]
TR a[146:147],v232 offset:53760
CV v0,v0,v1
MM v[12:15],a[220:223],v[212:215],v[12:15]
EX v2,v2
MM v[8:11],a[204:207],v[212:215],v[132:135]
EX v3,v3
MM v[8:11],a[200:203],v[196:199],v[8:11]
TR a[148:149],v232 offset:57856
CV v1,v2,v3
MM v[8:11],a[196:199],v[180:183],v[8:11]
EX v4,v4
MM v[8:11],a[192:195],v[164:167],v[8:11]
TR a[150:151],v232 offset:61952
TR a[152:153],v233 offset:49664
MM v[16:19],a[192:195],v[168:171],v[136:139]
EX v5,v5
MM v[16:19],a[196:199],v[184:187],v[16:19]
TR a[154:155],v233 offset:53760
CV v2,v4,v5
MM v[16:19],a[200:203],v[200:203],v[16:19]
EX v6,v6
MM v[16:19],a[204:207],v[216:219],v[16:19]
EX v7,v7
MM v[20:23],a[220:223],v[216:219],v[136:139]
TR a[156:157],v233 offset:57856
CV v3,v6,v7
MM v[20:23],a[216:219],v[200:203],v[20:23]
EX v8,v8
MM v[20:23],a[212:215],v[184:187],v[20:23]
TR a[158:159],v233 offset:61952
TR a[160:161],v232 offset:50176
MM v[20:23],a[208:211],v[168:171],v[20:23]
EX v9,v9
MM v[28:31],a[208:211],v[172:175],v[140:143]
s_add_u32 m0, s45, 147456
s_add_u32 s44, s44, 1024
buffer_load_dwordx4 v248, s[24:27], s44 offen offset:0 lds
MM v[28:31],a[212:215],v[188:191],v[28:31]
TR a[162:163],v232 offset:54272
CV v8,v8,v9
MM v[28:31],a[216:219],v[204:207],v[28:31]
EX v10,v10
MM v[28:31],a[220:223],v[220:223],v[28:31]
EX v11,v11
MM v[24:27],a[204:207],v[220:223],v[140:143]
TR a[164:165],v232 offset:58368
CV v9,v10,v11
MM v[24:27],a[200:203],v[204:207],v[24:27]
EX v12,v12
MM v[24:27],a[196:199],v[188:191],v[24:27]
TR a[166:167],v232 offset:62464
TR a[168:169],v233 offset:50176
MM v[24:27],a[192:195],v[172:175],v[24:27]
EX v13,v13
MM v[32:35],a[224:227],v[160:163],v[128:131]
TR a[170:171],v233 offset:54272
CV v10,v12,v13
MM v[32:35],a[228:231],v[176:179],v[32:35]
EX v14,v14
MM v[32:35],a[232:235],v[192:195],v[32:35]
EX v15,v15
MM v[32:35],a[236:239],v[208:211],v[32:35]
TR a[172:173],v233 offset:58368
CV v11,v14,v15
MM v[36:39],a[252:255],v[208:211],v[128:131]
EX v16,v16
MM v[36:39],a[248:251],v[192:195],v[36:39]
TR a[174:175],v233 offset:62464
TR a[176:177],v232 offset:50688
MM v[36:39],a[244:247],v[176:179],v[36:39]
EX v17,v17
MM v[36:39],a[240:243],v[160:163],v[36:39]
TR a[178:179],v232 offset:54784
CV v16,v16,v17
MM v[44:47],a[240:243],v[164:167],v[132:135]
s_add_u32 s67, s95, 19456
EX v18,v18
MM v[44:47],a[244:247],v[180:183],v[44:47]
buffer_store_dwordx4 v[240:243], v248, s[20:23], s67 offen offset:0 sc1
MM v[44:47],a[248:251],v[196:199],v[44:47]
EX v19,v19
MM v[44:47],a[252:255],v[212:215],v[44:47]
TR a[180:181],v232 offset:58880
CV v17,v18,v19
MM v[40:43],a[236:239],v[212:215],v[132:135]
EX v20,v20
MM v[40:43],a[232:235],v[196:199],v[40:43]
TR a[182:183],v232 offset:62976
TR a[184:185],v233 offset:50688
MM v[40:43],a[228:231],v[180:183],v[40:43]
EX v21,v21
MM v[40:43],a[224:227],v[164:167],v[40:43]
TR a[186:187],v233 offset:54784
CV v18,v20,v21
MM v[48:51],a[224:227],v[168:171],v[136:139]
EX v22,v22
MM v[48:51],a[228:231],v[184:187],v[48:51]
EX v23,v23
MM v[48:51],a[232:235],v[200:203],v[48:51]
TR a[188:189],v233 offset:58880
CV v19,v22,v23
MM v[48:51],a[236:239],v[216:219],v[48:51]
EX v24,v24
MM v[52:55],a[252:255],v[216:219],v[136:139]
EX v25,v25
MM v[52:55],a[248:251],v[200:203],v[52:55]
TR a[190:191],v233 offset:62976
CV v24,v24,v25
MM v[52:55],a[244:247],v[184:187],v[52:55]
EX v26,v26
MM v[52:55],a[240:243],v[168:171],v[52:55]
EX v27,v27
MM v[60:63],a[240:243],v[172:175],v[140:143]
EX v28,v28
MM v[60:63],a[244:247],v[188:191],v[60:63]
EX v29,v29
MM v[60:63],a[248:251],v[204:207],v[60:63]
EX v30,v30
MM v[60:63],a[252:255],v[220:223],v[60:63]
EX v31,v31
MM v[56:59],a[236:239],v[220:223],v[140:143]
EX v32,v32
MM v[56:59],a[232:235],v[204:207],v[56:59]
s_nop 0
EX v33,v33
MM v[56:59],a[228:231],v[188:191],v[56:59]
s_nop 0
EX v34,v34
MM v[56:59],a[224:227],v[172:175],v[56:59]
s_waitcnt vmcnt(18) lgkmcnt(0)
s_barrier
MM a[0:3],a[128:131],v[64:67],a[0:3]
s_add_u32 m0, s65, 49152
CV v25,v26,v27
CV v26,v28,v29
MM a[0:3],a[132:135],v[68:71],a[0:3]
s_nop 0
RD a[192:195],v224 offset:16384
CV v27,v30,v31
MM a[8:11],a[132:135],v[76:79],a[8:11]
buffer_load_dwordx4 v228, s[8:11], s64 offen offset:0 lds
MM a[8:11],a[128:131],v[72:75],a[8:11]
s_nop 0
RD a[196:199],v225 offset:16384
CV v4,v32,v33
MM a[16:19],a[128:131],v[80:83],a[16:19]
s_nop 0
RD a[200:203],v226 offset:16384
RD a[204:207],v227 offset:16384
MM a[16:19],a[132:135],v[84:87],a[16:19]
buffer_load_dwordx4 v229, s[8:11], s64 offen offset:1024 lds
MM a[24:27],a[132:135],v[92:95],a[24:27]
s_nop 0
RD a[208:211],v224 offset:20480
RD a[212:215],v225 offset:20480
MM a[24:27],a[128:131],v[88:91],a[24:27]
s_nop 0
EX v35,v35
MM a[28:31],a[136:139],v[88:91],a[28:31]
s_nop 0
RD a[216:219],v226 offset:20480
CV v5,v34,v35
MM a[28:31],a[140:143],v[92:95],a[28:31]
s_nop 0
EX v36,v36
MM a[20:23],a[140:143],v[84:87],a[20:23]
buffer_load_dwordx4 v230, s[8:11], s64 offen offset:2048 lds
MM a[20:23],a[136:139],v[80:83],a[20:23]
s_nop 0
EX v37,v37
MM a[12:15],a[136:139],v[72:75],a[12:15]
s_nop 0
RD a[220:223],v227 offset:20480
CV v6,v36,v37
MM a[12:15],a[140:143],v[76:79],a[12:15]
s_nop 0
EX v38,v38
MM a[4:7],a[140:143],v[68:71],a[4:7]
s_nop 0
EX v39,v39
MM a[4:7],a[136:139],v[64:67],a[4:7]
s_nop 0
RD a[224:227],v224 offset:24576
CV v7,v38,v39
MM a[32:35],a[144:147],v[64:67],a[32:35]
LPG15_%=:
s_cbranch_execz LTR2_%=
EX v40,v40
MM a[32:35],a[148:151],v[68:71],a[32:35]
buffer_load_dwordx4 v231, s[8:11], s64 offen offset:3072 lds
MM a[40:43],a[148:151],v[76:79],a[40:43]
s_add_u32 m0, s65, 114688
EX v41,v41
MM a[40:43],a[144:147],v[72:75],a[40:43]
RD a[228:231],v225 offset:24576
CV v12,v40,v41
MM a[48:51],a[144:147],v[80:83],a[48:51]
EX v42,v42
MM a[48:51],a[148:151],v[84:87],a[48:51]
EX v43,v43
MM a[56:59],a[148:151],v[92:95],a[56:59]
RD a[232:235],v226 offset:24576
CV v13,v42,v43
MM a[56:59],a[144:147],v[88:91],a[56:59]
EX v44,v44
MM a[60:63],a[152:155],v[88:91],a[60:63]
buffer_load_dwordx4 v235, s[12:15], s81 offen offset:0 lds
MM a[60:63],a[156:159],v[92:95],a[60:63]
EX v45,v45
MM a[52:55],a[156:159],v[84:87],a[52:55]
RD a[236:239],v227 offset:24576
CV v14,v44,v45
MM a[52:55],a[152:155],v[80:83],a[52:55]
EX v46,v46
MM a[44:47],a[152:155],v[72:75],a[44:47]
EX v47,v47
MM a[44:47],a[156:159],v[76:79],a[44:47]
RD a[240:243],v224 offset:28672
CV v15,v46,v47
MM a[36:39],a[156:159],v[68:71],a[36:39]
buffer_load_dwordx4 v235, s[12:15], s64 offen offset:1024 lds
MM a[36:39],a[152:155],v[64:67],a[36:39]
EX v48,v48
MM a[64:67],a[160:163],v[64:67],a[64:67]
EX v49,v49
MM a[64:67],a[164:167],v[68:71],a[64:67]
RD a[244:247],v225 offset:28672
CV v20,v48,v49
MM a[72:75],a[164:167],v[76:79],a[72:75]
EX v50,v50
MM a[72:75],a[160:163],v[72:75],a[72:75]
EX v51,v51
MM a[80:83],a[160:163],v[80:83],a[80:83]
RD a[248:251],v226 offset:28672
CV v21,v50,v51
MM a[80:83],a[164:167],v[84:87],a[80:83]
buffer_load_dwordx4 v235, s[12:15], s81 offen offset:2048 lds
MM a[88:91],a[164:167],v[92:95],a[88:91]
EX v52,v52
MM a[88:91],a[160:163],v[88:91],a[88:91]
EX v53,v53
MM a[92:95],a[168:171],v[88:91],a[92:95]
RD a[252:255],v227 offset:28672
CV v22,v52,v53
MM a[92:95],a[172:175],v[92:95],a[92:95]
EX v54,v54
MM a[84:87],a[172:175],v[84:87],a[84:87]
buffer_load_dwordx4 v235, s[12:15], s64 offen offset:3072 lds
MM a[84:87],a[168:171],v[80:83],a[84:87]
EX v55,v55
MM a[76:79],a[168:171],v[72:75],a[76:79]
CV v23,v54,v55
MM a[76:79],a[172:175],v[76:79],a[76:79]
EX v56,v56
MM a[68:71],a[172:175],v[68:71],a[68:71]
EX v57,v57
MM a[68:71],a[168:171],v[64:67],a[68:71]
EX v58,v58
MM a[96:99],a[176:179],v[64:67],a[96:99]
EX v59,v59
MM a[96:99],a[180:183],v[68:71],a[96:99]
EX v60,v60
MM a[104:107],a[180:183],v[76:79],a[104:107]
EX v61,v61
MM a[104:107],a[176:179],v[72:75],a[104:107]
EX v62,v62
MM a[112:115],a[176:179],v[80:83],a[112:115]
EX v63,v63
MM a[112:115],a[180:183],v[84:87],a[112:115]
CV v28,v56,v57
CV v29,v58,v59
MM a[120:123],a[180:183],v[92:95],a[120:123]
CV v30,v60,v61
CV v31,v62,v63
MM a[120:123],a[176:179],v[88:91],a[120:123]
MM a[124:127],a[184:187],v[88:91],a[124:127]
MM a[124:127],a[188:191],v[92:95],a[124:127]
MM a[116:119],a[188:191],v[84:87],a[116:119]
MM a[116:119],a[184:187],v[80:83],a[116:119]
MM a[108:111],a[184:187],v[72:75],a[108:111]
MM a[108:111],a[188:191],v[76:79],a[108:111]
MM a[100:103],a[188:191],v[68:71],a[100:103]
MM a[100:103],a[184:187],v[64:67],a[100:103]
v_smfmac_f32_16x16x64_bf16 v[236:239], v[144:147], v[0:7], v234
v_smfmac_f32_16x16x64_bf16 v[236:239], v[148:151], v[8:15], v234
v_smfmac_f32_16x16x64_bf16 v[236:239], v[152:155], v[16:23], v234
v_smfmac_f32_16x16x64_bf16 v[236:239], v[156:159], v[24:31], v234
s_waitcnt lgkmcnt(0)
MM v[64:67],a[192:195],v[160:163],v[128:131]
s_add_u32 s64, s64, 16384
TR a[128:129],v232 offset:0
s_add_u32 s81, s64, 896
MM v[64:67],a[196:199],v[176:179],v[64:67]
TR a[130:131],v232 offset:4096
TR a[132:133],v232 offset:8192
MM v[64:67],a[200:203],v[192:195],v[64:67]
TR a[134:135],v232 offset:12288
TR a[136:137],v233 offset:0
MM v[64:67],a[204:207],v[208:211],v[64:67]
TR a[138:139],v233 offset:4096
v_add_f32 v246, v236, v237
MM v[68:71],a[220:223],v[208:211],v[128:131]
TR a[140:141],v233 offset:8192
v_cmp_nge_f32 vcc, s71, v246
MM v[68:71],a[216:219],v[192:195],v[68:71]
s_cbranch_vccnz LSITE24_%=
LBACK24_%=:
MM v[68:71],a[212:215],v[176:179],v[68:71]
v_bfe_u32 v242, v248, 6, 3
v_mul_u32_u24 v242, 1280, v242
MM v[68:71],a[208:211],v[160:163],v[68:71]
v_and_b32 v240, 48, v248
v_bfe_u32 v243, v248, 8, 1
MM v[76:79],a[208:211],v[164:167],v[132:135]
)ASM"
R"ASM(
v_lshlrev_b32 v243, 5, v243
v_xor_b32 v240, v240, v243
MM v[76:79],a[212:215],v[180:183],v[76:79]
v_add_u32 v240, v240, v242
v_lshrrev_b32 v243, 9, v248
MM v[76:79],a[216:219],v[196:199],v[76:79]
v_mul_u32_u24 v242, 10240, v243
v_add_u32 v241, v240, v242
MM v[76:79],a[220:223],v[212:215],v[76:79]
TR a[142:143],v233 offset:12288
v_lshl_add_u32 v240, v243, 6, v240
MM v[72:75],a[204:207],v[212:215],v[132:135]
s_and_b32 s66, s84, 1
EX v64,v64
MM v[72:75],a[200:203],v[196:199],v[72:75]
s_lshl_b32 s94, s66, 6
TR a[144:145],v232 offset:512
v_add_u32 v240, s94, v240
MM v[72:75],a[196:199],v[180:183],v[72:75]
s_xor_b32 s66, s66, 1
EX v65,v65
MM v[72:75],a[192:195],v[164:167],v[72:75]
s_lshl_b32 s66, s66, 8
v_add_u32 v241, s66, v241
CV v64,v64,v65
MM v[80:83],a[192:195],v[168:171],v[136:139]
s_mul_i32 s89, %[swave], 20480
EX v66,v66
MM v[80:83],a[196:199],v[184:187],v[80:83]
s_lshl_b32 s94, %[swave], 10
EX v67,v67
MM v[80:83],a[200:203],v[200:203],v[80:83]
s_add_u32 s94, s94, %[slds]
EX v68,v68
MM v[80:83],a[204:207],v[216:219],v[80:83]
s_xor_b32 s66, s84, 0
EX v69,v69
MM v[84:87],a[220:223],v[216:219],v[136:139]
s_mul_i32 s66, s66, 163840
TR a[146:147],v232 offset:4608
CV v65,v66,v67
MM v[84:87],a[216:219],v[200:203],v[84:87]
s_add_u32 s85, s66, s89
TR a[148:149],v232 offset:8704
TR a[150:151],v232 offset:12800
MM v[84:87],a[212:215],v[184:187],v[84:87]
s_xor_b32 s66, s84, 1
EX v70,v70
MM v[84:87],a[208:211],v[168:171],v[84:87]
s_mul_i32 s66, s66, 163840
EX v71,v71
MM v[92:95],a[208:211],v[172:175],v[140:143]
s_add_u32 s86, s66, s89
s_add_u32 m0, s45, 147456
s_add_u32 s44, s44, 1024
buffer_load_dwordx4 v248, s[24:27], s44 offen offset:0 lds
MM v[92:95],a[212:215],v[188:191],v[92:95]
s_xor_b32 s66, s84, 2
EX v72,v72
MM v[92:95],a[216:219],v[204:207],v[92:95]
s_mul_i32 s66, s66, 163840
TR a[152:153],v233 offset:512
CV v66,v68,v69
MM v[92:95],a[220:223],v[220:223],v[92:95]
s_add_u32 s87, s66, s89
EX v73,v73
MM v[88:91],a[204:207],v[220:223],v[140:143]
s_xor_b32 s66, s84, 3
TR a[154:155],v233 offset:4608
TR a[156:157],v233 offset:8704
MM v[88:91],a[200:203],v[204:207],v[88:91]
s_mul_i32 s66, s66, 163840
EX v74,v74
MM v[88:91],a[196:199],v[188:191],v[88:91]
s_add_u32 s88, s66, s89
TR a[158:159],v233 offset:12800
CV v67,v70,v71
MM v[88:91],a[192:195],v[172:175],v[88:91]
s_lshl_b32 s89, %[swave], 11
EX v75,v75
MM v[96:99],a[224:227],v[160:163],v[128:131]
s_add_u32 s89, s89, %[slds]
TR a[160:161],v232 offset:1024
CV v72,v72,v73
MM v[96:99],a[228:231],v[176:179],v[96:99]
s_lshr_b32 s96, s49, 2
EX v76,v76
MM v[96:99],a[232:235],v[192:195],v[96:99]
s_lshl_b32 s97, s50, 3
TR a[162:163],v232 offset:5120
CV v73,v74,v75
MM v[96:99],a[236:239],v[208:211],v[96:99]
s_add_u32 s96, s96, s97
EX v77,v77
MM v[100:103],a[252:255],v[208:211],v[128:131]
s_lshl_b32 s96, s96, 2
TR a[164:165],v232 offset:9216
TR a[166:167],v232 offset:13312
MM v[100:103],a[248:251],v[192:195],v[100:103]
s_add_u32 s96, s96, %[swave]
EX v78,v78
MM v[100:103],a[244:247],v[176:179],v[100:103]
s_lshl_b32 s96, s96, 6
EX v79,v79
MM v[100:103],a[240:243],v[160:163],v[100:103]
s_add_u32 s96, s96, 0
TR a[168:169],v233 offset:1024
CV v74,v76,v77
MM v[108:111],a[240:243],v[164:167],v[132:135]
buffer_load_dword v242, off, s[20:23], s96 sc1
MM v[108:111],a[244:247],v[180:183],v[108:111]
buffer_load_dword v243, off, s[20:23], s19 sc1
MM v[108:111],a[248:251],v[196:199],v[108:111]
EX v80,v80
MM v[108:111],a[252:255],v[212:215],v[108:111]
EX v81,v81
MM v[104:107],a[236:239],v[212:215],v[132:135]
TR a[170:171],v233 offset:5120
TR a[172:173],v233 offset:9216
MM v[104:107],a[232:235],v[196:199],v[104:107]
EX v82,v82
MM v[104:107],a[228:231],v[180:183],v[104:107]
EX v83,v83
MM v[104:107],a[224:227],v[164:167],v[104:107]
TR a[174:175],v233 offset:13312
CV v75,v78,v79
MM v[112:115],a[224:227],v[168:171],v[136:139]
EX v84,v84
MM v[112:115],a[228:231],v[184:187],v[112:115]
TR a[176:177],v232 offset:1536
TR a[178:179],v232 offset:5632
MM v[112:115],a[232:235],v[200:203],v[112:115]
EX v85,v85
MM v[112:115],a[236:239],v[216:219],v[112:115]
TR a[180:181],v232 offset:9728
CV v80,v80,v81
MM v[116:119],a[252:255],v[216:219],v[136:139]
EX v86,v86
MM v[116:119],a[248:251],v[200:203],v[116:119]
TR a[182:183],v232 offset:13824
CV v81,v82,v83
MM v[116:119],a[244:247],v[184:187],v[116:119]
EX v87,v87
MM v[116:119],a[240:243],v[168:171],v[116:119]
TR a[184:185],v233 offset:1536
CV v82,v84,v85
MM v[124:127],a[240:243],v[172:175],v[140:143]
EX v88,v88
MM v[124:127],a[244:247],v[188:191],v[124:127]
TR a[186:187],v233 offset:5632
TR a[188:189],v233 offset:9728
MM v[124:127],a[248:251],v[204:207],v[124:127]
TR a[190:191],v233 offset:13824
CV v83,v86,v87
MM v[124:127],a[252:255],v[220:223],v[124:127]
EX v89,v89
MM v[120:123],a[236:239],v[220:223],v[140:143]
EX v90,v90
MM v[120:123],a[232:235],v[204:207],v[120:123]
EX v91,v91
MM v[120:123],a[228:231],v[188:191],v[120:123]
EX v92,v92
MM v[120:123],a[224:227],v[172:175],v[120:123]
s_waitcnt vmcnt(18) lgkmcnt(0)
s_barrier
MM a[0:3],a[128:131],v[0:3],a[0:3]
s_add_u32 m0, s65, 0
CV v88,v88,v89
CV v89,v90,v91
MM a[0:3],a[132:135],v[4:7],a[0:3]
EX v93,v93
MM a[8:11],a[132:135],v[12:15],a[8:11]
EX v94,v94
MM a[8:11],a[128:131],v[8:11],a[8:11]
EX v95,v95
MM a[16:19],a[128:131],v[16:19],a[16:19]
RD a[192:195],v224 offset:32768
CV v90,v92,v93
MM a[16:19],a[132:135],v[20:23],a[16:19]
EX v96,v96
MM a[24:27],a[132:135],v[28:31],a[24:27]
RD a[196:199],v225 offset:32768
CV v91,v94,v95
MM a[24:27],a[128:131],v[24:27],a[24:27]
EX v97,v97
MM a[28:31],a[136:139],v[24:27],a[28:31]
buffer_load_dwordx4 v228, s[8:11], s64 offen offset:0 lds
MM a[28:31],a[140:143],v[28:31],a[28:31]
EX v98,v98
MM a[20:23],a[140:143],v[20:23],a[20:23]
EX v99,v99
MM a[20:23],a[136:139],v[16:19],a[20:23]
RD a[200:203],v226 offset:32768
CV v68,v96,v97
MM a[12:15],a[136:139],v[8:11],a[12:15]
EX v100,v100
MM a[12:15],a[140:143],v[12:15],a[12:15]
RD a[204:207],v227 offset:32768
CV v69,v98,v99
MM a[4:7],a[140:143],v[4:7],a[4:7]
EX v101,v101
MM a[4:7],a[136:139],v[0:3],a[4:7]
buffer_load_dwordx4 v229, s[8:11], s64 offen offset:1024 lds
MM a[32:35],a[144:147],v[0:3],a[32:35]
EX v102,v102
MM a[32:35],a[148:151],v[4:7],a[32:35]
RD a[208:211],v224 offset:36864
CV v70,v100,v101
MM a[40:43],a[148:151],v[12:15],a[40:43]
EX v103,v103
MM a[40:43],a[144:147],v[8:11],a[40:43]
RD a[212:215],v225 offset:36864
CV v71,v102,v103
MM a[48:51],a[144:147],v[16:19],a[48:51]
EX v104,v104
MM a[48:51],a[148:151],v[20:23],a[48:51]
EX v105,v105
MM a[56:59],a[148:151],v[28:31],a[56:59]
buffer_load_dwordx4 v230, s[8:11], s64 offen offset:2048 lds
MM a[56:59],a[144:147],v[24:27],a[56:59]
EX v106,v106
MM a[60:63],a[152:155],v[24:27],a[60:63]
RD a[216:219],v226 offset:36864
CV v76,v104,v105
MM a[60:63],a[156:159],v[28:31],a[60:63]
EX v107,v107
MM a[52:55],a[156:159],v[20:23],a[52:55]
RD a[220:223],v227 offset:36864
CV v77,v106,v107
MM a[52:55],a[152:155],v[16:19],a[52:55]
EX v108,v108
MM a[44:47],a[152:155],v[8:11],a[44:47]
EX v109,v109
MM a[44:47],a[156:159],v[12:15],a[44:47]
buffer_load_dwordx4 v231, s[8:11], s64 offen offset:3072 lds
MM a[36:39],a[156:159],v[4:7],a[36:39]
s_add_u32 m0, s65, 65536
RD a[224:227],v224 offset:40960
CV v78,v108,v109
MM a[36:39],a[152:155],v[0:3],a[36:39]
EX v110,v110
MM a[64:67],a[160:163],v[0:3],a[64:67]
EX v111,v111
MM a[64:67],a[164:167],v[4:7],a[64:67]
RD a[228:231],v225 offset:40960
CV v79,v110,v111
MM a[72:75],a[164:167],v[12:15],a[72:75]
EX v112,v112
MM a[72:75],a[160:163],v[8:11],a[72:75]
buffer_load_dwordx4 v235, s[12:15], s81 offen offset:0 lds
MM a[80:83],a[160:163],v[16:19],a[80:83]
EX v113,v113
MM a[80:83],a[164:167],v[20:23],a[80:83]
RD a[232:235],v226 offset:40960
CV v84,v112,v113
MM a[88:91],a[164:167],v[28:31],a[88:91]
EX v114,v114
MM a[88:91],a[160:163],v[24:27],a[88:91]
EX v115,v115
MM a[92:95],a[168:171],v[24:27],a[92:95]
RD a[236:239],v227 offset:40960
CV v85,v114,v115
MM a[92:95],a[172:175],v[28:31],a[92:95]
EX v116,v116
MM a[84:87],a[172:175],v[20:23],a[84:87]
buffer_load_dwordx4 v235, s[12:15], s64 offen offset:1024 lds
MM a[84:87],a[168:171],v[16:19],a[84:87]
EX v117,v117
MM a[76:79],a[168:171],v[8:11],a[76:79]
RD a[240:243],v224 offset:45056
CV v86,v116,v117
MM a[76:79],a[172:175],v[12:15],a[76:79]
EX v118,v118
MM a[68:71],a[172:175],v[4:7],a[68:71]
EX v119,v119
MM a[68:71],a[168:171],v[0:3],a[68:71]
RD a[244:247],v225 offset:45056
CV v87,v118,v119
MM a[96:99],a[176:179],v[0:3],a[96:99]
EX v120,v120
MM a[96:99],a[180:183],v[4:7],a[96:99]
buffer_load_dwordx4 v235, s[12:15], s81 offen offset:2048 lds
MM a[104:107],a[180:183],v[12:15],a[104:107]
EX v121,v121
MM a[104:107],a[176:179],v[8:11],a[104:107]
RD a[248:251],v226 offset:45056
CV v92,v120,v121
MM a[112:115],a[176:179],v[16:19],a[112:115]
EX v122,v122
MM a[112:115],a[180:183],v[20:23],a[112:115]
EX v123,v123
MM a[120:123],a[180:183],v[28:31],a[120:123]
RD a[252:255],v227 offset:45056
CV v93,v122,v123
MM a[120:123],a[176:179],v[24:27],a[120:123]
EX v124,v124
MM a[124:127],a[184:187],v[24:27],a[124:127]
buffer_load_dwordx4 v235, s[12:15], s64 offen offset:3072 lds
MM a[124:127],a[188:191],v[28:31],a[124:127]
EX v125,v125
MM a[116:119],a[188:191],v[20:23],a[116:119]
EX v126,v126
MM a[116:119],a[184:187],v[16:19],a[116:119]
EX v127,v127
MM a[108:111],a[184:187],v[8:11],a[108:111]
CV v94,v124,v125
CV v95,v126,v127
MM a[108:111],a[188:191],v[12:15],a[108:111]
MM a[100:103],a[188:191],v[4:7],a[100:103]
MM a[100:103],a[184:187],v[0:3],a[100:103]
v_smfmac_f32_16x16x64_bf16 v[236:239], v[144:147], v[64:71], v234
v_smfmac_f32_16x16x64_bf16 v[236:239], v[148:151], v[72:79], v234
v_smfmac_f32_16x16x64_bf16 v[236:239], v[152:155], v[80:87], v234
v_smfmac_f32_16x16x64_bf16 v[236:239], v[156:159], v[88:95], v234
s_waitcnt lgkmcnt(0)
MM v[0:3],a[192:195],v[160:163],v[128:131]
s_add_u32 s64, s64, 16384
TR a[128:129],v232 offset:16384
s_add_u32 s81, s64, 896
MM v[0:3],a[196:199],v[176:179],v[0:3]
TR a[130:131],v232 offset:20480
TR a[132:133],v232 offset:24576
MM v[0:3],a[200:203],v[192:195],v[0:3]
TR a[134:135],v232 offset:28672
TR a[136:137],v233 offset:16384
MM v[0:3],a[204:207],v[208:211],v[0:3]
TR a[138:139],v233 offset:20480
v_add_f32 v247, v236, v237
MM v[4:7],a[220:223],v[208:211],v[128:131]
TR a[140:141],v233 offset:24576
v_cmp_nge_f32 vcc, s71, v247
MM v[4:7],a[216:219],v[192:195],v[4:7]
s_cbranch_vccnz LSITE25_%=
LBACK25_%=:
MM v[4:7],a[212:215],v[176:179],v[4:7]
TR a[142:143],v233 offset:28672
TR a[144:145],v232 offset:16896
MM v[4:7],a[208:211],v[160:163],v[4:7]
s_mul_i32 s76, s46, 192
TR a[146:147],v232 offset:20992
TR a[148:149],v232 offset:25088
MM v[12:15],a[208:211],v[164:167],v[132:135]
s_add_u32 s76, s76, 69632
v_lshrrev_b32 v253, 2, v248
v_min_u32 v253, 188, v253
MM v[12:15],a[212:215],v[180:183],v[12:15]
buffer_load_dword v252, v253, s[20:23], s76 offen offset:0 sc1
MM v[12:15],a[216:219],v[196:199],v[12:15]
EX v0,v0
MM v[12:15],a[220:223],v[212:215],v[12:15]
EX v1,v1
MM v[8:11],a[204:207],v[212:215],v[132:135]
EX v2,v2
MM v[8:11],a[200:203],v[196:199],v[8:11]
TR a[150:151],v232 offset:29184
CV v0,v0,v1
MM v[8:11],a[196:199],v[180:183],v[8:11]
EX v3,v3
MM v[8:11],a[192:195],v[164:167],v[8:11]
TR a[152:153],v233 offset:16896
CV v1,v2,v3
MM v[16:19],a[192:195],v[168:171],v[136:139]
EX v4,v4
MM v[16:19],a[196:199],v[184:187],v[16:19]
EX v5,v5
MM v[16:19],a[200:203],v[200:203],v[16:19]
TR a[154:155],v233 offset:20992
CV v2,v4,v5
MM v[16:19],a[204:207],v[216:219],v[16:19]
EX v6,v6
MM v[20:23],a[220:223],v[216:219],v[136:139]
EX v7,v7
MM v[20:23],a[216:219],v[200:203],v[20:23]
TR a[156:157],v233 offset:25088
CV v3,v6,v7
MM v[20:23],a[212:215],v[184:187],v[20:23]
EX v8,v8
MM v[20:23],a[208:211],v[168:171],v[20:23]
TR a[158:159],v233 offset:29184
TR a[160:161],v232 offset:17408
MM v[28:31],a[208:211],v[172:175],v[140:143]
EX v9,v9
MM v[28:31],a[212:215],v[188:191],v[28:31]
TR a[162:163],v232 offset:21504
CV v8,v8,v9
MM v[28:31],a[216:219],v[204:207],v[28:31]
EX v10,v10
MM v[28:31],a[220:223],v[220:223],v[28:31]
EX v11,v11
MM v[24:27],a[204:207],v[220:223],v[140:143]
TR a[164:165],v232 offset:25600
)ASM"
R"ASM(
CV v9,v10,v11
MM v[24:27],a[200:203],v[204:207],v[24:27]
EX v12,v12
MM v[24:27],a[196:199],v[188:191],v[24:27]
TR a[166:167],v232 offset:29696
TR a[168:169],v233 offset:17408
MM v[24:27],a[192:195],v[172:175],v[24:27]
EX v13,v13
MM v[32:35],a[224:227],v[160:163],v[128:131]
TR a[170:171],v233 offset:21504
CV v10,v12,v13
MM v[32:35],a[228:231],v[176:179],v[32:35]
EX v14,v14
MM v[32:35],a[232:235],v[192:195],v[32:35]
EX v15,v15
MM v[32:35],a[236:239],v[208:211],v[32:35]
TR a[172:173],v233 offset:25600
CV v11,v14,v15
MM v[36:39],a[252:255],v[208:211],v[128:131]
EX v16,v16
MM v[36:39],a[248:251],v[192:195],v[36:39]
TR a[174:175],v233 offset:29696
TR a[176:177],v232 offset:17920
MM v[36:39],a[244:247],v[176:179],v[36:39]
EX v17,v17
MM v[36:39],a[240:243],v[160:163],v[36:39]
TR a[178:179],v232 offset:22016
CV v16,v16,v17
MM v[44:47],a[240:243],v[164:167],v[132:135]
EX v18,v18
MM v[44:47],a[244:247],v[180:183],v[44:47]
EX v19,v19
MM v[44:47],a[248:251],v[196:199],v[44:47]
TR a[180:181],v232 offset:26112
CV v17,v18,v19
MM v[44:47],a[252:255],v[212:215],v[44:47]
EX v20,v20
MM v[40:43],a[236:239],v[212:215],v[132:135]
TR a[182:183],v232 offset:30208
TR a[184:185],v233 offset:17920
MM v[40:43],a[232:235],v[196:199],v[40:43]
EX v21,v21
MM v[40:43],a[228:231],v[180:183],v[40:43]
TR a[186:187],v233 offset:22016
CV v18,v20,v21
MM v[40:43],a[224:227],v[164:167],v[40:43]
EX v22,v22
MM v[48:51],a[224:227],v[168:171],v[136:139]
TR a[188:189],v233 offset:26112
TR a[190:191],v233 offset:30208
MM v[48:51],a[228:231],v[184:187],v[48:51]
EX v23,v23
MM v[48:51],a[232:235],v[200:203],v[48:51]
CV v19,v22,v23
MM v[48:51],a[236:239],v[216:219],v[48:51]
EX v24,v24
MM v[52:55],a[252:255],v[216:219],v[136:139]
EX v25,v25
MM v[52:55],a[248:251],v[200:203],v[52:55]
EX v26,v26
MM v[52:55],a[244:247],v[184:187],v[52:55]
EX v27,v27
MM v[52:55],a[240:243],v[168:171],v[52:55]
EX v28,v28
MM v[60:63],a[240:243],v[172:175],v[140:143]
EX v29,v29
MM v[60:63],a[244:247],v[188:191],v[60:63]
EX v30,v30
MM v[60:63],a[248:251],v[204:207],v[60:63]
EX v31,v31
MM v[60:63],a[252:255],v[220:223],v[60:63]
s_nop 0
EX v32,v32
MM v[56:59],a[236:239],v[220:223],v[140:143]
s_nop 0
EX v33,v33
MM v[56:59],a[232:235],v[204:207],v[56:59]
s_nop 0
EX v34,v34
MM v[56:59],a[228:231],v[188:191],v[56:59]
LPG16_%=:
s_cbranch_execz LTR2_%=
EX v35,v35
MM v[56:59],a[224:227],v[172:175],v[56:59]
s_waitcnt vmcnt(16) lgkmcnt(0)
s_barrier
MM a[0:3],a[128:131],v[64:67],a[0:3]
s_waitcnt vmcnt(0)
v_cmp_ne_u32 s[90:91], s69, v252
CV v24,v24,v25
MM a[0:3],a[132:135],v[68:71],a[0:3]
s_add_u32 m0, s65, 16384
CV v25,v26,v27
CV v26,v28,v29
MM a[8:11],a[132:135],v[76:79],a[8:11]
s_cmp_lg_u64 s[90:91], 0
s_cbranch_scc1 LRFH_%=
LRFHb_%=:
v_lshrrev_b32 v253, 7, v248
MM a[8:11],a[128:131],v[72:75],a[8:11]
v_mul_u32_u24 v253, 3328, v253
v_add_u32 v253, v253, v248
MM a[16:19],a[128:131],v[80:83],a[16:19]
v_lshrrev_b32 v252, 6, v248
v_mul_u32_u24 v252, 3392, v252
MM a[16:19],a[132:135],v[84:87],a[16:19]
RD a[192:195],v224 offset:49152
v_add_u32 v252, v252, v248
MM a[24:27],a[132:135],v[92:95],a[24:27]
RD a[196:199],v225 offset:49152
CV v27,v30,v31
MM a[24:27],a[128:131],v[88:91],a[24:27]
buffer_load_dwordx4 v228, s[8:11], s64 offen offset:0 lds
MM a[28:31],a[136:139],v[88:91],a[28:31]
s_add_u32 s76, s76, 21557248
RD a[200:203],v226 offset:49152
CV v4,v32,v33
MM a[28:31],a[140:143],v[92:95],a[28:31]
s_lshl_b32 s90, %[swave], 11
CV v5,v34,v35
MM a[20:23],a[140:143],v[84:87],a[20:23]
buffer_load_dwordx4 v229, s[8:11], s64 offen offset:1024 lds
MM a[20:23],a[136:139],v[80:83],a[20:23]
s_add_u32 s90, s90, %[slds]
s_add_u32 m0, s90, 139264
s_nop 0
buffer_load_dwordx4 v253, s[20:23], s76 offen offset:0 sc1 lds
MM a[12:15],a[136:139],v[72:75],a[12:15]
s_add_u32 m0, s65, 16384
EX v36,v36
MM a[12:15],a[140:143],v[76:79],a[12:15]
s_add_u32 s76, s76, 128
s_add_u32 m0, s90, 140288
s_mov_b32 exec_hi, 0
buffer_load_dwordx4 v252, s[20:23], s76 offen offset:0 sc1 lds
s_mov_b64 exec, -1
MM a[4:7],a[140:143],v[68:71],a[4:7]
EX v37,v37
MM a[4:7],a[136:139],v[64:67],a[4:7]
RD a[204:207],v227 offset:49152
CV v6,v36,v37
MM a[32:35],a[144:147],v[64:67],a[32:35]
EX v38,v38
MM a[32:35],a[148:151],v[68:71],a[32:35]
EX v39,v39
MM a[40:43],a[148:151],v[76:79],a[40:43]
RD a[208:211],v224 offset:53248
CV v7,v38,v39
MM a[40:43],a[144:147],v[72:75],a[40:43]
EX v40,v40
MM a[48:51],a[144:147],v[80:83],a[48:51]
s_add_u32 m0, s65, 16384
s_nop 0
buffer_load_dwordx4 v230, s[8:11], s64 offen offset:2048 lds
MM a[48:51],a[148:151],v[84:87],a[48:51]
EX v41,v41
MM a[56:59],a[148:151],v[92:95],a[56:59]
RD a[212:215],v225 offset:53248
CV v12,v40,v41
MM a[56:59],a[144:147],v[88:91],a[56:59]
EX v42,v42
MM a[60:63],a[152:155],v[88:91],a[60:63]
EX v43,v43
MM a[60:63],a[156:159],v[92:95],a[60:63]
RD a[216:219],v226 offset:53248
CV v13,v42,v43
MM a[52:55],a[156:159],v[84:87],a[52:55]
EX v44,v44
MM a[52:55],a[152:155],v[80:83],a[52:55]
buffer_load_dwordx4 v231, s[8:11], s64 offen offset:3072 lds
MM a[44:47],a[152:155],v[72:75],a[44:47]
s_add_u32 m0, s65, 81920
EX v45,v45
MM a[44:47],a[156:159],v[76:79],a[44:47]
RD a[220:223],v227 offset:53248
CV v14,v44,v45
MM a[36:39],a[156:159],v[68:71],a[36:39]
EX v46,v46
MM a[36:39],a[152:155],v[64:67],a[36:39]
RD a[224:227],v224 offset:57344
RD a[228:231],v225 offset:57344
MM a[64:67],a[160:163],v[64:67],a[64:67]
EX v47,v47
MM a[64:67],a[164:167],v[68:71],a[64:67]
RD a[232:235],v226 offset:57344
CV v15,v46,v47
MM a[72:75],a[164:167],v[76:79],a[72:75]
EX v48,v48
MM a[72:75],a[160:163],v[72:75],a[72:75]
buffer_load_dwordx4 v235, s[12:15], s81 offen offset:0 lds
MM a[80:83],a[160:163],v[80:83],a[80:83]
EX v49,v49
MM a[80:83],a[164:167],v[84:87],a[80:83]
RD a[236:239],v227 offset:57344
CV v20,v48,v49
MM a[88:91],a[164:167],v[92:95],a[88:91]
EX v50,v50
MM a[88:91],a[160:163],v[88:91],a[88:91]
EX v51,v51
MM a[92:95],a[168:171],v[88:91],a[92:95]
RD a[240:243],v224 offset:61440
CV v21,v50,v51
MM a[92:95],a[172:175],v[92:95],a[92:95]
EX v52,v52
MM a[84:87],a[172:175],v[84:87],a[84:87]
buffer_load_dwordx4 v235, s[12:15], s64 offen offset:1024 lds
MM a[84:87],a[168:171],v[80:83],a[84:87]
EX v53,v53
MM a[76:79],a[168:171],v[72:75],a[76:79]
RD a[244:247],v225 offset:61440
CV v22,v52,v53
MM a[76:79],a[172:175],v[76:79],a[76:79]
EX v54,v54
MM a[68:71],a[172:175],v[68:71],a[68:71]
EX v55,v55
MM a[68:71],a[168:171],v[64:67],a[68:71]
RD a[248:251],v226 offset:61440
CV v23,v54,v55
MM a[96:99],a[176:179],v[64:67],a[96:99]
EX v56,v56
MM a[96:99],a[180:183],v[68:71],a[96:99]
buffer_load_dwordx4 v235, s[12:15], s81 offen offset:2048 lds
MM a[104:107],a[180:183],v[76:79],a[104:107]
EX v57,v57
MM a[104:107],a[176:179],v[72:75],a[104:107]
RD a[252:255],v227 offset:61440
CV v28,v56,v57
MM a[112:115],a[176:179],v[80:83],a[112:115]
EX v58,v58
MM a[112:115],a[180:183],v[84:87],a[112:115]
EX v59,v59
MM a[120:123],a[180:183],v[92:95],a[120:123]
buffer_load_dwordx4 v235, s[12:15], s64 offen offset:3072 lds
MM a[120:123],a[176:179],v[88:91],a[120:123]
EX v60,v60
MM a[124:127],a[184:187],v[88:91],a[124:127]
EX v61,v61
MM a[124:127],a[188:191],v[92:95],a[124:127]
EX v62,v62
MM a[116:119],a[188:191],v[84:87],a[116:119]
EX v63,v63
MM a[116:119],a[184:187],v[80:83],a[116:119]
CV v29,v58,v59
CV v30,v60,v61
MM a[108:111],a[184:187],v[72:75],a[108:111]
CV v31,v62,v63
MM a[108:111],a[188:191],v[76:79],a[108:111]
MM a[100:103],a[188:191],v[68:71],a[100:103]
MM a[100:103],a[184:187],v[64:67],a[100:103]
v_smfmac_f32_16x16x64_bf16 v[236:239], v[144:147], v[0:7], v234
v_smfmac_f32_16x16x64_bf16 v[236:239], v[148:151], v[8:15], v234
v_smfmac_f32_16x16x64_bf16 v[236:239], v[152:155], v[16:23], v234
v_smfmac_f32_16x16x64_bf16 v[236:239], v[156:159], v[24:31], v234
s_waitcnt lgkmcnt(0)
MM v[64:67],a[192:195],v[160:163],v[128:131]
s_add_u32 s64, s64, 16384
TR a[128:129],v232 offset:32768
s_add_u32 s81, s64, 896
MM v[64:67],a[196:199],v[176:179],v[64:67]
TR a[130:131],v232 offset:36864
TR a[132:133],v232 offset:40960
MM v[64:67],a[200:203],v[192:195],v[64:67]
TR a[134:135],v232 offset:45056
TR a[136:137],v233 offset:32768
MM v[64:67],a[204:207],v[208:211],v[64:67]
TR a[138:139],v233 offset:36864
v_add_f32 v246, v236, v237
MM v[68:71],a[220:223],v[208:211],v[128:131]
TR a[140:141],v233 offset:40960
v_cmp_nge_f32 vcc, s71, v246
MM v[68:71],a[216:219],v[192:195],v[68:71]
s_cbranch_vccnz LSITE26_%=
LBACK26_%=:
MM v[68:71],a[212:215],v[176:179],v[68:71]
s_add_u32 m0, s94, 131072
s_add_u32 s66, s85, 0
buffer_load_dwordx4 v241, s[24:27], s66 offen offset:0 lds
MM v[68:71],a[208:211],v[160:163],v[68:71]
TR a[142:143],v233 offset:45056
TR a[144:145],v232 offset:33280
MM v[76:79],a[208:211],v[164:167],v[132:135]
EX v64,v64
MM v[76:79],a[212:215],v[180:183],v[76:79]
EX v65,v65
MM v[76:79],a[216:219],v[196:199],v[76:79]
TR a[146:147],v232 offset:37376
CV v64,v64,v65
MM v[76:79],a[220:223],v[212:215],v[76:79]
EX v66,v66
MM v[72:75],a[204:207],v[212:215],v[132:135]
EX v67,v67
MM v[72:75],a[200:203],v[196:199],v[72:75]
TR a[148:149],v232 offset:41472
CV v65,v66,v67
MM v[72:75],a[196:199],v[180:183],v[72:75]
s_add_u32 m0, s94, 135168
s_add_u32 s66, s85, 81920
buffer_load_dwordx4 v241, s[24:27], s66 offen offset:0 lds
MM v[72:75],a[192:195],v[164:167],v[72:75]
EX v68,v68
MM v[80:83],a[192:195],v[168:171],v[136:139]
EX v69,v69
MM v[80:83],a[196:199],v[184:187],v[80:83]
TR a[150:151],v232 offset:45568
CV v66,v68,v69
MM v[80:83],a[200:203],v[200:203],v[80:83]
EX v70,v70
MM v[80:83],a[204:207],v[216:219],v[80:83]
TR a[152:153],v233 offset:33280
TR a[154:155],v233 offset:37376
MM v[84:87],a[220:223],v[216:219],v[136:139]
EX v71,v71
MM v[84:87],a[216:219],v[200:203],v[84:87]
TR a[156:157],v233 offset:41472
CV v67,v70,v71
MM v[84:87],a[212:215],v[184:187],v[84:87]
s_add_u32 m0, s94, 147456
s_add_u32 s66, s87, 0
buffer_load_dwordx4 v241, s[24:27], s66 offen offset:0 lds
MM v[84:87],a[208:211],v[168:171],v[84:87]
EX v72,v72
MM v[92:95],a[208:211],v[172:175],v[140:143]
EX v73,v73
MM v[92:95],a[212:215],v[188:191],v[92:95]
TR a[158:159],v233 offset:45568
CV v72,v72,v73
MM v[92:95],a[216:219],v[204:207],v[92:95]
EX v74,v74
MM v[92:95],a[220:223],v[220:223],v[92:95]
TR a[160:161],v232 offset:33792
TR a[162:163],v232 offset:37888
MM v[88:91],a[204:207],v[220:223],v[140:143]
EX v75,v75
MM v[88:91],a[200:203],v[204:207],v[88:91]
TR a[164:165],v232 offset:41984
CV v73,v74,v75
MM v[88:91],a[196:199],v[188:191],v[88:91]
EX v76,v76
MM v[88:91],a[192:195],v[172:175],v[88:91]
EX v77,v77
MM v[96:99],a[224:227],v[160:163],v[128:131]
TR a[166:167],v232 offset:46080
CV v74,v76,v77
MM v[96:99],a[228:231],v[176:179],v[96:99]
EX v78,v78
MM v[96:99],a[232:235],v[192:195],v[96:99]
TR a[168:169],v233 offset:33792
TR a[170:171],v233 offset:37888
MM v[96:99],a[236:239],v[208:211],v[96:99]
EX v79,v79
MM v[100:103],a[252:255],v[208:211],v[128:131]
TR a[172:173],v233 offset:41984
CV v75,v78,v79
MM v[100:103],a[248:251],v[192:195],v[100:103]
EX v80,v80
MM v[100:103],a[244:247],v[176:179],v[100:103]
TR a[174:175],v233 offset:46080
TR a[176:177],v232 offset:34304
MM v[100:103],a[240:243],v[160:163],v[100:103]
EX v81,v81
MM v[108:111],a[240:243],v[164:167],v[132:135]
TR a[178:179],v232 offset:38400
CV v80,v80,v81
MM v[108:111],a[244:247],v[180:183],v[108:111]
EX v82,v82
MM v[108:111],a[248:251],v[196:199],v[108:111]
EX v83,v83
MM v[108:111],a[252:255],v[212:215],v[108:111]
TR a[180:181],v232 offset:42496
CV v81,v82,v83
MM v[104:107],a[236:239],v[212:215],v[132:135]
EX v84,v84
MM v[104:107],a[232:235],v[196:199],v[104:107]
TR a[182:183],v232 offset:46592
TR a[184:185],v233 offset:34304
MM v[104:107],a[228:231],v[180:183],v[104:107]
EX v85,v85
MM v[104:107],a[224:227],v[164:167],v[104:107]
TR a[186:187],v233 offset:38400
CV v82,v84,v85
MM v[112:115],a[224:227],v[168:171],v[136:139]
EX v86,v86
MM v[112:115],a[228:231],v[184:187],v[112:115]
EX v87,v87
MM v[112:115],a[232:235],v[200:203],v[112:115]
TR a[188:189],v233 offset:42496
CV v83,v86,v87
MM v[112:115],a[236:239],v[216:219],v[112:115]
EX v88,v88
MM v[116:119],a[252:255],v[216:219],v[136:139]
EX v89,v89
MM v[116:119],a[248:251],v[200:203],v[116:119]
)ASM"
R"ASM(
TR a[190:191],v233 offset:46592
CV v88,v88,v89
MM v[116:119],a[244:247],v[184:187],v[116:119]
EX v90,v90
MM v[116:119],a[240:243],v[168:171],v[116:119]
EX v91,v91
MM v[124:127],a[240:243],v[172:175],v[140:143]
EX v92,v92
MM v[124:127],a[244:247],v[188:191],v[124:127]
EX v93,v93
MM v[124:127],a[248:251],v[204:207],v[124:127]
EX v94,v94
MM v[124:127],a[252:255],v[220:223],v[124:127]
EX v95,v95
MM v[120:123],a[236:239],v[220:223],v[140:143]
EX v96,v96
MM v[120:123],a[232:235],v[204:207],v[120:123]
EX v97,v97
MM v[120:123],a[228:231],v[188:191],v[120:123]
s_getpc_b64 s[98:99]
LTA3_%=:
s_lshl_b32 s73, %[swave], 12
s_add_u32 s73, s73, LPG17_%=-LTA3_%=
s_add_u32 s98, s98, s73
s_addc_u32 s99, s99, 0
s_mov_b64 exec, 0
s_setpc_b64 s[98:99]
LTR3_%=:
s_mov_b64 exec, -1
s_nop 0
CV v89,v90,v91
MM v[120:123],a[224:227],v[172:175],v[120:123]
s_waitcnt lgkmcnt(0)
s_barrier
MM a[0:3],a[128:131],v[0:3],a[0:3]
s_add_u32 m0, s65, 32768
RD a[192:195],v224 offset:0
CV v90,v92,v93
MM a[0:3],a[132:135],v[4:7],a[0:3]
RD a[196:199],v225 offset:0
CV v91,v94,v95
MM a[8:11],a[132:135],v[12:15],a[8:11]
buffer_load_dwordx4 v228, s[8:11], s64 offen offset:0 lds
MM a[8:11],a[128:131],v[8:11],a[8:11]
RD a[200:203],v226 offset:0
CV v68,v96,v97
MM a[16:19],a[128:131],v[16:19],a[16:19]
buffer_load_dwordx4 v229, s[8:11], s64 offen offset:1024 lds
MM a[16:19],a[132:135],v[20:23],a[16:19]
RD a[204:207],v227 offset:0
RD a[208:211],v224 offset:4096
MM a[24:27],a[132:135],v[28:31],a[24:27]
EX v98,v98
MM a[24:27],a[128:131],v[24:27],a[24:27]
EX v99,v99
MM a[28:31],a[136:139],v[24:27],a[28:31]
RD a[212:215],v225 offset:4096
CV v69,v98,v99
MM a[28:31],a[140:143],v[28:31],a[28:31]
EX v100,v100
MM a[20:23],a[140:143],v[20:23],a[20:23]
buffer_load_dwordx4 v230, s[8:11], s64 offen offset:2048 lds
MM a[20:23],a[136:139],v[16:19],a[20:23]
EX v101,v101
MM a[12:15],a[136:139],v[8:11],a[12:15]
RD a[216:219],v226 offset:4096
CV v70,v100,v101
MM a[12:15],a[140:143],v[12:15],a[12:15]
EX v102,v102
MM a[4:7],a[140:143],v[4:7],a[4:7]
EX v103,v103
MM a[4:7],a[136:139],v[0:3],a[4:7]
RD a[220:223],v227 offset:4096
CV v71,v102,v103
MM a[32:35],a[144:147],v[0:3],a[32:35]
EX v104,v104
MM a[32:35],a[148:151],v[4:7],a[32:35]
buffer_load_dwordx4 v231, s[8:11], s64 offen offset:3072 lds
MM a[40:43],a[148:151],v[12:15],a[40:43]
s_add_u32 m0, s65, 98304
EX v105,v105
MM a[40:43],a[144:147],v[8:11],a[40:43]
RD a[224:227],v224 offset:8192
CV v76,v104,v105
MM a[48:51],a[144:147],v[16:19],a[48:51]
EX v106,v106
MM a[48:51],a[148:151],v[20:23],a[48:51]
EX v107,v107
MM a[56:59],a[148:151],v[28:31],a[56:59]
RD a[228:231],v225 offset:8192
CV v77,v106,v107
MM a[56:59],a[144:147],v[24:27],a[56:59]
EX v108,v108
MM a[60:63],a[152:155],v[24:27],a[60:63]
buffer_load_dwordx4 v235, s[12:15], s81 offen offset:0 lds
MM a[60:63],a[156:159],v[28:31],a[60:63]
EX v109,v109
MM a[52:55],a[156:159],v[20:23],a[52:55]
RD a[232:235],v226 offset:8192
CV v78,v108,v109
MM a[52:55],a[152:155],v[16:19],a[52:55]
EX v110,v110
MM a[44:47],a[152:155],v[8:11],a[44:47]
EX v111,v111
MM a[44:47],a[156:159],v[12:15],a[44:47]
RD a[236:239],v227 offset:8192
CV v79,v110,v111
MM a[36:39],a[156:159],v[4:7],a[36:39]
buffer_load_dwordx4 v235, s[12:15], s64 offen offset:1024 lds
MM a[36:39],a[152:155],v[0:3],a[36:39]
EX v112,v112
MM a[64:67],a[160:163],v[0:3],a[64:67]
EX v113,v113
MM a[64:67],a[164:167],v[4:7],a[64:67]
RD a[240:243],v224 offset:12288
CV v84,v112,v113
MM a[72:75],a[164:167],v[12:15],a[72:75]
EX v114,v114
MM a[72:75],a[160:163],v[8:11],a[72:75]
RD a[244:247],v225 offset:12288
RD a[248:251],v226 offset:12288
MM a[80:83],a[160:163],v[16:19],a[80:83]
EX v115,v115
MM a[80:83],a[164:167],v[20:23],a[80:83]
RD a[252:255],v227 offset:12288
CV v85,v114,v115
MM a[88:91],a[164:167],v[28:31],a[88:91]
EX v116,v116
MM a[88:91],a[160:163],v[24:27],a[88:91]
buffer_load_dwordx4 v235, s[12:15], s81 offen offset:2048 lds
MM a[92:95],a[168:171],v[24:27],a[92:95]
EX v117,v117
MM a[92:95],a[172:175],v[28:31],a[92:95]
EX v118,v118
MM a[84:87],a[172:175],v[20:23],a[84:87]
buffer_load_dwordx4 v235, s[12:15], s64 offen offset:3072 lds
MM a[84:87],a[168:171],v[16:19],a[84:87]
EX v119,v119
MM a[76:79],a[168:171],v[8:11],a[76:79]
EX v120,v120
MM a[76:79],a[172:175],v[12:15],a[76:79]
EX v121,v121
MM a[68:71],a[172:175],v[4:7],a[68:71]
EX v122,v122
MM a[68:71],a[168:171],v[0:3],a[68:71]
EX v123,v123
MM a[96:99],a[176:179],v[0:3],a[96:99]
EX v124,v124
MM a[96:99],a[180:183],v[4:7],a[96:99]
EX v125,v125
MM a[104:107],a[180:183],v[12:15],a[104:107]
EX v126,v126
MM a[104:107],a[176:179],v[8:11],a[104:107]
EX v127,v127
MM a[112:115],a[176:179],v[16:19],a[112:115]
CV v86,v116,v117
CV v87,v118,v119
MM a[112:115],a[180:183],v[20:23],a[112:115]
CV v92,v120,v121
CV v93,v122,v123
MM a[120:123],a[180:183],v[28:31],a[120:123]
CV v94,v124,v125
CV v95,v126,v127
MM a[120:123],a[176:179],v[24:27],a[120:123]
MM a[124:127],a[184:187],v[24:27],a[124:127]
MM a[124:127],a[188:191],v[28:31],a[124:127]
MM a[116:119],a[188:191],v[20:23],a[116:119]
MM a[116:119],a[184:187],v[16:19],a[116:119]
MM a[108:111],a[184:187],v[8:11],a[108:111]
MM a[108:111],a[188:191],v[12:15],a[108:111]
MM a[100:103],a[188:191],v[4:7],a[100:103]
MM a[100:103],a[184:187],v[0:3],a[100:103]
v_smfmac_f32_16x16x64_bf16 v[236:239], v[144:147], v[64:71], v234
v_smfmac_f32_16x16x64_bf16 v[236:239], v[148:151], v[72:79], v234
v_smfmac_f32_16x16x64_bf16 v[236:239], v[152:155], v[80:87], v234
v_smfmac_f32_16x16x64_bf16 v[236:239], v[156:159], v[88:95], v234
s_waitcnt lgkmcnt(0)
MM v[0:3],a[192:195],v[160:163],v[128:131]
s_add_u32 s64, s64, 16384
TR a[128:129],v232 offset:49152
s_add_u32 s81, s64, 896
MM v[0:3],a[196:199],v[176:179],v[0:3]
TR a[130:131],v232 offset:53248
TR a[132:133],v232 offset:57344
MM v[0:3],a[200:203],v[192:195],v[0:3]
TR a[134:135],v232 offset:61440
TR a[136:137],v233 offset:49152
MM v[0:3],a[204:207],v[208:211],v[0:3]
TR a[138:139],v233 offset:53248
v_add_f32 v247, v236, v237
MM v[4:7],a[220:223],v[208:211],v[128:131]
TR a[140:141],v233 offset:57344
v_cmp_nge_f32 vcc, s71, v247
MM v[4:7],a[216:219],v[192:195],v[4:7]
s_cbranch_vccnz LSITE27_%=
LBACK27_%=:
MM v[4:7],a[212:215],v[176:179],v[4:7]
v_lshrrev_b32 v125, 8, v248
v_lshlrev_b32 v124, 10, v125
MM v[4:7],a[208:211],v[160:163],v[4:7]
v_bfe_u32 v126, v248, 6, 2
v_lshl_add_u32 v124, v126, 7, v124
MM v[12:15],a[208:211],v[164:167],v[132:135]
v_bfe_u32 v126, v248, 4, 2
v_lshl_add_u32 v124, v126, 3, v124
MM v[12:15],a[212:215],v[180:183],v[12:15]
s_lshl_b32 s91, %[swave], 5
EX v0,v0
MM v[12:15],a[216:219],v[196:199],v[12:15]
s_add_u32 s91, s91, %[slds]
EX v1,v1
MM v[12:15],a[220:223],v[212:215],v[12:15]
s_add_u32 s91, s91, 151552
v_add_u32 v124, s91, v124
v_and_b32 v126, 112, v248
MM v[8:11],a[204:207],v[212:215],v[132:135]
s_lshl_b32 s91, %[swave], 11
EX v2,v2
MM v[8:11],a[200:203],v[196:199],v[8:11]
s_add_u32 s91, s91, %[slds]
TR a[142:143],v233 offset:61440
CV v0,v0,v1
MM v[8:11],a[196:199],v[180:183],v[8:11]
s_add_u32 s91, s91, 139264
v_lshl_add_u32 v127, v125, 4, s91
v_lshl_add_u32 v127, v126, 2, v127
MM v[8:11],a[192:195],v[164:167],v[8:11]
TR a[144:145],v232 offset:49664
v_lshl_add_u32 v126, v126, 2, v127
MM v[16:19],a[192:195],v[168:171],v[136:139]
s_waitcnt vmcnt(17)
TR v[96:97],v124 offset:0
TR v[98:99],v124 offset:512
MM v[16:19],a[196:199],v[184:187],v[16:19]
RD v[108:111],v126 offset:0
TR v[100:101],v124 offset:4096
MM v[16:19],a[200:203],v[200:203],v[16:19]
TR v[102:103],v124 offset:4608
RD v[112:115],v126 offset:64
MM v[16:19],a[204:207],v[216:219],v[16:19]
TR v[104:105],v124 offset:8192
TR v[106:107],v124 offset:8704
EX v3,v3
MM v[20:23],a[220:223],v[216:219],v[136:139]
RD v[116:119],v127 offset:1024
v_bfe_u32 v127, v248, 4, 3
EX v4,v4
MM v[20:23],a[216:219],v[200:203],v[20:23]
s_nop 0
v_mul_u32_u24 v127, 2560, v127
v_lshl_add_u32 v127, v125, 4, v127
EX v5,v5
MM v[20:23],a[212:215],v[184:187],v[20:23]
s_lshl_b32 s90, %[swave], 6
TR a[146:147],v232 offset:53760
TR a[148:149],v232 offset:57856
MM v[20:23],a[208:211],v[168:171],v[20:23]
s_lshl_b32 s91, s47, 8
EX v6,v6
MM v[28:31],a[208:211],v[172:175],v[140:143]
s_add_u32 s90, s90, s91
EX v7,v7
MM v[28:31],a[212:215],v[188:191],v[28:31]
s_mul_i32 s91, s46, 17920
TR a[150:151],v232 offset:61952
CV v1,v2,v3
MM v[28:31],a[216:219],v[204:207],v[28:31]
s_add_u32 s90, s90, s91
EX v8,v8
MM v[28:31],a[220:223],v[220:223],v[28:31]
s_add_u32 s90, s90, 21659648
TR a[152:153],v233 offset:49664
CV v2,v4,v5
MM v[24:27],a[204:207],v[220:223],v[140:143]
s_nop 0
EX v9,v9
MM v[24:27],a[200:203],v[204:207],v[24:27]
s_nop 0
TR a[154:155],v233 offset:53760
TR a[156:157],v233 offset:57856
MM v[24:27],a[196:199],v[188:191],v[24:27]
s_nop 0
EX v10,v10
MM v[24:27],a[192:195],v[172:175],v[24:27]
LPG17_%=:
s_cbranch_execz LPG21_%=
EX v11,v11
MM v[32:35],a[224:227],v[160:163],v[128:131]
TR a[158:159],v233 offset:61952
CV v3,v6,v7
MM v[32:35],a[228:231],v[176:179],v[32:35]
EX v12,v12
MM v[32:35],a[232:235],v[192:195],v[32:35]
TR a[160:161],v232 offset:50176
CV v8,v8,v9
MM v[32:35],a[236:239],v[208:211],v[32:35]
s_waitcnt lgkmcnt(8)
EX v13,v13
MM v[36:39],a[252:255],v[208:211],v[128:131]
TR a[162:163],v232 offset:54272
TR a[164:165],v232 offset:58368
MM v[120:123],v[96:99],v[108:111],0
MM v[120:123],v[100:103],v[112:115],v[120:123]
MM v[120:123],v[104:107],v[116:119],v[120:123]
MM v[36:39],a[248:251],v[192:195],v[36:39]
EX v14,v14
MM v[36:39],a[244:247],v[176:179],v[36:39]
EX v15,v15
MM v[36:39],a[240:243],v[160:163],v[36:39]
TR a[166:167],v232 offset:62464
CV v9,v10,v11
MM v[44:47],a[240:243],v[164:167],v[132:135]
TR a[168:169],v233 offset:50176
CV v10,v12,v13
MM v[44:47],a[244:247],v[180:183],v[44:47]
EX v16,v16
MM v[44:47],a[248:251],v[196:199],v[44:47]
EX v17,v17
MM v[44:47],a[252:255],v[212:215],v[44:47]
s_mov_b32 exec_lo, 0x00ff00ff
s_mov_b32 exec_hi, 0x00ff00ff
buffer_store_dwordx4 v[120:123], v127, s[20:23], s90 offen offset:0 sc1
s_mov_b64 exec, -1
s_nop 0
MM v[40:43],a[236:239],v[212:215],v[132:135]
TR a[170:171],v233 offset:54272
TR a[172:173],v233 offset:58368
MM v[40:43],a[232:235],v[196:199],v[40:43]
EX v18,v18
MM v[40:43],a[228:231],v[180:183],v[40:43]
EX v19,v19
MM v[40:43],a[224:227],v[164:167],v[40:43]
TR a[174:175],v233 offset:62464
CV v11,v14,v15
MM v[48:51],a[224:227],v[168:171],v[136:139]
EX v20,v20
MM v[48:51],a[228:231],v[184:187],v[48:51]
TR a[176:177],v232 offset:50688
TR a[178:179],v232 offset:54784
MM v[48:51],a[232:235],v[200:203],v[48:51]
EX v21,v21
MM v[48:51],a[236:239],v[216:219],v[48:51]
TR a[180:181],v232 offset:58880
CV v16,v16,v17
MM v[52:55],a[252:255],v[216:219],v[136:139]
EX v22,v22
MM v[52:55],a[248:251],v[200:203],v[52:55]
TR a[182:183],v232 offset:62976
CV v17,v18,v19
MM v[52:55],a[244:247],v[184:187],v[52:55]
EX v23,v23
MM v[52:55],a[240:243],v[168:171],v[52:55]
TR a[184:185],v233 offset:50688
CV v18,v20,v21
MM v[60:63],a[240:243],v[172:175],v[140:143]
EX v24,v24
MM v[60:63],a[244:247],v[188:191],v[60:63]
TR a[186:187],v233 offset:54784
TR a[188:189],v233 offset:58880
MM v[60:63],a[248:251],v[204:207],v[60:63]
TR a[190:191],v233 offset:62976
CV v19,v22,v23
MM v[60:63],a[252:255],v[220:223],v[60:63]
EX v25,v25
MM v[56:59],a[236:239],v[220:223],v[140:143]
EX v26,v26
MM v[56:59],a[232:235],v[204:207],v[56:59]
EX v27,v27
MM v[56:59],a[228:231],v[188:191],v[56:59]
EX v28,v28
MM v[56:59],a[224:227],v[172:175],v[56:59]
s_waitcnt vmcnt(16) lgkmcnt(0)
s_barrier
MM a[0:3],a[128:131],v[64:67],a[0:3]
s_add_u32 m0, s65, 49152
CV v24,v24,v25
CV v25,v26,v27
MM a[0:3],a[132:135],v[68:71],a[0:3]
EX v29,v29
MM a[8:11],a[132:135],v[76:79],a[8:11]
EX v30,v30
MM a[8:11],a[128:131],v[72:75],a[8:11]
EX v31,v31
MM a[16:19],a[128:131],v[80:83],a[16:19]
RD a[192:195],v224 offset:16384
CV v26,v28,v29
MM a[16:19],a[132:135],v[84:87],a[16:19]
EX v32,v32
MM a[24:27],a[132:135],v[92:95],a[24:27]
RD a[196:199],v225 offset:16384
CV v27,v30,v31
MM a[24:27],a[128:131],v[88:91],a[24:27]
EX v33,v33
MM a[28:31],a[136:139],v[88:91],a[28:31]
buffer_load_dwordx4 v228, s[8:11], s64 offen offset:0 lds
MM a[28:31],a[140:143],v[92:95],a[28:31]
EX v34,v34
MM a[20:23],a[140:143],v[84:87],a[20:23]
)ASM"
R"ASM(
EX v35,v35
MM a[20:23],a[136:139],v[80:83],a[20:23]
RD a[200:203],v226 offset:16384
CV v4,v32,v33
MM a[12:15],a[136:139],v[72:75],a[12:15]
EX v36,v36
MM a[12:15],a[140:143],v[76:79],a[12:15]
RD a[204:207],v227 offset:16384
CV v5,v34,v35
MM a[4:7],a[140:143],v[68:71],a[4:7]
EX v37,v37
MM a[4:7],a[136:139],v[64:67],a[4:7]
buffer_load_dwordx4 v229, s[8:11], s64 offen offset:1024 lds
MM a[32:35],a[144:147],v[64:67],a[32:35]
EX v38,v38
MM a[32:35],a[148:151],v[68:71],a[32:35]
RD a[208:211],v224 offset:20480
CV v6,v36,v37
MM a[40:43],a[148:151],v[76:79],a[40:43]
EX v39,v39
MM a[40:43],a[144:147],v[72:75],a[40:43]
RD a[212:215],v225 offset:20480
CV v7,v38,v39
MM a[48:51],a[144:147],v[80:83],a[48:51]
EX v40,v40
MM a[48:51],a[148:151],v[84:87],a[48:51]
EX v41,v41
MM a[56:59],a[148:151],v[92:95],a[56:59]
buffer_load_dwordx4 v230, s[8:11], s64 offen offset:2048 lds
MM a[56:59],a[144:147],v[88:91],a[56:59]
EX v42,v42
MM a[60:63],a[152:155],v[88:91],a[60:63]
RD a[216:219],v226 offset:20480
CV v12,v40,v41
MM a[60:63],a[156:159],v[92:95],a[60:63]
EX v43,v43
MM a[52:55],a[156:159],v[84:87],a[52:55]
RD a[220:223],v227 offset:20480
CV v13,v42,v43
MM a[52:55],a[152:155],v[80:83],a[52:55]
EX v44,v44
MM a[44:47],a[152:155],v[72:75],a[44:47]
EX v45,v45
MM a[44:47],a[156:159],v[76:79],a[44:47]
buffer_load_dwordx4 v231, s[8:11], s64 offen offset:3072 lds
MM a[36:39],a[156:159],v[68:71],a[36:39]
s_add_u32 m0, s65, 114688
RD a[224:227],v224 offset:24576
CV v14,v44,v45
MM a[36:39],a[152:155],v[64:67],a[36:39]
EX v46,v46
MM a[64:67],a[160:163],v[64:67],a[64:67]
EX v47,v47
MM a[64:67],a[164:167],v[68:71],a[64:67]
RD a[228:231],v225 offset:24576
CV v15,v46,v47
MM a[72:75],a[164:167],v[76:79],a[72:75]
EX v48,v48
MM a[72:75],a[160:163],v[72:75],a[72:75]
buffer_load_dwordx4 v235, s[12:15], s81 offen offset:0 lds
MM a[80:83],a[160:163],v[80:83],a[80:83]
EX v49,v49
MM a[80:83],a[164:167],v[84:87],a[80:83]
RD a[232:235],v226 offset:24576
CV v20,v48,v49
MM a[88:91],a[164:167],v[92:95],a[88:91]
EX v50,v50
MM a[88:91],a[160:163],v[88:91],a[88:91]
EX v51,v51
MM a[92:95],a[168:171],v[88:91],a[92:95]
RD a[236:239],v227 offset:24576
CV v21,v50,v51
MM a[92:95],a[172:175],v[92:95],a[92:95]
EX v52,v52
MM a[84:87],a[172:175],v[84:87],a[84:87]
buffer_load_dwordx4 v235, s[12:15], s64 offen offset:1024 lds
MM a[84:87],a[168:171],v[80:83],a[84:87]
EX v53,v53
MM a[76:79],a[168:171],v[72:75],a[76:79]
RD a[240:243],v224 offset:28672
CV v22,v52,v53
MM a[76:79],a[172:175],v[76:79],a[76:79]
EX v54,v54
MM a[68:71],a[172:175],v[68:71],a[68:71]
EX v55,v55
MM a[68:71],a[168:171],v[64:67],a[68:71]
RD a[244:247],v225 offset:28672
CV v23,v54,v55
MM a[96:99],a[176:179],v[64:67],a[96:99]
EX v56,v56
MM a[96:99],a[180:183],v[68:71],a[96:99]
buffer_load_dwordx4 v235, s[12:15], s81 offen offset:2048 lds
MM a[104:107],a[180:183],v[76:79],a[104:107]
EX v57,v57
MM a[104:107],a[176:179],v[72:75],a[104:107]
RD a[248:251],v226 offset:28672
CV v28,v56,v57
MM a[112:115],a[176:179],v[80:83],a[112:115]
EX v58,v58
MM a[112:115],a[180:183],v[84:87],a[112:115]
EX v59,v59
MM a[120:123],a[180:183],v[92:95],a[120:123]
RD a[252:255],v227 offset:28672
CV v29,v58,v59
MM a[120:123],a[176:179],v[88:91],a[120:123]
EX v60,v60
MM a[124:127],a[184:187],v[88:91],a[124:127]
buffer_load_dwordx4 v235, s[12:15], s64 offen offset:3072 lds
MM a[124:127],a[188:191],v[92:95],a[124:127]
EX v61,v61
MM a[116:119],a[188:191],v[84:87],a[116:119]
EX v62,v62
MM a[116:119],a[184:187],v[80:83],a[116:119]
EX v63,v63
MM a[108:111],a[184:187],v[72:75],a[108:111]
CV v30,v60,v61
CV v31,v62,v63
MM a[108:111],a[188:191],v[76:79],a[108:111]
MM a[100:103],a[188:191],v[68:71],a[100:103]
MM a[100:103],a[184:187],v[64:67],a[100:103]
v_smfmac_f32_16x16x64_bf16 v[236:239], v[144:147], v[0:7], v234
v_smfmac_f32_16x16x64_bf16 v[236:239], v[148:151], v[8:15], v234
v_smfmac_f32_16x16x64_bf16 v[236:239], v[152:155], v[16:23], v234
v_smfmac_f32_16x16x64_bf16 v[236:239], v[156:159], v[24:31], v234
s_waitcnt lgkmcnt(0)
MM v[64:67],a[192:195],v[160:163],v[128:131]
s_sub_u32 s64, s64, 507904
TR a[128:129],v232 offset:0
s_add_u32 s81, s64, 896
MM v[64:67],a[196:199],v[176:179],v[64:67]
TR a[130:131],v232 offset:4096
TR a[132:133],v232 offset:8192
MM v[64:67],a[200:203],v[192:195],v[64:67]
TR a[134:135],v232 offset:12288
TR a[136:137],v233 offset:0
MM v[64:67],a[204:207],v[208:211],v[64:67]
TR a[138:139],v233 offset:4096
v_add_f32 v246, v236, v237
MM v[68:71],a[220:223],v[208:211],v[128:131]
TR a[140:141],v233 offset:8192
v_cmp_nge_f32 vcc, s71, v246
MM v[68:71],a[216:219],v[192:195],v[68:71]
s_cbranch_vccnz LSITE28_%=
LBACK28_%=:
MM v[68:71],a[212:215],v[176:179],v[68:71]
TR a[142:143],v233 offset:12288
TR a[144:145],v232 offset:512
MM v[68:71],a[208:211],v[160:163],v[68:71]
TR a[146:147],v232 offset:4608
TR a[148:149],v232 offset:8704
MM v[76:79],a[208:211],v[164:167],v[132:135]
EX v64,v64
MM v[76:79],a[212:215],v[180:183],v[76:79]
EX v65,v65
MM v[76:79],a[216:219],v[196:199],v[76:79]
TR a[150:151],v232 offset:12800
CV v64,v64,v65
MM v[76:79],a[220:223],v[212:215],v[76:79]
EX v66,v66
MM v[72:75],a[204:207],v[212:215],v[132:135]
EX v67,v67
MM v[72:75],a[200:203],v[196:199],v[72:75]
TR a[152:153],v233 offset:512
CV v65,v66,v67
MM v[72:75],a[196:199],v[180:183],v[72:75]
EX v68,v68
MM v[72:75],a[192:195],v[164:167],v[72:75]
EX v69,v69
MM v[80:83],a[192:195],v[168:171],v[136:139]
TR a[154:155],v233 offset:4608
CV v66,v68,v69
MM v[80:83],a[196:199],v[184:187],v[80:83]
EX v70,v70
MM v[80:83],a[200:203],v[200:203],v[80:83]
EX v71,v71
MM v[80:83],a[204:207],v[216:219],v[80:83]
TR a[156:157],v233 offset:8704
CV v67,v70,v71
MM v[84:87],a[220:223],v[216:219],v[136:139]
EX v72,v72
MM v[84:87],a[216:219],v[200:203],v[84:87]
TR a[158:159],v233 offset:12800
TR a[160:161],v232 offset:1024
MM v[84:87],a[212:215],v[184:187],v[84:87]
EX v73,v73
MM v[84:87],a[208:211],v[168:171],v[84:87]
TR a[162:163],v232 offset:5120
CV v72,v72,v73
MM v[92:95],a[208:211],v[172:175],v[140:143]
EX v74,v74
MM v[92:95],a[212:215],v[188:191],v[92:95]
TR a[164:165],v232 offset:9216
TR a[166:167],v232 offset:13312
MM v[92:95],a[216:219],v[204:207],v[92:95]
EX v75,v75
MM v[92:95],a[220:223],v[220:223],v[92:95]
TR a[168:169],v233 offset:1024
CV v73,v74,v75
MM v[88:91],a[204:207],v[220:223],v[140:143]
EX v76,v76
MM v[88:91],a[200:203],v[204:207],v[88:91]
EX v77,v77
MM v[88:91],a[196:199],v[188:191],v[88:91]
TR a[170:171],v233 offset:5120
CV v74,v76,v77
MM v[88:91],a[192:195],v[172:175],v[88:91]
EX v78,v78
MM v[96:99],a[224:227],v[160:163],v[128:131]
TR a[172:173],v233 offset:9216
TR a[174:175],v233 offset:13312
MM v[96:99],a[228:231],v[176:179],v[96:99]
EX v79,v79
MM v[96:99],a[232:235],v[192:195],v[96:99]
TR a[176:177],v232 offset:1536
CV v75,v78,v79
MM v[96:99],a[236:239],v[208:211],v[96:99]
EX v80,v80
MM v[100:103],a[252:255],v[208:211],v[128:131]
EX v81,v81
MM v[100:103],a[248:251],v[192:195],v[100:103]
TR a[178:179],v232 offset:5632
CV v80,v80,v81
MM v[100:103],a[244:247],v[176:179],v[100:103]
EX v82,v82
MM v[100:103],a[240:243],v[160:163],v[100:103]
TR a[180:181],v232 offset:9728
TR a[182:183],v232 offset:13824
MM v[108:111],a[240:243],v[164:167],v[132:135]
EX v83,v83
MM v[108:111],a[244:247],v[180:183],v[108:111]
TR a[184:185],v233 offset:1536
CV v81,v82,v83
MM v[108:111],a[248:251],v[196:199],v[108:111]
EX v84,v84
MM v[108:111],a[252:255],v[212:215],v[108:111]
EX v85,v85
MM v[104:107],a[236:239],v[212:215],v[132:135]
TR a[186:187],v233 offset:5632
CV v82,v84,v85
MM v[104:107],a[232:235],v[196:199],v[104:107]
EX v86,v86
MM v[104:107],a[228:231],v[180:183],v[104:107]
TR a[188:189],v233 offset:9728
TR a[190:191],v233 offset:13824
MM v[104:107],a[224:227],v[164:167],v[104:107]
EX v87,v87
MM v[112:115],a[224:227],v[168:171],v[136:139]
CV v83,v86,v87
MM v[112:115],a[228:231],v[184:187],v[112:115]
EX v88,v88
MM v[112:115],a[232:235],v[200:203],v[112:115]
EX v89,v89
MM v[112:115],a[236:239],v[216:219],v[112:115]
EX v90,v90
MM v[116:119],a[252:255],v[216:219],v[136:139]
EX v91,v91
MM v[116:119],a[248:251],v[200:203],v[116:119]
EX v92,v92
MM v[116:119],a[244:247],v[184:187],v[116:119]
EX v93,v93
MM v[116:119],a[240:243],v[168:171],v[116:119]
EX v94,v94
MM v[124:127],a[240:243],v[172:175],v[140:143]
EX v95,v95
MM v[124:127],a[244:247],v[188:191],v[124:127]
EX v96,v96
MM v[124:127],a[248:251],v[204:207],v[124:127]
EX v97,v97
MM v[124:127],a[252:255],v[220:223],v[124:127]
EX v98,v98
MM v[120:123],a[236:239],v[220:223],v[140:143]
EX v99,v99
MM v[120:123],a[232:235],v[204:207],v[120:123]
EX v100,v100
MM v[120:123],a[228:231],v[188:191],v[120:123]
EX v101,v101
MM v[120:123],a[224:227],v[172:175],v[120:123]
s_waitcnt vmcnt(13) lgkmcnt(0)
s_barrier
MM a[0:3],a[128:131],v[0:3],a[0:3]
s_add_u32 m0, s65, 0
s_add_u32 m0, s94, 139264
s_add_u32 s66, s86, 0
buffer_load_dwordx4 v241, s[24:27], s66 offen offset:0 lds
MM a[0:3],a[132:135],v[4:7],a[0:3]
CV v88,v88,v89
CV v89,v90,v91
MM a[8:11],a[132:135],v[12:15],a[8:11]
RD a[192:195],v224 offset:32768
CV v90,v92,v93
MM a[8:11],a[128:131],v[8:11],a[8:11]
RD a[196:199],v225 offset:32768
CV v91,v94,v95
MM a[16:19],a[128:131],v[16:19],a[16:19]
s_add_u32 m0, s65, 0
s_nop 0
buffer_load_dwordx4 v228, s[8:11], s64 offen offset:0 lds
MM a[16:19],a[132:135],v[20:23],a[16:19]
RD a[200:203],v226 offset:32768
CV v68,v96,v97
MM a[24:27],a[132:135],v[28:31],a[24:27]
RD a[204:207],v227 offset:32768
CV v69,v98,v99
MM a[24:27],a[128:131],v[24:27],a[24:27]
buffer_load_dwordx4 v229, s[8:11], s64 offen offset:1024 lds
MM a[28:31],a[136:139],v[24:27],a[28:31]
s_add_u32 m0, s94, 143360
s_add_u32 s66, s86, 81920
buffer_load_dwordx4 v241, s[24:27], s66 offen offset:0 lds
MM a[28:31],a[140:143],v[28:31],a[28:31]
RD a[208:211],v224 offset:36864
CV v70,v100,v101
MM a[20:23],a[140:143],v[20:23],a[20:23]
s_add_u32 m0, s65, 0
s_nop 0
buffer_load_dwordx4 v230, s[8:11], s64 offen offset:2048 lds
MM a[20:23],a[136:139],v[16:19],a[20:23]
RD a[212:215],v225 offset:36864
RD a[216:219],v226 offset:36864
MM a[12:15],a[136:139],v[8:11],a[12:15]
EX v102,v102
MM a[12:15],a[140:143],v[12:15],a[12:15]
RD a[220:223],v227 offset:36864
RD a[224:227],v224 offset:40960
MM a[4:7],a[140:143],v[4:7],a[4:7]
EX v103,v103
MM a[4:7],a[136:139],v[0:3],a[4:7]
RD a[228:231],v225 offset:40960
CV v71,v102,v103
MM a[32:35],a[144:147],v[0:3],a[32:35]
EX v104,v104
MM a[32:35],a[148:151],v[4:7],a[32:35]
buffer_load_dwordx4 v231, s[8:11], s64 offen offset:3072 lds
MM a[40:43],a[148:151],v[12:15],a[40:43]
s_add_u32 m0, s65, 65536
EX v105,v105
MM a[40:43],a[144:147],v[8:11],a[40:43]
RD a[232:235],v226 offset:40960
CV v76,v104,v105
MM a[48:51],a[144:147],v[16:19],a[48:51]
EX v106,v106
MM a[48:51],a[148:151],v[20:23],a[48:51]
EX v107,v107
MM a[56:59],a[148:151],v[28:31],a[56:59]
RD a[236:239],v227 offset:40960
CV v77,v106,v107
MM a[56:59],a[144:147],v[24:27],a[56:59]
EX v108,v108
MM a[60:63],a[152:155],v[24:27],a[60:63]
buffer_load_dwordx4 v235, s[12:15], s81 offen offset:0 lds
MM a[60:63],a[156:159],v[28:31],a[60:63]
EX v109,v109
MM a[52:55],a[156:159],v[20:23],a[52:55]
RD a[240:243],v224 offset:45056
CV v78,v108,v109
MM a[52:55],a[152:155],v[16:19],a[52:55]
EX v110,v110
MM a[44:47],a[152:155],v[8:11],a[44:47]
EX v111,v111
MM a[44:47],a[156:159],v[12:15],a[44:47]
RD a[244:247],v225 offset:45056
CV v79,v110,v111
MM a[36:39],a[156:159],v[4:7],a[36:39]
buffer_load_dwordx4 v235, s[12:15], s64 offen offset:1024 lds
MM a[36:39],a[152:155],v[0:3],a[36:39]
EX v112,v112
MM a[64:67],a[160:163],v[0:3],a[64:67]
EX v113,v113
MM a[64:67],a[164:167],v[4:7],a[64:67]
RD a[248:251],v226 offset:45056
CV v84,v112,v113
MM a[72:75],a[164:167],v[12:15],a[72:75]
EX v114,v114
MM a[72:75],a[160:163],v[8:11],a[72:75]
EX v115,v115
MM a[80:83],a[160:163],v[16:19],a[80:83]
RD a[252:255],v227 offset:45056
CV v85,v114,v115
MM a[80:83],a[164:167],v[20:23],a[80:83]
buffer_load_dwordx4 v235, s[12:15], s81 offen offset:2048 lds
MM a[88:91],a[164:167],v[28:31],a[88:91]
EX v116,v116
MM a[88:91],a[160:163],v[24:27],a[88:91]
EX v117,v117
MM a[92:95],a[168:171],v[24:27],a[92:95]
EX v118,v118
MM a[92:95],a[172:175],v[28:31],a[92:95]
)ASM"
R"ASM(
buffer_load_dwordx4 v235, s[12:15], s64 offen offset:3072 lds
MM a[84:87],a[172:175],v[20:23],a[84:87]
EX v119,v119
MM a[84:87],a[168:171],v[16:19],a[84:87]
EX v120,v120
MM a[76:79],a[168:171],v[8:11],a[76:79]
EX v121,v121
MM a[76:79],a[172:175],v[12:15],a[76:79]
EX v122,v122
MM a[68:71],a[172:175],v[4:7],a[68:71]
EX v123,v123
MM a[68:71],a[168:171],v[0:3],a[68:71]
EX v124,v124
MM a[96:99],a[176:179],v[0:3],a[96:99]
EX v125,v125
MM a[96:99],a[180:183],v[4:7],a[96:99]
EX v126,v126
MM a[104:107],a[180:183],v[12:15],a[104:107]
EX v127,v127
MM a[104:107],a[176:179],v[8:11],a[104:107]
CV v86,v116,v117
CV v87,v118,v119
MM a[112:115],a[176:179],v[16:19],a[112:115]
CV v92,v120,v121
CV v93,v122,v123
MM a[112:115],a[180:183],v[20:23],a[112:115]
CV v94,v124,v125
CV v95,v126,v127
MM a[120:123],a[180:183],v[28:31],a[120:123]
MM a[120:123],a[176:179],v[24:27],a[120:123]
MM a[124:127],a[184:187],v[24:27],a[124:127]
MM a[124:127],a[188:191],v[28:31],a[124:127]
MM a[116:119],a[188:191],v[20:23],a[116:119]
MM a[116:119],a[184:187],v[16:19],a[116:119]
MM a[108:111],a[184:187],v[8:11],a[108:111]
MM a[108:111],a[188:191],v[12:15],a[108:111]
MM a[100:103],a[188:191],v[4:7],a[100:103]
MM a[100:103],a[184:187],v[0:3],a[100:103]
v_smfmac_f32_16x16x64_bf16 v[236:239], v[144:147], v[64:71], v234
v_smfmac_f32_16x16x64_bf16 v[236:239], v[148:151], v[72:79], v234
v_smfmac_f32_16x16x64_bf16 v[236:239], v[152:155], v[80:87], v234
v_smfmac_f32_16x16x64_bf16 v[236:239], v[156:159], v[88:95], v234
s_waitcnt lgkmcnt(0)
MM v[0:3],a[192:195],v[160:163],v[128:131]
s_add_u32 s64, s64, 16384
TR a[128:129],v232 offset:16384
s_add_u32 s81, s64, 896
MM v[0:3],a[196:199],v[176:179],v[0:3]
TR a[130:131],v232 offset:20480
TR a[132:133],v232 offset:24576
MM v[0:3],a[200:203],v[192:195],v[0:3]
TR a[134:135],v232 offset:28672
TR a[136:137],v233 offset:16384
MM v[0:3],a[204:207],v[208:211],v[0:3]
TR a[138:139],v233 offset:20480
v_add_f32 v247, v236, v237
MM v[4:7],a[220:223],v[208:211],v[128:131]
TR a[140:141],v233 offset:24576
v_cmp_nge_f32 vcc, s71, v247
MM v[4:7],a[216:219],v[192:195],v[4:7]
s_cbranch_vccnz LSITE29_%=
LBACK29_%=:
MM v[4:7],a[212:215],v[176:179],v[4:7]
s_add_u32 m0, s94, 151552
s_add_u32 s66, s87, 81920
buffer_load_dwordx4 v241, s[24:27], s66 offen offset:0 lds
MM v[4:7],a[208:211],v[160:163],v[4:7]
v_bfe_u32 v96, v248, 6, 2
v_mul_u32_u24 v96, 1280, v96
MM v[12:15],a[208:211],v[164:167],v[132:135]
v_bfe_u32 v97, v248, 8, 1
v_mul_u32_u24 v97, 40960, v97
MM v[12:15],a[212:215],v[180:183],v[12:15]
v_add_u32 v96, v96, v97
v_and_b32 v97, 48, v248
MM v[12:15],a[216:219],v[196:199],v[12:15]
s_nop 0
v_mul_u32_u24 v97, 6, v97
v_add_u32 v96, v96, v97
MM v[12:15],a[220:223],v[212:215],v[12:15]
s_min_u32 s60, s18, s63
EX v0,v0
MM v[8:11],a[204:207],v[212:215],v[132:135]
s_add_u32 s61, s60, 131072
s_mov_b32 exec_hi, 0
buffer_load_dword v100, v96, s[32:35], s60 offen offset:0
s_mov_b64 exec, -1
MM v[8:11],a[200:203],v[196:199],v[8:11]
LPG18_%=:
s_cbranch_execz LPG22_%=
EX v1,v1
MM v[8:11],a[196:199],v[180:183],v[8:11]
s_add_u32 m0, s94, 155648
s_add_u32 s66, s88, 0
buffer_load_dwordx4 v241, s[24:27], s66 offen offset:0 lds
MM v[8:11],a[192:195],v[164:167],v[8:11]
s_mov_b32 exec_hi, 0
buffer_load_dword v101, v96, s[20:23], s61 offen offset:0 sc1
s_mov_b64 exec, -1
MM v[16:19],a[192:195],v[168:171],v[136:139]
EX v2,v2
MM v[16:19],a[196:199],v[184:187],v[16:19]
EX v3,v3
MM v[16:19],a[200:203],v[200:203],v[16:19]
TR a[142:143],v233 offset:28672
TR a[144:145],v232 offset:16896
MM v[16:19],a[204:207],v[216:219],v[16:19]
EX v4,v4
MM v[20:23],a[220:223],v[216:219],v[136:139]
EX v5,v5
MM v[20:23],a[216:219],v[200:203],v[20:23]
TR a[146:147],v232 offset:20992
CV v0,v0,v1
MM v[20:23],a[212:215],v[184:187],v[20:23]
s_add_u32 m0, s94, 159744
s_add_u32 s66, s88, 81920
buffer_load_dwordx4 v241, s[24:27], s66 offen offset:0 lds
MM v[20:23],a[208:211],v[168:171],v[20:23]
EX v6,v6
MM v[28:31],a[208:211],v[172:175],v[140:143]
TR a[148:149],v232 offset:25088
TR a[150:151],v232 offset:29184
MM v[28:31],a[212:215],v[188:191],v[28:31]
EX v7,v7
MM v[28:31],a[216:219],v[204:207],v[28:31]
EX v8,v8
MM v[28:31],a[220:223],v[220:223],v[28:31]
TR a[152:153],v233 offset:16896
CV v1,v2,v3
MM v[24:27],a[204:207],v[220:223],v[140:143]
EX v9,v9
MM v[24:27],a[200:203],v[204:207],v[24:27]
TR a[154:155],v233 offset:20992
CV v2,v4,v5
MM v[24:27],a[196:199],v[188:191],v[24:27]
EX v10,v10
MM v[24:27],a[192:195],v[172:175],v[24:27]
TR a[156:157],v233 offset:25088
CV v3,v6,v7
MM v[32:35],a[224:227],v[160:163],v[128:131]
EX v11,v11
MM v[32:35],a[228:231],v[176:179],v[32:35]
TR a[158:159],v233 offset:29184
TR a[160:161],v232 offset:17408
MM v[32:35],a[232:235],v[192:195],v[32:35]
EX v12,v12
MM v[32:35],a[236:239],v[208:211],v[32:35]
EX v13,v13
MM v[36:39],a[252:255],v[208:211],v[128:131]
TR a[162:163],v232 offset:21504
CV v8,v8,v9
MM v[36:39],a[248:251],v[192:195],v[36:39]
EX v14,v14
MM v[36:39],a[244:247],v[176:179],v[36:39]
TR a[164:165],v232 offset:25600
CV v9,v10,v11
MM v[36:39],a[240:243],v[160:163],v[36:39]
EX v15,v15
MM v[44:47],a[240:243],v[164:167],v[132:135]
TR a[166:167],v232 offset:29696
TR a[168:169],v233 offset:17408
MM v[44:47],a[244:247],v[180:183],v[44:47]
EX v16,v16
MM v[44:47],a[248:251],v[196:199],v[44:47]
EX v17,v17
MM v[44:47],a[252:255],v[212:215],v[44:47]
TR a[170:171],v233 offset:21504
CV v10,v12,v13
MM v[40:43],a[236:239],v[212:215],v[132:135]
EX v18,v18
MM v[40:43],a[232:235],v[196:199],v[40:43]
TR a[172:173],v233 offset:25600
CV v11,v14,v15
MM v[40:43],a[228:231],v[180:183],v[40:43]
EX v19,v19
MM v[40:43],a[224:227],v[164:167],v[40:43]
TR a[174:175],v233 offset:29696
TR a[176:177],v232 offset:17920
MM v[48:51],a[224:227],v[168:171],v[136:139]
EX v20,v20
MM v[48:51],a[228:231],v[184:187],v[48:51]
TR a[178:179],v232 offset:22016
CV v16,v16,v17
MM v[48:51],a[232:235],v[200:203],v[48:51]
EX v21,v21
MM v[48:51],a[236:239],v[216:219],v[48:51]
TR a[180:181],v232 offset:26112
CV v17,v18,v19
MM v[52:55],a[252:255],v[216:219],v[136:139]
EX v22,v22
MM v[52:55],a[248:251],v[200:203],v[52:55]
TR a[182:183],v232 offset:30208
TR a[184:185],v233 offset:17920
MM v[52:55],a[244:247],v[184:187],v[52:55]
EX v23,v23
MM v[52:55],a[240:243],v[168:171],v[52:55]
EX v24,v24
MM v[60:63],a[240:243],v[172:175],v[140:143]
TR a[186:187],v233 offset:22016
CV v18,v20,v21
MM v[60:63],a[244:247],v[188:191],v[60:63]
EX v25,v25
MM v[60:63],a[248:251],v[204:207],v[60:63]
TR a[188:189],v233 offset:26112
TR a[190:191],v233 offset:30208
MM v[60:63],a[252:255],v[220:223],v[60:63]
EX v26,v26
MM v[56:59],a[236:239],v[220:223],v[140:143]
CV v19,v22,v23
CV v24,v24,v25
MM v[56:59],a[232:235],v[204:207],v[56:59]
EX v27,v27
MM v[56:59],a[228:231],v[188:191],v[56:59]
EX v28,v28
MM v[56:59],a[224:227],v[172:175],v[56:59]
s_waitcnt vmcnt(19) lgkmcnt(0)
s_barrier
MM a[0:3],a[128:131],v[64:67],a[0:3]
EX v29,v29
MM a[0:3],a[132:135],v[68:71],a[0:3]
s_lshl_b32 s91, s47, 2
CV v25,v26,v27
CV v26,v28,v29
MM a[8:11],a[132:135],v[76:79],a[8:11]
s_add_u32 s91, s91, %[swave]
EX v30,v30
MM a[8:11],a[128:131],v[72:75],a[8:11]
s_lshl_b32 s91, s91, 2
EX v31,v31
MM a[16:19],a[128:131],v[80:83],a[16:19]
s_add_u32 s91, s91, 73728
RD a[192:195],v224 offset:49152
CV v27,v30,v31
MM a[16:19],a[132:135],v[84:87],a[16:19]
v_mov_b32 v250, s69
s_mov_b64 exec, 1
buffer_store_dword v250, off, s[20:23], s91 offset:0 sc1
s_mov_b64 exec, -1
MM a[24:27],a[132:135],v[92:95],a[24:27]
s_lshr_b32 s77, %[sb], 3
EX v32,v32
MM a[24:27],a[128:131],v[88:91],a[24:27]
s_lshl_b32 s77, s77, 2
EX v33,v33
MM a[28:31],a[136:139],v[88:91],a[28:31]
s_add_u32 s77, s77, %[swave]
s_add_u32 m0, s65, 16384
RD a[196:199],v225 offset:49152
MM a[28:31],a[140:143],v[92:95],a[28:31]
EX v34,v34
MM a[20:23],a[140:143],v[84:87],a[20:23]
buffer_load_dwordx4 v228, s[8:11], s64 offen offset:0 lds
MM a[20:23],a[136:139],v[80:83],a[20:23]
s_lshl_b32 s77, s77, 9
EX v35,v35
MM a[12:15],a[136:139],v[72:75],a[12:15]
s_add_u32 s77, s77, 0
v_lshrrev_b32 v253, 1, v248
v_add_u32 v253, s77, v253
MM a[12:15],a[140:143],v[76:79],a[12:15]
s_mov_b32 s77, LKEND_ka-LPD2_%=-16
v_min_u32 v253, s77, v253
CV v4,v32,v33
MM a[4:7],a[140:143],v[68:71],a[4:7]
s_getpc_b64 s[98:99]
LPD2_%=:
global_load_dword v253, v253, s[98:99]
MM a[4:7],a[136:139],v[64:67],a[4:7]
EX v36,v36
MM a[32:35],a[144:147],v[64:67],a[32:35]
EX v37,v37
MM a[32:35],a[148:151],v[68:71],a[32:35]
EX v38,v38
MM a[40:43],a[148:151],v[76:79],a[40:43]
RD a[200:203],v226 offset:49152
CV v5,v34,v35
MM a[40:43],a[144:147],v[72:75],a[40:43]
EX v39,v39
MM a[48:51],a[144:147],v[80:83],a[48:51]
buffer_load_dwordx4 v229, s[8:11], s64 offen offset:1024 lds
MM a[48:51],a[148:151],v[84:87],a[48:51]
EX v40,v40
MM a[56:59],a[148:151],v[92:95],a[56:59]
RD a[204:207],v227 offset:49152
CV v6,v36,v37
MM a[56:59],a[144:147],v[88:91],a[56:59]
EX v41,v41
MM a[60:63],a[152:155],v[88:91],a[60:63]
RD a[208:211],v224 offset:53248
CV v7,v38,v39
MM a[60:63],a[156:159],v[92:95],a[60:63]
EX v42,v42
MM a[52:55],a[156:159],v[84:87],a[52:55]
EX v43,v43
MM a[52:55],a[152:155],v[80:83],a[52:55]
buffer_load_dwordx4 v230, s[8:11], s64 offen offset:2048 lds
MM a[44:47],a[152:155],v[72:75],a[44:47]
EX v44,v44
MM a[44:47],a[156:159],v[76:79],a[44:47]
RD a[212:215],v225 offset:53248
CV v12,v40,v41
MM a[36:39],a[156:159],v[68:71],a[36:39]
EX v45,v45
MM a[36:39],a[152:155],v[64:67],a[36:39]
RD a[216:219],v226 offset:53248
CV v13,v42,v43
MM a[64:67],a[160:163],v[64:67],a[64:67]
EX v46,v46
MM a[64:67],a[164:167],v[68:71],a[64:67]
EX v47,v47
MM a[72:75],a[164:167],v[76:79],a[72:75]
buffer_load_dwordx4 v231, s[8:11], s64 offen offset:3072 lds
MM a[72:75],a[160:163],v[72:75],a[72:75]
s_add_u32 m0, s65, 81920
RD a[220:223],v227 offset:53248
CV v14,v44,v45
MM a[80:83],a[160:163],v[80:83],a[80:83]
EX v48,v48
MM a[80:83],a[164:167],v[84:87],a[80:83]
EX v49,v49
MM a[88:91],a[164:167],v[92:95],a[88:91]
RD a[224:227],v224 offset:57344
CV v15,v46,v47
MM a[88:91],a[160:163],v[88:91],a[88:91]
EX v50,v50
MM a[92:95],a[168:171],v[88:91],a[92:95]
EX v51,v51
MM a[92:95],a[172:175],v[92:95],a[92:95]
buffer_load_dwordx4 v235, s[12:15], s81 offen offset:0 lds
MM a[84:87],a[172:175],v[84:87],a[84:87]
RD a[228:231],v225 offset:57344
CV v20,v48,v49
MM a[84:87],a[168:171],v[80:83],a[84:87]
EX v52,v52
MM a[76:79],a[168:171],v[72:75],a[76:79]
EX v53,v53
MM a[76:79],a[172:175],v[76:79],a[76:79]
RD a[232:235],v226 offset:57344
CV v21,v50,v51
MM a[68:71],a[172:175],v[68:71],a[68:71]
EX v54,v54
MM a[68:71],a[168:171],v[64:67],a[68:71]
RD a[236:239],v227 offset:57344
CV v22,v52,v53
MM a[96:99],a[176:179],v[64:67],a[96:99]
EX v55,v55
MM a[96:99],a[180:183],v[68:71],a[96:99]
buffer_load_dwordx4 v235, s[12:15], s64 offen offset:1024 lds
MM a[104:107],a[180:183],v[76:79],a[104:107]
EX v56,v56
MM a[104:107],a[176:179],v[72:75],a[104:107]
RD a[240:243],v224 offset:61440
CV v23,v54,v55
MM a[112:115],a[176:179],v[80:83],a[112:115]
EX v57,v57
MM a[112:115],a[180:183],v[84:87],a[112:115]
EX v58,v58
MM a[120:123],a[180:183],v[92:95],a[120:123]
RD a[244:247],v225 offset:61440
CV v28,v56,v57
MM a[120:123],a[176:179],v[88:91],a[120:123]
EX v59,v59
MM a[124:127],a[184:187],v[88:91],a[124:127]
buffer_load_dwordx4 v235, s[12:15], s81 offen offset:2048 lds
MM a[124:127],a[188:191],v[92:95],a[124:127]
s_waitcnt vmcnt(10)
EX v60,v60
MM a[116:119],a[188:191],v[84:87],a[116:119]
RD a[248:251],v226 offset:61440
CV v29,v58,v59
MM a[116:119],a[184:187],v[80:83],a[116:119]
EX v61,v61
MM a[108:111],a[184:187],v[72:75],a[108:111]
EX v62,v62
MM a[108:111],a[188:191],v[76:79],a[108:111]
RD a[252:255],v227 offset:61440
CV v30,v60,v61
MM a[100:103],a[188:191],v[68:71],a[100:103]
EX v63,v63
MM a[100:103],a[184:187],v[64:67],a[100:103]
buffer_load_dwordx4 v235, s[12:15], s64 offen offset:3072 lds
v_smfmac_f32_16x16x64_bf16 v[236:239], v[144:147], v[0:7], v234
CV v31,v62,v63
v_smfmac_f32_16x16x64_bf16 v[236:239], v[148:151], v[8:15], v234
v_smfmac_f32_16x16x64_bf16 v[236:239], v[152:155], v[16:23], v234
v_smfmac_f32_16x16x64_bf16 v[236:239], v[156:159], v[24:31], v234
s_waitcnt lgkmcnt(0)
MM v[64:67],a[192:195],v[160:163],v[128:131]
)ASM"
R"ASM(
s_add_u32 s64, s64, 16384
TR a[128:129],v232 offset:32768
s_add_u32 s81, s64, 896
MM v[64:67],a[196:199],v[176:179],v[64:67]
TR a[130:131],v232 offset:36864
TR a[132:133],v232 offset:40960
MM v[64:67],a[200:203],v[192:195],v[64:67]
TR a[134:135],v232 offset:45056
TR a[136:137],v233 offset:32768
MM v[64:67],a[204:207],v[208:211],v[64:67]
TR a[138:139],v233 offset:36864
v_add_f32 v246, v236, v237
MM v[68:71],a[220:223],v[208:211],v[128:131]
TR a[140:141],v233 offset:40960
v_cmp_nge_f32 vcc, s71, v246
MM v[68:71],a[216:219],v[192:195],v[68:71]
s_cbranch_vccnz LSITE30_%=
LBACK30_%=:
MM v[68:71],a[212:215],v[176:179],v[68:71]
TR a[142:143],v233 offset:45056
TR a[144:145],v232 offset:33280
MM v[68:71],a[208:211],v[160:163],v[68:71]
TR a[146:147],v232 offset:37376
TR a[148:149],v232 offset:41472
MM v[76:79],a[208:211],v[164:167],v[132:135]
EX v64,v64
MM v[76:79],a[212:215],v[180:183],v[76:79]
EX v65,v65
MM v[76:79],a[216:219],v[196:199],v[76:79]
TR a[150:151],v232 offset:45568
CV v64,v64,v65
MM v[76:79],a[220:223],v[212:215],v[76:79]
EX v66,v66
MM v[72:75],a[204:207],v[212:215],v[132:135]
EX v67,v67
MM v[72:75],a[200:203],v[196:199],v[72:75]
TR a[152:153],v233 offset:33280
CV v65,v66,v67
MM v[72:75],a[196:199],v[180:183],v[72:75]
EX v68,v68
MM v[72:75],a[192:195],v[164:167],v[72:75]
EX v69,v69
MM v[80:83],a[192:195],v[168:171],v[136:139]
TR a[154:155],v233 offset:37376
CV v66,v68,v69
MM v[80:83],a[196:199],v[184:187],v[80:83]
EX v70,v70
MM v[80:83],a[200:203],v[200:203],v[80:83]
EX v71,v71
MM v[80:83],a[204:207],v[216:219],v[80:83]
TR a[156:157],v233 offset:41472
CV v67,v70,v71
MM v[84:87],a[220:223],v[216:219],v[136:139]
EX v72,v72
MM v[84:87],a[216:219],v[200:203],v[84:87]
TR a[158:159],v233 offset:45568
TR a[160:161],v232 offset:33792
MM v[84:87],a[212:215],v[184:187],v[84:87]
EX v73,v73
MM v[84:87],a[208:211],v[168:171],v[84:87]
TR a[162:163],v232 offset:37888
CV v72,v72,v73
MM v[92:95],a[208:211],v[172:175],v[140:143]
EX v74,v74
MM v[92:95],a[212:215],v[188:191],v[92:95]
TR a[164:165],v232 offset:41984
TR a[166:167],v232 offset:46080
MM v[92:95],a[216:219],v[204:207],v[92:95]
EX v75,v75
MM v[92:95],a[220:223],v[220:223],v[92:95]
TR a[168:169],v233 offset:33792
CV v73,v74,v75
MM v[88:91],a[204:207],v[220:223],v[140:143]
EX v76,v76
MM v[88:91],a[200:203],v[204:207],v[88:91]
EX v77,v77
MM v[88:91],a[196:199],v[188:191],v[88:91]
TR a[170:171],v233 offset:37888
CV v74,v76,v77
MM v[88:91],a[192:195],v[172:175],v[88:91]
EX v78,v78
MM v[96:99],a[224:227],v[160:163],v[128:131]
TR a[172:173],v233 offset:41984
TR a[174:175],v233 offset:46080
MM v[96:99],a[228:231],v[176:179],v[96:99]
EX v79,v79
MM v[96:99],a[232:235],v[192:195],v[96:99]
TR a[176:177],v232 offset:34304
CV v75,v78,v79
MM v[96:99],a[236:239],v[208:211],v[96:99]
EX v80,v80
MM v[100:103],a[252:255],v[208:211],v[128:131]
EX v81,v81
MM v[100:103],a[248:251],v[192:195],v[100:103]
TR a[178:179],v232 offset:38400
CV v80,v80,v81
MM v[100:103],a[244:247],v[176:179],v[100:103]
EX v82,v82
MM v[100:103],a[240:243],v[160:163],v[100:103]
TR a[180:181],v232 offset:42496
TR a[182:183],v232 offset:46592
MM v[108:111],a[240:243],v[164:167],v[132:135]
EX v83,v83
MM v[108:111],a[244:247],v[180:183],v[108:111]
TR a[184:185],v233 offset:34304
CV v81,v82,v83
MM v[108:111],a[248:251],v[196:199],v[108:111]
EX v84,v84
MM v[108:111],a[252:255],v[212:215],v[108:111]
EX v85,v85
MM v[104:107],a[236:239],v[212:215],v[132:135]
TR a[186:187],v233 offset:38400
CV v82,v84,v85
MM v[104:107],a[232:235],v[196:199],v[104:107]
EX v86,v86
MM v[104:107],a[228:231],v[180:183],v[104:107]
TR a[188:189],v233 offset:42496
TR a[190:191],v233 offset:46592
MM v[104:107],a[224:227],v[164:167],v[104:107]
EX v87,v87
MM v[112:115],a[224:227],v[168:171],v[136:139]
CV v83,v86,v87
MM v[112:115],a[228:231],v[184:187],v[112:115]
EX v88,v88
MM v[112:115],a[232:235],v[200:203],v[112:115]
EX v89,v89
MM v[112:115],a[236:239],v[216:219],v[112:115]
EX v90,v90
MM v[116:119],a[252:255],v[216:219],v[136:139]
EX v91,v91
MM v[116:119],a[248:251],v[200:203],v[116:119]
EX v92,v92
MM v[116:119],a[244:247],v[184:187],v[116:119]
EX v93,v93
MM v[116:119],a[240:243],v[168:171],v[116:119]
EX v94,v94
MM v[124:127],a[240:243],v[172:175],v[140:143]
EX v95,v95
MM v[124:127],a[244:247],v[188:191],v[124:127]
EX v96,v96
MM v[124:127],a[248:251],v[204:207],v[124:127]
EX v97,v97
MM v[124:127],a[252:255],v[220:223],v[124:127]
EX v98,v98
MM v[120:123],a[236:239],v[220:223],v[140:143]
EX v99,v99
MM v[120:123],a[232:235],v[204:207],v[120:123]
EX v100,v100
MM v[120:123],a[228:231],v[188:191],v[120:123]
EX v101,v101
MM v[120:123],a[224:227],v[172:175],v[120:123]
s_waitcnt lgkmcnt(0)
s_barrier
MM a[0:3],a[128:131],v[0:3],a[0:3]
s_add_u32 m0, s65, 32768
CV v88,v88,v89
CV v89,v90,v91
MM a[0:3],a[132:135],v[4:7],a[0:3]
RD a[192:195],v224 offset:0
CV v90,v92,v93
MM a[8:11],a[132:135],v[12:15],a[8:11]
RD a[196:199],v225 offset:0
CV v91,v94,v95
MM a[8:11],a[128:131],v[8:11],a[8:11]
buffer_load_dwordx4 v228, s[8:11], s64 offen offset:0 lds
MM a[16:19],a[128:131],v[16:19],a[16:19]
RD a[200:203],v226 offset:0
CV v68,v96,v97
MM a[16:19],a[132:135],v[20:23],a[16:19]
RD a[204:207],v227 offset:0
CV v69,v98,v99
MM a[24:27],a[132:135],v[28:31],a[24:27]
buffer_load_dwordx4 v229, s[8:11], s64 offen offset:1024 lds
MM a[24:27],a[128:131],v[24:27],a[24:27]
RD a[208:211],v224 offset:4096
CV v70,v100,v101
MM a[28:31],a[136:139],v[24:27],a[28:31]
RD a[212:215],v225 offset:4096
RD a[216:219],v226 offset:4096
MM a[28:31],a[140:143],v[28:31],a[28:31]
buffer_load_dwordx4 v230, s[8:11], s64 offen offset:2048 lds
MM a[20:23],a[140:143],v[20:23],a[20:23]
RD a[220:223],v227 offset:4096
RD a[224:227],v224 offset:8192
MM a[20:23],a[136:139],v[16:19],a[20:23]
EX v102,v102
MM a[12:15],a[136:139],v[8:11],a[12:15]
buffer_load_dwordx4 v231, s[8:11], s64 offen offset:3072 lds
MM a[12:15],a[140:143],v[12:15],a[12:15]
s_add_u32 m0, s65, 98304
EX v103,v103
MM a[4:7],a[140:143],v[4:7],a[4:7]
RD a[228:231],v225 offset:8192
CV v71,v102,v103
MM a[4:7],a[136:139],v[0:3],a[4:7]
EX v104,v104
MM a[32:35],a[144:147],v[0:3],a[32:35]
EX v105,v105
MM a[32:35],a[148:151],v[4:7],a[32:35]
RD a[232:235],v226 offset:8192
CV v76,v104,v105
MM a[40:43],a[148:151],v[12:15],a[40:43]
EX v106,v106
MM a[40:43],a[144:147],v[8:11],a[40:43]
buffer_load_dwordx4 v235, s[12:15], s81 offen offset:0 lds
MM a[48:51],a[144:147],v[16:19],a[48:51]
EX v107,v107
MM a[48:51],a[148:151],v[20:23],a[48:51]
RD a[236:239],v227 offset:8192
CV v77,v106,v107
MM a[56:59],a[148:151],v[28:31],a[56:59]
EX v108,v108
MM a[56:59],a[144:147],v[24:27],a[56:59]
EX v109,v109
MM a[60:63],a[152:155],v[24:27],a[60:63]
RD a[240:243],v224 offset:12288
CV v78,v108,v109
MM a[60:63],a[156:159],v[28:31],a[60:63]
buffer_load_dwordx4 v235, s[12:15], s64 offen offset:1024 lds
MM a[52:55],a[156:159],v[20:23],a[52:55]
EX v110,v110
MM a[52:55],a[152:155],v[16:19],a[52:55]
EX v111,v111
MM a[44:47],a[152:155],v[8:11],a[44:47]
RD a[244:247],v225 offset:12288
CV v79,v110,v111
MM a[44:47],a[156:159],v[12:15],a[44:47]
EX v112,v112
MM a[36:39],a[156:159],v[4:7],a[36:39]
EX v113,v113
MM a[36:39],a[152:155],v[0:3],a[36:39]
RD a[248:251],v226 offset:12288
CV v84,v112,v113
MM a[64:67],a[160:163],v[0:3],a[64:67]
buffer_load_dwordx4 v235, s[12:15], s81 offen offset:2048 lds
MM a[64:67],a[164:167],v[4:7],a[64:67]
EX v114,v114
MM a[72:75],a[164:167],v[12:15],a[72:75]
EX v115,v115
MM a[72:75],a[160:163],v[8:11],a[72:75]
RD a[252:255],v227 offset:12288
CV v85,v114,v115
MM a[80:83],a[160:163],v[16:19],a[80:83]
EX v116,v116
MM a[80:83],a[164:167],v[20:23],a[80:83]
buffer_load_dwordx4 v235, s[12:15], s64 offen offset:3072 lds
MM a[88:91],a[164:167],v[28:31],a[88:91]
EX v117,v117
MM a[88:91],a[160:163],v[24:27],a[88:91]
EX v118,v118
MM a[92:95],a[168:171],v[24:27],a[92:95]
EX v119,v119
MM a[92:95],a[172:175],v[28:31],a[92:95]
EX v120,v120
MM a[84:87],a[172:175],v[20:23],a[84:87]
EX v121,v121
MM a[84:87],a[168:171],v[16:19],a[84:87]
EX v122,v122
MM a[76:79],a[168:171],v[8:11],a[76:79]
EX v123,v123
MM a[76:79],a[172:175],v[12:15],a[76:79]
EX v124,v124
MM a[68:71],a[172:175],v[4:7],a[68:71]
EX v125,v125
MM a[68:71],a[168:171],v[0:3],a[68:71]
EX v126,v126
MM a[96:99],a[176:179],v[0:3],a[96:99]
EX v127,v127
MM a[96:99],a[180:183],v[4:7],a[96:99]
CV v86,v116,v117
CV v87,v118,v119
MM a[104:107],a[180:183],v[12:15],a[104:107]
CV v92,v120,v121
CV v93,v122,v123
MM a[104:107],a[176:179],v[8:11],a[104:107]
CV v94,v124,v125
CV v95,v126,v127
MM a[112:115],a[176:179],v[16:19],a[112:115]
MM a[112:115],a[180:183],v[20:23],a[112:115]
MM a[120:123],a[180:183],v[28:31],a[120:123]
MM a[120:123],a[176:179],v[24:27],a[120:123]
MM a[124:127],a[184:187],v[24:27],a[124:127]
s_nop 0
MM a[124:127],a[188:191],v[28:31],a[124:127]
s_nop 0
MM a[116:119],a[188:191],v[20:23],a[116:119]
s_nop 0
MM a[116:119],a[184:187],v[16:19],a[116:119]
s_nop 0
MM a[108:111],a[184:187],v[8:11],a[108:111]
s_nop 0
MM a[108:111],a[188:191],v[12:15],a[108:111]
s_nop 0
MM a[100:103],a[188:191],v[4:7],a[100:103]
s_nop 0
MM a[100:103],a[184:187],v[0:3],a[100:103]
LPG19_%=:
s_cbranch_execz LPG23_%=
v_smfmac_f32_16x16x64_bf16 v[236:239], v[144:147], v[64:71], v234
v_smfmac_f32_16x16x64_bf16 v[236:239], v[148:151], v[72:79], v234
v_smfmac_f32_16x16x64_bf16 v[236:239], v[152:155], v[80:87], v234
v_smfmac_f32_16x16x64_bf16 v[236:239], v[156:159], v[88:95], v234
s_waitcnt lgkmcnt(0)
MM v[16:19],a[192:195],v[168:171],v[136:139]
TR a[128:129],v232 offset:49152
TR a[130:131],v232 offset:53248
MM v[16:19],a[196:199],v[184:187],v[16:19]
TR a[132:133],v232 offset:57344
TR a[134:135],v232 offset:61440
MM v[16:19],a[200:203],v[200:203],v[16:19]
TR a[136:137],v233 offset:49152
TR a[138:139],v233 offset:53248
MM v[16:19],a[204:207],v[216:219],v[16:19]
TR a[140:141],v233 offset:57344
v_add_f32 v247, v236, v237
MM v[20:23],a[220:223],v[216:219],v[136:139]
TR a[142:143],v233 offset:61440
v_cmp_nge_f32 vcc, s71, v247
MM v[20:23],a[216:219],v[200:203],v[20:23]
s_cbranch_vccnz LSITE31_%=
LBACK31_%=:
TR a[144:145],v232 offset:49664
TR a[146:147],v232 offset:53760
MM v[20:23],a[212:215],v[184:187],v[20:23]
v_rcp_f32 v145, v247
MM v[20:23],a[208:211],v[168:171],v[20:23]
TR a[148:149],v232 offset:57856
TR a[150:151],v232 offset:61952
MM v[28:31],a[208:211],v[172:175],v[140:143]
EX v16,v16
MM v[28:31],a[212:215],v[188:191],v[28:31]
TR a[152:153],v233 offset:49664
TR a[154:155],v233 offset:53760
MM v[28:31],a[216:219],v[204:207],v[28:31]
EX v17,v17
MM v[28:31],a[220:223],v[220:223],v[28:31]
TR a[156:157],v233 offset:57856
CV v16,v16,v17
MM v[24:27],a[204:207],v[220:223],v[140:143]
TR a[158:159],v233 offset:61952
TR a[160:161],v232 offset:50176
MM v[24:27],a[200:203],v[204:207],v[24:27]
TR a[162:163],v232 offset:54272
TR a[164:165],v232 offset:58368
MM v[24:27],a[196:199],v[188:191],v[24:27]
EX v18,v18
MM v[24:27],a[192:195],v[172:175],v[24:27]
TR a[166:167],v232 offset:62464
TR a[168:169],v233 offset:50176
MM v[48:51],a[224:227],v[168:171],v[136:139]
EX v19,v19
MM v[48:51],a[228:231],v[184:187],v[48:51]
TR a[170:171],v233 offset:54272
CV v17,v18,v19
MM v[48:51],a[232:235],v[200:203],v[48:51]
TR a[172:173],v233 offset:58368
TR a[174:175],v233 offset:62464
MM v[48:51],a[236:239],v[216:219],v[48:51]
TR a[176:177],v232 offset:50688
TR a[178:179],v232 offset:54784
MM v[52:55],a[252:255],v[216:219],v[136:139]
EX v20,v20
MM v[52:55],a[248:251],v[200:203],v[52:55]
TR a[180:181],v232 offset:58880
TR a[182:183],v232 offset:62976
MM v[52:55],a[244:247],v[184:187],v[52:55]
EX v21,v21
MM v[52:55],a[240:243],v[168:171],v[52:55]
TR a[184:185],v233 offset:50688
TR a[186:187],v233 offset:54784
MM v[60:63],a[240:243],v[172:175],v[140:143]
TR a[188:189],v233 offset:58880
CV v18,v20,v21
MM v[60:63],a[244:247],v[188:191],v[60:63]
TR a[190:191],v233 offset:62976
v_mov_b32 v144, v145
MM v[60:63],a[248:251],v[204:207],v[60:63]
EX v22,v22
MM v[60:63],a[252:255],v[220:223],v[60:63]
EX v23,v23
MM v[56:59],a[236:239],v[220:223],v[140:143]
CV v19,v22,v23
v_permlane32_swap_b32 v145, v144
MM v[56:59],a[232:235],v[204:207],v[56:59]
EX v24,v24
MM v[56:59],a[228:231],v[188:191],v[56:59]
)ASM"
R"ASM(
EX v25,v25
MM v[56:59],a[224:227],v[172:175],v[56:59]
s_waitcnt vmcnt(12) lgkmcnt(0)
s_barrier
CV v24,v24,v25
MM a[0:3],a[128:131],v[64:67],a[0:3]
s_add_u32 m0, s89, 49152
RD a[192:195],v224 offset:16384
RD a[196:199],v225 offset:16384
MM a[0:3],a[132:135],v[68:71],a[0:3]
s_add_u32 s66, s85, 0
EX v26,v26
MM a[8:11],a[132:135],v[76:79],a[8:11]
buffer_load_dwordx4 v240, s[24:27], s66 offen offset:0 lds
MM a[8:11],a[128:131],v[72:75],a[8:11]
s_add_u32 m0, s89, 50176
RD a[200:203],v226 offset:16384
RD a[204:207],v227 offset:16384
MM a[12:15],a[136:139],v[72:75],a[12:15]
s_add_u32 s66, s85, 10240
EX v27,v27
MM a[12:15],a[140:143],v[76:79],a[12:15]
RD a[208:211],v224 offset:20480
CV v25,v26,v27
MM a[4:7],a[140:143],v[68:71],a[4:7]
buffer_load_dwordx4 v240, s[24:27], s66 offen offset:0 lds
MM a[4:7],a[136:139],v[64:67],a[4:7]
s_add_u32 m0, s89, 57344
EX v28,v28
MM a[32:35],a[144:147],v[64:67],a[32:35]
s_add_u32 s66, s85, 81920
EX v29,v29
MM a[32:35],a[148:151],v[68:71],a[32:35]
CV v26,v28,v29
v_mov_b32 v149, v145
MM a[40:43],a[148:151],v[76:79],a[40:43]
RD a[212:215],v225 offset:20480
v_permlane16_swap_b32 v145, v149
MM a[40:43],a[144:147],v[72:75],a[40:43]
s_mul_i32 s78, s49, 17408
s_add_u32 s78, s83, s78
MM a[44:47],a[152:155],v[72:75],a[44:47]
s_add_u32 s78, s78, 0
s_add_u32 s79, s78, 4096
MM a[44:47],a[156:159],v[76:79],a[44:47]
AR v0,a0
AR v1,a1
MM a[36:39],a[156:159],v[68:71],a[36:39]
AR v2,a2
AR v3,a3
MM a[36:39],a[152:155],v[64:67],a[36:39]
AR v4,a4
AR v5,a5
MM a[64:67],a[160:163],v[64:67],a[64:67]
AR v6,a6
AR v7,a7
MM a[64:67],a[164:167],v[68:71],a[64:67]
v_mul_f32 v0, v0, v145
v_mul_f32 v1, v1, v145
MM a[72:75],a[164:167],v[76:79],a[72:75]
v_mul_f32 v2, v2, v145
v_mul_f32 v3, v3, v145
MM a[72:75],a[160:163],v[72:75],a[72:75]
EX v30,v30
MM a[76:79],a[168:171],v[72:75],a[76:79]
v_mul_f32 v4, v4, v145
v_mul_f32 v5, v5, v145
MM a[76:79],a[172:175],v[76:79],a[76:79]
v_mul_f32 v6, v6, v145
v_mul_f32 v7, v7, v145
EX v31,v31
MM a[68:71],a[172:175],v[68:71],a[68:71]
CV v160,v0,v1
CV v161,v2,v3
MM a[68:71],a[168:171],v[64:67],a[68:71]
CV v162,v4,v5
CV v163,v6,v7
MM a[96:99],a[176:179],v[64:67],a[96:99]
buffer_store_dwordx4 v[160:163], v248, s[20:23], s78 offen offset:0 sc1
MM a[96:99],a[180:183],v[68:71],a[96:99]
AR v8,a8
AR v9,a9
EX v48,v48
MM a[104:107],a[180:183],v[76:79],a[104:107]
AR v10,a10
AR v11,a11
MM a[104:107],a[176:179],v[72:75],a[104:107]
AR v12,a12
AR v13,a13
EX v49,v49
MM a[108:111],a[184:187],v[72:75],a[108:111]
AR v14,a14
AR v15,a15
MM a[108:111],a[188:191],v[76:79],a[108:111]
v_mul_f32 v8, v8, v149
v_mul_f32 v9, v9, v149
EX v50,v50
MM a[100:103],a[188:191],v[68:71],a[100:103]
v_mul_f32 v10, v10, v149
v_mul_f32 v11, v11, v149
MM a[100:103],a[184:187],v[64:67],a[100:103]
v_mul_f32 v12, v12, v149
v_mul_f32 v13, v13, v149
MM a[16:19],a[128:131],v[80:83],a[16:19]
v_mul_f32 v14, v14, v149
v_mul_f32 v15, v15, v149
EX v51,v51
MM a[16:19],a[132:135],v[84:87],a[16:19]
buffer_load_dwordx4 v240, s[24:27], s66 offen offset:0 lds
MM a[24:27],a[132:135],v[92:95],a[24:27]
s_add_u32 m0, s89, 58368
CV v164,v8,v9
CV v165,v10,v11
CV v166,v12,v13
MM a[24:27],a[128:131],v[88:91],a[24:27]
s_add_u32 s66, s85, 92160
CV v167,v14,v15
CV v27,v30,v31
EX v52,v52
MM a[28:31],a[136:139],v[88:91],a[28:31]
buffer_store_dwordx4 v[164:167], v248, s[20:23], s79 offen offset:0 sc1
MM a[28:31],a[140:143],v[92:95],a[28:31]
AR v32,a32
AR v33,a33
EX v53,v53
AR v34,a34
MM a[20:23],a[140:143],v[84:87],a[20:23]
AR v35,a35
AR v36,a36
MM a[20:23],a[136:139],v[80:83],a[20:23]
RD a[216:219],v226 offset:20480
AR v37,a37
AR v38,a38
MM a[48:51],a[144:147],v[80:83],a[48:51]
AR v39,a39
v_mul_f32 v32, v32, v145
EX v54,v54
MM a[48:51],a[148:151],v[84:87],a[48:51]
buffer_load_dwordx4 v240, s[24:27], s66 offen offset:0 lds
MM a[56:59],a[148:151],v[92:95],a[56:59]
s_add_u32 m0, s89, 114688
RD a[220:223],v227 offset:20480
v_mul_f32 v33, v33, v145
v_mul_f32 v34, v34, v145
v_mul_f32 v35, v35, v145
CV v20,v48,v49
EX v55,v55
MM a[56:59],a[144:147],v[88:91],a[56:59]
s_add_u32 s66, s86, 0
v_mul_f32 v36, v36, v145
v_mul_f32 v37, v37, v145
MM a[60:63],a[152:155],v[88:91],a[60:63]
v_mul_f32 v38, v38, v145
v_mul_f32 v39, v39, v145
MM a[60:63],a[156:159],v[92:95],a[60:63]
CV v176,v32,v33
CV v177,v34,v35
EX v56,v56
MM a[52:55],a[156:159],v[84:87],a[52:55]
RD a[224:227],v224 offset:24576
CV v178,v36,v37
CV v179,v38,v39
CV v21,v50,v51
MM a[52:55],a[152:155],v[80:83],a[52:55]
buffer_store_dwordx4 v[176:179], v248, s[20:23], s78 offen offset:1024 sc1
MM a[80:83],a[160:163],v[80:83],a[80:83]
AR v40,a40
AR v41,a41
EX v57,v57
MM a[80:83],a[164:167],v[84:87],a[80:83]
AR v42,a42
AR v43,a43
MM a[88:91],a[164:167],v[92:95],a[88:91]
buffer_load_dwordx4 v240, s[24:27], s66 offen offset:0 lds
MM a[88:91],a[160:163],v[88:91],a[88:91]
s_add_u32 m0, s89, 115712
RD a[228:231],v225 offset:24576
AR v44,a44
AR v45,a45
AR v46,a46
EX v58,v58
CV v22,v52,v53
MM a[92:95],a[168:171],v[88:91],a[92:95]
s_add_u32 s66, s86, 10240
AR v47,a47
v_mul_f32 v40, v40, v149
MM a[92:95],a[172:175],v[92:95],a[92:95]
v_mul_f32 v41, v41, v149
v_mul_f32 v42, v42, v149
EX v59,v59
MM a[84:87],a[172:175],v[84:87],a[84:87]
RD a[232:235],v226 offset:24576
v_mul_f32 v43, v43, v149
v_mul_f32 v44, v44, v149
MM a[84:87],a[168:171],v[80:83],a[84:87]
v_mul_f32 v45, v45, v149
v_mul_f32 v46, v46, v149
MM a[112:115],a[176:179],v[80:83],a[112:115]
v_mul_f32 v47, v47, v149
CV v180,v40,v41
EX v60,v60
CV v23,v54,v55
MM a[112:115],a[180:183],v[84:87],a[112:115]
CV v181,v42,v43
CV v182,v44,v45
MM a[120:123],a[180:183],v[92:95],a[120:123]
buffer_load_dwordx4 v240, s[24:27], s66 offen offset:0 lds
MM a[120:123],a[176:179],v[88:91],a[120:123]
s_add_u32 m0, s89, 122880
RD a[236:239],v227 offset:24576
CV v183,v46,v47
EX v61,v61
MM a[124:127],a[184:187],v[88:91],a[124:127]
buffer_store_dwordx4 v[180:183], v248, s[20:23], s79 offen offset:1024 sc1
MM a[124:127],a[188:191],v[92:95],a[124:127]
s_add_u32 s66, s86, 81920
AR v0,a64
AR v1,a65
AR v2,a66
AR v3,a67
EX v62,v62
AR v4,a68
MM a[116:119],a[188:191],v[84:87],a[116:119]
RD a[240:243],v224 offset:28672
RD a[244:247],v225 offset:28672
RD a[248:251],v226 offset:28672
RD a[252:255],v227 offset:28672
EX v63,v63
AR v5,a69
CV v28,v56,v57
MM a[116:119],a[184:187],v[80:83],a[116:119]
AR v6,a70
AR v7,a71
v_smfmac_f32_16x16x64_bf16 v[236:239], v[152:155], v[16:23], v234
buffer_load_dwordx4 v240, s[24:27], s66 offen offset:0 lds
CV v29,v58,v59
CV v30,v60,v61
CV v31,v62,v63
s_nop 1
v_smfmac_f32_16x16x64_bf16 v[236:239], v[156:159], v[24:31], v234
s_waitcnt lgkmcnt(0)
s_add_u32 m0, s89, 123904
v_mul_f32 v0, v0, v145
v_mul_f32 v1, v1, v145
v_mul_f32 v2, v2, v145
v_mul_f32 v3, v3, v145
s_add_u32 s66, s86, 92160
buffer_load_dwordx4 v240, s[24:27], s66 offen offset:0 lds
MM v[80:83],a[192:195],v[168:171],v[136:139]
v_mul_f32 v4, v4, v145
v_mul_f32 v5, v5, v145
MM v[80:83],a[196:199],v[184:187],v[80:83]
v_mul_f32 v6, v6, v145
v_mul_f32 v7, v7, v145
MM v[80:83],a[200:203],v[200:203],v[80:83]
CV v192,v0,v1
CV v193,v2,v3
MM v[80:83],a[204:207],v[216:219],v[80:83]
TR a[128:129],v232 offset:0
v_add_f32 v246, v236, v237
CV v194,v4,v5
CV v195,v6,v7
MM v[84:87],a[220:223],v[216:219],v[136:139]
TR a[130:131],v232 offset:4096
TR a[132:133],v232 offset:8192
v_cmp_nge_f32 vcc, s71, v246
MM v[84:87],a[216:219],v[200:203],v[84:87]
s_cbranch_vccnz LSITE32_%=
LBACK32_%=:
buffer_store_dwordx4 v[192:195], v248, s[20:23], s78 offen offset:2048 sc1
MM v[84:87],a[212:215],v[184:187],v[84:87]
TR a[134:135],v232 offset:12288
TR a[136:137],v233 offset:0
AR v8,a72
AR v9,a73
AR v10,a74
AR v11,a75
MM v[84:87],a[208:211],v[168:171],v[84:87]
TR a[138:139],v233 offset:4096
AR v12,a76
MM v[92:95],a[208:211],v[172:175],v[140:143]
TR a[140:141],v233 offset:8192
TR a[142:143],v233 offset:12288
AR v13,a77
AR v14,a78
MM v[92:95],a[212:215],v[188:191],v[92:95]
TR a[144:145],v232 offset:512
AR v15,a79
v_mul_f32 v8, v8, v149
MM v[92:95],a[216:219],v[204:207],v[92:95]
TR a[146:147],v232 offset:4608
v_mul_f32 v9, v9, v149
v_mul_f32 v10, v10, v149
MM v[92:95],a[220:223],v[220:223],v[92:95]
TR a[148:149],v232 offset:8704
v_mul_f32 v11, v11, v149
v_mul_f32 v12, v12, v149
MM v[88:91],a[204:207],v[220:223],v[140:143]
TR a[150:151],v232 offset:12800
v_mul_f32 v13, v13, v149
EX v80,v80
v_mul_f32 v14, v14, v149
MM v[88:91],a[200:203],v[204:207],v[88:91]
TR a[152:153],v233 offset:512
TR a[154:155],v233 offset:4608
v_mul_f32 v15, v15, v149
CV v196,v8,v9
EX v81,v81
MM v[88:91],a[196:199],v[188:191],v[88:91]
TR a[156:157],v233 offset:8704
CV v197,v10,v11
CV v198,v12,v13
MM v[88:91],a[192:195],v[172:175],v[88:91]
TR a[158:159],v233 offset:12800
CV v199,v14,v15
EX v82,v82
MM v[112:115],a[224:227],v[168:171],v[136:139]
buffer_store_dwordx4 v[196:199], v248, s[20:23], s79 offen offset:2048 sc1
MM v[112:115],a[228:231],v[184:187],v[112:115]
TR a[160:161],v232 offset:1024
TR a[162:163],v232 offset:5120
TR a[164:165],v232 offset:9216
AR v32,a96
EX v83,v83
AR v33,a97
AR v34,a98
MM v[112:115],a[232:235],v[200:203],v[112:115]
TR a[166:167],v232 offset:13312
AR v35,a99
EX v84,v84
MM v[112:115],a[236:239],v[216:219],v[112:115]
TR a[168:169],v233 offset:1024
AR v36,a100
AR v37,a101
MM v[116:119],a[252:255],v[216:219],v[136:139]
TR a[170:171],v233 offset:5120
AR v38,a102
EX v85,v85
AR v39,a103
MM v[116:119],a[248:251],v[200:203],v[116:119]
TR a[172:173],v233 offset:9216
TR a[174:175],v233 offset:13312
v_mul_f32 v32, v32, v145
v_mul_f32 v33, v33, v145
MM v[116:119],a[244:247],v[184:187],v[116:119]
TR a[176:177],v232 offset:1536
v_mul_f32 v34, v34, v145
v_mul_f32 v35, v35, v145
EX v86,v86
MM v[116:119],a[240:243],v[168:171],v[116:119]
TR a[178:179],v232 offset:5632
v_mul_f32 v36, v36, v145
v_mul_f32 v37, v37, v145
EX v87,v87
MM v[124:127],a[240:243],v[172:175],v[140:143]
TR a[180:181],v232 offset:9728
v_mul_f32 v38, v38, v145
v_mul_f32 v39, v39, v145
CV v80,v80,v81
MM v[124:127],a[244:247],v[188:191],v[124:127]
TR a[182:183],v232 offset:13824
CV v208,v32,v33
EX v88,v88
CV v209,v34,v35
MM v[124:127],a[248:251],v[204:207],v[124:127]
TR a[184:185],v233 offset:1536
TR a[186:187],v233 offset:5632
TR a[188:189],v233 offset:9728
TR a[190:191],v233 offset:13824
CV v210,v36,v37
CV v211,v38,v39
EX v89,v89
MM v[124:127],a[252:255],v[220:223],v[124:127]
buffer_store_dwordx4 v[208:211], v248, s[20:23], s78 offen offset:3072 sc1
MM v[120:123],a[236:239],v[220:223],v[140:143]
AR v40,a104
CV v81,v82,v83
EX v90,v90
AR v41,a105
MM v[120:123],a[232:235],v[204:207],v[120:123]
AR v42,a106
AR v43,a107
MM v[120:123],a[228:231],v[188:191],v[120:123]
EX v91,v91
AR v44,a108
CV v82,v84,v85
MM v[120:123],a[224:227],v[172:175],v[120:123]
s_waitcnt vmcnt(19) lgkmcnt(0)
s_barrier
AR v45,a109
AR v46,a110
EX v92,v92
MM a[16:19],a[128:131],v[16:19],a[16:19]
s_add_u32 m0, s89, 0
AR v47,a111
v_mul_f32 v40, v40, v149
MM a[16:19],a[132:135],v[20:23],a[16:19]
s_add_u32 s66, s87, 0
v_mul_f32 v41, v41, v149
v_mul_f32 v42, v42, v149
EX v93,v93
CV v83,v86,v87
MM a[24:27],a[132:135],v[28:31],a[24:27]
v_mul_f32 v43, v43, v149
v_mul_f32 v44, v44, v149
MM a[24:27],a[128:131],v[24:27],a[24:27]
v_mul_f32 v45, v45, v149
v_mul_f32 v46, v46, v149
EX v94,v94
MM a[28:31],a[136:139],v[24:27],a[28:31]
v_mul_f32 v47, v47, v149
CV v212,v40,v41
EX v95,v95
MM a[28:31],a[140:143],v[28:31],a[28:31]
CV v213,v42,v43
CV v88,v88,v89
CV v214,v44,v45
MM a[20:23],a[140:143],v[20:23],a[20:23]
CV v215,v46,v47
CV v89,v90,v91
EX v112,v112
MM a[20:23],a[136:139],v[16:19],a[20:23]
buffer_store_dwordx4 v[212:215], v248, s[20:23], s79 offen offset:3072 sc1
MM a[48:51],a[144:147],v[16:19],a[48:51]
RD a[192:195],v224 offset:32768
EX v113,v113
MM a[48:51],a[148:151],v[20:23],a[48:51]
buffer_load_dwordx4 v240, s[24:27], s66 offen offset:0 lds
MM a[56:59],a[148:151],v[28:31],a[56:59]
s_add_u32 m0, s89, 1024
RD a[196:199],v225 offset:32768
EX v114,v114
MM a[56:59],a[144:147],v[24:27],a[56:59]
s_add_u32 s66, s87, 10240
RD a[200:203],v226 offset:32768
EX v115,v115
CV v90,v92,v93
MM a[60:63],a[152:155],v[24:27],a[60:63]
buffer_load_dwordx4 v240, s[24:27], s66 offen offset:0 lds
MM a[60:63],a[156:159],v[28:31],a[60:63]
s_add_u32 m0, s89, 8192
)ASM"
R"ASM(
RD a[204:207],v227 offset:32768
EX v116,v116
MM a[52:55],a[156:159],v[20:23],a[52:55]
s_add_u32 s66, s87, 81920
EX v117,v117
CV v91,v94,v95
MM a[52:55],a[152:155],v[16:19],a[52:55]
RD a[208:211],v224 offset:36864
RD a[212:215],v225 offset:36864
MM a[80:83],a[160:163],v[16:19],a[80:83]
buffer_load_dwordx4 v240, s[24:27], s66 offen offset:0 lds
MM a[80:83],a[164:167],v[20:23],a[80:83]
s_add_u32 m0, s89, 9216
EX v118,v118
MM a[88:91],a[164:167],v[28:31],a[88:91]
s_add_u32 s66, s87, 92160
EX v119,v119
CV v84,v112,v113
MM a[88:91],a[160:163],v[24:27],a[88:91]
RD a[216:219],v226 offset:36864
CV v85,v114,v115
EX v120,v120
MM a[92:95],a[168:171],v[24:27],a[92:95]
buffer_load_dwordx4 v240, s[24:27], s66 offen offset:0 lds
MM a[92:95],a[172:175],v[28:31],a[92:95]
s_add_u32 m0, s89, 65536
RD a[220:223],v227 offset:36864
EX v121,v121
MM a[84:87],a[172:175],v[20:23],a[84:87]
s_add_u32 s66, s88, 0
RD a[224:227],v224 offset:40960
CV v86,v116,v117
MM a[84:87],a[168:171],v[16:19],a[84:87]
EX v122,v122
MM a[112:115],a[176:179],v[16:19],a[112:115]
buffer_load_dwordx4 v240, s[24:27], s66 offen offset:0 lds
MM a[112:115],a[180:183],v[20:23],a[112:115]
s_add_u32 m0, s89, 66560
RD a[228:231],v225 offset:40960
EX v123,v123
MM a[120:123],a[180:183],v[28:31],a[120:123]
s_add_u32 s66, s88, 10240
RD a[232:235],v226 offset:40960
EX v124,v124
MM a[120:123],a[176:179],v[24:27],a[120:123]
buffer_load_dwordx4 v240, s[24:27], s66 offen offset:0 lds
MM a[124:127],a[184:187],v[24:27],a[124:127]
s_add_u32 m0, s89, 73728
RD a[236:239],v227 offset:40960
EX v125,v125
CV v87,v118,v119
MM a[124:127],a[188:191],v[28:31],a[124:127]
s_add_u32 s66, s88, 81920
EX v126,v126
MM a[116:119],a[188:191],v[20:23],a[116:119]
RD a[240:243],v224 offset:45056
RD a[244:247],v225 offset:45056
RD a[248:251],v226 offset:45056
RD a[252:255],v227 offset:45056
EX v127,v127
MM a[116:119],a[184:187],v[16:19],a[116:119]
LPG20_%=:
s_cbranch_execz LPG24_%=
buffer_load_dwordx4 v240, s[24:27], s66 offen offset:0 lds
v_smfmac_f32_16x16x64_bf16 v[236:239], v[152:155], v[80:87], v234
s_add_u32 m0, s89, 74752
CV v92,v120,v121
CV v93,v122,v123
CV v94,v124,v125
CV v95,v126,v127
s_nop 1
v_smfmac_f32_16x16x64_bf16 v[236:239], v[156:159], v[88:95], v234
s_waitcnt lgkmcnt(0)
s_add_u32 s66, s88, 92160
buffer_load_dwordx4 v240, s[24:27], s66 offen offset:0 lds
v_lshrrev_b32 v230, 9, v248
v_lshlrev_b32 v230, 9, v230
v_sub_u32 v244, v232, v230
v_sub_u32 v235, v233, v230
v_sub_u32 v244, v244, v230
v_sub_u32 v235, v235, v230
v_sub_u32 v228, v244, v230
v_sub_u32 v229, v235, v230
v_add_u32 v228, 65536, v228
v_add_u32 v229, 65536, v229
v_subrev_u32 v230, 65536, v244
v_subrev_u32 v231, 65536, v235
MM v[16:19],a[192:195],v[168:171],v[136:139]
TR a[128:129],v232 offset:16384
TR a[130:131],v232 offset:20480
MM v[16:19],a[196:199],v[184:187],v[16:19]
TR a[132:133],v232 offset:24576
TR a[134:135],v232 offset:28672
MM v[16:19],a[200:203],v[200:203],v[16:19]
TR a[136:137],v233 offset:16384
TR a[138:139],v233 offset:20480
MM v[16:19],a[204:207],v[216:219],v[16:19]
TR a[140:141],v233 offset:24576
v_add_f32 v247, v236, v237
MM v[20:23],a[220:223],v[216:219],v[136:139]
TR a[142:143],v233 offset:28672
v_cmp_nge_f32 vcc, s71, v247
MM v[20:23],a[216:219],v[200:203],v[20:23]
s_cbranch_vccnz LSITE33_%=
LBACK33_%=:
TR a[144:145],v232 offset:16896
TR a[146:147],v232 offset:20992
MM v[20:23],a[212:215],v[184:187],v[20:23]
TR a[148:149],v232 offset:25088
TR a[150:151],v232 offset:29184
MM v[20:23],a[208:211],v[168:171],v[20:23]
TR a[152:153],v233 offset:16896
TR a[154:155],v233 offset:20992
MM v[28:31],a[208:211],v[172:175],v[140:143]
EX v16,v16
MM v[28:31],a[212:215],v[188:191],v[28:31]
EX v17,v17
MM v[28:31],a[216:219],v[204:207],v[28:31]
s_lshl_b32 s97, s84, 2
s_add_u32 s97, s97, s96
s_add_u32 s97, s97, 0
MM v[28:31],a[220:223],v[220:223],v[28:31]
s_waitcnt vmcnt(8)
v_mov_b32 v242, s69
s_mov_b64 exec, 1
buffer_store_dword v242, off, s[20:23], s97 offset:0 sc1
s_mov_b64 exec, -1
MM v[24:27],a[204:207],v[220:223],v[140:143]
TR a[156:157],v233 offset:25088
TR a[158:159],v233 offset:29184
MM v[24:27],a[200:203],v[204:207],v[24:27]
EX v18,v18
MM v[24:27],a[196:199],v[188:191],v[24:27]
TR a[160:161],v232 offset:17408
TR a[162:163],v232 offset:21504
MM v[24:27],a[192:195],v[172:175],v[24:27]
EX v19,v19
MM v[48:51],a[224:227],v[168:171],v[136:139]
TR a[164:165],v232 offset:25600
TR a[166:167],v232 offset:29696
MM v[48:51],a[228:231],v[184:187],v[48:51]
EX v20,v20
MM v[48:51],a[232:235],v[200:203],v[48:51]
TR a[168:169],v233 offset:17408
TR a[170:171],v233 offset:21504
MM v[48:51],a[236:239],v[216:219],v[48:51]
EX v21,v21
MM v[52:55],a[252:255],v[216:219],v[136:139]
TR a[172:173],v233 offset:25600
CV v16,v16,v17
MM v[52:55],a[248:251],v[200:203],v[52:55]
TR a[174:175],v233 offset:29696
TR a[176:177],v232 offset:17920
MM v[52:55],a[244:247],v[184:187],v[52:55]
EX v22,v22
MM v[52:55],a[240:243],v[168:171],v[52:55]
TR a[178:179],v232 offset:22016
TR a[180:181],v232 offset:26112
EX v23,v23
MM v[60:63],a[240:243],v[172:175],v[140:143]
TR a[182:183],v232 offset:30208
CV v17,v18,v19
MM v[60:63],a[244:247],v[188:191],v[60:63]
EX v24,v24
MM v[60:63],a[248:251],v[204:207],v[60:63]
TR a[184:185],v233 offset:17920
TR a[186:187],v233 offset:22016
TR a[188:189],v233 offset:26112
TR a[190:191],v233 offset:30208
EX v25,v25
MM v[60:63],a[252:255],v[220:223],v[60:63]
EX v26,v26
MM v[56:59],a[236:239],v[220:223],v[140:143]
EX v27,v27
MM v[56:59],a[232:235],v[204:207],v[56:59]
EX v28,v28
MM v[56:59],a[228:231],v[188:191],v[56:59]
EX v29,v29
CV v18,v20,v21
MM v[56:59],a[224:227],v[172:175],v[56:59]
s_waitcnt lgkmcnt(0)
s_barrier
CV v19,v22,v23
MM a[16:19],a[128:131],v[80:83],a[16:19]
s_add_u32 m0, s89, 16384
EX v30,v30
MM a[16:19],a[132:135],v[84:87],a[16:19]
s_add_u32 s66, s85, 128
EX v31,v31
MM a[24:27],a[132:135],v[92:95],a[24:27]
buffer_load_dwordx4 v240, s[24:27], s66 offen offset:0 lds
MM a[24:27],a[128:131],v[88:91],a[24:27]
s_add_u32 m0, s89, 17408
EX v48,v48
MM a[28:31],a[136:139],v[88:91],a[28:31]
s_add_u32 s66, s85, 10368
CV v24,v24,v25
CV v25,v26,v27
MM a[28:31],a[140:143],v[92:95],a[28:31]
CV v26,v28,v29
CV v27,v30,v31
MM a[20:23],a[140:143],v[84:87],a[20:23]
EX v49,v49
MM a[20:23],a[136:139],v[80:83],a[20:23]
EX v50,v50
MM a[48:51],a[144:147],v[80:83],a[48:51]
buffer_load_dwordx4 v240, s[24:27], s66 offen offset:0 lds
MM a[48:51],a[148:151],v[84:87],a[48:51]
s_add_u32 m0, s89, 24576
EX v51,v51
MM a[56:59],a[148:151],v[92:95],a[56:59]
s_add_u32 s66, s85, 82048
TR a[128:129],v232 offset:32768
CV v20,v48,v49
MM a[56:59],a[144:147],v[88:91],a[56:59]
EX v52,v52
MM a[60:63],a[152:155],v[88:91],a[60:63]
TR a[130:131],v232 offset:36864
TR a[132:133],v232 offset:40960
MM a[60:63],a[156:159],v[92:95],a[60:63]
EX v53,v53
MM a[52:55],a[156:159],v[84:87],a[52:55]
TR a[134:135],v232 offset:45056
TR a[136:137],v233 offset:32768
MM a[52:55],a[152:155],v[80:83],a[52:55]
buffer_load_dwordx4 v240, s[24:27], s66 offen offset:0 lds
MM a[80:83],a[160:163],v[80:83],a[80:83]
s_add_u32 m0, s89, 25600
TR a[138:139],v233 offset:36864
TR a[140:141],v233 offset:40960
EX v54,v54
MM a[80:83],a[164:167],v[84:87],a[80:83]
s_add_u32 s66, s85, 92288
TR a[142:143],v233 offset:45056
TR a[144:145],v232 offset:33280
MM a[88:91],a[164:167],v[92:95],a[88:91]
EX v55,v55
MM a[88:91],a[160:163],v[88:91],a[88:91]
TR a[146:147],v232 offset:37376
TR a[148:149],v232 offset:41472
EX v56,v56
MM a[92:95],a[168:171],v[88:91],a[92:95]
buffer_load_dwordx4 v240, s[24:27], s66 offen offset:0 lds
MM a[92:95],a[172:175],v[92:95],a[92:95]
s_add_u32 m0, s89, 81920
TR a[150:151],v232 offset:45568
TR a[152:153],v233 offset:33280
EX v57,v57
CV v21,v50,v51
MM a[84:87],a[172:175],v[84:87],a[84:87]
s_add_u32 s66, s86, 128
TR a[154:155],v233 offset:37376
TR a[156:157],v233 offset:41472
MM a[84:87],a[168:171],v[80:83],a[84:87]
TR a[158:159],v233 offset:45568
EX v58,v58
MM a[112:115],a[176:179],v[80:83],a[112:115]
buffer_load_dwordx4 v240, s[24:27], s66 offen offset:0 lds
MM a[112:115],a[180:183],v[84:87],a[112:115]
s_add_u32 m0, s89, 82944
TR a[160:161],v232 offset:33792
TR a[162:163],v232 offset:37888
TR a[164:165],v232 offset:41984
EX v59,v59
CV v22,v52,v53
MM a[120:123],a[180:183],v[92:95],a[120:123]
s_add_u32 s66, s86, 10368
TR a[166:167],v232 offset:46080
CV v23,v54,v55
EX v60,v60
MM a[120:123],a[176:179],v[88:91],a[120:123]
TR a[168:169],v233 offset:33792
TR a[170:171],v233 offset:37888
MM a[124:127],a[184:187],v[88:91],a[124:127]
TR a[172:173],v233 offset:41984
v_lshrrev_b32 v243, 2, v248
v_min_u32 v243, 12, v243
EX v61,v61
MM a[124:127],a[188:191],v[92:95],a[124:127]
buffer_load_dword v242, v243, s[20:23], s96 offen offset:0 sc1
MM a[116:119],a[188:191],v[84:87],a[116:119]
TR a[174:175],v233 offset:46080
TR a[176:177],v232 offset:34304
TR a[178:179],v232 offset:38400
EX v62,v62
EX v63,v63
MM a[116:119],a[184:187],v[80:83],a[116:119]
buffer_load_dwordx4 v240, s[24:27], s66 offen offset:0 lds
v_smfmac_f32_16x16x64_bf16 v[236:239], v[152:155], v[16:23], v234
s_add_u32 m0, s89, 90112
TR a[180:181],v232 offset:42496
TR a[182:183],v232 offset:46592
CV v28,v56,v57
CV v29,v58,v59
CV v30,v60,v61
CV v31,v62,v63
s_nop 1
v_smfmac_f32_16x16x64_bf16 v[236:239], v[156:159], v[24:31], v234
TR a[184:185],v233 offset:34304
TR a[186:187],v233 offset:38400
TR a[188:189],v233 offset:42496
TR a[190:191],v233 offset:46592
s_waitcnt lgkmcnt(0)
s_add_u32 s66, s86, 82048
buffer_load_dwordx4 v240, s[24:27], s66 offen offset:0 lds
s_add_u32 m0, s89, 91136
s_add_u32 s66, s86, 92288
buffer_load_dwordx4 v240, s[24:27], s66 offen offset:0 lds
s_nop 6
v_add_f32 v246, v236, v237
v_cmp_nge_f32 vcc, s71, v246
s_cbranch_vccnz LSITE34_%=
LBACK34_%=:
s_waitcnt lgkmcnt(0)
MM a[16:19],a[128:131],v[16:19],a[16:19]
s_waitcnt vmcnt(10)
s_barrier
MM a[16:19],a[132:135],v[20:23],a[16:19]
v_rcp_f32 v152, v246
MM a[24:27],a[132:135],v[28:31],a[24:27]
v_mov_b32 v153, v152
MM a[24:27],a[128:131],v[24:27],a[24:27]
s_nop 0
v_permlane32_swap_b32 v152, v153
MM a[28:31],a[136:139],v[24:27],a[28:31]
v_mov_b32 v157, v153
MM a[28:31],a[140:143],v[28:31],a[28:31]
s_nop 0
v_permlane16_swap_b32 v153, v157
MM a[20:23],a[140:143],v[20:23],a[20:23]
s_mul_i32 s78, s49, 17408
s_add_u32 s78, s83, s78
s_add_u32 s78, s78, 8192
MM a[20:23],a[136:139],v[16:19],a[20:23]
s_add_u32 s79, s78, 4096
MM a[48:51],a[144:147],v[16:19],a[48:51]
TR a[128:129],v230 offset:49152
TR a[130:131],v230 offset:51200
MM a[48:51],a[148:151],v[20:23],a[48:51]
MM a[56:59],a[148:151],v[28:31],a[56:59]
TR a[132:133],v230 offset:53248
TR a[134:135],v230 offset:55296
MM a[56:59],a[144:147],v[24:27],a[56:59]
AR v80,a16
AR v81,a17
MM a[60:63],a[152:155],v[24:27],a[60:63]
TR a[136:137],v230 offset:57344
TR a[138:139],v230 offset:59392
MM a[60:63],a[156:159],v[28:31],a[60:63]
AR v82,a18
AR v83,a19
MM a[52:55],a[156:159],v[20:23],a[52:55]
TR a[140:141],v230 offset:61440
TR a[142:143],v230 offset:63488
MM a[52:55],a[152:155],v[16:19],a[52:55]
AR v84,a20
AR v85,a21
MM a[80:83],a[160:163],v[16:19],a[80:83]
TR a[144:145],v231 offset:49152
TR a[146:147],v231 offset:51200
MM a[80:83],a[164:167],v[20:23],a[80:83]
AR v86,a22
AR v87,a23
MM a[88:91],a[164:167],v[28:31],a[88:91]
TR a[148:149],v231 offset:53248
TR a[150:151],v231 offset:55296
MM a[88:91],a[160:163],v[24:27],a[88:91]
s_waitcnt vmcnt(3)
v_cmp_ne_u32 s[74:75], s69, v242
v_mul_f32 v80, v80, v153
MM a[92:95],a[168:171],v[24:27],a[92:95]
TR a[152:153],v231 offset:57344
TR a[154:155],v231 offset:59392
v_mul_f32 v81, v81, v153
v_mul_f32 v82, v82, v153
MM a[92:95],a[172:175],v[28:31],a[92:95]
s_cmp_lg_u64 s[74:75], 0
s_cbranch_scc1 LRPO0_%=
LRPO0b_%=:
s_xor_b32 s92, s49, 1
v_mul_f32 v83, v83, v153
MM a[84:87],a[172:175],v[20:23],a[84:87]
s_mul_i32 s92, s92, 17408
TR a[156:157],v231 offset:61440
TR a[158:159],v231 offset:63488
v_mul_f32 v84, v84, v153
v_mul_f32 v85, v85, v153
MM a[84:87],a[168:171],v[16:19],a[84:87]
s_add_u32 s92, s83, s92
v_mul_f32 v86, v86, v153
v_mul_f32 v87, v87, v153
MM a[112:115],a[176:179],v[16:19],a[112:115]
TR a[160:161],v230 offset:49664
TR a[162:163],v230 offset:51712
CV v168,v80,v81
)ASM"
R"ASM(
MM a[112:115],a[180:183],v[20:23],a[112:115]
buffer_load_dwordx4 a[0:3], v248, s[20:23], s92 offen offset:0 sc1
MM a[120:123],a[180:183],v[28:31],a[120:123]
TR a[164:165],v230 offset:53760
TR a[166:167],v230 offset:55808
CV v169,v82,v83
CV v170,v84,v85
CV v171,v86,v87
MM a[120:123],a[176:179],v[24:27],a[120:123]
buffer_load_dwordx4 a[32:35], v248, s[20:23], s92 offen offset:1024 sc1
MM a[124:127],a[184:187],v[24:27],a[124:127]
TR a[168:169],v230 offset:57856
TR a[170:171],v230 offset:59904
MM a[124:127],a[188:191],v[28:31],a[124:127]
buffer_load_dwordx4 a[64:67], v248, s[20:23], s92 offen offset:2048 sc1
MM a[116:119],a[188:191],v[20:23],a[116:119]
TR a[172:173],v230 offset:61952
TR a[174:175],v230 offset:64000
MM a[116:119],a[184:187],v[16:19],a[116:119]
buffer_load_dwordx4 a[96:99], v248, s[20:23], s92 offen offset:3072 sc1
s_waitcnt lgkmcnt(15)
MM v[0:3],a[128:131],v[160:163],0
TR a[176:177],v231 offset:49664
TR a[178:179],v231 offset:51712
MM v[0:3],a[132:135],v[176:179],v[0:3]
s_add_u32 s92, s92, 4096
v_bfe_u32 v249, v248, 6, 2
v_lshlrev_b32 v249, 8, v249
MM v[0:3],a[136:139],v[192:195],v[0:3]
buffer_store_dwordx4 v[168:171], v248, s[20:23], s78 offen offset:0 sc1
TR a[180:181],v231 offset:53760
TR a[182:183],v231 offset:55808
AR v88,a24
AR v89,a25
AR v90,a26
AR v91,a27
AR v92,a28
AR v93,a29
MM v[0:3],a[140:143],v[208:211],v[0:3]
v_lshrrev_b32 v250, 8, v248
v_lshl_add_u32 v249, v250, 6, v249
AR v94,a30
AR v95,a31
v_mul_f32 v88, v88, v157
v_mul_f32 v89, v89, v157
v_mul_f32 v90, v90, v157
v_mul_f32 v91, v91, v157
v_mul_f32 v92, v92, v157
MM v[8:11],a[140:143],v[212:215],0
TR a[184:185],v231 offset:57856
TR a[186:187],v231 offset:59904
v_mul_f32 v93, v93, v157
v_mul_f32 v94, v94, v157
MM v[8:11],a[136:139],v[196:199],v[8:11]
v_and_b32 v251, 48, v248
v_add_u32 v249, v249, v251
v_mul_f32 v95, v95, v157
CV v172,v88,v89
MM v[8:11],a[132:135],v[180:183],v[8:11]
s_lshr_b32 s45, s49, 3
TR a[188:189],v231 offset:61952
TR a[190:191],v231 offset:64000
CV v173,v90,v91
MM v[8:11],a[128:131],v[164:167],v[8:11]
s_waitcnt lgkmcnt(15)
buffer_load_dwordx4 a[4:7], v248, s[20:23], s92 offen offset:0 sc1
MM v[4:7],a[144:147],v[160:163],0
TR a[128:129],v228 offset:0
TR a[130:131],v228 offset:1024
CV v174,v92,v93
CV v175,v94,v95
v_bfe_u32 v245, v248, 6, 2
MM v[4:7],a[148:151],v[176:179],v[4:7]
buffer_load_dwordx4 a[36:39], v248, s[20:23], s92 offen offset:1024 sc1
MM v[4:7],a[152:155],v[192:195],v[4:7]
TR a[132:133],v228 offset:2048
TR a[134:135],v228 offset:3072
MM v[4:7],a[156:159],v[208:211],v[4:7]
v_xor_b32 v251, s45, v245
v_add_u32 v251, -1, v251
MM v[12:15],a[156:159],v[212:215],0
buffer_store_dwordx4 v[172:175], v248, s[20:23], s79 offen offset:0 sc1
TR a[136:137],v228 offset:4096
TR a[138:139],v228 offset:5120
MM v[12:15],a[152:155],v[196:199],v[12:15]
v_and_b32 v251, 0x40000000, v251
v_lshlrev_b32 v245, 6, v245
AR v112,a48
AR v113,a49
AR v114,a50
AR v115,a51
AR v116,a52
AR v117,a53
AR v118,a54
AR v119,a55
MM v[12:15],a[148:151],v[180:183],v[12:15]
TR a[140:141],v228 offset:6144
TR a[142:143],v228 offset:7168
v_mul_f32 v112, v112, v153
MM v[12:15],a[144:147],v[164:167],v[12:15]
s_waitcnt lgkmcnt(15)
v_lshl_add_u32 v245, s45, 4, v245
v_mov_b32 v234, v249
v_mul_f32 v113, v113, v153
v_mul_f32 v114, v114, v153
MM v[32:35],a[160:163],v[160:163],0
TR a[144:145],v229 offset:0
TR a[146:147],v229 offset:1024
v_mul_f32 v115, v115, v153
v_mul_f32 v116, v116, v153
MM v[32:35],a[164:167],v[176:179],v[32:35]
v_add_u32 v249, v249, v251
v_bfe_u32 v251, v248, 4, 2
v_mul_f32 v117, v117, v153
MM v[32:35],a[168:171],v[192:195],v[32:35]
TR a[148:149],v229 offset:2048
TR a[150:151],v229 offset:3072
v_mul_f32 v118, v118, v153
v_mul_f32 v119, v119, v153
MM v[32:35],a[172:175],v[208:211],v[32:35]
buffer_load_dwordx4 a[68:71], v248, s[20:23], s92 offen offset:2048 sc1
MM v[40:43],a[172:175],v[212:215],0
TR a[152:153],v229 offset:4096
TR a[154:155],v229 offset:5120
CV v184,v112,v113
CV v185,v114,v115
CV v186,v116,v117
v_lshl_add_u32 v245, v251, 2, v245
MM v[40:43],a[168:171],v[196:199],v[40:43]
s_lshl_b32 s47, %[sb], 10
s_and_b32 s47, s47, 1024
s_add_u32 s46, s95, s47
CV v187,v118,v119
MM v[40:43],a[164:167],v[180:183],v[40:43]
s_sub_u32 s47, s95, s47
TR a[156:157],v229 offset:6144
TR a[158:159],v229 offset:7168
MM v[40:43],a[160:163],v[164:167],v[40:43]
s_waitcnt lgkmcnt(15)
s_bitcmp1_b32 s49, 4
s_cselect_b32 s44, -1, 1
s_waitcnt vmcnt(9)
s_barrier
MM v[36:39],a[176:179],v[160:163],0
s_mul_i32 s44, s44, 331776
TR a[160:161],v230 offset:16384
TR a[162:163],v230 offset:18432
MM v[36:39],a[180:183],v[176:179],v[36:39]
buffer_load_dwordx4 a[100:103], v248, s[20:23], s92 offen offset:3072 sc1
MM v[36:39],a[184:187],v[192:195],v[36:39]
s_add_u32 s44, s44, s16
TR a[164:165],v230 offset:20480
TR a[166:167],v230 offset:22528
MM v[36:39],a[188:191],v[208:211],v[36:39]
s_xor_b32 s45, s68, 64
buffer_store_dwordx4 v[184:187], v248, s[20:23], s78 offen offset:1024 sc1
AR v120,a56
AR v121,a57
AR v122,a58
MM v[44:47],a[188:191],v[212:215],0
s_bfe_u32 s80, s49, 131074
TR a[168:169],v230 offset:24576
TR a[170:171],v230 offset:26624
v_xor_b32 v250, s80, v250
v_mul_u32_u24 v250, 82944, v250
v_and_b32 v252, 240, v248
v_add_u32 v250, v250, v252
v_lshrrev_b32 v251, 8, v248
v_mul_u32_u24 v251, 1280, v251
v_bfe_u32 v252, v248, 4, 3
v_lshl_add_u32 v251, v252, 3, v251
v_bfe_u32 v252, v248, 7, 1
v_mul_u32_u24 v254, 40960, v252
v_add_u32 v254, v254, v251
AR v123,a59
v_lshl_add_u32 v251, v252, 6, v251
AR v124,a60
v_lshrrev_b32 v253, 8, v248
AR v125,a61
v_and_b32 v252, 240, v248
AR v126,a62
v_lshl_add_u32 v232, v253, 2, v252
AR v127,a63
v_xor_b32 v253, s80, v253
v_mul_f32 v120, v120, v157
v_lshlrev_b32 v253, 4, v253
v_mul_f32 v121, v121, v157
v_mov_b32 v233, s69
MM v[44:47],a[184:187],v[196:199],v[44:47]
s_add_u32 s80, s18, 131072
v_mul_f32 v122, v122, v157
v_mul_f32 v123, v123, v157
MM v[44:47],a[180:183],v[180:183],v[44:47]
TR a[172:173],v230 offset:28672
TR a[174:175],v230 offset:30720
v_mul_f32 v124, v124, v157
MM v[44:47],a[176:179],v[164:167],v[44:47]
s_waitcnt lgkmcnt(15)
v_mul_f32 v125, v125, v157
v_mul_f32 v126, v126, v157
MM v[128:131],a[128:131],v[160:163],0
TR a[176:177],v231 offset:16384
TR a[178:179],v231 offset:18432
v_mul_f32 v127, v127, v157
CV v188,v120,v121
MM v[128:131],a[132:135],v[176:179],v[128:131]
buffer_load_dwordx2 v[224:225], v251, s[20:23], s80 offen offset:0 sc1
MM v[128:131],a[136:139],v[192:195],v[128:131]
TR a[180:181],v231 offset:20480
TR a[182:183],v231 offset:22528
CV v189,v122,v123
CV v190,v124,v125
CV v191,v126,v127
MM v[128:131],a[140:143],v[208:211],v[128:131]
s_add_u32 s80, s18, 131200
MM v[136:139],a[140:143],v[212:215],0
TR a[184:185],v231 offset:24576
TR a[186:187],v231 offset:26624
MM v[136:139],a[136:139],v[196:199],v[136:139]
buffer_load_dwordx2 v[226:227], v251, s[20:23], s80 offen offset:0 sc1
MM v[136:139],a[132:135],v[180:183],v[136:139]
TR a[188:189],v231 offset:28672
TR a[190:191],v231 offset:30720
MM v[136:139],a[128:131],v[164:167],v[136:139]
s_waitcnt lgkmcnt(15)
s_add_u32 s80, s18, 172032
MM v[132:135],a[144:147],v[160:163],0
buffer_load_dwordx2 v[236:237], v251, s[20:23], s80 offen offset:0 sc1
buffer_store_dwordx4 v[188:191], v248, s[20:23], s79 offen offset:1024 sc1
TR a[128:129],v230 offset:16896
TR a[130:131],v230 offset:18944
AR v80,a80
AR v81,a81
MM v[132:135],a[148:151],v[176:179],v[132:135]
AR v82,a82
AR v83,a83
AR v84,a84
AR v85,a85
AR v86,a86
AR v87,a87
v_mul_f32 v80, v80, v153
MM v[132:135],a[152:155],v[192:195],v[132:135]
s_add_u32 s80, s18, 172160
TR a[132:133],v230 offset:20992
TR a[134:135],v230 offset:23040
v_mul_f32 v81, v81, v153
v_mul_f32 v82, v82, v153
MM v[132:135],a[156:159],v[208:211],v[132:135]
buffer_load_dwordx2 v[238:239], v251, s[20:23], s80 offen offset:0 sc1
MM v[140:143],a[156:159],v[212:215],0
TR a[136:137],v230 offset:25088
TR a[138:139],v230 offset:27136
v_mul_f32 v83, v83, v153
v_mul_f32 v84, v84, v153
v_mul_f32 v85, v85, v153
MM v[140:143],a[152:155],v[196:199],v[140:143]
s_add_u32 s80, s63, 131072
v_mul_f32 v86, v86, v153
LPG21_%=:
s_cbranch_execz LTR3_%=
v_mul_f32 v87, v87, v153
MM v[140:143],a[148:151],v[180:183],v[140:143]
buffer_load_dwordx2 v[246:247], v254, s[20:23], s80 offen offset:0 sc1
TR a[140:141],v230 offset:29184
TR a[142:143],v230 offset:31232
MM v[140:143],a[144:147],v[164:167],v[140:143]
s_waitcnt lgkmcnt(15)
CV v200,v80,v81
CV v201,v82,v83
CV v202,v84,v85
MM v[64:67],a[160:163],v[160:163],0
TR a[144:145],v231 offset:16896
TR a[146:147],v231 offset:18944
CV v203,v86,v87
MM v[64:67],a[164:167],v[176:179],v[64:67]
MM v[64:67],a[168:171],v[192:195],v[64:67]
TR a[148:149],v231 offset:20992
TR a[150:151],v231 offset:23040
MM v[64:67],a[172:175],v[208:211],v[64:67]
MM v[72:75],a[172:175],v[212:215],0
TR a[152:153],v231 offset:25088
TR a[154:155],v231 offset:27136
MM v[72:75],a[168:171],v[196:199],v[72:75]
buffer_store_dwordx4 v[200:203], v248, s[20:23], s78 offen offset:2048 sc1
AR v88,a88
MM v[72:75],a[164:167],v[180:183],v[72:75]
TR a[156:157],v231 offset:29184
TR a[158:159],v231 offset:31232
AR v89,a89
AR v90,a90
AR v91,a91
AR v92,a92
AR v93,a93
AR v94,a94
AR v95,a95
v_mul_f32 v88, v88, v157
MM v[72:75],a[160:163],v[164:167],v[72:75]
s_waitcnt lgkmcnt(15)
v_mul_f32 v89, v89, v157
v_mul_f32 v90, v90, v157
MM v[68:71],a[176:179],v[160:163],0
TR a[160:161],v244 offset:49152
TR a[162:163],v244 offset:51200
v_mul_f32 v91, v91, v157
MM v[68:71],a[180:183],v[176:179],v[68:71]
v_mul_f32 v92, v92, v157
v_mul_f32 v93, v93, v157
MM v[68:71],a[184:187],v[192:195],v[68:71]
TR a[164:165],v244 offset:53248
TR a[166:167],v244 offset:55296
v_mul_f32 v94, v94, v157
MM v[68:71],a[188:191],v[208:211],v[68:71]
v_mul_f32 v95, v95, v157
CV v204,v88,v89
MM v[76:79],a[188:191],v[212:215],0
TR a[168:169],v244 offset:57344
TR a[170:171],v244 offset:59392
CV v205,v90,v91
CV v206,v92,v93
MM v[76:79],a[184:187],v[196:199],v[76:79]
CV v207,v94,v95
MM v[76:79],a[180:183],v[180:183],v[76:79]
TR a[172:173],v244 offset:61440
TR a[174:175],v244 offset:63488
MM v[76:79],a[176:179],v[164:167],v[76:79]
s_waitcnt lgkmcnt(15)
MM v[96:99],a[128:131],v[160:163],0
s_xor_b32 s92, s49, 2
TR a[176:177],v235 offset:49152
TR a[178:179],v235 offset:51200
MM v[96:99],a[132:135],v[176:179],v[96:99]
s_mul_i32 s92, s92, 17408
s_add_u32 s92, s83, s92
MM v[96:99],a[136:139],v[192:195],v[96:99]
buffer_store_dwordx4 v[204:207], v248, s[20:23], s79 offen offset:2048 sc1
TR a[180:181],v235 offset:53248
TR a[182:183],v235 offset:55296
MM v[96:99],a[140:143],v[208:211],v[96:99]
buffer_load_dwordx4 a[8:11], v248, s[20:23], s92 offen offset:0 sc1
AR v112,a112
AR v113,a113
MM v[104:107],a[140:143],v[212:215],0
TR a[184:185],v235 offset:57344
TR a[186:187],v235 offset:59392
AR v114,a114
AR v115,a115
AR v116,a116
AR v117,a117
AR v118,a118
AR v119,a119
v_mul_f32 v112, v112, v153
v_mul_f32 v113, v113, v153
MM v[104:107],a[136:139],v[196:199],v[104:107]
buffer_load_dwordx4 a[40:43], v248, s[20:23], s92 offen offset:1024 sc1
MM v[104:107],a[132:135],v[180:183],v[104:107]
TR a[188:189],v235 offset:61440
TR a[190:191],v235 offset:63488
v_mul_f32 v114, v114, v153
v_mul_f32 v115, v115, v153
v_mul_f32 v116, v116, v153
MM v[104:107],a[128:131],v[164:167],v[104:107]
s_waitcnt lgkmcnt(15)
v_mul_f32 v117, v117, v153
v_mul_f32 v118, v118, v153
MM v[100:103],a[144:147],v[160:163],0
TR a[128:129],v244 offset:49664
TR a[130:131],v244 offset:51712
v_mul_f32 v119, v119, v153
MM v[100:103],a[148:151],v[176:179],v[100:103]
buffer_load_dwordx4 a[72:75], v248, s[20:23], s92 offen offset:2048 sc1
MM v[100:103],a[152:155],v[192:195],v[100:103]
TR a[132:133],v244 offset:53760
TR a[134:135],v244 offset:55808
CV v216,v112,v113
CV v217,v114,v115
CV v218,v116,v117
MM v[100:103],a[156:159],v[208:211],v[100:103]
buffer_load_dwordx4 a[104:107], v248, s[20:23], s92 offen offset:3072 sc1
MM v[108:111],a[156:159],v[212:215],0
s_add_u32 s92, s92, 4096
TR a[136:137],v244 offset:57856
TR a[138:139],v244 offset:59904
CV v219,v118,v119
MM v[108:111],a[152:155],v[196:199],v[108:111]
MM v[108:111],a[148:151],v[180:183],v[108:111]
TR a[140:141],v244 offset:61952
)ASM"
R"ASM(
TR a[142:143],v244 offset:64000
MM v[108:111],a[144:147],v[164:167],v[108:111]
s_waitcnt lgkmcnt(15)
buffer_load_dwordx4 a[12:15], v248, s[20:23], s92 offen offset:0 sc1
s_waitcnt vmcnt(20)
MM v[0:3],a[160:163],a[0:3],v[0:3]
TR a[144:145],v235 offset:49664
TR a[146:147],v235 offset:51712
MM v[0:3],a[164:167],a[32:35],v[0:3]
buffer_load_dwordx4 a[44:47], v248, s[20:23], s92 offen offset:1024 sc1
MM v[0:3],a[168:171],a[64:67],v[0:3]
TR a[148:149],v235 offset:53760
TR a[150:151],v235 offset:55808
MM v[0:3],a[172:175],a[96:99],v[0:3]
s_waitcnt vmcnt(15)
MM v[8:11],a[172:175],a[100:103],v[8:11]
buffer_store_dwordx4 v[216:219], v248, s[20:23], s78 offen offset:3072 sc1
TR a[152:153],v235 offset:57856
TR a[154:155],v235 offset:59904
AR v120,a120
AR v121,a121
AR v122,a122
AR v123,a123
AR v124,a124
AR v125,a125
AR v126,a126
AR v127,a127
MM v[8:11],a[168:171],a[68:71],v[8:11]
v_mul_f32 v120, v120, v157
v_mul_f32 v121, v121, v157
v_mul_f32 v122, v122, v157
v_mul_f32 v123, v123, v157
v_mul_f32 v124, v124, v157
v_mul_f32 v125, v125, v157
v_mul_f32 v126, v126, v157
MM v[8:11],a[164:167],a[36:39],v[8:11]
TR a[156:157],v235 offset:61952
TR a[158:159],v235 offset:64000
v_mul_f32 v127, v127, v157
CV v220,v120,v121
MM v[8:11],a[160:163],a[4:7],v[8:11]
s_waitcnt lgkmcnt(15)
buffer_load_dwordx4 a[76:79], v248, s[20:23], s92 offen offset:2048 sc1
MM v[4:7],a[176:179],a[0:3],v[4:7]
TR a[160:161],v228 offset:8192
TR a[162:163],v228 offset:9216
CV v221,v122,v123
CV v222,v124,v125
CV v223,v126,v127
MM v[4:7],a[180:183],a[32:35],v[4:7]
MM v[4:7],a[184:187],a[64:67],v[4:7]
TR a[164:165],v228 offset:10240
TR a[166:167],v228 offset:11264
MM v[4:7],a[188:191],a[96:99],v[4:7]
buffer_load_dwordx4 a[108:111], v248, s[20:23], s92 offen offset:3072 sc1
MM v[12:15],a[188:191],a[100:103],v[12:15]
TR a[168:169],v228 offset:12288
TR a[170:171],v228 offset:13312
MM v[12:15],a[184:187],a[68:71],v[12:15]
buffer_store_dwordx4 v[220:223], v248, s[20:23], s79 offen offset:3072 sc1
MM v[12:15],a[180:183],a[36:39],v[12:15]
TR a[172:173],v228 offset:14336
TR a[174:175],v228 offset:15360
MM v[12:15],a[176:179],a[4:7],v[12:15]
s_waitcnt lgkmcnt(15)
MM v[32:35],a[128:131],a[0:3],v[32:35]
TR a[176:177],v229 offset:8192
TR a[178:179],v229 offset:9216
MM v[32:35],a[132:135],a[32:35],v[32:35]
MM v[32:35],a[136:139],a[64:67],v[32:35]
TR a[180:181],v229 offset:10240
TR a[182:183],v229 offset:11264
MM v[32:35],a[140:143],a[96:99],v[32:35]
MM v[40:43],a[140:143],a[100:103],v[40:43]
TR a[184:185],v229 offset:12288
TR a[186:187],v229 offset:13312
MM v[40:43],a[136:139],a[68:71],v[40:43]
MM v[40:43],a[132:135],a[36:39],v[40:43]
TR a[188:189],v229 offset:14336
TR a[190:191],v229 offset:15360
MM v[40:43],a[128:131],a[4:7],v[40:43]
s_waitcnt lgkmcnt(15)
MM v[36:39],a[144:147],a[0:3],v[36:39]
s_waitcnt vmcnt(12)
TR a[128:129],v230 offset:0
TR a[130:131],v230 offset:2048
MM v[36:39],a[148:151],a[32:35],v[36:39]
v_lshlrev_b32 v112, 16, v224
v_and_b32 v113, 0xffff0000, v224
MM v[36:39],a[152:155],a[64:67],v[36:39]
TR a[132:133],v230 offset:4096
TR a[134:135],v230 offset:6144
MM v[36:39],a[156:159],a[96:99],v[36:39]
v_lshlrev_b32 v114, 16, v225
v_and_b32 v115, 0xffff0000, v225
MM v[44:47],a[156:159],a[100:103],v[44:47]
TR a[136:137],v230 offset:8192
TR a[138:139],v230 offset:10240
MM v[44:47],a[152:155],a[68:71],v[44:47]
v_mul_f32 v116, 0xbfb8aa3b, v112
v_mul_f32 v117, 0xbfb8aa3b, v113
MM v[44:47],a[148:151],a[36:39],v[44:47]
TR a[140:141],v230 offset:12288
TR a[142:143],v230 offset:14336
MM v[44:47],a[144:147],a[4:7],v[44:47]
s_waitcnt lgkmcnt(15)
v_mul_f32 v118, 0xbfb8aa3b, v114
v_mul_f32 v119, 0xbfb8aa3b, v115
MM v[128:131],a[160:163],a[0:3],v[128:131]
TR a[144:145],v231 offset:0
TR a[146:147],v231 offset:2048
MM v[128:131],a[164:167],a[32:35],v[128:131]
EX v116,v116
MM v[128:131],a[168:171],a[64:67],v[128:131]
TR a[148:149],v231 offset:4096
TR a[150:151],v231 offset:6144
MM v[128:131],a[172:175],a[96:99],v[128:131]
EX v117,v117
MM v[136:139],a[172:175],a[100:103],v[136:139]
TR a[152:153],v231 offset:8192
TR a[154:155],v231 offset:10240
MM v[136:139],a[168:171],a[68:71],v[136:139]
EX v118,v118
MM v[136:139],a[164:167],a[36:39],v[136:139]
TR a[156:157],v231 offset:12288
TR a[158:159],v231 offset:14336
MM v[136:139],a[160:163],a[4:7],v[136:139]
s_waitcnt lgkmcnt(15)
EX v119,v119
MM v[132:135],a[176:179],a[0:3],v[132:135]
s_xor_b32 s92, s49, 3
TR a[160:161],v230 offset:512
TR a[162:163],v230 offset:2560
MM v[132:135],a[180:183],a[32:35],v[132:135]
s_mul_i32 s92, s92, 17408
s_add_u32 s92, s83, s92
v_add_f32 v116, 1.0, v116
MM v[132:135],a[184:187],a[64:67],v[132:135]
TR a[164:165],v230 offset:4608
TR a[166:167],v230 offset:6656
MM v[132:135],a[188:191],a[96:99],v[132:135]
buffer_load_dwordx4 a[192:195], v248, s[20:23], s92 offen offset:0 sc1
MM v[140:143],a[188:191],a[100:103],v[140:143]
TR a[168:169],v230 offset:8704
TR a[170:171],v230 offset:10752
MM v[140:143],a[184:187],a[68:71],v[140:143]
buffer_load_dwordx4 a[208:211], v248, s[20:23], s92 offen offset:1024 sc1
MM v[140:143],a[180:183],a[36:39],v[140:143]
TR a[172:173],v230 offset:12800
TR a[174:175],v230 offset:14848
MM v[140:143],a[176:179],a[4:7],v[140:143]
s_waitcnt lgkmcnt(15)
buffer_load_dwordx4 a[224:227], v248, s[20:23], s92 offen offset:2048 sc1
s_waitcnt vmcnt(9)
MM v[0:3],a[128:131],a[8:11],v[0:3]
TR a[176:177],v231 offset:512
TR a[178:179],v231 offset:2560
MM v[0:3],a[132:135],a[40:43],v[0:3]
buffer_load_dwordx4 a[240:243], v248, s[20:23], s92 offen offset:3072 sc1
MM v[0:3],a[136:139],a[72:75],v[0:3]
s_add_u32 s92, s92, 4096
TR a[180:181],v231 offset:4608
TR a[182:183],v231 offset:6656
MM v[0:3],a[140:143],a[104:107],v[0:3]
s_waitcnt vmcnt(5)
v_add_f32 v117, 1.0, v117
v_add_f32 v118, 1.0, v118
MM v[8:11],a[140:143],a[108:111],v[8:11]
TR a[184:185],v231 offset:8704
TR a[186:187],v231 offset:10752
MM v[8:11],a[136:139],a[76:79],v[8:11]
buffer_load_dwordx4 a[196:199], v248, s[20:23], s92 offen offset:0 sc1
MM v[8:11],a[132:135],a[44:47],v[8:11]
TR a[188:189],v231 offset:12800
TR a[190:191],v231 offset:14848
MM v[8:11],a[128:131],a[12:15],v[8:11]
s_waitcnt lgkmcnt(15)
buffer_load_dwordx4 a[212:215], v248, s[20:23], s92 offen offset:1024 sc1
MM v[4:7],a[144:147],a[8:11],v[4:7]
TR a[128:129],v228 offset:16384
TR a[130:131],v228 offset:17408
MM v[4:7],a[148:151],a[40:43],v[4:7]
v_add_f32 v119, 1.0, v119
MM v[4:7],a[152:155],a[72:75],v[4:7]
TR a[132:133],v228 offset:18432
TR a[134:135],v228 offset:19456
MM v[4:7],a[156:159],a[104:107],v[4:7]
buffer_load_dwordx4 a[228:231], v248, s[20:23], s92 offen offset:2048 sc1
MM v[12:15],a[156:159],a[108:111],v[12:15]
TR a[136:137],v228 offset:20480
TR a[138:139],v228 offset:21504
MM v[12:15],a[152:155],a[76:79],v[12:15]
buffer_load_dwordx4 a[244:247], v248, s[20:23], s92 offen offset:3072 sc1
MM v[12:15],a[148:151],a[44:47],v[12:15]
TR a[140:141],v228 offset:22528
TR a[142:143],v228 offset:23552
MM v[12:15],a[144:147],a[12:15],v[12:15]
s_waitcnt lgkmcnt(15)
v_rcp_f32 v116, v116
MM v[32:35],a[160:163],a[8:11],v[32:35]
s_add_u32 m0, s89, 32768
TR a[144:145],v229 offset:16384
TR a[146:147],v229 offset:17408
MM v[32:35],a[164:167],a[40:43],v[32:35]
s_add_u32 s66, s87, 128
v_rcp_f32 v117, v117
MM v[32:35],a[168:171],a[72:75],v[32:35]
TR a[148:149],v229 offset:18432
TR a[150:151],v229 offset:19456
MM v[32:35],a[172:175],a[104:107],v[32:35]
buffer_load_dwordx4 v240, s[24:27], s66 offen offset:0 lds
MM v[40:43],a[172:175],a[108:111],v[40:43]
s_add_u32 m0, s89, 33792
TR a[152:153],v229 offset:20480
TR a[154:155],v229 offset:21504
MM v[40:43],a[168:171],a[76:79],v[40:43]
s_add_u32 s66, s87, 10368
v_rcp_f32 v118, v118
MM v[40:43],a[164:167],a[44:47],v[40:43]
TR a[156:157],v229 offset:22528
TR a[158:159],v229 offset:23552
MM v[40:43],a[160:163],a[12:15],v[40:43]
s_waitcnt lgkmcnt(15)
buffer_load_dwordx4 v240, s[24:27], s66 offen offset:0 lds
MM v[36:39],a[176:179],a[8:11],v[36:39]
s_add_u32 m0, s89, 40960
TR a[160:161],v244 offset:0
TR a[162:163],v244 offset:2048
MM v[36:39],a[180:183],a[40:43],v[36:39]
s_add_u32 s66, s87, 82048
v_rcp_f32 v119, v119
MM v[36:39],a[184:187],a[72:75],v[36:39]
TR a[164:165],v244 offset:4096
TR a[166:167],v244 offset:6144
MM v[36:39],a[188:191],a[104:107],v[36:39]
buffer_load_dwordx4 v240, s[24:27], s66 offen offset:0 lds
MM v[44:47],a[188:191],a[108:111],v[44:47]
s_add_u32 m0, s89, 41984
TR a[168:169],v244 offset:8192
TR a[170:171],v244 offset:10240
MM v[44:47],a[184:187],a[76:79],v[44:47]
s_add_u32 s66, s87, 92288
v_mul_f32 v116, v112, v116
v_mul_f32 v117, v113, v117
MM v[44:47],a[180:183],a[44:47],v[44:47]
TR a[172:173],v244 offset:12288
TR a[174:175],v244 offset:14336
MM v[44:47],a[176:179],a[12:15],v[44:47]
s_waitcnt lgkmcnt(15)
buffer_load_dwordx4 v240, s[24:27], s66 offen offset:0 lds
MM v[128:131],a[128:131],a[8:11],v[128:131]
s_add_u32 m0, s89, 98304
TR a[176:177],v235 offset:0
TR a[178:179],v235 offset:2048
MM v[128:131],a[132:135],a[40:43],v[128:131]
s_add_u32 s66, s88, 128
v_mul_f32 v118, v114, v118
v_mul_f32 v119, v115, v119
MM v[128:131],a[136:139],a[72:75],v[128:131]
TR a[180:181],v235 offset:4096
TR a[182:183],v235 offset:6144
MM v[128:131],a[140:143],a[104:107],v[128:131]
buffer_load_dwordx4 v240, s[24:27], s66 offen offset:0 lds
MM v[136:139],a[140:143],a[108:111],v[136:139]
s_add_u32 m0, s89, 99328
TR a[184:185],v235 offset:8192
TR a[186:187],v235 offset:10240
MM v[136:139],a[136:139],a[76:79],v[136:139]
s_add_u32 s66, s88, 10368
MM v[136:139],a[132:135],a[44:47],v[136:139]
TR a[188:189],v235 offset:12288
TR a[190:191],v235 offset:14336
MM v[136:139],a[128:131],a[12:15],v[136:139]
s_waitcnt lgkmcnt(15)
buffer_load_dwordx4 v240, s[24:27], s66 offen offset:0 lds
MM v[132:135],a[144:147],a[8:11],v[132:135]
s_lshl_b32 s97, s84, 2
TR a[128:129],v244 offset:512
TR a[130:131],v244 offset:2560
MM v[132:135],a[148:151],a[40:43],v[132:135]
s_add_u32 s97, s97, s96
s_add_u32 s97, s97, 16
s_waitcnt vmcnt(14)
MM v[132:135],a[152:155],a[72:75],v[132:135]
s_add_u32 m0, s89, 106496
TR a[132:133],v244 offset:4608
TR a[134:135],v244 offset:6656
MM v[132:135],a[156:159],a[104:107],v[132:135]
s_add_u32 s66, s88, 82048
v_mov_b32 v242, s69
s_mov_b64 exec, 1
buffer_store_dword v242, off, s[20:23], s97 offset:0 sc1
s_mov_b64 exec, -1
MM v[140:143],a[156:159],a[108:111],v[140:143]
TR a[136:137],v244 offset:8704
TR a[138:139],v244 offset:10752
MM v[140:143],a[152:155],a[76:79],v[140:143]
buffer_load_dwordx4 v240, s[24:27], s66 offen offset:0 lds
MM v[140:143],a[148:151],a[44:47],v[140:143]
s_add_u32 m0, s89, 107520
TR a[140:141],v244 offset:12800
TR a[142:143],v244 offset:14848
MM v[140:143],a[144:147],a[12:15],v[140:143]
s_waitcnt lgkmcnt(15)
s_add_u32 s66, s88, 92288
s_waitcnt vmcnt(12)
MM v[0:3],a[160:163],a[192:195],v[0:3]
TR a[144:145],v235 offset:512
TR a[146:147],v235 offset:2560
MM v[0:3],a[164:167],a[208:211],v[0:3]
buffer_load_dwordx4 v240, s[24:27], s66 offen offset:0 lds
MM v[0:3],a[168:171],a[224:227],v[0:3]
TR a[148:149],v235 offset:4608
TR a[150:151],v235 offset:6656
MM v[0:3],a[172:175],a[240:243],v[0:3]
s_waitcnt vmcnt(9)
MM v[8:11],a[172:175],a[244:247],v[8:11]
TR a[152:153],v235 offset:8704
TR a[154:155],v235 offset:10752
MM v[8:11],a[168:171],a[228:231],v[8:11]
MM v[8:11],a[164:167],a[212:215],v[8:11]
TR a[156:157],v235 offset:12800
TR a[158:159],v235 offset:14848
MM v[8:11],a[160:163],a[196:199],v[8:11]
s_waitcnt lgkmcnt(15)
MM v[4:7],a[176:179],a[192:195],v[4:7]
TR a[160:161],v228 offset:24576
TR a[162:163],v228 offset:25600
MM v[4:7],a[180:183],a[208:211],v[4:7]
MM v[4:7],a[184:187],a[224:227],v[4:7]
TR a[164:165],v228 offset:26624
TR a[166:167],v228 offset:27648
MM v[4:7],a[188:191],a[240:243],v[4:7]
MM v[12:15],a[188:191],a[244:247],v[12:15]
TR a[168:169],v228 offset:28672
TR a[170:171],v228 offset:29696
MM v[12:15],a[184:187],a[228:231],v[12:15]
MM v[12:15],a[180:183],a[212:215],v[12:15]
TR a[172:173],v228 offset:30720
TR a[174:175],v228 offset:31744
MM v[12:15],a[176:179],a[196:199],v[12:15]
s_waitcnt lgkmcnt(15)
MM v[32:35],a[128:131],a[192:195],v[32:35]
TR a[176:177],v229 offset:24576
TR a[178:179],v229 offset:25600
)ASM"
R"ASM(
MM v[32:35],a[132:135],a[208:211],v[32:35]
MM v[32:35],a[136:139],a[224:227],v[32:35]
TR a[180:181],v229 offset:26624
TR a[182:183],v229 offset:27648
MM v[32:35],a[140:143],a[240:243],v[32:35]
s_add_u32 s67, s46, 0
CV v0,v0,v1
CV v1,v2,v3
MM v[40:43],a[140:143],a[244:247],v[40:43]
TR a[184:185],v229 offset:28672
TR a[186:187],v229 offset:29696
MM v[40:43],a[136:139],a[228:231],v[40:43]
CV v2,v4,v5
CV v3,v6,v7
MM v[40:43],a[132:135],a[212:215],v[40:43]
TR a[188:189],v229 offset:30720
TR a[190:191],v229 offset:31744
MM v[40:43],a[128:131],a[196:199],v[40:43]
s_waitcnt lgkmcnt(15)
buffer_store_dwordx4 v[0:3], v249, s[20:23], s67 offen offset:0 sc1
MM v[36:39],a[144:147],a[192:195],v[36:39]
s_add_u32 s67, s47, 0
TR a[128:129],v244 offset:16384
TR a[130:131],v244 offset:18432
MM v[36:39],a[148:151],a[208:211],v[36:39]
CV v8,v8,v9
CV v9,v10,v11
MM v[36:39],a[152:155],a[224:227],v[36:39]
TR a[132:133],v244 offset:20480
TR a[134:135],v244 offset:22528
MM v[36:39],a[156:159],a[240:243],v[36:39]
CV v10,v12,v13
CV v11,v14,v15
MM v[44:47],a[156:159],a[244:247],v[44:47]
TR a[136:137],v244 offset:24576
TR a[138:139],v244 offset:26624
MM v[44:47],a[152:155],a[228:231],v[44:47]
buffer_store_dwordx4 v[8:11], v234, s[20:23], s67 offen offset:1024 sc1
MM v[44:47],a[148:151],a[212:215],v[44:47]
TR a[140:141],v244 offset:28672
TR a[142:143],v244 offset:30720
MM v[44:47],a[144:147],a[196:199],v[44:47]
s_waitcnt lgkmcnt(15)
ds_bpermute_b32 v160, v245, v0
MM v[128:131],a[160:163],a[192:195],v[128:131]
TR a[144:145],v235 offset:16384
TR a[146:147],v235 offset:18432
MM v[128:131],a[164:167],a[208:211],v[128:131]
ds_bpermute_b32 v161, v245, v1
MM v[128:131],a[168:171],a[224:227],v[128:131]
TR a[148:149],v235 offset:20480
TR a[150:151],v235 offset:22528
MM v[128:131],a[172:175],a[240:243],v[128:131]
ds_bpermute_b32 v162, v245, v2
MM v[136:139],a[172:175],a[244:247],v[136:139]
TR a[152:153],v235 offset:24576
TR a[154:155],v235 offset:26624
MM v[136:139],a[168:171],a[228:231],v[136:139]
ds_bpermute_b32 v163, v245, v3
MM v[136:139],a[164:167],a[212:215],v[136:139]
s_nop 0
TR a[156:157],v235 offset:28672
TR a[158:159],v235 offset:30720
MM v[136:139],a[160:163],a[196:199],v[136:139]
s_waitcnt lgkmcnt(15)
CV v0,v116,v117
CV v1,v118,v119
s_waitcnt vmcnt(2)
s_barrier
MM v[132:135],a[176:179],a[192:195],v[132:135]
s_add_u32 s67, s46, 0
TR a[160:161],v230 offset:32768
TR a[162:163],v230 offset:34816
MM v[132:135],a[180:183],a[208:211],v[132:135]
s_nop 0
CV v32,v32,v33
CV v33,v34,v35
MM v[132:135],a[184:187],a[224:227],v[132:135]
s_nop 0
TR a[164:165],v230 offset:36864
TR a[166:167],v230 offset:38912
v_lshrrev_b32 v243, 2, v248
MM v[132:135],a[188:191],a[240:243],v[132:135]
s_nop 0
CV v34,v36,v37
CV v35,v38,v39
MM v[140:143],a[188:191],a[244:247],v[140:143]
LPG22_%=:
s_cbranch_execz LTR3_%=
TR a[168:169],v230 offset:40960
TR a[170:171],v230 offset:43008
MM v[140:143],a[184:187],a[228:231],v[140:143]
buffer_store_dwordx4 v[32:35], v249, s[20:23], s67 offen offset:2048 sc1
MM v[140:143],a[180:183],a[212:215],v[140:143]
s_add_u32 s67, s47, 0
TR a[172:173],v230 offset:45056
TR a[174:175],v230 offset:47104
v_min_u32 v243, 12, v243
MM v[140:143],a[176:179],a[196:199],v[140:143]
s_waitcnt lgkmcnt(15)
CV v40,v40,v41
CV v41,v42,v43
MM v[64:67],a[128:131],a[0:3],v[64:67]
TR a[176:177],v231 offset:32768
TR a[178:179],v231 offset:34816
MM v[64:67],a[132:135],a[32:35],v[64:67]
buffer_load_dword v242, v243, s[20:23], s96 offen offset:16 sc1
MM v[64:67],a[136:139],a[64:67],v[64:67]
TR a[180:181],v231 offset:36864
TR a[182:183],v231 offset:38912
CV v42,v44,v45
MM v[64:67],a[140:143],a[96:99],v[64:67]
CV v43,v46,v47
v_lshlrev_b32 v112, 16, v226
MM v[72:75],a[140:143],a[100:103],v[72:75]
TR a[184:185],v231 offset:40960
TR a[186:187],v231 offset:43008
MM v[72:75],a[136:139],a[68:71],v[72:75]
buffer_store_dwordx4 v[40:43], v234, s[20:23], s67 offen offset:3072 sc1
MM v[72:75],a[132:135],a[36:39],v[72:75]
TR a[188:189],v231 offset:45056
TR a[190:191],v231 offset:47104
MM v[72:75],a[128:131],a[4:7],v[72:75]
s_waitcnt lgkmcnt(15)
ds_bpermute_b32 v176, v245, v32
MM v[68:71],a[144:147],a[0:3],v[68:71]
TR a[128:129],v244 offset:32768
TR a[130:131],v244 offset:34816
MM v[68:71],a[148:151],a[32:35],v[68:71]
ds_bpermute_b32 v177, v245, v33
MM v[68:71],a[152:155],a[64:67],v[68:71]
TR a[132:133],v244 offset:36864
TR a[134:135],v244 offset:38912
MM v[68:71],a[156:159],a[96:99],v[68:71]
ds_bpermute_b32 v178, v245, v34
MM v[76:79],a[156:159],a[100:103],v[76:79]
TR a[136:137],v244 offset:40960
TR a[138:139],v244 offset:43008
MM v[76:79],a[152:155],a[68:71],v[76:79]
ds_bpermute_b32 v179, v245, v35
MM v[76:79],a[148:151],a[36:39],v[76:79]
TR a[140:141],v244 offset:45056
TR a[142:143],v244 offset:47104
MM v[76:79],a[144:147],a[4:7],v[76:79]
s_waitcnt lgkmcnt(15)
v_and_b32 v113, 0xffff0000, v226
v_lshlrev_b32 v114, 16, v227
MM v[64:67],a[160:163],a[8:11],v[64:67]
s_add_u32 s67, s46, 8192
TR a[144:145],v235 offset:32768
TR a[146:147],v235 offset:34816
MM v[64:67],a[164:167],a[40:43],v[64:67]
CV v128,v128,v129
CV v129,v130,v131
MM v[64:67],a[168:171],a[72:75],v[64:67]
TR a[148:149],v235 offset:36864
TR a[150:151],v235 offset:38912
MM v[64:67],a[172:175],a[104:107],v[64:67]
CV v130,v132,v133
CV v131,v134,v135
MM v[72:75],a[172:175],a[108:111],v[72:75]
TR a[152:153],v235 offset:40960
TR a[154:155],v235 offset:43008
MM v[72:75],a[168:171],a[76:79],v[72:75]
buffer_store_dwordx4 v[128:131], v249, s[20:23], s67 offen offset:0 sc1
MM v[72:75],a[164:167],a[44:47],v[72:75]
s_add_u32 s67, s47, 8192
TR a[156:157],v235 offset:45056
TR a[158:159],v235 offset:47104
MM v[72:75],a[160:163],a[12:15],v[72:75]
s_waitcnt lgkmcnt(15)
CV v136,v136,v137
CV v137,v138,v139
MM v[68:71],a[176:179],a[8:11],v[68:71]
TR a[160:161],v244 offset:16896
TR a[162:163],v244 offset:18944
MM v[68:71],a[180:183],a[40:43],v[68:71]
CV v138,v140,v141
CV v139,v142,v143
MM v[68:71],a[184:187],a[72:75],v[68:71]
TR a[164:165],v244 offset:20992
TR a[166:167],v244 offset:23040
MM v[68:71],a[188:191],a[104:107],v[68:71]
buffer_store_dwordx4 v[136:139], v234, s[20:23], s67 offen offset:1024 sc1
MM v[76:79],a[188:191],a[108:111],v[76:79]
TR a[168:169],v244 offset:25088
TR a[170:171],v244 offset:27136
MM v[76:79],a[184:187],a[76:79],v[76:79]
ds_bpermute_b32 v164, v245, v128
MM v[76:79],a[180:183],a[44:47],v[76:79]
TR a[172:173],v244 offset:29184
TR a[174:175],v244 offset:31232
MM v[76:79],a[176:179],a[12:15],v[76:79]
s_waitcnt lgkmcnt(15)
ds_bpermute_b32 v165, v245, v129
MM v[64:67],a[128:131],a[192:195],v[64:67]
TR a[176:177],v235 offset:16896
TR a[178:179],v235 offset:18944
MM v[64:67],a[132:135],a[208:211],v[64:67]
ds_bpermute_b32 v166, v245, v130
MM v[64:67],a[136:139],a[224:227],v[64:67]
TR a[180:181],v235 offset:20992
TR a[182:183],v235 offset:23040
MM v[64:67],a[140:143],a[240:243],v[64:67]
ds_bpermute_b32 v167, v245, v131
MM v[72:75],a[140:143],a[244:247],v[72:75]
TR a[184:185],v235 offset:25088
TR a[186:187],v235 offset:27136
MM v[72:75],a[136:139],a[228:231],v[72:75]
v_and_b32 v115, 0xffff0000, v227
v_mul_f32 v116, 0xbfb8aa3b, v112
MM v[72:75],a[132:135],a[212:215],v[72:75]
TR a[188:189],v235 offset:29184
TR a[190:191],v235 offset:31232
MM v[72:75],a[128:131],a[196:199],v[72:75]
s_waitcnt lgkmcnt(15)
v_mul_f32 v117, 0xbfb8aa3b, v113
v_mul_f32 v118, 0xbfb8aa3b, v114
MM v[68:71],a[144:147],a[192:195],v[68:71]
TR a[128:129],v230 offset:33280
TR a[130:131],v230 offset:35328
MM v[68:71],a[148:151],a[208:211],v[68:71]
v_mul_f32 v119, 0xbfb8aa3b, v115
MM v[68:71],a[152:155],a[224:227],v[68:71]
TR a[132:133],v230 offset:37376
TR a[134:135],v230 offset:39424
MM v[68:71],a[156:159],a[240:243],v[68:71]
EX v116,v116
MM v[76:79],a[156:159],a[244:247],v[76:79]
TR a[136:137],v230 offset:41472
TR a[138:139],v230 offset:43520
MM v[76:79],a[152:155],a[228:231],v[76:79]
EX v117,v117
MM v[76:79],a[148:151],a[212:215],v[76:79]
TR a[140:141],v230 offset:45568
TR a[142:143],v230 offset:47616
MM v[76:79],a[144:147],a[196:199],v[76:79]
s_waitcnt lgkmcnt(15)
EX v118,v118
MM v[96:99],a[160:163],a[0:3],v[96:99]
s_waitcnt vmcnt(3)
TR a[144:145],v231 offset:33280
TR a[146:147],v231 offset:35328
MM v[96:99],a[164:167],a[32:35],v[96:99]
EX v119,v119
MM v[96:99],a[168:171],a[64:67],v[96:99]
TR a[148:149],v231 offset:37376
TR a[150:151],v231 offset:39424
v_cmp_ne_u32 s[74:75], s69, v242
MM v[96:99],a[172:175],a[96:99],v[96:99]
s_add_u32 s67, s46, 4096
CV v64,v64,v65
CV v65,v66,v67
MM v[104:107],a[172:175],a[100:103],v[104:107]
TR a[152:153],v231 offset:41472
TR a[154:155],v231 offset:43520
MM v[104:107],a[168:171],a[68:71],v[104:107]
CV v66,v68,v69
CV v67,v70,v71
MM v[104:107],a[164:167],a[36:39],v[104:107]
s_cmp_lg_u64 s[74:75], 0
s_cbranch_scc1 LRPO1_%=
LRPO1b_%=:
TR a[156:157],v231 offset:45568
TR a[158:159],v231 offset:47616
MM v[104:107],a[160:163],a[4:7],v[104:107]
s_waitcnt lgkmcnt(15)
buffer_store_dwordx4 v[64:67], v249, s[20:23], s67 offen offset:0 sc1
MM v[100:103],a[176:179],a[0:3],v[100:103]
s_add_u32 s67, s47, 4096
TR a[160:161],v244 offset:33280
TR a[162:163],v244 offset:35328
MM v[100:103],a[180:183],a[32:35],v[100:103]
s_xor_b32 s92, s49, 1
CV v72,v72,v73
CV v73,v74,v75
MM v[100:103],a[184:187],a[64:67],v[100:103]
s_mul_i32 s92, s92, 17408
TR a[164:165],v244 offset:37376
TR a[166:167],v244 offset:39424
MM v[100:103],a[188:191],a[96:99],v[100:103]
s_add_u32 s92, s83, s92
CV v74,v76,v77
CV v75,v78,v79
MM v[108:111],a[188:191],a[100:103],v[108:111]
s_add_u32 s92, s92, 8192
TR a[168:169],v244 offset:41472
TR a[170:171],v244 offset:43520
MM v[108:111],a[184:187],a[68:71],v[108:111]
buffer_store_dwordx4 v[72:75], v234, s[20:23], s67 offen offset:1024 sc1
MM v[108:111],a[180:183],a[36:39],v[108:111]
s_cmp_eq_u32 s82, 0
s_cbranch_scc1 LY0_%=
v_lshrrev_b32 v135, 4, v248
v_min_u32 v135, 59, v135
v_mul_u32_u24 v132, 205, v135
v_lshrrev_b32 v132, 11, v132
v_mul_u32_u24 v132, 30, v132
v_add_lshl_u32 v135, v135, v132, 2
s_mul_i32 s78, s84, 40
s_add_u32 s78, s78, 73728
buffer_load_dword v132, v135, s[20:23], s78 offen offset:0 sc1
buffer_load_dword v133, v135, s[20:23], s78 offen offset:960 sc1
buffer_load_dword v134, v135, s[20:23], s78 offen offset:1920 sc1
LY0_%=:
TR a[172:173],v244 offset:45568
TR a[174:175],v244 offset:47616
MM v[108:111],a[176:179],a[4:7],v[108:111]
s_waitcnt lgkmcnt(15)
buffer_load_dwordx4 a[16:19], v248, s[20:23], s92 offen offset:0 sc1
MM v[96:99],a[128:131],a[8:11],v[96:99]
ds_bpermute_b32 v192, v245, v64
TR a[176:177],v235 offset:33280
TR a[178:179],v235 offset:35328
MM v[96:99],a[132:135],a[40:43],v[96:99]
buffer_load_dwordx4 a[48:51], v248, s[20:23], s92 offen offset:1024 sc1
MM v[96:99],a[136:139],a[72:75],v[96:99]
TR a[180:181],v235 offset:37376
TR a[182:183],v235 offset:39424
MM v[96:99],a[140:143],a[104:107],v[96:99]
ds_bpermute_b32 v193, v245, v65
MM v[104:107],a[140:143],a[108:111],v[104:107]
TR a[184:185],v235 offset:41472
TR a[186:187],v235 offset:43520
MM v[104:107],a[136:139],a[76:79],v[104:107]
buffer_load_dwordx4 a[80:83], v248, s[20:23], s92 offen offset:2048 sc1
MM v[104:107],a[132:135],a[44:47],v[104:107]
ds_bpermute_b32 v194, v245, v66
TR a[188:189],v235 offset:45568
TR a[190:191],v235 offset:47616
MM v[104:107],a[128:131],a[12:15],v[104:107]
s_waitcnt lgkmcnt(15)
buffer_load_dwordx4 a[112:115], v248, s[20:23], s92 offen offset:3072 sc1
MM v[100:103],a[144:147],a[8:11],v[100:103]
s_add_u32 s92, s92, 4096
TR a[128:129],v230 offset:49152
TR a[130:131],v230 offset:51200
MM v[100:103],a[148:151],a[40:43],v[100:103]
ds_bpermute_b32 v195, v245, v67
MM v[100:103],a[152:155],a[72:75],v[100:103]
TR a[132:133],v230 offset:53248
TR a[134:135],v230 offset:55296
MM v[100:103],a[156:159],a[104:107],v[100:103]
buffer_load_dwordx4 a[20:23], v248, s[20:23], s92 offen offset:0 sc1
MM v[108:111],a[156:159],a[108:111],v[108:111]
TR a[136:137],v230 offset:57344
TR a[138:139],v230 offset:59392
MM v[108:111],a[152:155],a[76:79],v[108:111]
buffer_load_dwordx4 a[52:55], v248, s[20:23], s92 offen offset:1024 sc1
MM v[108:111],a[148:151],a[44:47],v[108:111]
TR a[140:141],v230 offset:61440
TR a[142:143],v230 offset:63488
)ASM"
R"ASM(
MM v[108:111],a[144:147],a[12:15],v[108:111]
s_waitcnt lgkmcnt(15)
buffer_load_dwordx4 a[84:87], v248, s[20:23], s92 offen offset:2048 sc1
MM v[96:99],a[160:163],a[192:195],v[96:99]
TR a[144:145],v231 offset:49152
TR a[146:147],v231 offset:51200
MM v[96:99],a[164:167],a[208:211],v[96:99]
v_add_f32 v116, 1.0, v116
v_add_f32 v117, 1.0, v117
MM v[96:99],a[168:171],a[224:227],v[96:99]
TR a[148:149],v231 offset:53248
TR a[150:151],v231 offset:55296
MM v[96:99],a[172:175],a[240:243],v[96:99]
buffer_load_dwordx4 a[116:119], v248, s[20:23], s92 offen offset:3072 sc1
MM v[104:107],a[172:175],a[244:247],v[104:107]
s_waitcnt vmcnt(12)
TR a[152:153],v231 offset:57344
TR a[154:155],v231 offset:59392
MM v[104:107],a[168:171],a[228:231],v[104:107]
v_mov_b32 v252, s69
s_mov_b64 exec, 1
buffer_store_dword v252, off, s[20:23], s19 offset:0 sc1
s_mov_b64 exec, -1
MM v[104:107],a[164:167],a[212:215],v[104:107]
TR a[156:157],v231 offset:61440
TR a[158:159],v231 offset:63488
MM v[104:107],a[160:163],a[196:199],v[104:107]
s_waitcnt lgkmcnt(15)
v_add_f32 v118, 1.0, v118
v_add_f32 v119, 1.0, v119
MM v[100:103],a[176:179],a[192:195],v[100:103]
TR a[160:161],v230 offset:49664
TR a[162:163],v230 offset:51712
MM v[100:103],a[180:183],a[208:211],v[100:103]
v_rcp_f32 v116, v116
MM v[100:103],a[184:187],a[224:227],v[100:103]
TR a[164:165],v230 offset:53760
TR a[166:167],v230 offset:55808
MM v[100:103],a[188:191],a[240:243],v[100:103]
v_rcp_f32 v117, v117
MM v[108:111],a[188:191],a[244:247],v[108:111]
TR a[168:169],v230 offset:57856
TR a[170:171],v230 offset:59904
MM v[108:111],a[184:187],a[228:231],v[108:111]
v_rcp_f32 v118, v118
MM v[108:111],a[180:183],a[212:215],v[108:111]
TR a[172:173],v230 offset:61952
TR a[174:175],v230 offset:64000
MM v[108:111],a[176:179],a[196:199],v[108:111]
s_waitcnt lgkmcnt(15)
v_rcp_f32 v119, v119
MM v[16:19],a[128:131],v[168:171],0
TR a[176:177],v231 offset:49664
TR a[178:179],v231 offset:51712
MM v[16:19],a[132:135],v[184:187],v[16:19]
AW a3,-1
AW a7,-1
MM v[16:19],a[136:139],v[200:203],v[16:19]
TR a[180:181],v231 offset:53760
TR a[182:183],v231 offset:55808
MM v[16:19],a[140:143],v[216:219],v[16:19]
s_add_u32 s67, s46, 4096
CV v96,v96,v97
CV v97,v98,v99
AW a11,-1
MM v[24:27],a[140:143],v[220:223],0
TR a[184:185],v231 offset:57856
TR a[186:187],v231 offset:59904
MM v[24:27],a[136:139],v[204:207],v[24:27]
CV v98,v100,v101
CV v99,v102,v103
MM v[24:27],a[132:135],v[188:191],v[24:27]
TR a[188:189],v231 offset:61952
TR a[190:191],v231 offset:64000
AW a15,-1
MM v[24:27],a[128:131],v[172:175],v[24:27]
s_waitcnt lgkmcnt(15)
buffer_store_dwordx4 v[96:99], v249, s[20:23], s67 offen offset:2048 sc1
MM v[20:23],a[144:147],v[168:171],0
s_add_u32 s67, s47, 4096
TR a[128:129],v230 offset:16384
TR a[130:131],v230 offset:18432
MM v[20:23],a[148:151],v[184:187],v[20:23]
CV v104,v104,v105
CV v105,v106,v107
AW a35,-1
MM v[20:23],a[152:155],v[200:203],v[20:23]
TR a[132:133],v230 offset:20480
TR a[134:135],v230 offset:22528
MM v[20:23],a[156:159],v[216:219],v[20:23]
CV v106,v108,v109
CV v107,v110,v111
s_mov_b64 exec, 0xffff
AW a0,v160
AW a1,v161
AW a2,v162
AW a3,v163
s_mov_b64 exec, -1
MM v[28:31],a[156:159],v[220:223],0
TR a[136:137],v230 offset:24576
TR a[138:139],v230 offset:26624
AW a39,-1
MM v[28:31],a[152:155],v[204:207],v[28:31]
buffer_store_dwordx4 v[104:107], v234, s[20:23], s67 offen offset:3072 sc1
MM v[28:31],a[148:151],v[188:191],v[28:31]
TR a[140:141],v230 offset:28672
TR a[142:143],v230 offset:30720
s_mov_b64 exec, 0xffff
AW a8,v176
AW a9,v177
AW a10,v178
AW a11,v179
s_mov_b64 exec, -1
MM v[28:31],a[144:147],v[172:175],v[28:31]
s_waitcnt lgkmcnt(15)
ds_bpermute_b32 v208, v245, v96
AW a43,-1
MM v[48:51],a[160:163],v[168:171],0
TR a[144:145],v231 offset:16384
TR a[146:147],v231 offset:18432
MM v[48:51],a[164:167],v[184:187],v[48:51]
ds_bpermute_b32 v209, v245, v97
MM v[48:51],a[168:171],v[200:203],v[48:51]
TR a[148:149],v231 offset:20480
TR a[150:151],v231 offset:22528
AW a47,-1
MM v[48:51],a[172:175],v[216:219],v[48:51]
ds_bpermute_b32 v210, v245, v98
MM v[56:59],a[172:175],v[220:223],0
TR a[152:153],v231 offset:24576
TR a[154:155],v231 offset:26624
MM v[56:59],a[168:171],v[204:207],v[56:59]
ds_bpermute_b32 v211, v245, v99
AW a71,-1
MM v[56:59],a[164:167],v[188:191],v[56:59]
TR a[156:157],v231 offset:28672
TR a[158:159],v231 offset:30720
MM v[56:59],a[160:163],v[172:175],v[56:59]
s_waitcnt lgkmcnt(15)
AW a79,-1
AW a103,-1
MM v[52:55],a[176:179],v[168:171],0
TR a[160:161],v230 offset:16896
TR a[162:163],v230 offset:18944
MM v[52:55],a[180:183],v[184:187],v[52:55]
AW a111,-1
AW a195,-1
MM v[52:55],a[184:187],v[200:203],v[52:55]
TR a[164:165],v230 offset:20992
TR a[166:167],v230 offset:23040
MM v[52:55],a[188:191],v[216:219],v[52:55]
AW a199,-1
AW a215,-1
MM v[60:63],a[188:191],v[220:223],0
TR a[168:169],v230 offset:25088
TR a[170:171],v230 offset:27136
MM v[60:63],a[184:187],v[204:207],v[60:63]
buffer_load_dword v240, v253, s[20:23], s68 offen offset:0 sc1
buffer_load_dword v140, v253, s[20:23], s45 offen offset:0 sc1
MM v[60:63],a[180:183],v[188:191],v[60:63]
TR a[172:173],v230 offset:29184
TR a[174:175],v230 offset:31232
s_mov_b64 exec, 0xffff
AW a32,v192
AW a33,v193
AW a34,v194
AW a35,v195
s_mov_b64 exec, -1
MM v[60:63],a[176:179],v[172:175],v[60:63]
s_waitcnt lgkmcnt(15)
s_waitcnt lgkmcnt(10)
v_mul_f32 v116, v112, v116
MM v[80:83],a[128:131],v[168:171],0
TR a[176:177],v231 offset:16896
TR a[178:179],v231 offset:18944
MM v[80:83],a[132:135],v[184:187],v[80:83]
s_mov_b64 exec, 0xffff
AW a40,v208
AW a41,v209
AW a42,v210
AW a43,v211
s_mov_b64 exec, -1
MM v[80:83],a[136:139],v[200:203],v[80:83]
TR a[180:181],v231 offset:20992
TR a[182:183],v231 offset:23040
MM v[80:83],a[140:143],v[216:219],v[80:83]
s_mov_b64 exec, 0xffff
AW a192,v164
AW a193,v165
AW a194,v166
AW a195,v167
s_mov_b64 exec, -1
MM v[88:91],a[140:143],v[220:223],0
TR a[184:185],v231 offset:25088
TR a[186:187],v231 offset:27136
MM v[88:91],a[136:139],v[204:207],v[88:91]
v_lshrrev_b32 v104, 8, v248
v_min_u32 v104, 1, v104
MM v[88:91],a[132:135],v[188:191],v[88:91]
TR a[188:189],v231 offset:29184
TR a[190:191],v231 offset:31232
MM v[88:91],a[128:131],v[172:175],v[88:91]
s_waitcnt lgkmcnt(15)
v_sub_u32 v104, 0, v104
v_mul_f32 v117, v113, v117
MM v[84:87],a[144:147],v[168:171],0
TR a[128:129],v228 offset:0
TR a[130:131],v228 offset:1024
MM v[84:87],a[148:151],v[184:187],v[84:87]
v_mul_f32 v118, v114, v118
v_mul_f32 v119, v115, v119
MM v[84:87],a[152:155],v[200:203],v[84:87]
TR a[132:133],v228 offset:2048
TR a[134:135],v228 offset:3072
MM v[84:87],a[156:159],v[216:219],v[84:87]
CV v2,v116,v117
CV v3,v118,v119
MM v[92:95],a[156:159],v[220:223],0
TR a[136:137],v228 offset:4096
TR a[138:139],v228 offset:5120
MM v[92:95],a[152:155],v[204:207],v[92:95]
v_lshlrev_b32 v10, 16, v236
v_and_b32 v11, 0xffff0000, v236
MM v[92:95],a[148:151],v[188:191],v[92:95]
TR a[140:141],v228 offset:6144
TR a[142:143],v228 offset:7168
MM v[92:95],a[144:147],v[172:175],v[92:95]
s_waitcnt lgkmcnt(15)
v_lshlrev_b32 v12, 16, v237
v_and_b32 v13, 0xffff0000, v237
MM v[112:115],a[160:163],v[168:171],0
s_xor_b32 s92, s49, 2
TR a[144:145],v229 offset:0
TR a[146:147],v229 offset:1024
MM v[112:115],a[164:167],v[184:187],v[112:115]
s_mul_i32 s92, s92, 17408
s_add_u32 s92, s83, s92
s_add_u32 s92, s92, 8192
MM v[112:115],a[168:171],v[200:203],v[112:115]
TR a[148:149],v229 offset:2048
TR a[150:151],v229 offset:3072
MM v[112:115],a[172:175],v[216:219],v[112:115]
buffer_load_dwordx4 a[24:27], v248, s[20:23], s92 offen offset:0 sc1
MM v[120:123],a[172:175],v[220:223],0
TR a[152:153],v229 offset:4096
TR a[154:155],v229 offset:5120
MM v[120:123],a[168:171],v[204:207],v[120:123]
buffer_load_dwordx4 a[56:59], v248, s[20:23], s92 offen offset:1024 sc1
MM v[120:123],a[164:167],v[188:191],v[120:123]
TR a[156:157],v229 offset:6144
TR a[158:159],v229 offset:7168
MM v[120:123],a[160:163],v[172:175],v[120:123]
s_waitcnt lgkmcnt(15)
buffer_load_dwordx4 a[88:91], v248, s[20:23], s92 offen offset:2048 sc1
MM v[116:119],a[176:179],v[168:171],0
TR a[160:161],v244 offset:49152
TR a[162:163],v244 offset:51200
MM v[116:119],a[180:183],v[184:187],v[116:119]
buffer_load_dwordx4 a[120:123], v248, s[20:23], s92 offen offset:3072 sc1
MM v[116:119],a[184:187],v[200:203],v[116:119]
s_add_u32 s92, s92, 4096
TR a[164:165],v244 offset:53248
TR a[166:167],v244 offset:55296
MM v[116:119],a[188:191],v[216:219],v[116:119]
v_mul_f32 v14, 0xbfb8aa3b, v10
v_mul_f32 v15, 0xbfb8aa3b, v11
MM v[124:127],a[188:191],v[220:223],0
TR a[168:169],v244 offset:57344
TR a[170:171],v244 offset:59392
MM v[124:127],a[184:187],v[204:207],v[124:127]
buffer_load_dwordx4 a[28:31], v248, s[20:23], s92 offen offset:0 sc1
MM v[124:127],a[180:183],v[188:191],v[124:127]
s_nop 0
TR a[172:173],v244 offset:61440
TR a[174:175],v244 offset:63488
MM v[124:127],a[176:179],v[172:175],v[124:127]
s_waitcnt lgkmcnt(15)
buffer_load_dwordx4 a[60:63], v248, s[20:23], s92 offen offset:1024 sc1
MM v[144:147],a[128:131],v[168:171],0
s_nop 0
TR a[176:177],v235 offset:49152
TR a[178:179],v235 offset:51200
MM v[144:147],a[132:135],v[184:187],v[144:147]
s_nop 0
v_mul_f32 v42, 0xbfb8aa3b, v12
v_mul_f32 v43, 0xbfb8aa3b, v13
MM v[144:147],a[136:139],v[200:203],v[144:147]
LPG23_%=:
s_cbranch_execz LTR3_%=
TR a[180:181],v235 offset:53248
TR a[182:183],v235 offset:55296
MM v[144:147],a[140:143],v[216:219],v[144:147]
buffer_load_dwordx4 a[92:95], v248, s[20:23], s92 offen offset:2048 sc1
MM v[152:155],a[140:143],v[220:223],0
TR a[184:185],v235 offset:57344
TR a[186:187],v235 offset:59392
MM v[152:155],a[136:139],v[204:207],v[152:155]
buffer_load_dwordx4 a[124:127], v248, s[20:23], s92 offen offset:3072 sc1
MM v[152:155],a[132:135],v[188:191],v[152:155]
s_xor_b32 s92, s49, 3
TR a[188:189],v235 offset:61440
TR a[190:191],v235 offset:63488
MM v[152:155],a[128:131],v[172:175],v[152:155]
s_waitcnt lgkmcnt(15)
s_mul_i32 s92, s92, 17408
s_add_u32 s92, s83, s92
MM v[148:151],a[144:147],v[168:171],0
s_add_u32 s92, s92, 8192
TR a[128:129],v244 offset:49664
TR a[130:131],v244 offset:51712
MM v[148:151],a[148:151],v[184:187],v[148:151]
buffer_load_dwordx4 a[200:203], v248, s[20:23], s92 offen offset:0 sc1
MM v[148:151],a[152:155],v[200:203],v[148:151]
TR a[132:133],v244 offset:53760
TR a[134:135],v244 offset:55808
MM v[148:151],a[156:159],v[216:219],v[148:151]
buffer_load_dwordx4 a[216:219], v248, s[20:23], s92 offen offset:1024 sc1
MM v[156:159],a[156:159],v[220:223],0
TR a[136:137],v244 offset:57856
TR a[138:139],v244 offset:59904
MM v[156:159],a[152:155],v[204:207],v[156:159]
EX v14,v14
MM v[156:159],a[148:151],v[188:191],v[156:159]
TR a[140:141],v244 offset:61952
TR a[142:143],v244 offset:64000
MM v[156:159],a[144:147],v[172:175],v[156:159]
s_waitcnt lgkmcnt(15)
buffer_load_dwordx4 a[232:235], v248, s[20:23], s92 offen offset:2048 sc1
s_waitcnt vmcnt(20)
MM v[16:19],a[160:163],a[16:19],v[16:19]
TR a[144:145],v235 offset:49664
TR a[146:147],v235 offset:51712
MM v[16:19],a[164:167],a[48:51],v[16:19]
buffer_load_dwordx4 a[248:251], v248, s[20:23], s92 offen offset:3072 sc1
MM v[16:19],a[168:171],a[80:83],v[16:19]
s_add_u32 s92, s92, 4096
TR a[148:149],v235 offset:53760
TR a[150:151],v235 offset:55808
MM v[16:19],a[172:175],a[112:115],v[16:19]
s_waitcnt vmcnt(17)
EX v15,v15
MM v[24:27],a[172:175],a[116:119],v[24:27]
TR a[152:153],v235 offset:57856
TR a[154:155],v235 offset:59904
MM v[24:27],a[168:171],a[84:87],v[24:27]
buffer_load_dwordx4 a[204:207], v248, s[20:23], s92 offen offset:0 sc1
MM v[24:27],a[164:167],a[52:55],v[24:27]
TR a[156:157],v235 offset:61952
TR a[158:159],v235 offset:64000
MM v[24:27],a[160:163],a[20:23],v[24:27]
s_waitcnt lgkmcnt(15)
buffer_load_dwordx4 a[220:223], v248, s[20:23], s92 offen offset:1024 sc1
MM v[20:23],a[176:179],a[16:19],v[20:23]
TR a[160:161],v244 offset:16384
TR a[162:163],v244 offset:18432
MM v[20:23],a[180:183],a[48:51],v[20:23]
s_waitcnt vmcnt(14)
v_cmp_eq_u32 s[36:37], s69, v240
v_cmp_eq_u32 s[38:39], s69, v140
MM v[20:23],a[184:187],a[80:83],v[20:23]
TR a[164:165],v244 offset:20480
TR a[166:167],v244 offset:22528
MM v[20:23],a[188:191],a[112:115],v[20:23]
)ASM"
R"ASM(
buffer_load_dwordx4 a[236:239], v248, s[20:23], s92 offen offset:2048 sc1
MM v[28:31],a[188:191],a[116:119],v[28:31]
s_nop 0
TR a[168:169],v244 offset:24576
TR a[170:171],v244 offset:26624
MM v[28:31],a[184:187],a[84:87],v[28:31]
buffer_load_dwordx4 a[252:255], v248, s[20:23], s92 offen offset:3072 sc1
MM v[28:31],a[180:183],a[52:55],v[28:31]
s_and_b32 s36, s36, 0xffff0000
TR a[172:173],v244 offset:28672
TR a[174:175],v244 offset:30720
MM v[28:31],a[176:179],a[20:23],v[28:31]
s_waitcnt lgkmcnt(15)
s_add_u32 s66, s16, 0
MM v[48:51],a[128:131],a[16:19],v[48:51]
TR a[176:177],v235 offset:16384
TR a[178:179],v235 offset:18432
MM v[48:51],a[132:135],a[48:51],v[48:51]
v_mov_b32 v252, s69
s_mov_b64 exec, 1
buffer_store_dword v252, off, s[20:23], s19 offset:4 sc1
s_mov_b64 exec, -1
MM v[48:51],a[136:139],a[80:83],v[48:51]
s_mov_b64 exec, s[36:37]
buffer_load_dwordx4 a[0:3], v250, s[20:23], s66 offen offset:0 sc1
s_mov_b64 exec, -1
TR a[180:181],v235 offset:20480
TR a[182:183],v235 offset:22528
MM v[48:51],a[140:143],a[112:115],v[48:51]
s_add_u32 s66, s44, 0
s_mov_b64 exec, s[38:39]
buffer_load_dwordx4 a[4:7], v250, s[20:23], s66 offen offset:0 sc1
s_mov_b64 exec, -1
MM v[56:59],a[140:143],a[116:119],v[56:59]
s_add_u32 s66, s16, 2048
TR a[184:185],v235 offset:24576
TR a[186:187],v235 offset:26624
MM v[56:59],a[136:139],a[84:87],v[56:59]
EX v42,v42
MM v[56:59],a[132:135],a[52:55],v[56:59]
TR a[188:189],v235 offset:28672
TR a[190:191],v235 offset:30720
MM v[56:59],a[128:131],a[20:23],v[56:59]
s_waitcnt lgkmcnt(15)
s_mov_b64 exec, s[36:37]
buffer_load_dwordx4 a[8:11], v250, s[20:23], s66 offen offset:0 sc1
s_mov_b64 exec, -1
MM v[52:55],a[144:147],a[16:19],v[52:55]
s_add_u32 s66, s44, 2048
TR a[128:129],v244 offset:16896
TR a[130:131],v244 offset:18944
MM v[52:55],a[148:151],a[48:51],v[52:55]
s_mov_b64 exec, s[38:39]
buffer_load_dwordx4 a[12:15], v250, s[20:23], s66 offen offset:0 sc1
s_mov_b64 exec, -1
MM v[52:55],a[152:155],a[80:83],v[52:55]
TR a[132:133],v244 offset:20992
TR a[134:135],v244 offset:23040
MM v[52:55],a[156:159],a[112:115],v[52:55]
EX v43,v43
MM v[60:63],a[156:159],a[116:119],v[60:63]
TR a[136:137],v244 offset:25088
TR a[138:139],v244 offset:27136
MM v[60:63],a[152:155],a[84:87],v[60:63]
v_add_f32 v14, 1.0, v14
v_add_f32 v15, 1.0, v15
MM v[60:63],a[148:151],a[52:55],v[60:63]
TR a[140:141],v244 offset:29184
TR a[142:143],v244 offset:31232
MM v[60:63],a[144:147],a[20:23],v[60:63]
s_waitcnt lgkmcnt(15)
v_add_f32 v42, 1.0, v42
v_add_f32 v43, 1.0, v43
MM v[80:83],a[160:163],a[16:19],v[80:83]
TR a[144:145],v235 offset:16896
TR a[146:147],v235 offset:18944
MM v[80:83],a[164:167],a[48:51],v[80:83]
v_rcp_f32 v14, v14
MM v[80:83],a[168:171],a[80:83],v[80:83]
TR a[148:149],v235 offset:20992
TR a[150:151],v235 offset:23040
MM v[80:83],a[172:175],a[112:115],v[80:83]
v_rcp_f32 v15, v15
MM v[88:91],a[172:175],a[116:119],v[88:91]
TR a[152:153],v235 offset:25088
TR a[154:155],v235 offset:27136
MM v[88:91],a[168:171],a[84:87],v[88:91]
v_rcp_f32 v42, v42
MM v[88:91],a[164:167],a[52:55],v[88:91]
TR a[156:157],v235 offset:29184
TR a[158:159],v235 offset:31232
MM v[88:91],a[160:163],a[20:23],v[88:91]
s_waitcnt lgkmcnt(15)
v_rcp_f32 v43, v43
MM v[84:87],a[176:179],a[16:19],v[84:87]
TR a[160:161],v228 offset:8192
TR a[162:163],v228 offset:9216
MM v[84:87],a[180:183],a[48:51],v[84:87]
v_mul_f32 v14, v10, v14
v_mul_f32 v15, v11, v15
MM v[84:87],a[184:187],a[80:83],v[84:87]
TR a[164:165],v228 offset:10240
TR a[166:167],v228 offset:11264
MM v[84:87],a[188:191],a[112:115],v[84:87]
v_mul_f32 v42, v12, v42
v_mul_f32 v43, v13, v43
MM v[92:95],a[188:191],a[116:119],v[92:95]
TR a[168:169],v228 offset:12288
TR a[170:171],v228 offset:13312
MM v[92:95],a[184:187],a[84:87],v[92:95]
CV v4,v14,v15
CV v5,v42,v43
MM v[92:95],a[180:183],a[52:55],v[92:95]
TR a[172:173],v228 offset:14336
TR a[174:175],v228 offset:15360
MM v[92:95],a[176:179],a[20:23],v[92:95]
s_waitcnt lgkmcnt(15)
v_lshlrev_b32 v10, 16, v238
v_and_b32 v11, 0xffff0000, v238
MM v[112:115],a[128:131],a[16:19],v[112:115]
TR a[176:177],v229 offset:8192
TR a[178:179],v229 offset:9216
MM v[112:115],a[132:135],a[48:51],v[112:115]
v_lshlrev_b32 v12, 16, v239
v_and_b32 v13, 0xffff0000, v239
MM v[112:115],a[136:139],a[80:83],v[112:115]
TR a[180:181],v229 offset:10240
TR a[182:183],v229 offset:11264
MM v[112:115],a[140:143],a[112:115],v[112:115]
v_mul_f32 v14, 0xbfb8aa3b, v10
v_mul_f32 v15, 0xbfb8aa3b, v11
MM v[120:123],a[140:143],a[116:119],v[120:123]
TR a[184:185],v229 offset:12288
TR a[186:187],v229 offset:13312
MM v[120:123],a[136:139],a[84:87],v[120:123]
v_mul_f32 v42, 0xbfb8aa3b, v12
v_mul_f32 v43, 0xbfb8aa3b, v13
MM v[120:123],a[132:135],a[52:55],v[120:123]
TR a[188:189],v229 offset:14336
TR a[190:191],v229 offset:15360
MM v[120:123],a[128:131],a[20:23],v[120:123]
s_waitcnt lgkmcnt(15)
EX v14,v14
MM v[116:119],a[144:147],a[16:19],v[116:119]
TR a[128:129],v230 offset:0
TR a[130:131],v230 offset:2048
MM v[116:119],a[148:151],a[48:51],v[116:119]
buffer_load_dword v241, v253, s[20:23], s68 offen offset:4 sc1
buffer_load_dword v141, v253, s[20:23], s45 offen offset:4 sc1
MM v[116:119],a[152:155],a[80:83],v[116:119]
TR a[132:133],v230 offset:4096
TR a[134:135],v230 offset:6144
MM v[116:119],a[156:159],a[112:115],v[116:119]
EX v15,v15
MM v[124:127],a[156:159],a[116:119],v[124:127]
TR a[136:137],v230 offset:8192
TR a[138:139],v230 offset:10240
MM v[124:127],a[152:155],a[84:87],v[124:127]
EX v42,v42
MM v[124:127],a[148:151],a[52:55],v[124:127]
TR a[140:141],v230 offset:12288
TR a[142:143],v230 offset:14336
MM v[124:127],a[144:147],a[20:23],v[124:127]
s_waitcnt lgkmcnt(15)
EX v43,v43
MM v[144:147],a[160:163],a[16:19],v[144:147]
TR a[144:145],v244 offset:0
TR a[146:147],v244 offset:2048
MM v[144:147],a[164:167],a[48:51],v[144:147]
v_add_f32 v14, 1.0, v14
v_add_f32 v15, 1.0, v15
MM v[144:147],a[168:171],a[80:83],v[144:147]
TR a[148:149],v244 offset:4096
TR a[150:151],v244 offset:6144
MM v[144:147],a[172:175],a[112:115],v[144:147]
v_add_f32 v42, 1.0, v42
v_add_f32 v43, 1.0, v43
MM v[152:155],a[172:175],a[116:119],v[152:155]
TR a[152:153],v244 offset:8192
TR a[154:155],v244 offset:10240
MM v[152:155],a[168:171],a[84:87],v[152:155]
v_rcp_f32 v14, v14
MM v[152:155],a[164:167],a[52:55],v[152:155]
TR a[156:157],v244 offset:12288
TR a[158:159],v244 offset:14336
MM v[152:155],a[160:163],a[20:23],v[152:155]
s_waitcnt lgkmcnt(15)
v_rcp_f32 v15, v15
MM v[148:151],a[176:179],a[16:19],v[148:151]
TR a[160:161],v231 offset:0
TR a[162:163],v231 offset:2048
MM v[148:151],a[180:183],a[48:51],v[148:151]
v_rcp_f32 v42, v42
MM v[148:151],a[184:187],a[80:83],v[148:151]
TR a[164:165],v231 offset:4096
TR a[166:167],v231 offset:6144
MM v[148:151],a[188:191],a[112:115],v[148:151]
v_rcp_f32 v43, v43
MM v[156:159],a[188:191],a[116:119],v[156:159]
TR a[168:169],v231 offset:8192
TR a[170:171],v231 offset:10240
MM v[156:159],a[184:187],a[84:87],v[156:159]
v_mul_f32 v14, v10, v14
v_mul_f32 v15, v11, v15
MM v[156:159],a[180:183],a[52:55],v[156:159]
TR a[172:173],v231 offset:12288
TR a[174:175],v231 offset:14336
MM v[156:159],a[176:179],a[20:23],v[156:159]
s_waitcnt lgkmcnt(15)
v_mul_f32 v42, v12, v42
v_mul_f32 v43, v13, v43
s_waitcnt vmcnt(19)
MM v[16:19],a[128:131],a[24:27],v[16:19]
TR a[176:177],v235 offset:0
TR a[178:179],v235 offset:2048
MM v[16:19],a[132:135],a[56:59],v[16:19]
CV v6,v14,v15
CV v7,v42,v43
MM v[16:19],a[136:139],a[88:91],v[16:19]
s_add_u32 s80, s18, 0
TR a[180:181],v235 offset:4096
TR a[182:183],v235 offset:6144
MM v[16:19],a[140:143],a[120:123],v[16:19]
s_waitcnt vmcnt(15)
buffer_load_dwordx2 v[32:33], v251, s[32:35], s80 offen offset:0
MM v[24:27],a[140:143],a[124:127],v[24:27]
s_add_u32 s80, s18, 128
TR a[184:185],v235 offset:8192
TR a[186:187],v235 offset:10240
MM v[24:27],a[136:139],a[92:95],v[24:27]
buffer_load_dwordx2 v[34:35], v251, s[32:35], s80 offen offset:0
MM v[24:27],a[132:135],a[60:63],v[24:27]
s_add_u32 s80, s18, 40960
TR a[188:189],v235 offset:12288
TR a[190:191],v235 offset:14336
MM v[24:27],a[128:131],a[28:31],v[24:27]
s_waitcnt lgkmcnt(15)
buffer_load_dwordx2 v[36:37], v251, s[32:35], s80 offen offset:0
s_waitcnt vmcnt(14)
MM v[16:19],a[144:147],a[200:203],v[16:19]
s_add_u32 s80, s18, 41088
TR a[128:129],v230 offset:512
TR a[130:131],v230 offset:2560
MM v[16:19],a[148:151],a[216:219],v[16:19]
buffer_load_dwordx2 v[38:39], v251, s[32:35], s80 offen offset:0
MM v[16:19],a[152:155],a[232:235],v[16:19]
s_add_u32 s80, s63, 0
TR a[132:133],v230 offset:4608
TR a[134:135],v230 offset:6656
MM v[16:19],a[156:159],a[248:251],v[16:19]
s_waitcnt vmcnt(11)
buffer_load_dwordx2 v[40:41], v254, s[32:35], s80 offen offset:0
MM v[24:27],a[156:159],a[252:255],v[24:27]
TR a[136:137],v230 offset:8704
TR a[138:139],v230 offset:10752
MM v[24:27],a[152:155],a[236:239],v[24:27]
v_mov_b32 v77, 0x3f80
v_and_b32 v76, 16, v248
MM v[24:27],a[148:151],a[220:223],v[24:27]
TR a[140:141],v230 offset:12800
TR a[142:143],v230 offset:14848
MM v[24:27],a[144:147],a[204:207],v[24:27]
s_waitcnt lgkmcnt(15)
v_lshlrev_b32 v76, v76, v77
s_mov_b32 s28, 0x30003
MM v[20:23],a[160:163],a[24:27],v[20:23]
s_mov_b32 s29, s28
TR a[144:145],v244 offset:512
TR a[146:147],v244 offset:2560
MM v[20:23],a[164:167],a[56:59],v[20:23]
v_cndmask_b32 v64, 0, v76, s[28:29]
s_lshl_b64 s[28:29], s[28:29], 2
MM v[20:23],a[168:171],a[88:91],v[20:23]
TR a[148:149],v244 offset:4608
TR a[150:151],v244 offset:6656
MM v[20:23],a[172:175],a[120:123],v[20:23]
v_cndmask_b32 v65, 0, v76, s[28:29]
s_lshl_b64 s[28:29], s[28:29], 2
MM v[28:31],a[172:175],a[124:127],v[28:31]
TR a[152:153],v244 offset:8704
TR a[154:155],v244 offset:10752
MM v[28:31],a[168:171],a[92:95],v[28:31]
v_cndmask_b32 v66, 0, v76, s[28:29]
s_lshl_b64 s[28:29], s[28:29], 2
MM v[28:31],a[164:167],a[60:63],v[28:31]
TR a[156:157],v244 offset:12800
TR a[158:159],v244 offset:14848
MM v[28:31],a[160:163],a[28:31],v[28:31]
s_waitcnt lgkmcnt(15)
v_cndmask_b32 v67, 0, v76, s[28:29]
s_lshl_b64 s[28:29], s[28:29], 2
MM v[28:31],a[188:191],a[252:255],v[28:31]
TR a[160:161],v231 offset:512
TR a[162:163],v231 offset:2560
MM v[28:31],a[184:187],a[236:239],v[28:31]
v_cndmask_b32 v68, 0, v76, s[28:29]
s_lshl_b64 s[28:29], s[28:29], 2
MM v[28:31],a[180:183],a[220:223],v[28:31]
TR a[164:165],v231 offset:4608
TR a[166:167],v231 offset:6656
MM v[28:31],a[176:179],a[204:207],v[28:31]
v_cndmask_b32 v69, 0, v76, s[28:29]
s_lshl_b64 s[28:29], s[28:29], 2
MM v[20:23],a[176:179],a[200:203],v[20:23]
TR a[168:169],v231 offset:8704
TR a[170:171],v231 offset:10752
MM v[20:23],a[180:183],a[216:219],v[20:23]
v_cndmask_b32 v70, 0, v76, s[28:29]
s_lshl_b64 s[28:29], s[28:29], 2
MM v[20:23],a[184:187],a[232:235],v[20:23]
TR a[172:173],v231 offset:12800
TR a[174:175],v231 offset:14848
MM v[20:23],a[188:191],a[248:251],v[20:23]
s_waitcnt lgkmcnt(15)
v_cndmask_b32 v71, 0, v76, s[28:29]
MM v[48:51],a[128:131],a[24:27],v[48:51]
TR a[176:177],v235 offset:512
TR a[178:179],v235 offset:2560
MM v[48:51],a[132:135],a[56:59],v[48:51]
MM v[48:51],a[136:139],a[88:91],v[48:51]
TR a[180:181],v235 offset:4608
TR a[182:183],v235 offset:6656
MM v[48:51],a[140:143],a[120:123],v[48:51]
s_add_u32 s67, s46, 8192
CV v16,v16,v17
CV v17,v18,v19
MM v[56:59],a[140:143],a[124:127],v[56:59]
TR a[184:185],v235 offset:8704
TR a[186:187],v235 offset:10752
MM v[56:59],a[136:139],a[92:95],v[56:59]
s_waitcnt vmcnt(5)
CV v18,v20,v21
CV v19,v22,v23
MM v[56:59],a[132:135],a[60:63],v[56:59]
TR a[188:189],v235 offset:12800
TR a[190:191],v235 offset:14848
MM v[56:59],a[128:131],a[28:31],v[56:59]
s_waitcnt lgkmcnt(15)
buffer_store_dwordx4 v[16:19], v249, s[20:23], s67 offen offset:2048 sc1
MM v[56:59],a[156:159],a[252:255],v[56:59]
s_add_u32 s67, s47, 8192
TR a[128:129],v230 offset:32768
TR a[130:131],v230 offset:34816
v_cmp_eq_u32 s[8:9], s69, v241
MM v[56:59],a[152:155],a[236:239],v[56:59]
CV v24,v24,v25
CV v25,v26,v27
MM v[56:59],a[148:151],a[220:223],v[56:59]
TR a[132:133],v230 offset:36864
TR a[134:135],v230 offset:38912
MM v[56:59],a[144:147],a[204:207],v[56:59]
CV v26,v28,v29
CV v27,v30,v31
v_cmp_eq_u32 s[10:11], s69, v141
MM v[48:51],a[144:147],a[200:203],v[48:51]
)ASM"
R"ASM(
TR a[136:137],v230 offset:40960
TR a[138:139],v230 offset:43008
MM v[48:51],a[148:151],a[216:219],v[48:51]
buffer_store_dwordx4 v[24:27], v234, s[20:23], s67 offen offset:3072 sc1
MM v[48:51],a[152:155],a[232:235],v[48:51]
s_nop 0
TR a[140:141],v230 offset:45056
TR a[142:143],v230 offset:47104
MM v[48:51],a[156:159],a[248:251],v[48:51]
s_waitcnt lgkmcnt(15)
s_and_b32 s8, s8, 0xffff0000
MM v[52:55],a[160:163],a[24:27],v[52:55]
s_add_u32 s66, s16, 4096
TR a[144:145],v244 offset:32768
TR a[146:147],v244 offset:34816
MM v[52:55],a[164:167],a[56:59],v[52:55]
s_mov_b64 exec, s[8:9]
buffer_load_dwordx4 a[32:35], v250, s[20:23], s66 offen offset:0 sc1
s_mov_b64 exec, -1
ds_bpermute_b32 v16, v245, v16
MM v[52:55],a[168:171],a[88:91],v[52:55]
s_add_u32 s66, s44, 4096
TR a[148:149],v244 offset:36864
TR a[150:151],v244 offset:38912
MM v[52:55],a[172:175],a[120:123],v[52:55]
s_mov_b64 exec, s[10:11]
buffer_load_dwordx4 a[36:39], v250, s[20:23], s66 offen offset:0 sc1
s_mov_b64 exec, -1
MM v[60:63],a[172:175],a[124:127],v[60:63]
s_add_u32 s66, s16, 6144
ds_bpermute_b32 v17, v245, v17
TR a[152:153],v244 offset:40960
TR a[154:155],v244 offset:43008
MM v[60:63],a[168:171],a[92:95],v[60:63]
ds_bpermute_b32 v18, v245, v18
MM v[60:63],a[164:167],a[60:63],v[60:63]
TR a[156:157],v244 offset:45056
TR a[158:159],v244 offset:47104
MM v[60:63],a[160:163],a[28:31],v[60:63]
s_waitcnt lgkmcnt(15)
s_mov_b64 exec, s[8:9]
buffer_load_dwordx4 a[40:43], v250, s[20:23], s66 offen offset:0 sc1
s_mov_b64 exec, -1
ds_bpermute_b32 v19, v245, v19
MM v[60:63],a[188:191],a[252:255],v[60:63]
s_add_u32 s66, s44, 6144
TR a[160:161],v231 offset:32768
TR a[162:163],v231 offset:34816
MM v[60:63],a[184:187],a[236:239],v[60:63]
MM v[60:63],a[180:183],a[220:223],v[60:63]
TR a[164:165],v231 offset:36864
TR a[166:167],v231 offset:38912
MM v[60:63],a[176:179],a[204:207],v[60:63]
MM v[52:55],a[176:179],a[200:203],v[52:55]
TR a[168:169],v231 offset:40960
TR a[170:171],v231 offset:43008
MM v[52:55],a[180:183],a[216:219],v[52:55]
s_waitcnt lgkmcnt(6)
s_mov_b64 exec, s[10:11]
buffer_load_dwordx4 a[44:47], v250, s[20:23], s66 offen offset:0 sc1
s_mov_b64 exec, -1
MM v[52:55],a[184:187],a[232:235],v[52:55]
s_add_u32 s66, s16, 8192
TR a[172:173],v231 offset:45056
TR a[174:175],v231 offset:47104
MM v[52:55],a[188:191],a[248:251],v[52:55]
v_or_b32 v19, v104, v19
MM v[80:83],a[128:131],a[24:27],v[80:83]
TR a[176:177],v235 offset:32768
TR a[178:179],v235 offset:34816
MM v[80:83],a[132:135],a[56:59],v[80:83]
v_mov_b32 v252, s69
s_mov_b64 exec, 1
buffer_store_dword v252, off, s[20:23], s19 offset:8 sc1
s_mov_b64 exec, -1
MM v[80:83],a[136:139],a[88:91],v[80:83]
TR a[180:181],v235 offset:36864
TR a[182:183],v235 offset:38912
MM v[80:83],a[140:143],a[120:123],v[80:83]
s_add_u32 s67, s46, 12288
CV v48,v48,v49
CV v49,v50,v51
MM v[88:91],a[140:143],a[124:127],v[88:91]
TR a[184:185],v235 offset:40960
TR a[186:187],v235 offset:43008
MM v[88:91],a[136:139],a[92:95],v[88:91]
s_mov_b64 exec, s[8:9]
buffer_load_dwordx4 a[192:195], v250, s[20:23], s66 offen offset:0 sc1
s_mov_b64 exec, -1
CV v50,v52,v53
CV v51,v54,v55
MM v[88:91],a[132:135],a[60:63],v[88:91]
s_add_u32 s66, s44, 8192
TR a[188:189],v235 offset:45056
TR a[190:191],v235 offset:47104
MM v[88:91],a[128:131],a[28:31],v[88:91]
buffer_store_dwordx4 v[48:51], v249, s[20:23], s67 offen offset:0 sc1
MM v[88:91],a[156:159],a[252:255],v[88:91]
s_add_u32 s67, s47, 12288
TR a[128:129],v230 offset:33280
TR a[130:131],v230 offset:35328
MM v[88:91],a[152:155],a[236:239],v[88:91]
CV v56,v56,v57
CV v57,v58,v59
MM v[88:91],a[148:151],a[220:223],v[88:91]
TR a[132:133],v230 offset:37376
TR a[134:135],v230 offset:39424
MM v[88:91],a[144:147],a[204:207],v[88:91]
CV v58,v60,v61
CV v59,v62,v63
MM v[80:83],a[144:147],a[200:203],v[80:83]
TR a[136:137],v230 offset:41472
TR a[138:139],v230 offset:43520
MM v[80:83],a[148:151],a[216:219],v[80:83]
MM v[80:83],a[152:155],a[232:235],v[80:83]
s_nop 0
TR a[140:141],v230 offset:45568
TR a[142:143],v230 offset:47616
MM v[80:83],a[156:159],a[248:251],v[80:83]
s_waitcnt lgkmcnt(15)
MM v[84:87],a[160:163],a[24:27],v[84:87]
s_nop 0
TR a[144:145],v244 offset:33280
TR a[146:147],v244 offset:35328
MM v[84:87],a[164:167],a[56:59],v[84:87]
buffer_store_dwordx4 v[56:59], v234, s[20:23], s67 offen offset:1024 sc1
MM v[84:87],a[168:171],a[88:91],v[84:87]
s_nop 0
TR a[148:149],v244 offset:37376
TR a[150:151],v244 offset:39424
MM v[84:87],a[172:175],a[120:123],v[84:87]
s_nop 0
ds_bpermute_b32 v48, v245, v48
MM v[92:95],a[172:175],a[124:127],v[92:95]
s_nop 0
TR a[152:153],v244 offset:41472
TR a[154:155],v244 offset:43520
MM v[92:95],a[168:171],a[92:95],v[92:95]
LPG24_%=:
s_cbranch_execz LTR3_%=
ds_bpermute_b32 v49, v245, v49
MM v[92:95],a[164:167],a[60:63],v[92:95]
TR a[156:157],v244 offset:45568
TR a[158:159],v244 offset:47616
MM v[92:95],a[160:163],a[28:31],v[92:95]
s_waitcnt lgkmcnt(15)
ds_bpermute_b32 v50, v245, v50
MM v[92:95],a[188:191],a[252:255],v[92:95]
TR a[160:161],v231 offset:33280
TR a[162:163],v231 offset:35328
MM v[92:95],a[184:187],a[236:239],v[92:95]
ds_bpermute_b32 v51, v245, v51
MM v[92:95],a[180:183],a[220:223],v[92:95]
TR a[164:165],v231 offset:37376
TR a[166:167],v231 offset:39424
MM v[92:95],a[176:179],a[204:207],v[92:95]
s_waitcnt vmcnt(10)
MM v[84:87],a[176:179],a[200:203],v[84:87]
TR a[168:169],v231 offset:41472
TR a[170:171],v231 offset:43520
MM v[72:75],v[64:67],a[0:3],0
MM v[72:75],v[64:67],a[4:7],v[72:75]
MM v[72:75],v[68:71],a[8:11],v[72:75]
MM v[72:75],v[68:71],a[12:15],v[72:75]
MM v[84:87],a[180:183],a[216:219],v[84:87]
MM v[84:87],a[184:187],a[232:235],v[84:87]
TR a[172:173],v231 offset:45568
TR a[174:175],v231 offset:47616
MM v[84:87],a[188:191],a[248:251],v[84:87]
s_waitcnt lgkmcnt(15)
s_waitcnt lgkmcnt(6)
v_or_b32 v51, v104, v51
MM v[112:115],a[128:131],a[24:27],v[112:115]
s_cmp_eq_u32 s82, 0
s_cbranch_scc1 LY1_%=
v_xor_b32 v132, s69, v132
v_xor_b32 v133, s69, v133
v_xor_b32 v134, s69, v134
v_or3_b32 v139, v132, v133, v134
v_cmp_ne_u32 vcc, 0, v139
s_cbranch_vccnz LRYF_%=
LRYFb_%=:
s_ff1_i32_b32 s76, s82
s_and_b32 s76, s76, 7
s_mov_b32 s79, s52
s_cmp_eq_u32 s76, 1
s_cselect_b32 s79, s53, s79
s_cmp_eq_u32 s76, 2
s_cselect_b32 s79, s54, s79
s_cmp_eq_u32 s76, 3
s_cselect_b32 s79, s55, s79
s_cmp_eq_u32 s76, 4
s_cselect_b32 s79, s56, s79
s_cmp_eq_u32 s76, 5
s_cselect_b32 s79, s57, s79
s_cmp_eq_u32 s76, 6
s_cselect_b32 s79, s58, s79
s_cmp_eq_u32 s76, 7
s_cselect_b32 s79, s59, s79
s_mul_i32 s76, s76, 2560
s_mul_i32 s90, s84, 640
s_add_u32 s76, s76, s90
s_add_u32 s76, s76, 21659648
s_lshl_b32 s90, s79, 2
s_add_u32 s90, s90, 53248
buffer_load_dword v138, off, s[20:23], s90 sc1
s_mul_i32 s90, s79, 1280
s_mul_i32 s77, s84, 320
s_add_u32 s90, s90, s77
s_add_u32 s90, s90, 131072
v_lshrrev_b32 v139, 1, v248
s_mov_b32 exec_lo, -1
s_mov_b32 exec_hi, 255
buffer_load_dwordx2 v[136:137], v139, s[20:23], s90 offen offset:0 sc1
s_mov_b32 exec_lo, -1
s_mov_b32 exec_hi, 255
buffer_load_dwordx4 v[160:163], v248, s[20:23], s76 offen offset:0 sc1
s_add_u32 s76, s76, 20480
buffer_load_dwordx4 v[164:167], v248, s[20:23], s76 offen offset:0 sc1
s_add_u32 s76, s76, 20480
buffer_load_dwordx4 v[168:171], v248, s[20:23], s76 offen offset:0 sc1
s_add_u32 s76, s76, 20480
buffer_load_dwordx4 v[172:175], v248, s[20:23], s76 offen offset:0 sc1
s_add_u32 s76, s76, 20480
buffer_load_dwordx4 v[176:179], v248, s[20:23], s76 offen offset:0 sc1
s_add_u32 s76, s76, 20480
buffer_load_dwordx4 v[180:183], v248, s[20:23], s76 offen offset:0 sc1
s_add_u32 s76, s76, 20480
s_mov_b64 exec, -1
LY1_%=:
TR a[176:177],v235 offset:33280
TR a[178:179],v235 offset:35328
MM v[112:115],a[132:135],a[56:59],v[112:115]
s_mov_b64 exec, s[10:11]
buffer_load_dwordx4 a[196:199], v250, s[20:23], s66 offen offset:0 sc1
s_mov_b64 exec, -1
MM v[112:115],a[136:139],a[88:91],v[112:115]
TR a[180:181],v235 offset:37376
TR a[182:183],v235 offset:39424
MM v[112:115],a[140:143],a[120:123],v[112:115]
s_add_u32 s67, s46, 12288
CV v80,v80,v81
CV v81,v82,v83
MM v[120:123],a[140:143],a[124:127],v[120:123]
TR a[184:185],v235 offset:41472
TR a[186:187],v235 offset:43520
MM v[120:123],a[136:139],a[92:95],v[120:123]
CV v82,v84,v85
CV v83,v86,v87
MM v[120:123],a[132:135],a[60:63],v[120:123]
TR a[188:189],v235 offset:45568
TR a[190:191],v235 offset:47616
MM v[120:123],a[128:131],a[28:31],v[120:123]
buffer_store_dwordx4 v[80:83], v249, s[20:23], s67 offen offset:2048 sc1
MM v[120:123],a[156:159],a[252:255],v[120:123]
s_add_u32 s67, s47, 12288
TR a[128:129],v228 offset:16384
TR a[130:131],v228 offset:17408
MM v[120:123],a[152:155],a[236:239],v[120:123]
CV v88,v88,v89
CV v89,v90,v91
MM v[120:123],a[148:151],a[220:223],v[120:123]
TR a[132:133],v228 offset:18432
TR a[134:135],v228 offset:19456
MM v[120:123],a[144:147],a[204:207],v[120:123]
CV v90,v92,v93
CV v91,v94,v95
MM v[112:115],a[144:147],a[200:203],v[112:115]
TR a[136:137],v228 offset:20480
TR a[138:139],v228 offset:21504
MM v[112:115],a[148:151],a[216:219],v[112:115]
v_cmp_u_f32 s[30:31], v72, v72
CV v76,v72,v73
MM v[112:115],a[152:155],a[232:235],v[112:115]
TR a[140:141],v228 offset:22528
TR a[142:143],v228 offset:23552
MM v[112:115],a[156:159],a[248:251],v[112:115]
s_waitcnt lgkmcnt(15)
buffer_store_dwordx4 v[88:91], v234, s[20:23], s67 offen offset:3072 sc1
MM v[116:119],a[160:163],a[24:27],v[116:119]
TR a[144:145],v228 offset:24576
TR a[146:147],v228 offset:25600
MM v[116:119],a[164:167],a[56:59],v[116:119]
buffer_load_dword v242, v253, s[20:23], s68 offen offset:8 sc1
buffer_load_dword v142, v253, s[20:23], s45 offen offset:8 sc1
MM v[116:119],a[168:171],a[88:91],v[116:119]
TR a[148:149],v228 offset:26624
TR a[150:151],v228 offset:27648
MM v[116:119],a[172:175],a[120:123],v[116:119]
ds_bpermute_b32 v80, v245, v80
MM v[124:127],a[172:175],a[124:127],v[124:127]
TR a[152:153],v228 offset:28672
TR a[154:155],v228 offset:29696
MM v[124:127],a[168:171],a[92:95],v[124:127]
ds_bpermute_b32 v81, v245, v81
MM v[124:127],a[164:167],a[60:63],v[124:127]
TR a[156:157],v228 offset:30720
TR a[158:159],v228 offset:31744
MM v[124:127],a[160:163],a[28:31],v[124:127]
s_waitcnt lgkmcnt(15)
ds_bpermute_b32 v82, v245, v82
MM v[124:127],a[188:191],a[252:255],v[124:127]
s_cmp_eq_u32 s82, 0
s_cbranch_scc1 LY2_%=
s_mov_b32 exec_lo, -1
s_mov_b32 exec_hi, 255
buffer_load_dwordx4 v[184:187], v248, s[20:23], s76 offen offset:0 sc1
s_add_u32 s76, s76, 20480
buffer_load_dwordx4 v[188:191], v248, s[20:23], s76 offen offset:0 sc1
s_add_u32 s76, s76, 20480
buffer_load_dwordx4 v[192:195], v248, s[20:23], s76 offen offset:0 sc1
s_add_u32 s76, s76, 20480
buffer_load_dwordx4 v[196:199], v248, s[20:23], s76 offen offset:0 sc1
s_add_u32 s76, s76, 20480
buffer_load_dwordx4 v[200:203], v248, s[20:23], s76 offen offset:0 sc1
s_add_u32 s76, s76, 20480
buffer_load_dwordx4 v[204:207], v248, s[20:23], s76 offen offset:0 sc1
s_add_u32 s76, s76, 20480
s_mov_b64 exec, -1
LY2_%=:
TR a[160:161],v229 offset:16384
TR a[162:163],v229 offset:17408
MM v[124:127],a[184:187],a[236:239],v[124:127]
ds_bpermute_b32 v83, v245, v83
MM v[124:127],a[180:183],a[220:223],v[124:127]
TR a[164:165],v229 offset:18432
TR a[166:167],v229 offset:19456
MM v[124:127],a[176:179],a[204:207],v[124:127]
CV v77,v74,v75
MM v[116:119],a[176:179],a[200:203],v[116:119]
TR a[168:169],v229 offset:20480
TR a[170:171],v229 offset:21504
MM v[116:119],a[180:183],a[216:219],v[116:119]
ds_bpermute_b32 v76, v232, v76
MM v[116:119],a[184:187],a[232:235],v[116:119]
TR a[172:173],v229 offset:22528
TR a[174:175],v229 offset:23552
MM v[116:119],a[188:191],a[248:251],v[116:119]
s_waitcnt lgkmcnt(15)
s_waitcnt lgkmcnt(7)
v_or_b32 v83, v104, v83
MM v[144:147],a[128:131],a[24:27],v[144:147]
TR a[176:177],v229 offset:24576
TR a[178:179],v229 offset:25600
MM v[144:147],a[132:135],a[56:59],v[144:147]
ds_bpermute_b32 v77, v232, v77
MM v[144:147],a[136:139],a[88:91],v[144:147]
TR a[180:181],v229 offset:26624
TR a[182:183],v229 offset:27648
MM v[144:147],a[140:143],a[120:123],v[144:147]
s_add_u32 s67, s46, 16384
CV v112,v112,v113
CV v113,v114,v115
MM v[152:155],a[140:143],a[124:127],v[152:155]
)ASM"
R"ASM(
TR a[184:185],v229 offset:28672
TR a[186:187],v229 offset:29696
MM v[152:155],a[136:139],a[92:95],v[152:155]
CV v114,v116,v117
CV v115,v118,v119
MM v[152:155],a[132:135],a[60:63],v[152:155]
TR a[188:189],v229 offset:30720
TR a[190:191],v229 offset:31744
MM v[152:155],a[128:131],a[28:31],v[152:155]
buffer_store_dwordx4 v[112:115], v249, s[20:23], s67 offen offset:0 sc1
MM v[152:155],a[156:159],a[252:255],v[152:155]
s_add_u32 s67, s47, 16384
CV v120,v120,v121
CV v121,v122,v123
MM v[152:155],a[152:155],a[236:239],v[152:155]
CV v122,v124,v125
CV v123,v126,v127
MM v[152:155],a[148:151],a[220:223],v[152:155]
buffer_store_dwordx4 v[120:123], v234, s[20:23], s67 offen offset:1024 sc1
MM v[152:155],a[144:147],a[204:207],v[152:155]
v_lshlrev_b32 v10, 16, v0
v_and_b32 v11, 0xffff0000, v0
MM v[144:147],a[144:147],a[200:203],v[144:147]
ds_bpermute_b32 v112, v245, v112
MM v[144:147],a[148:151],a[216:219],v[144:147]
ds_bpermute_b32 v113, v245, v113
MM v[144:147],a[152:155],a[232:235],v[144:147]
ds_bpermute_b32 v114, v245, v114
MM v[144:147],a[156:159],a[248:251],v[144:147]
s_waitcnt lgkmcnt(12)
ds_bpermute_b32 v115, v245, v115
MM v[148:151],a[160:163],a[24:27],v[148:151]
s_cmp_eq_u32 s82, 0
s_cbranch_scc1 LY3_%=
s_mov_b32 exec_lo, -1
s_mov_b32 exec_hi, 255
buffer_load_dwordx4 v[208:211], v248, s[20:23], s76 offen offset:0 sc1
s_add_u32 s76, s76, 20480
buffer_load_dwordx4 v[212:215], v248, s[20:23], s76 offen offset:0 sc1
s_add_u32 s76, s76, 20480
buffer_load_dwordx4 v[216:219], v248, s[20:23], s76 offen offset:0 sc1
s_add_u32 s76, s76, 20480
buffer_load_dwordx4 v[220:223], v248, s[20:23], s76 offen offset:0 sc1
s_add_u32 s76, s76, 20480
buffer_load_dwordx4 v[96:99], v248, s[20:23], s76 offen offset:0 sc1
s_add_u32 s76, s76, 20480
buffer_load_dwordx4 v[100:103], v248, s[20:23], s76 offen offset:0 sc1
s_mov_b64 exec, -1
LY3_%=:
v_lshlrev_b32 v12, 16, v1
v_and_b32 v13, 0xffff0000, v1
MM v[148:151],a[164:167],a[56:59],v[148:151]
v_lshlrev_b32 v14, 16, v32
v_and_b32 v15, 0xffff0000, v32
MM v[148:151],a[168:171],a[88:91],v[148:151]
v_lshlrev_b32 v42, 16, v33
v_and_b32 v43, 0xffff0000, v33
MM v[148:151],a[172:175],a[120:123],v[148:151]
s_add_u32 s80, s18, 0
s_waitcnt lgkmcnt(10)
v_lshlrev_b32 v72, 16, v76
MM v[156:159],a[172:175],a[124:127],v[156:159]
v_and_b32 v73, 0xffff0000, v76
v_lshlrev_b32 v74, 16, v77
MM v[156:159],a[168:171],a[92:95],v[156:159]
s_waitcnt lgkmcnt(0)
v_or_b32 v115, v104, v115
v_and_b32 v75, 0xffff0000, v77
MM v[156:159],a[164:167],a[60:63],v[156:159]
v_mul_f32 v72, v72, v10
v_mul_f32 v73, v73, v11
MM v[156:159],a[160:163],a[28:31],v[156:159]
v_mul_f32 v74, v74, v12
v_mul_f32 v75, v75, v13
MM v[156:159],a[188:191],a[252:255],v[156:159]
CV v76,v72,v73
CV v77,v74,v75
MM v[156:159],a[184:187],a[236:239],v[156:159]
v_lshlrev_b32 v72, 16, v76
v_and_b32 v73, 0xffff0000, v76
MM v[156:159],a[180:183],a[220:223],v[156:159]
v_lshlrev_b32 v74, 16, v77
v_and_b32 v75, 0xffff0000, v77
MM v[156:159],a[176:179],a[204:207],v[156:159]
v_add_f32 v72, v72, v14
v_add_f32 v73, v73, v15
MM v[148:151],a[176:179],a[200:203],v[148:151]
v_add_f32 v74, v74, v42
v_add_f32 v75, v75, v43
MM v[148:151],a[180:183],a[216:219],v[148:151]
CV v44,v72,v73
CV v45,v74,v75
MM v[148:151],a[184:187],a[232:235],v[148:151]
s_cmp_eq_u64 s[30:31], 0
s_cselect_b32 s94, s51, 0
s_and_b32 s94, s94, 1
MM v[148:151],a[188:191],a[248:251],v[148:151]
s_xor_b32 s17, s17, s94
s_cmp_lg_u32 s94, 0
s_cselect_b64 exec, -1, 0
buffer_store_dwordx2 v[44:45], v251, s[40:43], s80 offen offset:0 nt
s_mov_b64 exec, -1
s_nop 5
s_add_u32 s67, s46, 16384
CV v144,v144,v145
CV v145,v146,v147
CV v146,v148,v149
CV v147,v150,v151
buffer_store_dwordx4 v[144:147], v249, s[20:23], s67 offen offset:2048 sc1
s_add_u32 s67, s47, 16384
CV v152,v152,v153
CV v153,v154,v155
CV v154,v156,v157
CV v155,v158,v159
buffer_store_dwordx4 v[152:155], v234, s[20:23], s67 offen offset:3072 sc1
ds_bpermute_b32 v144, v245, v144
ds_bpermute_b32 v145, v245, v145
ds_bpermute_b32 v146, v245, v146
ds_bpermute_b32 v147, v245, v147
s_waitcnt vmcnt(5)
v_cmp_eq_u32 s[12:13], s69, v242
v_cmp_eq_u32 s[14:15], s69, v142
s_nop 0
s_and_b32 s12, s12, 0xffff0000
s_nop 0
s_add_u32 s66, s16, 10240
s_mov_b64 exec, s[12:13]
buffer_load_dwordx4 v[16:19], v250, s[20:23], s66 offen offset:0 sc1
s_mov_b64 exec, -1
s_add_u32 s66, s44, 10240
s_mov_b64 exec, s[14:15]
buffer_load_dwordx4 a[68:71], v250, s[20:23], s66 offen offset:0 sc1
s_mov_b64 exec, -1
s_add_u32 s66, s16, 12288
s_mov_b64 exec, s[12:13]
buffer_load_dwordx4 v[48:51], v250, s[20:23], s66 offen offset:0 sc1
s_mov_b64 exec, -1
s_add_u32 s66, s44, 12288
s_mov_b64 exec, s[14:15]
buffer_load_dwordx4 a[76:79], v250, s[20:23], s66 offen offset:0 sc1
s_mov_b64 exec, -1
s_add_u32 s66, s16, 14336
s_mov_b64 exec, s[12:13]
buffer_load_dwordx4 v[80:83], v250, s[20:23], s66 offen offset:0 sc1
s_mov_b64 exec, -1
s_add_u32 s66, s44, 14336
s_mov_b64 exec, s[14:15]
buffer_load_dwordx4 a[100:103], v250, s[20:23], s66 offen offset:0 sc1
s_mov_b64 exec, -1
s_add_u32 s66, s16, 16384
s_mov_b64 exec, s[12:13]
buffer_load_dwordx4 v[112:115], v250, s[20:23], s66 offen offset:0 sc1
s_mov_b64 exec, -1
s_add_u32 s66, s44, 16384
s_mov_b64 exec, s[14:15]
buffer_load_dwordx4 a[108:111], v250, s[20:23], s66 offen offset:0 sc1
s_mov_b64 exec, -1
s_waitcnt lgkmcnt(0)
v_or_b32 v147, v104, v147
s_add_u32 s66, s16, 18432
s_mov_b64 exec, s[12:13]
buffer_load_dwordx4 v[144:147], v250, s[20:23], s66 offen offset:0 sc1
s_mov_b64 exec, -1
s_add_u32 s66, s44, 18432
s_mov_b64 exec, s[14:15]
buffer_load_dwordx4 a[212:215], v250, s[20:23], s66 offen offset:0 sc1
s_mov_b64 exec, -1
buffer_load_dwordx4 v[240:243], v253, s[20:23], s68 offen offset:0 sc1
buffer_load_dwordx4 v[140:143], v253, s[20:23], s45 offen offset:0 sc1
v_lshlrev_b32 v10, 16, v246
v_and_b32 v11, 0xffff0000, v246
v_lshlrev_b32 v12, 16, v247
v_and_b32 v13, 0xffff0000, v247
v_mul_f32 v14, 0xbfb8aa3b, v10
v_mul_f32 v15, 0xbfb8aa3b, v11
v_mul_f32 v42, 0xbfb8aa3b, v12
v_mul_f32 v43, 0xbfb8aa3b, v13
EX v14,v14
EX v15,v15
EX v42,v42
EX v43,v43
v_add_f32 v14, 1.0, v14
v_add_f32 v15, 1.0, v15
v_add_f32 v42, 1.0, v42
v_add_f32 v43, 1.0, v43
v_rcp_f32 v14, v14
v_rcp_f32 v15, v15
v_rcp_f32 v42, v42
v_rcp_f32 v43, v43
v_mul_f32 v14, v10, v14
v_mul_f32 v15, v11, v15
v_mul_f32 v42, v12, v42
v_mul_f32 v43, v13, v43
CV v8,v14,v15
CV v9,v42,v43
s_cmp_lg_u32 s82, 0
s_cbranch_scc1 LYS_%=
LYE_%=:
s_mov_b32 s73, 30001
s_mov_b32 s65, 1
s_bitcmp0_b32 s17, 0
s_cbranch_scc1 LSA0_%=
MM v[72:75],v[64:67],a[0:3],0
MM v[72:75],v[64:67],a[4:7],v[72:75]
MM v[72:75],v[68:71],a[8:11],v[72:75]
MM v[72:75],v[68:71],a[12:15],v[72:75]
s_nop 7
v_cmp_u_f32 s[30:31], v72, v72
s_nop 1
s_cmp_lg_u64 s[30:31], 0
s_cbranch_scc1 LSA0_%=
s_bitset0_b32 s17, 0
CV v76,v72,v73
CV v77,v74,v75
ds_bpermute_b32 v76, v232, v76
ds_bpermute_b32 v77, v232, v77
v_lshlrev_b32 v10, 16, v0
v_and_b32 v11, 0xffff0000, v0
v_lshlrev_b32 v12, 16, v1
v_and_b32 v13, 0xffff0000, v1
v_lshlrev_b32 v14, 16, v32
v_and_b32 v15, 0xffff0000, v32
v_lshlrev_b32 v42, 16, v33
v_and_b32 v43, 0xffff0000, v33
s_add_u32 s80, s18, 0
s_waitcnt lgkmcnt(0)
v_lshlrev_b32 v72, 16, v76
v_and_b32 v73, 0xffff0000, v76
v_lshlrev_b32 v74, 16, v77
v_and_b32 v75, 0xffff0000, v77
v_mul_f32 v72, v72, v10
v_mul_f32 v73, v73, v11
v_mul_f32 v74, v74, v12
v_mul_f32 v75, v75, v13
CV v76,v72,v73
CV v77,v74,v75
v_lshlrev_b32 v72, 16, v76
v_and_b32 v73, 0xffff0000, v76
v_lshlrev_b32 v74, 16, v77
v_and_b32 v75, 0xffff0000, v77
v_add_f32 v72, v72, v14
v_add_f32 v73, v73, v15
v_add_f32 v74, v74, v42
v_add_f32 v75, v75, v43
CV v44,v72,v73
CV v45,v74,v75
buffer_store_dwordx2 v[44:45], v251, s[40:43], s80 offen offset:0 nt
s_nop 1
LSA0_%=:
s_bitcmp0_b32 s17, 1
s_cbranch_scc1 LSA1_%=
MM v[72:75],v[64:67],a[32:35],0
MM v[72:75],v[64:67],a[36:39],v[72:75]
MM v[72:75],v[68:71],a[40:43],v[72:75]
MM v[72:75],v[68:71],a[44:47],v[72:75]
s_nop 7
v_cmp_u_f32 s[30:31], v72, v72
s_nop 1
s_cmp_lg_u64 s[30:31], 0
s_cbranch_scc1 LSA1_%=
s_bitset0_b32 s17, 1
CV v76,v72,v73
CV v77,v74,v75
ds_bpermute_b32 v76, v232, v76
ds_bpermute_b32 v77, v232, v77
v_lshlrev_b32 v10, 16, v2
v_and_b32 v11, 0xffff0000, v2
v_lshlrev_b32 v12, 16, v3
v_and_b32 v13, 0xffff0000, v3
v_lshlrev_b32 v14, 16, v34
v_and_b32 v15, 0xffff0000, v34
v_lshlrev_b32 v42, 16, v35
v_and_b32 v43, 0xffff0000, v35
s_add_u32 s80, s18, 128
s_waitcnt lgkmcnt(0)
v_lshlrev_b32 v72, 16, v76
v_and_b32 v73, 0xffff0000, v76
v_lshlrev_b32 v74, 16, v77
v_and_b32 v75, 0xffff0000, v77
v_mul_f32 v72, v72, v10
v_mul_f32 v73, v73, v11
v_mul_f32 v74, v74, v12
v_mul_f32 v75, v75, v13
CV v76,v72,v73
CV v77,v74,v75
v_lshlrev_b32 v72, 16, v76
v_and_b32 v73, 0xffff0000, v76
v_lshlrev_b32 v74, 16, v77
v_and_b32 v75, 0xffff0000, v77
v_add_f32 v72, v72, v14
v_add_f32 v73, v73, v15
v_add_f32 v74, v74, v42
v_add_f32 v75, v75, v43
CV v46,v72,v73
CV v47,v74,v75
buffer_store_dwordx2 v[46:47], v251, s[40:43], s80 offen offset:0 nt
s_nop 1
LSA1_%=:
s_waitcnt vmcnt(0)
s_mov_b64 exec, 1
buffer_store_dword v233, off, s[20:23], s19 offset:12 sc1
s_mov_b64 exec, -1
MM v[72:75],v[64:67],a[192:195],0
MM v[100:103],v[64:67],v[80:83],0
MM v[96:99],v[64:67],v[16:19],0
MM v[72:75],v[64:67],a[196:199],v[72:75]
MM v[100:103],v[64:67],a[100:103],v[100:103]
MM v[96:99],v[64:67],a[68:71],v[96:99]
MM v[72:75],v[68:71],v[144:147],v[72:75]
MM v[100:103],v[68:71],v[112:115],v[100:103]
MM v[96:99],v[68:71],v[48:51],v[96:99]
MM v[72:75],v[68:71],a[212:215],v[72:75]
MM v[100:103],v[68:71],a[108:111],v[100:103]
MM v[96:99],v[68:71],a[76:79],v[96:99]
s_mov_b32 s65, 0
s_mov_b32 s90, 250
s_nop 5
v_cmp_u_f32 vcc, v72, v100
v_cmp_u_f32 s[30:31], v96, v96
s_nop 1
s_or_b64 vcc, vcc, s[30:31]
s_cbranch_scc1 LQS_%=
CV v76,v72,v73
CV v77,v74,v75
CV v108,v100,v101
CV v109,v102,v103
CV v78,v96,v97
CV v79,v98,v99
ds_bpermute_b32 v76, v232, v76
ds_bpermute_b32 v77, v232, v77
ds_bpermute_b32 v108, v232, v108
ds_bpermute_b32 v109, v232, v109
ds_bpermute_b32 v78, v232, v78
ds_bpermute_b32 v79, v232, v79
v_lshlrev_b32 v10, 16, v8
v_and_b32 v11, 0xffff0000, v8
v_lshlrev_b32 v12, 16, v9
v_and_b32 v13, 0xffff0000, v9
v_lshlrev_b32 v14, 16, v40
v_and_b32 v15, 0xffff0000, v40
v_lshlrev_b32 v42, 16, v41
v_and_b32 v43, 0xffff0000, v41
s_add_u32 s80, s63, 0
s_waitcnt lgkmcnt(4)
v_lshlrev_b32 v72, 16, v76
v_and_b32 v73, 0xffff0000, v76
v_lshlrev_b32 v74, 16, v77
v_and_b32 v75, 0xffff0000, v77
v_mul_f32 v72, v72, v10
v_mul_f32 v73, v73, v11
v_mul_f32 v74, v74, v12
v_mul_f32 v75, v75, v13
CV v76,v72,v73
CV v77,v74,v75
v_lshlrev_b32 v72, 16, v76
v_and_b32 v73, 0xffff0000, v76
v_lshlrev_b32 v74, 16, v77
v_and_b32 v75, 0xffff0000, v77
v_add_f32 v72, v72, v14
v_add_f32 v73, v73, v15
v_add_f32 v74, v74, v42
v_add_f32 v75, v75, v43
CV v44,v72,v73
CV v45,v74,v75
buffer_store_dwordx2 v[44:45], v254, s[40:43], s80 offen offset:0 nt
v_lshlrev_b32 v10, 16, v6
v_and_b32 v11, 0xffff0000, v6
v_lshlrev_b32 v12, 16, v7
v_and_b32 v13, 0xffff0000, v7
v_lshlrev_b32 v14, 16, v38
v_and_b32 v15, 0xffff0000, v38
v_lshlrev_b32 v42, 16, v39
v_and_b32 v43, 0xffff0000, v39
s_add_u32 s80, s18, 41088
s_waitcnt lgkmcnt(2)
v_lshlrev_b32 v100, 16, v108
v_and_b32 v101, 0xffff0000, v108
v_lshlrev_b32 v102, 16, v109
v_and_b32 v103, 0xffff0000, v109
v_mul_f32 v100, v100, v10
v_mul_f32 v101, v101, v11
v_mul_f32 v102, v102, v12
v_mul_f32 v103, v103, v13
CV v108,v100,v101
CV v109,v102,v103
v_lshlrev_b32 v100, 16, v108
v_and_b32 v101, 0xffff0000, v108
v_lshlrev_b32 v102, 16, v109
v_and_b32 v103, 0xffff0000, v109
v_add_f32 v100, v100, v14
v_add_f32 v101, v101, v15
v_add_f32 v102, v102, v42
v_add_f32 v103, v103, v43
CV v46,v100,v101
CV v47,v102,v103
buffer_store_dwordx2 v[46:47], v251, s[40:43], s80 offen offset:0 nt
v_lshlrev_b32 v10, 16, v4
v_and_b32 v11, 0xffff0000, v4
v_lshlrev_b32 v12, 16, v5
v_and_b32 v13, 0xffff0000, v5
v_lshlrev_b32 v14, 16, v36
v_and_b32 v15, 0xffff0000, v36
v_lshlrev_b32 v42, 16, v37
v_and_b32 v43, 0xffff0000, v37
s_add_u32 s80, s18, 40960
s_waitcnt lgkmcnt(0)
v_lshlrev_b32 v96, 16, v78
v_and_b32 v97, 0xffff0000, v78
v_lshlrev_b32 v98, 16, v79
v_and_b32 v99, 0xffff0000, v79
v_mul_f32 v96, v96, v10
)ASM"
R"ASM(
v_mul_f32 v97, v97, v11
v_mul_f32 v98, v98, v12
v_mul_f32 v99, v99, v13
CV v78,v96,v97
CV v79,v98,v99
v_lshlrev_b32 v96, 16, v78
v_and_b32 v97, 0xffff0000, v78
v_lshlrev_b32 v98, 16, v79
v_and_b32 v99, 0xffff0000, v79
v_add_f32 v96, v96, v14
v_add_f32 v97, v97, v15
v_add_f32 v98, v98, v42
LPG25_%=:
s_nop 0
v_add_f32 v99, v99, v43
CV v44,v96,v97
CV v45,v98,v99
buffer_store_dwordx2 v[44:45], v251, s[40:43], s80 offen offset:0 nt
s_and_b32 s17, s17, 3
s_cbranch_scc0 LQE_%=
LQS_%=:
v_cmp_eq_u32 s[12:13], s69, v242
v_cmp_eq_u32 s[14:15], s69, v142
v_cmp_eq_u32 s[8:9], s69, v241
v_cmp_eq_u32 s[10:11], s69, v141
v_cmp_eq_u32 s[36:37], s69, v240
v_cmp_eq_u32 s[38:39], s69, v140
s_mov_b32 s94, 0
s_sub_u32 s90, s90, 1
s_cbranch_scc1 LSW_%=
s_and_b64 vcc, s[12:13], s[14:15]
s_and_b32 s12, s12, 0xffff0000
s_and_b32 s8, s8, 0xffff0000
s_and_b32 s36, s36, 0xffff0000
s_bitcmp0_b32 s17, 4
s_cbranch_scc1 LQG4_%=
AR v108,a195
AR v109,a199
AR v111,a215
v_cmp_eq_u32 s[24:25], -1, v108
v_cmp_eq_u32 s[26:27], -1, v109
v_cmp_eq_u32 s[28:29], -1, v147
v_cmp_eq_u32 s[46:47], -1, v111
s_nop 0
s_or_b64 s[30:31], s[24:25], s[26:27]
s_or_b64 s[30:31], s[30:31], s[28:29]
s_or_b64 s[30:31], s[30:31], s[46:47]
s_cselect_b32 s76, 256, 16
s_or_b32 s94, s94, s76
s_bitcmp1_b32 s94, 4
s_cbranch_scc1 LQG4_%=
s_and_b64 exec, s[46:47], s[14:15]
s_cbranch_scc0 LQK43_%=
s_add_u32 s66, s44, 18432
buffer_load_dwordx4 a[212:215], v250, s[20:23], s66 offen offset:0 sc1
LQK43_%=:
s_and_b64 exec, s[28:29], s[12:13]
s_cbranch_scc0 LQK42_%=
s_add_u32 s66, s16, 18432
buffer_load_dwordx4 v[144:147], v250, s[20:23], s66 offen offset:0 sc1
LQK42_%=:
s_and_b64 exec, s[26:27], s[10:11]
s_cbranch_scc0 LQK41_%=
s_add_u32 s66, s44, 8192
buffer_load_dwordx4 a[196:199], v250, s[20:23], s66 offen offset:0 sc1
LQK41_%=:
s_and_b64 exec, s[24:25], s[8:9]
s_cbranch_scc0 LQK40_%=
s_add_u32 s66, s16, 8192
buffer_load_dwordx4 a[192:195], v250, s[20:23], s66 offen offset:0 sc1
LQK40_%=:
s_mov_b64 exec, -1
LQG4_%=:
s_bitcmp0_b32 s17, 3
s_cbranch_scc1 LQG3_%=
AR v109,a103
AR v111,a111
v_cmp_eq_u32 s[24:25], -1, v83
v_cmp_eq_u32 s[26:27], -1, v109
v_cmp_eq_u32 s[28:29], -1, v115
v_cmp_eq_u32 s[46:47], -1, v111
s_nop 0
s_or_b64 s[30:31], s[24:25], s[26:27]
s_or_b64 s[30:31], s[30:31], s[28:29]
s_or_b64 s[30:31], s[30:31], s[46:47]
s_cselect_b32 s76, 256, 8
s_or_b32 s94, s94, s76
s_bitcmp1_b32 s94, 3
s_cbranch_scc1 LQG3_%=
s_and_b64 exec, s[46:47], s[14:15]
s_cbranch_scc0 LQK33_%=
s_add_u32 s66, s44, 16384
buffer_load_dwordx4 a[108:111], v250, s[20:23], s66 offen offset:0 sc1
LQK33_%=:
s_and_b64 exec, s[28:29], s[12:13]
s_cbranch_scc0 LQK32_%=
s_add_u32 s66, s16, 16384
buffer_load_dwordx4 v[112:115], v250, s[20:23], s66 offen offset:0 sc1
LQK32_%=:
s_and_b64 exec, s[26:27], s[14:15]
s_cbranch_scc0 LQK31_%=
s_add_u32 s66, s44, 14336
buffer_load_dwordx4 a[100:103], v250, s[20:23], s66 offen offset:0 sc1
LQK31_%=:
s_and_b64 exec, s[24:25], s[12:13]
s_cbranch_scc0 LQK30_%=
s_add_u32 s66, s16, 14336
buffer_load_dwordx4 v[80:83], v250, s[20:23], s66 offen offset:0 sc1
LQK30_%=:
s_mov_b64 exec, -1
LQG3_%=:
s_bitcmp0_b32 s17, 2
s_cbranch_scc1 LQG2_%=
AR v109,a71
AR v111,a79
v_cmp_eq_u32 s[24:25], -1, v19
v_cmp_eq_u32 s[26:27], -1, v109
v_cmp_eq_u32 s[28:29], -1, v51
v_cmp_eq_u32 s[46:47], -1, v111
s_nop 0
s_or_b64 s[30:31], s[24:25], s[26:27]
s_or_b64 s[30:31], s[30:31], s[28:29]
s_or_b64 s[30:31], s[30:31], s[46:47]
s_cselect_b32 s76, 256, 4
s_or_b32 s94, s94, s76
s_bitcmp1_b32 s94, 2
s_cbranch_scc1 LQG2_%=
s_and_b64 exec, s[46:47], s[14:15]
s_cbranch_scc0 LQK23_%=
s_add_u32 s66, s44, 12288
buffer_load_dwordx4 a[76:79], v250, s[20:23], s66 offen offset:0 sc1
LQK23_%=:
s_and_b64 exec, s[28:29], s[12:13]
s_cbranch_scc0 LQK22_%=
s_add_u32 s66, s16, 12288
buffer_load_dwordx4 v[48:51], v250, s[20:23], s66 offen offset:0 sc1
LQK22_%=:
s_and_b64 exec, s[26:27], s[14:15]
s_cbranch_scc0 LQK21_%=
s_add_u32 s66, s44, 10240
buffer_load_dwordx4 a[68:71], v250, s[20:23], s66 offen offset:0 sc1
LQK21_%=:
s_and_b64 exec, s[24:25], s[12:13]
s_cbranch_scc0 LQK20_%=
s_add_u32 s66, s16, 10240
buffer_load_dwordx4 v[16:19], v250, s[20:23], s66 offen offset:0 sc1
LQK20_%=:
s_mov_b64 exec, -1
LQG2_%=:
s_and_b32 s76, s17, 3
s_cbranch_scc1 LQA_%=
LQB_%=:
s_cmp_eq_u64 vcc, -1
s_cbranch_scc1 LQF_%=
buffer_load_dwordx4 v[240:243], v253, s[20:23], s68 offen offset:0 sc1
buffer_load_dwordx4 v[140:143], v253, s[20:23], s45 offen offset:0 sc1
LQF_%=:
s_and_b32 s76, s94, 3
s_cbranch_scc1 LQC_%=
LQD_%=:
s_bitcmp0_b32 s94, 2
s_cbranch_scc1 LQN2_%=
MM v[72:75],v[64:67],v[16:19],0
MM v[72:75],v[64:67],a[68:71],v[72:75]
MM v[72:75],v[68:71],v[48:51],v[72:75]
MM v[72:75],v[68:71],a[76:79],v[72:75]
s_bitset0_b32 s17, 2
s_nop 6
CV v76,v72,v73
CV v77,v74,v75
ds_bpermute_b32 v76, v232, v76
ds_bpermute_b32 v77, v232, v77
v_lshlrev_b32 v10, 16, v4
v_and_b32 v11, 0xffff0000, v4
v_lshlrev_b32 v12, 16, v5
v_and_b32 v13, 0xffff0000, v5
v_lshlrev_b32 v14, 16, v36
v_and_b32 v15, 0xffff0000, v36
v_lshlrev_b32 v42, 16, v37
v_and_b32 v43, 0xffff0000, v37
s_add_u32 s80, s18, 40960
s_waitcnt lgkmcnt(0)
v_lshlrev_b32 v72, 16, v76
v_and_b32 v73, 0xffff0000, v76
v_lshlrev_b32 v74, 16, v77
v_and_b32 v75, 0xffff0000, v77
v_mul_f32 v72, v72, v10
v_mul_f32 v73, v73, v11
v_mul_f32 v74, v74, v12
v_mul_f32 v75, v75, v13
CV v76,v72,v73
CV v77,v74,v75
v_lshlrev_b32 v72, 16, v76
v_and_b32 v73, 0xffff0000, v76
v_lshlrev_b32 v74, 16, v77
v_and_b32 v75, 0xffff0000, v77
v_add_f32 v72, v72, v14
v_add_f32 v73, v73, v15
v_add_f32 v74, v74, v42
v_add_f32 v75, v75, v43
CV v44,v72,v73
CV v45,v74,v75
buffer_store_dwordx2 v[44:45], v251, s[40:43], s80 offen offset:0 nt
s_bitcmp0_b32 s94, 8
s_cbranch_scc1 LQN2_%=
s_waitcnt vmcnt(1)
s_branch LQS_%=
LQN2_%=:
s_bitcmp0_b32 s94, 3
s_cbranch_scc1 LQN3_%=
MM v[72:75],v[64:67],v[80:83],0
MM v[72:75],v[64:67],a[100:103],v[72:75]
MM v[72:75],v[68:71],v[112:115],v[72:75]
MM v[72:75],v[68:71],a[108:111],v[72:75]
s_bitset0_b32 s17, 3
s_nop 6
CV v76,v72,v73
CV v77,v74,v75
ds_bpermute_b32 v76, v232, v76
ds_bpermute_b32 v77, v232, v77
v_lshlrev_b32 v10, 16, v6
v_and_b32 v11, 0xffff0000, v6
v_lshlrev_b32 v12, 16, v7
v_and_b32 v13, 0xffff0000, v7
v_lshlrev_b32 v14, 16, v38
v_and_b32 v15, 0xffff0000, v38
v_lshlrev_b32 v42, 16, v39
v_and_b32 v43, 0xffff0000, v39
s_add_u32 s80, s18, 41088
s_waitcnt lgkmcnt(0)
v_lshlrev_b32 v72, 16, v76
v_and_b32 v73, 0xffff0000, v76
v_lshlrev_b32 v74, 16, v77
v_and_b32 v75, 0xffff0000, v77
v_mul_f32 v72, v72, v10
v_mul_f32 v73, v73, v11
v_mul_f32 v74, v74, v12
v_mul_f32 v75, v75, v13
CV v76,v72,v73
CV v77,v74,v75
v_lshlrev_b32 v72, 16, v76
v_and_b32 v73, 0xffff0000, v76
v_lshlrev_b32 v74, 16, v77
v_and_b32 v75, 0xffff0000, v77
v_add_f32 v72, v72, v14
v_add_f32 v73, v73, v15
v_add_f32 v74, v74, v42
v_add_f32 v75, v75, v43
CV v46,v72,v73
CV v47,v74,v75
buffer_store_dwordx2 v[46:47], v251, s[40:43], s80 offen offset:0 nt
s_bitcmp0_b32 s94, 8
s_cbranch_scc1 LQN3_%=
s_waitcnt vmcnt(1)
s_branch LQS_%=
LQN3_%=:
s_bitcmp0_b32 s94, 4
s_cbranch_scc1 LQN4_%=
MM v[72:75],v[64:67],a[192:195],0
MM v[72:75],v[64:67],a[196:199],v[72:75]
MM v[72:75],v[68:71],v[144:147],v[72:75]
MM v[72:75],v[68:71],a[212:215],v[72:75]
s_bitset0_b32 s17, 4
s_nop 6
CV v76,v72,v73
CV v77,v74,v75
ds_bpermute_b32 v76, v232, v76
ds_bpermute_b32 v77, v232, v77
v_lshlrev_b32 v10, 16, v8
v_and_b32 v11, 0xffff0000, v8
v_lshlrev_b32 v12, 16, v9
v_and_b32 v13, 0xffff0000, v9
v_lshlrev_b32 v14, 16, v40
v_and_b32 v15, 0xffff0000, v40
v_lshlrev_b32 v42, 16, v41
v_and_b32 v43, 0xffff0000, v41
s_add_u32 s80, s63, 0
s_waitcnt lgkmcnt(0)
v_lshlrev_b32 v72, 16, v76
v_and_b32 v73, 0xffff0000, v76
v_lshlrev_b32 v74, 16, v77
v_and_b32 v75, 0xffff0000, v77
v_mul_f32 v72, v72, v10
v_mul_f32 v73, v73, v11
v_mul_f32 v74, v74, v12
v_mul_f32 v75, v75, v13
CV v76,v72,v73
CV v77,v74,v75
v_lshlrev_b32 v72, 16, v76
v_and_b32 v73, 0xffff0000, v76
v_lshlrev_b32 v74, 16, v77
v_and_b32 v75, 0xffff0000, v77
v_add_f32 v72, v72, v14
v_add_f32 v73, v73, v15
v_add_f32 v74, v74, v42
v_add_f32 v75, v75, v43
CV v44,v72,v73
CV v45,v74,v75
buffer_store_dwordx2 v[44:45], v254, s[40:43], s80 offen offset:0 nt
s_bitcmp0_b32 s94, 8
s_cbranch_scc1 LQN4_%=
s_waitcnt vmcnt(1)
s_branch LQS_%=
LQN4_%=:
s_cmp_eq_u32 s17, 0
s_cbranch_scc1 LQE_%=
s_waitcnt vmcnt(0)
s_branch LQS_%=
LQE_%=:
s_endpgm
LYS_%=:
s_waitcnt vmcnt(15)
LYT_%=:
v_ashrrev_i32 v139, 31, v138
v_not_b32 v139, v139
v_or_b32 v139, 0x80000000, v139
v_xor_b32 v138, v138, v139
v_and_b32 v138, 0xffff0000, v138
v_mul_f32 v138, 0xbfb8aa3b, v138
EX v138,v138
v_pk_add_f32 v[160:161], v[160:161], v[196:197]
v_pk_add_f32 v[162:163], v[162:163], v[198:199]
v_pk_add_f32 v[164:165], v[164:165], v[200:201]
v_pk_add_f32 v[166:167], v[166:167], v[202:203]
v_pk_add_f32 v[168:169], v[168:169], v[204:205]
v_pk_add_f32 v[170:171], v[170:171], v[206:207]
v_pk_add_f32 v[172:173], v[172:173], v[208:209]
v_pk_add_f32 v[174:175], v[174:175], v[210:211]
v_pk_add_f32 v[176:177], v[176:177], v[212:213]
v_pk_add_f32 v[178:179], v[178:179], v[214:215]
v_pk_add_f32 v[180:181], v[180:181], v[216:217]
v_pk_add_f32 v[182:183], v[182:183], v[218:219]
v_pk_add_f32 v[184:185], v[184:185], v[220:221]
v_pk_add_f32 v[186:187], v[186:187], v[222:223]
v_pk_add_f32 v[188:189], v[188:189], v[96:97]
v_pk_add_f32 v[190:191], v[190:191], v[98:99]
v_pk_add_f32 v[192:193], v[192:193], v[100:101]
v_pk_add_f32 v[194:195], v[194:195], v[102:103]
v_pk_add_f32 v[160:161], v[160:161], v[192:193]
v_pk_add_f32 v[162:163], v[162:163], v[194:195]
v_add_f32 v138, 1.0, v138
v_rcp_f32 v138, v138
v_pk_add_f32 v[160:161], v[160:161], v[176:177]
v_pk_add_f32 v[162:163], v[162:163], v[178:179]
v_pk_add_f32 v[164:165], v[164:165], v[180:181]
v_pk_add_f32 v[166:167], v[166:167], v[182:183]
v_pk_add_f32 v[168:169], v[168:169], v[184:185]
v_pk_add_f32 v[170:171], v[170:171], v[186:187]
v_pk_add_f32 v[172:173], v[172:173], v[188:189]
v_pk_add_f32 v[174:175], v[174:175], v[190:191]
CV v138,v138,v138
v_and_b32 v138, 0xffff0000, v138
v_pk_add_f32 v[160:161], v[160:161], v[168:169]
v_pk_add_f32 v[162:163], v[162:163], v[170:171]
v_pk_add_f32 v[164:165], v[164:165], v[172:173]
v_pk_add_f32 v[166:167], v[166:167], v[174:175]
v_pk_add_f32 v[160:161], v[160:161], v[164:165]
v_pk_add_f32 v[162:163], v[162:163], v[166:167]
CV v164,v160,v161
CV v165,v162,v163
v_lshlrev_b32 v160, 16, v164
v_and_b32 v161, 0xffff0000, v164
v_lshlrev_b32 v162, 16, v165
v_and_b32 v163, 0xffff0000, v165
v_mul_f32 v160, v160, v138
v_mul_f32 v161, v161, v138
v_mul_f32 v162, v162, v138
v_mul_f32 v163, v163, v138
CV v164,v160,v161
CV v165,v162,v163
v_lshlrev_b32 v160, 16, v164
v_and_b32 v161, 0xffff0000, v164
v_lshlrev_b32 v162, 16, v165
v_and_b32 v163, 0xffff0000, v165
v_lshlrev_b32 v168, 16, v136
v_and_b32 v169, 0xffff0000, v136
v_lshlrev_b32 v170, 16, v137
v_and_b32 v171, 0xffff0000, v137
v_add_f32 v160, v160, v168
v_add_f32 v161, v161, v169
v_add_f32 v162, v162, v170
v_add_f32 v163, v163, v171
CV v164,v160,v161
CV v165,v162,v163
v_lshlrev_b32 v160, 16, v164
v_and_b32 v161, 0xffff0000, v164
v_lshlrev_b32 v162, 16, v165
v_and_b32 v163, 0xffff0000, v165
v_mul_f32 v168, 0xbfb8aa3b, v160
v_mul_f32 v169, 0xbfb8aa3b, v161
v_mul_f32 v170, 0xbfb8aa3b, v162
v_mul_f32 v171, 0xbfb8aa3b, v163
EX v168,v168
EX v169,v169
EX v170,v170
EX v171,v171
v_bfe_u32 v139, v248, 2, 6
v_and_b32 v132, 28, v139
v_add_f32 v168, 1.0, v168
v_add_f32 v169, 1.0, v169
v_add_f32 v170, 1.0, v170
v_add_f32 v171, 1.0, v171
v_rcp_f32 v168, v168
v_rcp_f32 v169, v169
v_rcp_f32 v170, v170
v_rcp_f32 v171, v171
s_and_b32 s77, s84, 1
s_lshl_b32 s76, s77, 5
v_add_u32 v139, s76, v139
s_xor_b32 s77, s77, 1
s_lshl_b32 s77, s77, 7
v_add_u32 v132, s77, v132
v_mul_f32 v168, v160, v168
v_mul_f32 v169, v161, v169
v_mul_f32 v170, v162, v170
v_mul_f32 v171, v163, v171
CV v172,v168,v169
CV v173,v170,v171
ds_bpermute_b32 v176, v139, v172 offset:0
ds_bpermute_b32 v177, v139, v173 offset:0
ds_bpermute_b32 v180, v139, v172 offset:64
ds_bpermute_b32 v181, v139, v173 offset:64
ds_bpermute_b32 v184, v132, v172 offset:0
ds_bpermute_b32 v185, v132, v173 offset:0
s_ff1_i32_b32 s76, s82
)ASM"
R"ASM(
s_bitset0_b32 s82, s76
s_and_b32 s77, s79, 3
s_lshl_b32 s77, s77, 4
s_and_b32 s76, s76, 8
s_waitcnt lgkmcnt(0)
s_lshl_b64 vcc, 0xffff, s77
s_cmp_eq_u32 s76, 0
s_cselect_b64 exec, vcc, 0
v_mov_b32 v0, v176
v_mov_b32 v1, v177
v_mov_b32 v2, v180
v_mov_b32 v3, v181
s_cselect_b64 exec, 0, vcc
v_mov_b32 v4, v176
v_mov_b32 v5, v177
v_mov_b32 v6, v180
v_mov_b32 v7, v181
s_add_u32 s77, s77, s76
s_lshl_b64 exec, 0xff, s77
v_mov_b32 v8, v184
v_mov_b32 v9, v185
s_mov_b64 exec, -1
s_cmp_eq_u32 s82, 0
s_cbranch_scc1 LYE_%=
s_ff1_i32_b32 s76, s82
s_and_b32 s76, s76, 7
s_mov_b32 s79, s52
s_cmp_eq_u32 s76, 1
s_cselect_b32 s79, s53, s79
s_cmp_eq_u32 s76, 2
s_cselect_b32 s79, s54, s79
s_cmp_eq_u32 s76, 3
s_cselect_b32 s79, s55, s79
s_cmp_eq_u32 s76, 4
s_cselect_b32 s79, s56, s79
s_cmp_eq_u32 s76, 5
s_cselect_b32 s79, s57, s79
s_cmp_eq_u32 s76, 6
s_cselect_b32 s79, s58, s79
s_cmp_eq_u32 s76, 7
s_cselect_b32 s79, s59, s79
s_mul_i32 s76, s76, 2560
s_mul_i32 s90, s84, 640
s_add_u32 s76, s76, s90
s_add_u32 s76, s76, 21659648
s_lshl_b32 s90, s79, 2
s_add_u32 s90, s90, 53248
buffer_load_dword v138, off, s[20:23], s90 sc1
s_mul_i32 s90, s79, 1280
s_mul_i32 s77, s84, 320
s_add_u32 s90, s90, s77
s_add_u32 s90, s90, 131072
v_lshrrev_b32 v139, 1, v248
s_mov_b32 exec_lo, -1
s_mov_b32 exec_hi, 255
buffer_load_dwordx2 v[136:137], v139, s[20:23], s90 offen offset:0 sc1
s_mov_b32 exec_lo, -1
s_mov_b32 exec_hi, 255
buffer_load_dwordx4 v[160:163], v248, s[20:23], s76 offen offset:0 sc1
s_add_u32 s76, s76, 20480
buffer_load_dwordx4 v[164:167], v248, s[20:23], s76 offen offset:0 sc1
s_add_u32 s76, s76, 20480
buffer_load_dwordx4 v[168:171], v248, s[20:23], s76 offen offset:0 sc1
s_add_u32 s76, s76, 20480
buffer_load_dwordx4 v[172:175], v248, s[20:23], s76 offen offset:0 sc1
s_add_u32 s76, s76, 20480
buffer_load_dwordx4 v[176:179], v248, s[20:23], s76 offen offset:0 sc1
s_add_u32 s76, s76, 20480
buffer_load_dwordx4 v[180:183], v248, s[20:23], s76 offen offset:0 sc1
s_add_u32 s76, s76, 20480
buffer_load_dwordx4 v[184:187], v248, s[20:23], s76 offen offset:0 sc1
s_add_u32 s76, s76, 20480
buffer_load_dwordx4 v[188:191], v248, s[20:23], s76 offen offset:0 sc1
s_add_u32 s76, s76, 20480
buffer_load_dwordx4 v[192:195], v248, s[20:23], s76 offen offset:0 sc1
s_add_u32 s76, s76, 20480
buffer_load_dwordx4 v[196:199], v248, s[20:23], s76 offen offset:0 sc1
s_add_u32 s76, s76, 20480
buffer_load_dwordx4 v[200:203], v248, s[20:23], s76 offen offset:0 sc1
s_add_u32 s76, s76, 20480
buffer_load_dwordx4 v[204:207], v248, s[20:23], s76 offen offset:0 sc1
s_add_u32 s76, s76, 20480
buffer_load_dwordx4 v[208:211], v248, s[20:23], s76 offen offset:0 sc1
s_add_u32 s76, s76, 20480
buffer_load_dwordx4 v[212:215], v248, s[20:23], s76 offen offset:0 sc1
s_add_u32 s76, s76, 20480
buffer_load_dwordx4 v[216:219], v248, s[20:23], s76 offen offset:0 sc1
s_add_u32 s76, s76, 20480
buffer_load_dwordx4 v[220:223], v248, s[20:23], s76 offen offset:0 sc1
s_add_u32 s76, s76, 20480
buffer_load_dwordx4 v[96:99], v248, s[20:23], s76 offen offset:0 sc1
s_add_u32 s76, s76, 20480
buffer_load_dwordx4 v[100:103], v248, s[20:23], s76 offen offset:0 sc1
s_mov_b64 exec, -1
s_waitcnt vmcnt(0)
s_branch LYT_%=
.fill 302, 4, 0xbf800000
LXP_%=:
LPG26_%=:
s_cbranch_execz LTR1_%=
v_mov_b32 v250, s69
s_mov_b32 s76, 32768
s_mov_b64 exec, 1
buffer_store_dword v250, off, s[20:23], s76 sc1
s_mov_b64 exec, -1
s_add_u32 s79, %[slds], 154112
s_lshl_b32 s80, %[swave], 2
MM v[64:67],a[192:195],v[160:163],v[128:131]
s_add_u32 s64, s64, 16384
TR a[128:129],v232 offset:0
s_add_u32 s81, s64, 896
MM v[64:67],a[196:199],v[176:179],v[64:67]
TR a[130:131],v232 offset:4096
TR a[132:133],v232 offset:8192
MM v[64:67],a[200:203],v[192:195],v[64:67]
TR a[134:135],v232 offset:12288
TR a[136:137],v233 offset:0
MM v[64:67],a[204:207],v[208:211],v[64:67]
TR a[138:139],v233 offset:4096
v_add_f32 v246, v236, v237
MM v[68:71],a[220:223],v[208:211],v[128:131]
TR a[140:141],v233 offset:8192
v_cmp_nge_f32 vcc, s71, v246
MM v[68:71],a[216:219],v[192:195],v[68:71]
s_cbranch_vccnz LSITE4_%=
LBACK4_%=:
MM v[68:71],a[212:215],v[176:179],v[68:71]
s_mov_b32 m0, s67
TR a[142:143],v233 offset:12288
TR a[144:145],v232 offset:512
MM v[68:71],a[208:211],v[160:163],v[68:71]
buffer_load_dwordx4 v252, s[36:39], s66 offen offset:0 lds
MM v[76:79],a[208:211],v[164:167],v[132:135]
s_add_u32 s66, s66, 55296
EX v64,v64
MM v[76:79],a[212:215],v[180:183],v[76:79]
s_min_u32 s66, s66, s85
EX v65,v65
MM v[76:79],a[216:219],v[196:199],v[76:79]
s_add_u32 s67, s67, 1024
TR a[146:147],v232 offset:4608
CV v64,v64,v65
MM v[76:79],a[220:223],v[212:215],v[76:79]
s_min_u32 s67, s67, s86
EX v66,v66
MM v[72:75],a[204:207],v[212:215],v[132:135]
EX v67,v67
MM v[72:75],a[200:203],v[196:199],v[72:75]
TR a[148:149],v232 offset:8704
CV v65,v66,v67
MM v[72:75],a[196:199],v[180:183],v[72:75]
EX v68,v68
MM v[72:75],a[192:195],v[164:167],v[72:75]
TR a[150:151],v232 offset:12800
TR a[152:153],v233 offset:512
MM v[80:83],a[192:195],v[168:171],v[136:139]
EX v69,v69
MM v[80:83],a[196:199],v[184:187],v[80:83]
TR a[154:155],v233 offset:4608
CV v66,v68,v69
MM v[80:83],a[200:203],v[200:203],v[80:83]
EX v70,v70
MM v[80:83],a[204:207],v[216:219],v[80:83]
EX v71,v71
MM v[84:87],a[220:223],v[216:219],v[136:139]
TR a[156:157],v233 offset:8704
CV v67,v70,v71
MM v[84:87],a[216:219],v[200:203],v[84:87]
EX v72,v72
MM v[84:87],a[212:215],v[184:187],v[84:87]
TR a[158:159],v233 offset:12800
TR a[160:161],v232 offset:1024
MM v[84:87],a[208:211],v[168:171],v[84:87]
EX v73,v73
MM v[92:95],a[208:211],v[172:175],v[140:143]
TR a[162:163],v232 offset:5120
CV v72,v72,v73
MM v[92:95],a[212:215],v[188:191],v[92:95]
EX v74,v74
MM v[92:95],a[216:219],v[204:207],v[92:95]
EX v75,v75
MM v[92:95],a[220:223],v[220:223],v[92:95]
TR a[164:165],v232 offset:9216
CV v73,v74,v75
MM v[88:91],a[204:207],v[220:223],v[140:143]
EX v76,v76
MM v[88:91],a[200:203],v[204:207],v[88:91]
TR a[166:167],v232 offset:13312
TR a[168:169],v233 offset:1024
MM v[88:91],a[196:199],v[188:191],v[88:91]
EX v77,v77
MM v[88:91],a[192:195],v[172:175],v[88:91]
TR a[170:171],v233 offset:5120
CV v74,v76,v77
MM v[96:99],a[224:227],v[160:163],v[128:131]
EX v78,v78
MM v[96:99],a[228:231],v[176:179],v[96:99]
EX v79,v79
MM v[96:99],a[232:235],v[192:195],v[96:99]
TR a[172:173],v233 offset:9216
CV v75,v78,v79
MM v[96:99],a[236:239],v[208:211],v[96:99]
TR a[174:175],v233 offset:13312
TR a[176:177],v232 offset:1536
MM v[100:103],a[252:255],v[208:211],v[128:131]
EX v80,v80
MM v[100:103],a[248:251],v[192:195],v[100:103]
EX v81,v81
MM v[100:103],a[244:247],v[176:179],v[100:103]
TR a[178:179],v232 offset:5632
CV v80,v80,v81
MM v[100:103],a[240:243],v[160:163],v[100:103]
EX v82,v82
MM v[108:111],a[240:243],v[164:167],v[132:135]
v_add_u32 v48, s78, v248
v_mov_b32 v49, s79
MM v[108:111],a[244:247],v[180:183],v[108:111]
EX v83,v83
MM v[108:111],a[248:251],v[196:199],v[108:111]
TR a[180:181],v232 offset:9728
CV v81,v82,v83
MM v[108:111],a[252:255],v[212:215],v[108:111]
EX v84,v84
MM v[104:107],a[236:239],v[212:215],v[132:135]
TR a[182:183],v232 offset:13824
TR a[184:185],v233 offset:1536
MM v[104:107],a[232:235],v[196:199],v[104:107]
EX v85,v85
MM v[104:107],a[228:231],v[180:183],v[104:107]
TR a[186:187],v233 offset:5632
CV v82,v84,v85
MM v[104:107],a[224:227],v[164:167],v[104:107]
EX v86,v86
MM v[112:115],a[224:227],v[168:171],v[136:139]
TR a[188:189],v233 offset:9728
TR a[190:191],v233 offset:13824
MM v[112:115],a[228:231],v[184:187],v[112:115]
EX v87,v87
MM v[112:115],a[232:235],v[200:203],v[112:115]
CV v83,v86,v87
MM v[112:115],a[236:239],v[216:219],v[112:115]
EX v88,v88
MM v[116:119],a[252:255],v[216:219],v[136:139]
EX v89,v89
MM v[116:119],a[248:251],v[200:203],v[116:119]
EX v90,v90
MM v[116:119],a[244:247],v[184:187],v[116:119]
EX v91,v91
MM v[116:119],a[240:243],v[168:171],v[116:119]
EX v92,v92
MM v[124:127],a[240:243],v[172:175],v[140:143]
EX v93,v93
MM v[124:127],a[244:247],v[188:191],v[124:127]
EX v94,v94
MM v[124:127],a[248:251],v[204:207],v[124:127]
EX v95,v95
MM v[124:127],a[252:255],v[220:223],v[124:127]
EX v96,v96
MM v[120:123],a[236:239],v[220:223],v[140:143]
EX v97,v97
MM v[120:123],a[232:235],v[204:207],v[120:123]
EX v98,v98
MM v[120:123],a[228:231],v[188:191],v[120:123]
EX v99,v99
MM v[120:123],a[224:227],v[172:175],v[120:123]
s_waitcnt vmcnt(13) lgkmcnt(0)
s_barrier
MM a[0:3],a[128:131],v[0:3],a[0:3]
s_add_u32 m0, s65, 0
RD v[32:35],v48 offset:0
RD v[36:39],v48 offset:1024
MM a[0:3],a[132:135],v[4:7],a[0:3]
s_mov_b32 s92, -1
CV v88,v88,v89
CV v89,v90,v91
MM a[8:11],a[132:135],v[12:15],a[8:11]
RD a[192:195],v224 offset:32768
CV v90,v92,v93
MM a[8:11],a[128:131],v[8:11],a[8:11]
RD a[196:199],v225 offset:32768
CV v91,v94,v95
MM a[16:19],a[128:131],v[16:19],a[16:19]
buffer_load_dwordx4 v228, s[8:11], s64 offen offset:0 lds
MM a[16:19],a[132:135],v[20:23],a[16:19]
s_waitcnt lgkmcnt(2)
v_sub_u32 v32, s92, v32
v_sub_u32 v33, s92, v33
MM a[24:27],a[132:135],v[28:31],a[24:27]
v_sub_u32 v34, s92, v34
v_sub_u32 v35, s92, v35
MM a[24:27],a[128:131],v[24:27],a[24:27]
v_sub_u32 v36, s92, v36
v_sub_u32 v37, s92, v37
MM a[28:31],a[136:139],v[24:27],a[28:31]
v_sub_u32 v38, s92, v38
v_sub_u32 v39, s92, v39
MM a[28:31],a[140:143],v[28:31],a[28:31]
v_min3_u32 v52, v32, v33, v34
v_min3_u32 v51, v35, v36, v37
MM a[20:23],a[140:143],v[20:23],a[20:23]
v_min3_u32 v52, v52, v38, v39
v_min_u32 v52, v52, v51
MM a[20:23],a[136:139],v[16:19],a[20:23]
s_nop 0
v_min_u32_dpp v52, v52, v52 row_shr:1 row_mask:0xf bank_mask:0xf
CV v68,v96,v97
MM a[12:15],a[136:139],v[8:11],a[12:15]
RD a[200:203],v226 offset:32768
v_min_u32_dpp v52, v52, v52 row_shr:2 row_mask:0xf bank_mask:0xf
MM a[12:15],a[140:143],v[12:15],a[12:15]
s_nop 0
v_min_u32_dpp v52, v52, v52 row_shr:4 row_mask:0xf bank_mask:0xf
CV v69,v98,v99
MM a[4:7],a[140:143],v[4:7],a[4:7]
RD a[204:207],v227 offset:32768
v_min_u32_dpp v52, v52, v52 row_shr:8 row_mask:0xf bank_mask:0xf
MM a[4:7],a[136:139],v[0:3],a[4:7]
RD a[208:211],v224 offset:36864
v_min_u32_dpp v52, v52, v52 row_bcast:15 row_mask:0xa bank_mask:0xf
MM a[32:35],a[144:147],v[0:3],a[32:35]
RD a[212:215],v225 offset:36864
v_min_u32_dpp v52, v52, v52 row_bcast:31 row_mask:0xc bank_mask:0xf
EX v100,v100
MM a[32:35],a[148:151],v[4:7],a[32:35]
v_readlane_b32 s76, v52, 63
v_lshl_add_u32 v49, s80, 1, v49
EX v101,v101
MM a[40:43],a[148:151],v[12:15],a[40:43]
EX v102,v102
MM a[40:43],a[144:147],v[8:11],a[40:43]
EX v103,v103
MM a[48:51],a[144:147],v[16:19],a[48:51]
buffer_load_dwordx4 v229, s[8:11], s64 offen offset:1024 lds
MM a[48:51],a[148:151],v[20:23],a[48:51]
s_add_u32 s76, s76, 1
v_subrev_u32 v32, s76, v32
v_subrev_u32 v33, s76, v33
MM a[56:59],a[148:151],v[28:31],a[56:59]
v_subrev_u32 v34, s76, v34
v_subrev_u32 v35, s76, v35
EX v104,v104
MM a[56:59],a[144:147],v[24:27],a[56:59]
v_subrev_u32 v36, s76, v36
v_subrev_u32 v37, s76, v37
MM a[60:63],a[152:155],v[24:27],a[60:63]
v_subrev_u32 v38, s76, v38
v_subrev_u32 v39, s76, v39
EX v105,v105
MM a[60:63],a[156:159],v[28:31],a[60:63]
v_min3_u32 v53, v32, v33, v34
v_min3_u32 v51, v35, v36, v37
MM a[52:55],a[156:159],v[20:23],a[52:55]
v_min3_u32 v53, v53, v38, v39
v_min_u32 v53, v53, v51
EX v106,v106
MM a[52:55],a[152:155],v[16:19],a[52:55]
v_min_u32_dpp v53, v53, v53 row_shr:1 row_mask:0xf bank_mask:0xf
CV v70,v100,v101
MM a[44:47],a[152:155],v[8:11],a[44:47]
v_min_u32_dpp v53, v53, v53 row_shr:2 row_mask:0xf bank_mask:0xf
CV v71,v102,v103
EX v107,v107
MM a[44:47],a[156:159],v[12:15],a[44:47]
v_min_u32_dpp v53, v53, v53 row_shr:4 row_mask:0xf bank_mask:0xf
CV v76,v104,v105
MM a[36:39],a[156:159],v[4:7],a[36:39]
v_min_u32_dpp v53, v53, v53 row_shr:8 row_mask:0xf bank_mask:0xf
CV v77,v106,v107
EX v108,v108
MM a[36:39],a[152:155],v[0:3],a[36:39]
RD a[216:219],v226 offset:36864
v_min_u32_dpp v53, v53, v53 row_bcast:15 row_mask:0xa bank_mask:0xf
MM a[64:67],a[160:163],v[0:3],a[64:67]
RD a[220:223],v227 offset:36864
v_min_u32_dpp v53, v53, v53 row_bcast:31 row_mask:0xc bank_mask:0xf
EX v109,v109
MM a[64:67],a[164:167],v[4:7],a[64:67]
v_add_u32 v53, s76, v53
CV v78,v108,v109
MM a[72:75],a[164:167],v[12:15],a[72:75]
)ASM"
R"ASM(
s_mov_b32 exec_lo, 0
s_mov_b32 exec_hi, 0x80000000
ds_write_b64 v49, v[52:53] offset:0
s_mov_b64 exec, -1
EX v110,v110
MM a[72:75],a[160:163],v[8:11],a[72:75]
buffer_load_dwordx4 v230, s[8:11], s64 offen offset:2048 lds
MM a[80:83],a[160:163],v[16:19],a[80:83]
EX v111,v111
MM a[80:83],a[164:167],v[20:23],a[80:83]
EX v112,v112
MM a[88:91],a[164:167],v[28:31],a[88:91]
EX v113,v113
MM a[88:91],a[160:163],v[24:27],a[88:91]
buffer_load_dwordx4 v231, s[8:11], s64 offen offset:3072 lds
MM a[92:95],a[168:171],v[24:27],a[92:95]
s_add_u32 m0, s65, 65536
EX v114,v114
MM a[92:95],a[172:175],v[28:31],a[92:95]
RD a[224:227],v224 offset:40960
CV v79,v110,v111
MM a[84:87],a[172:175],v[20:23],a[84:87]
EX v115,v115
MM a[84:87],a[168:171],v[16:19],a[84:87]
EX v116,v116
MM a[76:79],a[168:171],v[8:11],a[76:79]
buffer_load_dwordx4 v235, s[12:15], s81 offen offset:0 lds
MM a[76:79],a[172:175],v[12:15],a[76:79]
EX v117,v117
MM a[68:71],a[172:175],v[4:7],a[68:71]
RD a[228:231],v225 offset:40960
CV v84,v112,v113
MM a[68:71],a[168:171],v[0:3],a[68:71]
EX v118,v118
MM a[96:99],a[176:179],v[0:3],a[96:99]
RD a[232:235],v226 offset:40960
CV v85,v114,v115
MM a[96:99],a[180:183],v[4:7],a[96:99]
EX v119,v119
MM a[104:107],a[180:183],v[12:15],a[104:107]
RD a[236:239],v227 offset:40960
CV v86,v116,v117
MM a[104:107],a[176:179],v[8:11],a[104:107]
EX v120,v120
MM a[112:115],a[176:179],v[16:19],a[112:115]
buffer_load_dwordx4 v235, s[12:15], s64 offen offset:1024 lds
MM a[112:115],a[180:183],v[20:23],a[112:115]
EX v121,v121
MM a[120:123],a[180:183],v[28:31],a[120:123]
EX v122,v122
MM a[120:123],a[176:179],v[24:27],a[120:123]
RD a[240:243],v224 offset:45056
CV v87,v118,v119
MM a[124:127],a[184:187],v[24:27],a[124:127]
EX v123,v123
MM a[124:127],a[188:191],v[28:31],a[124:127]
RD a[244:247],v225 offset:45056
CV v92,v120,v121
MM a[116:119],a[188:191],v[20:23],a[116:119]
EX v124,v124
MM a[116:119],a[184:187],v[16:19],a[116:119]
buffer_load_dwordx4 v235, s[12:15], s81 offen offset:2048 lds
MM a[108:111],a[184:187],v[8:11],a[108:111]
EX v125,v125
MM a[108:111],a[188:191],v[12:15],a[108:111]
RD a[248:251],v226 offset:45056
CV v93,v122,v123
MM a[100:103],a[188:191],v[4:7],a[100:103]
EX v126,v126
MM a[100:103],a[184:187],v[0:3],a[100:103]
EX v127,v127
v_smfmac_f32_16x16x64_bf16 v[236:239], v[144:147], v[64:71], v234
RD a[252:255],v227 offset:45056
v_smfmac_f32_16x16x64_bf16 v[236:239], v[148:151], v[72:79], v234
CV v94,v124,v125
v_smfmac_f32_16x16x64_bf16 v[236:239], v[152:155], v[80:87], v234
CV v95,v126,v127
s_nop 1
v_smfmac_f32_16x16x64_bf16 v[236:239], v[156:159], v[88:95], v234
s_waitcnt lgkmcnt(0)
buffer_load_dwordx4 v235, s[12:15], s64 offen offset:3072 lds
MM v[0:3],a[192:195],v[160:163],v[128:131]
s_add_u32 s64, s64, 16384
TR a[128:129],v232 offset:16384
s_add_u32 s81, s64, 896
MM v[0:3],a[196:199],v[176:179],v[0:3]
TR a[130:131],v232 offset:20480
TR a[132:133],v232 offset:24576
MM v[0:3],a[200:203],v[192:195],v[0:3]
TR a[134:135],v232 offset:28672
TR a[136:137],v233 offset:16384
MM v[0:3],a[204:207],v[208:211],v[0:3]
TR a[138:139],v233 offset:20480
v_add_f32 v247, v236, v237
MM v[4:7],a[220:223],v[208:211],v[128:131]
TR a[140:141],v233 offset:24576
v_cmp_nge_f32 vcc, s71, v247
MM v[4:7],a[216:219],v[192:195],v[4:7]
s_cbranch_vccnz LSITE5_%=
LBACK5_%=:
MM v[4:7],a[212:215],v[176:179],v[4:7]
s_mov_b32 m0, s67
TR a[142:143],v233 offset:28672
TR a[144:145],v232 offset:16896
MM v[4:7],a[208:211],v[160:163],v[4:7]
buffer_load_dwordx4 v252, s[36:39], s66 offen offset:0 lds
MM v[12:15],a[208:211],v[164:167],v[132:135]
s_add_u32 s66, s66, 55296
EX v0,v0
MM v[12:15],a[212:215],v[180:183],v[12:15]
s_min_u32 s66, s66, s85
EX v1,v1
MM v[12:15],a[216:219],v[196:199],v[12:15]
s_add_u32 s67, s67, 1024
TR a[146:147],v232 offset:20992
CV v0,v0,v1
MM v[12:15],a[220:223],v[212:215],v[12:15]
s_min_u32 s67, s67, s86
EX v2,v2
MM v[8:11],a[204:207],v[212:215],v[132:135]
EX v3,v3
MM v[8:11],a[200:203],v[196:199],v[8:11]
TR a[148:149],v232 offset:25088
CV v1,v2,v3
MM v[8:11],a[196:199],v[180:183],v[8:11]
EX v4,v4
MM v[8:11],a[192:195],v[164:167],v[8:11]
TR a[150:151],v232 offset:29184
TR a[152:153],v233 offset:16896
MM v[16:19],a[192:195],v[168:171],v[136:139]
EX v5,v5
MM v[16:19],a[196:199],v[184:187],v[16:19]
TR a[154:155],v233 offset:20992
CV v2,v4,v5
MM v[16:19],a[200:203],v[200:203],v[16:19]
EX v6,v6
MM v[16:19],a[204:207],v[216:219],v[16:19]
EX v7,v7
MM v[20:23],a[220:223],v[216:219],v[136:139]
TR a[156:157],v233 offset:25088
CV v3,v6,v7
MM v[20:23],a[216:219],v[200:203],v[20:23]
EX v8,v8
MM v[20:23],a[212:215],v[184:187],v[20:23]
TR a[158:159],v233 offset:29184
TR a[160:161],v232 offset:17408
MM v[20:23],a[208:211],v[168:171],v[20:23]
EX v9,v9
MM v[28:31],a[208:211],v[172:175],v[140:143]
TR a[162:163],v232 offset:21504
CV v8,v8,v9
MM v[28:31],a[212:215],v[188:191],v[28:31]
EX v10,v10
MM v[28:31],a[216:219],v[204:207],v[28:31]
EX v11,v11
MM v[28:31],a[220:223],v[220:223],v[28:31]
TR a[164:165],v232 offset:25600
CV v9,v10,v11
MM v[24:27],a[204:207],v[220:223],v[140:143]
EX v12,v12
MM v[24:27],a[200:203],v[204:207],v[24:27]
TR a[166:167],v232 offset:29696
TR a[168:169],v233 offset:17408
MM v[24:27],a[196:199],v[188:191],v[24:27]
EX v13,v13
MM v[24:27],a[192:195],v[172:175],v[24:27]
TR a[170:171],v233 offset:21504
CV v10,v12,v13
MM v[32:35],a[224:227],v[160:163],v[128:131]
EX v14,v14
MM v[32:35],a[228:231],v[176:179],v[32:35]
EX v15,v15
MM v[32:35],a[232:235],v[192:195],v[32:35]
TR a[172:173],v233 offset:25600
CV v11,v14,v15
MM v[32:35],a[236:239],v[208:211],v[32:35]
TR a[174:175],v233 offset:29696
TR a[176:177],v232 offset:17920
MM v[36:39],a[252:255],v[208:211],v[128:131]
EX v16,v16
MM v[36:39],a[248:251],v[192:195],v[36:39]
EX v17,v17
MM v[36:39],a[244:247],v[176:179],v[36:39]
TR a[178:179],v232 offset:22016
CV v16,v16,v17
MM v[36:39],a[240:243],v[160:163],v[36:39]
EX v18,v18
MM v[44:47],a[240:243],v[164:167],v[132:135]
v_add_u32 v112, s78, v248
v_mov_b32 v113, s79
MM v[44:47],a[244:247],v[180:183],v[44:47]
EX v19,v19
MM v[44:47],a[248:251],v[196:199],v[44:47]
TR a[180:181],v232 offset:26112
CV v17,v18,v19
MM v[44:47],a[252:255],v[212:215],v[44:47]
EX v20,v20
MM v[40:43],a[236:239],v[212:215],v[132:135]
TR a[182:183],v232 offset:30208
TR a[184:185],v233 offset:17920
MM v[40:43],a[232:235],v[196:199],v[40:43]
EX v21,v21
MM v[40:43],a[228:231],v[180:183],v[40:43]
TR a[186:187],v233 offset:22016
CV v18,v20,v21
MM v[40:43],a[224:227],v[164:167],v[40:43]
EX v22,v22
MM v[48:51],a[224:227],v[168:171],v[136:139]
TR a[188:189],v233 offset:26112
TR a[190:191],v233 offset:30208
MM v[48:51],a[228:231],v[184:187],v[48:51]
EX v23,v23
MM v[48:51],a[232:235],v[200:203],v[48:51]
CV v19,v22,v23
MM v[48:51],a[236:239],v[216:219],v[48:51]
EX v24,v24
MM v[52:55],a[252:255],v[216:219],v[136:139]
EX v25,v25
MM v[52:55],a[248:251],v[200:203],v[52:55]
EX v26,v26
MM v[52:55],a[244:247],v[184:187],v[52:55]
EX v27,v27
MM v[52:55],a[240:243],v[168:171],v[52:55]
EX v28,v28
MM v[60:63],a[240:243],v[172:175],v[140:143]
EX v29,v29
MM v[60:63],a[244:247],v[188:191],v[60:63]
EX v30,v30
MM v[60:63],a[248:251],v[204:207],v[60:63]
EX v31,v31
MM v[60:63],a[252:255],v[220:223],v[60:63]
EX v32,v32
MM v[56:59],a[236:239],v[220:223],v[140:143]
EX v33,v33
MM v[56:59],a[232:235],v[204:207],v[56:59]
EX v34,v34
MM v[56:59],a[228:231],v[188:191],v[56:59]
EX v35,v35
MM v[56:59],a[224:227],v[172:175],v[56:59]
s_waitcnt vmcnt(13) lgkmcnt(0)
s_barrier
MM a[0:3],a[128:131],v[64:67],a[0:3]
s_add_u32 m0, s65, 16384
RD v[104:107],v113 offset:0
RD v[108:111],v113 offset:16
MM a[0:3],a[132:135],v[68:71],a[0:3]
RD v[96:99],v112 offset:0
RD v[100:103],v112 offset:1024
MM a[8:11],a[132:135],v[76:79],a[8:11]
CV v24,v24,v25
CV v25,v26,v27
MM a[8:11],a[128:131],v[72:75],a[8:11]
RD a[192:195],v224 offset:49152
CV v26,v28,v29
MM a[16:19],a[128:131],v[80:83],a[16:19]
RD a[196:199],v225 offset:49152
CV v27,v30,v31
MM a[16:19],a[132:135],v[84:87],a[16:19]
buffer_load_dwordx4 v228, s[8:11], s64 offen offset:0 lds
MM a[24:27],a[132:135],v[92:95],a[24:27]
s_waitcnt lgkmcnt(4)
v_min3_u32 v114, v104, v105, v106
v_min3_u32 v115, v107, v108, v109
MM a[24:27],a[128:131],v[88:91],a[24:27]
v_min3_u32 v114, v114, v110, v111
v_min_u32 v114, v114, v115
MM a[28:31],a[136:139],v[88:91],a[28:31]
v_readfirstlane_b32 s76, v114
CV v4,v32,v33
MM a[28:31],a[140:143],v[92:95],a[28:31]
s_sub_u32 s92, s92, s76
RD a[200:203],v226 offset:49152
CV v5,v34,v35
MM a[20:23],a[140:143],v[84:87],a[20:23]
buffer_load_dwordx4 v229, s[8:11], s64 offen offset:1024 lds
MM a[20:23],a[136:139],v[80:83],a[20:23]
s_add_u32 s76, s76, 1
v_subrev_u32 v104, s76, v104
v_subrev_u32 v105, s76, v105
MM a[12:15],a[136:139],v[72:75],a[12:15]
v_subrev_u32 v106, s76, v106
v_subrev_u32 v107, s76, v107
MM a[12:15],a[140:143],v[76:79],a[12:15]
v_subrev_u32 v108, s76, v108
v_subrev_u32 v109, s76, v109
MM a[4:7],a[140:143],v[68:71],a[4:7]
v_subrev_u32 v110, s76, v110
v_subrev_u32 v111, s76, v111
MM a[4:7],a[136:139],v[64:67],a[4:7]
s_and_b32 s77, s92, 65535
EX v36,v36
MM a[32:35],a[144:147],v[64:67],a[32:35]
s_sub_u32 s52, 65535, s77
v_min3_u32 v114, v104, v105, v106
v_min3_u32 v115, v107, v108, v109
MM a[32:35],a[148:151],v[68:71],a[32:35]
v_min3_u32 v114, v114, v110, v111
v_min_u32 v114, v114, v115
EX v37,v37
MM a[40:43],a[148:151],v[76:79],a[40:43]
RD a[204:207],v227 offset:49152
v_readfirstlane_b32 s76, v114
MM a[40:43],a[144:147],v[72:75],a[40:43]
s_sub_u32 s92, s92, s76
EX v38,v38
MM a[48:51],a[144:147],v[80:83],a[48:51]
s_sub_u32 s92, s92, 1
EX v39,v39
MM a[48:51],a[148:151],v[84:87],a[48:51]
s_and_b32 s77, s92, 65535
EX v40,v40
MM a[56:59],a[148:151],v[92:95],a[56:59]
s_sub_u32 s53, 65535, s77
CV v6,v36,v37
CV v7,v38,v39
MM a[56:59],a[144:147],v[88:91],a[56:59]
s_sub_u32 s92, s92, 1
EX v41,v41
MM a[60:63],a[152:155],v[88:91],a[60:63]
s_waitcnt lgkmcnt(4)
v_sub_u32 v96, s92, v96
v_sub_u32 v97, s92, v97
MM a[60:63],a[156:159],v[92:95],a[60:63]
v_sub_u32 v98, s92, v98
LPG27_%=:
s_cbranch_execz LTR1_%=
v_sub_u32 v99, s92, v99
MM a[52:55],a[156:159],v[84:87],a[52:55]
v_sub_u32 v100, s92, v100
v_sub_u32 v101, s92, v101
EX v42,v42
MM a[52:55],a[152:155],v[80:83],a[52:55]
v_sub_u32 v102, s92, v102
v_sub_u32 v103, s92, v103
MM a[44:47],a[152:155],v[72:75],a[44:47]
v_min3_u32 v116, v96, v97, v98
v_min3_u32 v115, v99, v100, v101
EX v43,v43
MM a[44:47],a[156:159],v[76:79],a[44:47]
v_min3_u32 v116, v116, v102, v103
v_min_u32 v116, v116, v115
MM a[36:39],a[156:159],v[68:71],a[36:39]
RD a[208:211],v224 offset:53248
v_min_u32_dpp v116, v116, v116 row_shr:1 row_mask:0xf bank_mask:0xf
EX v44,v44
MM a[36:39],a[152:155],v[64:67],a[36:39]
v_min_u32_dpp v116, v116, v116 row_shr:2 row_mask:0xf bank_mask:0xf
CV v12,v40,v41
MM a[64:67],a[160:163],v[64:67],a[64:67]
RD a[212:215],v225 offset:53248
v_min_u32_dpp v116, v116, v116 row_shr:4 row_mask:0xf bank_mask:0xf
EX v45,v45
MM a[64:67],a[164:167],v[68:71],a[64:67]
v_min_u32_dpp v116, v116, v116 row_shr:8 row_mask:0xf bank_mask:0xf
CV v13,v42,v43
MM a[72:75],a[164:167],v[76:79],a[72:75]
RD a[216:219],v226 offset:53248
v_min_u32_dpp v116, v116, v116 row_bcast:15 row_mask:0xa bank_mask:0xf
EX v46,v46
MM a[72:75],a[160:163],v[72:75],a[72:75]
v_min_u32_dpp v116, v116, v116 row_bcast:31 row_mask:0xc bank_mask:0xf
CV v14,v44,v45
EX v47,v47
MM a[80:83],a[160:163],v[80:83],a[80:83]
v_readlane_b32 s76, v116, 63
v_lshl_add_u32 v113, s80, 1, v113
MM a[80:83],a[164:167],v[84:87],a[80:83]
buffer_load_dwordx4 v230, s[8:11], s64 offen offset:2048 lds
MM a[88:91],a[164:167],v[92:95],a[88:91]
s_add_u32 s76, s76, 1
v_subrev_u32 v96, s76, v96
v_subrev_u32 v97, s76, v97
EX v48,v48
MM a[88:91],a[160:163],v[88:91],a[88:91]
v_subrev_u32 v98, s76, v98
v_subrev_u32 v99, s76, v99
EX v49,v49
MM a[92:95],a[168:171],v[88:91],a[92:95]
v_subrev_u32 v100, s76, v100
v_subrev_u32 v101, s76, v101
MM a[92:95],a[172:175],v[92:95],a[92:95]
v_subrev_u32 v102, s76, v102
v_subrev_u32 v103, s76, v103
EX v50,v50
MM a[84:87],a[172:175],v[84:87],a[84:87]
v_min3_u32 v117, v96, v97, v98
v_min3_u32 v115, v99, v100, v101
MM a[84:87],a[168:171],v[80:83],a[84:87]
v_min3_u32 v117, v117, v102, v103
)ASM"
R"ASM(
v_min_u32 v117, v117, v115
EX v51,v51
MM a[76:79],a[168:171],v[72:75],a[76:79]
RD a[220:223],v227 offset:53248
v_min_u32_dpp v117, v117, v117 row_shr:1 row_mask:0xf bank_mask:0xf
MM a[76:79],a[172:175],v[76:79],a[76:79]
s_nop 0
v_min_u32_dpp v117, v117, v117 row_shr:2 row_mask:0xf bank_mask:0xf
CV v15,v46,v47
EX v52,v52
MM a[68:71],a[172:175],v[68:71],a[68:71]
RD a[224:227],v224 offset:57344
v_min_u32_dpp v117, v117, v117 row_shr:4 row_mask:0xf bank_mask:0xf
EX v53,v53
MM a[68:71],a[168:171],v[64:67],a[68:71]
v_min_u32_dpp v117, v117, v117 row_shr:8 row_mask:0xf bank_mask:0xf
CV v20,v48,v49
MM a[96:99],a[176:179],v[64:67],a[96:99]
RD a[228:231],v225 offset:57344
v_min_u32_dpp v117, v117, v117 row_bcast:15 row_mask:0xa bank_mask:0xf
EX v54,v54
MM a[96:99],a[180:183],v[68:71],a[96:99]
v_min_u32_dpp v117, v117, v117 row_bcast:31 row_mask:0xc bank_mask:0xf
CV v21,v50,v51
MM a[104:107],a[180:183],v[76:79],a[104:107]
RD a[232:235],v226 offset:57344
v_add_u32 v117, s76, v117
EX v55,v55
MM a[104:107],a[176:179],v[72:75],a[104:107]
s_mov_b32 exec_lo, 0
s_mov_b32 exec_hi, 0x80000000
ds_write_b64 v113, v[116:117] offset:32
s_mov_b64 exec, -1
MM a[112:115],a[176:179],v[80:83],a[112:115]
buffer_load_dwordx4 v231, s[8:11], s64 offen offset:3072 lds
MM a[112:115],a[180:183],v[84:87],a[112:115]
s_add_u32 m0, s65, 81920
EX v56,v56
MM a[120:123],a[180:183],v[92:95],a[120:123]
buffer_load_dwordx4 v235, s[12:15], s81 offen offset:0 lds
MM a[120:123],a[176:179],v[88:91],a[120:123]
EX v57,v57
MM a[124:127],a[184:187],v[88:91],a[124:127]
EX v58,v58
MM a[124:127],a[188:191],v[92:95],a[124:127]
RD a[236:239],v227 offset:57344
CV v22,v52,v53
MM a[116:119],a[188:191],v[84:87],a[116:119]
EX v59,v59
MM a[116:119],a[184:187],v[80:83],a[116:119]
buffer_load_dwordx4 v235, s[12:15], s64 offen offset:1024 lds
MM a[108:111],a[184:187],v[72:75],a[108:111]
EX v60,v60
CV v23,v54,v55
MM a[108:111],a[188:191],v[76:79],a[108:111]
EX v61,v61
MM a[100:103],a[188:191],v[68:71],a[100:103]
RD a[240:243],v224 offset:61440
CV v28,v56,v57
MM a[100:103],a[184:187],v[64:67],a[100:103]
EX v62,v62
v_smfmac_f32_16x16x64_bf16 v[236:239], v[144:147], v[0:7], v234
RD a[244:247],v225 offset:61440
RD a[248:251],v226 offset:61440
RD a[252:255],v227 offset:61440
EX v63,v63
v_smfmac_f32_16x16x64_bf16 v[236:239], v[148:151], v[8:15], v234
CV v29,v58,v59
v_smfmac_f32_16x16x64_bf16 v[236:239], v[152:155], v[16:23], v234
CV v30,v60,v61
CV v31,v62,v63
s_nop 1
v_smfmac_f32_16x16x64_bf16 v[236:239], v[156:159], v[24:31], v234
s_waitcnt lgkmcnt(0)
buffer_load_dwordx4 v235, s[12:15], s81 offen offset:2048 lds
buffer_load_dwordx4 v235, s[12:15], s64 offen offset:3072 lds
MM v[64:67],a[192:195],v[160:163],v[128:131]
s_add_u32 s64, s64, 16384
TR a[128:129],v232 offset:32768
s_add_u32 s81, s64, 896
MM v[64:67],a[196:199],v[176:179],v[64:67]
TR a[130:131],v232 offset:36864
TR a[132:133],v232 offset:40960
MM v[64:67],a[200:203],v[192:195],v[64:67]
TR a[134:135],v232 offset:45056
TR a[136:137],v233 offset:32768
MM v[64:67],a[204:207],v[208:211],v[64:67]
TR a[138:139],v233 offset:36864
v_add_f32 v246, v236, v237
MM v[68:71],a[220:223],v[208:211],v[128:131]
TR a[140:141],v233 offset:40960
v_cmp_nge_f32 vcc, s71, v246
MM v[68:71],a[216:219],v[192:195],v[68:71]
s_cbranch_vccnz LSITE6_%=
LBACK6_%=:
MM v[68:71],a[212:215],v[176:179],v[68:71]
s_mov_b32 m0, s67
TR a[142:143],v233 offset:45056
TR a[144:145],v232 offset:33280
MM v[68:71],a[208:211],v[160:163],v[68:71]
buffer_load_dwordx4 v252, s[36:39], s66 offen offset:0 lds
MM v[76:79],a[208:211],v[164:167],v[132:135]
s_add_u32 s66, s66, 55296
EX v64,v64
MM v[76:79],a[212:215],v[180:183],v[76:79]
s_min_u32 s66, s66, s85
EX v65,v65
MM v[76:79],a[216:219],v[196:199],v[76:79]
s_add_u32 s67, s67, 1024
TR a[146:147],v232 offset:37376
CV v64,v64,v65
MM v[76:79],a[220:223],v[212:215],v[76:79]
s_min_u32 s67, s67, s86
EX v66,v66
MM v[72:75],a[204:207],v[212:215],v[132:135]
EX v67,v67
MM v[72:75],a[200:203],v[196:199],v[72:75]
TR a[148:149],v232 offset:41472
CV v65,v66,v67
MM v[72:75],a[196:199],v[180:183],v[72:75]
EX v68,v68
MM v[72:75],a[192:195],v[164:167],v[72:75]
TR a[150:151],v232 offset:45568
TR a[152:153],v233 offset:33280
MM v[80:83],a[192:195],v[168:171],v[136:139]
EX v69,v69
MM v[80:83],a[196:199],v[184:187],v[80:83]
TR a[154:155],v233 offset:37376
CV v66,v68,v69
MM v[80:83],a[200:203],v[200:203],v[80:83]
EX v70,v70
MM v[80:83],a[204:207],v[216:219],v[80:83]
EX v71,v71
MM v[84:87],a[220:223],v[216:219],v[136:139]
TR a[156:157],v233 offset:41472
CV v67,v70,v71
MM v[84:87],a[216:219],v[200:203],v[84:87]
EX v72,v72
MM v[84:87],a[212:215],v[184:187],v[84:87]
TR a[158:159],v233 offset:45568
TR a[160:161],v232 offset:33792
MM v[84:87],a[208:211],v[168:171],v[84:87]
EX v73,v73
MM v[92:95],a[208:211],v[172:175],v[140:143]
TR a[162:163],v232 offset:37888
CV v72,v72,v73
MM v[92:95],a[212:215],v[188:191],v[92:95]
EX v74,v74
MM v[92:95],a[216:219],v[204:207],v[92:95]
EX v75,v75
MM v[92:95],a[220:223],v[220:223],v[92:95]
TR a[164:165],v232 offset:41984
CV v73,v74,v75
MM v[88:91],a[204:207],v[220:223],v[140:143]
EX v76,v76
MM v[88:91],a[200:203],v[204:207],v[88:91]
TR a[166:167],v232 offset:46080
TR a[168:169],v233 offset:33792
MM v[88:91],a[196:199],v[188:191],v[88:91]
EX v77,v77
MM v[88:91],a[192:195],v[172:175],v[88:91]
TR a[170:171],v233 offset:37888
CV v74,v76,v77
MM v[96:99],a[224:227],v[160:163],v[128:131]
EX v78,v78
MM v[96:99],a[228:231],v[176:179],v[96:99]
EX v79,v79
MM v[96:99],a[232:235],v[192:195],v[96:99]
TR a[172:173],v233 offset:41984
CV v75,v78,v79
MM v[96:99],a[236:239],v[208:211],v[96:99]
TR a[174:175],v233 offset:46080
TR a[176:177],v232 offset:34304
MM v[100:103],a[252:255],v[208:211],v[128:131]
EX v80,v80
MM v[100:103],a[248:251],v[192:195],v[100:103]
EX v81,v81
MM v[100:103],a[244:247],v[176:179],v[100:103]
TR a[178:179],v232 offset:38400
CV v80,v80,v81
MM v[100:103],a[240:243],v[160:163],v[100:103]
EX v82,v82
MM v[108:111],a[240:243],v[164:167],v[132:135]
v_add_u32 v48, s78, v248
v_mov_b32 v49, s79
MM v[108:111],a[244:247],v[180:183],v[108:111]
EX v83,v83
MM v[108:111],a[248:251],v[196:199],v[108:111]
TR a[180:181],v232 offset:42496
CV v81,v82,v83
MM v[108:111],a[252:255],v[212:215],v[108:111]
EX v84,v84
MM v[104:107],a[236:239],v[212:215],v[132:135]
TR a[182:183],v232 offset:46592
TR a[184:185],v233 offset:34304
MM v[104:107],a[232:235],v[196:199],v[104:107]
EX v85,v85
MM v[104:107],a[228:231],v[180:183],v[104:107]
TR a[186:187],v233 offset:38400
CV v82,v84,v85
MM v[104:107],a[224:227],v[164:167],v[104:107]
EX v86,v86
MM v[112:115],a[224:227],v[168:171],v[136:139]
TR a[188:189],v233 offset:42496
TR a[190:191],v233 offset:46592
MM v[112:115],a[228:231],v[184:187],v[112:115]
EX v87,v87
MM v[112:115],a[232:235],v[200:203],v[112:115]
CV v83,v86,v87
MM v[112:115],a[236:239],v[216:219],v[112:115]
EX v88,v88
MM v[116:119],a[252:255],v[216:219],v[136:139]
EX v89,v89
MM v[116:119],a[248:251],v[200:203],v[116:119]
EX v90,v90
MM v[116:119],a[244:247],v[184:187],v[116:119]
EX v91,v91
MM v[116:119],a[240:243],v[168:171],v[116:119]
EX v92,v92
MM v[124:127],a[240:243],v[172:175],v[140:143]
EX v93,v93
MM v[124:127],a[244:247],v[188:191],v[124:127]
EX v94,v94
MM v[124:127],a[248:251],v[204:207],v[124:127]
EX v95,v95
MM v[124:127],a[252:255],v[220:223],v[124:127]
EX v96,v96
MM v[120:123],a[236:239],v[220:223],v[140:143]
EX v97,v97
MM v[120:123],a[232:235],v[204:207],v[120:123]
EX v98,v98
MM v[120:123],a[228:231],v[188:191],v[120:123]
EX v99,v99
MM v[120:123],a[224:227],v[172:175],v[120:123]
s_waitcnt vmcnt(13) lgkmcnt(0)
s_barrier
MM a[0:3],a[128:131],v[0:3],a[0:3]
s_add_u32 m0, s65, 32768
RD v[40:43],v49 offset:32
RD v[44:47],v49 offset:48
MM a[0:3],a[132:135],v[4:7],a[0:3]
RD v[32:35],v48 offset:0
RD v[36:39],v48 offset:1024
MM a[8:11],a[132:135],v[12:15],a[8:11]
CV v88,v88,v89
CV v89,v90,v91
MM a[8:11],a[128:131],v[8:11],a[8:11]
RD a[192:195],v224 offset:0
CV v90,v92,v93
MM a[16:19],a[128:131],v[16:19],a[16:19]
RD a[196:199],v225 offset:0
CV v91,v94,v95
MM a[16:19],a[132:135],v[20:23],a[16:19]
buffer_load_dwordx4 v228, s[8:11], s64 offen offset:0 lds
MM a[24:27],a[132:135],v[28:31],a[24:27]
s_waitcnt lgkmcnt(4)
v_min3_u32 v50, v40, v41, v42
v_min3_u32 v51, v43, v44, v45
MM a[24:27],a[128:131],v[24:27],a[24:27]
v_min3_u32 v50, v50, v46, v47
v_min_u32 v50, v50, v51
MM a[28:31],a[136:139],v[24:27],a[28:31]
v_readfirstlane_b32 s76, v50
CV v68,v96,v97
MM a[28:31],a[140:143],v[28:31],a[28:31]
s_sub_u32 s92, s92, s76
RD a[200:203],v226 offset:0
CV v69,v98,v99
MM a[20:23],a[140:143],v[20:23],a[20:23]
buffer_load_dwordx4 v229, s[8:11], s64 offen offset:1024 lds
MM a[20:23],a[136:139],v[16:19],a[20:23]
s_add_u32 s76, s76, 1
v_subrev_u32 v40, s76, v40
v_subrev_u32 v41, s76, v41
MM a[12:15],a[136:139],v[8:11],a[12:15]
v_subrev_u32 v42, s76, v42
v_subrev_u32 v43, s76, v43
MM a[12:15],a[140:143],v[12:15],a[12:15]
v_subrev_u32 v44, s76, v44
v_subrev_u32 v45, s76, v45
MM a[4:7],a[140:143],v[4:7],a[4:7]
v_subrev_u32 v46, s76, v46
v_subrev_u32 v47, s76, v47
MM a[4:7],a[136:139],v[0:3],a[4:7]
s_and_b32 s77, s92, 65535
EX v100,v100
MM a[32:35],a[144:147],v[0:3],a[32:35]
s_sub_u32 s54, 65535, s77
v_min3_u32 v50, v40, v41, v42
v_min3_u32 v51, v43, v44, v45
MM a[32:35],a[148:151],v[4:7],a[32:35]
v_min3_u32 v50, v50, v46, v47
v_min_u32 v50, v50, v51
EX v101,v101
MM a[40:43],a[148:151],v[12:15],a[40:43]
RD a[204:207],v227 offset:0
v_readfirstlane_b32 s76, v50
MM a[40:43],a[144:147],v[8:11],a[40:43]
s_sub_u32 s92, s92, s76
EX v102,v102
MM a[48:51],a[144:147],v[16:19],a[48:51]
s_sub_u32 s92, s92, 1
EX v103,v103
MM a[48:51],a[148:151],v[20:23],a[48:51]
s_and_b32 s77, s92, 65535
EX v104,v104
MM a[56:59],a[148:151],v[28:31],a[56:59]
s_sub_u32 s55, 65535, s77
CV v70,v100,v101
CV v71,v102,v103
MM a[56:59],a[144:147],v[24:27],a[56:59]
s_sub_u32 s92, s92, 1
EX v105,v105
MM a[60:63],a[152:155],v[24:27],a[60:63]
s_waitcnt lgkmcnt(4)
v_sub_u32 v32, s92, v32
v_sub_u32 v33, s92, v33
MM a[60:63],a[156:159],v[28:31],a[60:63]
v_sub_u32 v34, s92, v34
v_sub_u32 v35, s92, v35
MM a[52:55],a[156:159],v[20:23],a[52:55]
v_sub_u32 v36, s92, v36
v_sub_u32 v37, s92, v37
EX v106,v106
MM a[52:55],a[152:155],v[16:19],a[52:55]
v_sub_u32 v38, s92, v38
v_sub_u32 v39, s92, v39
MM a[44:47],a[152:155],v[8:11],a[44:47]
v_min3_u32 v52, v32, v33, v34
v_min3_u32 v51, v35, v36, v37
EX v107,v107
MM a[44:47],a[156:159],v[12:15],a[44:47]
v_min3_u32 v52, v52, v38, v39
v_min_u32 v52, v52, v51
MM a[36:39],a[156:159],v[4:7],a[36:39]
RD a[208:211],v224 offset:4096
v_min_u32_dpp v52, v52, v52 row_shr:1 row_mask:0xf bank_mask:0xf
EX v108,v108
MM a[36:39],a[152:155],v[0:3],a[36:39]
v_min_u32_dpp v52, v52, v52 row_shr:2 row_mask:0xf bank_mask:0xf
CV v76,v104,v105
MM a[64:67],a[160:163],v[0:3],a[64:67]
RD a[212:215],v225 offset:4096
v_min_u32_dpp v52, v52, v52 row_shr:4 row_mask:0xf bank_mask:0xf
EX v109,v109
MM a[64:67],a[164:167],v[4:7],a[64:67]
v_min_u32_dpp v52, v52, v52 row_shr:8 row_mask:0xf bank_mask:0xf
CV v77,v106,v107
MM a[72:75],a[164:167],v[12:15],a[72:75]
RD a[216:219],v226 offset:4096
v_min_u32_dpp v52, v52, v52 row_bcast:15 row_mask:0xa bank_mask:0xf
EX v110,v110
MM a[72:75],a[160:163],v[8:11],a[72:75]
v_min_u32_dpp v52, v52, v52 row_bcast:31 row_mask:0xc bank_mask:0xf
CV v78,v108,v109
EX v111,v111
MM a[80:83],a[160:163],v[16:19],a[80:83]
v_readlane_b32 s76, v52, 63
v_lshl_add_u32 v49, s80, 1, v49
MM a[80:83],a[164:167],v[20:23],a[80:83]
buffer_load_dwordx4 v230, s[8:11], s64 offen offset:2048 lds
MM a[88:91],a[164:167],v[28:31],a[88:91]
s_add_u32 s76, s76, 1
v_subrev_u32 v32, s76, v32
v_subrev_u32 v33, s76, v33
EX v112,v112
MM a[88:91],a[160:163],v[24:27],a[88:91]
v_subrev_u32 v34, s76, v34
v_subrev_u32 v35, s76, v35
EX v113,v113
MM a[92:95],a[168:171],v[24:27],a[92:95]
v_subrev_u32 v36, s76, v36
v_subrev_u32 v37, s76, v37
MM a[92:95],a[172:175],v[28:31],a[92:95]
v_subrev_u32 v38, s76, v38
v_subrev_u32 v39, s76, v39
EX v114,v114
MM a[84:87],a[172:175],v[20:23],a[84:87]
v_min3_u32 v53, v32, v33, v34
v_min3_u32 v51, v35, v36, v37
MM a[84:87],a[168:171],v[16:19],a[84:87]
v_min3_u32 v53, v53, v38, v39
)ASM"
R"ASM(
v_min_u32 v53, v53, v51
EX v115,v115
MM a[76:79],a[168:171],v[8:11],a[76:79]
RD a[220:223],v227 offset:4096
v_min_u32_dpp v53, v53, v53 row_shr:1 row_mask:0xf bank_mask:0xf
MM a[76:79],a[172:175],v[12:15],a[76:79]
s_nop 0
v_min_u32_dpp v53, v53, v53 row_shr:2 row_mask:0xf bank_mask:0xf
CV v79,v110,v111
EX v116,v116
MM a[68:71],a[172:175],v[4:7],a[68:71]
RD a[224:227],v224 offset:8192
v_min_u32_dpp v53, v53, v53 row_shr:4 row_mask:0xf bank_mask:0xf
EX v117,v117
MM a[68:71],a[168:171],v[0:3],a[68:71]
v_min_u32_dpp v53, v53, v53 row_shr:8 row_mask:0xf bank_mask:0xf
CV v84,v112,v113
MM a[96:99],a[176:179],v[0:3],a[96:99]
RD a[228:231],v225 offset:8192
v_min_u32_dpp v53, v53, v53 row_bcast:15 row_mask:0xa bank_mask:0xf
EX v118,v118
MM a[96:99],a[180:183],v[4:7],a[96:99]
v_min_u32_dpp v53, v53, v53 row_bcast:31 row_mask:0xc bank_mask:0xf
CV v85,v114,v115
MM a[104:107],a[180:183],v[12:15],a[104:107]
RD a[232:235],v226 offset:8192
v_add_u32 v53, s76, v53
EX v119,v119
MM a[104:107],a[176:179],v[8:11],a[104:107]
s_mov_b32 exec_lo, 0
s_mov_b32 exec_hi, 0x80000000
ds_write_b64 v49, v[52:53] offset:0
s_mov_b64 exec, -1
MM a[112:115],a[176:179],v[16:19],a[112:115]
buffer_load_dwordx4 v231, s[8:11], s64 offen offset:3072 lds
MM a[112:115],a[180:183],v[20:23],a[112:115]
s_add_u32 m0, s65, 98304
EX v120,v120
MM a[120:123],a[180:183],v[28:31],a[120:123]
buffer_load_dwordx4 v235, s[12:15], s81 offen offset:0 lds
MM a[120:123],a[176:179],v[24:27],a[120:123]
EX v121,v121
MM a[124:127],a[184:187],v[24:27],a[124:127]
EX v122,v122
MM a[124:127],a[188:191],v[28:31],a[124:127]
RD a[236:239],v227 offset:8192
CV v86,v116,v117
MM a[116:119],a[188:191],v[20:23],a[116:119]
EX v123,v123
MM a[116:119],a[184:187],v[16:19],a[116:119]
buffer_load_dwordx4 v235, s[12:15], s64 offen offset:1024 lds
MM a[108:111],a[184:187],v[8:11],a[108:111]
EX v124,v124
CV v87,v118,v119
MM a[108:111],a[188:191],v[12:15],a[108:111]
EX v125,v125
MM a[100:103],a[188:191],v[4:7],a[100:103]
RD a[240:243],v224 offset:12288
CV v92,v120,v121
MM a[100:103],a[184:187],v[0:3],a[100:103]
EX v126,v126
v_smfmac_f32_16x16x64_bf16 v[236:239], v[144:147], v[64:71], v234
RD a[244:247],v225 offset:12288
RD a[248:251],v226 offset:12288
RD a[252:255],v227 offset:12288
EX v127,v127
v_smfmac_f32_16x16x64_bf16 v[236:239], v[148:151], v[72:79], v234
CV v93,v122,v123
v_smfmac_f32_16x16x64_bf16 v[236:239], v[152:155], v[80:87], v234
CV v94,v124,v125
CV v95,v126,v127
s_nop 1
v_smfmac_f32_16x16x64_bf16 v[236:239], v[156:159], v[88:95], v234
s_waitcnt lgkmcnt(0)
buffer_load_dwordx4 v235, s[12:15], s81 offen offset:2048 lds
buffer_load_dwordx4 v235, s[12:15], s64 offen offset:3072 lds
MM v[0:3],a[192:195],v[160:163],v[128:131]
s_add_u32 s64, s64, 16384
TR a[128:129],v232 offset:49152
s_add_u32 s81, s64, 896
MM v[0:3],a[196:199],v[176:179],v[0:3]
TR a[130:131],v232 offset:53248
TR a[132:133],v232 offset:57344
MM v[0:3],a[200:203],v[192:195],v[0:3]
TR a[134:135],v232 offset:61440
TR a[136:137],v233 offset:49152
MM v[0:3],a[204:207],v[208:211],v[0:3]
TR a[138:139],v233 offset:53248
v_add_f32 v247, v236, v237
MM v[4:7],a[220:223],v[208:211],v[128:131]
TR a[140:141],v233 offset:57344
v_cmp_nge_f32 vcc, s71, v247
MM v[4:7],a[216:219],v[192:195],v[4:7]
s_cbranch_vccnz LSITE7_%=
LBACK7_%=:
MM v[4:7],a[212:215],v[176:179],v[4:7]
s_mov_b32 m0, s67
TR a[142:143],v233 offset:61440
TR a[144:145],v232 offset:49664
MM v[4:7],a[208:211],v[160:163],v[4:7]
buffer_load_dwordx4 v252, s[36:39], s66 offen offset:0 lds
MM v[12:15],a[208:211],v[164:167],v[132:135]
s_add_u32 s66, s66, 55296
EX v0,v0
MM v[12:15],a[212:215],v[180:183],v[12:15]
s_min_u32 s66, s66, s85
EX v1,v1
MM v[12:15],a[216:219],v[196:199],v[12:15]
s_add_u32 s67, s67, 1024
TR a[146:147],v232 offset:53760
CV v0,v0,v1
MM v[12:15],a[220:223],v[212:215],v[12:15]
s_min_u32 s67, s67, s86
EX v2,v2
MM v[8:11],a[204:207],v[212:215],v[132:135]
EX v3,v3
MM v[8:11],a[200:203],v[196:199],v[8:11]
TR a[148:149],v232 offset:57856
CV v1,v2,v3
MM v[8:11],a[196:199],v[180:183],v[8:11]
EX v4,v4
MM v[8:11],a[192:195],v[164:167],v[8:11]
TR a[150:151],v232 offset:61952
TR a[152:153],v233 offset:49664
MM v[16:19],a[192:195],v[168:171],v[136:139]
EX v5,v5
MM v[16:19],a[196:199],v[184:187],v[16:19]
TR a[154:155],v233 offset:53760
CV v2,v4,v5
MM v[16:19],a[200:203],v[200:203],v[16:19]
EX v6,v6
MM v[16:19],a[204:207],v[216:219],v[16:19]
EX v7,v7
MM v[20:23],a[220:223],v[216:219],v[136:139]
TR a[156:157],v233 offset:57856
CV v3,v6,v7
MM v[20:23],a[216:219],v[200:203],v[20:23]
EX v8,v8
MM v[20:23],a[212:215],v[184:187],v[20:23]
TR a[158:159],v233 offset:61952
TR a[160:161],v232 offset:50176
MM v[20:23],a[208:211],v[168:171],v[20:23]
EX v9,v9
MM v[28:31],a[208:211],v[172:175],v[140:143]
TR a[162:163],v232 offset:54272
CV v8,v8,v9
MM v[28:31],a[212:215],v[188:191],v[28:31]
EX v10,v10
MM v[28:31],a[216:219],v[204:207],v[28:31]
EX v11,v11
MM v[28:31],a[220:223],v[220:223],v[28:31]
TR a[164:165],v232 offset:58368
CV v9,v10,v11
MM v[24:27],a[204:207],v[220:223],v[140:143]
EX v12,v12
MM v[24:27],a[200:203],v[204:207],v[24:27]
TR a[166:167],v232 offset:62464
TR a[168:169],v233 offset:50176
MM v[24:27],a[196:199],v[188:191],v[24:27]
LPG28_%=:
s_cbranch_execz LTR1_%=
EX v13,v13
MM v[24:27],a[192:195],v[172:175],v[24:27]
TR a[170:171],v233 offset:54272
CV v10,v12,v13
MM v[32:35],a[224:227],v[160:163],v[128:131]
EX v14,v14
MM v[32:35],a[228:231],v[176:179],v[32:35]
EX v15,v15
MM v[32:35],a[232:235],v[192:195],v[32:35]
TR a[172:173],v233 offset:58368
CV v11,v14,v15
MM v[32:35],a[236:239],v[208:211],v[32:35]
TR a[174:175],v233 offset:62464
TR a[176:177],v232 offset:50688
MM v[36:39],a[252:255],v[208:211],v[128:131]
EX v16,v16
MM v[36:39],a[248:251],v[192:195],v[36:39]
EX v17,v17
MM v[36:39],a[244:247],v[176:179],v[36:39]
TR a[178:179],v232 offset:54784
CV v16,v16,v17
MM v[36:39],a[240:243],v[160:163],v[36:39]
EX v18,v18
MM v[44:47],a[240:243],v[164:167],v[132:135]
v_add_u32 v112, s78, v248
v_mov_b32 v113, s79
MM v[44:47],a[244:247],v[180:183],v[44:47]
EX v19,v19
MM v[44:47],a[248:251],v[196:199],v[44:47]
TR a[180:181],v232 offset:58880
CV v17,v18,v19
MM v[44:47],a[252:255],v[212:215],v[44:47]
EX v20,v20
MM v[40:43],a[236:239],v[212:215],v[132:135]
TR a[182:183],v232 offset:62976
TR a[184:185],v233 offset:50688
MM v[40:43],a[232:235],v[196:199],v[40:43]
EX v21,v21
MM v[40:43],a[228:231],v[180:183],v[40:43]
TR a[186:187],v233 offset:54784
CV v18,v20,v21
MM v[40:43],a[224:227],v[164:167],v[40:43]
EX v22,v22
MM v[48:51],a[224:227],v[168:171],v[136:139]
TR a[188:189],v233 offset:58880
TR a[190:191],v233 offset:62976
MM v[48:51],a[228:231],v[184:187],v[48:51]
EX v23,v23
MM v[48:51],a[232:235],v[200:203],v[48:51]
CV v19,v22,v23
MM v[48:51],a[236:239],v[216:219],v[48:51]
EX v24,v24
MM v[52:55],a[252:255],v[216:219],v[136:139]
EX v25,v25
MM v[52:55],a[248:251],v[200:203],v[52:55]
EX v26,v26
MM v[52:55],a[244:247],v[184:187],v[52:55]
EX v27,v27
MM v[52:55],a[240:243],v[168:171],v[52:55]
EX v28,v28
MM v[60:63],a[240:243],v[172:175],v[140:143]
EX v29,v29
MM v[60:63],a[244:247],v[188:191],v[60:63]
EX v30,v30
MM v[60:63],a[248:251],v[204:207],v[60:63]
EX v31,v31
MM v[60:63],a[252:255],v[220:223],v[60:63]
EX v32,v32
MM v[56:59],a[236:239],v[220:223],v[140:143]
EX v33,v33
MM v[56:59],a[232:235],v[204:207],v[56:59]
EX v34,v34
MM v[56:59],a[228:231],v[188:191],v[56:59]
EX v35,v35
MM v[56:59],a[224:227],v[172:175],v[56:59]
s_waitcnt vmcnt(13) lgkmcnt(0)
s_barrier
MM a[0:3],a[128:131],v[64:67],a[0:3]
s_add_u32 m0, s65, 49152
RD v[104:107],v113 offset:0
RD v[108:111],v113 offset:16
MM a[0:3],a[132:135],v[68:71],a[0:3]
RD v[96:99],v112 offset:0
RD v[100:103],v112 offset:1024
MM a[8:11],a[132:135],v[76:79],a[8:11]
CV v24,v24,v25
CV v25,v26,v27
MM a[8:11],a[128:131],v[72:75],a[8:11]
RD a[192:195],v224 offset:16384
CV v26,v28,v29
MM a[16:19],a[128:131],v[80:83],a[16:19]
RD a[196:199],v225 offset:16384
CV v27,v30,v31
MM a[16:19],a[132:135],v[84:87],a[16:19]
buffer_load_dwordx4 v228, s[8:11], s64 offen offset:0 lds
MM a[24:27],a[132:135],v[92:95],a[24:27]
s_waitcnt lgkmcnt(4)
v_min3_u32 v114, v104, v105, v106
v_min3_u32 v115, v107, v108, v109
MM a[24:27],a[128:131],v[88:91],a[24:27]
v_min3_u32 v114, v114, v110, v111
v_min_u32 v114, v114, v115
MM a[28:31],a[136:139],v[88:91],a[28:31]
v_readfirstlane_b32 s76, v114
CV v4,v32,v33
MM a[28:31],a[140:143],v[92:95],a[28:31]
s_sub_u32 s92, s92, s76
RD a[200:203],v226 offset:16384
CV v5,v34,v35
MM a[20:23],a[140:143],v[84:87],a[20:23]
buffer_load_dwordx4 v229, s[8:11], s64 offen offset:1024 lds
MM a[20:23],a[136:139],v[80:83],a[20:23]
s_add_u32 s76, s76, 1
v_subrev_u32 v104, s76, v104
v_subrev_u32 v105, s76, v105
MM a[12:15],a[136:139],v[72:75],a[12:15]
v_subrev_u32 v106, s76, v106
v_subrev_u32 v107, s76, v107
MM a[12:15],a[140:143],v[76:79],a[12:15]
v_subrev_u32 v108, s76, v108
v_subrev_u32 v109, s76, v109
MM a[4:7],a[140:143],v[68:71],a[4:7]
v_subrev_u32 v110, s76, v110
v_subrev_u32 v111, s76, v111
MM a[4:7],a[136:139],v[64:67],a[4:7]
s_and_b32 s77, s92, 65535
EX v36,v36
MM a[32:35],a[144:147],v[64:67],a[32:35]
s_sub_u32 s56, 65535, s77
v_min3_u32 v114, v104, v105, v106
v_min3_u32 v115, v107, v108, v109
MM a[32:35],a[148:151],v[68:71],a[32:35]
v_min3_u32 v114, v114, v110, v111
v_min_u32 v114, v114, v115
EX v37,v37
MM a[40:43],a[148:151],v[76:79],a[40:43]
RD a[204:207],v227 offset:16384
v_readfirstlane_b32 s76, v114
MM a[40:43],a[144:147],v[72:75],a[40:43]
s_sub_u32 s92, s92, s76
EX v38,v38
MM a[48:51],a[144:147],v[80:83],a[48:51]
s_sub_u32 s92, s92, 1
EX v39,v39
MM a[48:51],a[148:151],v[84:87],a[48:51]
s_and_b32 s77, s92, 65535
EX v40,v40
MM a[56:59],a[148:151],v[92:95],a[56:59]
s_sub_u32 s57, 65535, s77
CV v6,v36,v37
CV v7,v38,v39
MM a[56:59],a[144:147],v[88:91],a[56:59]
s_sub_u32 s92, s92, 1
EX v41,v41
MM a[60:63],a[152:155],v[88:91],a[60:63]
s_waitcnt lgkmcnt(4)
v_sub_u32 v96, s92, v96
v_sub_u32 v97, s92, v97
MM a[60:63],a[156:159],v[92:95],a[60:63]
v_sub_u32 v98, s92, v98
v_sub_u32 v99, s92, v99
MM a[52:55],a[156:159],v[84:87],a[52:55]
v_sub_u32 v100, s92, v100
v_sub_u32 v101, s92, v101
EX v42,v42
MM a[52:55],a[152:155],v[80:83],a[52:55]
v_sub_u32 v102, s92, v102
v_sub_u32 v103, s92, v103
MM a[44:47],a[152:155],v[72:75],a[44:47]
v_min3_u32 v116, v96, v97, v98
v_min3_u32 v115, v99, v100, v101
EX v43,v43
MM a[44:47],a[156:159],v[76:79],a[44:47]
v_min3_u32 v116, v116, v102, v103
v_min_u32 v116, v116, v115
MM a[36:39],a[156:159],v[68:71],a[36:39]
RD a[208:211],v224 offset:20480
v_min_u32_dpp v116, v116, v116 row_shr:1 row_mask:0xf bank_mask:0xf
EX v44,v44
MM a[36:39],a[152:155],v[64:67],a[36:39]
v_min_u32_dpp v116, v116, v116 row_shr:2 row_mask:0xf bank_mask:0xf
CV v12,v40,v41
MM a[64:67],a[160:163],v[64:67],a[64:67]
RD a[212:215],v225 offset:20480
v_min_u32_dpp v116, v116, v116 row_shr:4 row_mask:0xf bank_mask:0xf
EX v45,v45
MM a[64:67],a[164:167],v[68:71],a[64:67]
v_min_u32_dpp v116, v116, v116 row_shr:8 row_mask:0xf bank_mask:0xf
CV v13,v42,v43
MM a[72:75],a[164:167],v[76:79],a[72:75]
RD a[216:219],v226 offset:20480
v_min_u32_dpp v116, v116, v116 row_bcast:15 row_mask:0xa bank_mask:0xf
EX v46,v46
MM a[72:75],a[160:163],v[72:75],a[72:75]
v_min_u32_dpp v116, v116, v116 row_bcast:31 row_mask:0xc bank_mask:0xf
CV v14,v44,v45
EX v47,v47
MM a[80:83],a[160:163],v[80:83],a[80:83]
v_readlane_b32 s76, v116, 63
v_lshl_add_u32 v113, s80, 1, v113
MM a[80:83],a[164:167],v[84:87],a[80:83]
buffer_load_dwordx4 v230, s[8:11], s64 offen offset:2048 lds
MM a[88:91],a[164:167],v[92:95],a[88:91]
s_add_u32 s76, s76, 1
v_subrev_u32 v96, s76, v96
v_subrev_u32 v97, s76, v97
EX v48,v48
MM a[88:91],a[160:163],v[88:91],a[88:91]
v_subrev_u32 v98, s76, v98
v_subrev_u32 v99, s76, v99
EX v49,v49
MM a[92:95],a[168:171],v[88:91],a[92:95]
v_subrev_u32 v100, s76, v100
v_subrev_u32 v101, s76, v101
MM a[92:95],a[172:175],v[92:95],a[92:95]
v_subrev_u32 v102, s76, v102
v_subrev_u32 v103, s76, v103
EX v50,v50
MM a[84:87],a[172:175],v[84:87],a[84:87]
v_min3_u32 v117, v96, v97, v98
v_min3_u32 v115, v99, v100, v101
MM a[84:87],a[168:171],v[80:83],a[84:87]
v_min3_u32 v117, v117, v102, v103
)ASM"
R"ASM(
v_min_u32 v117, v117, v115
EX v51,v51
MM a[76:79],a[168:171],v[72:75],a[76:79]
RD a[220:223],v227 offset:20480
v_min_u32_dpp v117, v117, v117 row_shr:1 row_mask:0xf bank_mask:0xf
MM a[76:79],a[172:175],v[76:79],a[76:79]
s_nop 0
v_min_u32_dpp v117, v117, v117 row_shr:2 row_mask:0xf bank_mask:0xf
CV v15,v46,v47
EX v52,v52
MM a[68:71],a[172:175],v[68:71],a[68:71]
RD a[224:227],v224 offset:24576
v_min_u32_dpp v117, v117, v117 row_shr:4 row_mask:0xf bank_mask:0xf
EX v53,v53
MM a[68:71],a[168:171],v[64:67],a[68:71]
v_min_u32_dpp v117, v117, v117 row_shr:8 row_mask:0xf bank_mask:0xf
CV v20,v48,v49
MM a[96:99],a[176:179],v[64:67],a[96:99]
RD a[228:231],v225 offset:24576
v_min_u32_dpp v117, v117, v117 row_bcast:15 row_mask:0xa bank_mask:0xf
EX v54,v54
MM a[96:99],a[180:183],v[68:71],a[96:99]
v_min_u32_dpp v117, v117, v117 row_bcast:31 row_mask:0xc bank_mask:0xf
CV v21,v50,v51
MM a[104:107],a[180:183],v[76:79],a[104:107]
RD a[232:235],v226 offset:24576
v_add_u32 v117, s76, v117
EX v55,v55
MM a[104:107],a[176:179],v[72:75],a[104:107]
s_mov_b32 exec_lo, 0
s_mov_b32 exec_hi, 0x80000000
ds_write_b64 v113, v[116:117] offset:32
s_mov_b64 exec, -1
MM a[112:115],a[176:179],v[80:83],a[112:115]
buffer_load_dwordx4 v231, s[8:11], s64 offen offset:3072 lds
MM a[112:115],a[180:183],v[84:87],a[112:115]
s_add_u32 m0, s65, 114688
EX v56,v56
MM a[120:123],a[180:183],v[92:95],a[120:123]
buffer_load_dwordx4 v235, s[12:15], s81 offen offset:0 lds
MM a[120:123],a[176:179],v[88:91],a[120:123]
EX v57,v57
MM a[124:127],a[184:187],v[88:91],a[124:127]
EX v58,v58
MM a[124:127],a[188:191],v[92:95],a[124:127]
RD a[236:239],v227 offset:24576
CV v22,v52,v53
MM a[116:119],a[188:191],v[84:87],a[116:119]
EX v59,v59
MM a[116:119],a[184:187],v[80:83],a[116:119]
buffer_load_dwordx4 v235, s[12:15], s64 offen offset:1024 lds
MM a[108:111],a[184:187],v[72:75],a[108:111]
EX v60,v60
CV v23,v54,v55
MM a[108:111],a[188:191],v[76:79],a[108:111]
EX v61,v61
MM a[100:103],a[188:191],v[68:71],a[100:103]
RD a[240:243],v224 offset:28672
CV v28,v56,v57
MM a[100:103],a[184:187],v[64:67],a[100:103]
EX v62,v62
v_smfmac_f32_16x16x64_bf16 v[236:239], v[144:147], v[0:7], v234
RD a[244:247],v225 offset:28672
RD a[248:251],v226 offset:28672
RD a[252:255],v227 offset:28672
EX v63,v63
v_smfmac_f32_16x16x64_bf16 v[236:239], v[148:151], v[8:15], v234
CV v29,v58,v59
v_smfmac_f32_16x16x64_bf16 v[236:239], v[152:155], v[16:23], v234
CV v30,v60,v61
CV v31,v62,v63
s_nop 1
v_smfmac_f32_16x16x64_bf16 v[236:239], v[156:159], v[24:31], v234
s_waitcnt lgkmcnt(0)
buffer_load_dwordx4 v235, s[12:15], s81 offen offset:2048 lds
buffer_load_dwordx4 v235, s[12:15], s64 offen offset:3072 lds
s_waitcnt lgkmcnt(0)
s_barrier
v_mov_b32 v113, s79
RD v[104:107],v113 offset:32
RD v[108:111],v113 offset:48
s_waitcnt lgkmcnt(0)
v_min3_u32 v114, v104, v105, v106
v_min3_u32 v115, v107, v108, v109
v_min3_u32 v114, v114, v110, v111
v_min_u32 v114, v114, v115
s_nop 0
v_readfirstlane_b32 s76, v114
s_nop 0
s_sub_u32 s92, s92, s76
s_add_u32 s76, s76, 1
v_subrev_u32 v104, s76, v104
v_subrev_u32 v105, s76, v105
v_subrev_u32 v106, s76, v106
v_subrev_u32 v107, s76, v107
v_subrev_u32 v108, s76, v108
v_subrev_u32 v109, s76, v109
v_subrev_u32 v110, s76, v110
v_subrev_u32 v111, s76, v111
s_and_b32 s77, s92, 65535
s_sub_u32 s58, 65535, s77
v_min3_u32 v114, v104, v105, v106
v_min3_u32 v115, v107, v108, v109
v_min3_u32 v114, v114, v110, v111
v_min_u32 v114, v114, v115
s_nop 0
v_readfirstlane_b32 s76, v114
s_nop 0
s_sub_u32 s92, s92, s76
s_sub_u32 s92, s92, 1
s_and_b32 s77, s92, 65535
s_sub_u32 s59, 65535, s77
s_sub_u32 s92, s92, 1
s_lshl_b32 s90, %[swave], 8
s_add_u32 s90, s90, %[sb]
s_cmp_lg_u32 s90, 0
s_cbranch_scc1 LIX_%=
v_writelane_b32 v112, s52, 0
v_writelane_b32 v112, s53, 1
v_writelane_b32 v112, s54, 2
v_writelane_b32 v112, s55, 3
v_writelane_b32 v112, s56, 4
v_writelane_b32 v112, s57, 5
v_writelane_b32 v112, s58, 6
v_writelane_b32 v112, s59, 7
v_mov_b32 v113, s69
v_lshrrev_b32 v116, 1, v248
s_mov_b32 s90, 86016
s_mov_b64 exec, 0xff
buffer_store_dwordx2 v[112:113], v116, s[20:23], s90 offen offset:0 sc1
s_mov_b64 exec, -1
s_nop 1
LIX_%=:
s_mov_b32 s70, 1
s_branch LLA_%=
.fill 443, 4, 0xbf800000
LPG29_%=:
s_cbranch_execz LTR1_%=
s_nop 0
LRG1o_%=:
s_mov_b32 s70, 30001
LRG1ol_%=:
s_sleep 2
buffer_load_dwordx4 v[200:203], v208, s[20:23], s93 offen offset:0 sc1
buffer_load_dwordx4 v[204:207], v208, s[20:23], s93 offen offset:256 sc1
s_waitcnt vmcnt(0)
v_xor_b32 v201, s69, v201
v_xor_b32 v203, s69, v203
v_xor_b32 v205, s69, v205
v_xor_b32 v207, s69, v207
v_or3_b32 v209, v201, v203, v205
v_or_b32 v209, v209, v207
v_cmp_ne_u32 vcc, 0, v209
s_cbranch_vccz LRG1ob_%=
s_sub_u32 s70, s70, 1
s_cbranch_scc0 LRG1ol_%=
s_branch LRG1ob_%=
LRFR_%=:
s_mov_b32 s91, 30001
LRFRl_%=:
s_sleep 2
buffer_load_dwordx4 v[48:51], v248, s[20:23], s90 offen offset:0 sc1
s_add_u32 s78, s90, 8192
buffer_load_dwordx4 v[56:59], v248, s[20:23], s78 offen offset:0 sc1
s_waitcnt vmcnt(0)
v_xor_b32 v48, s69, v48
v_xor_b32 v49, s69, v49
v_xor_b32 v50, s69, v50
v_xor_b32 v51, s69, v51
v_xor_b32 v56, s69, v56
v_xor_b32 v57, s69, v57
v_xor_b32 v58, s69, v58
v_xor_b32 v59, s69, v59
v_or3_b32 v52, v48, v49, v50
v_or3_b32 v52, v52, v51, v56
v_or3_b32 v52, v52, v57, v58
v_or_b32 v52, v52, v59
v_cmp_ne_u32 vcc, 0, v52
s_cbranch_vccz LRFRb_%=
s_sub_u32 s91, s91, 1
s_cbranch_scc0 LRFRl_%=
s_branch LRFRb_%=
LRIX_%=:
s_mov_b32 s91, 30001
LRIXl_%=:
s_sleep 2
buffer_load_dwordx2 v[48:49], v52, s[20:23], s90 offen offset:0 sc1
s_waitcnt vmcnt(0)
v_cmp_ne_u32 vcc, s69, v49
s_cbranch_vccz LRIXb_%=
s_sub_u32 s91, s91, 1
s_cbranch_scc0 LRIXl_%=
s_branch LRIXb_%=
LRFG_%=:
s_mov_b32 s91, 30001
LRFGl_%=:
s_sleep 2
buffer_load_dword v252, v253, s[20:23], s76 offen offset:0 sc1
s_waitcnt vmcnt(0)
v_cmp_ne_u32 vcc, s69, v252
s_cbranch_vccz LRFGb_%=
s_sub_u32 s91, s91, 1
s_cbranch_scc0 LRFGl_%=
s_branch LRFGb_%=
LRFH_%=:
s_mov_b32 s91, 30001
LRFHl_%=:
s_sleep 2
buffer_load_dword v252, v253, s[20:23], s76 offen offset:0 sc1
s_waitcnt vmcnt(0)
v_cmp_ne_u32 vcc, s69, v252
s_cbranch_vccz LRFHb_%=
s_sub_u32 s91, s91, 1
s_cbranch_scc0 LRFHl_%=
s_branch LRFHb_%=
LRPO0_%=:
s_mov_b32 s97, 30001
LRPO0l_%=:
s_sleep 2
buffer_load_dword v242, v243, s[20:23], s96 offen offset:0 sc1
s_waitcnt vmcnt(0)
v_cmp_ne_u32 vcc, s69, v242
s_cbranch_vccz LRPO0b_%=
s_sub_u32 s97, s97, 1
s_cbranch_scc0 LRPO0l_%=
s_branch LRPO0b_%=
LRPO1_%=:
s_mov_b32 s97, 30001
LRPO1l_%=:
s_sleep 2
buffer_load_dword v242, v243, s[20:23], s96 offen offset:16 sc1
s_waitcnt vmcnt(0)
v_cmp_ne_u32 vcc, s69, v242
s_cbranch_vccz LRPO1b_%=
s_sub_u32 s97, s97, 1
s_cbranch_scc0 LRPO1l_%=
s_branch LRPO1b_%=
LRYF_%=:
s_mov_b32 s91, 30001
LRYFl_%=:
s_sleep 2
buffer_load_dword v132, v135, s[20:23], s78 offen offset:0 sc1
buffer_load_dword v133, v135, s[20:23], s78 offen offset:960 sc1
buffer_load_dword v134, v135, s[20:23], s78 offen offset:1920 sc1
s_waitcnt vmcnt(0)
v_xor_b32 v132, s69, v132
v_xor_b32 v133, s69, v133
v_xor_b32 v134, s69, v134
v_or3_b32 v139, v132, v133, v134
v_cmp_ne_u32 vcc, 0, v139
s_cbranch_vccz LRYFb_%=
s_sub_u32 s91, s91, 1
s_cbranch_scc0 LRYFl_%=
s_branch LRYFb_%=
LQA_%=:
s_bitcmp0_b32 s17, 1
s_cbranch_scc1 LQG1_%=
AR v108,a35
AR v109,a39
AR v110,a43
AR v111,a47
v_cmp_eq_u32 s[24:25], -1, v108
v_cmp_eq_u32 s[26:27], -1, v109
v_cmp_eq_u32 s[28:29], -1, v110
v_cmp_eq_u32 s[46:47], -1, v111
s_nop 0
s_or_b64 s[30:31], s[24:25], s[26:27]
s_or_b64 s[30:31], s[30:31], s[28:29]
s_or_b64 s[30:31], s[30:31], s[46:47]
s_cselect_b32 s76, 256, 2
s_or_b32 s94, s94, s76
s_bitcmp1_b32 s94, 1
s_cbranch_scc1 LQG1_%=
s_and_b64 exec, s[46:47], s[10:11]
s_cbranch_scc0 LQK13_%=
s_add_u32 s66, s44, 6144
buffer_load_dwordx4 a[44:47], v250, s[20:23], s66 offen offset:0 sc1
LQK13_%=:
s_and_b64 exec, s[28:29], s[8:9]
s_cbranch_scc0 LQK12_%=
s_add_u32 s66, s16, 6144
buffer_load_dwordx4 a[40:43], v250, s[20:23], s66 offen offset:0 sc1
LQK12_%=:
s_and_b64 exec, s[26:27], s[10:11]
s_cbranch_scc0 LQK11_%=
s_add_u32 s66, s44, 4096
buffer_load_dwordx4 a[36:39], v250, s[20:23], s66 offen offset:0 sc1
LQK11_%=:
s_and_b64 exec, s[24:25], s[8:9]
s_cbranch_scc0 LQK10_%=
s_add_u32 s66, s16, 4096
buffer_load_dwordx4 a[32:35], v250, s[20:23], s66 offen offset:0 sc1
LQK10_%=:
s_mov_b64 exec, -1
LQG1_%=:
s_bitcmp0_b32 s17, 0
s_cbranch_scc1 LQG0_%=
AR v108,a3
AR v109,a7
AR v110,a11
AR v111,a15
v_cmp_eq_u32 s[24:25], -1, v108
v_cmp_eq_u32 s[26:27], -1, v109
v_cmp_eq_u32 s[28:29], -1, v110
v_cmp_eq_u32 s[46:47], -1, v111
s_nop 0
s_or_b64 s[30:31], s[24:25], s[26:27]
s_or_b64 s[30:31], s[30:31], s[28:29]
s_or_b64 s[30:31], s[30:31], s[46:47]
s_cselect_b32 s76, 256, 1
s_or_b32 s94, s94, s76
s_bitcmp1_b32 s94, 0
s_cbranch_scc1 LQG0_%=
s_and_b64 exec, s[46:47], s[38:39]
s_cbranch_scc0 LQK03_%=
s_add_u32 s66, s44, 2048
buffer_load_dwordx4 a[12:15], v250, s[20:23], s66 offen offset:0 sc1
LQK03_%=:
s_and_b64 exec, s[28:29], s[36:37]
s_cbranch_scc0 LQK02_%=
s_add_u32 s66, s16, 2048
buffer_load_dwordx4 a[8:11], v250, s[20:23], s66 offen offset:0 sc1
LQK02_%=:
s_and_b64 exec, s[26:27], s[38:39]
s_cbranch_scc0 LQK01_%=
s_add_u32 s66, s44, 0
buffer_load_dwordx4 a[4:7], v250, s[20:23], s66 offen offset:0 sc1
LQK01_%=:
s_and_b64 exec, s[24:25], s[36:37]
s_cbranch_scc0 LQK00_%=
s_add_u32 s66, s16, 0
buffer_load_dwordx4 a[0:3], v250, s[20:23], s66 offen offset:0 sc1
LQK00_%=:
s_mov_b64 exec, -1
LQG0_%=:
s_branch LQB_%=
LQC_%=:
s_bitcmp0_b32 s94, 0
s_cbranch_scc1 LQN0_%=
MM v[72:75],v[64:67],a[0:3],0
MM v[72:75],v[64:67],a[4:7],v[72:75]
MM v[72:75],v[68:71],a[8:11],v[72:75]
MM v[72:75],v[68:71],a[12:15],v[72:75]
s_bitset0_b32 s17, 0
s_nop 6
CV v76,v72,v73
CV v77,v74,v75
ds_bpermute_b32 v76, v232, v76
ds_bpermute_b32 v77, v232, v77
v_lshlrev_b32 v10, 16, v0
v_and_b32 v11, 0xffff0000, v0
v_lshlrev_b32 v12, 16, v1
v_and_b32 v13, 0xffff0000, v1
v_lshlrev_b32 v14, 16, v32
v_and_b32 v15, 0xffff0000, v32
v_lshlrev_b32 v42, 16, v33
v_and_b32 v43, 0xffff0000, v33
s_add_u32 s80, s18, 0
s_waitcnt lgkmcnt(0)
v_lshlrev_b32 v72, 16, v76
v_and_b32 v73, 0xffff0000, v76
v_lshlrev_b32 v74, 16, v77
v_and_b32 v75, 0xffff0000, v77
v_mul_f32 v72, v72, v10
v_mul_f32 v73, v73, v11
v_mul_f32 v74, v74, v12
v_mul_f32 v75, v75, v13
CV v76,v72,v73
CV v77,v74,v75
v_lshlrev_b32 v72, 16, v76
v_and_b32 v73, 0xffff0000, v76
v_lshlrev_b32 v74, 16, v77
v_and_b32 v75, 0xffff0000, v77
v_add_f32 v72, v72, v14
v_add_f32 v73, v73, v15
v_add_f32 v74, v74, v42
v_add_f32 v75, v75, v43
CV v44,v72,v73
CV v45,v74,v75
buffer_store_dwordx2 v[44:45], v251, s[40:43], s80 offen offset:0 nt
s_bitcmp0_b32 s94, 8
s_cbranch_scc1 LQN0_%=
s_waitcnt vmcnt(1)
s_branch LQS_%=
LQN0_%=:
s_bitcmp0_b32 s94, 1
s_cbranch_scc1 LQN1_%=
MM v[72:75],v[64:67],a[32:35],0
MM v[72:75],v[64:67],a[36:39],v[72:75]
MM v[72:75],v[68:71],a[40:43],v[72:75]
MM v[72:75],v[68:71],a[44:47],v[72:75]
s_bitset0_b32 s17, 1
s_nop 6
CV v76,v72,v73
CV v77,v74,v75
ds_bpermute_b32 v76, v232, v76
ds_bpermute_b32 v77, v232, v77
v_lshlrev_b32 v10, 16, v2
v_and_b32 v11, 0xffff0000, v2
v_lshlrev_b32 v12, 16, v3
v_and_b32 v13, 0xffff0000, v3
v_lshlrev_b32 v14, 16, v34
v_and_b32 v15, 0xffff0000, v34
v_lshlrev_b32 v42, 16, v35
v_and_b32 v43, 0xffff0000, v35
s_add_u32 s80, s18, 128
s_waitcnt lgkmcnt(0)
v_lshlrev_b32 v72, 16, v76
v_and_b32 v73, 0xffff0000, v76
v_lshlrev_b32 v74, 16, v77
v_and_b32 v75, 0xffff0000, v77
v_mul_f32 v72, v72, v10
v_mul_f32 v73, v73, v11
v_mul_f32 v74, v74, v12
v_mul_f32 v75, v75, v13
CV v76,v72,v73
CV v77,v74,v75
v_lshlrev_b32 v72, 16, v76
v_and_b32 v73, 0xffff0000, v76
v_lshlrev_b32 v74, 16, v77
v_and_b32 v75, 0xffff0000, v77
v_add_f32 v72, v72, v14
v_add_f32 v73, v73, v15
v_add_f32 v74, v74, v42
v_add_f32 v75, v75, v43
CV v46,v72,v73
CV v47,v74,v75
buffer_store_dwordx2 v[46:47], v251, s[40:43], s80 offen offset:0 nt
s_bitcmp0_b32 s94, 8
s_cbranch_scc1 LQN1_%=
s_waitcnt vmcnt(1)
s_branch LQS_%=
LQN1_%=:
s_branch LQD_%=
LSW_%=:
s_sleep 1
s_waitcnt vmcnt(0)
s_cmp_eq_u32 s65, 0
s_cbranch_scc1 LSX_%=
s_mov_b64 exec, 1
buffer_store_dword v233, off, s[20:23], s19 offset:12 sc1
s_mov_b64 exec, -1
s_mov_b32 s65, 0
LSX_%=:
v_cmp_ne_u32 vcc, s69, v243
)ASM"
R"ASM(
v_cmp_ne_u32 s[36:37], s69, v143
s_nop 1
s_or_b64 vcc, vcc, s[36:37]
s_cselect_b32 s76, 0, 1
s_add_u32 s72, s72, s76
s_sub_u32 s73, s73, 1
s_cselect_b32 s72, 2, s72
s_bitcmp0_b32 s17, 4
s_cbranch_scc1 LSN4_%=
MM v[72:75],v[64:67],a[192:195],0
MM v[72:75],v[64:67],a[196:199],v[72:75]
MM v[72:75],v[68:71],v[144:147],v[72:75]
MM v[72:75],v[68:71],a[212:215],v[72:75]
AR v76,a195
AR v77,a199
v_mov_b32 v78, v147
AR v79,a215
s_nop 3
v_cmp_u_f32 s[30:31], v72, v72
s_nop 1
s_cmp_eq_u64 s[30:31], 0
s_cbranch_scc1 LSF4_%=
s_cmp_ge_u32 s72, 2
s_cbranch_scc1 LSF4_%=
v_cmp_eq_u32 vcc, s69, v242
v_cmp_eq_u32 s[36:37], -1, v78
s_add_u32 s66, s16, 18432
s_and_b64 vcc, vcc, s[36:37]
s_cmp_eq_u32 s72, 0
s_cselect_b64 exec, vcc, -1
s_and_b32 exec_lo, exec_lo, 0xffff0000
buffer_load_dwordx4 v[144:147], v250, s[20:23], s66 offen offset:0 sc1
s_mov_b64 exec, -1
v_cmp_eq_u32 vcc, s69, v142
v_cmp_eq_u32 s[36:37], -1, v79
s_add_u32 s66, s44, 18432
s_and_b64 vcc, vcc, s[36:37]
s_cmp_eq_u32 s72, 0
s_cselect_b64 exec, vcc, -1
buffer_load_dwordx4 a[212:215], v250, s[20:23], s66 offen offset:0 sc1
s_mov_b64 exec, -1
v_cmp_eq_u32 vcc, s69, v241
v_cmp_eq_u32 s[36:37], -1, v76
s_add_u32 s66, s16, 8192
s_and_b64 vcc, vcc, s[36:37]
s_cmp_eq_u32 s72, 0
s_cselect_b64 exec, vcc, -1
s_and_b32 exec_lo, exec_lo, 0xffff0000
buffer_load_dwordx4 a[192:195], v250, s[20:23], s66 offen offset:0 sc1
s_mov_b64 exec, -1
v_cmp_eq_u32 vcc, s69, v141
v_cmp_eq_u32 s[36:37], -1, v77
s_add_u32 s66, s44, 8192
s_and_b64 vcc, vcc, s[36:37]
s_cmp_eq_u32 s72, 0
s_cselect_b64 exec, vcc, -1
buffer_load_dwordx4 a[196:199], v250, s[20:23], s66 offen offset:0 sc1
s_mov_b64 exec, -1
s_branch LSN4_%=
LSF4_%=:
s_bitset0_b32 s17, 4
CV v76,v72,v73
CV v77,v74,v75
ds_bpermute_b32 v76, v232, v76
ds_bpermute_b32 v77, v232, v77
v_lshlrev_b32 v10, 16, v8
v_and_b32 v11, 0xffff0000, v8
v_lshlrev_b32 v12, 16, v9
v_and_b32 v13, 0xffff0000, v9
v_lshlrev_b32 v14, 16, v40
v_and_b32 v15, 0xffff0000, v40
v_lshlrev_b32 v42, 16, v41
v_and_b32 v43, 0xffff0000, v41
s_add_u32 s80, s63, 0
s_waitcnt lgkmcnt(0)
v_lshlrev_b32 v72, 16, v76
v_and_b32 v73, 0xffff0000, v76
v_lshlrev_b32 v74, 16, v77
v_and_b32 v75, 0xffff0000, v77
v_mul_f32 v72, v72, v10
v_mul_f32 v73, v73, v11
v_mul_f32 v74, v74, v12
v_mul_f32 v75, v75, v13
CV v76,v72,v73
CV v77,v74,v75
v_lshlrev_b32 v72, 16, v76
v_and_b32 v73, 0xffff0000, v76
v_lshlrev_b32 v74, 16, v77
v_and_b32 v75, 0xffff0000, v77
v_add_f32 v72, v72, v14
v_add_f32 v73, v73, v15
v_add_f32 v74, v74, v42
v_add_f32 v75, v75, v43
CV v44,v72,v73
CV v45,v74,v75
buffer_store_dwordx2 v[44:45], v254, s[40:43], s80 offen offset:0 nt
s_nop 1
LSN4_%=:
s_bitcmp0_b32 s17, 3
s_cbranch_scc1 LSN3_%=
MM v[72:75],v[64:67],v[80:83],0
MM v[72:75],v[64:67],a[100:103],v[72:75]
MM v[72:75],v[68:71],v[112:115],v[72:75]
MM v[72:75],v[68:71],a[108:111],v[72:75]
v_mov_b32 v76, v83
AR v77,a103
v_mov_b32 v78, v115
AR v79,a111
s_nop 3
v_cmp_u_f32 s[30:31], v72, v72
s_nop 1
s_cmp_eq_u64 s[30:31], 0
s_cbranch_scc1 LSF3_%=
s_cmp_ge_u32 s72, 2
s_cbranch_scc1 LSF3_%=
v_cmp_eq_u32 vcc, s69, v242
v_cmp_eq_u32 s[36:37], -1, v78
s_add_u32 s66, s16, 16384
s_and_b64 vcc, vcc, s[36:37]
s_cmp_eq_u32 s72, 0
s_cselect_b64 exec, vcc, -1
s_and_b32 exec_lo, exec_lo, 0xffff0000
buffer_load_dwordx4 v[112:115], v250, s[20:23], s66 offen offset:0 sc1
s_mov_b64 exec, -1
v_cmp_eq_u32 vcc, s69, v142
v_cmp_eq_u32 s[36:37], -1, v79
s_add_u32 s66, s44, 16384
s_and_b64 vcc, vcc, s[36:37]
s_cmp_eq_u32 s72, 0
s_cselect_b64 exec, vcc, -1
buffer_load_dwordx4 a[108:111], v250, s[20:23], s66 offen offset:0 sc1
s_mov_b64 exec, -1
v_cmp_eq_u32 vcc, s69, v242
v_cmp_eq_u32 s[36:37], -1, v76
s_add_u32 s66, s16, 14336
s_and_b64 vcc, vcc, s[36:37]
s_cmp_eq_u32 s72, 0
s_cselect_b64 exec, vcc, -1
s_and_b32 exec_lo, exec_lo, 0xffff0000
buffer_load_dwordx4 v[80:83], v250, s[20:23], s66 offen offset:0 sc1
s_mov_b64 exec, -1
v_cmp_eq_u32 vcc, s69, v142
v_cmp_eq_u32 s[36:37], -1, v77
s_add_u32 s66, s44, 14336
s_and_b64 vcc, vcc, s[36:37]
s_cmp_eq_u32 s72, 0
s_cselect_b64 exec, vcc, -1
buffer_load_dwordx4 a[100:103], v250, s[20:23], s66 offen offset:0 sc1
s_mov_b64 exec, -1
s_branch LSN3_%=
LSF3_%=:
s_bitset0_b32 s17, 3
CV v76,v72,v73
CV v77,v74,v75
ds_bpermute_b32 v76, v232, v76
ds_bpermute_b32 v77, v232, v77
v_lshlrev_b32 v10, 16, v6
v_and_b32 v11, 0xffff0000, v6
v_lshlrev_b32 v12, 16, v7
v_and_b32 v13, 0xffff0000, v7
v_lshlrev_b32 v14, 16, v38
v_and_b32 v15, 0xffff0000, v38
v_lshlrev_b32 v42, 16, v39
v_and_b32 v43, 0xffff0000, v39
s_add_u32 s80, s18, 41088
s_waitcnt lgkmcnt(0)
v_lshlrev_b32 v72, 16, v76
v_and_b32 v73, 0xffff0000, v76
v_lshlrev_b32 v74, 16, v77
v_and_b32 v75, 0xffff0000, v77
v_mul_f32 v72, v72, v10
v_mul_f32 v73, v73, v11
v_mul_f32 v74, v74, v12
v_mul_f32 v75, v75, v13
CV v76,v72,v73
CV v77,v74,v75
v_lshlrev_b32 v72, 16, v76
v_and_b32 v73, 0xffff0000, v76
v_lshlrev_b32 v74, 16, v77
v_and_b32 v75, 0xffff0000, v77
v_add_f32 v72, v72, v14
v_add_f32 v73, v73, v15
v_add_f32 v74, v74, v42
v_add_f32 v75, v75, v43
CV v46,v72,v73
CV v47,v74,v75
buffer_store_dwordx2 v[46:47], v251, s[40:43], s80 offen offset:0 nt
s_nop 1
LSN3_%=:
s_bitcmp0_b32 s17, 2
s_cbranch_scc1 LSN2_%=
MM v[72:75],v[64:67],v[16:19],0
MM v[72:75],v[64:67],a[68:71],v[72:75]
MM v[72:75],v[68:71],v[48:51],v[72:75]
MM v[72:75],v[68:71],a[76:79],v[72:75]
v_mov_b32 v76, v19
AR v77,a71
v_mov_b32 v78, v51
AR v79,a79
s_nop 3
v_cmp_u_f32 s[30:31], v72, v72
s_nop 1
s_cmp_eq_u64 s[30:31], 0
s_cbranch_scc1 LSF2_%=
s_cmp_ge_u32 s72, 2
s_cbranch_scc1 LSF2_%=
v_cmp_eq_u32 vcc, s69, v242
v_cmp_eq_u32 s[36:37], -1, v78
s_add_u32 s66, s16, 12288
s_and_b64 vcc, vcc, s[36:37]
s_cmp_eq_u32 s72, 0
s_cselect_b64 exec, vcc, -1
s_and_b32 exec_lo, exec_lo, 0xffff0000
buffer_load_dwordx4 v[48:51], v250, s[20:23], s66 offen offset:0 sc1
s_mov_b64 exec, -1
v_cmp_eq_u32 vcc, s69, v142
v_cmp_eq_u32 s[36:37], -1, v79
s_add_u32 s66, s44, 12288
s_and_b64 vcc, vcc, s[36:37]
s_cmp_eq_u32 s72, 0
s_cselect_b64 exec, vcc, -1
buffer_load_dwordx4 a[76:79], v250, s[20:23], s66 offen offset:0 sc1
s_mov_b64 exec, -1
v_cmp_eq_u32 vcc, s69, v242
v_cmp_eq_u32 s[36:37], -1, v76
s_add_u32 s66, s16, 10240
s_and_b64 vcc, vcc, s[36:37]
s_cmp_eq_u32 s72, 0
s_cselect_b64 exec, vcc, -1
s_and_b32 exec_lo, exec_lo, 0xffff0000
buffer_load_dwordx4 v[16:19], v250, s[20:23], s66 offen offset:0 sc1
s_mov_b64 exec, -1
v_cmp_eq_u32 vcc, s69, v142
v_cmp_eq_u32 s[36:37], -1, v77
s_add_u32 s66, s44, 10240
s_and_b64 vcc, vcc, s[36:37]
s_cmp_eq_u32 s72, 0
s_cselect_b64 exec, vcc, -1
buffer_load_dwordx4 a[68:71], v250, s[20:23], s66 offen offset:0 sc1
s_mov_b64 exec, -1
s_branch LSN2_%=
LSF2_%=:
s_bitset0_b32 s17, 2
CV v76,v72,v73
CV v77,v74,v75
ds_bpermute_b32 v76, v232, v76
ds_bpermute_b32 v77, v232, v77
v_lshlrev_b32 v10, 16, v4
v_and_b32 v11, 0xffff0000, v4
v_lshlrev_b32 v12, 16, v5
v_and_b32 v13, 0xffff0000, v5
v_lshlrev_b32 v14, 16, v36
v_and_b32 v15, 0xffff0000, v36
v_lshlrev_b32 v42, 16, v37
v_and_b32 v43, 0xffff0000, v37
s_add_u32 s80, s18, 40960
s_waitcnt lgkmcnt(0)
v_lshlrev_b32 v72, 16, v76
v_and_b32 v73, 0xffff0000, v76
v_lshlrev_b32 v74, 16, v77
v_and_b32 v75, 0xffff0000, v77
v_mul_f32 v72, v72, v10
v_mul_f32 v73, v73, v11
v_mul_f32 v74, v74, v12
v_mul_f32 v75, v75, v13
CV v76,v72,v73
CV v77,v74,v75
v_lshlrev_b32 v72, 16, v76
v_and_b32 v73, 0xffff0000, v76
v_lshlrev_b32 v74, 16, v77
v_and_b32 v75, 0xffff0000, v77
v_add_f32 v72, v72, v14
v_add_f32 v73, v73, v15
v_add_f32 v74, v74, v42
v_add_f32 v75, v75, v43
CV v44,v72,v73
CV v45,v74,v75
buffer_store_dwordx2 v[44:45], v251, s[40:43], s80 offen offset:0 nt
s_nop 1
LSN2_%=:
s_bitcmp0_b32 s17, 1
s_cbranch_scc1 LSN1_%=
MM v[72:75],v[64:67],a[32:35],0
MM v[72:75],v[64:67],a[36:39],v[72:75]
MM v[72:75],v[68:71],a[40:43],v[72:75]
MM v[72:75],v[68:71],a[44:47],v[72:75]
AR v76,a35
AR v77,a39
AR v78,a43
AR v79,a47
s_nop 3
v_cmp_u_f32 s[30:31], v72, v72
s_nop 1
s_cmp_eq_u64 s[30:31], 0
s_cbranch_scc1 LSF1_%=
s_cmp_ge_u32 s72, 2
s_cbranch_scc1 LSF1_%=
v_cmp_eq_u32 vcc, s69, v241
v_cmp_eq_u32 s[36:37], -1, v78
s_add_u32 s66, s16, 6144
s_and_b64 vcc, vcc, s[36:37]
s_cmp_eq_u32 s72, 0
s_cselect_b64 exec, vcc, -1
s_and_b32 exec_lo, exec_lo, 0xffff0000
buffer_load_dwordx4 a[40:43], v250, s[20:23], s66 offen offset:0 sc1
s_mov_b64 exec, -1
v_cmp_eq_u32 vcc, s69, v141
v_cmp_eq_u32 s[36:37], -1, v79
s_add_u32 s66, s44, 6144
s_and_b64 vcc, vcc, s[36:37]
s_cmp_eq_u32 s72, 0
s_cselect_b64 exec, vcc, -1
buffer_load_dwordx4 a[44:47], v250, s[20:23], s66 offen offset:0 sc1
s_mov_b64 exec, -1
v_cmp_eq_u32 vcc, s69, v241
v_cmp_eq_u32 s[36:37], -1, v76
s_add_u32 s66, s16, 4096
s_and_b64 vcc, vcc, s[36:37]
s_cmp_eq_u32 s72, 0
s_cselect_b64 exec, vcc, -1
s_and_b32 exec_lo, exec_lo, 0xffff0000
buffer_load_dwordx4 a[32:35], v250, s[20:23], s66 offen offset:0 sc1
s_mov_b64 exec, -1
v_cmp_eq_u32 vcc, s69, v141
v_cmp_eq_u32 s[36:37], -1, v77
s_add_u32 s66, s44, 4096
s_and_b64 vcc, vcc, s[36:37]
s_cmp_eq_u32 s72, 0
s_cselect_b64 exec, vcc, -1
buffer_load_dwordx4 a[36:39], v250, s[20:23], s66 offen offset:0 sc1
s_mov_b64 exec, -1
s_branch LSN1_%=
LSF1_%=:
s_bitset0_b32 s17, 1
CV v76,v72,v73
CV v77,v74,v75
ds_bpermute_b32 v76, v232, v76
ds_bpermute_b32 v77, v232, v77
v_lshlrev_b32 v10, 16, v2
v_and_b32 v11, 0xffff0000, v2
v_lshlrev_b32 v12, 16, v3
v_and_b32 v13, 0xffff0000, v3
v_lshlrev_b32 v14, 16, v34
v_and_b32 v15, 0xffff0000, v34
v_lshlrev_b32 v42, 16, v35
v_and_b32 v43, 0xffff0000, v35
s_add_u32 s80, s18, 128
s_waitcnt lgkmcnt(0)
v_lshlrev_b32 v72, 16, v76
v_and_b32 v73, 0xffff0000, v76
v_lshlrev_b32 v74, 16, v77
v_and_b32 v75, 0xffff0000, v77
v_mul_f32 v72, v72, v10
v_mul_f32 v73, v73, v11
v_mul_f32 v74, v74, v12
v_mul_f32 v75, v75, v13
CV v76,v72,v73
CV v77,v74,v75
v_lshlrev_b32 v72, 16, v76
v_and_b32 v73, 0xffff0000, v76
v_lshlrev_b32 v74, 16, v77
v_and_b32 v75, 0xffff0000, v77
v_add_f32 v72, v72, v14
v_add_f32 v73, v73, v15
v_add_f32 v74, v74, v42
v_add_f32 v75, v75, v43
CV v46,v72,v73
CV v47,v74,v75
buffer_store_dwordx2 v[46:47], v251, s[40:43], s80 offen offset:0 nt
s_nop 1
LSN1_%=:
s_bitcmp0_b32 s17, 0
s_cbranch_scc1 LSN0_%=
MM v[72:75],v[64:67],a[0:3],0
MM v[72:75],v[64:67],a[4:7],v[72:75]
MM v[72:75],v[68:71],a[8:11],v[72:75]
MM v[72:75],v[68:71],a[12:15],v[72:75]
AR v76,a3
AR v77,a7
AR v78,a11
AR v79,a15
s_nop 3
v_cmp_u_f32 s[30:31], v72, v72
s_nop 1
s_cmp_eq_u64 s[30:31], 0
s_cbranch_scc1 LSF0_%=
s_cmp_ge_u32 s72, 2
s_cbranch_scc1 LSF0_%=
v_cmp_eq_u32 vcc, s69, v240
v_cmp_eq_u32 s[36:37], -1, v78
s_add_u32 s66, s16, 2048
s_and_b64 vcc, vcc, s[36:37]
s_cmp_eq_u32 s72, 0
s_cselect_b64 exec, vcc, -1
s_and_b32 exec_lo, exec_lo, 0xffff0000
buffer_load_dwordx4 a[8:11], v250, s[20:23], s66 offen offset:0 sc1
s_mov_b64 exec, -1
v_cmp_eq_u32 vcc, s69, v140
v_cmp_eq_u32 s[36:37], -1, v79
s_add_u32 s66, s44, 2048
s_and_b64 vcc, vcc, s[36:37]
s_cmp_eq_u32 s72, 0
s_cselect_b64 exec, vcc, -1
buffer_load_dwordx4 a[12:15], v250, s[20:23], s66 offen offset:0 sc1
s_mov_b64 exec, -1
v_cmp_eq_u32 vcc, s69, v240
v_cmp_eq_u32 s[36:37], -1, v76
s_add_u32 s66, s16, 0
s_and_b64 vcc, vcc, s[36:37]
s_cmp_eq_u32 s72, 0
s_cselect_b64 exec, vcc, -1
s_and_b32 exec_lo, exec_lo, 0xffff0000
buffer_load_dwordx4 a[0:3], v250, s[20:23], s66 offen offset:0 sc1
s_mov_b64 exec, -1
v_cmp_eq_u32 vcc, s69, v140
v_cmp_eq_u32 s[36:37], -1, v77
s_add_u32 s66, s44, 0
s_and_b64 vcc, vcc, s[36:37]
s_cmp_eq_u32 s72, 0
s_cselect_b64 exec, vcc, -1
buffer_load_dwordx4 a[4:7], v250, s[20:23], s66 offen offset:0 sc1
s_mov_b64 exec, -1
s_branch LSN0_%=
LSF0_%=:
s_bitset0_b32 s17, 0
CV v76,v72,v73
CV v77,v74,v75
ds_bpermute_b32 v76, v232, v76
ds_bpermute_b32 v77, v232, v77
v_lshlrev_b32 v10, 16, v0
v_and_b32 v11, 0xffff0000, v0
v_lshlrev_b32 v12, 16, v1
v_and_b32 v13, 0xffff0000, v1
v_lshlrev_b32 v14, 16, v32
v_and_b32 v15, 0xffff0000, v32
v_lshlrev_b32 v42, 16, v33
v_and_b32 v43, 0xffff0000, v33
s_add_u32 s80, s18, 0
s_waitcnt lgkmcnt(0)
v_lshlrev_b32 v72, 16, v76
v_and_b32 v73, 0xffff0000, v76
v_lshlrev_b32 v74, 16, v77
v_and_b32 v75, 0xffff0000, v77
v_mul_f32 v72, v72, v10
v_mul_f32 v73, v73, v11
v_mul_f32 v74, v74, v12
)ASM"
R"ASM(
v_mul_f32 v75, v75, v13
CV v76,v72,v73
CV v77,v74,v75
v_lshlrev_b32 v72, 16, v76
v_and_b32 v73, 0xffff0000, v76
v_lshlrev_b32 v74, 16, v77
v_and_b32 v75, 0xffff0000, v77
v_add_f32 v72, v72, v14
v_add_f32 v73, v73, v15
v_add_f32 v74, v74, v42
v_add_f32 v75, v75, v43
CV v44,v72,v73
CV v45,v74,v75
buffer_store_dwordx2 v[44:45], v251, s[40:43], s80 offen offset:0 nt
s_nop 1
LSN0_%=:
v_cmp_ne_u32 vcc, s69, v242
v_cmp_ne_u32 s[36:37], s69, v142
s_nop 1
s_or_b64 vcc, vcc, s[36:37]
s_cselect_b32 s76, 0, 15
s_and_b32 s76, s76, s73
s_cbranch_scc1 LSU_%=
buffer_load_dwordx4 v[240:243], v253, s[20:23], s68 offen offset:0 sc1
buffer_load_dwordx4 v[140:143], v253, s[20:23], s45 offen offset:0 sc1
LSU_%=:
s_or_b32 s76, s17, s65
s_cbranch_scc1 LSW_%=
s_branch LQE_%=
LKEND_ka:
LSITE0_%=:
s_mov_b32 s73, 0
s_mov_b32 s75, 16384
s_mov_b32 s74, 0
s_branch LRARE002_%=
LSITE2_%=:
s_mov_b32 s73, 2
s_mov_b32 s75, 49152
s_mov_b32 s74, 32768
s_branch LRARE002_%=
LRARE002_%=:
s_nop 15
s_waitcnt vmcnt(0) lgkmcnt(0)
v_add_u32 v249, s74, v224
RD a[192:195],v249 offset:0
RD a[208:211],v249 offset:4096
RD a[224:227],v249 offset:8192
RD a[240:243],v249 offset:12288
v_add_u32 v250, s74, v225
RD a[196:199],v250 offset:0
RD a[212:215],v250 offset:4096
RD a[228:231],v250 offset:8192
RD a[244:247],v250 offset:12288
v_add_u32 v251, s74, v226
RD a[200:203],v251 offset:0
RD a[216:219],v251 offset:4096
RD a[232:235],v251 offset:8192
RD a[248:251],v251 offset:12288
v_add_u32 v254, s74, v227
RD a[204:207],v254 offset:0
RD a[220:223],v254 offset:4096
RD a[236:239],v254 offset:8192
RD a[252:255],v254 offset:12288
s_waitcnt lgkmcnt(0)
MM v[0:3],a[192:195],v[160:163],v[128:131]
MM v[0:3],a[196:199],v[176:179],v[0:3]
MM v[0:3],a[200:203],v[192:195],v[0:3]
MM v[0:3],a[204:207],v[208:211],v[0:3]
MM v[4:7],a[220:223],v[208:211],v[128:131]
MM v[4:7],a[216:219],v[192:195],v[4:7]
MM v[4:7],a[212:215],v[176:179],v[4:7]
MM v[4:7],a[208:211],v[160:163],v[4:7]
MM v[12:15],a[208:211],v[164:167],v[132:135]
MM v[12:15],a[212:215],v[180:183],v[12:15]
MM v[12:15],a[216:219],v[196:199],v[12:15]
MM v[12:15],a[220:223],v[212:215],v[12:15]
MM v[8:11],a[204:207],v[212:215],v[132:135]
MM v[8:11],a[200:203],v[196:199],v[8:11]
MM v[8:11],a[196:199],v[180:183],v[8:11]
MM v[8:11],a[192:195],v[164:167],v[8:11]
MM v[32:35],a[224:227],v[160:163],v[128:131]
MM v[32:35],a[228:231],v[176:179],v[32:35]
MM v[32:35],a[232:235],v[192:195],v[32:35]
MM v[32:35],a[236:239],v[208:211],v[32:35]
MM v[36:39],a[252:255],v[208:211],v[128:131]
MM v[36:39],a[248:251],v[192:195],v[36:39]
MM v[36:39],a[244:247],v[176:179],v[36:39]
MM v[36:39],a[240:243],v[160:163],v[36:39]
MM v[44:47],a[240:243],v[164:167],v[132:135]
MM v[44:47],a[244:247],v[180:183],v[44:47]
MM v[44:47],a[248:251],v[196:199],v[44:47]
MM v[44:47],a[252:255],v[212:215],v[44:47]
MM v[40:43],a[236:239],v[212:215],v[132:135]
MM v[40:43],a[232:235],v[196:199],v[40:43]
MM v[40:43],a[228:231],v[180:183],v[40:43]
MM v[40:43],a[224:227],v[164:167],v[40:43]
s_nop 15
v_add_u32 v249, s75, v224
RD a[192:195],v249 offset:0
RD a[208:211],v249 offset:4096
RD a[224:227],v249 offset:8192
RD a[240:243],v249 offset:12288
v_add_u32 v250, s75, v225
RD a[196:199],v250 offset:0
RD a[212:215],v250 offset:4096
RD a[228:231],v250 offset:8192
RD a[244:247],v250 offset:12288
v_add_u32 v251, s75, v226
RD a[200:203],v251 offset:0
RD a[216:219],v251 offset:4096
RD a[232:235],v251 offset:8192
RD a[248:251],v251 offset:12288
v_add_u32 v254, s75, v227
RD a[204:207],v254 offset:0
RD a[220:223],v254 offset:4096
RD a[236:239],v254 offset:8192
RD a[252:255],v254 offset:12288
s_waitcnt lgkmcnt(0)
v_mov_b32 v236, v247
v_mov_b32 v237, 0
v_max3_f32 v249, v0, v1, v2
v_max3_f32 v251, v32, v33, v34
v_max3_f32 v249, v249, v3, v4
v_max3_f32 v251, v251, v35, v36
v_max3_f32 v249, v249, v5, v6
v_max3_f32 v251, v251, v37, v38
v_max3_f32 v249, v249, v7, v39
v_max_f32 v249, v249, v251
v_log_f32 v250, v247
s_lshl_b64 exec, 0xffff, 0
v_max_f32 v249, v249, v250
s_mov_b64 exec, -1
v_mov_b32 v251, v249
s_nop 1
v_permlane16_swap_b32 v249, v251
v_max_f32 v249, v249, v251
v_mov_b32 v251, v249
s_nop 1
v_permlane32_swap_b32 v249, v251
v_max_f32 v249, v249, v251
v_max_f32 v249, 0, v249
v_sub_f32 v250, 0, v249
EX v250,v250
v_sub_f32 v128, v128, v249
v_sub_f32 v129, v129, v249
v_sub_f32 v130, v130, v249
v_sub_f32 v131, v131, v249
v_sub_f32 v64, v64, v249
v_sub_f32 v0, v0, v249
v_sub_f32 v65, v65, v249
v_sub_f32 v1, v1, v249
v_sub_f32 v66, v66, v249
v_sub_f32 v2, v2, v249
v_sub_f32 v67, v67, v249
v_sub_f32 v3, v3, v249
v_sub_f32 v68, v68, v249
v_sub_f32 v4, v4, v249
v_sub_f32 v69, v69, v249
v_sub_f32 v5, v5, v249
v_sub_f32 v70, v70, v249
v_sub_f32 v6, v6, v249
v_sub_f32 v71, v71, v249
v_sub_f32 v7, v7, v249
v_sub_f32 v96, v96, v249
v_sub_f32 v32, v32, v249
v_sub_f32 v97, v97, v249
v_sub_f32 v33, v33, v249
v_sub_f32 v98, v98, v249
v_sub_f32 v34, v34, v249
v_sub_f32 v99, v99, v249
v_sub_f32 v35, v35, v249
v_sub_f32 v100, v100, v249
v_sub_f32 v36, v36, v249
v_sub_f32 v101, v101, v249
v_sub_f32 v37, v37, v249
v_sub_f32 v102, v102, v249
v_sub_f32 v38, v38, v249
v_sub_f32 v103, v103, v249
v_sub_f32 v39, v39, v249
s_lshl_b64 exec, 0xffff, 0
v_mul_f32 v236, v236, v250
s_mov_b64 exec, -1
AR v254,a0
v_mul_f32 v254, v254, v250
AW a0,v254
AR v251,a1
v_mul_f32 v251, v251, v250
AW a1,v251
AR v254,a2
v_mul_f32 v254, v254, v250
AW a2,v254
AR v251,a3
v_mul_f32 v251, v251, v250
AW a3,v251
AR v254,a4
v_mul_f32 v254, v254, v250
AW a4,v254
AR v251,a5
v_mul_f32 v251, v251, v250
AW a5,v251
AR v254,a6
v_mul_f32 v254, v254, v250
AW a6,v254
AR v251,a7
v_mul_f32 v251, v251, v250
AW a7,v251
AR v254,a32
v_mul_f32 v254, v254, v250
AW a32,v254
AR v251,a33
v_mul_f32 v251, v251, v250
AW a33,v251
AR v254,a34
v_mul_f32 v254, v254, v250
AW a34,v254
AR v251,a35
v_mul_f32 v251, v251, v250
AW a35,v251
AR v254,a36
v_mul_f32 v254, v254, v250
AW a36,v254
AR v251,a37
v_mul_f32 v251, v251, v250
AW a37,v251
AR v254,a38
v_mul_f32 v254, v254, v250
AW a38,v254
AR v251,a39
v_mul_f32 v251, v251, v250
AW a39,v251
AR v254,a64
v_mul_f32 v254, v254, v250
AW a64,v254
AR v251,a65
v_mul_f32 v251, v251, v250
AW a65,v251
AR v254,a66
v_mul_f32 v254, v254, v250
AW a66,v254
AR v251,a67
v_mul_f32 v251, v251, v250
AW a67,v251
AR v254,a68
v_mul_f32 v254, v254, v250
AW a68,v254
AR v251,a69
v_mul_f32 v251, v251, v250
AW a69,v251
AR v254,a70
v_mul_f32 v254, v254, v250
AW a70,v254
AR v251,a71
v_mul_f32 v251, v251, v250
AW a71,v251
AR v254,a96
v_mul_f32 v254, v254, v250
AW a96,v254
AR v251,a97
v_mul_f32 v251, v251, v250
AW a97,v251
AR v254,a98
v_mul_f32 v254, v254, v250
AW a98,v254
AR v251,a99
v_mul_f32 v251, v251, v250
AW a99,v251
AR v254,a100
v_mul_f32 v254, v254, v250
AW a100,v254
AR v251,a101
v_mul_f32 v251, v251, v250
AW a101,v251
AR v254,a102
v_mul_f32 v254, v254, v250
AW a102,v254
AR v251,a103
v_mul_f32 v251, v251, v250
AW a103,v251
v_max3_f32 v249, v8, v9, v10
v_max3_f32 v251, v40, v41, v42
v_max3_f32 v249, v249, v11, v12
v_max3_f32 v251, v251, v43, v44
v_max3_f32 v249, v249, v13, v14
v_max3_f32 v251, v251, v45, v46
v_max3_f32 v249, v249, v15, v47
v_max_f32 v249, v249, v251
v_log_f32 v250, v247
s_lshl_b64 exec, 0xffff, 16
v_max_f32 v249, v249, v250
s_mov_b64 exec, -1
v_mov_b32 v251, v249
s_nop 1
v_permlane16_swap_b32 v249, v251
v_max_f32 v249, v249, v251
v_mov_b32 v251, v249
s_nop 1
v_permlane32_swap_b32 v249, v251
v_max_f32 v249, v249, v251
v_max_f32 v249, 0, v249
v_sub_f32 v250, 0, v249
EX v250,v250
v_sub_f32 v132, v132, v249
v_sub_f32 v133, v133, v249
v_sub_f32 v134, v134, v249
v_sub_f32 v135, v135, v249
v_sub_f32 v72, v72, v249
v_sub_f32 v8, v8, v249
v_sub_f32 v73, v73, v249
v_sub_f32 v9, v9, v249
v_sub_f32 v74, v74, v249
v_sub_f32 v10, v10, v249
v_sub_f32 v75, v75, v249
v_sub_f32 v11, v11, v249
v_sub_f32 v76, v76, v249
v_sub_f32 v12, v12, v249
v_sub_f32 v77, v77, v249
v_sub_f32 v13, v13, v249
v_sub_f32 v78, v78, v249
v_sub_f32 v14, v14, v249
v_sub_f32 v79, v79, v249
v_sub_f32 v15, v15, v249
v_sub_f32 v104, v104, v249
v_sub_f32 v40, v40, v249
v_sub_f32 v105, v105, v249
v_sub_f32 v41, v41, v249
v_sub_f32 v106, v106, v249
v_sub_f32 v42, v42, v249
v_sub_f32 v107, v107, v249
v_sub_f32 v43, v43, v249
v_sub_f32 v108, v108, v249
v_sub_f32 v44, v44, v249
v_sub_f32 v109, v109, v249
v_sub_f32 v45, v45, v249
v_sub_f32 v110, v110, v249
v_sub_f32 v46, v46, v249
v_sub_f32 v111, v111, v249
v_sub_f32 v47, v47, v249
s_lshl_b64 exec, 0xffff, 16
v_mul_f32 v236, v236, v250
s_mov_b64 exec, -1
AR v254,a8
v_mul_f32 v254, v254, v250
AW a8,v254
AR v251,a9
v_mul_f32 v251, v251, v250
AW a9,v251
AR v254,a10
v_mul_f32 v254, v254, v250
AW a10,v254
AR v251,a11
v_mul_f32 v251, v251, v250
AW a11,v251
AR v254,a12
v_mul_f32 v254, v254, v250
AW a12,v254
AR v251,a13
v_mul_f32 v251, v251, v250
AW a13,v251
AR v254,a14
v_mul_f32 v254, v254, v250
AW a14,v254
AR v251,a15
v_mul_f32 v251, v251, v250
AW a15,v251
AR v254,a40
v_mul_f32 v254, v254, v250
AW a40,v254
AR v251,a41
v_mul_f32 v251, v251, v250
AW a41,v251
AR v254,a42
v_mul_f32 v254, v254, v250
AW a42,v254
AR v251,a43
v_mul_f32 v251, v251, v250
AW a43,v251
AR v254,a44
v_mul_f32 v254, v254, v250
AW a44,v254
AR v251,a45
v_mul_f32 v251, v251, v250
AW a45,v251
AR v254,a46
v_mul_f32 v254, v254, v250
AW a46,v254
AR v251,a47
v_mul_f32 v251, v251, v250
AW a47,v251
AR v254,a72
v_mul_f32 v254, v254, v250
AW a72,v254
AR v251,a73
v_mul_f32 v251, v251, v250
AW a73,v251
AR v254,a74
v_mul_f32 v254, v254, v250
AW a74,v254
AR v251,a75
v_mul_f32 v251, v251, v250
AW a75,v251
AR v254,a76
v_mul_f32 v254, v254, v250
AW a76,v254
AR v251,a77
v_mul_f32 v251, v251, v250
AW a77,v251
AR v254,a78
v_mul_f32 v254, v254, v250
AW a78,v254
AR v251,a79
v_mul_f32 v251, v251, v250
AW a79,v251
AR v254,a104
v_mul_f32 v254, v254, v250
AW a104,v254
AR v251,a105
v_mul_f32 v251, v251, v250
AW a105,v251
AR v254,a106
v_mul_f32 v254, v254, v250
AW a106,v254
AR v251,a107
v_mul_f32 v251, v251, v250
AW a107,v251
AR v254,a108
v_mul_f32 v254, v254, v250
AW a108,v254
AR v251,a109
v_mul_f32 v251, v251, v250
AW a109,v251
AR v254,a110
v_mul_f32 v254, v254, v250
AW a110,v254
AR v251,a111
v_mul_f32 v251, v251, v250
AW a111,v251
EX v0,v0
EX v1,v1
EX v2,v2
EX v3,v3
EX v4,v4
EX v5,v5
EX v6,v6
EX v7,v7
EX v8,v8
EX v9,v9
EX v10,v10
EX v11,v11
EX v12,v12
EX v13,v13
EX v14,v14
EX v15,v15
EX v32,v32
EX v33,v33
EX v34,v34
EX v35,v35
EX v36,v36
EX v37,v37
EX v38,v38
EX v39,v39
EX v40,v40
EX v41,v41
EX v42,v42
EX v43,v43
EX v44,v44
EX v45,v45
EX v46,v46
EX v47,v47
CV v0,v0,v1
CV v1,v2,v3
CV v2,v4,v5
CV v3,v6,v7
CV v8,v8,v9
CV v9,v10,v11
CV v10,v12,v13
CV v11,v14,v15
CV v4,v32,v33
CV v5,v34,v35
CV v6,v36,v37
CV v7,v38,v39
CV v12,v40,v41
CV v13,v42,v43
CV v14,v44,v45
CV v15,v46,v47
v_smfmac_f32_16x16x64_bf16 v[236:239], v[144:147], v[0:7], v234
s_nop 0
v_smfmac_f32_16x16x64_bf16 v[236:239], v[148:151], v[8:15], v234
s_nop 9
v_add_f32 v246, v236, v237
s_cmp_eq_u32 s73, 0
s_cbranch_scc1 LBACK0_%=
s_cmp_eq_u32 s73, 2
s_cbranch_scc1 LBACK2_%=
s_endpgm
LSITE1_%=:
s_mov_b32 s73, 1
s_mov_b32 s75, 32768
s_mov_b32 s74, 16384
s_branch LRARE102_%=
LRARE102_%=:
s_nop 15
s_waitcnt vmcnt(0) lgkmcnt(0)
v_add_u32 v249, s74, v224
RD a[192:195],v249 offset:0
RD a[208:211],v249 offset:4096
RD a[224:227],v249 offset:8192
RD a[240:243],v249 offset:12288
v_add_u32 v250, s74, v225
RD a[196:199],v250 offset:0
RD a[212:215],v250 offset:4096
RD a[228:231],v250 offset:8192
RD a[244:247],v250 offset:12288
v_add_u32 v251, s74, v226
RD a[200:203],v251 offset:0
RD a[216:219],v251 offset:4096
RD a[232:235],v251 offset:8192
RD a[248:251],v251 offset:12288
v_add_u32 v254, s74, v227
RD a[204:207],v254 offset:0
RD a[220:223],v254 offset:4096
RD a[236:239],v254 offset:8192
RD a[252:255],v254 offset:12288
s_waitcnt lgkmcnt(0)
MM v[64:67],a[192:195],v[160:163],v[128:131]
MM v[64:67],a[196:199],v[176:179],v[64:67]
MM v[64:67],a[200:203],v[192:195],v[64:67]
MM v[64:67],a[204:207],v[208:211],v[64:67]
MM v[68:71],a[220:223],v[208:211],v[128:131]
MM v[68:71],a[216:219],v[192:195],v[68:71]
MM v[68:71],a[212:215],v[176:179],v[68:71]
MM v[68:71],a[208:211],v[160:163],v[68:71]
MM v[76:79],a[208:211],v[164:167],v[132:135]
MM v[76:79],a[212:215],v[180:183],v[76:79]
)ASM"
R"ASM(
MM v[76:79],a[216:219],v[196:199],v[76:79]
MM v[76:79],a[220:223],v[212:215],v[76:79]
MM v[72:75],a[204:207],v[212:215],v[132:135]
MM v[72:75],a[200:203],v[196:199],v[72:75]
MM v[72:75],a[196:199],v[180:183],v[72:75]
MM v[72:75],a[192:195],v[164:167],v[72:75]
MM v[96:99],a[224:227],v[160:163],v[128:131]
MM v[96:99],a[228:231],v[176:179],v[96:99]
MM v[96:99],a[232:235],v[192:195],v[96:99]
MM v[96:99],a[236:239],v[208:211],v[96:99]
MM v[100:103],a[252:255],v[208:211],v[128:131]
MM v[100:103],a[248:251],v[192:195],v[100:103]
MM v[100:103],a[244:247],v[176:179],v[100:103]
MM v[100:103],a[240:243],v[160:163],v[100:103]
MM v[108:111],a[240:243],v[164:167],v[132:135]
MM v[108:111],a[244:247],v[180:183],v[108:111]
MM v[108:111],a[248:251],v[196:199],v[108:111]
MM v[108:111],a[252:255],v[212:215],v[108:111]
MM v[104:107],a[236:239],v[212:215],v[132:135]
MM v[104:107],a[232:235],v[196:199],v[104:107]
MM v[104:107],a[228:231],v[180:183],v[104:107]
MM v[104:107],a[224:227],v[164:167],v[104:107]
s_nop 15
v_add_u32 v249, s75, v224
RD a[192:195],v249 offset:0
RD a[208:211],v249 offset:4096
RD a[224:227],v249 offset:8192
RD a[240:243],v249 offset:12288
v_add_u32 v250, s75, v225
RD a[196:199],v250 offset:0
RD a[212:215],v250 offset:4096
RD a[228:231],v250 offset:8192
RD a[244:247],v250 offset:12288
v_add_u32 v251, s75, v226
RD a[200:203],v251 offset:0
RD a[216:219],v251 offset:4096
RD a[232:235],v251 offset:8192
RD a[248:251],v251 offset:12288
v_add_u32 v254, s75, v227
RD a[204:207],v254 offset:0
RD a[220:223],v254 offset:4096
RD a[236:239],v254 offset:8192
RD a[252:255],v254 offset:12288
s_waitcnt lgkmcnt(0)
v_mov_b32 v236, v246
v_mov_b32 v237, 0
v_max3_f32 v249, v64, v65, v66
v_max3_f32 v251, v96, v97, v98
v_max3_f32 v249, v249, v67, v68
v_max3_f32 v251, v251, v99, v100
v_max3_f32 v249, v249, v69, v70
v_max3_f32 v251, v251, v101, v102
v_max3_f32 v249, v249, v71, v103
v_max_f32 v249, v249, v251
v_log_f32 v250, v246
s_lshl_b64 exec, 0xffff, 0
v_max_f32 v249, v249, v250
s_mov_b64 exec, -1
v_mov_b32 v251, v249
s_nop 1
v_permlane16_swap_b32 v249, v251
v_max_f32 v249, v249, v251
v_mov_b32 v251, v249
s_nop 1
v_permlane32_swap_b32 v249, v251
v_max_f32 v249, v249, v251
v_max_f32 v249, 0, v249
v_sub_f32 v250, 0, v249
EX v250,v250
v_sub_f32 v128, v128, v249
v_sub_f32 v129, v129, v249
v_sub_f32 v130, v130, v249
v_sub_f32 v131, v131, v249
v_sub_f32 v0, v0, v249
v_sub_f32 v64, v64, v249
v_sub_f32 v1, v1, v249
v_sub_f32 v65, v65, v249
v_sub_f32 v2, v2, v249
v_sub_f32 v66, v66, v249
v_sub_f32 v3, v3, v249
v_sub_f32 v67, v67, v249
v_sub_f32 v4, v4, v249
v_sub_f32 v68, v68, v249
v_sub_f32 v5, v5, v249
v_sub_f32 v69, v69, v249
v_sub_f32 v6, v6, v249
v_sub_f32 v70, v70, v249
v_sub_f32 v7, v7, v249
v_sub_f32 v71, v71, v249
v_sub_f32 v32, v32, v249
v_sub_f32 v96, v96, v249
v_sub_f32 v33, v33, v249
v_sub_f32 v97, v97, v249
v_sub_f32 v34, v34, v249
v_sub_f32 v98, v98, v249
v_sub_f32 v35, v35, v249
v_sub_f32 v99, v99, v249
v_sub_f32 v36, v36, v249
v_sub_f32 v100, v100, v249
v_sub_f32 v37, v37, v249
v_sub_f32 v101, v101, v249
v_sub_f32 v38, v38, v249
v_sub_f32 v102, v102, v249
v_sub_f32 v39, v39, v249
v_sub_f32 v103, v103, v249
s_lshl_b64 exec, 0xffff, 0
v_mul_f32 v236, v236, v250
s_mov_b64 exec, -1
AR v254,a0
v_mul_f32 v254, v254, v250
AW a0,v254
AR v251,a1
v_mul_f32 v251, v251, v250
AW a1,v251
AR v254,a2
v_mul_f32 v254, v254, v250
AW a2,v254
AR v251,a3
v_mul_f32 v251, v251, v250
AW a3,v251
AR v254,a4
v_mul_f32 v254, v254, v250
AW a4,v254
AR v251,a5
v_mul_f32 v251, v251, v250
AW a5,v251
AR v254,a6
v_mul_f32 v254, v254, v250
AW a6,v254
AR v251,a7
v_mul_f32 v251, v251, v250
AW a7,v251
AR v254,a32
v_mul_f32 v254, v254, v250
AW a32,v254
AR v251,a33
v_mul_f32 v251, v251, v250
AW a33,v251
AR v254,a34
v_mul_f32 v254, v254, v250
AW a34,v254
AR v251,a35
v_mul_f32 v251, v251, v250
AW a35,v251
AR v254,a36
v_mul_f32 v254, v254, v250
AW a36,v254
AR v251,a37
v_mul_f32 v251, v251, v250
AW a37,v251
AR v254,a38
v_mul_f32 v254, v254, v250
AW a38,v254
AR v251,a39
v_mul_f32 v251, v251, v250
AW a39,v251
AR v254,a64
v_mul_f32 v254, v254, v250
AW a64,v254
AR v251,a65
v_mul_f32 v251, v251, v250
AW a65,v251
AR v254,a66
v_mul_f32 v254, v254, v250
AW a66,v254
AR v251,a67
v_mul_f32 v251, v251, v250
AW a67,v251
AR v254,a68
v_mul_f32 v254, v254, v250
AW a68,v254
AR v251,a69
v_mul_f32 v251, v251, v250
AW a69,v251
AR v254,a70
v_mul_f32 v254, v254, v250
AW a70,v254
AR v251,a71
v_mul_f32 v251, v251, v250
AW a71,v251
AR v254,a96
v_mul_f32 v254, v254, v250
AW a96,v254
AR v251,a97
v_mul_f32 v251, v251, v250
AW a97,v251
AR v254,a98
v_mul_f32 v254, v254, v250
AW a98,v254
AR v251,a99
v_mul_f32 v251, v251, v250
AW a99,v251
AR v254,a100
v_mul_f32 v254, v254, v250
AW a100,v254
AR v251,a101
v_mul_f32 v251, v251, v250
AW a101,v251
AR v254,a102
v_mul_f32 v254, v254, v250
AW a102,v254
AR v251,a103
v_mul_f32 v251, v251, v250
AW a103,v251
v_max3_f32 v249, v72, v73, v74
v_max3_f32 v251, v104, v105, v106
v_max3_f32 v249, v249, v75, v76
v_max3_f32 v251, v251, v107, v108
v_max3_f32 v249, v249, v77, v78
v_max3_f32 v251, v251, v109, v110
v_max3_f32 v249, v249, v79, v111
v_max_f32 v249, v249, v251
v_log_f32 v250, v246
s_lshl_b64 exec, 0xffff, 16
v_max_f32 v249, v249, v250
s_mov_b64 exec, -1
v_mov_b32 v251, v249
s_nop 1
v_permlane16_swap_b32 v249, v251
v_max_f32 v249, v249, v251
v_mov_b32 v251, v249
s_nop 1
v_permlane32_swap_b32 v249, v251
v_max_f32 v249, v249, v251
v_max_f32 v249, 0, v249
v_sub_f32 v250, 0, v249
EX v250,v250
v_sub_f32 v132, v132, v249
v_sub_f32 v133, v133, v249
v_sub_f32 v134, v134, v249
v_sub_f32 v135, v135, v249
v_sub_f32 v8, v8, v249
v_sub_f32 v72, v72, v249
v_sub_f32 v9, v9, v249
v_sub_f32 v73, v73, v249
v_sub_f32 v10, v10, v249
v_sub_f32 v74, v74, v249
v_sub_f32 v11, v11, v249
v_sub_f32 v75, v75, v249
v_sub_f32 v12, v12, v249
v_sub_f32 v76, v76, v249
v_sub_f32 v13, v13, v249
v_sub_f32 v77, v77, v249
v_sub_f32 v14, v14, v249
v_sub_f32 v78, v78, v249
v_sub_f32 v15, v15, v249
v_sub_f32 v79, v79, v249
v_sub_f32 v40, v40, v249
v_sub_f32 v104, v104, v249
v_sub_f32 v41, v41, v249
v_sub_f32 v105, v105, v249
v_sub_f32 v42, v42, v249
v_sub_f32 v106, v106, v249
v_sub_f32 v43, v43, v249
v_sub_f32 v107, v107, v249
v_sub_f32 v44, v44, v249
v_sub_f32 v108, v108, v249
v_sub_f32 v45, v45, v249
v_sub_f32 v109, v109, v249
v_sub_f32 v46, v46, v249
v_sub_f32 v110, v110, v249
v_sub_f32 v47, v47, v249
v_sub_f32 v111, v111, v249
s_lshl_b64 exec, 0xffff, 16
v_mul_f32 v236, v236, v250
s_mov_b64 exec, -1
AR v254,a8
v_mul_f32 v254, v254, v250
AW a8,v254
AR v251,a9
v_mul_f32 v251, v251, v250
AW a9,v251
AR v254,a10
v_mul_f32 v254, v254, v250
AW a10,v254
AR v251,a11
v_mul_f32 v251, v251, v250
AW a11,v251
AR v254,a12
v_mul_f32 v254, v254, v250
AW a12,v254
AR v251,a13
v_mul_f32 v251, v251, v250
AW a13,v251
AR v254,a14
v_mul_f32 v254, v254, v250
AW a14,v254
AR v251,a15
v_mul_f32 v251, v251, v250
AW a15,v251
AR v254,a40
v_mul_f32 v254, v254, v250
AW a40,v254
AR v251,a41
v_mul_f32 v251, v251, v250
AW a41,v251
AR v254,a42
v_mul_f32 v254, v254, v250
AW a42,v254
AR v251,a43
v_mul_f32 v251, v251, v250
AW a43,v251
AR v254,a44
v_mul_f32 v254, v254, v250
AW a44,v254
AR v251,a45
v_mul_f32 v251, v251, v250
AW a45,v251
AR v254,a46
v_mul_f32 v254, v254, v250
AW a46,v254
AR v251,a47
v_mul_f32 v251, v251, v250
AW a47,v251
AR v254,a72
v_mul_f32 v254, v254, v250
AW a72,v254
AR v251,a73
v_mul_f32 v251, v251, v250
AW a73,v251
AR v254,a74
v_mul_f32 v254, v254, v250
AW a74,v254
AR v251,a75
v_mul_f32 v251, v251, v250
AW a75,v251
AR v254,a76
v_mul_f32 v254, v254, v250
AW a76,v254
AR v251,a77
v_mul_f32 v251, v251, v250
AW a77,v251
AR v254,a78
v_mul_f32 v254, v254, v250
AW a78,v254
AR v251,a79
v_mul_f32 v251, v251, v250
AW a79,v251
AR v254,a104
v_mul_f32 v254, v254, v250
AW a104,v254
AR v251,a105
v_mul_f32 v251, v251, v250
AW a105,v251
AR v254,a106
v_mul_f32 v254, v254, v250
AW a106,v254
AR v251,a107
v_mul_f32 v251, v251, v250
AW a107,v251
AR v254,a108
v_mul_f32 v254, v254, v250
AW a108,v254
AR v251,a109
v_mul_f32 v251, v251, v250
AW a109,v251
AR v254,a110
v_mul_f32 v254, v254, v250
AW a110,v254
AR v251,a111
v_mul_f32 v251, v251, v250
AW a111,v251
EX v64,v64
EX v65,v65
EX v66,v66
EX v67,v67
EX v68,v68
EX v69,v69
EX v70,v70
EX v71,v71
EX v72,v72
EX v73,v73
EX v74,v74
EX v75,v75
EX v76,v76
EX v77,v77
EX v78,v78
EX v79,v79
EX v96,v96
EX v97,v97
EX v98,v98
EX v99,v99
EX v100,v100
EX v101,v101
EX v102,v102
EX v103,v103
EX v104,v104
EX v105,v105
EX v106,v106
EX v107,v107
EX v108,v108
EX v109,v109
EX v110,v110
EX v111,v111
CV v64,v64,v65
CV v65,v66,v67
CV v66,v68,v69
CV v67,v70,v71
CV v72,v72,v73
CV v73,v74,v75
CV v74,v76,v77
CV v75,v78,v79
CV v68,v96,v97
CV v69,v98,v99
CV v70,v100,v101
CV v71,v102,v103
CV v76,v104,v105
CV v77,v106,v107
CV v78,v108,v109
CV v79,v110,v111
v_smfmac_f32_16x16x64_bf16 v[236:239], v[144:147], v[64:71], v234
s_nop 0
v_smfmac_f32_16x16x64_bf16 v[236:239], v[148:151], v[72:79], v234
s_nop 9
v_add_f32 v247, v236, v237
s_cmp_eq_u32 s73, 1
s_cbranch_scc1 LBACK1_%=
s_endpgm
LSITE3_%=:
s_mov_b32 s73, 3
s_mov_b32 s75, 0
s_mov_b32 s74, 49152
s_branch LRARE104_%=
LSITE5_%=:
s_mov_b32 s73, 5
s_mov_b32 s75, 32768
s_mov_b32 s74, 16384
s_branch LRARE104_%=
LSITE7_%=:
s_mov_b32 s73, 7
s_mov_b32 s75, 0
s_mov_b32 s74, 49152
s_branch LRARE104_%=
LSITE9_%=:
s_mov_b32 s73, 9
s_mov_b32 s75, 32768
s_mov_b32 s74, 16384
s_branch LRARE104_%=
LSITE11_%=:
s_mov_b32 s73, 11
s_mov_b32 s75, 0
s_mov_b32 s74, 49152
s_branch LRARE104_%=
LSITE13_%=:
s_mov_b32 s73, 13
s_mov_b32 s75, 32768
s_mov_b32 s74, 16384
s_branch LRARE104_%=
LSITE15_%=:
s_mov_b32 s73, 15
s_mov_b32 s75, 0
s_mov_b32 s74, 49152
s_branch LRARE104_%=
LSITE17_%=:
s_mov_b32 s73, 17
s_mov_b32 s75, 32768
s_mov_b32 s74, 16384
s_branch LRARE104_%=
LSITE19_%=:
s_mov_b32 s73, 19
s_mov_b32 s75, 0
s_mov_b32 s74, 49152
s_branch LRARE104_%=
LSITE21_%=:
s_mov_b32 s73, 21
s_mov_b32 s75, 32768
s_mov_b32 s74, 16384
s_branch LRARE104_%=
LSITE23_%=:
s_mov_b32 s73, 23
s_mov_b32 s75, 0
s_mov_b32 s74, 49152
s_branch LRARE104_%=
LSITE25_%=:
s_mov_b32 s73, 25
s_mov_b32 s75, 32768
s_mov_b32 s74, 16384
s_branch LRARE104_%=
LSITE27_%=:
s_mov_b32 s73, 27
s_mov_b32 s75, 0
s_mov_b32 s74, 49152
s_branch LRARE104_%=
LSITE29_%=:
s_mov_b32 s73, 29
s_mov_b32 s75, 32768
s_mov_b32 s74, 16384
s_branch LRARE104_%=
LSITE31_%=:
s_mov_b32 s73, 31
s_mov_b32 s75, 0
s_mov_b32 s74, 49152
s_branch LRARE104_%=
LRARE104_%=:
s_nop 15
s_waitcnt vmcnt(0) lgkmcnt(0)
v_add_u32 v249, s74, v224
RD a[192:195],v249 offset:0
RD a[208:211],v249 offset:4096
RD a[224:227],v249 offset:8192
RD a[240:243],v249 offset:12288
v_add_u32 v250, s74, v225
RD a[196:199],v250 offset:0
RD a[212:215],v250 offset:4096
RD a[228:231],v250 offset:8192
RD a[244:247],v250 offset:12288
v_add_u32 v251, s74, v226
RD a[200:203],v251 offset:0
RD a[216:219],v251 offset:4096
RD a[232:235],v251 offset:8192
RD a[248:251],v251 offset:12288
v_add_u32 v254, s74, v227
RD a[204:207],v254 offset:0
RD a[220:223],v254 offset:4096
RD a[236:239],v254 offset:8192
RD a[252:255],v254 offset:12288
s_waitcnt lgkmcnt(0)
MM v[64:67],a[192:195],v[160:163],v[128:131]
MM v[64:67],a[196:199],v[176:179],v[64:67]
MM v[64:67],a[200:203],v[192:195],v[64:67]
MM v[64:67],a[204:207],v[208:211],v[64:67]
MM v[68:71],a[220:223],v[208:211],v[128:131]
MM v[68:71],a[216:219],v[192:195],v[68:71]
MM v[68:71],a[212:215],v[176:179],v[68:71]
MM v[68:71],a[208:211],v[160:163],v[68:71]
MM v[76:79],a[208:211],v[164:167],v[132:135]
MM v[76:79],a[212:215],v[180:183],v[76:79]
MM v[76:79],a[216:219],v[196:199],v[76:79]
MM v[76:79],a[220:223],v[212:215],v[76:79]
MM v[72:75],a[204:207],v[212:215],v[132:135]
MM v[72:75],a[200:203],v[196:199],v[72:75]
MM v[72:75],a[196:199],v[180:183],v[72:75]
MM v[72:75],a[192:195],v[164:167],v[72:75]
MM v[80:83],a[192:195],v[168:171],v[136:139]
MM v[80:83],a[196:199],v[184:187],v[80:83]
MM v[80:83],a[200:203],v[200:203],v[80:83]
MM v[80:83],a[204:207],v[216:219],v[80:83]
MM v[84:87],a[220:223],v[216:219],v[136:139]
MM v[84:87],a[216:219],v[200:203],v[84:87]
MM v[84:87],a[212:215],v[184:187],v[84:87]
MM v[84:87],a[208:211],v[168:171],v[84:87]
MM v[92:95],a[208:211],v[172:175],v[140:143]
)ASM"
R"ASM(
MM v[92:95],a[212:215],v[188:191],v[92:95]
MM v[92:95],a[216:219],v[204:207],v[92:95]
MM v[92:95],a[220:223],v[220:223],v[92:95]
MM v[88:91],a[204:207],v[220:223],v[140:143]
MM v[88:91],a[200:203],v[204:207],v[88:91]
MM v[88:91],a[196:199],v[188:191],v[88:91]
MM v[88:91],a[192:195],v[172:175],v[88:91]
MM v[96:99],a[224:227],v[160:163],v[128:131]
MM v[96:99],a[228:231],v[176:179],v[96:99]
MM v[96:99],a[232:235],v[192:195],v[96:99]
MM v[96:99],a[236:239],v[208:211],v[96:99]
MM v[100:103],a[252:255],v[208:211],v[128:131]
MM v[100:103],a[248:251],v[192:195],v[100:103]
MM v[100:103],a[244:247],v[176:179],v[100:103]
MM v[100:103],a[240:243],v[160:163],v[100:103]
MM v[108:111],a[240:243],v[164:167],v[132:135]
MM v[108:111],a[244:247],v[180:183],v[108:111]
MM v[108:111],a[248:251],v[196:199],v[108:111]
MM v[108:111],a[252:255],v[212:215],v[108:111]
MM v[104:107],a[236:239],v[212:215],v[132:135]
MM v[104:107],a[232:235],v[196:199],v[104:107]
MM v[104:107],a[228:231],v[180:183],v[104:107]
MM v[104:107],a[224:227],v[164:167],v[104:107]
MM v[112:115],a[224:227],v[168:171],v[136:139]
MM v[112:115],a[228:231],v[184:187],v[112:115]
MM v[112:115],a[232:235],v[200:203],v[112:115]
MM v[112:115],a[236:239],v[216:219],v[112:115]
MM v[116:119],a[252:255],v[216:219],v[136:139]
MM v[116:119],a[248:251],v[200:203],v[116:119]
MM v[116:119],a[244:247],v[184:187],v[116:119]
MM v[116:119],a[240:243],v[168:171],v[116:119]
MM v[124:127],a[240:243],v[172:175],v[140:143]
MM v[124:127],a[244:247],v[188:191],v[124:127]
MM v[124:127],a[248:251],v[204:207],v[124:127]
MM v[124:127],a[252:255],v[220:223],v[124:127]
MM v[120:123],a[236:239],v[220:223],v[140:143]
MM v[120:123],a[232:235],v[204:207],v[120:123]
MM v[120:123],a[228:231],v[188:191],v[120:123]
MM v[120:123],a[224:227],v[172:175],v[120:123]
s_nop 15
v_add_u32 v249, s75, v224
RD a[192:195],v249 offset:0
RD a[208:211],v249 offset:4096
RD a[224:227],v249 offset:8192
RD a[240:243],v249 offset:12288
v_add_u32 v250, s75, v225
RD a[196:199],v250 offset:0
RD a[212:215],v250 offset:4096
RD a[228:231],v250 offset:8192
RD a[244:247],v250 offset:12288
v_add_u32 v251, s75, v226
RD a[200:203],v251 offset:0
RD a[216:219],v251 offset:4096
RD a[232:235],v251 offset:8192
RD a[248:251],v251 offset:12288
v_add_u32 v254, s75, v227
RD a[204:207],v254 offset:0
RD a[220:223],v254 offset:4096
RD a[236:239],v254 offset:8192
RD a[252:255],v254 offset:12288
s_waitcnt lgkmcnt(0)
v_mov_b32 v236, v246
v_mov_b32 v237, 0
v_max3_f32 v249, v64, v65, v66
v_max3_f32 v251, v96, v97, v98
v_max3_f32 v249, v249, v67, v68
v_max3_f32 v251, v251, v99, v100
v_max3_f32 v249, v249, v69, v70
v_max3_f32 v251, v251, v101, v102
v_max3_f32 v249, v249, v71, v103
v_max_f32 v249, v249, v251
v_log_f32 v250, v246
s_lshl_b64 exec, 0xffff, 0
v_max_f32 v249, v249, v250
s_mov_b64 exec, -1
v_mov_b32 v251, v249
s_nop 1
v_permlane16_swap_b32 v249, v251
v_max_f32 v249, v249, v251
v_mov_b32 v251, v249
s_nop 1
v_permlane32_swap_b32 v249, v251
v_max_f32 v249, v249, v251
v_max_f32 v249, 0, v249
v_sub_f32 v250, 0, v249
EX v250,v250
v_sub_f32 v128, v128, v249
v_sub_f32 v129, v129, v249
v_sub_f32 v130, v130, v249
v_sub_f32 v131, v131, v249
v_sub_f32 v0, v0, v249
v_sub_f32 v64, v64, v249
v_sub_f32 v1, v1, v249
v_sub_f32 v65, v65, v249
v_sub_f32 v2, v2, v249
v_sub_f32 v66, v66, v249
v_sub_f32 v3, v3, v249
v_sub_f32 v67, v67, v249
v_sub_f32 v4, v4, v249
v_sub_f32 v68, v68, v249
v_sub_f32 v5, v5, v249
v_sub_f32 v69, v69, v249
v_sub_f32 v6, v6, v249
v_sub_f32 v70, v70, v249
v_sub_f32 v7, v7, v249
v_sub_f32 v71, v71, v249
v_sub_f32 v32, v32, v249
v_sub_f32 v96, v96, v249
v_sub_f32 v33, v33, v249
v_sub_f32 v97, v97, v249
v_sub_f32 v34, v34, v249
v_sub_f32 v98, v98, v249
v_sub_f32 v35, v35, v249
v_sub_f32 v99, v99, v249
v_sub_f32 v36, v36, v249
v_sub_f32 v100, v100, v249
v_sub_f32 v37, v37, v249
v_sub_f32 v101, v101, v249
v_sub_f32 v38, v38, v249
v_sub_f32 v102, v102, v249
v_sub_f32 v39, v39, v249
v_sub_f32 v103, v103, v249
s_lshl_b64 exec, 0xffff, 0
v_mul_f32 v236, v236, v250
s_mov_b64 exec, -1
AR v254,a0
v_mul_f32 v254, v254, v250
AW a0,v254
AR v251,a1
v_mul_f32 v251, v251, v250
AW a1,v251
AR v254,a2
v_mul_f32 v254, v254, v250
AW a2,v254
AR v251,a3
v_mul_f32 v251, v251, v250
AW a3,v251
AR v254,a4
v_mul_f32 v254, v254, v250
AW a4,v254
AR v251,a5
v_mul_f32 v251, v251, v250
AW a5,v251
AR v254,a6
v_mul_f32 v254, v254, v250
AW a6,v254
AR v251,a7
v_mul_f32 v251, v251, v250
AW a7,v251
AR v254,a32
v_mul_f32 v254, v254, v250
AW a32,v254
AR v251,a33
v_mul_f32 v251, v251, v250
AW a33,v251
AR v254,a34
v_mul_f32 v254, v254, v250
AW a34,v254
AR v251,a35
v_mul_f32 v251, v251, v250
AW a35,v251
AR v254,a36
v_mul_f32 v254, v254, v250
AW a36,v254
AR v251,a37
v_mul_f32 v251, v251, v250
AW a37,v251
AR v254,a38
v_mul_f32 v254, v254, v250
AW a38,v254
AR v251,a39
v_mul_f32 v251, v251, v250
AW a39,v251
AR v254,a64
v_mul_f32 v254, v254, v250
AW a64,v254
AR v251,a65
v_mul_f32 v251, v251, v250
AW a65,v251
AR v254,a66
v_mul_f32 v254, v254, v250
AW a66,v254
AR v251,a67
v_mul_f32 v251, v251, v250
AW a67,v251
AR v254,a68
v_mul_f32 v254, v254, v250
AW a68,v254
AR v251,a69
v_mul_f32 v251, v251, v250
AW a69,v251
AR v254,a70
v_mul_f32 v254, v254, v250
AW a70,v254
AR v251,a71
v_mul_f32 v251, v251, v250
AW a71,v251
AR v254,a96
v_mul_f32 v254, v254, v250
AW a96,v254
AR v251,a97
v_mul_f32 v251, v251, v250
AW a97,v251
AR v254,a98
v_mul_f32 v254, v254, v250
AW a98,v254
AR v251,a99
v_mul_f32 v251, v251, v250
AW a99,v251
AR v254,a100
v_mul_f32 v254, v254, v250
AW a100,v254
AR v251,a101
v_mul_f32 v251, v251, v250
AW a101,v251
AR v254,a102
v_mul_f32 v254, v254, v250
AW a102,v254
AR v251,a103
v_mul_f32 v251, v251, v250
AW a103,v251
v_max3_f32 v249, v72, v73, v74
v_max3_f32 v251, v104, v105, v106
v_max3_f32 v249, v249, v75, v76
v_max3_f32 v251, v251, v107, v108
v_max3_f32 v249, v249, v77, v78
v_max3_f32 v251, v251, v109, v110
v_max3_f32 v249, v249, v79, v111
v_max_f32 v249, v249, v251
v_log_f32 v250, v246
s_lshl_b64 exec, 0xffff, 16
v_max_f32 v249, v249, v250
s_mov_b64 exec, -1
v_mov_b32 v251, v249
s_nop 1
v_permlane16_swap_b32 v249, v251
v_max_f32 v249, v249, v251
v_mov_b32 v251, v249
s_nop 1
v_permlane32_swap_b32 v249, v251
v_max_f32 v249, v249, v251
v_max_f32 v249, 0, v249
v_sub_f32 v250, 0, v249
EX v250,v250
v_sub_f32 v132, v132, v249
v_sub_f32 v133, v133, v249
v_sub_f32 v134, v134, v249
v_sub_f32 v135, v135, v249
v_sub_f32 v8, v8, v249
v_sub_f32 v72, v72, v249
v_sub_f32 v9, v9, v249
v_sub_f32 v73, v73, v249
v_sub_f32 v10, v10, v249
v_sub_f32 v74, v74, v249
v_sub_f32 v11, v11, v249
v_sub_f32 v75, v75, v249
v_sub_f32 v12, v12, v249
v_sub_f32 v76, v76, v249
v_sub_f32 v13, v13, v249
v_sub_f32 v77, v77, v249
v_sub_f32 v14, v14, v249
v_sub_f32 v78, v78, v249
v_sub_f32 v15, v15, v249
v_sub_f32 v79, v79, v249
v_sub_f32 v40, v40, v249
v_sub_f32 v104, v104, v249
v_sub_f32 v41, v41, v249
v_sub_f32 v105, v105, v249
v_sub_f32 v42, v42, v249
v_sub_f32 v106, v106, v249
v_sub_f32 v43, v43, v249
v_sub_f32 v107, v107, v249
v_sub_f32 v44, v44, v249
v_sub_f32 v108, v108, v249
v_sub_f32 v45, v45, v249
v_sub_f32 v109, v109, v249
v_sub_f32 v46, v46, v249
v_sub_f32 v110, v110, v249
v_sub_f32 v47, v47, v249
v_sub_f32 v111, v111, v249
s_lshl_b64 exec, 0xffff, 16
v_mul_f32 v236, v236, v250
s_mov_b64 exec, -1
AR v254,a8
v_mul_f32 v254, v254, v250
AW a8,v254
AR v251,a9
v_mul_f32 v251, v251, v250
AW a9,v251
AR v254,a10
v_mul_f32 v254, v254, v250
AW a10,v254
AR v251,a11
v_mul_f32 v251, v251, v250
AW a11,v251
AR v254,a12
v_mul_f32 v254, v254, v250
AW a12,v254
AR v251,a13
v_mul_f32 v251, v251, v250
AW a13,v251
AR v254,a14
v_mul_f32 v254, v254, v250
AW a14,v254
AR v251,a15
v_mul_f32 v251, v251, v250
AW a15,v251
AR v254,a40
v_mul_f32 v254, v254, v250
AW a40,v254
AR v251,a41
v_mul_f32 v251, v251, v250
AW a41,v251
AR v254,a42
v_mul_f32 v254, v254, v250
AW a42,v254
AR v251,a43
v_mul_f32 v251, v251, v250
AW a43,v251
AR v254,a44
v_mul_f32 v254, v254, v250
AW a44,v254
AR v251,a45
v_mul_f32 v251, v251, v250
AW a45,v251
AR v254,a46
v_mul_f32 v254, v254, v250
AW a46,v254
AR v251,a47
v_mul_f32 v251, v251, v250
AW a47,v251
AR v254,a72
v_mul_f32 v254, v254, v250
AW a72,v254
AR v251,a73
v_mul_f32 v251, v251, v250
AW a73,v251
AR v254,a74
v_mul_f32 v254, v254, v250
AW a74,v254
AR v251,a75
v_mul_f32 v251, v251, v250
AW a75,v251
AR v254,a76
v_mul_f32 v254, v254, v250
AW a76,v254
AR v251,a77
v_mul_f32 v251, v251, v250
AW a77,v251
AR v254,a78
v_mul_f32 v254, v254, v250
AW a78,v254
AR v251,a79
v_mul_f32 v251, v251, v250
AW a79,v251
AR v254,a104
v_mul_f32 v254, v254, v250
AW a104,v254
AR v251,a105
v_mul_f32 v251, v251, v250
AW a105,v251
AR v254,a106
v_mul_f32 v254, v254, v250
AW a106,v254
AR v251,a107
v_mul_f32 v251, v251, v250
AW a107,v251
AR v254,a108
v_mul_f32 v254, v254, v250
AW a108,v254
AR v251,a109
v_mul_f32 v251, v251, v250
AW a109,v251
AR v254,a110
v_mul_f32 v254, v254, v250
AW a110,v254
AR v251,a111
v_mul_f32 v251, v251, v250
AW a111,v251
v_max3_f32 v249, v80, v81, v82
v_max3_f32 v251, v112, v113, v114
v_max3_f32 v249, v249, v83, v84
v_max3_f32 v251, v251, v115, v116
v_max3_f32 v249, v249, v85, v86
v_max3_f32 v251, v251, v117, v118
v_max3_f32 v249, v249, v87, v119
v_max_f32 v249, v249, v251
v_log_f32 v250, v246
s_lshl_b64 exec, 0xffff, 32
v_max_f32 v249, v249, v250
s_mov_b64 exec, -1
v_mov_b32 v251, v249
s_nop 1
v_permlane16_swap_b32 v249, v251
v_max_f32 v249, v249, v251
v_mov_b32 v251, v249
s_nop 1
v_permlane32_swap_b32 v249, v251
v_max_f32 v249, v249, v251
v_max_f32 v249, 0, v249
v_sub_f32 v250, 0, v249
EX v250,v250
v_sub_f32 v136, v136, v249
v_sub_f32 v137, v137, v249
v_sub_f32 v138, v138, v249
v_sub_f32 v139, v139, v249
v_sub_f32 v16, v16, v249
v_sub_f32 v80, v80, v249
v_sub_f32 v17, v17, v249
v_sub_f32 v81, v81, v249
v_sub_f32 v18, v18, v249
v_sub_f32 v82, v82, v249
v_sub_f32 v19, v19, v249
v_sub_f32 v83, v83, v249
v_sub_f32 v20, v20, v249
v_sub_f32 v84, v84, v249
v_sub_f32 v21, v21, v249
v_sub_f32 v85, v85, v249
v_sub_f32 v22, v22, v249
v_sub_f32 v86, v86, v249
v_sub_f32 v23, v23, v249
v_sub_f32 v87, v87, v249
v_sub_f32 v48, v48, v249
v_sub_f32 v112, v112, v249
v_sub_f32 v49, v49, v249
v_sub_f32 v113, v113, v249
v_sub_f32 v50, v50, v249
v_sub_f32 v114, v114, v249
v_sub_f32 v51, v51, v249
v_sub_f32 v115, v115, v249
v_sub_f32 v52, v52, v249
v_sub_f32 v116, v116, v249
v_sub_f32 v53, v53, v249
v_sub_f32 v117, v117, v249
v_sub_f32 v54, v54, v249
v_sub_f32 v118, v118, v249
v_sub_f32 v55, v55, v249
v_sub_f32 v119, v119, v249
s_lshl_b64 exec, 0xffff, 32
v_mul_f32 v236, v236, v250
s_mov_b64 exec, -1
AR v254,a16
v_mul_f32 v254, v254, v250
AW a16,v254
AR v251,a17
v_mul_f32 v251, v251, v250
AW a17,v251
AR v254,a18
v_mul_f32 v254, v254, v250
AW a18,v254
AR v251,a19
v_mul_f32 v251, v251, v250
AW a19,v251
AR v254,a20
v_mul_f32 v254, v254, v250
AW a20,v254
AR v251,a21
v_mul_f32 v251, v251, v250
AW a21,v251
AR v254,a22
v_mul_f32 v254, v254, v250
AW a22,v254
AR v251,a23
v_mul_f32 v251, v251, v250
AW a23,v251
AR v254,a48
v_mul_f32 v254, v254, v250
AW a48,v254
AR v251,a49
v_mul_f32 v251, v251, v250
AW a49,v251
AR v254,a50
v_mul_f32 v254, v254, v250
AW a50,v254
AR v251,a51
v_mul_f32 v251, v251, v250
AW a51,v251
AR v254,a52
v_mul_f32 v254, v254, v250
AW a52,v254
AR v251,a53
v_mul_f32 v251, v251, v250
AW a53,v251
AR v254,a54
v_mul_f32 v254, v254, v250
AW a54,v254
AR v251,a55
v_mul_f32 v251, v251, v250
AW a55,v251
AR v254,a80
v_mul_f32 v254, v254, v250
AW a80,v254
AR v251,a81
v_mul_f32 v251, v251, v250
AW a81,v251
AR v254,a82
v_mul_f32 v254, v254, v250
AW a82,v254
AR v251,a83
v_mul_f32 v251, v251, v250
AW a83,v251
AR v254,a84
v_mul_f32 v254, v254, v250
AW a84,v254
AR v251,a85
v_mul_f32 v251, v251, v250
AW a85,v251
AR v254,a86
v_mul_f32 v254, v254, v250
AW a86,v254
AR v251,a87
v_mul_f32 v251, v251, v250
AW a87,v251
AR v254,a112
v_mul_f32 v254, v254, v250
AW a112,v254
AR v251,a113
v_mul_f32 v251, v251, v250
AW a113,v251
AR v254,a114
v_mul_f32 v254, v254, v250
AW a114,v254
AR v251,a115
v_mul_f32 v251, v251, v250
AW a115,v251
AR v254,a116
v_mul_f32 v254, v254, v250
AW a116,v254
AR v251,a117
v_mul_f32 v251, v251, v250
AW a117,v251
AR v254,a118
)ASM"
R"ASM(
v_mul_f32 v254, v254, v250
AW a118,v254
AR v251,a119
v_mul_f32 v251, v251, v250
AW a119,v251
v_max3_f32 v249, v88, v89, v90
v_max3_f32 v251, v120, v121, v122
v_max3_f32 v249, v249, v91, v92
v_max3_f32 v251, v251, v123, v124
v_max3_f32 v249, v249, v93, v94
v_max3_f32 v251, v251, v125, v126
v_max3_f32 v249, v249, v95, v127
v_max_f32 v249, v249, v251
v_log_f32 v250, v246
s_lshl_b64 exec, 0xffff, 48
v_max_f32 v249, v249, v250
s_mov_b64 exec, -1
v_mov_b32 v251, v249
s_nop 1
v_permlane16_swap_b32 v249, v251
v_max_f32 v249, v249, v251
v_mov_b32 v251, v249
s_nop 1
v_permlane32_swap_b32 v249, v251
v_max_f32 v249, v249, v251
v_max_f32 v249, 0, v249
v_sub_f32 v250, 0, v249
EX v250,v250
v_sub_f32 v140, v140, v249
v_sub_f32 v141, v141, v249
v_sub_f32 v142, v142, v249
v_sub_f32 v143, v143, v249
v_sub_f32 v24, v24, v249
v_sub_f32 v88, v88, v249
v_sub_f32 v25, v25, v249
v_sub_f32 v89, v89, v249
v_sub_f32 v26, v26, v249
v_sub_f32 v90, v90, v249
v_sub_f32 v27, v27, v249
v_sub_f32 v91, v91, v249
v_sub_f32 v28, v28, v249
v_sub_f32 v92, v92, v249
v_sub_f32 v29, v29, v249
v_sub_f32 v93, v93, v249
v_sub_f32 v30, v30, v249
v_sub_f32 v94, v94, v249
v_sub_f32 v31, v31, v249
v_sub_f32 v95, v95, v249
v_sub_f32 v56, v56, v249
v_sub_f32 v120, v120, v249
v_sub_f32 v57, v57, v249
v_sub_f32 v121, v121, v249
v_sub_f32 v58, v58, v249
v_sub_f32 v122, v122, v249
v_sub_f32 v59, v59, v249
v_sub_f32 v123, v123, v249
v_sub_f32 v60, v60, v249
v_sub_f32 v124, v124, v249
v_sub_f32 v61, v61, v249
v_sub_f32 v125, v125, v249
v_sub_f32 v62, v62, v249
v_sub_f32 v126, v126, v249
v_sub_f32 v63, v63, v249
v_sub_f32 v127, v127, v249
s_lshl_b64 exec, 0xffff, 48
v_mul_f32 v236, v236, v250
s_mov_b64 exec, -1
AR v254,a24
v_mul_f32 v254, v254, v250
AW a24,v254
AR v251,a25
v_mul_f32 v251, v251, v250
AW a25,v251
AR v254,a26
v_mul_f32 v254, v254, v250
AW a26,v254
AR v251,a27
v_mul_f32 v251, v251, v250
AW a27,v251
AR v254,a28
v_mul_f32 v254, v254, v250
AW a28,v254
AR v251,a29
v_mul_f32 v251, v251, v250
AW a29,v251
AR v254,a30
v_mul_f32 v254, v254, v250
AW a30,v254
AR v251,a31
v_mul_f32 v251, v251, v250
AW a31,v251
AR v254,a56
v_mul_f32 v254, v254, v250
AW a56,v254
AR v251,a57
v_mul_f32 v251, v251, v250
AW a57,v251
AR v254,a58
v_mul_f32 v254, v254, v250
AW a58,v254
AR v251,a59
v_mul_f32 v251, v251, v250
AW a59,v251
AR v254,a60
v_mul_f32 v254, v254, v250
AW a60,v254
AR v251,a61
v_mul_f32 v251, v251, v250
AW a61,v251
AR v254,a62
v_mul_f32 v254, v254, v250
AW a62,v254
AR v251,a63
v_mul_f32 v251, v251, v250
AW a63,v251
AR v254,a88
v_mul_f32 v254, v254, v250
AW a88,v254
AR v251,a89
v_mul_f32 v251, v251, v250
AW a89,v251
AR v254,a90
v_mul_f32 v254, v254, v250
AW a90,v254
AR v251,a91
v_mul_f32 v251, v251, v250
AW a91,v251
AR v254,a92
v_mul_f32 v254, v254, v250
AW a92,v254
AR v251,a93
v_mul_f32 v251, v251, v250
AW a93,v251
AR v254,a94
v_mul_f32 v254, v254, v250
AW a94,v254
AR v251,a95
v_mul_f32 v251, v251, v250
AW a95,v251
AR v254,a120
v_mul_f32 v254, v254, v250
AW a120,v254
AR v251,a121
v_mul_f32 v251, v251, v250
AW a121,v251
AR v254,a122
v_mul_f32 v254, v254, v250
AW a122,v254
AR v251,a123
v_mul_f32 v251, v251, v250
AW a123,v251
AR v254,a124
v_mul_f32 v254, v254, v250
AW a124,v254
AR v251,a125
v_mul_f32 v251, v251, v250
AW a125,v251
AR v254,a126
v_mul_f32 v254, v254, v250
AW a126,v254
AR v251,a127
v_mul_f32 v251, v251, v250
AW a127,v251
EX v64,v64
EX v65,v65
EX v66,v66
EX v67,v67
EX v68,v68
EX v69,v69
EX v70,v70
EX v71,v71
EX v72,v72
EX v73,v73
EX v74,v74
EX v75,v75
EX v76,v76
EX v77,v77
EX v78,v78
EX v79,v79
EX v80,v80
EX v81,v81
EX v82,v82
EX v83,v83
EX v84,v84
EX v85,v85
EX v86,v86
EX v87,v87
EX v88,v88
EX v89,v89
EX v90,v90
EX v91,v91
EX v92,v92
EX v93,v93
EX v94,v94
EX v95,v95
EX v96,v96
EX v97,v97
EX v98,v98
EX v99,v99
EX v100,v100
EX v101,v101
EX v102,v102
EX v103,v103
EX v104,v104
EX v105,v105
EX v106,v106
EX v107,v107
EX v108,v108
EX v109,v109
EX v110,v110
EX v111,v111
EX v112,v112
EX v113,v113
EX v114,v114
EX v115,v115
EX v116,v116
EX v117,v117
EX v118,v118
EX v119,v119
EX v120,v120
EX v121,v121
EX v122,v122
EX v123,v123
EX v124,v124
EX v125,v125
EX v126,v126
EX v127,v127
CV v64,v64,v65
CV v65,v66,v67
CV v66,v68,v69
CV v67,v70,v71
CV v72,v72,v73
CV v73,v74,v75
CV v74,v76,v77
CV v75,v78,v79
CV v80,v80,v81
CV v81,v82,v83
CV v82,v84,v85
CV v83,v86,v87
CV v88,v88,v89
CV v89,v90,v91
CV v90,v92,v93
CV v91,v94,v95
CV v68,v96,v97
CV v69,v98,v99
CV v70,v100,v101
CV v71,v102,v103
CV v76,v104,v105
CV v77,v106,v107
CV v78,v108,v109
CV v79,v110,v111
CV v84,v112,v113
CV v85,v114,v115
CV v86,v116,v117
CV v87,v118,v119
CV v92,v120,v121
CV v93,v122,v123
CV v94,v124,v125
CV v95,v126,v127
v_smfmac_f32_16x16x64_bf16 v[236:239], v[144:147], v[64:71], v234
v_smfmac_f32_16x16x64_bf16 v[236:239], v[148:151], v[72:79], v234
v_smfmac_f32_16x16x64_bf16 v[236:239], v[152:155], v[80:87], v234
v_smfmac_f32_16x16x64_bf16 v[236:239], v[156:159], v[88:95], v234
s_nop 9
v_add_f32 v247, v236, v237
s_cmp_eq_u32 s73, 3
s_cbranch_scc1 LBACK3_%=
s_cmp_eq_u32 s73, 5
s_cbranch_scc1 LBACK5_%=
s_cmp_eq_u32 s73, 7
s_cbranch_scc1 LBACK7_%=
s_cmp_eq_u32 s73, 9
s_cbranch_scc1 LBACK9_%=
s_cmp_eq_u32 s73, 11
s_cbranch_scc1 LBACK11_%=
s_cmp_eq_u32 s73, 13
s_cbranch_scc1 LBACK13_%=
s_cmp_eq_u32 s73, 15
s_cbranch_scc1 LBACK15_%=
s_cmp_eq_u32 s73, 17
s_cbranch_scc1 LBACK17_%=
s_cmp_eq_u32 s73, 19
s_cbranch_scc1 LBACK19_%=
s_cmp_eq_u32 s73, 21
s_cbranch_scc1 LBACK21_%=
s_cmp_eq_u32 s73, 23
s_cbranch_scc1 LBACK23_%=
s_cmp_eq_u32 s73, 25
s_cbranch_scc1 LBACK25_%=
s_cmp_eq_u32 s73, 27
s_cbranch_scc1 LBACK27_%=
s_cmp_eq_u32 s73, 29
s_cbranch_scc1 LBACK29_%=
s_cmp_eq_u32 s73, 31
s_cbranch_scc1 LBACK31_%=
s_endpgm
LSITE4_%=:
s_mov_b32 s73, 4
s_mov_b32 s75, 16384
s_mov_b32 s74, 0
s_branch LRARE004_%=
LSITE6_%=:
s_mov_b32 s73, 6
s_mov_b32 s75, 49152
s_mov_b32 s74, 32768
s_branch LRARE004_%=
LSITE8_%=:
s_mov_b32 s73, 8
s_mov_b32 s75, 16384
s_mov_b32 s74, 0
s_branch LRARE004_%=
LSITE10_%=:
s_mov_b32 s73, 10
s_mov_b32 s75, 49152
s_mov_b32 s74, 32768
s_branch LRARE004_%=
LSITE12_%=:
s_mov_b32 s73, 12
s_mov_b32 s75, 16384
s_mov_b32 s74, 0
s_branch LRARE004_%=
LSITE14_%=:
s_mov_b32 s73, 14
s_mov_b32 s75, 49152
s_mov_b32 s74, 32768
s_branch LRARE004_%=
LSITE16_%=:
s_mov_b32 s73, 16
s_mov_b32 s75, 16384
s_mov_b32 s74, 0
s_branch LRARE004_%=
LSITE18_%=:
s_mov_b32 s73, 18
s_mov_b32 s75, 49152
s_mov_b32 s74, 32768
s_branch LRARE004_%=
LSITE20_%=:
s_mov_b32 s73, 20
s_mov_b32 s75, 16384
s_mov_b32 s74, 0
s_branch LRARE004_%=
LSITE22_%=:
s_mov_b32 s73, 22
s_mov_b32 s75, 49152
s_mov_b32 s74, 32768
s_branch LRARE004_%=
LSITE24_%=:
s_mov_b32 s73, 24
s_mov_b32 s75, 16384
s_mov_b32 s74, 0
s_branch LRARE004_%=
LSITE26_%=:
s_mov_b32 s73, 26
s_mov_b32 s75, 49152
s_mov_b32 s74, 32768
s_branch LRARE004_%=
LSITE28_%=:
s_mov_b32 s73, 28
s_mov_b32 s75, 16384
s_mov_b32 s74, 0
s_branch LRARE004_%=
LSITE30_%=:
s_mov_b32 s73, 30
s_mov_b32 s75, 49152
s_mov_b32 s74, 32768
s_branch LRARE004_%=
LRARE004_%=:
s_nop 15
s_waitcnt vmcnt(0) lgkmcnt(0)
v_add_u32 v249, s74, v224
RD a[192:195],v249 offset:0
RD a[208:211],v249 offset:4096
RD a[224:227],v249 offset:8192
RD a[240:243],v249 offset:12288
v_add_u32 v250, s74, v225
RD a[196:199],v250 offset:0
RD a[212:215],v250 offset:4096
RD a[228:231],v250 offset:8192
RD a[244:247],v250 offset:12288
v_add_u32 v251, s74, v226
RD a[200:203],v251 offset:0
RD a[216:219],v251 offset:4096
RD a[232:235],v251 offset:8192
RD a[248:251],v251 offset:12288
v_add_u32 v254, s74, v227
RD a[204:207],v254 offset:0
RD a[220:223],v254 offset:4096
RD a[236:239],v254 offset:8192
RD a[252:255],v254 offset:12288
s_waitcnt lgkmcnt(0)
MM v[0:3],a[192:195],v[160:163],v[128:131]
MM v[0:3],a[196:199],v[176:179],v[0:3]
MM v[0:3],a[200:203],v[192:195],v[0:3]
MM v[0:3],a[204:207],v[208:211],v[0:3]
MM v[4:7],a[220:223],v[208:211],v[128:131]
MM v[4:7],a[216:219],v[192:195],v[4:7]
MM v[4:7],a[212:215],v[176:179],v[4:7]
MM v[4:7],a[208:211],v[160:163],v[4:7]
MM v[12:15],a[208:211],v[164:167],v[132:135]
MM v[12:15],a[212:215],v[180:183],v[12:15]
MM v[12:15],a[216:219],v[196:199],v[12:15]
MM v[12:15],a[220:223],v[212:215],v[12:15]
MM v[8:11],a[204:207],v[212:215],v[132:135]
MM v[8:11],a[200:203],v[196:199],v[8:11]
MM v[8:11],a[196:199],v[180:183],v[8:11]
MM v[8:11],a[192:195],v[164:167],v[8:11]
MM v[16:19],a[192:195],v[168:171],v[136:139]
MM v[16:19],a[196:199],v[184:187],v[16:19]
MM v[16:19],a[200:203],v[200:203],v[16:19]
MM v[16:19],a[204:207],v[216:219],v[16:19]
MM v[20:23],a[220:223],v[216:219],v[136:139]
MM v[20:23],a[216:219],v[200:203],v[20:23]
MM v[20:23],a[212:215],v[184:187],v[20:23]
MM v[20:23],a[208:211],v[168:171],v[20:23]
MM v[28:31],a[208:211],v[172:175],v[140:143]
MM v[28:31],a[212:215],v[188:191],v[28:31]
MM v[28:31],a[216:219],v[204:207],v[28:31]
MM v[28:31],a[220:223],v[220:223],v[28:31]
MM v[24:27],a[204:207],v[220:223],v[140:143]
MM v[24:27],a[200:203],v[204:207],v[24:27]
MM v[24:27],a[196:199],v[188:191],v[24:27]
MM v[24:27],a[192:195],v[172:175],v[24:27]
MM v[32:35],a[224:227],v[160:163],v[128:131]
MM v[32:35],a[228:231],v[176:179],v[32:35]
MM v[32:35],a[232:235],v[192:195],v[32:35]
MM v[32:35],a[236:239],v[208:211],v[32:35]
MM v[36:39],a[252:255],v[208:211],v[128:131]
MM v[36:39],a[248:251],v[192:195],v[36:39]
MM v[36:39],a[244:247],v[176:179],v[36:39]
MM v[36:39],a[240:243],v[160:163],v[36:39]
MM v[44:47],a[240:243],v[164:167],v[132:135]
MM v[44:47],a[244:247],v[180:183],v[44:47]
MM v[44:47],a[248:251],v[196:199],v[44:47]
MM v[44:47],a[252:255],v[212:215],v[44:47]
MM v[40:43],a[236:239],v[212:215],v[132:135]
MM v[40:43],a[232:235],v[196:199],v[40:43]
MM v[40:43],a[228:231],v[180:183],v[40:43]
MM v[40:43],a[224:227],v[164:167],v[40:43]
MM v[48:51],a[224:227],v[168:171],v[136:139]
MM v[48:51],a[228:231],v[184:187],v[48:51]
MM v[48:51],a[232:235],v[200:203],v[48:51]
MM v[48:51],a[236:239],v[216:219],v[48:51]
MM v[52:55],a[252:255],v[216:219],v[136:139]
MM v[52:55],a[248:251],v[200:203],v[52:55]
MM v[52:55],a[244:247],v[184:187],v[52:55]
MM v[52:55],a[240:243],v[168:171],v[52:55]
MM v[60:63],a[240:243],v[172:175],v[140:143]
MM v[60:63],a[244:247],v[188:191],v[60:63]
MM v[60:63],a[248:251],v[204:207],v[60:63]
MM v[60:63],a[252:255],v[220:223],v[60:63]
MM v[56:59],a[236:239],v[220:223],v[140:143]
MM v[56:59],a[232:235],v[204:207],v[56:59]
MM v[56:59],a[228:231],v[188:191],v[56:59]
MM v[56:59],a[224:227],v[172:175],v[56:59]
s_nop 15
v_add_u32 v249, s75, v224
RD a[192:195],v249 offset:0
RD a[208:211],v249 offset:4096
RD a[224:227],v249 offset:8192
RD a[240:243],v249 offset:12288
v_add_u32 v250, s75, v225
RD a[196:199],v250 offset:0
RD a[212:215],v250 offset:4096
RD a[228:231],v250 offset:8192
RD a[244:247],v250 offset:12288
v_add_u32 v251, s75, v226
RD a[200:203],v251 offset:0
RD a[216:219],v251 offset:4096
RD a[232:235],v251 offset:8192
RD a[248:251],v251 offset:12288
v_add_u32 v254, s75, v227
RD a[204:207],v254 offset:0
RD a[220:223],v254 offset:4096
RD a[236:239],v254 offset:8192
RD a[252:255],v254 offset:12288
s_waitcnt lgkmcnt(0)
v_mov_b32 v236, v247
v_mov_b32 v237, 0
v_max3_f32 v249, v0, v1, v2
v_max3_f32 v251, v32, v33, v34
v_max3_f32 v249, v249, v3, v4
v_max3_f32 v251, v251, v35, v36
v_max3_f32 v249, v249, v5, v6
v_max3_f32 v251, v251, v37, v38
v_max3_f32 v249, v249, v7, v39
v_max_f32 v249, v249, v251
v_log_f32 v250, v247
s_lshl_b64 exec, 0xffff, 0
v_max_f32 v249, v249, v250
s_mov_b64 exec, -1
v_mov_b32 v251, v249
s_nop 1
v_permlane16_swap_b32 v249, v251
v_max_f32 v249, v249, v251
v_mov_b32 v251, v249
s_nop 1
v_permlane32_swap_b32 v249, v251
v_max_f32 v249, v249, v251
v_max_f32 v249, 0, v249
v_sub_f32 v250, 0, v249
EX v250,v250
v_sub_f32 v128, v128, v249
v_sub_f32 v129, v129, v249
v_sub_f32 v130, v130, v249
v_sub_f32 v131, v131, v249
v_sub_f32 v64, v64, v249
v_sub_f32 v0, v0, v249
v_sub_f32 v65, v65, v249
v_sub_f32 v1, v1, v249
v_sub_f32 v66, v66, v249
v_sub_f32 v2, v2, v249
v_sub_f32 v67, v67, v249
v_sub_f32 v3, v3, v249
v_sub_f32 v68, v68, v249
v_sub_f32 v4, v4, v249
v_sub_f32 v69, v69, v249
v_sub_f32 v5, v5, v249
v_sub_f32 v70, v70, v249
v_sub_f32 v6, v6, v249
v_sub_f32 v71, v71, v249
v_sub_f32 v7, v7, v249
)ASM"
R"ASM(
v_sub_f32 v96, v96, v249
v_sub_f32 v32, v32, v249
v_sub_f32 v97, v97, v249
v_sub_f32 v33, v33, v249
v_sub_f32 v98, v98, v249
v_sub_f32 v34, v34, v249
v_sub_f32 v99, v99, v249
v_sub_f32 v35, v35, v249
v_sub_f32 v100, v100, v249
v_sub_f32 v36, v36, v249
v_sub_f32 v101, v101, v249
v_sub_f32 v37, v37, v249
v_sub_f32 v102, v102, v249
v_sub_f32 v38, v38, v249
v_sub_f32 v103, v103, v249
v_sub_f32 v39, v39, v249
s_lshl_b64 exec, 0xffff, 0
v_mul_f32 v236, v236, v250
s_mov_b64 exec, -1
AR v254,a0
v_mul_f32 v254, v254, v250
AW a0,v254
AR v251,a1
v_mul_f32 v251, v251, v250
AW a1,v251
AR v254,a2
v_mul_f32 v254, v254, v250
AW a2,v254
AR v251,a3
v_mul_f32 v251, v251, v250
AW a3,v251
AR v254,a4
v_mul_f32 v254, v254, v250
AW a4,v254
AR v251,a5
v_mul_f32 v251, v251, v250
AW a5,v251
AR v254,a6
v_mul_f32 v254, v254, v250
AW a6,v254
AR v251,a7
v_mul_f32 v251, v251, v250
AW a7,v251
AR v254,a32
v_mul_f32 v254, v254, v250
AW a32,v254
AR v251,a33
v_mul_f32 v251, v251, v250
AW a33,v251
AR v254,a34
v_mul_f32 v254, v254, v250
AW a34,v254
AR v251,a35
v_mul_f32 v251, v251, v250
AW a35,v251
AR v254,a36
v_mul_f32 v254, v254, v250
AW a36,v254
AR v251,a37
v_mul_f32 v251, v251, v250
AW a37,v251
AR v254,a38
v_mul_f32 v254, v254, v250
AW a38,v254
AR v251,a39
v_mul_f32 v251, v251, v250
AW a39,v251
AR v254,a64
v_mul_f32 v254, v254, v250
AW a64,v254
AR v251,a65
v_mul_f32 v251, v251, v250
AW a65,v251
AR v254,a66
v_mul_f32 v254, v254, v250
AW a66,v254
AR v251,a67
v_mul_f32 v251, v251, v250
AW a67,v251
AR v254,a68
v_mul_f32 v254, v254, v250
AW a68,v254
AR v251,a69
v_mul_f32 v251, v251, v250
AW a69,v251
AR v254,a70
v_mul_f32 v254, v254, v250
AW a70,v254
AR v251,a71
v_mul_f32 v251, v251, v250
AW a71,v251
AR v254,a96
v_mul_f32 v254, v254, v250
AW a96,v254
AR v251,a97
v_mul_f32 v251, v251, v250
AW a97,v251
AR v254,a98
v_mul_f32 v254, v254, v250
AW a98,v254
AR v251,a99
v_mul_f32 v251, v251, v250
AW a99,v251
AR v254,a100
v_mul_f32 v254, v254, v250
AW a100,v254
AR v251,a101
v_mul_f32 v251, v251, v250
AW a101,v251
AR v254,a102
v_mul_f32 v254, v254, v250
AW a102,v254
AR v251,a103
v_mul_f32 v251, v251, v250
AW a103,v251
v_max3_f32 v249, v8, v9, v10
v_max3_f32 v251, v40, v41, v42
v_max3_f32 v249, v249, v11, v12
v_max3_f32 v251, v251, v43, v44
v_max3_f32 v249, v249, v13, v14
v_max3_f32 v251, v251, v45, v46
v_max3_f32 v249, v249, v15, v47
v_max_f32 v249, v249, v251
v_log_f32 v250, v247
s_lshl_b64 exec, 0xffff, 16
v_max_f32 v249, v249, v250
s_mov_b64 exec, -1
v_mov_b32 v251, v249
s_nop 1
v_permlane16_swap_b32 v249, v251
v_max_f32 v249, v249, v251
v_mov_b32 v251, v249
s_nop 1
v_permlane32_swap_b32 v249, v251
v_max_f32 v249, v249, v251
v_max_f32 v249, 0, v249
v_sub_f32 v250, 0, v249
EX v250,v250
v_sub_f32 v132, v132, v249
v_sub_f32 v133, v133, v249
v_sub_f32 v134, v134, v249
v_sub_f32 v135, v135, v249
v_sub_f32 v72, v72, v249
v_sub_f32 v8, v8, v249
v_sub_f32 v73, v73, v249
v_sub_f32 v9, v9, v249
v_sub_f32 v74, v74, v249
v_sub_f32 v10, v10, v249
v_sub_f32 v75, v75, v249
v_sub_f32 v11, v11, v249
v_sub_f32 v76, v76, v249
v_sub_f32 v12, v12, v249
v_sub_f32 v77, v77, v249
v_sub_f32 v13, v13, v249
v_sub_f32 v78, v78, v249
v_sub_f32 v14, v14, v249
v_sub_f32 v79, v79, v249
v_sub_f32 v15, v15, v249
v_sub_f32 v104, v104, v249
v_sub_f32 v40, v40, v249
v_sub_f32 v105, v105, v249
v_sub_f32 v41, v41, v249
v_sub_f32 v106, v106, v249
v_sub_f32 v42, v42, v249
v_sub_f32 v107, v107, v249
v_sub_f32 v43, v43, v249
v_sub_f32 v108, v108, v249
v_sub_f32 v44, v44, v249
v_sub_f32 v109, v109, v249
v_sub_f32 v45, v45, v249
v_sub_f32 v110, v110, v249
v_sub_f32 v46, v46, v249
v_sub_f32 v111, v111, v249
v_sub_f32 v47, v47, v249
s_lshl_b64 exec, 0xffff, 16
v_mul_f32 v236, v236, v250
s_mov_b64 exec, -1
AR v254,a8
v_mul_f32 v254, v254, v250
AW a8,v254
AR v251,a9
v_mul_f32 v251, v251, v250
AW a9,v251
AR v254,a10
v_mul_f32 v254, v254, v250
AW a10,v254
AR v251,a11
v_mul_f32 v251, v251, v250
AW a11,v251
AR v254,a12
v_mul_f32 v254, v254, v250
AW a12,v254
AR v251,a13
v_mul_f32 v251, v251, v250
AW a13,v251
AR v254,a14
v_mul_f32 v254, v254, v250
AW a14,v254
AR v251,a15
v_mul_f32 v251, v251, v250
AW a15,v251
AR v254,a40
v_mul_f32 v254, v254, v250
AW a40,v254
AR v251,a41
v_mul_f32 v251, v251, v250
AW a41,v251
AR v254,a42
v_mul_f32 v254, v254, v250
AW a42,v254
AR v251,a43
v_mul_f32 v251, v251, v250
AW a43,v251
AR v254,a44
v_mul_f32 v254, v254, v250
AW a44,v254
AR v251,a45
v_mul_f32 v251, v251, v250
AW a45,v251
AR v254,a46
v_mul_f32 v254, v254, v250
AW a46,v254
AR v251,a47
v_mul_f32 v251, v251, v250
AW a47,v251
AR v254,a72
v_mul_f32 v254, v254, v250
AW a72,v254
AR v251,a73
v_mul_f32 v251, v251, v250
AW a73,v251
AR v254,a74
v_mul_f32 v254, v254, v250
AW a74,v254
AR v251,a75
v_mul_f32 v251, v251, v250
AW a75,v251
AR v254,a76
v_mul_f32 v254, v254, v250
AW a76,v254
AR v251,a77
v_mul_f32 v251, v251, v250
AW a77,v251
AR v254,a78
v_mul_f32 v254, v254, v250
AW a78,v254
AR v251,a79
v_mul_f32 v251, v251, v250
AW a79,v251
AR v254,a104
v_mul_f32 v254, v254, v250
AW a104,v254
AR v251,a105
v_mul_f32 v251, v251, v250
AW a105,v251
AR v254,a106
v_mul_f32 v254, v254, v250
AW a106,v254
AR v251,a107
v_mul_f32 v251, v251, v250
AW a107,v251
AR v254,a108
v_mul_f32 v254, v254, v250
AW a108,v254
AR v251,a109
v_mul_f32 v251, v251, v250
AW a109,v251
AR v254,a110
v_mul_f32 v254, v254, v250
AW a110,v254
AR v251,a111
v_mul_f32 v251, v251, v250
AW a111,v251
v_max3_f32 v249, v16, v17, v18
v_max3_f32 v251, v48, v49, v50
v_max3_f32 v249, v249, v19, v20
v_max3_f32 v251, v251, v51, v52
v_max3_f32 v249, v249, v21, v22
v_max3_f32 v251, v251, v53, v54
v_max3_f32 v249, v249, v23, v55
v_max_f32 v249, v249, v251
v_log_f32 v250, v247
s_lshl_b64 exec, 0xffff, 32
v_max_f32 v249, v249, v250
s_mov_b64 exec, -1
v_mov_b32 v251, v249
s_nop 1
v_permlane16_swap_b32 v249, v251
v_max_f32 v249, v249, v251
v_mov_b32 v251, v249
s_nop 1
v_permlane32_swap_b32 v249, v251
v_max_f32 v249, v249, v251
v_max_f32 v249, 0, v249
v_sub_f32 v250, 0, v249
EX v250,v250
v_sub_f32 v136, v136, v249
v_sub_f32 v137, v137, v249
v_sub_f32 v138, v138, v249
v_sub_f32 v139, v139, v249
v_sub_f32 v80, v80, v249
v_sub_f32 v16, v16, v249
v_sub_f32 v81, v81, v249
v_sub_f32 v17, v17, v249
v_sub_f32 v82, v82, v249
v_sub_f32 v18, v18, v249
v_sub_f32 v83, v83, v249
v_sub_f32 v19, v19, v249
v_sub_f32 v84, v84, v249
v_sub_f32 v20, v20, v249
v_sub_f32 v85, v85, v249
v_sub_f32 v21, v21, v249
v_sub_f32 v86, v86, v249
v_sub_f32 v22, v22, v249
v_sub_f32 v87, v87, v249
v_sub_f32 v23, v23, v249
v_sub_f32 v112, v112, v249
v_sub_f32 v48, v48, v249
v_sub_f32 v113, v113, v249
v_sub_f32 v49, v49, v249
v_sub_f32 v114, v114, v249
v_sub_f32 v50, v50, v249
v_sub_f32 v115, v115, v249
v_sub_f32 v51, v51, v249
v_sub_f32 v116, v116, v249
v_sub_f32 v52, v52, v249
v_sub_f32 v117, v117, v249
v_sub_f32 v53, v53, v249
v_sub_f32 v118, v118, v249
v_sub_f32 v54, v54, v249
v_sub_f32 v119, v119, v249
v_sub_f32 v55, v55, v249
s_lshl_b64 exec, 0xffff, 32
v_mul_f32 v236, v236, v250
s_mov_b64 exec, -1
AR v254,a16
v_mul_f32 v254, v254, v250
AW a16,v254
AR v251,a17
v_mul_f32 v251, v251, v250
AW a17,v251
AR v254,a18
v_mul_f32 v254, v254, v250
AW a18,v254
AR v251,a19
v_mul_f32 v251, v251, v250
AW a19,v251
AR v254,a20
v_mul_f32 v254, v254, v250
AW a20,v254
AR v251,a21
v_mul_f32 v251, v251, v250
AW a21,v251
AR v254,a22
v_mul_f32 v254, v254, v250
AW a22,v254
AR v251,a23
v_mul_f32 v251, v251, v250
AW a23,v251
AR v254,a48
v_mul_f32 v254, v254, v250
AW a48,v254
AR v251,a49
v_mul_f32 v251, v251, v250
AW a49,v251
AR v254,a50
v_mul_f32 v254, v254, v250
AW a50,v254
AR v251,a51
v_mul_f32 v251, v251, v250
AW a51,v251
AR v254,a52
v_mul_f32 v254, v254, v250
AW a52,v254
AR v251,a53
v_mul_f32 v251, v251, v250
AW a53,v251
AR v254,a54
v_mul_f32 v254, v254, v250
AW a54,v254
AR v251,a55
v_mul_f32 v251, v251, v250
AW a55,v251
AR v254,a80
v_mul_f32 v254, v254, v250
AW a80,v254
AR v251,a81
v_mul_f32 v251, v251, v250
AW a81,v251
AR v254,a82
v_mul_f32 v254, v254, v250
AW a82,v254
AR v251,a83
v_mul_f32 v251, v251, v250
AW a83,v251
AR v254,a84
v_mul_f32 v254, v254, v250
AW a84,v254
AR v251,a85
v_mul_f32 v251, v251, v250
AW a85,v251
AR v254,a86
v_mul_f32 v254, v254, v250
AW a86,v254
AR v251,a87
v_mul_f32 v251, v251, v250
AW a87,v251
AR v254,a112
v_mul_f32 v254, v254, v250
AW a112,v254
AR v251,a113
v_mul_f32 v251, v251, v250
AW a113,v251
AR v254,a114
v_mul_f32 v254, v254, v250
AW a114,v254
AR v251,a115
v_mul_f32 v251, v251, v250
AW a115,v251
AR v254,a116
v_mul_f32 v254, v254, v250
AW a116,v254
AR v251,a117
v_mul_f32 v251, v251, v250
AW a117,v251
AR v254,a118
v_mul_f32 v254, v254, v250
AW a118,v254
AR v251,a119
v_mul_f32 v251, v251, v250
AW a119,v251
v_max3_f32 v249, v24, v25, v26
v_max3_f32 v251, v56, v57, v58
v_max3_f32 v249, v249, v27, v28
v_max3_f32 v251, v251, v59, v60
v_max3_f32 v249, v249, v29, v30
v_max3_f32 v251, v251, v61, v62
v_max3_f32 v249, v249, v31, v63
v_max_f32 v249, v249, v251
v_log_f32 v250, v247
s_lshl_b64 exec, 0xffff, 48
v_max_f32 v249, v249, v250
s_mov_b64 exec, -1
v_mov_b32 v251, v249
s_nop 1
v_permlane16_swap_b32 v249, v251
v_max_f32 v249, v249, v251
v_mov_b32 v251, v249
s_nop 1
v_permlane32_swap_b32 v249, v251
v_max_f32 v249, v249, v251
v_max_f32 v249, 0, v249
v_sub_f32 v250, 0, v249
EX v250,v250
v_sub_f32 v140, v140, v249
v_sub_f32 v141, v141, v249
v_sub_f32 v142, v142, v249
v_sub_f32 v143, v143, v249
v_sub_f32 v88, v88, v249
v_sub_f32 v24, v24, v249
v_sub_f32 v89, v89, v249
v_sub_f32 v25, v25, v249
v_sub_f32 v90, v90, v249
v_sub_f32 v26, v26, v249
v_sub_f32 v91, v91, v249
v_sub_f32 v27, v27, v249
v_sub_f32 v92, v92, v249
v_sub_f32 v28, v28, v249
v_sub_f32 v93, v93, v249
v_sub_f32 v29, v29, v249
v_sub_f32 v94, v94, v249
v_sub_f32 v30, v30, v249
v_sub_f32 v95, v95, v249
v_sub_f32 v31, v31, v249
v_sub_f32 v120, v120, v249
v_sub_f32 v56, v56, v249
v_sub_f32 v121, v121, v249
v_sub_f32 v57, v57, v249
v_sub_f32 v122, v122, v249
v_sub_f32 v58, v58, v249
v_sub_f32 v123, v123, v249
v_sub_f32 v59, v59, v249
v_sub_f32 v124, v124, v249
v_sub_f32 v60, v60, v249
v_sub_f32 v125, v125, v249
v_sub_f32 v61, v61, v249
v_sub_f32 v126, v126, v249
v_sub_f32 v62, v62, v249
v_sub_f32 v127, v127, v249
v_sub_f32 v63, v63, v249
s_lshl_b64 exec, 0xffff, 48
v_mul_f32 v236, v236, v250
s_mov_b64 exec, -1
AR v254,a24
v_mul_f32 v254, v254, v250
AW a24,v254
AR v251,a25
v_mul_f32 v251, v251, v250
AW a25,v251
AR v254,a26
v_mul_f32 v254, v254, v250
AW a26,v254
AR v251,a27
v_mul_f32 v251, v251, v250
AW a27,v251
AR v254,a28
v_mul_f32 v254, v254, v250
AW a28,v254
AR v251,a29
v_mul_f32 v251, v251, v250
AW a29,v251
AR v254,a30
v_mul_f32 v254, v254, v250
AW a30,v254
AR v251,a31
v_mul_f32 v251, v251, v250
AW a31,v251
AR v254,a56
v_mul_f32 v254, v254, v250
AW a56,v254
AR v251,a57
v_mul_f32 v251, v251, v250
AW a57,v251
AR v254,a58
v_mul_f32 v254, v254, v250
AW a58,v254
AR v251,a59
v_mul_f32 v251, v251, v250
AW a59,v251
AR v254,a60
v_mul_f32 v254, v254, v250
AW a60,v254
AR v251,a61
v_mul_f32 v251, v251, v250
AW a61,v251
AR v254,a62
v_mul_f32 v254, v254, v250
AW a62,v254
AR v251,a63
v_mul_f32 v251, v251, v250
AW a63,v251
AR v254,a88
v_mul_f32 v254, v254, v250
AW a88,v254
AR v251,a89
v_mul_f32 v251, v251, v250
AW a89,v251
AR v254,a90
v_mul_f32 v254, v254, v250
AW a90,v254
AR v251,a91
v_mul_f32 v251, v251, v250
AW a91,v251
AR v254,a92
v_mul_f32 v254, v254, v250
AW a92,v254
AR v251,a93
v_mul_f32 v251, v251, v250
AW a93,v251
AR v254,a94
v_mul_f32 v254, v254, v250
AW a94,v254
AR v251,a95
v_mul_f32 v251, v251, v250
AW a95,v251
AR v254,a120
v_mul_f32 v254, v254, v250
AW a120,v254
AR v251,a121
v_mul_f32 v251, v251, v250
AW a121,v251
AR v254,a122
v_mul_f32 v254, v254, v250
AW a122,v254
AR v251,a123
v_mul_f32 v251, v251, v250
AW a123,v251
AR v254,a124
v_mul_f32 v254, v254, v250
AW a124,v254
AR v251,a125
v_mul_f32 v251, v251, v250
AW a125,v251
AR v254,a126
v_mul_f32 v254, v254, v250
AW a126,v254
AR v251,a127
v_mul_f32 v251, v251, v250
AW a127,v251
EX v0,v0
EX v1,v1
EX v2,v2
EX v3,v3
EX v4,v4
EX v5,v5
EX v6,v6
EX v7,v7
EX v8,v8
EX v9,v9
EX v10,v10
EX v11,v11
EX v12,v12
EX v13,v13
EX v14,v14
EX v15,v15
EX v16,v16
EX v17,v17
EX v18,v18
EX v19,v19
EX v20,v20
EX v21,v21
)ASM"
R"ASM(
EX v22,v22
EX v23,v23
EX v24,v24
EX v25,v25
EX v26,v26
EX v27,v27
EX v28,v28
EX v29,v29
EX v30,v30
EX v31,v31
EX v32,v32
EX v33,v33
EX v34,v34
EX v35,v35
EX v36,v36
EX v37,v37
EX v38,v38
EX v39,v39
EX v40,v40
EX v41,v41
EX v42,v42
EX v43,v43
EX v44,v44
EX v45,v45
EX v46,v46
EX v47,v47
EX v48,v48
EX v49,v49
EX v50,v50
EX v51,v51
EX v52,v52
EX v53,v53
EX v54,v54
EX v55,v55
EX v56,v56
EX v57,v57
EX v58,v58
EX v59,v59
EX v60,v60
EX v61,v61
EX v62,v62
EX v63,v63
CV v0,v0,v1
CV v1,v2,v3
CV v2,v4,v5
CV v3,v6,v7
CV v8,v8,v9
CV v9,v10,v11
CV v10,v12,v13
CV v11,v14,v15
CV v16,v16,v17
CV v17,v18,v19
CV v18,v20,v21
CV v19,v22,v23
CV v24,v24,v25
CV v25,v26,v27
CV v26,v28,v29
CV v27,v30,v31
CV v4,v32,v33
CV v5,v34,v35
CV v6,v36,v37
CV v7,v38,v39
CV v12,v40,v41
CV v13,v42,v43
CV v14,v44,v45
CV v15,v46,v47
CV v20,v48,v49
CV v21,v50,v51
CV v22,v52,v53
CV v23,v54,v55
CV v28,v56,v57
CV v29,v58,v59
CV v30,v60,v61
CV v31,v62,v63
v_smfmac_f32_16x16x64_bf16 v[236:239], v[144:147], v[0:7], v234
v_smfmac_f32_16x16x64_bf16 v[236:239], v[148:151], v[8:15], v234
v_smfmac_f32_16x16x64_bf16 v[236:239], v[152:155], v[16:23], v234
v_smfmac_f32_16x16x64_bf16 v[236:239], v[156:159], v[24:31], v234
s_nop 9
v_add_f32 v246, v236, v237
s_cmp_eq_u32 s73, 4
s_cbranch_scc1 LBACK4_%=
s_cmp_eq_u32 s73, 6
s_cbranch_scc1 LBACK6_%=
s_cmp_eq_u32 s73, 8
s_cbranch_scc1 LBACK8_%=
s_cmp_eq_u32 s73, 10
s_cbranch_scc1 LBACK10_%=
s_cmp_eq_u32 s73, 12
s_cbranch_scc1 LBACK12_%=
s_cmp_eq_u32 s73, 14
s_cbranch_scc1 LBACK14_%=
s_cmp_eq_u32 s73, 16
s_cbranch_scc1 LBACK16_%=
s_cmp_eq_u32 s73, 18
s_cbranch_scc1 LBACK18_%=
s_cmp_eq_u32 s73, 20
s_cbranch_scc1 LBACK20_%=
s_cmp_eq_u32 s73, 22
s_cbranch_scc1 LBACK22_%=
s_cmp_eq_u32 s73, 24
s_cbranch_scc1 LBACK24_%=
s_cmp_eq_u32 s73, 26
s_cbranch_scc1 LBACK26_%=
s_cmp_eq_u32 s73, 28
s_cbranch_scc1 LBACK28_%=
s_cmp_eq_u32 s73, 30
s_cbranch_scc1 LBACK30_%=
s_endpgm
LSITE32_%=:
s_mov_b32 s73, 32
s_mov_b32 s75, 16384
s_mov_b32 s74, 0
s_branch LRARE022_%=
LSITE34_%=:
s_mov_b32 s73, 34
s_mov_b32 s75, 49152
s_mov_b32 s74, 32768
s_branch LRARE022_%=
LRARE022_%=:
s_nop 15
s_waitcnt vmcnt(0) lgkmcnt(0)
v_add_u32 v249, s74, v224
RD a[192:195],v249 offset:0
RD a[208:211],v249 offset:4096
RD a[224:227],v249 offset:8192
RD a[240:243],v249 offset:12288
v_add_u32 v250, s74, v225
RD a[196:199],v250 offset:0
RD a[212:215],v250 offset:4096
RD a[228:231],v250 offset:8192
RD a[244:247],v250 offset:12288
v_add_u32 v251, s74, v226
RD a[200:203],v251 offset:0
RD a[216:219],v251 offset:4096
RD a[232:235],v251 offset:8192
RD a[248:251],v251 offset:12288
v_add_u32 v254, s74, v227
RD a[204:207],v254 offset:0
RD a[220:223],v254 offset:4096
RD a[236:239],v254 offset:8192
RD a[252:255],v254 offset:12288
s_waitcnt lgkmcnt(0)
MM v[16:19],a[192:195],v[168:171],v[136:139]
MM v[16:19],a[196:199],v[184:187],v[16:19]
MM v[16:19],a[200:203],v[200:203],v[16:19]
MM v[16:19],a[204:207],v[216:219],v[16:19]
MM v[20:23],a[220:223],v[216:219],v[136:139]
MM v[20:23],a[216:219],v[200:203],v[20:23]
MM v[20:23],a[212:215],v[184:187],v[20:23]
MM v[20:23],a[208:211],v[168:171],v[20:23]
MM v[28:31],a[208:211],v[172:175],v[140:143]
MM v[28:31],a[212:215],v[188:191],v[28:31]
MM v[28:31],a[216:219],v[204:207],v[28:31]
MM v[28:31],a[220:223],v[220:223],v[28:31]
MM v[24:27],a[204:207],v[220:223],v[140:143]
MM v[24:27],a[200:203],v[204:207],v[24:27]
MM v[24:27],a[196:199],v[188:191],v[24:27]
MM v[24:27],a[192:195],v[172:175],v[24:27]
MM v[48:51],a[224:227],v[168:171],v[136:139]
MM v[48:51],a[228:231],v[184:187],v[48:51]
MM v[48:51],a[232:235],v[200:203],v[48:51]
MM v[48:51],a[236:239],v[216:219],v[48:51]
MM v[52:55],a[252:255],v[216:219],v[136:139]
MM v[52:55],a[248:251],v[200:203],v[52:55]
MM v[52:55],a[244:247],v[184:187],v[52:55]
MM v[52:55],a[240:243],v[168:171],v[52:55]
MM v[60:63],a[240:243],v[172:175],v[140:143]
MM v[60:63],a[244:247],v[188:191],v[60:63]
MM v[60:63],a[248:251],v[204:207],v[60:63]
MM v[60:63],a[252:255],v[220:223],v[60:63]
MM v[56:59],a[236:239],v[220:223],v[140:143]
MM v[56:59],a[232:235],v[204:207],v[56:59]
MM v[56:59],a[228:231],v[188:191],v[56:59]
MM v[56:59],a[224:227],v[172:175],v[56:59]
s_nop 15
v_add_u32 v249, s75, v224
RD a[192:195],v249 offset:0
RD a[208:211],v249 offset:4096
RD a[224:227],v249 offset:8192
RD a[240:243],v249 offset:12288
v_add_u32 v250, s75, v225
RD a[196:199],v250 offset:0
RD a[212:215],v250 offset:4096
RD a[228:231],v250 offset:8192
RD a[244:247],v250 offset:12288
v_add_u32 v251, s75, v226
RD a[200:203],v251 offset:0
RD a[216:219],v251 offset:4096
RD a[232:235],v251 offset:8192
RD a[248:251],v251 offset:12288
v_add_u32 v254, s75, v227
RD a[204:207],v254 offset:0
RD a[220:223],v254 offset:4096
RD a[236:239],v254 offset:8192
RD a[252:255],v254 offset:12288
s_waitcnt lgkmcnt(0)
v_mov_b32 v236, v247
v_mov_b32 v237, 0
v_max3_f32 v249, v16, v17, v18
v_max3_f32 v251, v48, v49, v50
v_max3_f32 v249, v249, v19, v20
v_max3_f32 v251, v251, v51, v52
v_max3_f32 v249, v249, v21, v22
v_max3_f32 v251, v251, v53, v54
v_max3_f32 v249, v249, v23, v55
v_max_f32 v249, v249, v251
v_log_f32 v250, v247
s_lshl_b64 exec, 0xffff, 32
v_max_f32 v249, v249, v250
s_mov_b64 exec, -1
v_mov_b32 v251, v249
s_nop 1
v_permlane16_swap_b32 v249, v251
v_max_f32 v249, v249, v251
v_mov_b32 v251, v249
s_nop 1
v_permlane32_swap_b32 v249, v251
v_max_f32 v249, v249, v251
v_max_f32 v249, 0, v249
v_sub_f32 v250, 0, v249
EX v250,v250
v_sub_f32 v136, v136, v249
v_sub_f32 v137, v137, v249
v_sub_f32 v138, v138, v249
v_sub_f32 v139, v139, v249
v_sub_f32 v80, v80, v249
v_sub_f32 v16, v16, v249
v_sub_f32 v81, v81, v249
v_sub_f32 v17, v17, v249
v_sub_f32 v82, v82, v249
v_sub_f32 v18, v18, v249
v_sub_f32 v83, v83, v249
v_sub_f32 v19, v19, v249
v_sub_f32 v84, v84, v249
v_sub_f32 v20, v20, v249
v_sub_f32 v85, v85, v249
v_sub_f32 v21, v21, v249
v_sub_f32 v86, v86, v249
v_sub_f32 v22, v22, v249
v_sub_f32 v87, v87, v249
v_sub_f32 v23, v23, v249
v_sub_f32 v112, v112, v249
v_sub_f32 v48, v48, v249
v_sub_f32 v113, v113, v249
v_sub_f32 v49, v49, v249
v_sub_f32 v114, v114, v249
v_sub_f32 v50, v50, v249
v_sub_f32 v115, v115, v249
v_sub_f32 v51, v51, v249
v_sub_f32 v116, v116, v249
v_sub_f32 v52, v52, v249
v_sub_f32 v117, v117, v249
v_sub_f32 v53, v53, v249
v_sub_f32 v118, v118, v249
v_sub_f32 v54, v54, v249
v_sub_f32 v119, v119, v249
v_sub_f32 v55, v55, v249
s_lshl_b64 exec, 0xffff, 32
v_mul_f32 v236, v236, v250
s_mov_b64 exec, -1
AR v254,a16
v_mul_f32 v254, v254, v250
AW a16,v254
AR v251,a17
v_mul_f32 v251, v251, v250
AW a17,v251
AR v254,a18
v_mul_f32 v254, v254, v250
AW a18,v254
AR v251,a19
v_mul_f32 v251, v251, v250
AW a19,v251
AR v254,a20
v_mul_f32 v254, v254, v250
AW a20,v254
AR v251,a21
v_mul_f32 v251, v251, v250
AW a21,v251
AR v254,a22
v_mul_f32 v254, v254, v250
AW a22,v254
AR v251,a23
v_mul_f32 v251, v251, v250
AW a23,v251
AR v254,a48
v_mul_f32 v254, v254, v250
AW a48,v254
AR v251,a49
v_mul_f32 v251, v251, v250
AW a49,v251
AR v254,a50
v_mul_f32 v254, v254, v250
AW a50,v254
AR v251,a51
v_mul_f32 v251, v251, v250
AW a51,v251
AR v254,a52
v_mul_f32 v254, v254, v250
AW a52,v254
AR v251,a53
v_mul_f32 v251, v251, v250
AW a53,v251
AR v254,a54
v_mul_f32 v254, v254, v250
AW a54,v254
AR v251,a55
v_mul_f32 v251, v251, v250
AW a55,v251
AR v254,a80
v_mul_f32 v254, v254, v250
AW a80,v254
AR v251,a81
v_mul_f32 v251, v251, v250
AW a81,v251
AR v254,a82
v_mul_f32 v254, v254, v250
AW a82,v254
AR v251,a83
v_mul_f32 v251, v251, v250
AW a83,v251
AR v254,a84
v_mul_f32 v254, v254, v250
AW a84,v254
AR v251,a85
v_mul_f32 v251, v251, v250
AW a85,v251
AR v254,a86
v_mul_f32 v254, v254, v250
AW a86,v254
AR v251,a87
v_mul_f32 v251, v251, v250
AW a87,v251
AR v254,a112
v_mul_f32 v254, v254, v250
AW a112,v254
AR v251,a113
v_mul_f32 v251, v251, v250
AW a113,v251
AR v254,a114
v_mul_f32 v254, v254, v250
AW a114,v254
AR v251,a115
v_mul_f32 v251, v251, v250
AW a115,v251
AR v254,a116
v_mul_f32 v254, v254, v250
AW a116,v254
AR v251,a117
v_mul_f32 v251, v251, v250
AW a117,v251
AR v254,a118
v_mul_f32 v254, v254, v250
AW a118,v254
AR v251,a119
v_mul_f32 v251, v251, v250
AW a119,v251
v_max3_f32 v249, v24, v25, v26
v_max3_f32 v251, v56, v57, v58
v_max3_f32 v249, v249, v27, v28
v_max3_f32 v251, v251, v59, v60
v_max3_f32 v249, v249, v29, v30
v_max3_f32 v251, v251, v61, v62
v_max3_f32 v249, v249, v31, v63
v_max_f32 v249, v249, v251
v_log_f32 v250, v247
s_lshl_b64 exec, 0xffff, 48
v_max_f32 v249, v249, v250
s_mov_b64 exec, -1
v_mov_b32 v251, v249
s_nop 1
v_permlane16_swap_b32 v249, v251
v_max_f32 v249, v249, v251
v_mov_b32 v251, v249
s_nop 1
v_permlane32_swap_b32 v249, v251
v_max_f32 v249, v249, v251
v_max_f32 v249, 0, v249
v_sub_f32 v250, 0, v249
EX v250,v250
v_sub_f32 v140, v140, v249
v_sub_f32 v141, v141, v249
v_sub_f32 v142, v142, v249
v_sub_f32 v143, v143, v249
v_sub_f32 v88, v88, v249
v_sub_f32 v24, v24, v249
v_sub_f32 v89, v89, v249
v_sub_f32 v25, v25, v249
v_sub_f32 v90, v90, v249
v_sub_f32 v26, v26, v249
v_sub_f32 v91, v91, v249
v_sub_f32 v27, v27, v249
v_sub_f32 v92, v92, v249
v_sub_f32 v28, v28, v249
v_sub_f32 v93, v93, v249
v_sub_f32 v29, v29, v249
v_sub_f32 v94, v94, v249
v_sub_f32 v30, v30, v249
v_sub_f32 v95, v95, v249
v_sub_f32 v31, v31, v249
v_sub_f32 v120, v120, v249
v_sub_f32 v56, v56, v249
v_sub_f32 v121, v121, v249
v_sub_f32 v57, v57, v249
v_sub_f32 v122, v122, v249
v_sub_f32 v58, v58, v249
v_sub_f32 v123, v123, v249
v_sub_f32 v59, v59, v249
v_sub_f32 v124, v124, v249
v_sub_f32 v60, v60, v249
v_sub_f32 v125, v125, v249
v_sub_f32 v61, v61, v249
v_sub_f32 v126, v126, v249
v_sub_f32 v62, v62, v249
v_sub_f32 v127, v127, v249
v_sub_f32 v63, v63, v249
s_lshl_b64 exec, 0xffff, 48
v_mul_f32 v236, v236, v250
s_mov_b64 exec, -1
AR v254,a24
v_mul_f32 v254, v254, v250
AW a24,v254
AR v251,a25
v_mul_f32 v251, v251, v250
AW a25,v251
AR v254,a26
v_mul_f32 v254, v254, v250
AW a26,v254
AR v251,a27
v_mul_f32 v251, v251, v250
AW a27,v251
AR v254,a28
v_mul_f32 v254, v254, v250
AW a28,v254
AR v251,a29
v_mul_f32 v251, v251, v250
AW a29,v251
AR v254,a30
v_mul_f32 v254, v254, v250
AW a30,v254
AR v251,a31
v_mul_f32 v251, v251, v250
AW a31,v251
AR v254,a56
v_mul_f32 v254, v254, v250
AW a56,v254
AR v251,a57
v_mul_f32 v251, v251, v250
AW a57,v251
AR v254,a58
v_mul_f32 v254, v254, v250
AW a58,v254
AR v251,a59
v_mul_f32 v251, v251, v250
AW a59,v251
AR v254,a60
v_mul_f32 v254, v254, v250
AW a60,v254
AR v251,a61
v_mul_f32 v251, v251, v250
AW a61,v251
AR v254,a62
v_mul_f32 v254, v254, v250
AW a62,v254
AR v251,a63
v_mul_f32 v251, v251, v250
AW a63,v251
AR v254,a88
v_mul_f32 v254, v254, v250
AW a88,v254
AR v251,a89
v_mul_f32 v251, v251, v250
AW a89,v251
AR v254,a90
v_mul_f32 v254, v254, v250
AW a90,v254
AR v251,a91
v_mul_f32 v251, v251, v250
AW a91,v251
AR v254,a92
v_mul_f32 v254, v254, v250
AW a92,v254
AR v251,a93
v_mul_f32 v251, v251, v250
AW a93,v251
AR v254,a94
v_mul_f32 v254, v254, v250
AW a94,v254
AR v251,a95
v_mul_f32 v251, v251, v250
AW a95,v251
AR v254,a120
v_mul_f32 v254, v254, v250
AW a120,v254
AR v251,a121
v_mul_f32 v251, v251, v250
AW a121,v251
AR v254,a122
v_mul_f32 v254, v254, v250
AW a122,v254
AR v251,a123
v_mul_f32 v251, v251, v250
AW a123,v251
AR v254,a124
v_mul_f32 v254, v254, v250
AW a124,v254
AR v251,a125
v_mul_f32 v251, v251, v250
AW a125,v251
AR v254,a126
v_mul_f32 v254, v254, v250
AW a126,v254
AR v251,a127
v_mul_f32 v251, v251, v250
AW a127,v251
EX v16,v16
EX v17,v17
EX v18,v18
EX v19,v19
EX v20,v20
EX v21,v21
EX v22,v22
EX v23,v23
EX v24,v24
EX v25,v25
EX v26,v26
EX v27,v27
EX v28,v28
EX v29,v29
EX v30,v30
EX v31,v31
EX v48,v48
EX v49,v49
EX v50,v50
EX v51,v51
EX v52,v52
EX v53,v53
EX v54,v54
EX v55,v55
EX v56,v56
EX v57,v57
EX v58,v58
EX v59,v59
EX v60,v60
EX v61,v61
EX v62,v62
EX v63,v63
CV v16,v16,v17
CV v17,v18,v19
CV v18,v20,v21
CV v19,v22,v23
CV v24,v24,v25
CV v25,v26,v27
CV v26,v28,v29
CV v27,v30,v31
CV v20,v48,v49
CV v21,v50,v51
CV v22,v52,v53
CV v23,v54,v55
CV v28,v56,v57
CV v29,v58,v59
CV v30,v60,v61
CV v31,v62,v63
v_smfmac_f32_16x16x64_bf16 v[236:239], v[152:155], v[16:23], v234
s_nop 0
v_smfmac_f32_16x16x64_bf16 v[236:239], v[156:159], v[24:31], v234
s_nop 9
v_add_f32 v246, v236, v237
)ASM"
R"ASM(
s_cmp_eq_u32 s73, 32
s_cbranch_scc1 LBACK32_%=
s_cmp_eq_u32 s73, 34
s_cbranch_scc1 LBACK34_%=
s_endpgm
LSITE33_%=:
s_mov_b32 s73, 33
s_mov_b32 s75, 32768
s_mov_b32 s74, 16384
s_branch LRARE122_%=
LRARE122_%=:
s_nop 15
s_waitcnt vmcnt(0) lgkmcnt(0)
v_add_u32 v249, s74, v224
RD a[192:195],v249 offset:0
RD a[208:211],v249 offset:4096
RD a[224:227],v249 offset:8192
RD a[240:243],v249 offset:12288
v_add_u32 v250, s74, v225
RD a[196:199],v250 offset:0
RD a[212:215],v250 offset:4096
RD a[228:231],v250 offset:8192
RD a[244:247],v250 offset:12288
v_add_u32 v251, s74, v226
RD a[200:203],v251 offset:0
RD a[216:219],v251 offset:4096
RD a[232:235],v251 offset:8192
RD a[248:251],v251 offset:12288
v_add_u32 v254, s74, v227
RD a[204:207],v254 offset:0
RD a[220:223],v254 offset:4096
RD a[236:239],v254 offset:8192
RD a[252:255],v254 offset:12288
s_waitcnt lgkmcnt(0)
MM v[80:83],a[192:195],v[168:171],v[136:139]
MM v[80:83],a[196:199],v[184:187],v[80:83]
MM v[80:83],a[200:203],v[200:203],v[80:83]
MM v[80:83],a[204:207],v[216:219],v[80:83]
MM v[84:87],a[220:223],v[216:219],v[136:139]
MM v[84:87],a[216:219],v[200:203],v[84:87]
MM v[84:87],a[212:215],v[184:187],v[84:87]
MM v[84:87],a[208:211],v[168:171],v[84:87]
MM v[92:95],a[208:211],v[172:175],v[140:143]
MM v[92:95],a[212:215],v[188:191],v[92:95]
MM v[92:95],a[216:219],v[204:207],v[92:95]
MM v[92:95],a[220:223],v[220:223],v[92:95]
MM v[88:91],a[204:207],v[220:223],v[140:143]
MM v[88:91],a[200:203],v[204:207],v[88:91]
MM v[88:91],a[196:199],v[188:191],v[88:91]
MM v[88:91],a[192:195],v[172:175],v[88:91]
MM v[112:115],a[224:227],v[168:171],v[136:139]
MM v[112:115],a[228:231],v[184:187],v[112:115]
MM v[112:115],a[232:235],v[200:203],v[112:115]
MM v[112:115],a[236:239],v[216:219],v[112:115]
MM v[116:119],a[252:255],v[216:219],v[136:139]
MM v[116:119],a[248:251],v[200:203],v[116:119]
MM v[116:119],a[244:247],v[184:187],v[116:119]
MM v[116:119],a[240:243],v[168:171],v[116:119]
MM v[124:127],a[240:243],v[172:175],v[140:143]
MM v[124:127],a[244:247],v[188:191],v[124:127]
MM v[124:127],a[248:251],v[204:207],v[124:127]
MM v[124:127],a[252:255],v[220:223],v[124:127]
MM v[120:123],a[236:239],v[220:223],v[140:143]
MM v[120:123],a[232:235],v[204:207],v[120:123]
MM v[120:123],a[228:231],v[188:191],v[120:123]
MM v[120:123],a[224:227],v[172:175],v[120:123]
s_nop 15
v_add_u32 v249, s75, v224
RD a[192:195],v249 offset:0
RD a[208:211],v249 offset:4096
RD a[224:227],v249 offset:8192
RD a[240:243],v249 offset:12288
v_add_u32 v250, s75, v225
RD a[196:199],v250 offset:0
RD a[212:215],v250 offset:4096
RD a[228:231],v250 offset:8192
RD a[244:247],v250 offset:12288
v_add_u32 v251, s75, v226
RD a[200:203],v251 offset:0
RD a[216:219],v251 offset:4096
RD a[232:235],v251 offset:8192
RD a[248:251],v251 offset:12288
v_add_u32 v254, s75, v227
RD a[204:207],v254 offset:0
RD a[220:223],v254 offset:4096
RD a[236:239],v254 offset:8192
RD a[252:255],v254 offset:12288
s_waitcnt lgkmcnt(0)
v_mov_b32 v236, v246
v_mov_b32 v237, 0
v_max3_f32 v249, v80, v81, v82
v_max3_f32 v251, v112, v113, v114
v_max3_f32 v249, v249, v83, v84
v_max3_f32 v251, v251, v115, v116
v_max3_f32 v249, v249, v85, v86
v_max3_f32 v251, v251, v117, v118
v_max3_f32 v249, v249, v87, v119
v_max_f32 v249, v249, v251
v_log_f32 v250, v246
s_lshl_b64 exec, 0xffff, 32
v_max_f32 v249, v249, v250
s_mov_b64 exec, -1
v_mov_b32 v251, v249
s_nop 1
v_permlane16_swap_b32 v249, v251
v_max_f32 v249, v249, v251
v_mov_b32 v251, v249
s_nop 1
v_permlane32_swap_b32 v249, v251
v_max_f32 v249, v249, v251
v_max_f32 v249, 0, v249
v_sub_f32 v250, 0, v249
EX v250,v250
v_sub_f32 v136, v136, v249
v_sub_f32 v137, v137, v249
v_sub_f32 v138, v138, v249
v_sub_f32 v139, v139, v249
v_sub_f32 v16, v16, v249
v_sub_f32 v80, v80, v249
v_sub_f32 v17, v17, v249
v_sub_f32 v81, v81, v249
v_sub_f32 v18, v18, v249
v_sub_f32 v82, v82, v249
v_sub_f32 v19, v19, v249
v_sub_f32 v83, v83, v249
v_sub_f32 v20, v20, v249
v_sub_f32 v84, v84, v249
v_sub_f32 v21, v21, v249
v_sub_f32 v85, v85, v249
v_sub_f32 v22, v22, v249
v_sub_f32 v86, v86, v249
v_sub_f32 v23, v23, v249
v_sub_f32 v87, v87, v249
v_sub_f32 v48, v48, v249
v_sub_f32 v112, v112, v249
v_sub_f32 v49, v49, v249
v_sub_f32 v113, v113, v249
v_sub_f32 v50, v50, v249
v_sub_f32 v114, v114, v249
v_sub_f32 v51, v51, v249
v_sub_f32 v115, v115, v249
v_sub_f32 v52, v52, v249
v_sub_f32 v116, v116, v249
v_sub_f32 v53, v53, v249
v_sub_f32 v117, v117, v249
v_sub_f32 v54, v54, v249
v_sub_f32 v118, v118, v249
v_sub_f32 v55, v55, v249
v_sub_f32 v119, v119, v249
s_lshl_b64 exec, 0xffff, 32
v_mul_f32 v236, v236, v250
s_mov_b64 exec, -1
AR v254,a16
v_mul_f32 v254, v254, v250
AW a16,v254
AR v251,a17
v_mul_f32 v251, v251, v250
AW a17,v251
AR v254,a18
v_mul_f32 v254, v254, v250
AW a18,v254
AR v251,a19
v_mul_f32 v251, v251, v250
AW a19,v251
AR v254,a20
v_mul_f32 v254, v254, v250
AW a20,v254
AR v251,a21
v_mul_f32 v251, v251, v250
AW a21,v251
AR v254,a22
v_mul_f32 v254, v254, v250
AW a22,v254
AR v251,a23
v_mul_f32 v251, v251, v250
AW a23,v251
AR v254,a48
v_mul_f32 v254, v254, v250
AW a48,v254
AR v251,a49
v_mul_f32 v251, v251, v250
AW a49,v251
AR v254,a50
v_mul_f32 v254, v254, v250
AW a50,v254
AR v251,a51
v_mul_f32 v251, v251, v250
AW a51,v251
AR v254,a52
v_mul_f32 v254, v254, v250
AW a52,v254
AR v251,a53
v_mul_f32 v251, v251, v250
AW a53,v251
AR v254,a54
v_mul_f32 v254, v254, v250
AW a54,v254
AR v251,a55
v_mul_f32 v251, v251, v250
AW a55,v251
AR v254,a80
v_mul_f32 v254, v254, v250
AW a80,v254
AR v251,a81
v_mul_f32 v251, v251, v250
AW a81,v251
AR v254,a82
v_mul_f32 v254, v254, v250
AW a82,v254
AR v251,a83
v_mul_f32 v251, v251, v250
AW a83,v251
AR v254,a84
v_mul_f32 v254, v254, v250
AW a84,v254
AR v251,a85
v_mul_f32 v251, v251, v250
AW a85,v251
AR v254,a86
v_mul_f32 v254, v254, v250
AW a86,v254
AR v251,a87
v_mul_f32 v251, v251, v250
AW a87,v251
AR v254,a112
v_mul_f32 v254, v254, v250
AW a112,v254
AR v251,a113
v_mul_f32 v251, v251, v250
AW a113,v251
AR v254,a114
v_mul_f32 v254, v254, v250
AW a114,v254
AR v251,a115
v_mul_f32 v251, v251, v250
AW a115,v251
AR v254,a116
v_mul_f32 v254, v254, v250
AW a116,v254
AR v251,a117
v_mul_f32 v251, v251, v250
AW a117,v251
AR v254,a118
v_mul_f32 v254, v254, v250
AW a118,v254
AR v251,a119
v_mul_f32 v251, v251, v250
AW a119,v251
v_max3_f32 v249, v88, v89, v90
v_max3_f32 v251, v120, v121, v122
v_max3_f32 v249, v249, v91, v92
v_max3_f32 v251, v251, v123, v124
v_max3_f32 v249, v249, v93, v94
v_max3_f32 v251, v251, v125, v126
v_max3_f32 v249, v249, v95, v127
v_max_f32 v249, v249, v251
v_log_f32 v250, v246
s_lshl_b64 exec, 0xffff, 48
v_max_f32 v249, v249, v250
s_mov_b64 exec, -1
v_mov_b32 v251, v249
s_nop 1
v_permlane16_swap_b32 v249, v251
v_max_f32 v249, v249, v251
v_mov_b32 v251, v249
s_nop 1
v_permlane32_swap_b32 v249, v251
v_max_f32 v249, v249, v251
v_max_f32 v249, 0, v249
v_sub_f32 v250, 0, v249
EX v250,v250
v_sub_f32 v140, v140, v249
v_sub_f32 v141, v141, v249
v_sub_f32 v142, v142, v249
v_sub_f32 v143, v143, v249
v_sub_f32 v24, v24, v249
v_sub_f32 v88, v88, v249
v_sub_f32 v25, v25, v249
v_sub_f32 v89, v89, v249
v_sub_f32 v26, v26, v249
v_sub_f32 v90, v90, v249
v_sub_f32 v27, v27, v249
v_sub_f32 v91, v91, v249
v_sub_f32 v28, v28, v249
v_sub_f32 v92, v92, v249
v_sub_f32 v29, v29, v249
v_sub_f32 v93, v93, v249
v_sub_f32 v30, v30, v249
v_sub_f32 v94, v94, v249
v_sub_f32 v31, v31, v249
v_sub_f32 v95, v95, v249
v_sub_f32 v56, v56, v249
v_sub_f32 v120, v120, v249
v_sub_f32 v57, v57, v249
v_sub_f32 v121, v121, v249
v_sub_f32 v58, v58, v249
v_sub_f32 v122, v122, v249
v_sub_f32 v59, v59, v249
v_sub_f32 v123, v123, v249
v_sub_f32 v60, v60, v249
v_sub_f32 v124, v124, v249
v_sub_f32 v61, v61, v249
v_sub_f32 v125, v125, v249
v_sub_f32 v62, v62, v249
v_sub_f32 v126, v126, v249
v_sub_f32 v63, v63, v249
v_sub_f32 v127, v127, v249
s_lshl_b64 exec, 0xffff, 48
v_mul_f32 v236, v236, v250
s_mov_b64 exec, -1
AR v254,a24
v_mul_f32 v254, v254, v250
AW a24,v254
AR v251,a25
v_mul_f32 v251, v251, v250
AW a25,v251
AR v254,a26
v_mul_f32 v254, v254, v250
AW a26,v254
AR v251,a27
v_mul_f32 v251, v251, v250
AW a27,v251
AR v254,a28
v_mul_f32 v254, v254, v250
AW a28,v254
AR v251,a29
v_mul_f32 v251, v251, v250
AW a29,v251
AR v254,a30
v_mul_f32 v254, v254, v250
AW a30,v254
AR v251,a31
v_mul_f32 v251, v251, v250
AW a31,v251
AR v254,a56
v_mul_f32 v254, v254, v250
AW a56,v254
AR v251,a57
v_mul_f32 v251, v251, v250
AW a57,v251
AR v254,a58
v_mul_f32 v254, v254, v250
AW a58,v254
AR v251,a59
v_mul_f32 v251, v251, v250
AW a59,v251
AR v254,a60
v_mul_f32 v254, v254, v250
AW a60,v254
AR v251,a61
v_mul_f32 v251, v251, v250
AW a61,v251
AR v254,a62
v_mul_f32 v254, v254, v250
AW a62,v254
AR v251,a63
v_mul_f32 v251, v251, v250
AW a63,v251
AR v254,a88
v_mul_f32 v254, v254, v250
AW a88,v254
AR v251,a89
v_mul_f32 v251, v251, v250
AW a89,v251
AR v254,a90
v_mul_f32 v254, v254, v250
AW a90,v254
AR v251,a91
v_mul_f32 v251, v251, v250
AW a91,v251
AR v254,a92
v_mul_f32 v254, v254, v250
AW a92,v254
AR v251,a93
v_mul_f32 v251, v251, v250
AW a93,v251
AR v254,a94
v_mul_f32 v254, v254, v250
AW a94,v254
AR v251,a95
v_mul_f32 v251, v251, v250
AW a95,v251
AR v254,a120
v_mul_f32 v254, v254, v250
AW a120,v254
AR v251,a121
v_mul_f32 v251, v251, v250
AW a121,v251
AR v254,a122
v_mul_f32 v254, v254, v250
AW a122,v254
AR v251,a123
v_mul_f32 v251, v251, v250
AW a123,v251
AR v254,a124
v_mul_f32 v254, v254, v250
AW a124,v254
AR v251,a125
v_mul_f32 v251, v251, v250
AW a125,v251
AR v254,a126
v_mul_f32 v254, v254, v250
AW a126,v254
AR v251,a127
v_mul_f32 v251, v251, v250
AW a127,v251
EX v80,v80
EX v81,v81
EX v82,v82
EX v83,v83
EX v84,v84
EX v85,v85
EX v86,v86
EX v87,v87
EX v88,v88
EX v89,v89
EX v90,v90
EX v91,v91
EX v92,v92
EX v93,v93
EX v94,v94
EX v95,v95
EX v112,v112
EX v113,v113
EX v114,v114
EX v115,v115
EX v116,v116
EX v117,v117
EX v118,v118
EX v119,v119
EX v120,v120
EX v121,v121
EX v122,v122
EX v123,v123
EX v124,v124
EX v125,v125
EX v126,v126
EX v127,v127
CV v80,v80,v81
CV v81,v82,v83
CV v82,v84,v85
CV v83,v86,v87
CV v88,v88,v89
CV v89,v90,v91
CV v90,v92,v93
CV v91,v94,v95
CV v84,v112,v113
CV v85,v114,v115
CV v86,v116,v117
CV v87,v118,v119
CV v92,v120,v121
CV v93,v122,v123
CV v94,v124,v125
CV v95,v126,v127
v_smfmac_f32_16x16x64_bf16 v[236:239], v[152:155], v[80:87], v234
s_nop 0
v_smfmac_f32_16x16x64_bf16 v[236:239], v[156:159], v[88:95], v234
s_nop 9
v_add_f32 v247, v236, v237
s_cmp_eq_u32 s73, 33
s_cbranch_scc1 LBACK33_%=
s_endpgm
LEND_%=:
)ASM"

    :: [ka] "s"(__builtin_amdgcn_kernarg_segment_ptr()), [slds] "s"(slds), [swave] "s"(w), [sb] "s"(blockIdx.x)
    : "memory", "vcc", "scc", CLOB_V, CLOB_A, CLOB_S);
}

extern "C" void kernel_launch(void* const* d_in, const int* in_sizes, int n_in, void* d_out, int out_size, void* d_ws, size_t ws_size, hipStream_t stream) {
  hipFuncSetAttribute((const void*)ka_fused, hipFuncAttributeMaxDynamicSharedMemorySize, 163840);
  ka_fused<<<256, 256, 163840, stream>>>((const unsigned short*)d_in[0], (const unsigned short*)d_in[1], (const unsigned short*)d_in[2], (const unsigned short*)d_in[3],
                                         (const unsigned short*)d_in[4], (const unsigned short*)d_in[6], (const unsigned short*)d_in[7], (const unsigned short*)d_in[8],
                                         (const unsigned short*)d_in[9], (char*)d_ws, (unsigned short*)d_out
                                         );
}
```

```cpp
#include <hip/hip_runtime.h>
#include <stdint.h>

using f32x4  = __attribute__((ext_vector_type(4))) float;
using u32x4  = __attribute__((ext_vector_type(4))) unsigned;
using u32x2  = __attribute__((ext_vector_type(2))) unsigned;

constexpr int SEQ = 2048, DM = 640, NH = 32, DH = 128, HD = NH * DH, FF = 1728, TOPC = 8;

constexpr unsigned WS_FO  = 0;
constexpr unsigned WS_EP  = 32768;
constexpr unsigned WS_G1  = 36864;
constexpr unsigned WS_G2  = 53248;
constexpr unsigned WS_FH  = 69632;
constexpr unsigned WS_FY  = 73728;
constexpr unsigned WS_FG  = 77824;
constexpr unsigned WS_FR  = 81920;
constexpr unsigned WS_IX  = 86016;
constexpr unsigned WS_XN  = 131072;
constexpr unsigned WS_OF  = WS_XN + SEQ * DM * 2;
constexpr unsigned WS_H   = WS_OF + (18u << 20);
constexpr unsigned WS_Y   = WS_H + 32768;
constexpr unsigned WS_P   = WS_Y + 1048576;
constexpr unsigned WS_SPAN = 64u << 20;
constexpr int DN_FIRST = 76, YP = 18, SPIN = 30000;

#define CLOB_V "v0","v1","v2","v3","v4","v5","v6","v7","v8","v9","v10","v11","v12","v13","v14","v15","v16","v17","v18","v19","v20","v21","v22","v23","v24","v25","v26","v27","v28","v29","v30","v31","v32","v33","v34","v35","v36","v37","v38","v39","v40","v41","v42","v43","v44","v45","v46","v47","v48","v49","v50","v51","v52","v53","v54","v55","v56","v57","v58","v59","v60","v61","v62","v63","v64","v65","v66","v67","v68","v69","v70","v71","v72","v73","v74","v75","v76","v77","v78","v79","v80","v81","v82","v83","v84","v85","v86","v87","v88","v89","v90","v91","v92","v93","v94","v95","v96","v97","v98","v99","v100","v101","v102","v103","v104","v105","v106","v107","v108","v109","v110","v111","v112","v113","v114","v115","v116","v117","v118","v119","v120","v121","v122","v123","v124","v125","v126","v127","v128","v129","v130","v131","v132","v133","v134","v135","v136","v137","v138","v139","v140","v141","v142","v143","v144","v145","v146","v147","v148","v149","v150","v151","v152","v153","v154","v155","v156","v157","v158","v159","v160","v161","v162","v163","v164","v165","v166","v167","v168","v169","v170","v171","v172","v173","v174","v175","v176","v177","v178","v179","v180","v181","v182","v183","v184","v185","v186","v187","v188","v189","v190","v191","v192","v193","v194","v195","v196","v197","v198","v199","v200","v201","v202","v203","v204","v205","v206","v207","v208","v209","v210","v211","v212","v213","v214","v215","v216","v217","v218","v219","v220","v221","v222","v223","v224","v225","v226","v227","v228","v229","v230","v231","v232","v233","v234","v235","v236","v237","v238","v239","v240","v241","v242","v243","v244","v245","v246","v247","v248","v249","v250","v251","v252","v253","v254","v255"
#define CLOB_A "a0","a1","a2","a3","a4","a5","a6","a7","a8","a9","a10","a11","a12","a13","a14","a15","a16","a17","a18","a19","a20","a21","a22","a23","a24","a25","a26","a27","a28","a29","a30","a31","a32","a33","a34","a35","a36","a37","a38","a39","a40","a41","a42","a43","a44","a45","a46","a47","a48","a49","a50","a51","a52","a53","a54","a55","a56","a57","a58","a59","a60","a61","a62","a63","a64","a65","a66","a67","a68","a69","a70","a71","a72","a73","a74","a75","a76","a77","a78","a79","a80","a81","a82","a83","a84","a85","a86","a87","a88","a89","a90","a91","a92","a93","a94","a95","a96","a97","a98","a99","a100","a101","a102","a103","a104","a105","a106","a107","a108","a109","a110","a111","a112","a113","a114","a115","a116","a117","a118","a119","a120","a121","a122","a123","a124","a125","a126","a127","a128","a129","a130","a131","a132","a133","a134","a135","a136","a137","a138","a139","a140","a141","a142","a143","a144","a145","a146","a147","a148","a149","a150","a151","a152","a153","a154","a155","a156","a157","a158","a159","a160","a161","a162","a163","a164","a165","a166","a167","a168","a169","a170","a171","a172","a173","a174","a175","a176","a177","a178","a179","a180","a181","a182","a183","a184","a185","a186","a187","a188","a189","a190","a191","a192","a193","a194","a195","a196","a197","a198","a199","a200","a201","a202","a203","a204","a205","a206","a207","a208","a209","a210","a211","a212","a213","a214","a215","a216","a217","a218","a219","a220","a221","a222","a223","a224","a225","a226","a227","a228","a229","a230","a231","a232","a233","a234","a235","a236","a237","a238","a239","a240","a241","a242","a243","a244","a245","a246","a247","a248","a249","a250","a251","a252","a253","a254","a255"
#define CLOB_S "s8","s9","s10","s11","s12","s13","s14","s15","s16","s17","s18","s19","s20","s21","s22","s23","s24","s25","s26","s27","s28","s29","s30","s31","s32","s33","s34","s35","s36","s37","s38","s39","s40","s41","s42","s43","s44","s45","s46","s47","s48","s49","s50","s51","s52","s53","s54","s55","s56","s57","s58","s59","s60","s61","s62","s63","s64","s65","s66","s67","s68","s69","s70","s71","s72","s73","s74","s75","s76","s77","s78","s79","s80","s81","s82","s83","s84","s85","s86","s87","s88","s89","s90","s91","s92","s93","s94","s95","s96","s97","s98","s99"
__global__ __launch_bounds__(256, 1) __attribute__((aligned(4096))) void ka_fused(const unsigned short* __restrict__ x, const unsigned short* __restrict__ Kc, const unsigned short* __restrict__ Vc,
                                                   const unsigned short* __restrict__ wq, const unsigned short* __restrict__ wo,
                                                   const unsigned short* __restrict__ wr, const unsigned short* __restrict__ wg, const unsigned short* __restrict__ wu,
                                                   const unsigned short* __restrict__ wd,
                                                   char* __restrict__ ws, unsigned short* __restrict__ out
                                                   ) {
  extern __shared__ __attribute__((aligned(16))) char lds[];
  const unsigned w = __builtin_amdgcn_readfirstlane(threadIdx.x >> 6);
  const unsigned slds = __builtin_amdgcn_readfirstlane((unsigned)(size_t)lds);
  asm volatile(
R"ASM(
.macro PM a, b, c, d:vararg
v_perm_b32 \a, \b, \c, \d
.endm
.macro MM a, b, c, d:vararg
v_mfma_f32_16x16x32_bf16 \a, \b, \c, \d
.endm
.macro TR a, b:vararg
ds_read_b64_tr_b16 \a, \b
.endm
.macro CV a, b, c:vararg
v_cvt_pk_bf16_f32 \a, \b, \c
.endm
.macro AR a, b:vararg
v_accvgpr_read_b32 \a, \b
.endm
.macro AW a, b:vararg
v_accvgpr_write_b32 \a, \b
.endm
.macro EX a, b:vararg
v_exp_f32 \a, \b
.endm
.macro RD a, b:vararg
ds_read_b128 \a, \b
.endm
s_branch LPG0_%=
.p2align 6
LPG0_%=:
s_load_dwordx2 s[16:17], %[ka], 24
s_load_dwordx2 s[32:33], %[ka], 0
s_load_dwordx2 s[20:21], %[ka], 72
s_load_dwordx2 s[8:9], %[ka], 8
s_load_dwordx2 s[12:13], %[ka], 16
s_load_dwordx2 s[24:25], %[ka], 32
s_load_dwordx2 s[44:45], %[ka], 40
s_load_dwordx2 s[28:29], %[ka], 64
s_load_dwordx2 s[40:41], %[ka], 80
s_lshl_b32 s77, %[swave], 2
s_and_b32 s77, s77, 8
s_add_u32 s77, s77, 48
s_load_dwordx2 s[36:37], %[ka], s77
v_mbcnt_lo_u32_b32 v127, -1, 0
v_mbcnt_hi_u32_b32 v127, -1, v127
v_lshlrev_b32 v248, 4, v127
s_lshr_b32 s77, %[sb], 3
s_lshl_b32 s77, s77, 2
s_add_u32 s77, s77, %[swave]
s_lshl_b32 s77, s77, 10
v_add_u32 v118, s77, v248
s_mov_b32 s77, LKEND_ka-LPC_%=-16
v_min_u32 v118, s77, v118
s_getpc_b64 s[98:99]
LPC_%=:
global_load_dwordx4 a[252:255], v118, s[98:99]
s_and_b32 s77, %[sb], 7
s_lshl_b32 s77, s77, 2
s_bfe_u32 s84, %[sb], 131075
s_add_u32 s49, s77, s84
s_lshr_b32 s50, %[sb], 5
s_lshr_b32 s76, %[sb], 8
s_lshl_b32 s76, s76, 7
s_and_b32 s77, %[sb], 127
s_or_b32 s76, s76, s77
s_lshl_b32 s76, s76, 2
s_add_u32 s76, s76, %[swave]
s_lshl_b32 s76, s76, 1
s_bitcmp1_b32 %[sb], 7
s_cselect_b64 s[52:53], -1, 0
s_and_b32 s54, s52, 1
s_mov_b32 s55, s54
s_and_b32 s54, s52, 0x10001
s_mov_b32 s55, s54
v_lshrrev_b32 v180, 5, v127
s_mul_i32 s82, s76, 2560
v_and_b32 v182, 240, v248
v_lshrrev_b32 v181, 4, v127
v_mul_u32_u24 v168, 1280, v181
v_add_u32 v168, v168, v182
s_lshl_b32 s77, %[swave], 12
s_add_u32 s65, s77, %[slds]
s_lshl_b32 s89, %[swave], 17
v_bfe_u32 v210, v127, 2, 3
v_and_b32 v212, 3, v127
v_lshlrev_b32 v213, 4, v212
v_lshl_add_u32 v213, v180, 6, v213
v_lshl_add_u32 v253, v210, 13, v213
v_add_u32 v254, 128, v253
s_mul_i32 s96, %[swave], 4112
s_add_u32 s96, s96, %[slds]
s_lshl_b32 s77, s50, 2
s_add_u32 s77, s77, %[swave]
s_mul_i32 s68, s77, 81920
s_mul_i32 s83, s77, 560128
s_add_u32 s83, s83, 2752512
s_lshl_b32 s93, s77, 9
s_lshl_b32 s94, %[swave], 13
s_add_u32 s94, s94, %[slds]
v_lshrrev_b32 v211, 3, v127
v_mul_u32_u24 v220, 1280, v211
v_lshrrev_b32 v212, 4, v127
v_and_b32 v211, 7, v127
v_xor_b32 v211, v211, v212
v_lshl_add_u32 v220, v211, 4, v220
v_xor_b32 v221, 64, v220
s_bitcmp1_b32 s84, 0
s_mov_b32 s77, 0x77552200
s_cselect_b32 s77, 0x77552200, s77
s_lshl_b32 s91, s50, 2
s_lshr_b32 s77, s77, s91
s_and_b32 s77, s77, 15
s_lshl_b32 s86, s77, 7
s_lshl_b32 s85, s77, 19
s_lshl_b32 s90, s49, 8
s_waitcnt lgkmcnt(0)
s_add_u32 s16, s16, s90
s_addc_u32 s17, s17, 0
s_sub_u32 s18, 5242880, s90
s_mov_b32 s19, 131072
s_mov_b32 s34, 2621440
s_mov_b32 s35, 131072
s_mov_b32 s22, 67108864
s_mov_b32 s23, 131072
s_bitcmp1_b32 %[sb], 7
s_cbranch_scc1 LCA_%=
s_add_u32 s87, s89, s85
s_add_u32 s85, s85, 524288
s_cmp_ge_u32 s85, 5242880
s_cselect_b32 s85, 0, s85
s_add_u32 m0, s96, 65536
s_nop 0
buffer_load_dwordx4 v253, s[16:19], s87 offen offset:0 lds
s_add_u32 m0, s96, 66560
s_nop 0
buffer_load_dwordx4 v254, s[16:19], s87 offen offset:0 lds
s_add_u32 s87, s87, 65536
s_add_u32 m0, s96, 67592
s_nop 0
buffer_load_dwordx4 v253, s[16:19], s87 offen offset:0 lds
s_add_u32 m0, s96, 68616
s_nop 0
buffer_load_dwordx4 v254, s[16:19], s87 offen offset:0 lds
s_add_u32 s88, s68, s86
s_add_u32 s86, s86, 128
s_cmp_ge_u32 s86, 1280
s_cselect_b32 s86, 0, s86
s_add_u32 m0, s94, 0
s_add_u32 s95, s88, 0
buffer_load_dwordx4 v220, s[32:35], s95 offen offset:0 lds
s_add_u32 s95, s88, 9216
buffer_load_dwordx4 v221, s[32:35], s95 offen offset:1024 lds
s_add_u32 s95, s88, 18432
buffer_load_dwordx4 v220, s[32:35], s95 offen offset:2048 lds
s_add_u32 s95, s88, 27648
buffer_load_dwordx4 v221, s[32:35], s95 offen offset:3072 lds
s_add_u32 m0, s94, 4096
s_add_u32 s95, s88, 40960
buffer_load_dwordx4 v220, s[32:35], s95 offen offset:0 lds
s_add_u32 s95, s88, 50176
buffer_load_dwordx4 v221, s[32:35], s95 offen offset:1024 lds
s_add_u32 s95, s88, 59392
buffer_load_dwordx4 v220, s[32:35], s95 offen offset:2048 lds
s_add_u32 s95, s88, 68608
buffer_load_dwordx4 v221, s[32:35], s95 offen offset:3072 lds
s_add_u32 s87, s89, s85
s_add_u32 s85, s85, 524288
s_cmp_ge_u32 s85, 5242880
s_cselect_b32 s85, 0, s85
s_add_u32 m0, s96, 81984
s_nop 0
buffer_load_dwordx4 v253, s[16:19], s87 offen offset:0 lds
s_add_u32 m0, s96, 83008
s_nop 0
buffer_load_dwordx4 v254, s[16:19], s87 offen offset:0 lds
s_add_u32 s87, s87, 65536
s_add_u32 m0, s96, 84040
s_nop 0
buffer_load_dwordx4 v253, s[16:19], s87 offen offset:0 lds
s_add_u32 m0, s96, 85064
s_nop 0
buffer_load_dwordx4 v254, s[16:19], s87 offen offset:0 lds
s_branch LCT_%=
LCA_%=:
buffer_load_dwordx4 v[128:131], v168, s[32:35], s82 offen offset:0
buffer_load_dwordx4 v[132:135], v168, s[32:35], s82 offen offset:256
buffer_load_dwordx4 v[136:139], v168, s[32:35], s82 offen offset:512
buffer_load_dwordx4 v[140:143], v168, s[32:35], s82 offen offset:768
buffer_load_dwordx4 v[144:147], v168, s[32:35], s82 offen offset:1024
global_load_dwordx4 v[100:103], v182, s[44:45] offset:0
global_load_dwordx4 v[104:107], v182, s[44:45] offset:256
global_load_dwordx4 v[108:111], v182, s[44:45] offset:512
global_load_dwordx4 v[112:115], v182, s[44:45] offset:768
global_load_dwordx4 v[116:119], v182, s[44:45] offset:1024
LCT_%=:
v_and_b32 v211, 15, v127
v_bfe_u32 v222, v127, 1, 3
v_xor_b32 v222, v222, v212
v_lshlrev_b32 v222, 4, v222
v_lshl_add_u32 v222, v211, 7, v222
s_lshl_b32 s77, %[sb], 11
s_and_b32 s77, s77, 2048
v_add_u32 v222, s77, v222
v_xor_b32 v223, 64, v222
v_xor_b32 v217, 2048, v222
v_add_u32 v217, s94, v217
v_xor_b32 v218, 2048, v223
v_add_u32 v218, s94, v218
v_add_u32 v222, s94, v222
v_add_u32 v223, s94, v223
v_and_b32 v214, 1, v212
v_lshlrev_b32 v215, 5, v214
v_xor_b32 v215, v215, v213
v_lshl_add_u32 v235, v210, 8, v215
v_add_u32 v235, 128, v235
v_and_b32 v234, 1, v127
v_mul_u32_u24 v234, 43690, v234
v_add_u32 v234, 17476, v234
v_bfe_u32 v215, v127, 2, 2
v_and_b32 v216, 3, v127
v_mul_u32_u24 v252, 2056, v212
v_lshl_add_u32 v252, v215, 6, v252
v_lshl_add_u32 v252, v216, 4, v252
s_xor_b32 s77, %[swave], 0
s_lshl_b32 s77, s77, 9
s_add_u32 s77, s77, %[slds]
s_add_u32 s77, s77, 65536
v_add_u32 v192, s77, v252
v_add_u32 v196, 49344, v192
s_xor_b32 s77, %[swave], 1
s_lshl_b32 s77, s77, 9
s_add_u32 s77, s77, %[slds]
s_add_u32 s77, s77, 65536
v_add_u32 v193, s77, v252
v_add_u32 v197, 49344, v193
s_xor_b32 s77, %[swave], 2
s_lshl_b32 s77, s77, 9
s_add_u32 s77, s77, %[slds]
s_add_u32 s77, s77, 65536
v_add_u32 v194, s77, v252
v_add_u32 v198, 49344, v194
s_xor_b32 s77, %[swave], 3
s_lshl_b32 s77, s77, 9
s_add_u32 s77, s77, %[slds]
s_add_u32 s77, s77, 65536
v_add_u32 v195, s77, v252
v_add_u32 v199, 49344, v195
v_mov_b32 v236, 0x3f803f80
v_mov_b32 v184, 0
v_mov_b32 v237, 0x3f803f80
v_mov_b32 v185, 0
v_mov_b32 v238, 0x3f803f80
v_mov_b32 v188, 0
v_mov_b32 v239, 0x3f803f80
v_mov_b32 v189, 0
s_getpc_b64 s[98:99]
LTA0_%=:
s_lshl_b32 s73, %[swave], 12
s_add_u32 s73, s73, LPG1_%=-LTA0_%=
s_add_u32 s98, s98, s73
s_addc_u32 s99, s99, 0
s_mov_b64 exec, 0
s_setpc_b64 s[98:99]
LTR0_%=:
s_mov_b64 exec, -1
s_nop 0
s_bitcmp1_b32 %[sb], 7
s_cbranch_scc1 LCB_%=
s_add_u32 s88, s68, s86
s_add_u32 s86, s86, 128
s_cmp_ge_u32 s86, 1280
s_cselect_b32 s86, 0, s86
s_add_u32 m0, s94, 32768
s_add_u32 s95, s88, 0
buffer_load_dwordx4 v220, s[32:35], s95 offen offset:0 lds
s_add_u32 s95, s88, 9216
buffer_load_dwordx4 v221, s[32:35], s95 offen offset:1024 lds
s_add_u32 s95, s88, 18432
buffer_load_dwordx4 v220, s[32:35], s95 offen offset:2048 lds
s_add_u32 s95, s88, 27648
buffer_load_dwordx4 v221, s[32:35], s95 offen offset:3072 lds
s_add_u32 m0, s94, 36864
s_add_u32 s95, s88, 40960
buffer_load_dwordx4 v220, s[32:35], s95 offen offset:0 lds
s_add_u32 s95, s88, 50176
buffer_load_dwordx4 v221, s[32:35], s95 offen offset:1024 lds
s_add_u32 s95, s88, 59392
buffer_load_dwordx4 v220, s[32:35], s95 offen offset:2048 lds
s_add_u32 s95, s88, 68608
buffer_load_dwordx4 v221, s[32:35], s95 offen offset:3072 lds
s_branch LCM_%=
LCB_%=:
s_waitcnt vmcnt(5)
s_mov_b32 s61, 0x3acccccd
s_add_u32 s87, s89, s85
s_add_u32 s85, s85, 524288
s_cmp_ge_u32 s85, 5242880
s_cselect_b32 s85, 0, s85
s_add_u32 m0, s96, 65536
s_nop 0
buffer_load_dwordx4 v253, s[16:19], s87 offen offset:0 lds
s_lshl_b32 s60, s76, 1
s_sub_u32 s60, 65535, s60
v_lshlrev_b32 v0, 16, v128
v_and_b32 v1, 0xffff0000, v128
v_lshlrev_b32 v2, 16, v129
v_and_b32 v3, 0xffff0000, v129
v_lshlrev_b32 v4, 16, v130
v_and_b32 v5, 0xffff0000, v130
s_add_u32 m0, s96, 66560
s_nop 0
buffer_load_dwordx4 v254, s[16:19], s87 offen offset:0 lds
v_lshlrev_b32 v6, 16, v131
v_and_b32 v7, 0xffff0000, v131
v_lshlrev_b32 v8, 16, v132
v_and_b32 v9, 0xffff0000, v132
v_lshlrev_b32 v10, 16, v133
v_and_b32 v11, 0xffff0000, v133
v_lshlrev_b32 v12, 16, v134
v_and_b32 v13, 0xffff0000, v134
s_add_u32 s87, s87, 65536
s_add_u32 m0, s96, 67592
s_nop 0
buffer_load_dwordx4 v253, s[16:19], s87 offen offset:0 lds
v_lshlrev_b32 v14, 16, v135
v_and_b32 v15, 0xffff0000, v135
v_lshlrev_b32 v16, 16, v136
v_and_b32 v17, 0xffff0000, v136
v_lshlrev_b32 v18, 16, v137
v_and_b32 v19, 0xffff0000, v137
v_lshlrev_b32 v20, 16, v138
v_and_b32 v21, 0xffff0000, v138
s_add_u32 m0, s96, 68616
s_nop 0
buffer_load_dwordx4 v254, s[16:19], s87 offen offset:0 lds
v_lshlrev_b32 v22, 16, v139
v_and_b32 v23, 0xffff0000, v139
v_lshlrev_b32 v24, 16, v140
v_and_b32 v25, 0xffff0000, v140
v_lshlrev_b32 v26, 16, v141
v_and_b32 v27, 0xffff0000, v141
v_lshlrev_b32 v28, 16, v142
v_and_b32 v29, 0xffff0000, v142
s_add_u32 s88, s68, s86
s_add_u32 s86, s86, 128
s_cmp_ge_u32 s86, 1280
s_cselect_b32 s86, 0, s86
s_add_u32 m0, s94, 0
s_add_u32 s95, s88, 0
buffer_load_dwordx4 v220, s[32:35], s95 offen offset:0 lds
v_lshlrev_b32 v30, 16, v143
v_and_b32 v31, 0xffff0000, v143
v_lshlrev_b32 v32, 16, v144
v_and_b32 v33, 0xffff0000, v144
v_lshlrev_b32 v34, 16, v145
v_and_b32 v35, 0xffff0000, v145
v_lshlrev_b32 v36, 16, v146
v_and_b32 v37, 0xffff0000, v146
s_add_u32 s95, s88, 9216
buffer_load_dwordx4 v221, s[32:35], s95 offen offset:1024 lds
v_lshlrev_b32 v38, 16, v147
v_and_b32 v39, 0xffff0000, v147
v_pk_add_f32 v[40:41], v[0:1], v[2:3]
v_pk_add_f32 v[42:43], v[4:5], v[6:7]
v_pk_add_f32 v[40:41], v[40:41], v[8:9]
v_pk_add_f32 v[42:43], v[42:43], v[10:11]
v_pk_add_f32 v[40:41], v[40:41], v[12:13]
v_pk_add_f32 v[42:43], v[42:43], v[14:15]
s_add_u32 s95, s88, 18432
buffer_load_dwordx4 v220, s[32:35], s95 offen offset:2048 lds
v_pk_add_f32 v[40:41], v[40:41], v[16:17]
v_pk_add_f32 v[42:43], v[42:43], v[18:19]
v_pk_add_f32 v[40:41], v[40:41], v[20:21]
v_pk_add_f32 v[42:43], v[42:43], v[22:23]
v_pk_add_f32 v[40:41], v[40:41], v[24:25]
v_pk_add_f32 v[42:43], v[42:43], v[26:27]
v_pk_add_f32 v[40:41], v[40:41], v[28:29]
v_pk_add_f32 v[42:43], v[42:43], v[30:31]
s_add_u32 s95, s88, 27648
buffer_load_dwordx4 v221, s[32:35], s95 offen offset:3072 lds
v_pk_add_f32 v[40:41], v[40:41], v[32:33]
v_pk_add_f32 v[42:43], v[42:43], v[34:35]
v_pk_add_f32 v[40:41], v[40:41], v[36:37]
v_pk_add_f32 v[42:43], v[42:43], v[38:39]
v_pk_add_f32 v[40:41], v[40:41], v[42:43]
v_add_f32 v48, v40, v41
s_nop 1
v_add_f32_dpp v48, v48, v48 row_ror:8 row_mask:0xf bank_mask:0xf
s_nop 1
v_add_f32_dpp v48, v48, v48 row_ror:4 row_mask:0xf bank_mask:0xf
s_add_u32 m0, s94, 4096
s_add_u32 s95, s88, 40960
buffer_load_dwordx4 v220, s[32:35], s95 offen offset:0 lds
v_add_f32_dpp v48, v48, v48 row_ror:2 row_mask:0xf bank_mask:0xf
s_nop 1
v_add_f32_dpp v48, v48, v48 row_ror:1 row_mask:0xf bank_mask:0xf
v_mul_f32 v48, 0x3acccccd, v48
CV v44,v48,v48
v_and_b32 v44, 0xffff0000, v44
v_sub_f32 v48, v48, v44
v_pk_add_f32 v[0:1], v[0:1], v[44:45] op_sel_hi:[1,0] neg_lo:[0,1] neg_hi:[0,1]
v_pk_add_f32 v[2:3], v[2:3], v[44:45] op_sel_hi:[1,0] neg_lo:[0,1] neg_hi:[0,1]
s_add_u32 s95, s88, 50176
buffer_load_dwordx4 v221, s[32:35], s95 offen offset:1024 lds
v_pk_add_f32 v[4:5], v[4:5], v[44:45] op_sel_hi:[1,0] neg_lo:[0,1] neg_hi:[0,1]
v_pk_add_f32 v[6:7], v[6:7], v[44:45] op_sel_hi:[1,0] neg_lo:[0,1] neg_hi:[0,1]
v_pk_add_f32 v[8:9], v[8:9], v[44:45] op_sel_hi:[1,0] neg_lo:[0,1] neg_hi:[0,1]
v_pk_add_f32 v[10:11], v[10:11], v[44:45] op_sel_hi:[1,0] neg_lo:[0,1] neg_hi:[0,1]
v_pk_add_f32 v[12:13], v[12:13], v[44:45] op_sel_hi:[1,0] neg_lo:[0,1] neg_hi:[0,1]
)ASM"
R"ASM(
v_pk_add_f32 v[14:15], v[14:15], v[44:45] op_sel_hi:[1,0] neg_lo:[0,1] neg_hi:[0,1]
v_pk_add_f32 v[16:17], v[16:17], v[44:45] op_sel_hi:[1,0] neg_lo:[0,1] neg_hi:[0,1]
v_pk_add_f32 v[18:19], v[18:19], v[44:45] op_sel_hi:[1,0] neg_lo:[0,1] neg_hi:[0,1]
s_add_u32 s95, s88, 59392
buffer_load_dwordx4 v220, s[32:35], s95 offen offset:2048 lds
v_pk_add_f32 v[20:21], v[20:21], v[44:45] op_sel_hi:[1,0] neg_lo:[0,1] neg_hi:[0,1]
v_pk_add_f32 v[22:23], v[22:23], v[44:45] op_sel_hi:[1,0] neg_lo:[0,1] neg_hi:[0,1]
v_pk_add_f32 v[24:25], v[24:25], v[44:45] op_sel_hi:[1,0] neg_lo:[0,1] neg_hi:[0,1]
v_pk_add_f32 v[26:27], v[26:27], v[44:45] op_sel_hi:[1,0] neg_lo:[0,1] neg_hi:[0,1]
v_pk_add_f32 v[28:29], v[28:29], v[44:45] op_sel_hi:[1,0] neg_lo:[0,1] neg_hi:[0,1]
v_pk_add_f32 v[30:31], v[30:31], v[44:45] op_sel_hi:[1,0] neg_lo:[0,1] neg_hi:[0,1]
v_pk_add_f32 v[32:33], v[32:33], v[44:45] op_sel_hi:[1,0] neg_lo:[0,1] neg_hi:[0,1]
v_pk_add_f32 v[34:35], v[34:35], v[44:45] op_sel_hi:[1,0] neg_lo:[0,1] neg_hi:[0,1]
s_add_u32 s95, s88, 68608
buffer_load_dwordx4 v221, s[32:35], s95 offen offset:3072 lds
v_pk_add_f32 v[36:37], v[36:37], v[44:45] op_sel_hi:[1,0] neg_lo:[0,1] neg_hi:[0,1]
v_pk_add_f32 v[38:39], v[38:39], v[44:45] op_sel_hi:[1,0] neg_lo:[0,1] neg_hi:[0,1]
v_pk_mul_f32 v[40:41], v[0:1], v[0:1]
v_pk_mul_f32 v[42:43], v[2:3], v[2:3]
v_pk_fma_f32 v[40:41], v[4:5], v[4:5], v[40:41]
v_pk_fma_f32 v[42:43], v[6:7], v[6:7], v[42:43]
v_pk_fma_f32 v[40:41], v[8:9], v[8:9], v[40:41]
v_pk_fma_f32 v[42:43], v[10:11], v[10:11], v[42:43]
s_add_u32 s87, s89, s85
s_add_u32 s85, s85, 524288
s_cmp_ge_u32 s85, 5242880
s_cselect_b32 s85, 0, s85
s_add_u32 m0, s96, 81984
s_nop 0
buffer_load_dwordx4 v253, s[16:19], s87 offen offset:0 lds
v_pk_fma_f32 v[40:41], v[12:13], v[12:13], v[40:41]
v_pk_fma_f32 v[42:43], v[14:15], v[14:15], v[42:43]
v_pk_fma_f32 v[40:41], v[16:17], v[16:17], v[40:41]
v_pk_fma_f32 v[42:43], v[18:19], v[18:19], v[42:43]
v_pk_fma_f32 v[40:41], v[20:21], v[20:21], v[40:41]
v_pk_fma_f32 v[42:43], v[22:23], v[22:23], v[42:43]
v_pk_fma_f32 v[40:41], v[24:25], v[24:25], v[40:41]
v_pk_fma_f32 v[42:43], v[26:27], v[26:27], v[42:43]
s_add_u32 m0, s96, 83008
s_nop 0
buffer_load_dwordx4 v254, s[16:19], s87 offen offset:0 lds
v_pk_fma_f32 v[40:41], v[28:29], v[28:29], v[40:41]
v_pk_fma_f32 v[42:43], v[30:31], v[30:31], v[42:43]
v_pk_fma_f32 v[40:41], v[32:33], v[32:33], v[40:41]
v_pk_fma_f32 v[42:43], v[34:35], v[34:35], v[42:43]
v_pk_fma_f32 v[40:41], v[36:37], v[36:37], v[40:41]
v_pk_fma_f32 v[42:43], v[38:39], v[38:39], v[42:43]
v_mul_f32 v48, v48, v48
v_pk_add_f32 v[40:41], v[40:41], v[42:43]
s_add_u32 s87, s87, 65536
s_add_u32 m0, s96, 84040
s_nop 0
buffer_load_dwordx4 v253, s[16:19], s87 offen offset:0 lds
v_add_f32 v49, v40, v41
CV v128,v0,v1
CV v129,v2,v3
v_add_f32_dpp v49, v49, v49 row_ror:8 row_mask:0xf bank_mask:0xf
CV v130,v4,v5
CV v131,v6,v7
v_add_f32_dpp v49, v49, v49 row_ror:4 row_mask:0xf bank_mask:0xf
CV v132,v8,v9
s_add_u32 m0, s96, 85064
s_nop 0
buffer_load_dwordx4 v254, s[16:19], s87 offen offset:0 lds
CV v133,v10,v11
v_add_f32_dpp v49, v49, v49 row_ror:2 row_mask:0xf bank_mask:0xf
CV v134,v12,v13
CV v135,v14,v15
v_add_f32_dpp v49, v49, v49 row_ror:1 row_mask:0xf bank_mask:0xf
v_fma_f32 v49, v49, s61, -v48
CV v136,v16,v17
v_max_f32 v49, 0, v49
s_add_u32 s88, s68, s86
s_add_u32 s86, s86, 128
s_cmp_ge_u32 s86, 1280
s_cselect_b32 s86, 0, s86
s_add_u32 m0, s94, 32768
s_add_u32 s95, s88, 0
buffer_load_dwordx4 v220, s[32:35], s95 offen offset:0 lds
CV v137,v18,v19
CV v49,v49,v49
CV v138,v20,v21
v_and_b32 v49, 0xffff0000, v49
CV v139,v22,v23
v_add_f32 v49, 0x3727c5ac, v49
CV v140,v24,v25
CV v49,v49,v49
s_add_u32 s95, s88, 9216
buffer_load_dwordx4 v221, s[32:35], s95 offen offset:1024 lds
CV v141,v26,v27
v_and_b32 v49, 0xffff0000, v49
CV v142,v28,v29
v_sqrt_f32 v49, v49
CV v143,v30,v31
CV v178,v44,v49
CV v144,v32,v33
v_and_b32 v49, 0xffff0000, v178
s_add_u32 s95, s88, 18432
buffer_load_dwordx4 v220, s[32:35], s95 offen offset:2048 lds
CV v145,v34,v35
v_rcp_f32 v46, v49
CV v146,v36,v37
CV v147,v38,v39
v_lshlrev_b32 v0, 16, v128
v_and_b32 v1, 0xffff0000, v128
v_lshlrev_b32 v2, 16, v129
v_and_b32 v3, 0xffff0000, v129
s_add_u32 s95, s88, 27648
buffer_load_dwordx4 v221, s[32:35], s95 offen offset:3072 lds
v_lshlrev_b32 v4, 16, v130
v_and_b32 v5, 0xffff0000, v130
v_lshlrev_b32 v6, 16, v131
v_and_b32 v7, 0xffff0000, v131
v_lshlrev_b32 v8, 16, v132
v_and_b32 v9, 0xffff0000, v132
v_lshlrev_b32 v10, 16, v133
v_and_b32 v11, 0xffff0000, v133
s_add_u32 m0, s94, 36864
s_add_u32 s95, s88, 40960
buffer_load_dwordx4 v220, s[32:35], s95 offen offset:0 lds
v_lshlrev_b32 v12, 16, v134
v_and_b32 v13, 0xffff0000, v134
v_lshlrev_b32 v14, 16, v135
v_and_b32 v15, 0xffff0000, v135
v_lshlrev_b32 v16, 16, v136
v_and_b32 v17, 0xffff0000, v136
v_lshlrev_b32 v18, 16, v137
v_and_b32 v19, 0xffff0000, v137
s_add_u32 s95, s88, 50176
buffer_load_dwordx4 v221, s[32:35], s95 offen offset:1024 lds
v_lshlrev_b32 v20, 16, v138
v_and_b32 v21, 0xffff0000, v138
v_lshlrev_b32 v22, 16, v139
v_and_b32 v23, 0xffff0000, v139
v_lshlrev_b32 v24, 16, v140
v_and_b32 v25, 0xffff0000, v140
v_lshlrev_b32 v26, 16, v141
v_and_b32 v27, 0xffff0000, v141
s_add_u32 s95, s88, 59392
buffer_load_dwordx4 v220, s[32:35], s95 offen offset:2048 lds
v_lshlrev_b32 v28, 16, v142
v_and_b32 v29, 0xffff0000, v142
v_lshlrev_b32 v30, 16, v143
v_and_b32 v31, 0xffff0000, v143
v_lshlrev_b32 v32, 16, v144
v_and_b32 v33, 0xffff0000, v144
v_lshlrev_b32 v34, 16, v145
v_and_b32 v35, 0xffff0000, v145
s_add_u32 s95, s88, 68608
buffer_load_dwordx4 v221, s[32:35], s95 offen offset:3072 lds
v_lshlrev_b32 v36, 16, v146
v_and_b32 v37, 0xffff0000, v146
v_lshlrev_b32 v38, 16, v147
v_and_b32 v39, 0xffff0000, v147
v_pk_mul_f32 v[0:1], v[0:1], v[46:47] op_sel_hi:[1,0]
v_pk_mul_f32 v[2:3], v[2:3], v[46:47] op_sel_hi:[1,0]
v_pk_mul_f32 v[4:5], v[4:5], v[46:47] op_sel_hi:[1,0]
v_pk_mul_f32 v[6:7], v[6:7], v[46:47] op_sel_hi:[1,0]
v_pk_mul_f32 v[8:9], v[8:9], v[46:47] op_sel_hi:[1,0]
v_pk_mul_f32 v[10:11], v[10:11], v[46:47] op_sel_hi:[1,0]
v_pk_mul_f32 v[12:13], v[12:13], v[46:47] op_sel_hi:[1,0]
v_pk_mul_f32 v[14:15], v[14:15], v[46:47] op_sel_hi:[1,0]
v_pk_mul_f32 v[16:17], v[16:17], v[46:47] op_sel_hi:[1,0]
v_pk_mul_f32 v[18:19], v[18:19], v[46:47] op_sel_hi:[1,0]
v_pk_mul_f32 v[20:21], v[20:21], v[46:47] op_sel_hi:[1,0]
v_pk_mul_f32 v[22:23], v[22:23], v[46:47] op_sel_hi:[1,0]
v_pk_mul_f32 v[24:25], v[24:25], v[46:47] op_sel_hi:[1,0]
v_pk_mul_f32 v[26:27], v[26:27], v[46:47] op_sel_hi:[1,0]
v_pk_mul_f32 v[28:29], v[28:29], v[46:47] op_sel_hi:[1,0]
v_pk_mul_f32 v[30:31], v[30:31], v[46:47] op_sel_hi:[1,0]
v_pk_mul_f32 v[32:33], v[32:33], v[46:47] op_sel_hi:[1,0]
v_pk_mul_f32 v[34:35], v[34:35], v[46:47] op_sel_hi:[1,0]
v_pk_mul_f32 v[36:37], v[36:37], v[46:47] op_sel_hi:[1,0]
v_pk_mul_f32 v[38:39], v[38:39], v[46:47] op_sel_hi:[1,0]
CV v128,v0,v1
CV v129,v2,v3
CV v130,v4,v5
CV v131,v6,v7
CV v132,v8,v9
CV v133,v10,v11
CV v134,v12,v13
CV v135,v14,v15
CV v136,v16,v17
CV v137,v18,v19
CV v138,v20,v21
CV v139,v22,v23
CV v140,v24,v25
CV v141,v26,v27
CV v142,v28,v29
CV v143,v30,v31
CV v144,v32,v33
CV v145,v34,v35
CV v146,v36,v37
CV v147,v38,v39
s_waitcnt vmcnt(24)
v_dot2_f32_bf16 v40, v128, v100, 0
v_dot2_f32_bf16 v41, v129, v101, 0
v_dot2_f32_bf16 v42, v130, v102, 0
v_dot2_f32_bf16 v43, v131, v103, 0
v_dot2_f32_bf16 v40, v132, v104, v40
v_dot2_f32_bf16 v41, v133, v105, v41
v_dot2_f32_bf16 v42, v134, v106, v42
v_dot2_f32_bf16 v43, v135, v107, v43
v_dot2_f32_bf16 v40, v136, v108, v40
v_dot2_f32_bf16 v41, v137, v109, v41
v_dot2_f32_bf16 v42, v138, v110, v42
v_dot2_f32_bf16 v43, v139, v111, v43
v_dot2_f32_bf16 v40, v140, v112, v40
v_dot2_f32_bf16 v41, v141, v113, v41
v_dot2_f32_bf16 v42, v142, v114, v42
v_dot2_f32_bf16 v43, v143, v115, v43
v_dot2_f32_bf16 v40, v144, v116, v40
v_dot2_f32_bf16 v41, v145, v117, v41
v_dot2_f32_bf16 v42, v146, v118, v42
v_dot2_f32_bf16 v43, v147, v119, v43
s_nop 2
v_pk_add_f32 v[40:41], v[40:41], v[42:43]
v_add_f32 v160, v40, v41
s_nop 1
v_add_f32_dpp v160, v160, v160 row_ror:8 row_mask:0xf bank_mask:0xf
s_nop 1
v_add_f32_dpp v160, v160, v160 row_ror:4 row_mask:0xf bank_mask:0xf
s_nop 1
v_add_f32_dpp v160, v160, v160 row_ror:2 row_mask:0xf bank_mask:0xf
s_nop 1
v_add_f32_dpp v160, v160, v160 row_ror:1 row_mask:0xf bank_mask:0xf
CV v160,v160,v160
v_ashrrev_i32 v50, 31, v160
v_or_b32 v50, 0x80000000, v50
v_xor_b32 v160, v160, v50
v_and_b32 v160, 0xffff0000, v160
v_sub_u32 v50, s60, v181
v_or_b32 v160, v160, v50
LCM_%=:
s_waitcnt vmcnt(12)
LPG1_%=:
s_cbranch_execz LPG5_%=
RD a[128:131],v222 offset:0
RD a[132:135],v223 offset:0
RD a[136:139],v217 offset:0
RD a[140:143],v218 offset:0
RD a[144:147],v222 offset:4096
RD a[148:151],v223 offset:4096
RD a[152:155],v217 offset:4096
RD a[156:159],v218 offset:4096
s_barrier
TR a[0:1],v192 offset:0
TR a[2:3],v192 offset:256
TR a[4:5],v192 offset:8224
TR a[6:7],v192 offset:8480
TR a[8:9],v192 offset:8
TR a[10:11],v192 offset:264
TR a[12:13],v192 offset:8232
TR a[14:15],v192 offset:8488
s_waitcnt lgkmcnt(8)
s_waitcnt lgkmcnt(4)
MM v[0:3],a[0:3],a[128:131],0
s_mov_b32 s66, 32768
TR a[16:17],v193 offset:0
TR a[18:19],v193 offset:256
MM v[0:3],a[4:7],a[132:135],v[0:3]
TR a[20:21],v193 offset:8224
TR a[22:23],v193 offset:8480
MM v[8:11],a[4:7],a[140:143],0
buffer_load_dword v169, off, s[20:23], s66 sc1
MM v[8:11],a[0:3],a[136:139],v[8:11]
s_add_u32 s82, s82, 131072
TR a[24:25],v193 offset:8
TR a[26:27],v193 offset:264
MM v[16:19],a[0:3],a[144:147],0
s_add_u32 s87, s89, s85
TR a[28:29],v193 offset:8232
TR a[30:31],v193 offset:8488
MM v[16:19],a[4:7],a[148:151],v[16:19]
s_add_u32 s85, s85, 524288
TR a[32:33],v194 offset:0
TR a[34:35],v194 offset:256
MM v[24:27],a[4:7],a[156:159],0
s_lshl_b32 s90, s76, 1
TR a[36:37],v194 offset:8224
TR a[38:39],v194 offset:8480
MM v[24:27],a[0:3],a[152:155],v[24:27]
s_waitcnt lgkmcnt(12)
TR a[40:41],v194 offset:8
TR a[42:43],v194 offset:264
s_cmp_ge_u32 s85, 5242880
s_cselect_b32 s85, 0, s85
MM v[4:7],a[8:11],a[128:131],0
s_add_u32 m0, s96, 98432
TR a[44:45],v194 offset:8232
TR a[46:47],v194 offset:8488
MM v[4:7],a[12:15],a[132:135],v[4:7]
TR a[48:49],v195 offset:0
TR a[50:51],v195 offset:256
MM v[12:15],a[12:15],a[140:143],0
buffer_load_dwordx4 v253, s[16:19], s87 offen offset:0 lds
MM v[12:15],a[8:11],a[136:139],v[12:15]
s_add_u32 m0, s96, 99456
TR a[52:53],v195 offset:8224
TR a[54:55],v195 offset:8480
MM v[20:23],a[8:11],a[144:147],0
TR a[56:57],v195 offset:8
TR a[58:59],v195 offset:264
MM v[20:23],a[12:15],a[148:151],v[20:23]
buffer_load_dwordx4 v254, s[16:19], s87 offen offset:0 lds
MM v[28:31],a[12:15],a[156:159],0
s_add_u32 s87, s87, 65536
TR a[60:61],v195 offset:8232
TR a[62:63],v195 offset:8488
MM v[28:31],a[8:11],a[152:155],v[28:31]
s_add_u32 m0, s96, 100488
s_and_b32 s77, s90, 15
s_lshl_b32 s77, s77, 1
v_smfmac_f32_16x16x64_bf16 v[184:187], v[236:239], a[0:7], v234
s_bfe_u32 s91, s90, 65540
s_add_u32 s77, s77, s91
v_smfmac_f32_16x16x64_bf16 v[188:191], v[236:239], a[8:15], v234
s_waitcnt lgkmcnt(15)
s_and_b32 s91, s90, 0xffffffe0
MM v[32:35],a[16:19],a[128:131],0
buffer_load_dwordx4 v253, s[16:19], s87 offen offset:0 lds
MM v[32:35],a[20:23],a[132:135],v[32:35]
s_add_u32 m0, s96, 101512
s_add_u32 s77, s77, s91
s_lshl_b32 s77, s77, 3
MM v[40:43],a[20:23],a[140:143],0
s_add_u32 s97, s77, 36864
s_sub_u32 s79, 65535, s90
s_waitcnt vmcnt(12)
s_barrier
MM v[40:43],a[16:19],a[136:139],v[40:43]
TR a[0:1],v192 offset:16448
TR a[2:3],v192 offset:16704
MM v[48:51],a[16:19],a[144:147],0
buffer_load_dwordx4 v254, s[16:19], s87 offen offset:0 lds
MM v[48:51],a[20:23],a[148:151],v[48:51]
TR a[4:5],v192 offset:24672
TR a[6:7],v192 offset:24928
MM v[56:59],a[20:23],a[156:159],0
s_add_u32 s88, s68, s86
TR a[8:9],v192 offset:16456
TR a[10:11],v192 offset:16712
MM v[56:59],a[16:19],a[152:155],v[56:59]
s_add_u32 s86, s86, 128
TR a[12:13],v192 offset:24680
TR a[14:15],v192 offset:24936
MM v[36:39],a[24:27],a[128:131],0
s_lshl_b32 s78, s90, 2
TR a[16:17],v193 offset:16448
TR a[18:19],v193 offset:16704
MM v[36:39],a[28:31],a[132:135],v[36:39]
)ASM"
R"ASM(
s_add_u32 s78, s78, 53248
TR a[20:21],v193 offset:24672
TR a[22:23],v193 offset:24928
s_cmp_ge_u32 s86, 1280
s_cselect_b32 s86, 0, s86
MM v[44:47],a[28:31],a[140:143],0
s_add_u32 m0, s94, 0
s_add_u32 s95, s88, 0
s_lshl_b32 s80, s76, 2
MM v[44:47],a[24:27],a[136:139],v[44:47]
buffer_load_dwordx4 v220, s[32:35], s95 offen offset:0 lds
MM v[52:55],a[24:27],a[144:147],0
s_add_u32 s95, s88, 9216
s_andn2_b32 s91, 8192, s52
s_add_u32 s80, s80, s91
MM v[52:55],a[28:31],a[148:151],v[52:55]
buffer_load_dwordx4 v221, s[32:35], s95 offen offset:1024 lds
MM v[60:63],a[28:31],a[156:159],0
s_add_u32 s95, s88, 18432
s_add_u32 s80, s80, 81920
v_lshlrev_b32 v232, 11, v180
MM v[60:63],a[24:27],a[152:155],v[60:63]
s_waitcnt lgkmcnt(15)
v_lshl_add_u32 v232, v214, 8, v232
v_lshl_add_u32 v232, v215, 6, v232
MM v[64:67],a[32:35],a[128:131],0
TR a[24:25],v193 offset:16456
TR a[26:27],v193 offset:16712
MM v[64:67],a[36:39],a[132:135],v[64:67]
buffer_load_dwordx4 v220, s[32:35], s95 offen offset:2048 lds
MM v[72:75],a[36:39],a[140:143],0
s_add_u32 s95, s88, 27648
TR a[28:29],v193 offset:24680
TR a[30:31],v193 offset:24936
MM v[72:75],a[32:35],a[136:139],v[72:75]
v_lshl_add_u32 v232, v216, 3, v232
v_lshl_add_u32 v232, v214, 5, v232
MM v[80:83],a[32:35],a[144:147],0
buffer_load_dwordx4 v221, s[32:35], s95 offen offset:3072 lds
MM v[80:83],a[36:39],a[148:151],v[80:83]
s_waitcnt vmcnt(9)
RD a[160:163],v222 offset:32768
v_xor_b32 v233, 32, v232
MM v[88:91],a[36:39],a[156:159],0
s_add_u32 s77, %[slds], 65536
RD a[164:167],v223 offset:32768
v_add_u32 v232, s77, v232
MM v[88:91],a[32:35],a[152:155],v[88:91]
RD a[168:171],v217 offset:32768
v_add_u32 v233, s77, v233
MM v[68:71],a[40:43],a[128:131],0
TR a[32:33],v194 offset:16448
TR a[34:35],v194 offset:16704
MM v[68:71],a[44:47],a[132:135],v[68:71]
s_waitcnt vmcnt(8)
TR a[36:37],v194 offset:24672
TR a[38:39],v194 offset:24928
MM v[76:79],a[44:47],a[140:143],0
RD a[172:175],v218 offset:32768
v_readfirstlane_b32 s69, v169
MM v[76:79],a[40:43],a[136:139],v[76:79]
RD a[176:179],v222 offset:36864
v_lshlrev_b32 v173, 4, v181
MM v[84:87],a[40:43],a[144:147],0
s_add_u32 s69, s69, 1
RD a[180:183],v223 offset:36864
v_mov_b32 v179, s69
MM v[84:87],a[44:47],a[148:151],v[84:87]
RD a[184:187],v217 offset:36864
v_lshlrev_b32 v214, 8, v211
MM v[92:95],a[44:47],a[156:159],0
RD a[188:191],v218 offset:36864
v_add_u32 v214, %[slds], v214
MM v[92:95],a[40:43],a[152:155],v[92:95]
s_add_u32 m0, s94, 4096
s_add_u32 s95, s88, 40960
s_mov_b64 exec, s[54:55]
buffer_store_dwordx2 v[178:179], v173, s[20:23], s97 offen offset:0 sc1
s_mov_b64 exec, -1
v_xor_b32 v215, v211, v212
MM v[96:99],a[48:51],a[128:131],0
TR a[40:41],v194 offset:16456
TR a[42:43],v194 offset:16712
MM v[96:99],a[52:55],a[132:135],v[96:99]
buffer_load_dwordx4 v220, s[32:35], s95 offen offset:0 lds
MM v[104:107],a[52:55],a[140:143],0
s_add_u32 s95, s88, 50176
TR a[44:45],v194 offset:24680
TR a[46:47],v194 offset:24936
MM v[104:107],a[48:51],a[136:139],v[104:107]
buffer_load_dwordx4 v221, s[32:35], s95 offen offset:1024 lds
MM v[112:115],a[48:51],a[144:147],0
s_add_u32 s95, s88, 59392
s_lshl_b32 s77, %[swave], 2
v_xor_b32 v213, s77, v215
MM v[112:115],a[52:55],a[148:151],v[112:115]
v_xor_b32 v216, 0, v213
v_lshl_add_u32 v224, v216, 4, v214
MM v[120:123],a[52:55],a[156:159],0
buffer_load_dwordx4 v220, s[32:35], s95 offen offset:2048 lds
MM v[120:123],a[48:51],a[152:155],v[120:123]
s_waitcnt lgkmcnt(15)
s_add_u32 s95, s88, 68608
v_xor_b32 v216, 4, v213
MM v[100:103],a[56:59],a[128:131],0
TR a[48:49],v195 offset:16448
TR a[50:51],v195 offset:16704
MM v[100:103],a[60:63],a[132:135],v[100:103]
TR a[52:53],v195 offset:24672
TR a[54:55],v195 offset:24928
MM v[108:111],a[60:63],a[140:143],0
buffer_load_dwordx4 v221, s[32:35], s95 offen offset:3072 lds
MM v[108:111],a[56:59],a[136:139],v[108:111]
s_add_u32 s87, s89, s85
s_add_u32 s85, s85, 524288
v_lshl_add_u32 v225, v216, 4, v214
MM v[116:119],a[56:59],a[144:147],0
s_cmp_ge_u32 s85, 5242880
s_cselect_b32 s85, 0, s85
s_add_u32 m0, s96, 114880
MM v[116:119],a[60:63],a[148:151],v[116:119]
v_xor_b32 v216, 8, v213
v_lshl_add_u32 v226, v216, 4, v214
MM v[124:127],a[60:63],a[156:159],0
buffer_load_dwordx4 v253, s[16:19], s87 offen offset:0 lds
MM v[124:127],a[56:59],a[152:155],v[124:127]
s_add_u32 m0, s96, 115904
v_xor_b32 v216, 12, v213
v_lshl_add_u32 v227, v216, 4, v214
s_waitcnt vmcnt(10) lgkmcnt(8)
s_barrier
MM v[0:3],a[0:3],a[160:163],v[0:3]
TR a[56:57],v195 offset:16456
TR a[58:59],v195 offset:16712
MM v[0:3],a[4:7],a[164:167],v[0:3]
TR a[60:61],v195 offset:24680
TR a[62:63],v195 offset:24936
MM v[8:11],a[4:7],a[172:175],v[8:11]
buffer_load_dwordx4 v254, s[16:19], s87 offen offset:0 lds
MM v[8:11],a[0:3],a[168:171],v[8:11]
s_add_u32 s87, s87, 65536
s_add_u32 m0, s96, 116936
v_lshlrev_b32 v214, 8, v212
MM v[16:19],a[0:3],a[176:179],v[16:19]
v_xor_b32 v216, 0, v215
v_lshl_add_u32 v228, v216, 4, v214
MM v[16:19],a[4:7],a[180:183],v[16:19]
v_xor_b32 v216, 4, v215
v_lshl_add_u32 v229, v216, 4, v214
MM v[24:27],a[4:7],a[188:191],v[24:27]
buffer_load_dwordx4 v253, s[16:19], s87 offen offset:0 lds
MM v[24:27],a[0:3],a[184:187],v[24:27]
s_add_u32 m0, s96, 117960
v_xor_b32 v216, 8, v215
v_lshl_add_u32 v230, v216, 4, v214
MM v[4:7],a[8:11],a[160:163],v[4:7]
v_xor_b32 v216, 12, v215
v_lshl_add_u32 v231, v216, 4, v214
MM v[4:7],a[12:15],a[164:167],v[4:7]
s_lshl_b32 s90, s49, 19
s_add_u32 s8, s8, s90
s_addc_u32 s9, s9, 0
MM v[12:15],a[12:15],a[172:175],v[12:15]
buffer_load_dwordx4 v254, s[16:19], s87 offen offset:0 lds
MM v[12:15],a[8:11],a[168:171],v[12:15]
s_mov_b32 s10, 524288
s_mov_b32 s11, 131072
MM v[20:23],a[8:11],a[176:179],v[20:23]
s_add_u32 s12, s12, s90
s_addc_u32 s13, s13, 0
s_mov_b32 s14, 525312
MM v[20:23],a[12:15],a[180:183],v[20:23]
s_add_u32 s88, s68, s86
s_add_u32 s86, s86, 128
MM v[28:31],a[12:15],a[188:191],v[28:31]
s_mov_b32 s15, 131072
s_cmp_ge_u32 s86, 1280
s_cselect_b32 s86, 0, s86
MM v[28:31],a[8:11],a[184:187],v[28:31]
s_add_u32 m0, s94, 32768
s_mov_b64 exec, s[52:53]
buffer_store_dwordx4 v[128:131], v168, s[20:23], s82 offen offset:0 sc1
s_mov_b64 exec, -1
v_smfmac_f32_16x16x64_bf16 v[184:187], v[236:239], a[0:7], v234
s_add_u32 s95, s88, 0
v_smfmac_f32_16x16x64_bf16 v[188:191], v[236:239], a[8:15], v234
s_sub_u32 s12, s12, 1024
s_subb_u32 s13, s13, 0
MM v[32:35],a[16:19],a[160:163],v[32:35]
TR a[0:1],v192 offset:32896
TR a[2:3],v192 offset:33152
MM v[32:35],a[20:23],a[164:167],v[32:35]
buffer_load_dwordx4 v220, s[32:35], s95 offen offset:0 lds
MM v[40:43],a[20:23],a[172:175],v[40:43]
s_add_u32 s95, s88, 9216
TR a[4:5],v192 offset:41120
TR a[6:7],v192 offset:41376
MM v[40:43],a[16:19],a[168:171],v[40:43]
s_lshr_b32 s90, s49, 2
TR a[8:9],v192 offset:32904
TR a[10:11],v192 offset:33160
MM v[48:51],a[16:19],a[176:179],v[48:51]
TR a[12:13],v192 offset:41128
TR a[14:15],v192 offset:41384
MM v[48:51],a[20:23],a[180:183],v[48:51]
buffer_load_dwordx4 v221, s[32:35], s95 offen offset:1024 lds
MM v[56:59],a[20:23],a[188:191],v[56:59]
s_add_u32 s95, s88, 18432
s_mul_i32 s90, s90, 655360
s_mul_i32 s77, s84, 320
MM v[56:59],a[16:19],a[184:187],v[56:59]
s_add_u32 s90, s90, s77
s_add_u32 s24, s24, s90
s_addc_u32 s25, s25, 0
MM v[36:39],a[24:27],a[160:163],v[36:39]
TR a[16:17],v193 offset:32896
TR a[18:19],v193 offset:33152
MM v[36:39],a[28:31],a[164:167],v[36:39]
buffer_load_dwordx4 v220, s[32:35], s95 offen offset:2048 lds
MM v[44:47],a[28:31],a[172:175],v[44:47]
s_add_u32 s95, s88, 27648
TR a[20:21],v193 offset:41120
TR a[22:23],v193 offset:41376
MM v[44:47],a[24:27],a[168:171],v[44:47]
s_sub_u32 s26, 5242880, s90
s_mov_b32 s27, 131072
s_sub_u32 s77, %[sb], 76
MM v[52:55],a[24:27],a[176:179],v[52:55]
buffer_load_dwordx4 v221, s[32:35], s95 offen offset:3072 lds
MM v[52:55],a[28:31],a[180:183],v[52:55]
s_add_u32 m0, s94, 36864
s_add_u32 s95, s88, 40960
s_min_u32 s47, s77, %[sb]
MM v[60:63],a[28:31],a[188:191],v[60:63]
s_mul_i32 s77, s47, 205
s_lshr_b32 s46, s77, 11
s_mul_i32 s90, s46, 121600
MM v[60:63],a[24:27],a[184:187],v[60:63]
s_lshl_b32 s77, s47, 7
s_add_u32 s90, s90, s77
MM v[64:67],a[32:35],a[160:163],v[64:67]
TR a[24:25],v193 offset:32904
TR a[26:27],v193 offset:33160
s_mov_b64 exec, s[52:53]
buffer_store_dwordx4 v[132:135], v168, s[20:23], s82 offen offset:256 sc1
s_mov_b64 exec, -1
MM v[64:67],a[36:39],a[164:167],v[64:67]
TR a[28:29],v193 offset:41128
TR a[30:31],v193 offset:41384
MM v[72:75],a[36:39],a[172:175],v[72:75]
s_add_u32 s28, s28, s90
s_addc_u32 s29, s29, 0
s_sub_u32 s30, 2211840, s90
MM v[72:75],a[32:35],a[168:171],v[72:75]
s_mov_b32 s31, 131072
s_mov_b32 s38, 2211840
s_mov_b32 s39, 131072
MM v[80:83],a[32:35],a[176:179],v[80:83]
buffer_load_dwordx4 v220, s[32:35], s95 offen offset:0 lds
MM v[80:83],a[36:39],a[180:183],v[80:83]
s_add_u32 s95, s88, 50176
s_mov_b32 s42, 2621440
s_mov_b32 s43, 131072
MM v[88:91],a[36:39],a[188:191],v[88:91]
s_lshr_b32 s77, %[sb], 6
s_lshl_b32 s77, s77, 3
s_and_b32 s90, %[sb], 7
MM v[88:91],a[32:35],a[184:187],v[88:91]
s_waitcnt lgkmcnt(15)
s_add_u32 s77, s77, s90
s_sub_u32 s90, s77, 8
MM v[68:71],a[40:43],a[160:163],v[68:71]
TR a[32:33],v194 offset:32896
TR a[34:35],v194 offset:33152
MM v[68:71],a[44:47],a[164:167],v[68:71]
buffer_load_dwordx4 v221, s[32:35], s95 offen offset:1024 lds
MM v[76:79],a[44:47],a[172:175],v[76:79]
s_add_u32 s95, s88, 59392
TR a[36:37],v194 offset:41120
TR a[38:39],v194 offset:41376
MM v[76:79],a[40:43],a[168:171],v[76:79]
s_cmp_ge_u32 s77, 27
s_cselect_b32 s77, s90, s77
s_lshl_b32 s77, s77, 7
MM v[84:87],a[40:43],a[176:179],v[84:87]
buffer_load_dwordx4 v220, s[32:35], s95 offen offset:2048 lds
MM v[84:87],a[44:47],a[180:183],v[84:87]
s_add_u32 s95, s88, 68608
s_lshl_b32 s90, %[sb], 1
s_and_b32 s90, s90, 112
MM v[92:95],a[44:47],a[188:191],v[92:95]
s_add_u32 s48, s77, s90
s_mov_b32 s71, 0x4e800000
s_mov_b32 s72, 0x3e0293ee
MM v[92:95],a[40:43],a[184:187],v[92:95]
s_lshl_b32 s64, %[swave], 12
MM v[96:99],a[48:51],a[160:163],v[96:99]
TR a[40:41],v194 offset:32904
TR a[42:43],v194 offset:33160
MM v[96:99],a[52:55],a[164:167],v[96:99]
buffer_load_dwordx4 v221, s[32:35], s95 offen offset:3072 lds
MM v[104:107],a[52:55],a[172:175],v[104:107]
s_add_u32 s87, s89, s85
TR a[44:45],v194 offset:41128
TR a[46:47],v194 offset:41384
MM v[104:107],a[48:51],a[168:171],v[104:107]
s_add_u32 s85, s85, 524288
s_cmp_ge_u32 s85, 5242880
s_cselect_b32 s85, 0, s85
MM v[112:115],a[48:51],a[176:179],v[112:115]
s_add_u32 m0, s96, 131328
s_mov_b64 exec, s[52:53]
buffer_store_dwordx4 v[136:139], v168, s[20:23], s82 offen offset:512 sc1
s_mov_b64 exec, -1
MM v[112:115],a[52:55],a[180:183],v[112:115]
MM v[120:123],a[52:55],a[188:191],v[120:123]
MM v[120:123],a[48:51],a[184:187],v[120:123]
MM v[100:103],a[56:59],a[160:163],v[100:103]
TR a[48:49],v195 offset:32896
TR a[50:51],v195 offset:33152
MM v[100:103],a[60:63],a[164:167],v[100:103]
buffer_load_dwordx4 v253, s[16:19], s87 offen offset:0 lds
MM v[108:111],a[60:63],a[172:175],v[108:111]
s_add_u32 m0, s96, 132352
TR a[52:53],v195 offset:41120
TR a[54:55],v195 offset:41376
MM v[108:111],a[56:59],a[168:171],v[108:111]
MM v[116:119],a[56:59],a[176:179],v[116:119]
buffer_load_dwordx4 v254, s[16:19], s87 offen offset:0 lds
MM v[116:119],a[60:63],a[180:183],v[116:119]
s_add_u32 s87, s87, 65536
s_add_u32 m0, s96, 133384
MM v[124:127],a[60:63],a[188:191],v[124:127]
MM v[124:127],a[56:59],a[184:187],v[124:127]
buffer_load_dwordx4 v253, s[16:19], s87 offen offset:0 lds
s_add_u32 m0, s96, 134408
s_nop 0
buffer_load_dwordx4 v254, s[16:19], s87 offen offset:0 lds
s_waitcnt vmcnt(19)
RD a[192:195],v222 offset:0
RD a[196:199],v223 offset:0
RD a[200:203],v217 offset:0
RD a[204:207],v218 offset:0
RD a[208:211],v222 offset:4096
RD a[212:215],v223 offset:4096
RD a[216:219],v217 offset:4096
RD a[220:223],v218 offset:4096
s_waitcnt vmcnt(15) lgkmcnt(0)
s_barrier
MM v[0:3],a[0:3],a[192:195],v[0:3]
TR a[56:57],v195 offset:32904
TR a[58:59],v195 offset:33160
MM v[0:3],a[4:7],a[196:199],v[0:3]
TR a[60:61],v195 offset:41128
TR a[62:63],v195 offset:41384
MM v[8:11],a[4:7],a[204:207],v[8:11]
s_mov_b64 exec, s[52:53]
)ASM"
R"ASM(
buffer_store_dwordx4 v[140:143], v168, s[20:23], s82 offen offset:768 sc1
s_mov_b64 exec, -1
MM v[8:11],a[0:3],a[200:203],v[8:11]
s_add_u32 s88, s68, s86
s_add_u32 s86, s86, 128
MM v[16:19],a[0:3],a[208:211],v[16:19]
s_cmp_ge_u32 s86, 1280
s_cselect_b32 s86, 0, s86
s_add_u32 m0, s94, 0
MM v[16:19],a[4:7],a[212:215],v[16:19]
s_add_u32 s95, s88, 0
MM v[24:27],a[4:7],a[220:223],v[24:27]
buffer_load_dwordx4 v220, s[32:35], s95 offen offset:0 lds
MM v[24:27],a[0:3],a[216:219],v[24:27]
s_add_u32 s95, s88, 9216
MM v[4:7],a[8:11],a[192:195],v[4:7]
buffer_load_dwordx4 v221, s[32:35], s95 offen offset:1024 lds
MM v[4:7],a[12:15],a[196:199],v[4:7]
s_add_u32 s95, s88, 18432
MM v[12:15],a[12:15],a[204:207],v[12:15]
MM v[12:15],a[8:11],a[200:203],v[12:15]
MM v[20:23],a[8:11],a[208:211],v[20:23]
buffer_load_dwordx4 v220, s[32:35], s95 offen offset:2048 lds
MM v[20:23],a[12:15],a[212:215],v[20:23]
s_add_u32 s95, s88, 27648
MM v[28:31],a[12:15],a[220:223],v[28:31]
MM v[28:31],a[8:11],a[216:219],v[28:31]
s_nop 0
v_smfmac_f32_16x16x64_bf16 v[184:187], v[236:239], a[0:7], v234
s_nop 0
v_smfmac_f32_16x16x64_bf16 v[188:191], v[236:239], a[8:15], v234
s_nop 0
MM v[32:35],a[16:19],a[192:195],v[32:35]
buffer_load_dwordx4 v221, s[32:35], s95 offen offset:3072 lds
MM v[32:35],a[20:23],a[196:199],v[32:35]
s_add_u32 m0, s94, 4096
TR a[0:1],v196 offset:0
TR a[2:3],v196 offset:256
MM v[40:43],a[20:23],a[204:207],v[40:43]
s_add_u32 s95, s88, 40960
TR a[4:5],v196 offset:8224
TR a[6:7],v196 offset:8480
s_mov_b64 exec, s[52:53]
buffer_store_dwordx4 v[144:147], v168, s[20:23], s82 offen offset:1024 sc1
s_mov_b64 exec, -1
MM v[40:43],a[16:19],a[200:203],v[40:43]
s_nop 0
TR a[8:9],v196 offset:8
TR a[10:11],v196 offset:264
MM v[48:51],a[16:19],a[208:211],v[48:51]
s_nop 0
TR a[12:13],v196 offset:8232
TR a[14:15],v196 offset:8488
MM v[48:51],a[20:23],a[212:215],v[48:51]
s_nop 0
MM v[56:59],a[20:23],a[220:223],v[56:59]
buffer_load_dwordx4 v220, s[32:35], s95 offen offset:0 lds
MM v[56:59],a[16:19],a[216:219],v[56:59]
s_add_u32 s95, s88, 50176
MM v[36:39],a[24:27],a[192:195],v[36:39]
s_nop 0
TR a[16:17],v197 offset:0
TR a[18:19],v197 offset:256
MM v[36:39],a[28:31],a[196:199],v[36:39]
s_nop 0
TR a[20:21],v197 offset:8224
TR a[22:23],v197 offset:8480
MM v[44:47],a[28:31],a[204:207],v[44:47]
buffer_load_dwordx4 v221, s[32:35], s95 offen offset:1024 lds
MM v[44:47],a[24:27],a[200:203],v[44:47]
s_add_u32 s95, s88, 59392
v_lshlrev_b32 v173, 2, v181
MM v[52:55],a[24:27],a[208:211],v[52:55]
s_mov_b64 exec, s[54:55]
buffer_store_dword v160, v173, s[20:23], s78 offen offset:0 sc1
s_mov_b64 exec, -1
MM v[52:55],a[28:31],a[212:215],v[52:55]
s_nop 0
v_mov_b32 v170, s69
v_mov_b32 v171, s69
MM v[60:63],a[28:31],a[220:223],v[60:63]
buffer_load_dwordx4 v220, s[32:35], s95 offen offset:2048 lds
MM v[60:63],a[24:27],a[216:219],v[60:63]
s_add_u32 s95, s88, 68608
MM v[64:67],a[32:35],a[192:195],v[64:67]
s_nop 0
TR a[24:25],v197 offset:8
TR a[26:27],v197 offset:264
MM v[64:67],a[36:39],a[196:199],v[64:67]
s_nop 0
TR a[28:29],v197 offset:8232
TR a[30:31],v197 offset:8488
MM v[72:75],a[36:39],a[204:207],v[72:75]
buffer_load_dwordx4 v221, s[32:35], s95 offen offset:3072 lds
MM v[72:75],a[32:35],a[200:203],v[72:75]
s_add_u32 s87, s89, s85
s_add_u32 s85, s85, 524288
MM v[80:83],a[32:35],a[208:211],v[80:83]
s_cmp_ge_u32 s85, 5242880
s_cselect_b32 s85, 0, s85
s_add_u32 m0, s96, 65536
MM v[80:83],a[36:39],a[212:215],v[80:83]
LPG2_%=:
s_cbranch_execz LPG6_%=
MM v[88:91],a[36:39],a[220:223],v[88:91]
buffer_load_dwordx4 v253, s[16:19], s87 offen offset:0 lds
MM v[88:91],a[32:35],a[216:219],v[88:91]
s_add_u32 m0, s96, 66560
MM v[68:71],a[40:43],a[192:195],v[68:71]
TR a[32:33],v198 offset:0
TR a[34:35],v198 offset:256
MM v[68:71],a[44:47],a[196:199],v[68:71]
TR a[36:37],v198 offset:8224
TR a[38:39],v198 offset:8480
MM v[76:79],a[44:47],a[204:207],v[76:79]
buffer_load_dwordx4 v254, s[16:19], s87 offen offset:0 lds
MM v[76:79],a[40:43],a[200:203],v[76:79]
s_add_u32 s87, s87, 65536
s_add_u32 m0, s96, 67592
MM v[84:87],a[40:43],a[208:211],v[84:87]
MM v[84:87],a[44:47],a[212:215],v[84:87]
MM v[92:95],a[44:47],a[220:223],v[92:95]
buffer_load_dwordx4 v253, s[16:19], s87 offen offset:0 lds
MM v[92:95],a[40:43],a[216:219],v[92:95]
s_add_u32 m0, s96, 68616
MM v[96:99],a[48:51],a[192:195],v[96:99]
TR a[40:41],v198 offset:8
TR a[42:43],v198 offset:264
MM v[96:99],a[52:55],a[196:199],v[96:99]
TR a[44:45],v198 offset:8232
TR a[46:47],v198 offset:8488
MM v[104:107],a[52:55],a[204:207],v[104:107]
buffer_load_dwordx4 v254, s[16:19], s87 offen offset:0 lds
MM v[104:107],a[48:51],a[200:203],v[104:107]
s_waitcnt vmcnt(20)
RD a[224:227],v222 offset:32768
MM v[112:115],a[48:51],a[208:211],v[112:115]
RD a[228:231],v223 offset:32768
MM v[112:115],a[52:55],a[212:215],v[112:115]
RD a[232:235],v217 offset:32768
MM v[120:123],a[52:55],a[220:223],v[120:123]
RD a[236:239],v218 offset:32768
MM v[120:123],a[48:51],a[216:219],v[120:123]
s_waitcnt lgkmcnt(15)
RD a[240:243],v222 offset:36864
MM v[100:103],a[56:59],a[192:195],v[100:103]
TR a[48:49],v199 offset:0
TR a[50:51],v199 offset:256
MM v[100:103],a[60:63],a[196:199],v[100:103]
TR a[52:53],v199 offset:8224
TR a[54:55],v199 offset:8480
MM v[108:111],a[60:63],a[204:207],v[108:111]
RD a[244:247],v223 offset:36864
MM v[108:111],a[56:59],a[200:203],v[108:111]
RD a[248:251],v217 offset:36864
MM v[116:119],a[56:59],a[208:211],v[116:119]
RD a[252:255],v218 offset:36864
MM v[116:119],a[60:63],a[212:215],v[116:119]
MM v[124:127],a[60:63],a[220:223],v[124:127]
MM v[124:127],a[56:59],a[216:219],v[124:127]
s_waitcnt vmcnt(15) lgkmcnt(0)
s_barrier
MM v[0:3],a[0:3],a[224:227],v[0:3]
TR a[56:57],v199 offset:8
TR a[58:59],v199 offset:264
MM v[0:3],a[4:7],a[228:231],v[0:3]
s_add_u32 s88, s68, s86
TR a[60:61],v199 offset:8232
TR a[62:63],v199 offset:8488
MM v[8:11],a[4:7],a[236:239],v[8:11]
s_add_u32 s86, s86, 128
s_cmp_ge_u32 s86, 1280
s_cselect_b32 s86, 0, s86
MM v[8:11],a[0:3],a[232:235],v[8:11]
s_add_u32 m0, s94, 32768
s_add_u32 s95, s88, 0
MM v[16:19],a[0:3],a[240:243],v[16:19]
buffer_load_dwordx4 v220, s[32:35], s95 offen offset:0 lds
MM v[16:19],a[4:7],a[244:247],v[16:19]
s_add_u32 s95, s88, 9216
MM v[24:27],a[4:7],a[252:255],v[24:27]
buffer_load_dwordx4 v221, s[32:35], s95 offen offset:1024 lds
MM v[24:27],a[0:3],a[248:251],v[24:27]
s_add_u32 s95, s88, 18432
MM v[4:7],a[8:11],a[224:227],v[4:7]
MM v[4:7],a[12:15],a[228:231],v[4:7]
buffer_load_dwordx4 v220, s[32:35], s95 offen offset:2048 lds
MM v[12:15],a[12:15],a[236:239],v[12:15]
s_add_u32 s95, s88, 27648
MM v[12:15],a[8:11],a[232:235],v[12:15]
MM v[20:23],a[8:11],a[240:243],v[20:23]
MM v[20:23],a[12:15],a[244:247],v[20:23]
buffer_load_dwordx4 v221, s[32:35], s95 offen offset:3072 lds
MM v[28:31],a[12:15],a[252:255],v[28:31]
s_add_u32 m0, s94, 36864
s_add_u32 s95, s88, 40960
MM v[28:31],a[8:11],a[248:251],v[28:31]
v_smfmac_f32_16x16x64_bf16 v[184:187], v[236:239], a[0:7], v234
v_smfmac_f32_16x16x64_bf16 v[188:191], v[236:239], a[8:15], v234
MM v[32:35],a[16:19],a[224:227],v[32:35]
buffer_load_dwordx4 v220, s[32:35], s95 offen offset:0 lds
MM v[32:35],a[20:23],a[228:231],v[32:35]
s_add_u32 s95, s88, 50176
TR a[0:1],v196 offset:16448
TR a[2:3],v196 offset:16704
MM v[40:43],a[20:23],a[236:239],v[40:43]
TR a[4:5],v196 offset:24672
TR a[6:7],v196 offset:24928
MM v[40:43],a[16:19],a[232:235],v[40:43]
TR a[8:9],v196 offset:16456
TR a[10:11],v196 offset:16712
MM v[48:51],a[16:19],a[240:243],v[48:51]
buffer_load_dwordx4 v221, s[32:35], s95 offen offset:1024 lds
MM v[48:51],a[20:23],a[244:247],v[48:51]
s_add_u32 s95, s88, 59392
TR a[12:13],v196 offset:24680
TR a[14:15],v196 offset:24936
MM v[56:59],a[20:23],a[252:255],v[56:59]
MM v[56:59],a[16:19],a[248:251],v[56:59]
buffer_load_dwordx4 v220, s[32:35], s95 offen offset:2048 lds
MM v[36:39],a[24:27],a[224:227],v[36:39]
s_add_u32 s95, s88, 68608
TR a[16:17],v197 offset:16448
TR a[18:19],v197 offset:16704
MM v[36:39],a[28:31],a[228:231],v[36:39]
TR a[20:21],v197 offset:24672
TR a[22:23],v197 offset:24928
MM v[44:47],a[28:31],a[236:239],v[44:47]
MM v[44:47],a[24:27],a[232:235],v[44:47]
buffer_load_dwordx4 v221, s[32:35], s95 offen offset:3072 lds
MM v[52:55],a[24:27],a[240:243],v[52:55]
s_add_u32 s87, s89, s85
s_add_u32 s85, s85, 524288
MM v[52:55],a[28:31],a[244:247],v[52:55]
s_cmp_ge_u32 s85, 5242880
s_cselect_b32 s85, 0, s85
s_add_u32 m0, s96, 81984
MM v[60:63],a[28:31],a[252:255],v[60:63]
MM v[60:63],a[24:27],a[248:251],v[60:63]
buffer_load_dwordx4 v253, s[16:19], s87 offen offset:0 lds
MM v[64:67],a[32:35],a[224:227],v[64:67]
s_add_u32 m0, s96, 83008
TR a[24:25],v197 offset:16456
TR a[26:27],v197 offset:16712
MM v[64:67],a[36:39],a[228:231],v[64:67]
TR a[28:29],v197 offset:24680
TR a[30:31],v197 offset:24936
MM v[72:75],a[36:39],a[236:239],v[72:75]
MM v[72:75],a[32:35],a[232:235],v[72:75]
buffer_load_dwordx4 v254, s[16:19], s87 offen offset:0 lds
MM v[80:83],a[32:35],a[240:243],v[80:83]
s_add_u32 s87, s87, 65536
s_add_u32 m0, s96, 84040
MM v[80:83],a[36:39],a[244:247],v[80:83]
MM v[88:91],a[36:39],a[252:255],v[88:91]
MM v[88:91],a[32:35],a[248:251],v[88:91]
buffer_load_dwordx4 v253, s[16:19], s87 offen offset:0 lds
MM v[68:71],a[40:43],a[224:227],v[68:71]
s_add_u32 m0, s96, 85064
TR a[32:33],v198 offset:16448
TR a[34:35],v198 offset:16704
MM v[68:71],a[44:47],a[228:231],v[68:71]
TR a[36:37],v198 offset:24672
TR a[38:39],v198 offset:24928
MM v[76:79],a[44:47],a[236:239],v[76:79]
MM v[76:79],a[40:43],a[232:235],v[76:79]
buffer_load_dwordx4 v254, s[16:19], s87 offen offset:0 lds
MM v[84:87],a[40:43],a[240:243],v[84:87]
s_waitcnt vmcnt(16)
RD a[128:131],v222 offset:0
MM v[84:87],a[44:47],a[244:247],v[84:87]
RD a[132:135],v223 offset:0
MM v[92:95],a[44:47],a[252:255],v[92:95]
RD a[136:139],v217 offset:0
MM v[92:95],a[40:43],a[248:251],v[92:95]
RD a[140:143],v218 offset:0
MM v[96:99],a[48:51],a[224:227],v[96:99]
TR a[40:41],v198 offset:16456
TR a[42:43],v198 offset:16712
MM v[96:99],a[52:55],a[228:231],v[96:99]
TR a[44:45],v198 offset:24680
TR a[46:47],v198 offset:24936
MM v[104:107],a[52:55],a[236:239],v[104:107]
RD a[144:147],v222 offset:4096
MM v[104:107],a[48:51],a[232:235],v[104:107]
RD a[148:151],v223 offset:4096
MM v[112:115],a[48:51],a[240:243],v[112:115]
RD a[152:155],v217 offset:4096
MM v[112:115],a[52:55],a[244:247],v[112:115]
RD a[156:159],v218 offset:4096
MM v[120:123],a[52:55],a[252:255],v[120:123]
MM v[120:123],a[48:51],a[248:251],v[120:123]
s_waitcnt lgkmcnt(15)
MM v[100:103],a[56:59],a[224:227],v[100:103]
TR a[48:49],v199 offset:16448
TR a[50:51],v199 offset:16704
MM v[100:103],a[60:63],a[228:231],v[100:103]
s_waitcnt lgkmcnt(2)
TR a[52:53],v199 offset:24672
TR a[54:55],v199 offset:24928
MM v[108:111],a[60:63],a[236:239],v[108:111]
s_add_u32 s88, s68, s86
s_add_u32 s86, s86, 128
MM v[108:111],a[56:59],a[232:235],v[108:111]
s_cmp_ge_u32 s86, 1280
s_cselect_b32 s86, 0, s86
s_add_u32 m0, s94, 0
MM v[116:119],a[56:59],a[240:243],v[116:119]
s_add_u32 s95, s88, 0
MM v[116:119],a[60:63],a[244:247],v[116:119]
buffer_load_dwordx4 v220, s[32:35], s95 offen offset:0 lds
MM v[124:127],a[60:63],a[252:255],v[124:127]
s_add_u32 s95, s88, 9216
MM v[124:127],a[56:59],a[248:251],v[124:127]
buffer_load_dwordx4 v221, s[32:35], s95 offen offset:1024 lds
s_waitcnt vmcnt(14)
s_barrier
MM v[0:3],a[0:3],a[128:131],v[0:3]
s_add_u32 s95, s88, 18432
TR a[56:57],v199 offset:16456
TR a[58:59],v199 offset:16712
MM v[0:3],a[4:7],a[132:135],v[0:3]
TR a[60:61],v199 offset:24680
TR a[62:63],v199 offset:24936
MM v[8:11],a[4:7],a[140:143],v[8:11]
buffer_load_dwordx4 v220, s[32:35], s95 offen offset:2048 lds
MM v[8:11],a[0:3],a[136:139],v[8:11]
s_add_u32 s95, s88, 27648
MM v[16:19],a[0:3],a[144:147],v[16:19]
MM v[16:19],a[4:7],a[148:151],v[16:19]
MM v[24:27],a[4:7],a[156:159],v[24:27]
buffer_load_dwordx4 v221, s[32:35], s95 offen offset:3072 lds
MM v[24:27],a[0:3],a[152:155],v[24:27]
s_add_u32 m0, s94, 4096
s_add_u32 s95, s88, 40960
MM v[4:7],a[8:11],a[128:131],v[4:7]
MM v[4:7],a[12:15],a[132:135],v[4:7]
MM v[12:15],a[12:15],a[140:143],v[12:15]
buffer_load_dwordx4 v220, s[32:35], s95 offen offset:0 lds
)ASM"
R"ASM(
MM v[12:15],a[8:11],a[136:139],v[12:15]
s_add_u32 s95, s88, 50176
MM v[20:23],a[8:11],a[144:147],v[20:23]
MM v[20:23],a[12:15],a[148:151],v[20:23]
MM v[28:31],a[12:15],a[156:159],v[28:31]
buffer_load_dwordx4 v221, s[32:35], s95 offen offset:1024 lds
MM v[28:31],a[8:11],a[152:155],v[28:31]
s_add_u32 s95, s88, 59392
v_smfmac_f32_16x16x64_bf16 v[184:187], v[236:239], a[0:7], v234
v_smfmac_f32_16x16x64_bf16 v[188:191], v[236:239], a[8:15], v234
MM v[32:35],a[16:19],a[128:131],v[32:35]
TR a[0:1],v192 offset:0
TR a[2:3],v192 offset:256
MM v[32:35],a[20:23],a[132:135],v[32:35]
buffer_load_dwordx4 v220, s[32:35], s95 offen offset:2048 lds
MM v[40:43],a[20:23],a[140:143],v[40:43]
s_add_u32 s95, s88, 68608
TR a[4:5],v192 offset:8224
TR a[6:7],v192 offset:8480
MM v[40:43],a[16:19],a[136:139],v[40:43]
TR a[8:9],v192 offset:8
TR a[10:11],v192 offset:264
MM v[48:51],a[16:19],a[144:147],v[48:51]
TR a[12:13],v192 offset:8232
TR a[14:15],v192 offset:8488
MM v[48:51],a[20:23],a[148:151],v[48:51]
buffer_load_dwordx4 v221, s[32:35], s95 offen offset:3072 lds
MM v[56:59],a[20:23],a[156:159],v[56:59]
s_add_u32 s87, s89, s85
s_add_u32 s85, s85, 524288
MM v[56:59],a[16:19],a[152:155],v[56:59]
s_cmp_ge_u32 s85, 5242880
s_cselect_b32 s85, 0, s85
s_add_u32 m0, s96, 98432
MM v[36:39],a[24:27],a[128:131],v[36:39]
TR a[16:17],v193 offset:0
TR a[18:19],v193 offset:256
MM v[36:39],a[28:31],a[132:135],v[36:39]
buffer_load_dwordx4 v253, s[16:19], s87 offen offset:0 lds
MM v[44:47],a[28:31],a[140:143],v[44:47]
s_add_u32 m0, s96, 99456
TR a[20:21],v193 offset:8224
TR a[22:23],v193 offset:8480
MM v[44:47],a[24:27],a[136:139],v[44:47]
MM v[52:55],a[24:27],a[144:147],v[52:55]
buffer_load_dwordx4 v254, s[16:19], s87 offen offset:0 lds
MM v[52:55],a[28:31],a[148:151],v[52:55]
s_add_u32 s87, s87, 65536
s_add_u32 m0, s96, 100488
MM v[60:63],a[28:31],a[156:159],v[60:63]
MM v[60:63],a[24:27],a[152:155],v[60:63]
MM v[64:67],a[32:35],a[128:131],v[64:67]
TR a[24:25],v193 offset:8
TR a[26:27],v193 offset:264
MM v[64:67],a[36:39],a[132:135],v[64:67]
buffer_load_dwordx4 v253, s[16:19], s87 offen offset:0 lds
MM v[72:75],a[36:39],a[140:143],v[72:75]
s_add_u32 m0, s96, 101512
TR a[28:29],v193 offset:8232
TR a[30:31],v193 offset:8488
MM v[72:75],a[32:35],a[136:139],v[72:75]
MM v[80:83],a[32:35],a[144:147],v[80:83]
buffer_load_dwordx4 v254, s[16:19], s87 offen offset:0 lds
MM v[80:83],a[36:39],a[148:151],v[80:83]
s_waitcnt vmcnt(16)
RD a[160:163],v222 offset:32768
MM v[88:91],a[36:39],a[156:159],v[88:91]
RD a[164:167],v223 offset:32768
MM v[88:91],a[32:35],a[152:155],v[88:91]
RD a[168:171],v217 offset:32768
MM v[68:71],a[40:43],a[128:131],v[68:71]
TR a[32:33],v194 offset:0
TR a[34:35],v194 offset:256
MM v[68:71],a[44:47],a[132:135],v[68:71]
TR a[36:37],v194 offset:8224
TR a[38:39],v194 offset:8480
MM v[76:79],a[44:47],a[140:143],v[76:79]
RD a[172:175],v218 offset:32768
MM v[76:79],a[40:43],a[136:139],v[76:79]
RD a[176:179],v222 offset:36864
MM v[84:87],a[40:43],a[144:147],v[84:87]
RD a[180:183],v223 offset:36864
MM v[84:87],a[44:47],a[148:151],v[84:87]
RD a[184:187],v217 offset:36864
MM v[92:95],a[44:47],a[156:159],v[92:95]
RD a[188:191],v218 offset:36864
MM v[92:95],a[40:43],a[152:155],v[92:95]
s_waitcnt lgkmcnt(15)
MM v[96:99],a[48:51],a[128:131],v[96:99]
TR a[40:41],v194 offset:8
TR a[42:43],v194 offset:264
MM v[96:99],a[52:55],a[132:135],v[96:99]
TR a[44:45],v194 offset:8232
TR a[46:47],v194 offset:8488
MM v[104:107],a[52:55],a[140:143],v[104:107]
s_waitcnt lgkmcnt(4)
s_add_u32 s88, s68, s86
s_add_u32 s86, s86, 128
MM v[104:107],a[48:51],a[136:139],v[104:107]
s_cmp_ge_u32 s86, 1280
s_cselect_b32 s86, 0, s86
s_add_u32 m0, s94, 32768
MM v[112:115],a[48:51],a[144:147],v[112:115]
s_add_u32 s95, s88, 0
MM v[112:115],a[52:55],a[148:151],v[112:115]
buffer_load_dwordx4 v220, s[32:35], s95 offen offset:0 lds
MM v[120:123],a[52:55],a[156:159],v[120:123]
s_add_u32 s95, s88, 9216
MM v[120:123],a[48:51],a[152:155],v[120:123]
buffer_load_dwordx4 v221, s[32:35], s95 offen offset:1024 lds
MM v[100:103],a[56:59],a[128:131],v[100:103]
s_add_u32 s95, s88, 18432
TR a[48:49],v195 offset:0
TR a[50:51],v195 offset:256
MM v[100:103],a[60:63],a[132:135],v[100:103]
TR a[52:53],v195 offset:8224
TR a[54:55],v195 offset:8480
MM v[108:111],a[60:63],a[140:143],v[108:111]
buffer_load_dwordx4 v220, s[32:35], s95 offen offset:2048 lds
MM v[108:111],a[56:59],a[136:139],v[108:111]
s_add_u32 s95, s88, 27648
MM v[116:119],a[56:59],a[144:147],v[116:119]
MM v[116:119],a[60:63],a[148:151],v[116:119]
MM v[124:127],a[60:63],a[156:159],v[124:127]
buffer_load_dwordx4 v221, s[32:35], s95 offen offset:3072 lds
MM v[124:127],a[56:59],a[152:155],v[124:127]
s_add_u32 m0, s94, 36864
s_add_u32 s95, s88, 40960
s_waitcnt vmcnt(16)
s_barrier
MM v[0:3],a[0:3],a[160:163],v[0:3]
TR a[56:57],v195 offset:8
TR a[58:59],v195 offset:264
MM v[0:3],a[4:7],a[164:167],v[0:3]
TR a[60:61],v195 offset:8232
TR a[62:63],v195 offset:8488
MM v[8:11],a[4:7],a[172:175],v[8:11]
buffer_load_dwordx4 v220, s[32:35], s95 offen offset:0 lds
MM v[8:11],a[0:3],a[168:171],v[8:11]
s_add_u32 s95, s88, 50176
MM v[16:19],a[0:3],a[176:179],v[16:19]
MM v[16:19],a[4:7],a[180:183],v[16:19]
MM v[24:27],a[4:7],a[188:191],v[24:27]
buffer_load_dwordx4 v221, s[32:35], s95 offen offset:1024 lds
MM v[24:27],a[0:3],a[184:187],v[24:27]
s_add_u32 s95, s88, 59392
MM v[4:7],a[8:11],a[160:163],v[4:7]
MM v[4:7],a[12:15],a[164:167],v[4:7]
MM v[12:15],a[12:15],a[172:175],v[12:15]
buffer_load_dwordx4 v220, s[32:35], s95 offen offset:2048 lds
MM v[12:15],a[8:11],a[168:171],v[12:15]
s_add_u32 s95, s88, 68608
MM v[20:23],a[8:11],a[176:179],v[20:23]
MM v[20:23],a[12:15],a[180:183],v[20:23]
MM v[28:31],a[12:15],a[188:191],v[28:31]
buffer_load_dwordx4 v221, s[32:35], s95 offen offset:3072 lds
MM v[28:31],a[8:11],a[184:187],v[28:31]
s_add_u32 s87, s89, s85
s_add_u32 s85, s85, 524288
v_smfmac_f32_16x16x64_bf16 v[184:187], v[236:239], a[0:7], v234
s_cmp_ge_u32 s85, 5242880
s_cselect_b32 s85, 0, s85
v_smfmac_f32_16x16x64_bf16 v[188:191], v[236:239], a[8:15], v234
s_add_u32 m0, s96, 114880
MM v[32:35],a[16:19],a[160:163],v[32:35]
TR a[0:1],v192 offset:16448
TR a[2:3],v192 offset:16704
MM v[32:35],a[20:23],a[164:167],v[32:35]
buffer_load_dwordx4 v253, s[16:19], s87 offen offset:0 lds
MM v[40:43],a[20:23],a[172:175],v[40:43]
s_add_u32 m0, s96, 115904
TR a[4:5],v192 offset:24672
TR a[6:7],v192 offset:24928
MM v[40:43],a[16:19],a[168:171],v[40:43]
TR a[8:9],v192 offset:16456
TR a[10:11],v192 offset:16712
MM v[48:51],a[16:19],a[176:179],v[48:51]
TR a[12:13],v192 offset:24680
TR a[14:15],v192 offset:24936
MM v[48:51],a[20:23],a[180:183],v[48:51]
buffer_load_dwordx4 v254, s[16:19], s87 offen offset:0 lds
MM v[56:59],a[20:23],a[188:191],v[56:59]
s_add_u32 s87, s87, 65536
s_add_u32 m0, s96, 116936
MM v[56:59],a[16:19],a[184:187],v[56:59]
MM v[36:39],a[24:27],a[160:163],v[36:39]
TR a[16:17],v193 offset:16448
TR a[18:19],v193 offset:16704
MM v[36:39],a[28:31],a[164:167],v[36:39]
buffer_load_dwordx4 v253, s[16:19], s87 offen offset:0 lds
MM v[44:47],a[28:31],a[172:175],v[44:47]
s_add_u32 m0, s96, 117960
TR a[20:21],v193 offset:24672
TR a[22:23],v193 offset:24928
MM v[44:47],a[24:27],a[168:171],v[44:47]
MM v[52:55],a[24:27],a[176:179],v[52:55]
buffer_load_dwordx4 v254, s[16:19], s87 offen offset:0 lds
MM v[52:55],a[28:31],a[180:183],v[52:55]
s_waitcnt vmcnt(16)
RD a[192:195],v222 offset:0
MM v[60:63],a[28:31],a[188:191],v[60:63]
RD a[196:199],v223 offset:0
MM v[60:63],a[24:27],a[184:187],v[60:63]
RD a[200:203],v217 offset:0
MM v[64:67],a[32:35],a[160:163],v[64:67]
TR a[24:25],v193 offset:16456
TR a[26:27],v193 offset:16712
MM v[64:67],a[36:39],a[164:167],v[64:67]
TR a[28:29],v193 offset:24680
TR a[30:31],v193 offset:24936
MM v[72:75],a[36:39],a[172:175],v[72:75]
RD a[204:207],v218 offset:0
MM v[72:75],a[32:35],a[168:171],v[72:75]
RD a[208:211],v222 offset:4096
MM v[80:83],a[32:35],a[176:179],v[80:83]
RD a[212:215],v223 offset:4096
MM v[80:83],a[36:39],a[180:183],v[80:83]
RD a[216:219],v217 offset:4096
MM v[88:91],a[36:39],a[188:191],v[88:91]
RD a[220:223],v218 offset:4096
MM v[88:91],a[32:35],a[184:187],v[88:91]
s_waitcnt lgkmcnt(15)
MM v[68:71],a[40:43],a[160:163],v[68:71]
TR a[32:33],v194 offset:16448
TR a[34:35],v194 offset:16704
MM v[68:71],a[44:47],a[164:167],v[68:71]
TR a[36:37],v194 offset:24672
TR a[38:39],v194 offset:24928
MM v[76:79],a[44:47],a[172:175],v[76:79]
s_waitcnt lgkmcnt(4)
s_add_u32 s88, s68, s86
s_add_u32 s86, s86, 128
MM v[76:79],a[40:43],a[168:171],v[76:79]
s_cmp_ge_u32 s86, 1280
s_cselect_b32 s86, 0, s86
s_add_u32 m0, s94, 0
MM v[84:87],a[40:43],a[176:179],v[84:87]
s_add_u32 s95, s88, 0
MM v[84:87],a[44:47],a[180:183],v[84:87]
buffer_load_dwordx4 v220, s[32:35], s95 offen offset:0 lds
MM v[92:95],a[44:47],a[188:191],v[92:95]
s_add_u32 s95, s88, 9216
MM v[92:95],a[40:43],a[184:187],v[92:95]
buffer_load_dwordx4 v221, s[32:35], s95 offen offset:1024 lds
MM v[96:99],a[48:51],a[160:163],v[96:99]
s_add_u32 s95, s88, 18432
TR a[40:41],v194 offset:16456
TR a[42:43],v194 offset:16712
MM v[96:99],a[52:55],a[164:167],v[96:99]
TR a[44:45],v194 offset:24680
TR a[46:47],v194 offset:24936
MM v[104:107],a[52:55],a[172:175],v[104:107]
buffer_load_dwordx4 v220, s[32:35], s95 offen offset:2048 lds
MM v[104:107],a[48:51],a[168:171],v[104:107]
s_add_u32 s95, s88, 27648
MM v[112:115],a[48:51],a[176:179],v[112:115]
MM v[112:115],a[52:55],a[180:183],v[112:115]
MM v[120:123],a[52:55],a[188:191],v[120:123]
buffer_load_dwordx4 v221, s[32:35], s95 offen offset:3072 lds
MM v[120:123],a[48:51],a[184:187],v[120:123]
s_add_u32 m0, s94, 4096
s_add_u32 s95, s88, 40960
MM v[100:103],a[56:59],a[160:163],v[100:103]
TR a[48:49],v195 offset:16448
TR a[50:51],v195 offset:16704
MM v[100:103],a[60:63],a[164:167],v[100:103]
TR a[52:53],v195 offset:24672
TR a[54:55],v195 offset:24928
MM v[108:111],a[60:63],a[172:175],v[108:111]
buffer_load_dwordx4 v220, s[32:35], s95 offen offset:0 lds
MM v[108:111],a[56:59],a[168:171],v[108:111]
s_add_u32 s95, s88, 50176
MM v[116:119],a[56:59],a[176:179],v[116:119]
MM v[116:119],a[60:63],a[180:183],v[116:119]
s_nop 0
MM v[124:127],a[60:63],a[188:191],v[124:127]
buffer_load_dwordx4 v221, s[32:35], s95 offen offset:1024 lds
MM v[124:127],a[56:59],a[184:187],v[124:127]
s_add_u32 s95, s88, 59392
s_waitcnt vmcnt(18)
s_barrier
MM v[0:3],a[0:3],a[192:195],v[0:3]
s_nop 0
TR a[56:57],v195 offset:16456
TR a[58:59],v195 offset:16712
MM v[0:3],a[4:7],a[196:199],v[0:3]
s_nop 0
TR a[60:61],v195 offset:24680
TR a[62:63],v195 offset:24936
MM v[8:11],a[4:7],a[204:207],v[8:11]
buffer_load_dwordx4 v220, s[32:35], s95 offen offset:2048 lds
MM v[8:11],a[0:3],a[200:203],v[8:11]
s_add_u32 s95, s88, 68608
v_and_b32 v208, 240, v248
s_add_u32 s93, s93, 36864
MM v[16:19],a[0:3],a[208:211],v[16:19]
s_nop 0
MM v[16:19],a[4:7],a[212:215],v[16:19]
LPG3_%=:
s_cbranch_execz LPG7_%=
MM v[24:27],a[4:7],a[220:223],v[24:27]
buffer_load_dwordx4 v221, s[32:35], s95 offen offset:3072 lds
MM v[24:27],a[0:3],a[216:219],v[24:27]
s_add_u32 s87, s89, s85
s_add_u32 s85, s85, 524288
MM v[4:7],a[8:11],a[192:195],v[4:7]
s_cmp_ge_u32 s85, 5242880
s_cselect_b32 s85, 0, s85
s_add_u32 m0, s96, 131328
MM v[4:7],a[12:15],a[196:199],v[4:7]
MM v[12:15],a[12:15],a[204:207],v[12:15]
buffer_load_dwordx4 v253, s[16:19], s87 offen offset:0 lds
MM v[12:15],a[8:11],a[200:203],v[12:15]
s_add_u32 m0, s96, 132352
MM v[20:23],a[8:11],a[208:211],v[20:23]
MM v[20:23],a[12:15],a[212:215],v[20:23]
MM v[28:31],a[12:15],a[220:223],v[28:31]
buffer_load_dwordx4 v254, s[16:19], s87 offen offset:0 lds
MM v[28:31],a[8:11],a[216:219],v[28:31]
s_add_u32 s87, s87, 65536
s_add_u32 m0, s96, 133384
v_smfmac_f32_16x16x64_bf16 v[184:187], v[236:239], a[0:7], v234
v_smfmac_f32_16x16x64_bf16 v[188:191], v[236:239], a[8:15], v234
MM v[32:35],a[16:19],a[192:195],v[32:35]
TR a[0:1],v192 offset:32896
TR a[2:3],v192 offset:33152
MM v[32:35],a[20:23],a[196:199],v[32:35]
buffer_load_dwordx4 v253, s[16:19], s87 offen offset:0 lds
MM v[40:43],a[20:23],a[204:207],v[40:43]
s_add_u32 m0, s96, 134408
TR a[4:5],v192 offset:41120
TR a[6:7],v192 offset:41376
)ASM"
R"ASM(
MM v[40:43],a[16:19],a[200:203],v[40:43]
TR a[8:9],v192 offset:32904
TR a[10:11],v192 offset:33160
MM v[48:51],a[16:19],a[208:211],v[48:51]
buffer_load_dwordx4 v[200:203], v208, s[20:23], s93 offen offset:0 sc1
MM v[48:51],a[20:23],a[212:215],v[48:51]
TR a[12:13],v192 offset:41128
TR a[14:15],v192 offset:41384
MM v[56:59],a[20:23],a[220:223],v[56:59]
MM v[56:59],a[16:19],a[216:219],v[56:59]
MM v[36:39],a[24:27],a[192:195],v[36:39]
TR a[16:17],v193 offset:32896
TR a[18:19],v193 offset:33152
MM v[36:39],a[28:31],a[196:199],v[36:39]
buffer_load_dwordx4 v254, s[16:19], s87 offen offset:0 lds
MM v[44:47],a[28:31],a[204:207],v[44:47]
s_waitcnt vmcnt(17)
TR a[20:21],v193 offset:41120
TR a[22:23],v193 offset:41376
MM v[44:47],a[24:27],a[200:203],v[44:47]
RD a[224:227],v222 offset:32768
MM v[52:55],a[24:27],a[208:211],v[52:55]
buffer_load_dwordx4 v[204:207], v208, s[20:23], s93 offen offset:256 sc1
MM v[52:55],a[28:31],a[212:215],v[52:55]
RD a[228:231],v223 offset:32768
MM v[60:63],a[28:31],a[220:223],v[60:63]
RD a[232:235],v217 offset:32768
MM v[60:63],a[24:27],a[216:219],v[60:63]
s_waitcnt lgkmcnt(15)
RD a[236:239],v218 offset:32768
MM v[64:67],a[32:35],a[192:195],v[64:67]
TR a[24:25],v193 offset:32904
TR a[26:27],v193 offset:33160
MM v[64:67],a[36:39],a[196:199],v[64:67]
TR a[28:29],v193 offset:41128
TR a[30:31],v193 offset:41384
MM v[72:75],a[36:39],a[204:207],v[72:75]
RD a[240:243],v222 offset:36864
MM v[72:75],a[32:35],a[200:203],v[72:75]
RD a[244:247],v223 offset:36864
MM v[80:83],a[32:35],a[208:211],v[80:83]
RD a[248:251],v217 offset:36864
MM v[80:83],a[36:39],a[212:215],v[80:83]
RD a[252:255],v218 offset:36864
MM v[88:91],a[36:39],a[220:223],v[88:91]
MM v[88:91],a[32:35],a[216:219],v[88:91]
MM v[68:71],a[40:43],a[192:195],v[68:71]
TR a[32:33],v194 offset:32896
TR a[34:35],v194 offset:33152
MM v[68:71],a[44:47],a[196:199],v[68:71]
s_waitcnt lgkmcnt(2)
TR a[36:37],v194 offset:41120
TR a[38:39],v194 offset:41376
MM v[76:79],a[44:47],a[204:207],v[76:79]
s_add_u32 s88, s68, s86
s_add_u32 s86, s86, 128
MM v[76:79],a[40:43],a[200:203],v[76:79]
s_cmp_ge_u32 s86, 1280
s_cselect_b32 s86, 0, s86
s_add_u32 m0, s94, 32768
MM v[84:87],a[40:43],a[208:211],v[84:87]
s_add_u32 s95, s88, 0
MM v[84:87],a[44:47],a[212:215],v[84:87]
buffer_load_dwordx4 v220, s[32:35], s95 offen offset:0 lds
MM v[92:95],a[44:47],a[220:223],v[92:95]
s_add_u32 s95, s88, 9216
MM v[92:95],a[40:43],a[216:219],v[92:95]
buffer_load_dwordx4 v221, s[32:35], s95 offen offset:1024 lds
MM v[96:99],a[48:51],a[192:195],v[96:99]
s_add_u32 s95, s88, 18432
TR a[40:41],v194 offset:32904
TR a[42:43],v194 offset:33160
MM v[96:99],a[52:55],a[196:199],v[96:99]
TR a[44:45],v194 offset:41128
TR a[46:47],v194 offset:41384
MM v[104:107],a[52:55],a[204:207],v[104:107]
buffer_load_dwordx4 v220, s[32:35], s95 offen offset:2048 lds
MM v[104:107],a[48:51],a[200:203],v[104:107]
s_add_u32 s95, s88, 27648
MM v[112:115],a[48:51],a[208:211],v[112:115]
MM v[112:115],a[52:55],a[212:215],v[112:115]
MM v[120:123],a[52:55],a[220:223],v[120:123]
buffer_load_dwordx4 v221, s[32:35], s95 offen offset:3072 lds
MM v[120:123],a[48:51],a[216:219],v[120:123]
s_waitcnt vmcnt(10)
RD a[128:131],v222 offset:0
MM v[100:103],a[56:59],a[192:195],v[100:103]
TR a[48:49],v195 offset:32896
TR a[50:51],v195 offset:33152
MM v[100:103],a[60:63],a[196:199],v[100:103]
TR a[52:53],v195 offset:41120
TR a[54:55],v195 offset:41376
MM v[108:111],a[60:63],a[204:207],v[108:111]
RD a[132:135],v223 offset:0
MM v[108:111],a[56:59],a[200:203],v[108:111]
RD a[136:139],v217 offset:0
MM v[116:119],a[56:59],a[208:211],v[116:119]
RD a[140:143],v218 offset:0
MM v[116:119],a[60:63],a[212:215],v[116:119]
RD a[144:147],v222 offset:4096
MM v[124:127],a[60:63],a[220:223],v[124:127]
RD a[148:151],v223 offset:4096
MM v[124:127],a[56:59],a[216:219],v[124:127]
RD a[152:155],v217 offset:4096
RD a[156:159],v218 offset:4096
s_add_u32 m0, s94, 36864
s_add_u32 s95, s88, 40960
buffer_load_dwordx4 v220, s[32:35], s95 offen offset:0 lds
s_add_u32 s95, s88, 50176
buffer_load_dwordx4 v221, s[32:35], s95 offen offset:1024 lds
s_add_u32 s95, s88, 59392
buffer_load_dwordx4 v220, s[32:35], s95 offen offset:2048 lds
s_add_u32 s95, s88, 68608
buffer_load_dwordx4 v221, s[32:35], s95 offen offset:3072 lds
s_waitcnt lgkmcnt(0)
s_waitcnt vmcnt(9)
s_barrier
MM v[0:3],a[0:3],a[224:227],v[0:3]
s_add_u32 s77, s64, 0
TR a[56:57],v195 offset:32904
TR a[58:59],v195 offset:33160
MM v[0:3],a[4:7],a[228:231],v[0:3]
s_add_u32 m0, s65, 0
TR a[60:61],v195 offset:41128
TR a[62:63],v195 offset:41384
MM v[8:11],a[4:7],a[236:239],v[8:11]
TR a[64:65],v196 offset:0
TR a[66:67],v196 offset:256
s_waitcnt vmcnt(8)
MM v[8:11],a[0:3],a[232:235],v[8:11]
s_lshl_b32 s76, %[swave], 7
v_lshrrev_b32 v253, 8, v248
v_lshlrev_b32 v253, 5, v253
MM v[16:19],a[0:3],a[240:243],v[16:19]
s_add_u32 s76, s76, %[slds]
TR a[68:69],v196 offset:8224
TR a[70:71],v196 offset:8480
MM v[16:19],a[4:7],a[244:247],v[16:19]
s_mov_b64 exec, 1
buffer_store_dwordx2 v[170:171], off, s[20:23], s80 sc1
s_mov_b64 exec, -1
v_add_u32 v253, s76, v253
v_add_u32 v253, 151552, v253
v_xor_b32 v201, s69, v201
MM v[24:27],a[4:7],a[252:255],v[24:27]
buffer_load_dwordx4 v228, s[8:11], s77 offen offset:0 lds
MM v[24:27],a[0:3],a[248:251],v[24:27]
TR a[72:73],v196 offset:16448
TR a[74:75],v196 offset:16704
MM v[4:7],a[8:11],a[224:227],v[4:7]
buffer_load_dwordx4 v229, s[8:11], s77 offen offset:1024 lds
MM v[4:7],a[12:15],a[228:231],v[4:7]
TR a[76:77],v196 offset:24672
TR a[78:79],v196 offset:24928
v_xor_b32 v203, s69, v203
MM v[12:15],a[12:15],a[236:239],v[12:15]
TR a[80:81],v196 offset:8
TR a[82:83],v196 offset:264
MM v[12:15],a[8:11],a[232:235],v[12:15]
TR a[84:85],v196 offset:8232
TR a[86:87],v196 offset:8488
MM v[20:23],a[8:11],a[240:243],v[20:23]
buffer_load_dwordx4 v230, s[8:11], s77 offen offset:2048 lds
MM v[20:23],a[12:15],a[244:247],v[20:23]
TR a[88:89],v196 offset:16456
TR a[90:91],v196 offset:16712
v_xor_b32 v205, s69, v205
MM v[28:31],a[12:15],a[252:255],v[28:31]
TR a[92:93],v196 offset:24680
TR a[94:95],v196 offset:24936
MM v[28:31],a[8:11],a[248:251],v[28:31]
TR a[96:97],v197 offset:0
TR a[98:99],v197 offset:256
v_smfmac_f32_16x16x64_bf16 v[184:187], v[236:239], a[0:7], v234
TR a[100:101],v197 offset:8224
v_xor_b32 v207, s69, v207
v_smfmac_f32_16x16x64_bf16 v[188:191], v[236:239], a[8:15], v234
TR a[102:103],v197 offset:8480
MM v[32:35],a[16:19],a[224:227],v[32:35]
buffer_load_dwordx4 v231, s[8:11], s77 offen offset:3072 lds
MM v[32:35],a[20:23],a[228:231],v[32:35]
s_add_u32 s77, s64, 16384
TR a[104:105],v197 offset:16448
TR a[106:107],v197 offset:16704
v_or3_b32 v209, v201, v203, v205
MM v[40:43],a[20:23],a[236:239],v[40:43]
s_add_u32 m0, s65, 16384
TR a[108:109],v197 offset:24672
TR a[110:111],v197 offset:24928
MM v[40:43],a[16:19],a[232:235],v[40:43]
TR a[112:113],v197 offset:8
TR a[114:115],v197 offset:264
MM v[48:51],a[16:19],a[240:243],v[48:51]
TR a[116:117],v197 offset:8232
TR a[118:119],v197 offset:8488
v_or_b32 v209, v209, v207
MM v[48:51],a[20:23],a[244:247],v[48:51]
buffer_load_dwordx4 v228, s[8:11], s77 offen offset:0 lds
MM v[56:59],a[20:23],a[252:255],v[56:59]
TR a[120:121],v197 offset:16456
TR a[122:123],v197 offset:16712
MM v[56:59],a[16:19],a[248:251],v[56:59]
TR a[124:125],v197 offset:24680
TR a[126:127],v197 offset:24936
v_cmp_ne_u32 s[74:75], 0, v209
MM v[36:39],a[24:27],a[224:227],v[36:39]
TR a[0:1],v198 offset:0
TR a[2:3],v198 offset:256
MM v[36:39],a[28:31],a[228:231],v[36:39]
TR a[4:5],v198 offset:8224
TR a[6:7],v198 offset:8480
MM v[44:47],a[28:31],a[236:239],v[44:47]
TR a[8:9],v198 offset:16448
TR a[10:11],v198 offset:16704
MM v[44:47],a[24:27],a[232:235],v[44:47]
TR a[12:13],v198 offset:24672
TR a[14:15],v198 offset:24928
s_cmp_lg_u64 s[74:75], 0
s_cbranch_scc1 LRG1o_%=
LRG1ob_%=:
MM v[52:55],a[24:27],a[240:243],v[52:55]
s_bitcmp1_b32 %[sb], 0
s_cselect_b64 exec, -1, 0
v_swap_b32 v200, v202
v_swap_b32 v204, v206
s_mov_b64 exec, -1
MM v[52:55],a[28:31],a[244:247],v[52:55]
v_lshlrev_b32 v240, 16, v200
v_and_b32 v200, 0xffff0000, v200
MM v[60:63],a[28:31],a[252:255],v[60:63]
buffer_load_dwordx4 v229, s[8:11], s77 offen offset:1024 lds
MM v[60:63],a[24:27],a[248:251],v[60:63]
v_rcp_f32 v246, v200
MM v[64:67],a[32:35],a[224:227],v[64:67]
TR a[16:17],v198 offset:8
TR a[18:19],v198 offset:264
MM v[64:67],a[36:39],a[228:231],v[64:67]
TR a[20:21],v198 offset:8232
TR a[22:23],v198 offset:8488
v_xor_b32 v240, 0x80000000, v240
MM v[72:75],a[36:39],a[236:239],v[72:75]
TR a[24:25],v198 offset:16456
TR a[26:27],v198 offset:16712
MM v[72:75],a[32:35],a[232:235],v[72:75]
TR a[28:29],v198 offset:24680
TR a[30:31],v198 offset:24936
MM v[80:83],a[32:35],a[240:243],v[80:83]
v_mul_f32 v246, s72, v246
v_lshlrev_b32 v241, 16, v202
MM v[80:83],a[36:39],a[244:247],v[80:83]
v_and_b32 v202, 0xffff0000, v202
MM v[88:91],a[36:39],a[252:255],v[88:91]
v_rcp_f32 v247, v202
MM v[88:91],a[32:35],a[248:251],v[88:91]
s_waitcnt lgkmcnt(15)
v_xor_b32 v241, 0x80000000, v241
v_mul_f32 v247, s72, v247
v_smfmac_f32_16x16x64_bf16 v[184:187], v[236:239], a[64:71], v234
v_lshlrev_b32 v242, 16, v204
v_smfmac_f32_16x16x64_bf16 v[184:187], v[236:239], a[72:79], v234
v_and_b32 v204, 0xffff0000, v204
v_smfmac_f32_16x16x64_bf16 v[188:191], v[236:239], a[80:87], v234
v_smfmac_f32_16x16x64_bf16 v[188:191], v[236:239], a[88:95], v234
v_rcp_f32 v244, v204
MM v[68:71],a[40:43],a[224:227],v[68:71]
v_bfe_u32 v183, v248, 6, 2
v_lshlrev_b32 v183, 5, v183
MM v[68:71],a[44:47],a[228:231],v[68:71]
buffer_load_dwordx4 v230, s[8:11], s77 offen offset:2048 lds
v_bfe_u32 v182, v248, 4, 2
v_lshl_add_u32 v183, v182, 2, v183
MM v[76:79],a[44:47],a[236:239],v[76:79]
v_add_u32 v183, s76, v183
v_add_u32 v183, 151552, v183
v_xor_b32 v242, 0x80000000, v242
MM v[76:79],a[40:43],a[232:235],v[76:79]
v_mul_f32 v244, s72, v244
v_lshlrev_b32 v243, 16, v206
MM v[84:87],a[40:43],a[240:243],v[84:87]
v_and_b32 v206, 0xffff0000, v206
MM v[84:87],a[44:47],a[244:247],v[84:87]
v_rcp_f32 v245, v206
MM v[92:95],a[44:47],a[252:255],v[92:95]
buffer_load_dwordx4 v231, s[8:11], s77 offen offset:3072 lds
MM v[92:95],a[40:43],a[248:251],v[92:95]
s_waitcnt vmcnt(9)
v_add_f32 v184, v184, v185
v_add_f32 v188, v188, v189
v_xor_b32 v243, 0x80000000, v243
MM v[96:99],a[48:51],a[224:227],v[96:99]
TR a[32:33],v199 offset:0
TR a[34:35],v199 offset:256
MM v[96:99],a[52:55],a[228:231],v[96:99]
RD a[160:163],v222 offset:32768
ds_write_b32 v183, v184 offset:0
ds_write_b32 v183, v188 offset:16
MM v[104:107],a[52:55],a[236:239],v[104:107]
TR a[36:37],v199 offset:8224
TR a[38:39],v199 offset:8480
v_mul_f32 v245, s72, v245
MM v[104:107],a[48:51],a[232:235],v[104:107]
TR a[40:41],v199 offset:16448
TR a[42:43],v199 offset:16704
MM v[112:115],a[48:51],a[240:243],v[112:115]
TR a[44:45],v199 offset:24672
TR a[46:47],v199 offset:24928
RD a[164:167],v223 offset:32768
MM v[112:115],a[52:55],a[244:247],v[112:115]
RD a[168:171],v217 offset:32768
MM v[120:123],a[52:55],a[252:255],v[120:123]
RD a[172:175],v218 offset:32768
MM v[120:123],a[48:51],a[248:251],v[120:123]
RD a[176:179],v222 offset:36864
MM v[100:103],a[56:59],a[224:227],v[100:103]
RD a[180:183],v223 offset:36864
MM v[100:103],a[60:63],a[228:231],v[100:103]
RD a[184:187],v217 offset:36864
MM v[108:111],a[60:63],a[236:239],v[108:111]
RD a[188:191],v218 offset:36864
MM v[108:111],a[56:59],a[232:235],v[108:111]
s_add_u32 s77, s64, 0
s_add_u32 s81, s77, 896
s_add_u32 m0, s65, 65536
MM v[116:119],a[56:59],a[240:243],v[116:119]
buffer_load_dwordx4 v235, s[12:15], s81 offen offset:0 lds
MM v[116:119],a[60:63],a[244:247],v[116:119]
MM v[124:127],a[60:63],a[252:255],v[124:127]
MM v[124:127],a[56:59],a[248:251],v[124:127]
s_waitcnt lgkmcnt(0)
s_barrier
MM v[0:3],a[64:67],a[128:131],v[0:3]
s_mov_b32 s56, 0x000f000f
TR a[48:49],v199 offset:8
TR a[50:51],v199 offset:264
MM v[0:3],a[68:71],a[132:135],v[0:3]
s_mov_b32 s57, s56
TR a[52:53],v199 offset:8232
TR a[54:55],v199 offset:8488
MM v[0:3],a[72:75],a[160:163],v[0:3]
TR a[56:57],v199 offset:16456
TR a[58:59],v199 offset:16712
MM v[0:3],a[76:79],a[164:167],v[0:3]
buffer_load_dwordx4 v235, s[12:15], s77 offen offset:1024 lds
)ASM"
R"ASM(
MM v[8:11],a[76:79],a[172:175],v[8:11]
s_lshl_b64 s[58:59], s[56:57], 4
TR a[60:61],v199 offset:24680
TR a[62:63],v199 offset:24936
MM v[8:11],a[72:75],a[168:171],v[8:11]
v_xor_b32 v151, 0, v253
v_xor_b32 v143, 0, v253
MM v[8:11],a[68:71],a[140:143],v[8:11]
RD v[144:147],v151 offset:0
RD v[148:151],v151 offset:16
MM v[8:11],a[64:67],a[136:139],v[8:11]
RD v[136:139],v143 offset:0
RD v[140:143],v143 offset:16
MM v[4:7],a[80:83],a[128:131],v[4:7]
MM v[4:7],a[84:87],a[132:135],v[4:7]
MM v[4:7],a[88:91],a[160:163],v[4:7]
s_waitcnt lgkmcnt(2)
MM v[4:7],a[92:95],a[164:167],v[4:7]
s_waitcnt lgkmcnt(0)
MM v[12:15],a[92:95],a[172:175],v[12:15]
buffer_load_dwordx4 v235, s[12:15], s81 offen offset:2048 lds
MM v[12:15],a[88:91],a[168:171],v[12:15]
MM v[12:15],a[84:87],a[140:143],v[12:15]
MM v[12:15],a[80:83],a[136:139],v[12:15]
MM v[32:35],a[96:99],a[128:131],v[32:35]
MM v[32:35],a[100:103],a[132:135],v[32:35]
MM v[32:35],a[104:107],a[160:163],v[32:35]
MM v[32:35],a[108:111],a[164:167],v[32:35]
v_pk_fma_f32 v[0:1], v[240:241], v[144:145], v[0:1] op_sel:[0,0,0] op_sel_hi:[0,1,1]
v_pk_fma_f32 v[2:3], v[240:241], v[146:147], v[2:3] op_sel:[0,0,0] op_sel_hi:[0,1,1]
v_pk_fma_f32 v[4:5], v[240:241], v[148:149], v[4:5] op_sel:[0,0,0] op_sel_hi:[0,1,1]
v_pk_fma_f32 v[6:7], v[240:241], v[150:151], v[6:7] op_sel:[0,0,0] op_sel_hi:[0,1,1]
v_pk_mul_f32 v[0:1], v[246:247], v[0:1] op_sel:[0,0] op_sel_hi:[0,1]
v_pk_mul_f32 v[2:3], v[246:247], v[2:3] op_sel:[0,0] op_sel_hi:[0,1]
v_pk_mul_f32 v[4:5], v[246:247], v[4:5] op_sel:[0,0] op_sel_hi:[0,1]
v_pk_mul_f32 v[6:7], v[246:247], v[6:7] op_sel:[0,0] op_sel_hi:[0,1]
MM v[40:43],a[108:111],a[172:175],v[40:43]
CV v160,v0,v1
CV v161,v2,v3
MM v[40:43],a[104:107],a[168:171],v[40:43]
CV v162,v4,v5
CV v163,v6,v7
MM v[40:43],a[100:103],a[140:143],v[40:43]
buffer_load_dwordx4 v235, s[12:15], s77 offen offset:3072 lds
MM v[40:43],a[96:99],a[136:139],v[40:43]
s_add_u32 s77, s64, 32768
v_pk_fma_f32 v[8:9], v[240:241], v[144:145], v[8:9] op_sel:[1,0,0] op_sel_hi:[1,1,1]
v_pk_fma_f32 v[10:11], v[240:241], v[146:147], v[10:11] op_sel:[1,0,0] op_sel_hi:[1,1,1]
v_pk_fma_f32 v[12:13], v[240:241], v[148:149], v[12:13] op_sel:[1,0,0] op_sel_hi:[1,1,1]
v_pk_fma_f32 v[14:15], v[240:241], v[150:151], v[14:15] op_sel:[1,0,0] op_sel_hi:[1,1,1]
v_pk_mul_f32 v[8:9], v[246:247], v[8:9] op_sel:[1,0] op_sel_hi:[1,1]
v_pk_mul_f32 v[10:11], v[246:247], v[10:11] op_sel:[1,0] op_sel_hi:[1,1]
v_pk_mul_f32 v[12:13], v[246:247], v[12:13] op_sel:[1,0] op_sel_hi:[1,1]
v_pk_mul_f32 v[14:15], v[246:247], v[14:15] op_sel:[1,0] op_sel_hi:[1,1]
MM v[36:39],a[112:115],a[128:131],v[36:39]
s_add_u32 m0, s65, 32768
CV v164,v8,v9
CV v165,v10,v11
MM v[36:39],a[116:119],a[132:135],v[36:39]
CV v166,v12,v13
CV v167,v14,v15
MM v[36:39],a[120:123],a[160:163],v[36:39]
v_xor_b32 v159, 128, v253
MM v[36:39],a[124:127],a[164:167],v[36:39]
RD v[152:155],v159 offset:0
RD v[156:159],v159 offset:16
MM v[44:47],a[124:127],a[172:175],v[44:47]
MM v[44:47],a[120:123],a[168:171],v[44:47]
MM v[44:47],a[116:119],a[140:143],v[44:47]
MM v[44:47],a[112:115],a[136:139],v[44:47]
s_waitcnt lgkmcnt(0)
MM v[64:67],a[0:3],a[128:131],v[64:67]
buffer_load_dwordx4 v228, s[8:11], s77 offen offset:0 lds
MM v[64:67],a[4:7],a[132:135],v[64:67]
MM v[64:67],a[8:11],a[160:163],v[64:67]
MM v[64:67],a[12:15],a[164:167],v[64:67]
v_pk_fma_f32 v[32:33], v[240:241], v[152:153], v[32:33] op_sel:[0,0,0] op_sel_hi:[0,1,1]
v_pk_fma_f32 v[34:35], v[240:241], v[154:155], v[34:35] op_sel:[0,0,0] op_sel_hi:[0,1,1]
v_pk_fma_f32 v[36:37], v[240:241], v[156:157], v[36:37] op_sel:[0,0,0] op_sel_hi:[0,1,1]
v_pk_fma_f32 v[38:39], v[240:241], v[158:159], v[38:39] op_sel:[0,0,0] op_sel_hi:[0,1,1]
v_pk_mul_f32 v[32:33], v[246:247], v[32:33] op_sel:[0,0] op_sel_hi:[0,1]
v_pk_mul_f32 v[34:35], v[246:247], v[34:35] op_sel:[0,0] op_sel_hi:[0,1]
v_pk_mul_f32 v[36:37], v[246:247], v[36:37] op_sel:[0,0] op_sel_hi:[0,1]
v_pk_mul_f32 v[38:39], v[246:247], v[38:39] op_sel:[0,0] op_sel_hi:[0,1]
MM v[72:75],a[12:15],a[172:175],v[72:75]
CV v176,v32,v33
CV v177,v34,v35
MM v[72:75],a[8:11],a[168:171],v[72:75]
CV v178,v36,v37
CV v179,v38,v39
MM v[72:75],a[4:7],a[140:143],v[72:75]
v_pk_fma_f32 v[40:41], v[240:241], v[152:153], v[40:41] op_sel:[1,0,0] op_sel_hi:[1,1,1]
v_pk_fma_f32 v[42:43], v[240:241], v[154:155], v[42:43] op_sel:[1,0,0] op_sel_hi:[1,1,1]
v_pk_fma_f32 v[44:45], v[240:241], v[156:157], v[44:45] op_sel:[1,0,0] op_sel_hi:[1,1,1]
v_pk_fma_f32 v[46:47], v[240:241], v[158:159], v[46:47] op_sel:[1,0,0] op_sel_hi:[1,1,1]
v_pk_mul_f32 v[40:41], v[246:247], v[40:41] op_sel:[1,0] op_sel_hi:[1,1]
v_pk_mul_f32 v[42:43], v[246:247], v[42:43] op_sel:[1,0] op_sel_hi:[1,1]
v_pk_mul_f32 v[44:45], v[246:247], v[44:45] op_sel:[1,0] op_sel_hi:[1,1]
v_pk_mul_f32 v[46:47], v[246:247], v[46:47] op_sel:[1,0] op_sel_hi:[1,1]
MM v[72:75],a[0:3],a[136:139],v[72:75]
CV v180,v40,v41
CV v181,v42,v43
MM v[68:71],a[16:19],a[128:131],v[68:71]
CV v182,v44,v45
CV v183,v46,v47
MM v[68:71],a[20:23],a[132:135],v[68:71]
v_xor_b32 v151, 256, v253
MM v[68:71],a[24:27],a[160:163],v[68:71]
buffer_load_dwordx4 v229, s[8:11], s77 offen offset:1024 lds
MM v[68:71],a[28:31],a[164:167],v[68:71]
RD v[144:147],v151 offset:0
RD v[148:151],v151 offset:16
MM v[76:79],a[28:31],a[172:175],v[76:79]
MM v[76:79],a[24:27],a[168:171],v[76:79]
MM v[76:79],a[20:23],a[140:143],v[76:79]
MM v[76:79],a[16:19],a[136:139],v[76:79]
s_waitcnt lgkmcnt(0)
MM v[96:99],a[32:35],a[128:131],v[96:99]
MM v[96:99],a[36:39],a[132:135],v[96:99]
MM v[96:99],a[40:43],a[160:163],v[96:99]
MM v[96:99],a[44:47],a[164:167],v[96:99]
v_pk_fma_f32 v[64:65], v[240:241], v[144:145], v[64:65] op_sel:[0,0,0] op_sel_hi:[0,1,1]
v_pk_fma_f32 v[66:67], v[240:241], v[146:147], v[66:67] op_sel:[0,0,0] op_sel_hi:[0,1,1]
v_pk_fma_f32 v[68:69], v[240:241], v[148:149], v[68:69] op_sel:[0,0,0] op_sel_hi:[0,1,1]
v_pk_fma_f32 v[70:71], v[240:241], v[150:151], v[70:71] op_sel:[0,0,0] op_sel_hi:[0,1,1]
v_pk_mul_f32 v[64:65], v[246:247], v[64:65] op_sel:[0,0] op_sel_hi:[0,1]
v_pk_mul_f32 v[66:67], v[246:247], v[66:67] op_sel:[0,0] op_sel_hi:[0,1]
v_pk_mul_f32 v[68:69], v[246:247], v[68:69] op_sel:[0,0] op_sel_hi:[0,1]
v_pk_mul_f32 v[70:71], v[246:247], v[70:71] op_sel:[0,0] op_sel_hi:[0,1]
MM v[104:107],a[44:47],a[172:175],v[104:107]
buffer_load_dwordx4 v230, s[8:11], s77 offen offset:2048 lds
MM v[104:107],a[40:43],a[168:171],v[104:107]
CV v192,v64,v65
CV v193,v66,v67
MM v[104:107],a[36:39],a[140:143],v[104:107]
CV v194,v68,v69
CV v195,v70,v71
MM v[104:107],a[32:35],a[136:139],v[104:107]
v_pk_fma_f32 v[72:73], v[240:241], v[144:145], v[72:73] op_sel:[1,0,0] op_sel_hi:[1,1,1]
v_pk_fma_f32 v[74:75], v[240:241], v[146:147], v[74:75] op_sel:[1,0,0] op_sel_hi:[1,1,1]
v_pk_fma_f32 v[76:77], v[240:241], v[148:149], v[76:77] op_sel:[1,0,0] op_sel_hi:[1,1,1]
v_pk_fma_f32 v[78:79], v[240:241], v[150:151], v[78:79] op_sel:[1,0,0] op_sel_hi:[1,1,1]
v_pk_mul_f32 v[72:73], v[246:247], v[72:73] op_sel:[1,0] op_sel_hi:[1,1]
v_pk_mul_f32 v[74:75], v[246:247], v[74:75] op_sel:[1,0] op_sel_hi:[1,1]
v_pk_mul_f32 v[76:77], v[246:247], v[76:77] op_sel:[1,0] op_sel_hi:[1,1]
v_pk_mul_f32 v[78:79], v[246:247], v[78:79] op_sel:[1,0] op_sel_hi:[1,1]
MM v[100:103],a[48:51],a[128:131],v[100:103]
CV v196,v72,v73
CV v197,v74,v75
MM v[100:103],a[52:55],a[132:135],v[100:103]
CV v198,v76,v77
CV v199,v78,v79
MM v[100:103],a[56:59],a[160:163],v[100:103]
v_xor_b32 v159, 384, v253
MM v[100:103],a[60:63],a[164:167],v[100:103]
RD v[152:155],v159 offset:0
RD v[156:159],v159 offset:16
MM v[108:111],a[60:63],a[172:175],v[108:111]
MM v[108:111],a[56:59],a[168:171],v[108:111]
MM v[108:111],a[52:55],a[140:143],v[108:111]
buffer_load_dwordx4 v231, s[8:11], s77 offen offset:3072 lds
MM v[108:111],a[48:51],a[136:139],v[108:111]
s_add_u32 s77, s64, 16384
s_add_u32 s81, s77, 896
s_add_u32 m0, s65, 81920
MM v[16:19],a[64:67],a[144:147],v[16:19]
s_waitcnt lgkmcnt(0)
MM v[16:19],a[68:71],a[148:151],v[16:19]
s_nop 0
MM v[16:19],a[72:75],a[176:179],v[16:19]
LPG4_%=:
s_cbranch_execz LPG8_%=
MM v[16:19],a[76:79],a[180:183],v[16:19]
v_pk_fma_f32 v[96:97], v[240:241], v[152:153], v[96:97] op_sel:[0,0,0] op_sel_hi:[0,1,1]
v_pk_fma_f32 v[98:99], v[240:241], v[154:155], v[98:99] op_sel:[0,0,0] op_sel_hi:[0,1,1]
v_pk_fma_f32 v[100:101], v[240:241], v[156:157], v[100:101] op_sel:[0,0,0] op_sel_hi:[0,1,1]
v_pk_fma_f32 v[102:103], v[240:241], v[158:159], v[102:103] op_sel:[0,0,0] op_sel_hi:[0,1,1]
v_pk_mul_f32 v[96:97], v[246:247], v[96:97] op_sel:[0,0] op_sel_hi:[0,1]
v_pk_mul_f32 v[98:99], v[246:247], v[98:99] op_sel:[0,0] op_sel_hi:[0,1]
v_pk_mul_f32 v[100:101], v[246:247], v[100:101] op_sel:[0,0] op_sel_hi:[0,1]
v_pk_mul_f32 v[102:103], v[246:247], v[102:103] op_sel:[0,0] op_sel_hi:[0,1]
MM v[24:27],a[76:79],a[188:191],v[24:27]
CV v208,v96,v97
CV v209,v98,v99
MM v[24:27],a[72:75],a[184:187],v[24:27]
CV v210,v100,v101
CV v211,v102,v103
MM v[24:27],a[68:71],a[156:159],v[24:27]
s_waitcnt vmcnt(12)
s_barrier
RD a[192:195],v224 offset:0
MM v[24:27],a[64:67],a[152:155],v[24:27]
RD a[196:199],v225 offset:0
RD a[200:203],v226 offset:0
MM v[20:23],a[80:83],a[144:147],v[20:23]
buffer_load_dwordx4 v235, s[12:15], s81 offen offset:0 lds
MM v[20:23],a[84:87],a[148:151],v[20:23]
RD a[204:207],v227 offset:0
RD a[208:211],v224 offset:4096
MM v[20:23],a[88:91],a[176:179],v[20:23]
RD a[212:215],v225 offset:4096
RD a[216:219],v226 offset:4096
MM v[20:23],a[92:95],a[180:183],v[20:23]
RD a[220:223],v227 offset:4096
RD a[224:227],v224 offset:8192
MM v[28:31],a[92:95],a[188:191],v[28:31]
RD a[228:231],v225 offset:8192
RD a[232:235],v226 offset:8192
MM v[28:31],a[88:91],a[184:187],v[28:31]
RD a[236:239],v227 offset:8192
RD a[240:243],v224 offset:12288
MM v[28:31],a[84:87],a[156:159],v[28:31]
RD a[244:247],v225 offset:12288
RD a[248:251],v226 offset:12288
MM v[28:31],a[80:83],a[152:155],v[28:31]
RD a[252:255],v227 offset:12288
MM v[48:51],a[96:99],a[144:147],v[48:51]
v_pk_fma_f32 v[104:105], v[240:241], v[152:153], v[104:105] op_sel:[1,0,0] op_sel_hi:[1,1,1]
v_pk_fma_f32 v[106:107], v[240:241], v[154:155], v[106:107] op_sel:[1,0,0] op_sel_hi:[1,1,1]
v_pk_fma_f32 v[108:109], v[240:241], v[156:157], v[108:109] op_sel:[1,0,0] op_sel_hi:[1,1,1]
v_pk_fma_f32 v[110:111], v[240:241], v[158:159], v[110:111] op_sel:[1,0,0] op_sel_hi:[1,1,1]
v_pk_mul_f32 v[104:105], v[246:247], v[104:105] op_sel:[1,0] op_sel_hi:[1,1]
v_pk_mul_f32 v[106:107], v[246:247], v[106:107] op_sel:[1,0] op_sel_hi:[1,1]
v_pk_mul_f32 v[108:109], v[246:247], v[108:109] op_sel:[1,0] op_sel_hi:[1,1]
v_pk_mul_f32 v[110:111], v[246:247], v[110:111] op_sel:[1,0] op_sel_hi:[1,1]
MM v[48:51],a[100:103],a[148:151],v[48:51]
CV v212,v104,v105
CV v213,v106,v107
MM v[48:51],a[104:107],a[176:179],v[48:51]
buffer_load_dwordx4 v235, s[12:15], s77 offen offset:1024 lds
MM v[48:51],a[108:111],a[180:183],v[48:51]
CV v214,v108,v109
CV v215,v110,v111
MM v[56:59],a[108:111],a[188:191],v[56:59]
v_mov_b32 v247, 0
v_mov_b32 v236, 0
MM v[56:59],a[104:107],a[184:187],v[56:59]
v_mov_b32 v237, 0
v_cndmask_b32 v144, 0, v238, s[56:57]
MM v[56:59],a[100:103],a[156:159],v[56:59]
v_cndmask_b32 v145, 0, v238, s[56:57]
v_cndmask_b32 v146, 0, v238, s[56:57]
MM v[56:59],a[96:99],a[152:155],v[56:59]
v_cndmask_b32 v147, 0, v238, s[56:57]
v_cndmask_b32 v148, 0, v238, s[58:59]
MM v[52:55],a[112:115],a[144:147],v[52:55]
v_cndmask_b32 v149, 0, v238, s[58:59]
v_cndmask_b32 v150, 0, v238, s[58:59]
MM v[52:55],a[116:119],a[148:151],v[52:55]
v_pk_fma_f32 v[16:17], v[242:243], v[136:137], v[16:17] op_sel:[0,0,0] op_sel_hi:[0,1,1]
v_pk_fma_f32 v[18:19], v[242:243], v[138:139], v[18:19] op_sel:[0,0,0] op_sel_hi:[0,1,1]
v_pk_fma_f32 v[20:21], v[242:243], v[140:141], v[20:21] op_sel:[0,0,0] op_sel_hi:[0,1,1]
v_pk_fma_f32 v[22:23], v[242:243], v[142:143], v[22:23] op_sel:[0,0,0] op_sel_hi:[0,1,1]
v_pk_mul_f32 v[16:17], v[244:245], v[16:17] op_sel:[0,0] op_sel_hi:[0,1]
v_pk_mul_f32 v[18:19], v[244:245], v[18:19] op_sel:[0,0] op_sel_hi:[0,1]
v_pk_mul_f32 v[20:21], v[244:245], v[20:21] op_sel:[0,0] op_sel_hi:[0,1]
v_pk_mul_f32 v[22:23], v[244:245], v[22:23] op_sel:[0,0] op_sel_hi:[0,1]
)ASM"
R"ASM(
MM v[52:55],a[120:123],a[176:179],v[52:55]
v_cndmask_b32 v151, 0, v238, s[58:59]
CV v168,v16,v17
MM v[52:55],a[124:127],a[180:183],v[52:55]
CV v169,v18,v19
CV v170,v20,v21
s_waitcnt lgkmcnt(0)
MM v[0:3],a[192:195],v[160:163],0
buffer_load_dwordx4 v235, s[12:15], s81 offen offset:2048 lds
MM v[0:3],a[196:199],v[176:179],v[0:3]
CV v171,v22,v23
MM v[0:3],a[200:203],v[192:195],v[0:3]
v_pk_fma_f32 v[24:25], v[242:243], v[136:137], v[24:25] op_sel:[1,0,0] op_sel_hi:[1,1,1]
v_pk_fma_f32 v[26:27], v[242:243], v[138:139], v[26:27] op_sel:[1,0,0] op_sel_hi:[1,1,1]
v_pk_fma_f32 v[28:29], v[242:243], v[140:141], v[28:29] op_sel:[1,0,0] op_sel_hi:[1,1,1]
v_pk_fma_f32 v[30:31], v[242:243], v[142:143], v[30:31] op_sel:[1,0,0] op_sel_hi:[1,1,1]
v_pk_mul_f32 v[24:25], v[244:245], v[24:25] op_sel:[1,0] op_sel_hi:[1,1]
v_pk_mul_f32 v[26:27], v[244:245], v[26:27] op_sel:[1,0] op_sel_hi:[1,1]
v_pk_mul_f32 v[28:29], v[244:245], v[28:29] op_sel:[1,0] op_sel_hi:[1,1]
v_pk_mul_f32 v[30:31], v[244:245], v[30:31] op_sel:[1,0] op_sel_hi:[1,1]
MM v[0:3],a[204:207],v[208:211],v[0:3]
CV v172,v24,v25
CV v173,v26,v27
MM v[8:11],a[192:195],v[164:167],0
CV v174,v28,v29
CV v175,v30,v31
MM v[8:11],a[196:199],v[180:183],v[8:11]
v_xor_b32 v143, 128, v253
MM v[8:11],a[200:203],v[196:199],v[8:11]
RD v[136:139],v143 offset:0
RD v[140:143],v143 offset:16
MM v[8:11],a[204:207],v[212:215],v[8:11]
MM v[4:7],a[208:211],v[160:163],0
v_max3_f32 v152, v0, v1, v2
v_max_f32 v152, v152, v3
MM v[4:7],a[212:215],v[176:179],v[4:7]
v_mov_b32 v153, v152
MM v[4:7],a[216:219],v[192:195],v[4:7]
buffer_load_dwordx4 v235, s[12:15], s77 offen offset:3072 lds
MM v[4:7],a[220:223],v[208:211],v[4:7]
s_add_u32 s77, s64, 32768
s_add_u32 s81, s77, 896
s_add_u32 m0, s65, 98304
v_permlane16_swap_b32 v152, v153
v_max_f32 v152, v152, v153
MM v[12:15],a[208:211],v[164:167],0
s_waitcnt vmcnt(8)
s_barrier
RD a[192:195],v224 offset:16384
v_mov_b32 v153, v152
MM v[12:15],a[212:215],v[180:183],v[12:15]
s_waitcnt lgkmcnt(1)
RD a[196:199],v225 offset:16384
RD a[200:203],v226 offset:16384
v_permlane32_swap_b32 v152, v153
v_max_f32 v152, v152, v153
v_sub_f32 v128, 0, v152
MM v[12:15],a[216:219],v[196:199],v[12:15]
RD a[204:207],v227 offset:16384
v_sub_f32 v129, 0, v152
v_sub_f32 v130, 0, v152
MM v[12:15],a[220:223],v[212:215],v[12:15]
v_sub_f32 v131, 0, v152
v_max3_f32 v154, v8, v9, v10
MM v[32:35],a[224:227],v[160:163],0
RD a[208:211],v224 offset:20480
RD a[212:215],v225 offset:20480
v_max_f32 v154, v154, v11
v_mov_b32 v155, v154
MM v[32:35],a[228:231],v[176:179],v[32:35]
RD a[216:219],v226 offset:20480
RD a[220:223],v227 offset:20480
v_permlane16_swap_b32 v154, v155
v_max_f32 v154, v154, v155
MM v[32:35],a[232:235],v[192:195],v[32:35]
v_mov_b32 v155, v154
v_sub_f32 v0, v0, v152
MM v[32:35],a[236:239],v[208:211],v[32:35]
v_permlane32_swap_b32 v154, v155
v_max_f32 v154, v154, v155
v_sub_f32 v132, 0, v154
MM v[40:43],a[224:227],v[164:167],0
buffer_load_dwordx4 v235, s[12:15], s81 offen offset:0 lds
v_sub_f32 v133, 0, v154
v_sub_f32 v134, 0, v154
v_sub_f32 v135, 0, v154
MM v[40:43],a[228:231],v[180:183],v[40:43]
v_sub_f32 v1, v1, v152
EX v0,v0
EX v1,v1
MM v[40:43],a[232:235],v[196:199],v[40:43]
v_sub_f32 v2, v2, v152
v_sub_f32 v3, v3, v152
MM v[40:43],a[236:239],v[212:215],v[40:43]
EX v2,v2
MM v[36:39],a[240:243],v[160:163],v[128:131]
RD a[224:227],v224 offset:24576
RD a[228:231],v225 offset:24576
EX v3,v3
MM v[36:39],a[244:247],v[176:179],v[36:39]
RD a[232:235],v226 offset:24576
RD a[236:239],v227 offset:24576
CV v0,v0,v1
v_sub_f32 v4, v4, v152
MM v[36:39],a[248:251],v[192:195],v[36:39]
v_sub_f32 v5, v5, v152
EX v4,v4
MM v[36:39],a[252:255],v[208:211],v[36:39]
EX v5,v5
MM v[44:47],a[240:243],v[164:167],v[132:135]
CV v1,v2,v3
v_sub_f32 v6, v6, v152
MM v[44:47],a[244:247],v[180:183],v[44:47]
v_sub_f32 v7, v7, v152
MM v[44:47],a[248:251],v[196:199],v[44:47]
buffer_load_dwordx4 v235, s[12:15], s77 offen offset:1024 lds
MM v[44:47],a[252:255],v[212:215],v[44:47]
EX v6,v6
EX v7,v7
CV v2,v4,v5
MM v[60:63],a[124:127],a[188:191],v[60:63]
RD a[240:243],v224 offset:28672
RD a[244:247],v225 offset:28672
v_sub_f32 v8, v8, v154
v_sub_f32 v9, v9, v154
MM v[60:63],a[120:123],a[184:187],v[60:63]
RD a[248:251],v226 offset:28672
RD a[252:255],v227 offset:28672
EX v8,v8
MM v[60:63],a[116:119],a[156:159],v[60:63]
EX v9,v9
MM v[60:63],a[112:115],a[152:155],v[60:63]
CV v3,v6,v7
v_sub_f32 v10, v10, v154
MM v[80:83],a[0:3],a[144:147],v[80:83]
v_sub_f32 v11, v11, v154
EX v10,v10
MM v[80:83],a[4:7],a[148:151],v[80:83]
EX v11,v11
MM v[80:83],a[8:11],a[176:179],v[80:83]
CV v8,v8,v9
v_sub_f32 v12, v12, v154
MM v[80:83],a[12:15],a[180:183],v[80:83]
v_pk_fma_f32 v[48:49], v[242:243], v[136:137], v[48:49] op_sel:[0,0,0] op_sel_hi:[0,1,1]
v_pk_fma_f32 v[50:51], v[242:243], v[138:139], v[50:51] op_sel:[0,0,0] op_sel_hi:[0,1,1]
v_pk_fma_f32 v[52:53], v[242:243], v[140:141], v[52:53] op_sel:[0,0,0] op_sel_hi:[0,1,1]
v_pk_fma_f32 v[54:55], v[242:243], v[142:143], v[54:55] op_sel:[0,0,0] op_sel_hi:[0,1,1]
v_pk_mul_f32 v[48:49], v[244:245], v[48:49] op_sel:[0,0] op_sel_hi:[0,1]
v_pk_mul_f32 v[50:51], v[244:245], v[50:51] op_sel:[0,0] op_sel_hi:[0,1]
v_pk_mul_f32 v[52:53], v[244:245], v[52:53] op_sel:[0,0] op_sel_hi:[0,1]
v_pk_mul_f32 v[54:55], v[244:245], v[54:55] op_sel:[0,0] op_sel_hi:[0,1]
v_sub_f32 v13, v13, v154
EX v12,v12
MM v[88:91],a[12:15],a[188:191],v[88:91]
buffer_load_dwordx4 v235, s[12:15], s81 offen offset:2048 lds
MM v[88:91],a[8:11],a[184:187],v[88:91]
EX v13,v13
CV v9,v10,v11
MM v[88:91],a[4:7],a[156:159],v[88:91]
v_sub_f32 v14, v14, v154
v_sub_f32 v15, v15, v154
EX v14,v14
MM v[88:91],a[0:3],a[152:155],v[88:91]
EX v15,v15
MM v[84:87],a[16:19],a[144:147],v[84:87]
CV v10,v12,v13
v_sub_f32 v32, v32, v152
MM v[84:87],a[20:23],a[148:151],v[84:87]
v_sub_f32 v33, v33, v152
CV v184,v48,v49
MM v[84:87],a[24:27],a[176:179],v[84:87]
EX v32,v32
MM v[84:87],a[28:31],a[180:183],v[84:87]
EX v33,v33
MM v[92:95],a[28:31],a[188:191],v[92:95]
CV v11,v14,v15
v_sub_f32 v34, v34, v152
v_sub_f32 v35, v35, v152
MM v[92:95],a[24:27],a[184:187],v[92:95]
EX v34,v34
MM v[92:95],a[20:23],a[156:159],v[92:95]
buffer_load_dwordx4 v235, s[12:15], s77 offen offset:3072 lds
MM v[92:95],a[16:19],a[152:155],v[92:95]
s_add_u32 s77, s64, 49152
s_add_u32 m0, s65, 49152
CV v185,v50,v51
EX v35,v35
CV v4,v32,v33
EX v36,v36
MM v[112:115],a[32:35],a[144:147],v[112:115]
buffer_load_dwordx4 v228, s[8:11], s77 offen offset:0 lds
MM v[112:115],a[36:39],a[148:151],v[112:115]
EX v37,v37
CV v5,v34,v35
MM v[112:115],a[40:43],a[176:179],v[112:115]
EX v38,v38
EX v39,v39
MM v[112:115],a[44:47],a[180:183],v[112:115]
buffer_load_dwordx4 v229, s[8:11], s77 offen offset:1024 lds
MM v[120:123],a[44:47],a[188:191],v[120:123]
CV v6,v36,v37
v_sub_f32 v40, v40, v154
v_sub_f32 v41, v41, v154
MM v[120:123],a[40:43],a[184:187],v[120:123]
EX v40,v40
MM v[120:123],a[36:39],a[156:159],v[120:123]
EX v41,v41
MM v[120:123],a[32:35],a[152:155],v[120:123]
buffer_load_dwordx4 v230, s[8:11], s77 offen offset:2048 lds
MM v[116:119],a[48:51],a[144:147],v[116:119]
CV v7,v38,v39
v_sub_f32 v42, v42, v154
v_sub_f32 v43, v43, v154
EX v42,v42
MM v[116:119],a[52:55],a[148:151],v[116:119]
EX v43,v43
MM v[116:119],a[56:59],a[176:179],v[116:119]
CV v12,v40,v41
CV v186,v52,v53
EX v44,v44
MM v[116:119],a[60:63],a[180:183],v[116:119]
buffer_load_dwordx4 v231, s[8:11], s77 offen offset:3072 lds
MM v[124:127],a[60:63],a[188:191],v[124:127]
s_add_u32 s77, s64, 49152
s_add_u32 s81, s77, 896
s_add_u32 m0, s65, 114688
EX v45,v45
CV v13,v42,v43
MM v[124:127],a[56:59],a[184:187],v[124:127]
EX v46,v46
MM v[124:127],a[52:55],a[156:159],v[124:127]
EX v47,v47
CV v14,v44,v45
CV v15,v46,v47
MM v[124:127],a[48:51],a[152:155],v[124:127]
buffer_load_dwordx4 v235, s[12:15], s81 offen offset:0 lds
v_smfmac_f32_16x16x64_bf16 v[236:239], v[144:147], v[0:7], v234
CV v187,v54,v55
v_smfmac_f32_16x16x64_bf16 v[236:239], v[148:151], v[8:15], v234
s_waitcnt lgkmcnt(0)
buffer_load_dwordx4 v235, s[12:15], s77 offen offset:1024 lds
buffer_load_dwordx4 v235, s[12:15], s81 offen offset:2048 lds
buffer_load_dwordx4 v235, s[12:15], s77 offen offset:3072 lds
s_add_u32 s64, s64, 49152
MM v[64:67],a[192:195],v[160:163],v[128:131]
s_add_u32 s64, s64, 16384
TR a[128:129],v232 offset:0
TR a[130:131],v232 offset:4096
MM v[64:67],a[196:199],v[176:179],v[64:67]
s_add_u32 s81, s64, 896
v_pk_fma_f32 v[56:57], v[242:243], v[136:137], v[56:57] op_sel:[1,0,0] op_sel_hi:[1,1,1]
v_pk_fma_f32 v[58:59], v[242:243], v[138:139], v[58:59] op_sel:[1,0,0] op_sel_hi:[1,1,1]
v_pk_fma_f32 v[60:61], v[242:243], v[140:141], v[60:61] op_sel:[1,0,0] op_sel_hi:[1,1,1]
v_pk_fma_f32 v[62:63], v[242:243], v[142:143], v[62:63] op_sel:[1,0,0] op_sel_hi:[1,1,1]
v_pk_mul_f32 v[56:57], v[244:245], v[56:57] op_sel:[1,0] op_sel_hi:[1,1]
v_pk_mul_f32 v[58:59], v[244:245], v[58:59] op_sel:[1,0] op_sel_hi:[1,1]
v_pk_mul_f32 v[60:61], v[244:245], v[60:61] op_sel:[1,0] op_sel_hi:[1,1]
v_pk_mul_f32 v[62:63], v[244:245], v[62:63] op_sel:[1,0] op_sel_hi:[1,1]
MM v[64:67],a[200:203],v[192:195],v[64:67]
TR a[132:133],v232 offset:8192
TR a[134:135],v232 offset:12288
MM v[64:67],a[204:207],v[208:211],v[64:67]
TR a[136:137],v233 offset:0
v_add_f32 v246, v236, v237
MM v[68:71],a[220:223],v[208:211],v[128:131]
TR a[138:139],v233 offset:4096
v_cmp_nge_f32 vcc, s71, v246
MM v[68:71],a[216:219],v[192:195],v[68:71]
s_cbranch_vccnz LSITE0_%=
LBACK0_%=:
TR a[140:141],v233 offset:8192
TR a[142:143],v233 offset:12288
MM v[68:71],a[212:215],v[176:179],v[68:71]
TR a[144:145],v232 offset:512
TR a[146:147],v232 offset:4608
MM v[68:71],a[208:211],v[160:163],v[68:71]
TR a[148:149],v232 offset:8704
CV v188,v56,v57
MM v[76:79],a[208:211],v[164:167],v[132:135]
TR a[150:151],v232 offset:12800
CV v189,v58,v59
MM v[76:79],a[212:215],v[180:183],v[76:79]
EX v64,v64
MM v[76:79],a[216:219],v[196:199],v[76:79]
TR a[152:153],v233 offset:512
CV v190,v60,v61
MM v[76:79],a[220:223],v[212:215],v[76:79]
TR a[154:155],v233 offset:4608
CV v191,v62,v63
MM v[72:75],a[204:207],v[212:215],v[132:135]
EX v65,v65
MM v[72:75],a[200:203],v[196:199],v[72:75]
TR a[156:157],v233 offset:8704
v_xor_b32 v143, 256, v253
MM v[72:75],a[196:199],v[180:183],v[72:75]
TR a[158:159],v233 offset:12800
RD v[136:139],v143 offset:0
MM v[72:75],a[192:195],v[164:167],v[72:75]
EX v66,v66
MM v[96:99],a[224:227],v[160:163],v[128:131]
TR a[160:161],v232 offset:1024
RD v[140:143],v143 offset:16
EX v67,v67
MM v[96:99],a[228:231],v[176:179],v[96:99]
TR a[162:163],v232 offset:5120
TR a[164:165],v232 offset:9216
MM v[96:99],a[232:235],v[192:195],v[96:99]
TR a[166:167],v232 offset:13312
TR a[168:169],v233 offset:1024
EX v68,v68
MM v[96:99],a[236:239],v[208:211],v[96:99]
TR a[170:171],v233 offset:5120
TR a[172:173],v233 offset:9216
MM v[100:103],a[252:255],v[208:211],v[128:131]
s_waitcnt lgkmcnt(6)
v_pk_fma_f32 v[80:81], v[242:243], v[136:137], v[80:81] op_sel:[0,0,0] op_sel_hi:[0,1,1]
v_pk_fma_f32 v[82:83], v[242:243], v[138:139], v[82:83] op_sel:[0,0,0] op_sel_hi:[0,1,1]
v_pk_fma_f32 v[84:85], v[242:243], v[140:141], v[84:85] op_sel:[0,0,0] op_sel_hi:[0,1,1]
v_pk_fma_f32 v[86:87], v[242:243], v[142:143], v[86:87] op_sel:[0,0,0] op_sel_hi:[0,1,1]
v_pk_mul_f32 v[80:81], v[244:245], v[80:81] op_sel:[0,0] op_sel_hi:[0,1]
v_pk_mul_f32 v[82:83], v[244:245], v[82:83] op_sel:[0,0] op_sel_hi:[0,1]
v_pk_mul_f32 v[84:85], v[244:245], v[84:85] op_sel:[0,0] op_sel_hi:[0,1]
v_pk_mul_f32 v[86:87], v[244:245], v[86:87] op_sel:[0,0] op_sel_hi:[0,1]
EX v69,v69
MM v[100:103],a[248:251],v[192:195],v[100:103]
TR a[174:175],v233 offset:13312
CV v64,v64,v65
MM v[100:103],a[244:247],v[176:179],v[100:103]
TR a[176:177],v232 offset:1536
EX v70,v70
MM v[100:103],a[240:243],v[160:163],v[100:103]
TR a[178:179],v232 offset:5632
TR a[180:181],v232 offset:9728
EX v71,v71
MM v[108:111],a[240:243],v[164:167],v[132:135]
TR a[182:183],v232 offset:13824
CV v200,v80,v81
MM v[108:111],a[244:247],v[180:183],v[108:111]
EX v72,v72
MM v[108:111],a[248:251],v[196:199],v[108:111]
TR a[184:185],v233 offset:1536
TR a[186:187],v233 offset:5632
TR a[188:189],v233 offset:9728
)ASM"
R"ASM(
TR a[190:191],v233 offset:13824
CV v201,v82,v83
EX v73,v73
MM v[108:111],a[252:255],v[212:215],v[108:111]
CV v202,v84,v85
CV v65,v66,v67
MM v[104:107],a[236:239],v[212:215],v[132:135]
CV v203,v86,v87
CV v66,v68,v69
EX v74,v74
MM v[104:107],a[232:235],v[196:199],v[104:107]
v_pk_fma_f32 v[88:89], v[242:243], v[136:137], v[88:89] op_sel:[1,0,0] op_sel_hi:[1,1,1]
v_pk_fma_f32 v[90:91], v[242:243], v[138:139], v[90:91] op_sel:[1,0,0] op_sel_hi:[1,1,1]
v_pk_fma_f32 v[92:93], v[242:243], v[140:141], v[92:93] op_sel:[1,0,0] op_sel_hi:[1,1,1]
v_pk_fma_f32 v[94:95], v[242:243], v[142:143], v[94:95] op_sel:[1,0,0] op_sel_hi:[1,1,1]
v_pk_mul_f32 v[88:89], v[244:245], v[88:89] op_sel:[1,0] op_sel_hi:[1,1]
v_pk_mul_f32 v[90:91], v[244:245], v[90:91] op_sel:[1,0] op_sel_hi:[1,1]
v_pk_mul_f32 v[92:93], v[244:245], v[92:93] op_sel:[1,0] op_sel_hi:[1,1]
v_pk_mul_f32 v[94:95], v[244:245], v[94:95] op_sel:[1,0] op_sel_hi:[1,1]
MM v[104:107],a[228:231],v[180:183],v[104:107]
EX v75,v75
MM v[104:107],a[224:227],v[164:167],v[104:107]
s_waitcnt vmcnt(12) lgkmcnt(0)
s_barrier
CV v67,v70,v71
EX v76,v76
MM a[0:3],a[128:131],v[0:3],0
s_add_u32 m0, s65, 0
EX v77,v77
MM a[0:3],a[132:135],v[4:7],a[0:3]
CV v72,v72,v73
CV v73,v74,v75
MM a[8:11],a[132:135],v[12:15],0
EX v78,v78
MM a[8:11],a[128:131],v[8:11],a[8:11]
CV v204,v88,v89
CV v205,v90,v91
MM a[12:15],a[136:139],v[8:11],0
EX v79,v79
MM a[12:15],a[140:143],v[12:15],a[12:15]
RD a[192:195],v224 offset:32768
CV v206,v92,v93
MM a[4:7],a[140:143],v[4:7],0
EX v96,v96
MM a[4:7],a[136:139],v[0:3],a[4:7]
CV v207,v94,v95
v_xor_b32 v143, 384, v253
MM a[32:35],a[144:147],v[0:3],0
EX v97,v97
MM a[32:35],a[148:151],v[4:7],a[32:35]
RD v[136:139],v143 offset:0
RD a[196:199],v225 offset:32768
EX v98,v98
MM a[40:43],a[148:151],v[12:15],0
buffer_load_dwordx4 v228, s[8:11], s64 offen offset:0 lds
MM a[40:43],a[144:147],v[8:11],a[40:43]
RD v[140:143],v143 offset:16
RD a[200:203],v226 offset:32768
CV v74,v76,v77
EX v99,v99
MM a[44:47],a[152:155],v[8:11],0
buffer_load_dwordx4 v229, s[8:11], s64 offen offset:1024 lds
MM a[44:47],a[156:159],v[12:15],a[44:47]
RD a[204:207],v227 offset:32768
EX v100,v100
MM a[36:39],a[156:159],v[4:7],0
EX v101,v101
CV v75,v78,v79
MM a[36:39],a[152:155],v[0:3],a[36:39]
s_waitcnt lgkmcnt(2)
RD a[208:211],v224 offset:36864
v_pk_fma_f32 v[112:113], v[242:243], v[136:137], v[112:113] op_sel:[0,0,0] op_sel_hi:[0,1,1]
v_pk_fma_f32 v[114:115], v[242:243], v[138:139], v[114:115] op_sel:[0,0,0] op_sel_hi:[0,1,1]
v_pk_fma_f32 v[116:117], v[242:243], v[140:141], v[116:117] op_sel:[0,0,0] op_sel_hi:[0,1,1]
v_pk_fma_f32 v[118:119], v[242:243], v[142:143], v[118:119] op_sel:[0,0,0] op_sel_hi:[0,1,1]
v_pk_mul_f32 v[112:113], v[244:245], v[112:113] op_sel:[0,0] op_sel_hi:[0,1]
v_pk_mul_f32 v[114:115], v[244:245], v[114:115] op_sel:[0,0] op_sel_hi:[0,1]
v_pk_mul_f32 v[116:117], v[244:245], v[116:117] op_sel:[0,0] op_sel_hi:[0,1]
v_pk_mul_f32 v[118:119], v[244:245], v[118:119] op_sel:[0,0] op_sel_hi:[0,1]
MM a[64:67],a[160:163],v[0:3],0
EX v102,v102
MM a[64:67],a[164:167],v[4:7],a[64:67]
buffer_load_dwordx4 v230, s[8:11], s64 offen offset:2048 lds
MM a[72:75],a[164:167],v[12:15],0
RD a[212:215],v225 offset:36864
EX v103,v103
CV v216,v112,v113
CV v68,v96,v97
MM a[72:75],a[160:163],v[8:11],a[72:75]
RD a[216:219],v226 offset:36864
CV v217,v114,v115
EX v104,v104
MM a[76:79],a[168:171],v[8:11],0
RD a[220:223],v227 offset:36864
CV v218,v116,v117
MM a[76:79],a[172:175],v[12:15],a[76:79]
EX v105,v105
CV v219,v118,v119
CV v69,v98,v99
MM a[68:71],a[172:175],v[4:7],0
buffer_load_dwordx4 v231, s[8:11], s64 offen offset:3072 lds
MM a[68:71],a[168:171],v[0:3],a[68:71]
s_add_u32 m0, s65, 65536
RD a[224:227],v224 offset:40960
v_pk_fma_f32 v[120:121], v[242:243], v[136:137], v[120:121] op_sel:[1,0,0] op_sel_hi:[1,1,1]
v_pk_fma_f32 v[122:123], v[242:243], v[138:139], v[122:123] op_sel:[1,0,0] op_sel_hi:[1,1,1]
v_pk_fma_f32 v[124:125], v[242:243], v[140:141], v[124:125] op_sel:[1,0,0] op_sel_hi:[1,1,1]
v_pk_fma_f32 v[126:127], v[242:243], v[142:143], v[126:127] op_sel:[1,0,0] op_sel_hi:[1,1,1]
v_pk_mul_f32 v[120:121], v[244:245], v[120:121] op_sel:[1,0] op_sel_hi:[1,1]
v_pk_mul_f32 v[122:123], v[244:245], v[122:123] op_sel:[1,0] op_sel_hi:[1,1]
v_pk_mul_f32 v[124:125], v[244:245], v[124:125] op_sel:[1,0] op_sel_hi:[1,1]
v_pk_mul_f32 v[126:127], v[244:245], v[126:127] op_sel:[1,0] op_sel_hi:[1,1]
EX v106,v106
MM a[96:99],a[176:179],v[0:3],0
RD a[228:231],v225 offset:40960
EX v107,v107
CV v70,v100,v101
MM a[96:99],a[180:183],v[4:7],a[96:99]
buffer_load_dwordx4 v235, s[12:15], s81 offen offset:0 lds
MM a[104:107],a[180:183],v[12:15],0
RD a[232:235],v226 offset:40960
EX v108,v108
MM a[104:107],a[176:179],v[8:11],a[104:107]
RD a[236:239],v227 offset:40960
CV v71,v102,v103
MM a[108:111],a[184:187],v[8:11],0
EX v109,v109
MM a[108:111],a[188:191],v[12:15],a[108:111]
buffer_load_dwordx4 v235, s[12:15], s64 offen offset:1024 lds
MM a[100:103],a[188:191],v[4:7],0
RD a[240:243],v224 offset:45056
RD a[244:247],v225 offset:45056
RD a[248:251],v226 offset:45056
RD a[252:255],v227 offset:45056
EX v110,v110
CV v220,v120,v121
CV v221,v122,v123
EX v111,v111
CV v222,v124,v125
CV v223,v126,v127
MM a[100:103],a[184:187],v[0:3],a[100:103]
buffer_load_dwordx4 v235, s[12:15], s81 offen offset:2048 lds
v_smfmac_f32_16x16x64_bf16 v[236:239], v[144:147], v[64:71], v234
CV v76,v104,v105
CV v77,v106,v107
CV v78,v108,v109
CV v79,v110,v111
s_nop 1
v_smfmac_f32_16x16x64_bf16 v[236:239], v[148:151], v[72:79], v234
LPG5_%=:
s_cbranch_execz LPG9_%=
s_waitcnt lgkmcnt(0)
buffer_load_dwordx4 v235, s[12:15], s64 offen offset:3072 lds
MM v[0:3],a[192:195],v[160:163],v[128:131]
s_add_u32 s64, s64, 16384
TR a[128:129],v232 offset:16384
TR a[130:131],v232 offset:20480
MM v[0:3],a[196:199],v[176:179],v[0:3]
s_add_u32 s81, s64, 896
TR a[132:133],v232 offset:24576
TR a[134:135],v232 offset:28672
MM v[0:3],a[200:203],v[192:195],v[0:3]
TR a[136:137],v233 offset:16384
TR a[138:139],v233 offset:20480
MM v[0:3],a[204:207],v[208:211],v[0:3]
TR a[140:141],v233 offset:24576
v_add_f32 v247, v236, v237
MM v[4:7],a[220:223],v[208:211],v[128:131]
TR a[142:143],v233 offset:28672
v_cmp_nge_f32 vcc, s71, v247
MM v[4:7],a[216:219],v[192:195],v[4:7]
s_cbranch_vccnz LSITE1_%=
LBACK1_%=:
TR a[144:145],v232 offset:16896
TR a[146:147],v232 offset:20992
MM v[4:7],a[212:215],v[176:179],v[4:7]
s_cmp_lg_u32 %[sb], 0
s_cbranch_scc1 LKA_%=
s_lshl_b32 s90, %[swave], 10
s_add_u32 s90, s90, 81920
buffer_load_dwordx4 v[48:51], v248, s[20:23], s90 offen offset:0 sc1
s_add_u32 s78, s90, 8192
buffer_load_dwordx4 v[56:59], v248, s[20:23], s78 offen offset:0 sc1
LKA_%=:
MM v[4:7],a[208:211],v[160:163],v[4:7]
TR a[148:149],v232 offset:25088
TR a[150:151],v232 offset:29184
MM v[12:15],a[208:211],v[164:167],v[132:135]
EX v0,v0
MM v[12:15],a[212:215],v[180:183],v[12:15]
EX v1,v1
MM v[12:15],a[216:219],v[196:199],v[12:15]
TR a[152:153],v233 offset:16896
CV v0,v0,v1
MM v[12:15],a[220:223],v[212:215],v[12:15]
EX v2,v2
MM v[8:11],a[204:207],v[212:215],v[132:135]
TR a[154:155],v233 offset:20992
TR a[156:157],v233 offset:25088
MM v[8:11],a[200:203],v[196:199],v[8:11]
EX v3,v3
MM v[8:11],a[196:199],v[180:183],v[8:11]
TR a[158:159],v233 offset:29184
CV v1,v2,v3
MM v[8:11],a[192:195],v[164:167],v[8:11]
EX v4,v4
MM v[32:35],a[224:227],v[160:163],v[128:131]
TR a[160:161],v232 offset:17408
TR a[162:163],v232 offset:21504
MM v[32:35],a[228:231],v[176:179],v[32:35]
EX v5,v5
MM v[32:35],a[232:235],v[192:195],v[32:35]
TR a[164:165],v232 offset:25600
CV v2,v4,v5
MM v[32:35],a[236:239],v[208:211],v[32:35]
TR a[166:167],v232 offset:29696
TR a[168:169],v233 offset:17408
MM v[36:39],a[252:255],v[208:211],v[128:131]
EX v6,v6
MM v[36:39],a[248:251],v[192:195],v[36:39]
EX v7,v7
MM v[36:39],a[244:247],v[176:179],v[36:39]
TR a[170:171],v233 offset:21504
CV v3,v6,v7
MM v[36:39],a[240:243],v[160:163],v[36:39]
TR a[172:173],v233 offset:25600
TR a[174:175],v233 offset:29696
MM v[44:47],a[240:243],v[164:167],v[132:135]
EX v8,v8
MM v[44:47],a[244:247],v[180:183],v[44:47]
TR a[176:177],v232 offset:17920
TR a[178:179],v232 offset:22016
MM v[44:47],a[248:251],v[196:199],v[44:47]
EX v9,v9
MM v[44:47],a[252:255],v[212:215],v[44:47]
TR a[180:181],v232 offset:26112
CV v8,v8,v9
MM v[40:43],a[236:239],v[212:215],v[132:135]
EX v10,v10
MM v[40:43],a[232:235],v[196:199],v[40:43]
TR a[182:183],v232 offset:30208
TR a[184:185],v233 offset:17920
MM v[40:43],a[228:231],v[180:183],v[40:43]
EX v11,v11
MM v[40:43],a[224:227],v[164:167],v[40:43]
TR a[186:187],v233 offset:22016
CV v9,v10,v11
MM v[16:19],a[192:195],v[168:171],0
EX v12,v12
MM v[16:19],a[196:199],v[184:187],v[16:19]
TR a[188:189],v233 offset:26112
TR a[190:191],v233 offset:30208
MM v[16:19],a[200:203],v[200:203],v[16:19]
EX v13,v13
MM v[16:19],a[204:207],v[216:219],v[16:19]
EX v14,v14
MM v[24:27],a[192:195],v[172:175],0
EX v15,v15
MM v[24:27],a[196:199],v[188:191],v[24:27]
EX v32,v32
MM v[24:27],a[200:203],v[204:207],v[24:27]
EX v33,v33
MM v[24:27],a[204:207],v[220:223],v[24:27]
s_waitcnt vmcnt(12) lgkmcnt(0)
s_barrier
CV v10,v12,v13
MM a[0:3],a[128:131],v[64:67],a[0:3]
s_add_u32 m0, s65, 16384
RD a[192:195],v224 offset:49152
CV v11,v14,v15
MM a[0:3],a[132:135],v[68:71],a[0:3]
CV v4,v32,v33
v_max3_f32 v154, v16, v17, v18
MM a[8:11],a[132:135],v[76:79],a[8:11]
buffer_load_dwordx4 v228, s[8:11], s64 offen offset:0 lds
MM a[8:11],a[128:131],v[72:75],a[8:11]
RD a[196:199],v225 offset:49152
v_max_f32 v154, v154, v19
MM a[12:15],a[136:139],v[72:75],a[12:15]
EX v34,v34
MM a[12:15],a[140:143],v[76:79],a[12:15]
RD a[200:203],v226 offset:49152
v_mov_b32 v155, v154
MM a[4:7],a[140:143],v[68:71],a[4:7]
s_nop 0
v_permlane16_swap_b32 v154, v155
v_max_f32 v154, v154, v155
MM a[4:7],a[136:139],v[64:67],a[4:7]
EX v35,v35
MM a[32:35],a[144:147],v[64:67],a[32:35]
RD a[204:207],v227 offset:49152
v_mov_b32 v155, v154
MM a[32:35],a[148:151],v[68:71],a[32:35]
s_nop 0
v_permlane32_swap_b32 v154, v155
v_max_f32 v154, v154, v155
MM a[40:43],a[148:151],v[76:79],a[40:43]
EX v36,v36
MM a[40:43],a[144:147],v[72:75],a[40:43]
v_sub_f32 v136, 0, v154
v_sub_f32 v137, 0, v154
MM a[44:47],a[152:155],v[72:75],a[44:47]
EX v37,v37
MM a[44:47],a[156:159],v[76:79],a[44:47]
v_sub_f32 v138, 0, v154
v_sub_f32 v139, 0, v154
MM a[36:39],a[156:159],v[68:71],a[36:39]
buffer_load_dwordx4 v229, s[8:11], s64 offen offset:1024 lds
MM a[36:39],a[152:155],v[64:67],a[36:39]
RD a[208:211],v224 offset:53248
EX v38,v38
v_mov_b32_dpp v152, v144 row_ror:8 row_mask:0xf bank_mask:0xf
v_mov_b32_dpp v153, v145 row_ror:8 row_mask:0xf bank_mask:0xf
MM a[64:67],a[160:163],v[64:67],a[64:67]
EX v39,v39
v_mov_b32_dpp v154, v146 row_ror:8 row_mask:0xf bank_mask:0xf
MM a[64:67],a[164:167],v[68:71],a[64:67]
RD a[212:215],v225 offset:53248
v_mov_b32_dpp v155, v147 row_ror:8 row_mask:0xf bank_mask:0xf
MM a[72:75],a[164:167],v[76:79],a[72:75]
buffer_load_dwordx4 v230, s[8:11], s64 offen offset:2048 lds
MM a[72:75],a[160:163],v[72:75],a[72:75]
RD a[216:219],v226 offset:53248
v_max3_f32 v158, v24, v25, v26
v_max_f32 v158, v158, v27
EX v40,v40
v_mov_b32 v159, v158
CV v5,v34,v35
MM a[76:79],a[168:171],v[72:75],a[76:79]
RD a[220:223],v227 offset:53248
EX v41,v41
v_permlane16_swap_b32 v158, v159
MM a[76:79],a[172:175],v[76:79],a[76:79]
buffer_load_dwordx4 v231, s[8:11], s64 offen offset:3072 lds
MM a[68:71],a[172:175],v[68:71],a[68:71]
s_add_u32 m0, s65, 81920
RD a[224:227],v224 offset:57344
v_max_f32 v158, v158, v159
v_mov_b32 v159, v158
EX v42,v42
MM a[68:71],a[168:171],v[64:67],a[68:71]
v_permlane32_swap_b32 v158, v159
CV v6,v36,v37
v_max_f32 v158, v158, v159
EX v43,v43
MM a[96:99],a[176:179],v[64:67],a[96:99]
RD a[228:231],v225 offset:57344
v_sub_f32 v140, 0, v158
MM a[96:99],a[180:183],v[68:71],a[96:99]
buffer_load_dwordx4 v235, s[12:15], s81 offen offset:0 lds
MM a[104:107],a[180:183],v[76:79],a[104:107]
RD a[232:235],v226 offset:57344
v_sub_f32 v141, 0, v158
v_sub_f32 v142, 0, v158
EX v44,v44
CV v7,v38,v39
MM a[104:107],a[176:179],v[72:75],a[104:107]
v_sub_f32 v143, 0, v158
v_mov_b32_dpp v156, v148 row_ror:8 row_mask:0xf bank_mask:0xf
)ASM"
R"ASM(
EX v45,v45
MM a[108:111],a[184:187],v[72:75],a[108:111]
RD a[236:239],v227 offset:57344
v_mov_b32_dpp v157, v149 row_ror:8 row_mask:0xf bank_mask:0xf
v_mov_b32_dpp v158, v150 row_ror:8 row_mask:0xf bank_mask:0xf
v_mov_b32_dpp v159, v151 row_ror:8 row_mask:0xf bank_mask:0xf
MM a[108:111],a[188:191],v[76:79],a[108:111]
buffer_load_dwordx4 v235, s[12:15], s64 offen offset:1024 lds
MM a[100:103],a[188:191],v[68:71],a[100:103]
RD a[240:243],v224 offset:61440
RD a[244:247],v225 offset:61440
RD a[248:251],v226 offset:61440
RD a[252:255],v227 offset:61440
EX v46,v46
EX v47,v47
MM a[100:103],a[184:187],v[64:67],a[100:103]
buffer_load_dwordx4 v235, s[12:15], s81 offen offset:2048 lds
v_smfmac_f32_16x16x64_bf16 v[236:239], v[144:147], v[0:7], v234
CV v12,v40,v41
CV v13,v42,v43
CV v14,v44,v45
CV v15,v46,v47
s_nop 1
v_smfmac_f32_16x16x64_bf16 v[236:239], v[148:151], v[8:15], v234
s_waitcnt lgkmcnt(0)
buffer_load_dwordx4 v235, s[12:15], s64 offen offset:3072 lds
MM v[64:67],a[192:195],v[160:163],v[128:131]
s_add_u32 s64, s64, 16384
TR a[128:129],v232 offset:32768
s_add_u32 s81, s64, 896
MM v[64:67],a[196:199],v[176:179],v[64:67]
TR a[130:131],v232 offset:36864
TR a[132:133],v232 offset:40960
MM v[64:67],a[200:203],v[192:195],v[64:67]
TR a[134:135],v232 offset:45056
TR a[136:137],v233 offset:32768
MM v[64:67],a[204:207],v[208:211],v[64:67]
TR a[138:139],v233 offset:36864
v_add_f32 v246, v236, v237
MM v[68:71],a[220:223],v[208:211],v[128:131]
TR a[140:141],v233 offset:40960
v_cmp_nge_f32 vcc, s71, v246
MM v[68:71],a[216:219],v[192:195],v[68:71]
s_cbranch_vccnz LSITE2_%=
LBACK2_%=:
TR a[142:143],v233 offset:45056
TR a[144:145],v232 offset:33280
MM v[68:71],a[212:215],v[176:179],v[68:71]
TR a[146:147],v232 offset:37376
TR a[148:149],v232 offset:41472
MM v[68:71],a[208:211],v[160:163],v[68:71]
TR a[150:151],v232 offset:45568
TR a[152:153],v233 offset:33280
MM v[76:79],a[208:211],v[164:167],v[132:135]
EX v64,v64
MM v[76:79],a[212:215],v[180:183],v[76:79]
EX v65,v65
MM v[76:79],a[216:219],v[196:199],v[76:79]
TR a[154:155],v233 offset:37376
CV v64,v64,v65
MM v[76:79],a[220:223],v[212:215],v[76:79]
EX v66,v66
MM v[72:75],a[204:207],v[212:215],v[132:135]
EX v67,v67
MM v[72:75],a[200:203],v[196:199],v[72:75]
TR a[156:157],v233 offset:41472
CV v65,v66,v67
MM v[72:75],a[196:199],v[180:183],v[72:75]
EX v68,v68
MM v[72:75],a[192:195],v[164:167],v[72:75]
EX v69,v69
MM v[80:83],a[192:195],v[168:171],v[136:139]
TR a[158:159],v233 offset:45568
CV v66,v68,v69
MM v[80:83],a[196:199],v[184:187],v[80:83]
EX v70,v70
MM v[80:83],a[200:203],v[200:203],v[80:83]
EX v71,v71
MM v[80:83],a[204:207],v[216:219],v[80:83]
TR a[160:161],v232 offset:33792
CV v67,v70,v71
MM v[84:87],a[220:223],v[216:219],v[136:139]
s_cmp_lg_u32 %[sb], 0
s_cbranch_scc1 LKB_%=
s_waitcnt vmcnt(8)
v_xor_b32 v48, s69, v48
v_xor_b32 v49, s69, v49
v_xor_b32 v50, s69, v50
v_xor_b32 v51, s69, v51
v_xor_b32 v56, s69, v56
v_xor_b32 v57, s69, v57
v_xor_b32 v58, s69, v58
v_xor_b32 v59, s69, v59
v_or3_b32 v52, v48, v49, v50
v_or3_b32 v52, v52, v51, v56
v_or3_b32 v52, v52, v57, v58
v_or_b32 v52, v52, v59
v_cmp_ne_u32 vcc, 0, v52
s_cbranch_vccnz LRFR_%=
LRFRb_%=:
s_lshl_b32 s90, %[swave], 11
s_add_u32 s78, s90, %[slds]
s_add_u32 s78, s78, 155648
s_add_u32 s90, s90, 53248
s_mov_b32 m0, s78
s_nop 0
buffer_load_dwordx4 v248, s[20:23], s90 offen offset:0 sc1 lds
buffer_load_dwordx4 v248, s[20:23], s90 offen offset:1024 sc1 lds
LKB_%=:
MM v[84:87],a[216:219],v[200:203],v[84:87]
EX v72,v72
MM v[84:87],a[212:215],v[184:187],v[84:87]
EX v73,v73
MM v[84:87],a[208:211],v[168:171],v[84:87]
EX v74,v74
MM v[92:95],a[208:211],v[172:175],v[140:143]
EX v75,v75
MM v[92:95],a[212:215],v[188:191],v[92:95]
CV v72,v72,v73
CV v73,v74,v75
MM v[92:95],a[216:219],v[204:207],v[92:95]
EX v76,v76
MM v[92:95],a[220:223],v[220:223],v[92:95]
EX v77,v77
MM v[88:91],a[204:207],v[220:223],v[140:143]
EX v78,v78
MM v[88:91],a[200:203],v[204:207],v[88:91]
EX v79,v79
MM v[88:91],a[196:199],v[188:191],v[88:91]
CV v74,v76,v77
CV v75,v78,v79
MM v[88:91],a[192:195],v[172:175],v[88:91]
EX v80,v80
MM v[96:99],a[224:227],v[160:163],v[128:131]
EX v81,v81
MM v[96:99],a[228:231],v[176:179],v[96:99]
TR a[162:163],v232 offset:37888
TR a[164:165],v232 offset:41984
MM v[96:99],a[232:235],v[192:195],v[96:99]
EX v82,v82
MM v[96:99],a[236:239],v[208:211],v[96:99]
EX v83,v83
MM v[100:103],a[252:255],v[208:211],v[128:131]
TR a[166:167],v232 offset:46080
CV v80,v80,v81
MM v[100:103],a[248:251],v[192:195],v[100:103]
EX v84,v84
MM v[100:103],a[244:247],v[176:179],v[100:103]
EX v85,v85
MM v[100:103],a[240:243],v[160:163],v[100:103]
TR a[168:169],v233 offset:33792
CV v81,v82,v83
MM v[108:111],a[240:243],v[164:167],v[132:135]
EX v86,v86
MM v[108:111],a[244:247],v[180:183],v[108:111]
EX v87,v87
MM v[108:111],a[248:251],v[196:199],v[108:111]
TR a[170:171],v233 offset:37888
CV v82,v84,v85
MM v[108:111],a[252:255],v[212:215],v[108:111]
EX v88,v88
MM v[104:107],a[236:239],v[212:215],v[132:135]
EX v89,v89
MM v[104:107],a[232:235],v[196:199],v[104:107]
TR a[172:173],v233 offset:41984
TR a[174:175],v233 offset:46080
MM v[104:107],a[228:231],v[180:183],v[104:107]
EX v90,v90
MM v[104:107],a[224:227],v[164:167],v[104:107]
EX v91,v91
MM v[112:115],a[224:227],v[168:171],v[136:139]
TR a[176:177],v232 offset:34304
CV v83,v86,v87
MM v[112:115],a[228:231],v[184:187],v[112:115]
EX v92,v92
MM v[112:115],a[232:235],v[200:203],v[112:115]
EX v93,v93
MM v[112:115],a[236:239],v[216:219],v[112:115]
TR a[178:179],v232 offset:38400
CV v88,v88,v89
MM v[116:119],a[252:255],v[216:219],v[136:139]
EX v94,v94
MM v[116:119],a[248:251],v[200:203],v[116:119]
EX v95,v95
MM v[116:119],a[244:247],v[184:187],v[116:119]
TR a[180:181],v232 offset:42496
TR a[182:183],v232 offset:46592
MM v[116:119],a[240:243],v[168:171],v[116:119]
EX v96,v96
MM v[124:127],a[240:243],v[172:175],v[140:143]
EX v97,v97
CV v89,v90,v91
MM v[124:127],a[244:247],v[188:191],v[124:127]
EX v98,v98
MM v[124:127],a[248:251],v[204:207],v[124:127]
TR a[184:185],v233 offset:34304
TR a[186:187],v233 offset:38400
TR a[188:189],v233 offset:42496
TR a[190:191],v233 offset:46592
MM v[124:127],a[252:255],v[220:223],v[124:127]
EX v99,v99
CV v90,v92,v93
MM v[120:123],a[236:239],v[220:223],v[140:143]
EX v100,v100
MM v[120:123],a[232:235],v[204:207],v[120:123]
EX v101,v101
CV v91,v94,v95
MM v[120:123],a[228:231],v[188:191],v[120:123]
EX v102,v102
MM v[120:123],a[224:227],v[172:175],v[120:123]
s_waitcnt vmcnt(12) lgkmcnt(0)
s_barrier
CV v68,v96,v97
MM a[0:3],a[128:131],v[0:3],a[0:3]
s_add_u32 m0, s65, 32768
EX v103,v103
MM a[0:3],a[132:135],v[4:7],a[0:3]
EX v104,v104
MM a[8:11],a[132:135],v[12:15],a[8:11]
CV v69,v98,v99
CV v70,v100,v101
MM a[8:11],a[128:131],v[8:11],a[8:11]
EX v105,v105
MM a[12:15],a[136:139],v[8:11],a[12:15]
EX v106,v106
MM a[12:15],a[140:143],v[12:15],a[12:15]
EX v107,v107
MM a[4:7],a[140:143],v[4:7],a[4:7]
EX v108,v108
MM a[4:7],a[136:139],v[0:3],a[4:7]
RD a[192:195],v224 offset:0
CV v71,v102,v103
MM a[32:35],a[144:147],v[0:3],a[32:35]
EX v109,v109
MM a[32:35],a[148:151],v[4:7],a[32:35]
buffer_load_dwordx4 v228, s[8:11], s64 offen offset:0 lds
MM a[40:43],a[148:151],v[12:15],a[40:43]
RD a[196:199],v225 offset:0
EX v110,v110
MM a[40:43],a[144:147],v[8:11],a[40:43]
EX v111,v111
CV v76,v104,v105
MM a[44:47],a[152:155],v[8:11],a[44:47]
RD a[200:203],v226 offset:0
EX v112,v112
MM a[44:47],a[156:159],v[12:15],a[44:47]
buffer_load_dwordx4 v229, s[8:11], s64 offen offset:1024 lds
MM a[36:39],a[156:159],v[4:7],a[36:39]
RD a[204:207],v227 offset:0
EX v113,v113
CV v77,v106,v107
MM a[36:39],a[152:155],v[0:3],a[36:39]
EX v114,v114
MM a[64:67],a[160:163],v[0:3],a[64:67]
RD a[208:211],v224 offset:4096
EX v115,v115
CV v78,v108,v109
MM a[64:67],a[164:167],v[4:7],a[64:67]
buffer_load_dwordx4 v230, s[8:11], s64 offen offset:2048 lds
MM a[72:75],a[164:167],v[12:15],a[72:75]
RD a[212:215],v225 offset:4096
EX v116,v116
MM a[72:75],a[160:163],v[8:11],a[72:75]
EX v117,v117
CV v79,v110,v111
MM a[76:79],a[168:171],v[8:11],a[76:79]
RD a[216:219],v226 offset:4096
CV v84,v112,v113
EX v118,v118
MM a[76:79],a[172:175],v[12:15],a[76:79]
buffer_load_dwordx4 v231, s[8:11], s64 offen offset:3072 lds
MM a[68:71],a[172:175],v[4:7],a[68:71]
s_add_u32 m0, s65, 98304
RD a[220:223],v227 offset:4096
EX v119,v119
MM a[68:71],a[168:171],v[0:3],a[68:71]
EX v120,v120
MM a[96:99],a[176:179],v[0:3],a[96:99]
RD a[224:227],v224 offset:8192
CV v85,v114,v115
EX v121,v121
MM a[96:99],a[180:183],v[4:7],a[96:99]
RD a[228:231],v225 offset:8192
CV v86,v116,v117
MM a[104:107],a[180:183],v[12:15],a[104:107]
EX v122,v122
MM a[104:107],a[176:179],v[8:11],a[104:107]
buffer_load_dwordx4 v235, s[12:15], s81 offen offset:0 lds
MM a[108:111],a[184:187],v[8:11],a[108:111]
RD a[232:235],v226 offset:8192
EX v123,v123
MM a[108:111],a[188:191],v[12:15],a[108:111]
EX v124,v124
MM a[100:103],a[188:191],v[4:7],a[100:103]
RD a[236:239],v227 offset:8192
EX v125,v125
CV v87,v118,v119
MM a[100:103],a[184:187],v[0:3],a[100:103]
buffer_load_dwordx4 v235, s[12:15], s64 offen offset:1024 lds
v_smfmac_f32_16x16x64_bf16 v[236:239], v[144:147], v[64:71], v234
RD a[240:243],v224 offset:12288
RD a[244:247],v225 offset:12288
RD a[248:251],v226 offset:12288
RD a[252:255],v227 offset:12288
EX v126,v126
EX v127,v127
v_smfmac_f32_16x16x64_bf16 v[236:239], v[148:151], v[72:79], v234
CV v92,v120,v121
v_smfmac_f32_16x16x64_bf16 v[236:239], v[152:155], v[80:87], v234
CV v93,v122,v123
CV v94,v124,v125
CV v95,v126,v127
s_nop 1
v_smfmac_f32_16x16x64_bf16 v[236:239], v[156:159], v[88:95], v234
s_waitcnt lgkmcnt(0)
buffer_load_dwordx4 v235, s[12:15], s81 offen offset:2048 lds
buffer_load_dwordx4 v235, s[12:15], s64 offen offset:3072 lds
MM v[0:3],a[192:195],v[160:163],v[128:131]
s_add_u32 s64, s64, 16384
TR a[128:129],v232 offset:49152
s_add_u32 s81, s64, 896
MM v[0:3],a[196:199],v[176:179],v[0:3]
TR a[130:131],v232 offset:53248
TR a[132:133],v232 offset:57344
MM v[0:3],a[200:203],v[192:195],v[0:3]
TR a[134:135],v232 offset:61440
TR a[136:137],v233 offset:49152
MM v[0:3],a[204:207],v[208:211],v[0:3]
TR a[138:139],v233 offset:53248
v_add_f32 v247, v236, v237
MM v[4:7],a[220:223],v[208:211],v[128:131]
TR a[140:141],v233 offset:57344
v_cmp_nge_f32 vcc, s71, v247
MM v[4:7],a[216:219],v[192:195],v[4:7]
s_cbranch_vccnz LSITE3_%=
LBACK3_%=:
MM v[4:7],a[212:215],v[176:179],v[4:7]
s_nop 0
v_lshrrev_b32 v252, 6, v248
v_mul_u32_u24 v252, 3456, v252
MM v[4:7],a[208:211],v[160:163],v[4:7]
s_nop 0
v_and_b32 v251, 48, v248
v_add_u32 v252, v252, v251
MM v[12:15],a[208:211],v[164:167],v[132:135]
s_and_b32 s90, %[swave], 1
EX v0,v0
MM v[12:15],a[212:215],v[180:183],v[12:15]
s_mul_i32 s90, s90, 276480
EX v1,v1
MM v[12:15],a[216:219],v[196:199],v[12:15]
s_bfe_u32 s91, s48, 131077
TR a[142:143],v233 offset:61440
CV v0,v0,v1
MM v[12:15],a[220:223],v[212:215],v[12:15]
s_mul_i32 s91, s91, 552960
EX v2,v2
MM v[8:11],a[204:207],v[212:215],v[132:135]
s_add_u32 s90, s90, s91
TR a[144:145],v232 offset:49664
TR a[146:147],v232 offset:53760
MM v[8:11],a[200:203],v[196:199],v[8:11]
s_and_b32 s91, s48, 0xffffff80
EX v3,v3
MM v[8:11],a[196:199],v[180:183],v[8:11]
s_add_u32 s90, s90, s91
TR a[148:149],v232 offset:57856
CV v1,v2,v3
MM v[8:11],a[192:195],v[164:167],v[8:11]
s_and_b32 s91, s48, 16
EX v4,v4
MM v[16:19],a[192:195],v[168:171],v[136:139]
s_lshl_b32 s91, s91, 2
EX v5,v5
MM v[16:19],a[196:199],v[184:187],v[16:19]
s_add_u32 s66, s90, s91
TR a[150:151],v232 offset:61952
CV v2,v4,v5
MM v[16:19],a[200:203],v[200:203],v[16:19]
s_mul_i32 s91, %[swave], 5120
EX v6,v6
MM v[16:19],a[204:207],v[216:219],v[16:19]
s_add_u32 s91, s91, %[slds]
TR a[152:153],v233 offset:49664
TR a[154:155],v233 offset:53760
MM v[20:23],a[220:223],v[216:219],v[136:139]
s_add_u32 s67, s91, 131072
EX v7,v7
MM v[20:23],a[216:219],v[200:203],v[20:23]
s_add_u32 s85, s66, 221184
TR a[156:157],v233 offset:57856
CV v3,v6,v7
MM v[20:23],a[212:215],v[184:187],v[20:23]
s_add_u32 s86, s67, 4096
EX v8,v8
MM v[20:23],a[208:211],v[168:171],v[20:23]
s_nop 0
TR a[158:159],v233 offset:61952
TR a[160:161],v232 offset:50176
MM v[28:31],a[208:211],v[172:175],v[140:143]
)ASM"
R"ASM(
LPG6_%=:
s_cbranch_execz LPG10_%=
EX v9,v9
MM v[28:31],a[212:215],v[188:191],v[28:31]
TR a[162:163],v232 offset:54272
CV v8,v8,v9
MM v[28:31],a[216:219],v[204:207],v[28:31]
EX v10,v10
MM v[28:31],a[220:223],v[220:223],v[28:31]
EX v11,v11
MM v[24:27],a[204:207],v[220:223],v[140:143]
TR a[164:165],v232 offset:58368
CV v9,v10,v11
MM v[24:27],a[200:203],v[204:207],v[24:27]
EX v12,v12
MM v[24:27],a[196:199],v[188:191],v[24:27]
TR a[166:167],v232 offset:62464
TR a[168:169],v233 offset:50176
MM v[24:27],a[192:195],v[172:175],v[24:27]
EX v13,v13
MM v[32:35],a[224:227],v[160:163],v[128:131]
TR a[170:171],v233 offset:54272
CV v10,v12,v13
MM v[32:35],a[228:231],v[176:179],v[32:35]
EX v14,v14
MM v[32:35],a[232:235],v[192:195],v[32:35]
EX v15,v15
MM v[32:35],a[236:239],v[208:211],v[32:35]
TR a[172:173],v233 offset:58368
CV v11,v14,v15
MM v[36:39],a[252:255],v[208:211],v[128:131]
EX v16,v16
MM v[36:39],a[248:251],v[192:195],v[36:39]
TR a[174:175],v233 offset:62464
TR a[176:177],v232 offset:50688
MM v[36:39],a[244:247],v[176:179],v[36:39]
EX v17,v17
MM v[36:39],a[240:243],v[160:163],v[36:39]
TR a[178:179],v232 offset:54784
CV v16,v16,v17
MM v[44:47],a[240:243],v[164:167],v[132:135]
EX v18,v18
MM v[44:47],a[244:247],v[180:183],v[44:47]
EX v19,v19
MM v[44:47],a[248:251],v[196:199],v[44:47]
TR a[180:181],v232 offset:58880
CV v17,v18,v19
MM v[44:47],a[252:255],v[212:215],v[44:47]
EX v20,v20
MM v[40:43],a[236:239],v[212:215],v[132:135]
TR a[182:183],v232 offset:62976
TR a[184:185],v233 offset:50688
MM v[40:43],a[232:235],v[196:199],v[40:43]
EX v21,v21
MM v[40:43],a[228:231],v[180:183],v[40:43]
TR a[186:187],v233 offset:54784
CV v18,v20,v21
MM v[40:43],a[224:227],v[164:167],v[40:43]
EX v22,v22
MM v[48:51],a[224:227],v[168:171],v[136:139]
TR a[188:189],v233 offset:58880
TR a[190:191],v233 offset:62976
MM v[48:51],a[228:231],v[184:187],v[48:51]
EX v23,v23
MM v[48:51],a[232:235],v[200:203],v[48:51]
CV v19,v22,v23
MM v[48:51],a[236:239],v[216:219],v[48:51]
EX v24,v24
MM v[52:55],a[252:255],v[216:219],v[136:139]
EX v25,v25
MM v[52:55],a[248:251],v[200:203],v[52:55]
EX v26,v26
MM v[52:55],a[244:247],v[184:187],v[52:55]
EX v27,v27
MM v[52:55],a[240:243],v[168:171],v[52:55]
EX v28,v28
MM v[60:63],a[240:243],v[172:175],v[140:143]
EX v29,v29
MM v[60:63],a[244:247],v[188:191],v[60:63]
EX v30,v30
MM v[60:63],a[248:251],v[204:207],v[60:63]
EX v31,v31
MM v[60:63],a[252:255],v[220:223],v[60:63]
EX v32,v32
MM v[56:59],a[236:239],v[220:223],v[140:143]
EX v33,v33
MM v[56:59],a[232:235],v[204:207],v[56:59]
EX v34,v34
MM v[56:59],a[228:231],v[188:191],v[56:59]
EX v35,v35
MM v[56:59],a[224:227],v[172:175],v[56:59]
s_waitcnt vmcnt(8) lgkmcnt(0)
s_barrier
MM a[0:3],a[128:131],v[64:67],a[0:3]
s_add_u32 m0, s65, 49152
CV v24,v24,v25
CV v25,v26,v27
MM a[0:3],a[132:135],v[68:71],a[0:3]
RD a[192:195],v224 offset:16384
CV v26,v28,v29
MM a[8:11],a[132:135],v[76:79],a[8:11]
RD a[196:199],v225 offset:16384
CV v27,v30,v31
MM a[8:11],a[128:131],v[72:75],a[8:11]
buffer_load_dwordx4 v228, s[8:11], s64 offen offset:0 lds
MM a[16:19],a[128:131],v[80:83],0
RD a[200:203],v226 offset:16384
CV v4,v32,v33
MM a[16:19],a[132:135],v[84:87],a[16:19]
RD a[204:207],v227 offset:16384
CV v5,v34,v35
MM a[24:27],a[132:135],v[92:95],0
buffer_load_dwordx4 v229, s[8:11], s64 offen offset:1024 lds
MM a[24:27],a[128:131],v[88:91],a[24:27]
RD a[208:211],v224 offset:20480
RD a[212:215],v225 offset:20480
MM a[28:31],a[136:139],v[88:91],0
buffer_load_dwordx4 v230, s[8:11], s64 offen offset:2048 lds
MM a[28:31],a[140:143],v[92:95],a[28:31]
EX v36,v36
MM a[20:23],a[140:143],v[84:87],0
EX v37,v37
MM a[20:23],a[136:139],v[80:83],a[20:23]
RD a[216:219],v226 offset:20480
CV v6,v36,v37
MM a[12:15],a[136:139],v[72:75],a[12:15]
EX v38,v38
MM a[12:15],a[140:143],v[76:79],a[12:15]
RD a[220:223],v227 offset:20480
RD a[224:227],v224 offset:24576
MM a[4:7],a[140:143],v[68:71],a[4:7]
EX v39,v39
MM a[4:7],a[136:139],v[64:67],a[4:7]
RD a[228:231],v225 offset:24576
CV v7,v38,v39
MM a[32:35],a[144:147],v[64:67],a[32:35]
EX v40,v40
MM a[32:35],a[148:151],v[68:71],a[32:35]
buffer_load_dwordx4 v231, s[8:11], s64 offen offset:3072 lds
MM a[40:43],a[148:151],v[76:79],a[40:43]
s_add_u32 m0, s65, 114688
EX v41,v41
MM a[40:43],a[144:147],v[72:75],a[40:43]
RD a[232:235],v226 offset:24576
CV v12,v40,v41
MM a[48:51],a[144:147],v[80:83],0
EX v42,v42
MM a[48:51],a[148:151],v[84:87],a[48:51]
EX v43,v43
MM a[56:59],a[148:151],v[92:95],0
RD a[236:239],v227 offset:24576
CV v13,v42,v43
MM a[56:59],a[144:147],v[88:91],a[56:59]
EX v44,v44
MM a[60:63],a[152:155],v[88:91],0
buffer_load_dwordx4 v235, s[12:15], s81 offen offset:0 lds
MM a[60:63],a[156:159],v[92:95],a[60:63]
EX v45,v45
MM a[52:55],a[156:159],v[84:87],0
RD a[240:243],v224 offset:28672
CV v14,v44,v45
MM a[52:55],a[152:155],v[80:83],a[52:55]
EX v46,v46
MM a[44:47],a[152:155],v[72:75],a[44:47]
EX v47,v47
MM a[44:47],a[156:159],v[76:79],a[44:47]
RD a[244:247],v225 offset:28672
CV v15,v46,v47
MM a[36:39],a[156:159],v[68:71],a[36:39]
buffer_load_dwordx4 v235, s[12:15], s64 offen offset:1024 lds
MM a[36:39],a[152:155],v[64:67],a[36:39]
EX v48,v48
MM a[64:67],a[160:163],v[64:67],a[64:67]
EX v49,v49
MM a[64:67],a[164:167],v[68:71],a[64:67]
RD a[248:251],v226 offset:28672
CV v20,v48,v49
MM a[72:75],a[164:167],v[76:79],a[72:75]
EX v50,v50
MM a[72:75],a[160:163],v[72:75],a[72:75]
EX v51,v51
MM a[80:83],a[160:163],v[80:83],0
RD a[252:255],v227 offset:28672
CV v21,v50,v51
MM a[80:83],a[164:167],v[84:87],a[80:83]
buffer_load_dwordx4 v235, s[12:15], s81 offen offset:2048 lds
MM a[88:91],a[164:167],v[92:95],0
EX v52,v52
MM a[88:91],a[160:163],v[88:91],a[88:91]
EX v53,v53
MM a[92:95],a[168:171],v[88:91],0
EX v54,v54
MM a[92:95],a[172:175],v[92:95],a[92:95]
buffer_load_dwordx4 v235, s[12:15], s64 offen offset:3072 lds
MM a[84:87],a[172:175],v[84:87],0
EX v55,v55
MM a[84:87],a[168:171],v[80:83],a[84:87]
EX v56,v56
MM a[76:79],a[168:171],v[72:75],a[76:79]
EX v57,v57
MM a[76:79],a[172:175],v[76:79],a[76:79]
EX v58,v58
MM a[68:71],a[172:175],v[68:71],a[68:71]
EX v59,v59
MM a[68:71],a[168:171],v[64:67],a[68:71]
EX v60,v60
MM a[96:99],a[176:179],v[64:67],a[96:99]
EX v61,v61
MM a[96:99],a[180:183],v[68:71],a[96:99]
EX v62,v62
MM a[104:107],a[180:183],v[76:79],a[104:107]
EX v63,v63
MM a[104:107],a[176:179],v[72:75],a[104:107]
CV v22,v52,v53
CV v23,v54,v55
MM a[112:115],a[176:179],v[80:83],0
CV v28,v56,v57
CV v29,v58,v59
MM a[112:115],a[180:183],v[84:87],a[112:115]
CV v30,v60,v61
CV v31,v62,v63
MM a[120:123],a[180:183],v[92:95],0
MM a[120:123],a[176:179],v[88:91],a[120:123]
MM a[124:127],a[184:187],v[88:91],0
MM a[124:127],a[188:191],v[92:95],a[124:127]
MM a[116:119],a[188:191],v[84:87],0
MM a[116:119],a[184:187],v[80:83],a[116:119]
MM a[108:111],a[184:187],v[72:75],a[108:111]
MM a[108:111],a[188:191],v[76:79],a[108:111]
MM a[100:103],a[188:191],v[68:71],a[100:103]
MM a[100:103],a[184:187],v[64:67],a[100:103]
v_smfmac_f32_16x16x64_bf16 v[236:239], v[144:147], v[0:7], v234
v_smfmac_f32_16x16x64_bf16 v[236:239], v[148:151], v[8:15], v234
v_smfmac_f32_16x16x64_bf16 v[236:239], v[152:155], v[16:23], v234
v_smfmac_f32_16x16x64_bf16 v[236:239], v[156:159], v[24:31], v234
s_waitcnt lgkmcnt(0)
s_cmp_lg_u32 %[sb], 0
s_cbranch_scc1 LXS_%=
s_getpc_b64 s[98:99]
LTA1_%=:
s_lshl_b32 s73, %[swave], 12
s_add_u32 s73, s73, LPG26_%=-LTA1_%=
s_ashr_i32 s74, s73, 31
s_add_u32 s98, s98, s73
s_addc_u32 s99, s99, s74
s_mov_b64 exec, 0
s_setpc_b64 s[98:99]
LTR1_%=:
s_mov_b64 exec, -1
s_branch LXP_%=
LXS_%=:
s_mov_b32 s70, 2
LLA_%=:
MM v[64:67],a[192:195],v[160:163],v[128:131]
s_add_u32 s64, s64, 16384
TR a[128:129],v232 offset:0
s_add_u32 s81, s64, 896
MM v[64:67],a[196:199],v[176:179],v[64:67]
TR a[130:131],v232 offset:4096
TR a[132:133],v232 offset:8192
MM v[64:67],a[200:203],v[192:195],v[64:67]
TR a[134:135],v232 offset:12288
TR a[136:137],v233 offset:0
MM v[64:67],a[204:207],v[208:211],v[64:67]
TR a[138:139],v233 offset:4096
v_add_f32 v246, v236, v237
MM v[68:71],a[220:223],v[208:211],v[128:131]
TR a[140:141],v233 offset:8192
v_cmp_nge_f32 vcc, s71, v246
MM v[68:71],a[216:219],v[192:195],v[68:71]
s_cbranch_vccnz LSITE8_%=
LBACK8_%=:
MM v[68:71],a[212:215],v[176:179],v[68:71]
s_mov_b32 m0, s67
TR a[142:143],v233 offset:12288
TR a[144:145],v232 offset:512
MM v[68:71],a[208:211],v[160:163],v[68:71]
buffer_load_dwordx4 v252, s[36:39], s66 offen offset:0 lds
MM v[76:79],a[208:211],v[164:167],v[132:135]
s_add_u32 s66, s66, 55296
EX v64,v64
MM v[76:79],a[212:215],v[180:183],v[76:79]
s_min_u32 s66, s66, s85
EX v65,v65
MM v[76:79],a[216:219],v[196:199],v[76:79]
s_add_u32 s67, s67, 1024
TR a[146:147],v232 offset:4608
CV v64,v64,v65
MM v[76:79],a[220:223],v[212:215],v[76:79]
s_min_u32 s67, s67, s86
EX v66,v66
MM v[72:75],a[204:207],v[212:215],v[132:135]
EX v67,v67
MM v[72:75],a[200:203],v[196:199],v[72:75]
TR a[148:149],v232 offset:8704
CV v65,v66,v67
MM v[72:75],a[196:199],v[180:183],v[72:75]
EX v68,v68
MM v[72:75],a[192:195],v[164:167],v[72:75]
TR a[150:151],v232 offset:12800
TR a[152:153],v233 offset:512
MM v[80:83],a[192:195],v[168:171],v[136:139]
EX v69,v69
MM v[80:83],a[196:199],v[184:187],v[80:83]
TR a[154:155],v233 offset:4608
CV v66,v68,v69
MM v[80:83],a[200:203],v[200:203],v[80:83]
EX v70,v70
MM v[80:83],a[204:207],v[216:219],v[80:83]
EX v71,v71
MM v[84:87],a[220:223],v[216:219],v[136:139]
TR a[156:157],v233 offset:8704
CV v67,v70,v71
MM v[84:87],a[216:219],v[200:203],v[84:87]
EX v72,v72
MM v[84:87],a[212:215],v[184:187],v[84:87]
TR a[158:159],v233 offset:12800
TR a[160:161],v232 offset:1024
MM v[84:87],a[208:211],v[168:171],v[84:87]
EX v73,v73
MM v[92:95],a[208:211],v[172:175],v[140:143]
TR a[162:163],v232 offset:5120
CV v72,v72,v73
MM v[92:95],a[212:215],v[188:191],v[92:95]
EX v74,v74
MM v[92:95],a[216:219],v[204:207],v[92:95]
EX v75,v75
MM v[92:95],a[220:223],v[220:223],v[92:95]
TR a[164:165],v232 offset:9216
CV v73,v74,v75
MM v[88:91],a[204:207],v[220:223],v[140:143]
EX v76,v76
MM v[88:91],a[200:203],v[204:207],v[88:91]
TR a[166:167],v232 offset:13312
TR a[168:169],v233 offset:1024
MM v[88:91],a[196:199],v[188:191],v[88:91]
EX v77,v77
MM v[88:91],a[192:195],v[172:175],v[88:91]
TR a[170:171],v233 offset:5120
CV v74,v76,v77
MM v[96:99],a[224:227],v[160:163],v[128:131]
EX v78,v78
MM v[96:99],a[228:231],v[176:179],v[96:99]
EX v79,v79
MM v[96:99],a[232:235],v[192:195],v[96:99]
TR a[172:173],v233 offset:9216
CV v75,v78,v79
MM v[96:99],a[236:239],v[208:211],v[96:99]
TR a[174:175],v233 offset:13312
TR a[176:177],v232 offset:1536
MM v[100:103],a[252:255],v[208:211],v[128:131]
EX v80,v80
MM v[100:103],a[248:251],v[192:195],v[100:103]
EX v81,v81
MM v[100:103],a[244:247],v[176:179],v[100:103]
TR a[178:179],v232 offset:5632
CV v80,v80,v81
MM v[100:103],a[240:243],v[160:163],v[100:103]
EX v82,v82
MM v[108:111],a[240:243],v[164:167],v[132:135]
TR a[180:181],v232 offset:9728
TR a[182:183],v232 offset:13824
MM v[108:111],a[244:247],v[180:183],v[108:111]
EX v83,v83
MM v[108:111],a[248:251],v[196:199],v[108:111]
TR a[184:185],v233 offset:1536
CV v81,v82,v83
MM v[108:111],a[252:255],v[212:215],v[108:111]
EX v84,v84
MM v[104:107],a[236:239],v[212:215],v[132:135]
EX v85,v85
MM v[104:107],a[232:235],v[196:199],v[104:107]
TR a[186:187],v233 offset:5632
CV v82,v84,v85
MM v[104:107],a[228:231],v[180:183],v[104:107]
EX v86,v86
MM v[104:107],a[224:227],v[164:167],v[104:107]
TR a[188:189],v233 offset:9728
TR a[190:191],v233 offset:13824
MM v[112:115],a[224:227],v[168:171],v[136:139]
EX v87,v87
MM v[112:115],a[228:231],v[184:187],v[112:115]
CV v83,v86,v87
MM v[112:115],a[232:235],v[200:203],v[112:115]
EX v88,v88
MM v[112:115],a[236:239],v[216:219],v[112:115]
EX v89,v89
MM v[116:119],a[252:255],v[216:219],v[136:139]
EX v90,v90
MM v[116:119],a[248:251],v[200:203],v[116:119]
EX v91,v91
MM v[116:119],a[244:247],v[184:187],v[116:119]
EX v92,v92
MM v[116:119],a[240:243],v[168:171],v[116:119]
EX v93,v93
MM v[124:127],a[240:243],v[172:175],v[140:143]
EX v94,v94
MM v[124:127],a[244:247],v[188:191],v[124:127]
EX v95,v95
MM v[124:127],a[248:251],v[204:207],v[124:127]
EX v96,v96
MM v[124:127],a[252:255],v[220:223],v[124:127]
)ASM"
R"ASM(
EX v97,v97
MM v[120:123],a[236:239],v[220:223],v[140:143]
EX v98,v98
MM v[120:123],a[232:235],v[204:207],v[120:123]
EX v99,v99
MM v[120:123],a[228:231],v[188:191],v[120:123]
EX v100,v100
MM v[120:123],a[224:227],v[172:175],v[120:123]
s_waitcnt vmcnt(13) lgkmcnt(0)
s_barrier
MM a[0:3],a[128:131],v[0:3],a[0:3]
s_add_u32 m0, s65, 0
CV v88,v88,v89
CV v89,v90,v91
MM a[0:3],a[132:135],v[4:7],a[0:3]
RD a[192:195],v224 offset:32768
CV v90,v92,v93
MM a[8:11],a[132:135],v[12:15],a[8:11]
RD a[196:199],v225 offset:32768
CV v91,v94,v95
MM a[8:11],a[128:131],v[8:11],a[8:11]
buffer_load_dwordx4 v228, s[8:11], s64 offen offset:0 lds
MM a[16:19],a[128:131],v[16:19],a[16:19]
RD a[200:203],v226 offset:32768
CV v68,v96,v97
MM a[16:19],a[132:135],v[20:23],a[16:19]
RD a[204:207],v227 offset:32768
CV v69,v98,v99
MM a[24:27],a[132:135],v[28:31],a[24:27]
buffer_load_dwordx4 v229, s[8:11], s64 offen offset:1024 lds
MM a[24:27],a[128:131],v[24:27],a[24:27]
RD a[208:211],v224 offset:36864
RD a[212:215],v225 offset:36864
MM a[28:31],a[136:139],v[24:27],a[28:31]
buffer_load_dwordx4 v230, s[8:11], s64 offen offset:2048 lds
MM a[28:31],a[140:143],v[28:31],a[28:31]
RD a[216:219],v226 offset:36864
RD a[220:223],v227 offset:36864
MM a[20:23],a[140:143],v[20:23],a[20:23]
EX v101,v101
MM a[20:23],a[136:139],v[16:19],a[20:23]
RD a[224:227],v224 offset:40960
CV v70,v100,v101
MM a[12:15],a[136:139],v[8:11],a[12:15]
EX v102,v102
MM a[12:15],a[140:143],v[12:15],a[12:15]
buffer_load_dwordx4 v231, s[8:11], s64 offen offset:3072 lds
MM a[4:7],a[140:143],v[4:7],a[4:7]
s_add_u32 m0, s65, 65536
EX v103,v103
MM a[4:7],a[136:139],v[0:3],a[4:7]
RD a[228:231],v225 offset:40960
CV v71,v102,v103
MM a[32:35],a[144:147],v[0:3],a[32:35]
EX v104,v104
MM a[32:35],a[148:151],v[4:7],a[32:35]
EX v105,v105
MM a[40:43],a[148:151],v[12:15],a[40:43]
RD a[232:235],v226 offset:40960
CV v76,v104,v105
MM a[40:43],a[144:147],v[8:11],a[40:43]
EX v106,v106
MM a[48:51],a[144:147],v[16:19],a[48:51]
buffer_load_dwordx4 v235, s[12:15], s81 offen offset:0 lds
MM a[48:51],a[148:151],v[20:23],a[48:51]
EX v107,v107
MM a[56:59],a[148:151],v[28:31],a[56:59]
RD a[236:239],v227 offset:40960
CV v77,v106,v107
MM a[56:59],a[144:147],v[24:27],a[56:59]
EX v108,v108
MM a[60:63],a[152:155],v[24:27],a[60:63]
EX v109,v109
MM a[60:63],a[156:159],v[28:31],a[60:63]
RD a[240:243],v224 offset:45056
CV v78,v108,v109
MM a[52:55],a[156:159],v[20:23],a[52:55]
EX v110,v110
MM a[52:55],a[152:155],v[16:19],a[52:55]
buffer_load_dwordx4 v235, s[12:15], s64 offen offset:1024 lds
MM a[44:47],a[152:155],v[8:11],a[44:47]
EX v111,v111
MM a[44:47],a[156:159],v[12:15],a[44:47]
RD a[244:247],v225 offset:45056
CV v79,v110,v111
MM a[36:39],a[156:159],v[4:7],a[36:39]
EX v112,v112
MM a[36:39],a[152:155],v[0:3],a[36:39]
EX v113,v113
MM a[64:67],a[160:163],v[0:3],a[64:67]
RD a[248:251],v226 offset:45056
CV v84,v112,v113
MM a[64:67],a[164:167],v[4:7],a[64:67]
EX v114,v114
MM a[72:75],a[164:167],v[12:15],a[72:75]
buffer_load_dwordx4 v235, s[12:15], s81 offen offset:2048 lds
MM a[72:75],a[160:163],v[8:11],a[72:75]
EX v115,v115
MM a[80:83],a[160:163],v[16:19],a[80:83]
RD a[252:255],v227 offset:45056
CV v85,v114,v115
MM a[80:83],a[164:167],v[20:23],a[80:83]
EX v116,v116
MM a[88:91],a[164:167],v[28:31],a[88:91]
EX v117,v117
MM a[88:91],a[160:163],v[24:27],a[88:91]
buffer_load_dwordx4 v235, s[12:15], s64 offen offset:3072 lds
MM a[92:95],a[168:171],v[24:27],a[92:95]
EX v118,v118
MM a[92:95],a[172:175],v[28:31],a[92:95]
EX v119,v119
MM a[84:87],a[172:175],v[20:23],a[84:87]
EX v120,v120
MM a[84:87],a[168:171],v[16:19],a[84:87]
EX v121,v121
MM a[76:79],a[168:171],v[8:11],a[76:79]
EX v122,v122
MM a[76:79],a[172:175],v[12:15],a[76:79]
EX v123,v123
MM a[68:71],a[172:175],v[4:7],a[68:71]
EX v124,v124
MM a[68:71],a[168:171],v[0:3],a[68:71]
EX v125,v125
MM a[96:99],a[176:179],v[0:3],a[96:99]
EX v126,v126
MM a[96:99],a[180:183],v[4:7],a[96:99]
EX v127,v127
MM a[104:107],a[180:183],v[12:15],a[104:107]
CV v86,v116,v117
CV v87,v118,v119
MM a[104:107],a[176:179],v[8:11],a[104:107]
CV v92,v120,v121
CV v93,v122,v123
MM a[112:115],a[176:179],v[16:19],a[112:115]
CV v94,v124,v125
CV v95,v126,v127
MM a[112:115],a[180:183],v[20:23],a[112:115]
MM a[120:123],a[180:183],v[28:31],a[120:123]
MM a[120:123],a[176:179],v[24:27],a[120:123]
MM a[124:127],a[184:187],v[24:27],a[124:127]
MM a[124:127],a[188:191],v[28:31],a[124:127]
MM a[116:119],a[188:191],v[20:23],a[116:119]
MM a[116:119],a[184:187],v[16:19],a[116:119]
MM a[108:111],a[184:187],v[8:11],a[108:111]
MM a[108:111],a[188:191],v[12:15],a[108:111]
MM a[100:103],a[188:191],v[4:7],a[100:103]
MM a[100:103],a[184:187],v[0:3],a[100:103]
v_smfmac_f32_16x16x64_bf16 v[236:239], v[144:147], v[64:71], v234
v_smfmac_f32_16x16x64_bf16 v[236:239], v[148:151], v[72:79], v234
s_nop 0
v_smfmac_f32_16x16x64_bf16 v[236:239], v[152:155], v[80:87], v234
s_nop 0
v_smfmac_f32_16x16x64_bf16 v[236:239], v[156:159], v[88:95], v234
s_waitcnt lgkmcnt(0)
MM v[0:3],a[192:195],v[160:163],v[128:131]
s_add_u32 s64, s64, 16384
TR a[128:129],v232 offset:16384
s_add_u32 s81, s64, 896
MM v[0:3],a[196:199],v[176:179],v[0:3]
s_nop 0
TR a[130:131],v232 offset:20480
TR a[132:133],v232 offset:24576
MM v[0:3],a[200:203],v[192:195],v[0:3]
s_nop 0
TR a[134:135],v232 offset:28672
TR a[136:137],v233 offset:16384
MM v[0:3],a[204:207],v[208:211],v[0:3]
s_nop 0
TR a[138:139],v233 offset:20480
v_add_f32 v247, v236, v237
MM v[4:7],a[220:223],v[208:211],v[128:131]
s_nop 0
TR a[140:141],v233 offset:24576
v_cmp_nge_f32 vcc, s71, v247
MM v[4:7],a[216:219],v[192:195],v[4:7]
s_cbranch_vccnz LSITE9_%=
LBACK9_%=:
MM v[4:7],a[212:215],v[176:179],v[4:7]
s_mov_b32 m0, s67
TR a[142:143],v233 offset:28672
TR a[144:145],v232 offset:16896
MM v[4:7],a[208:211],v[160:163],v[4:7]
buffer_load_dwordx4 v252, s[36:39], s66 offen offset:0 lds
MM v[12:15],a[208:211],v[164:167],v[132:135]
s_add_u32 s66, s66, 55296
EX v0,v0
MM v[12:15],a[212:215],v[180:183],v[12:15]
s_min_u32 s66, s66, s85
EX v1,v1
MM v[12:15],a[216:219],v[196:199],v[12:15]
s_add_u32 s67, s67, 1024
TR a[146:147],v232 offset:20992
CV v0,v0,v1
MM v[12:15],a[220:223],v[212:215],v[12:15]
s_min_u32 s67, s67, s86
EX v2,v2
MM v[8:11],a[204:207],v[212:215],v[132:135]
LPG7_%=:
s_cbranch_execz LPG11_%=
EX v3,v3
MM v[8:11],a[200:203],v[196:199],v[8:11]
TR a[148:149],v232 offset:25088
CV v1,v2,v3
MM v[8:11],a[196:199],v[180:183],v[8:11]
EX v4,v4
MM v[8:11],a[192:195],v[164:167],v[8:11]
TR a[150:151],v232 offset:29184
TR a[152:153],v233 offset:16896
MM v[16:19],a[192:195],v[168:171],v[136:139]
EX v5,v5
MM v[16:19],a[196:199],v[184:187],v[16:19]
TR a[154:155],v233 offset:20992
CV v2,v4,v5
MM v[16:19],a[200:203],v[200:203],v[16:19]
EX v6,v6
MM v[16:19],a[204:207],v[216:219],v[16:19]
EX v7,v7
MM v[20:23],a[220:223],v[216:219],v[136:139]
TR a[156:157],v233 offset:25088
CV v3,v6,v7
MM v[20:23],a[216:219],v[200:203],v[20:23]
EX v8,v8
MM v[20:23],a[212:215],v[184:187],v[20:23]
TR a[158:159],v233 offset:29184
TR a[160:161],v232 offset:17408
MM v[20:23],a[208:211],v[168:171],v[20:23]
EX v9,v9
MM v[28:31],a[208:211],v[172:175],v[140:143]
TR a[162:163],v232 offset:21504
CV v8,v8,v9
MM v[28:31],a[212:215],v[188:191],v[28:31]
EX v10,v10
MM v[28:31],a[216:219],v[204:207],v[28:31]
EX v11,v11
MM v[28:31],a[220:223],v[220:223],v[28:31]
TR a[164:165],v232 offset:25600
CV v9,v10,v11
MM v[24:27],a[204:207],v[220:223],v[140:143]
EX v12,v12
MM v[24:27],a[200:203],v[204:207],v[24:27]
TR a[166:167],v232 offset:29696
TR a[168:169],v233 offset:17408
MM v[24:27],a[196:199],v[188:191],v[24:27]
EX v13,v13
MM v[24:27],a[192:195],v[172:175],v[24:27]
TR a[170:171],v233 offset:21504
CV v10,v12,v13
MM v[32:35],a[224:227],v[160:163],v[128:131]
EX v14,v14
MM v[32:35],a[228:231],v[176:179],v[32:35]
EX v15,v15
MM v[32:35],a[232:235],v[192:195],v[32:35]
TR a[172:173],v233 offset:25600
CV v11,v14,v15
MM v[32:35],a[236:239],v[208:211],v[32:35]
TR a[174:175],v233 offset:29696
TR a[176:177],v232 offset:17920
MM v[36:39],a[252:255],v[208:211],v[128:131]
EX v16,v16
MM v[36:39],a[248:251],v[192:195],v[36:39]
EX v17,v17
MM v[36:39],a[244:247],v[176:179],v[36:39]
TR a[178:179],v232 offset:22016
CV v16,v16,v17
MM v[36:39],a[240:243],v[160:163],v[36:39]
EX v18,v18
MM v[44:47],a[240:243],v[164:167],v[132:135]
TR a[180:181],v232 offset:26112
TR a[182:183],v232 offset:30208
MM v[44:47],a[244:247],v[180:183],v[44:47]
EX v19,v19
MM v[44:47],a[248:251],v[196:199],v[44:47]
TR a[184:185],v233 offset:17920
CV v17,v18,v19
MM v[44:47],a[252:255],v[212:215],v[44:47]
EX v20,v20
MM v[40:43],a[236:239],v[212:215],v[132:135]
EX v21,v21
MM v[40:43],a[232:235],v[196:199],v[40:43]
TR a[186:187],v233 offset:22016
CV v18,v20,v21
MM v[40:43],a[228:231],v[180:183],v[40:43]
EX v22,v22
MM v[40:43],a[224:227],v[164:167],v[40:43]
TR a[188:189],v233 offset:26112
TR a[190:191],v233 offset:30208
MM v[48:51],a[224:227],v[168:171],v[136:139]
EX v23,v23
MM v[48:51],a[228:231],v[184:187],v[48:51]
CV v19,v22,v23
MM v[48:51],a[232:235],v[200:203],v[48:51]
EX v24,v24
MM v[48:51],a[236:239],v[216:219],v[48:51]
EX v25,v25
MM v[52:55],a[252:255],v[216:219],v[136:139]
EX v26,v26
MM v[52:55],a[248:251],v[200:203],v[52:55]
EX v27,v27
MM v[52:55],a[244:247],v[184:187],v[52:55]
EX v28,v28
MM v[52:55],a[240:243],v[168:171],v[52:55]
EX v29,v29
MM v[60:63],a[240:243],v[172:175],v[140:143]
EX v30,v30
MM v[60:63],a[244:247],v[188:191],v[60:63]
EX v31,v31
MM v[60:63],a[248:251],v[204:207],v[60:63]
EX v32,v32
MM v[60:63],a[252:255],v[220:223],v[60:63]
EX v33,v33
MM v[56:59],a[236:239],v[220:223],v[140:143]
EX v34,v34
MM v[56:59],a[232:235],v[204:207],v[56:59]
EX v35,v35
MM v[56:59],a[228:231],v[188:191],v[56:59]
EX v36,v36
MM v[56:59],a[224:227],v[172:175],v[56:59]
s_waitcnt vmcnt(13) lgkmcnt(0)
s_barrier
MM a[0:3],a[128:131],v[64:67],a[0:3]
s_add_u32 m0, s65, 16384
CV v24,v24,v25
CV v25,v26,v27
MM a[0:3],a[132:135],v[68:71],a[0:3]
RD a[192:195],v224 offset:49152
CV v26,v28,v29
MM a[8:11],a[132:135],v[76:79],a[8:11]
RD a[196:199],v225 offset:49152
CV v27,v30,v31
MM a[8:11],a[128:131],v[72:75],a[8:11]
buffer_load_dwordx4 v228, s[8:11], s64 offen offset:0 lds
MM a[16:19],a[128:131],v[80:83],a[16:19]
RD a[200:203],v226 offset:49152
CV v4,v32,v33
MM a[16:19],a[132:135],v[84:87],a[16:19]
RD a[204:207],v227 offset:49152
CV v5,v34,v35
MM a[24:27],a[132:135],v[92:95],a[24:27]
buffer_load_dwordx4 v229, s[8:11], s64 offen offset:1024 lds
MM a[24:27],a[128:131],v[88:91],a[24:27]
RD a[208:211],v224 offset:53248
RD a[212:215],v225 offset:53248
MM a[28:31],a[136:139],v[88:91],a[28:31]
buffer_load_dwordx4 v230, s[8:11], s64 offen offset:2048 lds
MM a[28:31],a[140:143],v[92:95],a[28:31]
RD a[216:219],v226 offset:53248
RD a[220:223],v227 offset:53248
MM a[20:23],a[140:143],v[84:87],a[20:23]
EX v37,v37
MM a[20:23],a[136:139],v[80:83],a[20:23]
RD a[224:227],v224 offset:57344
CV v6,v36,v37
MM a[12:15],a[136:139],v[72:75],a[12:15]
EX v38,v38
MM a[12:15],a[140:143],v[76:79],a[12:15]
buffer_load_dwordx4 v231, s[8:11], s64 offen offset:3072 lds
MM a[4:7],a[140:143],v[68:71],a[4:7]
s_add_u32 m0, s65, 81920
EX v39,v39
MM a[4:7],a[136:139],v[64:67],a[4:7]
RD a[228:231],v225 offset:57344
CV v7,v38,v39
MM a[32:35],a[144:147],v[64:67],a[32:35]
EX v40,v40
MM a[32:35],a[148:151],v[68:71],a[32:35]
EX v41,v41
MM a[40:43],a[148:151],v[76:79],a[40:43]
RD a[232:235],v226 offset:57344
CV v12,v40,v41
MM a[40:43],a[144:147],v[72:75],a[40:43]
EX v42,v42
MM a[48:51],a[144:147],v[80:83],a[48:51]
buffer_load_dwordx4 v235, s[12:15], s81 offen offset:0 lds
MM a[48:51],a[148:151],v[84:87],a[48:51]
EX v43,v43
MM a[56:59],a[148:151],v[92:95],a[56:59]
RD a[236:239],v227 offset:57344
CV v13,v42,v43
MM a[56:59],a[144:147],v[88:91],a[56:59]
EX v44,v44
MM a[60:63],a[152:155],v[88:91],a[60:63]
EX v45,v45
MM a[60:63],a[156:159],v[92:95],a[60:63]
RD a[240:243],v224 offset:61440
CV v14,v44,v45
MM a[52:55],a[156:159],v[84:87],a[52:55]
EX v46,v46
MM a[52:55],a[152:155],v[80:83],a[52:55]
buffer_load_dwordx4 v235, s[12:15], s64 offen offset:1024 lds
MM a[44:47],a[152:155],v[72:75],a[44:47]
EX v47,v47
MM a[44:47],a[156:159],v[76:79],a[44:47]
RD a[244:247],v225 offset:61440
)ASM"
R"ASM(
CV v15,v46,v47
MM a[36:39],a[156:159],v[68:71],a[36:39]
EX v48,v48
MM a[36:39],a[152:155],v[64:67],a[36:39]
EX v49,v49
MM a[64:67],a[160:163],v[64:67],a[64:67]
RD a[248:251],v226 offset:61440
CV v20,v48,v49
MM a[64:67],a[164:167],v[68:71],a[64:67]
EX v50,v50
MM a[72:75],a[164:167],v[76:79],a[72:75]
buffer_load_dwordx4 v235, s[12:15], s81 offen offset:2048 lds
MM a[72:75],a[160:163],v[72:75],a[72:75]
EX v51,v51
MM a[80:83],a[160:163],v[80:83],a[80:83]
RD a[252:255],v227 offset:61440
CV v21,v50,v51
MM a[80:83],a[164:167],v[84:87],a[80:83]
EX v52,v52
MM a[88:91],a[164:167],v[92:95],a[88:91]
EX v53,v53
MM a[88:91],a[160:163],v[88:91],a[88:91]
buffer_load_dwordx4 v235, s[12:15], s64 offen offset:3072 lds
MM a[92:95],a[168:171],v[88:91],a[92:95]
EX v54,v54
MM a[92:95],a[172:175],v[92:95],a[92:95]
EX v55,v55
MM a[84:87],a[172:175],v[84:87],a[84:87]
EX v56,v56
MM a[84:87],a[168:171],v[80:83],a[84:87]
EX v57,v57
MM a[76:79],a[168:171],v[72:75],a[76:79]
EX v58,v58
MM a[76:79],a[172:175],v[76:79],a[76:79]
EX v59,v59
MM a[68:71],a[172:175],v[68:71],a[68:71]
EX v60,v60
MM a[68:71],a[168:171],v[64:67],a[68:71]
EX v61,v61
MM a[96:99],a[176:179],v[64:67],a[96:99]
EX v62,v62
MM a[96:99],a[180:183],v[68:71],a[96:99]
EX v63,v63
MM a[104:107],a[180:183],v[76:79],a[104:107]
CV v22,v52,v53
CV v23,v54,v55
MM a[104:107],a[176:179],v[72:75],a[104:107]
CV v28,v56,v57
CV v29,v58,v59
MM a[112:115],a[176:179],v[80:83],a[112:115]
CV v30,v60,v61
CV v31,v62,v63
MM a[112:115],a[180:183],v[84:87],a[112:115]
MM a[120:123],a[180:183],v[92:95],a[120:123]
MM a[120:123],a[176:179],v[88:91],a[120:123]
MM a[124:127],a[184:187],v[88:91],a[124:127]
MM a[124:127],a[188:191],v[92:95],a[124:127]
MM a[116:119],a[188:191],v[84:87],a[116:119]
MM a[116:119],a[184:187],v[80:83],a[116:119]
MM a[108:111],a[184:187],v[72:75],a[108:111]
MM a[108:111],a[188:191],v[76:79],a[108:111]
MM a[100:103],a[188:191],v[68:71],a[100:103]
MM a[100:103],a[184:187],v[64:67],a[100:103]
v_smfmac_f32_16x16x64_bf16 v[236:239], v[144:147], v[0:7], v234
v_smfmac_f32_16x16x64_bf16 v[236:239], v[148:151], v[8:15], v234
v_smfmac_f32_16x16x64_bf16 v[236:239], v[152:155], v[16:23], v234
v_smfmac_f32_16x16x64_bf16 v[236:239], v[156:159], v[24:31], v234
s_waitcnt lgkmcnt(0)
MM v[64:67],a[192:195],v[160:163],v[128:131]
s_add_u32 s64, s64, 16384
TR a[128:129],v232 offset:32768
s_add_u32 s81, s64, 896
MM v[64:67],a[196:199],v[176:179],v[64:67]
TR a[130:131],v232 offset:36864
TR a[132:133],v232 offset:40960
MM v[64:67],a[200:203],v[192:195],v[64:67]
TR a[134:135],v232 offset:45056
TR a[136:137],v233 offset:32768
MM v[64:67],a[204:207],v[208:211],v[64:67]
TR a[138:139],v233 offset:36864
v_add_f32 v246, v236, v237
MM v[68:71],a[220:223],v[208:211],v[128:131]
TR a[140:141],v233 offset:40960
v_cmp_nge_f32 vcc, s71, v246
MM v[68:71],a[216:219],v[192:195],v[68:71]
s_cbranch_vccnz LSITE10_%=
LBACK10_%=:
MM v[68:71],a[212:215],v[176:179],v[68:71]
s_mov_b32 m0, s67
TR a[142:143],v233 offset:45056
TR a[144:145],v232 offset:33280
MM v[68:71],a[208:211],v[160:163],v[68:71]
buffer_load_dwordx4 v252, s[36:39], s66 offen offset:0 lds
MM v[76:79],a[208:211],v[164:167],v[132:135]
s_add_u32 s66, s66, 55296
EX v64,v64
MM v[76:79],a[212:215],v[180:183],v[76:79]
s_min_u32 s66, s66, s85
EX v65,v65
MM v[76:79],a[216:219],v[196:199],v[76:79]
s_add_u32 s67, s67, 1024
TR a[146:147],v232 offset:37376
CV v64,v64,v65
MM v[76:79],a[220:223],v[212:215],v[76:79]
s_min_u32 s67, s67, s86
EX v66,v66
MM v[72:75],a[204:207],v[212:215],v[132:135]
EX v67,v67
MM v[72:75],a[200:203],v[196:199],v[72:75]
TR a[148:149],v232 offset:41472
CV v65,v66,v67
MM v[72:75],a[196:199],v[180:183],v[72:75]
EX v68,v68
MM v[72:75],a[192:195],v[164:167],v[72:75]
TR a[150:151],v232 offset:45568
TR a[152:153],v233 offset:33280
MM v[80:83],a[192:195],v[168:171],v[136:139]
EX v69,v69
MM v[80:83],a[196:199],v[184:187],v[80:83]
TR a[154:155],v233 offset:37376
CV v66,v68,v69
MM v[80:83],a[200:203],v[200:203],v[80:83]
EX v70,v70
MM v[80:83],a[204:207],v[216:219],v[80:83]
EX v71,v71
MM v[84:87],a[220:223],v[216:219],v[136:139]
TR a[156:157],v233 offset:41472
CV v67,v70,v71
MM v[84:87],a[216:219],v[200:203],v[84:87]
EX v72,v72
MM v[84:87],a[212:215],v[184:187],v[84:87]
TR a[158:159],v233 offset:45568
TR a[160:161],v232 offset:33792
MM v[84:87],a[208:211],v[168:171],v[84:87]
EX v73,v73
MM v[92:95],a[208:211],v[172:175],v[140:143]
TR a[162:163],v232 offset:37888
CV v72,v72,v73
MM v[92:95],a[212:215],v[188:191],v[92:95]
EX v74,v74
MM v[92:95],a[216:219],v[204:207],v[92:95]
EX v75,v75
MM v[92:95],a[220:223],v[220:223],v[92:95]
TR a[164:165],v232 offset:41984
CV v73,v74,v75
MM v[88:91],a[204:207],v[220:223],v[140:143]
EX v76,v76
MM v[88:91],a[200:203],v[204:207],v[88:91]
TR a[166:167],v232 offset:46080
TR a[168:169],v233 offset:33792
MM v[88:91],a[196:199],v[188:191],v[88:91]
EX v77,v77
MM v[88:91],a[192:195],v[172:175],v[88:91]
TR a[170:171],v233 offset:37888
CV v74,v76,v77
MM v[96:99],a[224:227],v[160:163],v[128:131]
EX v78,v78
MM v[96:99],a[228:231],v[176:179],v[96:99]
EX v79,v79
MM v[96:99],a[232:235],v[192:195],v[96:99]
TR a[172:173],v233 offset:41984
CV v75,v78,v79
MM v[96:99],a[236:239],v[208:211],v[96:99]
TR a[174:175],v233 offset:46080
TR a[176:177],v232 offset:34304
MM v[100:103],a[252:255],v[208:211],v[128:131]
EX v80,v80
MM v[100:103],a[248:251],v[192:195],v[100:103]
EX v81,v81
MM v[100:103],a[244:247],v[176:179],v[100:103]
TR a[178:179],v232 offset:38400
CV v80,v80,v81
MM v[100:103],a[240:243],v[160:163],v[100:103]
EX v82,v82
MM v[108:111],a[240:243],v[164:167],v[132:135]
TR a[180:181],v232 offset:42496
TR a[182:183],v232 offset:46592
MM v[108:111],a[244:247],v[180:183],v[108:111]
EX v83,v83
MM v[108:111],a[248:251],v[196:199],v[108:111]
TR a[184:185],v233 offset:34304
CV v81,v82,v83
MM v[108:111],a[252:255],v[212:215],v[108:111]
EX v84,v84
MM v[104:107],a[236:239],v[212:215],v[132:135]
EX v85,v85
MM v[104:107],a[232:235],v[196:199],v[104:107]
TR a[186:187],v233 offset:38400
CV v82,v84,v85
MM v[104:107],a[228:231],v[180:183],v[104:107]
EX v86,v86
MM v[104:107],a[224:227],v[164:167],v[104:107]
TR a[188:189],v233 offset:42496
TR a[190:191],v233 offset:46592
MM v[112:115],a[224:227],v[168:171],v[136:139]
EX v87,v87
MM v[112:115],a[228:231],v[184:187],v[112:115]
CV v83,v86,v87
MM v[112:115],a[232:235],v[200:203],v[112:115]
EX v88,v88
MM v[112:115],a[236:239],v[216:219],v[112:115]
EX v89,v89
MM v[116:119],a[252:255],v[216:219],v[136:139]
EX v90,v90
MM v[116:119],a[248:251],v[200:203],v[116:119]
EX v91,v91
MM v[116:119],a[244:247],v[184:187],v[116:119]
EX v92,v92
MM v[116:119],a[240:243],v[168:171],v[116:119]
EX v93,v93
MM v[124:127],a[240:243],v[172:175],v[140:143]
EX v94,v94
MM v[124:127],a[244:247],v[188:191],v[124:127]
EX v95,v95
MM v[124:127],a[248:251],v[204:207],v[124:127]
EX v96,v96
MM v[124:127],a[252:255],v[220:223],v[124:127]
EX v97,v97
MM v[120:123],a[236:239],v[220:223],v[140:143]
EX v98,v98
MM v[120:123],a[232:235],v[204:207],v[120:123]
EX v99,v99
MM v[120:123],a[228:231],v[188:191],v[120:123]
EX v100,v100
MM v[120:123],a[224:227],v[172:175],v[120:123]
s_waitcnt vmcnt(13) lgkmcnt(0)
s_barrier
MM a[0:3],a[128:131],v[0:3],a[0:3]
s_add_u32 m0, s65, 32768
CV v88,v88,v89
CV v89,v90,v91
MM a[0:3],a[132:135],v[4:7],a[0:3]
RD a[192:195],v224 offset:0
CV v90,v92,v93
MM a[8:11],a[132:135],v[12:15],a[8:11]
RD a[196:199],v225 offset:0
CV v91,v94,v95
MM a[8:11],a[128:131],v[8:11],a[8:11]
buffer_load_dwordx4 v228, s[8:11], s64 offen offset:0 lds
MM a[16:19],a[128:131],v[16:19],a[16:19]
RD a[200:203],v226 offset:0
CV v68,v96,v97
MM a[16:19],a[132:135],v[20:23],a[16:19]
RD a[204:207],v227 offset:0
CV v69,v98,v99
MM a[24:27],a[132:135],v[28:31],a[24:27]
buffer_load_dwordx4 v229, s[8:11], s64 offen offset:1024 lds
MM a[24:27],a[128:131],v[24:27],a[24:27]
RD a[208:211],v224 offset:4096
RD a[212:215],v225 offset:4096
MM a[28:31],a[136:139],v[24:27],a[28:31]
buffer_load_dwordx4 v230, s[8:11], s64 offen offset:2048 lds
MM a[28:31],a[140:143],v[28:31],a[28:31]
RD a[216:219],v226 offset:4096
RD a[220:223],v227 offset:4096
MM a[20:23],a[140:143],v[20:23],a[20:23]
EX v101,v101
MM a[20:23],a[136:139],v[16:19],a[20:23]
RD a[224:227],v224 offset:8192
CV v70,v100,v101
MM a[12:15],a[136:139],v[8:11],a[12:15]
EX v102,v102
MM a[12:15],a[140:143],v[12:15],a[12:15]
buffer_load_dwordx4 v231, s[8:11], s64 offen offset:3072 lds
MM a[4:7],a[140:143],v[4:7],a[4:7]
s_add_u32 m0, s65, 98304
EX v103,v103
MM a[4:7],a[136:139],v[0:3],a[4:7]
RD a[228:231],v225 offset:8192
CV v71,v102,v103
MM a[32:35],a[144:147],v[0:3],a[32:35]
EX v104,v104
MM a[32:35],a[148:151],v[4:7],a[32:35]
EX v105,v105
MM a[40:43],a[148:151],v[12:15],a[40:43]
RD a[232:235],v226 offset:8192
CV v76,v104,v105
MM a[40:43],a[144:147],v[8:11],a[40:43]
EX v106,v106
MM a[48:51],a[144:147],v[16:19],a[48:51]
buffer_load_dwordx4 v235, s[12:15], s81 offen offset:0 lds
MM a[48:51],a[148:151],v[20:23],a[48:51]
EX v107,v107
MM a[56:59],a[148:151],v[28:31],a[56:59]
RD a[236:239],v227 offset:8192
CV v77,v106,v107
MM a[56:59],a[144:147],v[24:27],a[56:59]
EX v108,v108
MM a[60:63],a[152:155],v[24:27],a[60:63]
EX v109,v109
MM a[60:63],a[156:159],v[28:31],a[60:63]
RD a[240:243],v224 offset:12288
CV v78,v108,v109
MM a[52:55],a[156:159],v[20:23],a[52:55]
EX v110,v110
MM a[52:55],a[152:155],v[16:19],a[52:55]
buffer_load_dwordx4 v235, s[12:15], s64 offen offset:1024 lds
MM a[44:47],a[152:155],v[8:11],a[44:47]
EX v111,v111
MM a[44:47],a[156:159],v[12:15],a[44:47]
RD a[244:247],v225 offset:12288
CV v79,v110,v111
MM a[36:39],a[156:159],v[4:7],a[36:39]
EX v112,v112
MM a[36:39],a[152:155],v[0:3],a[36:39]
EX v113,v113
MM a[64:67],a[160:163],v[0:3],a[64:67]
RD a[248:251],v226 offset:12288
CV v84,v112,v113
MM a[64:67],a[164:167],v[4:7],a[64:67]
EX v114,v114
MM a[72:75],a[164:167],v[12:15],a[72:75]
buffer_load_dwordx4 v235, s[12:15], s81 offen offset:2048 lds
MM a[72:75],a[160:163],v[8:11],a[72:75]
EX v115,v115
MM a[80:83],a[160:163],v[16:19],a[80:83]
RD a[252:255],v227 offset:12288
CV v85,v114,v115
MM a[80:83],a[164:167],v[20:23],a[80:83]
EX v116,v116
MM a[88:91],a[164:167],v[28:31],a[88:91]
EX v117,v117
MM a[88:91],a[160:163],v[24:27],a[88:91]
buffer_load_dwordx4 v235, s[12:15], s64 offen offset:3072 lds
MM a[92:95],a[168:171],v[24:27],a[92:95]
EX v118,v118
MM a[92:95],a[172:175],v[28:31],a[92:95]
EX v119,v119
MM a[84:87],a[172:175],v[20:23],a[84:87]
EX v120,v120
MM a[84:87],a[168:171],v[16:19],a[84:87]
EX v121,v121
MM a[76:79],a[168:171],v[8:11],a[76:79]
EX v122,v122
MM a[76:79],a[172:175],v[12:15],a[76:79]
EX v123,v123
MM a[68:71],a[172:175],v[4:7],a[68:71]
EX v124,v124
MM a[68:71],a[168:171],v[0:3],a[68:71]
EX v125,v125
MM a[96:99],a[176:179],v[0:3],a[96:99]
EX v126,v126
MM a[96:99],a[180:183],v[4:7],a[96:99]
EX v127,v127
MM a[104:107],a[180:183],v[12:15],a[104:107]
CV v86,v116,v117
CV v87,v118,v119
MM a[104:107],a[176:179],v[8:11],a[104:107]
CV v92,v120,v121
CV v93,v122,v123
MM a[112:115],a[176:179],v[16:19],a[112:115]
CV v94,v124,v125
CV v95,v126,v127
MM a[112:115],a[180:183],v[20:23],a[112:115]
MM a[120:123],a[180:183],v[28:31],a[120:123]
MM a[120:123],a[176:179],v[24:27],a[120:123]
MM a[124:127],a[184:187],v[24:27],a[124:127]
MM a[124:127],a[188:191],v[28:31],a[124:127]
MM a[116:119],a[188:191],v[20:23],a[116:119]
MM a[116:119],a[184:187],v[16:19],a[116:119]
MM a[108:111],a[184:187],v[8:11],a[108:111]
MM a[108:111],a[188:191],v[12:15],a[108:111]
s_nop 0
MM a[100:103],a[188:191],v[4:7],a[100:103]
s_nop 0
MM a[100:103],a[184:187],v[0:3],a[100:103]
s_nop 0
v_smfmac_f32_16x16x64_bf16 v[236:239], v[144:147], v[64:71], v234
s_nop 0
v_smfmac_f32_16x16x64_bf16 v[236:239], v[148:151], v[72:79], v234
s_nop 0
v_smfmac_f32_16x16x64_bf16 v[236:239], v[152:155], v[80:87], v234
s_nop 0
v_smfmac_f32_16x16x64_bf16 v[236:239], v[156:159], v[88:95], v234
s_waitcnt lgkmcnt(0)
MM v[0:3],a[192:195],v[160:163],v[128:131]
s_add_u32 s64, s64, 16384
TR a[128:129],v232 offset:49152
s_add_u32 s81, s64, 896
MM v[0:3],a[196:199],v[176:179],v[0:3]
s_nop 0
TR a[130:131],v232 offset:53248
TR a[132:133],v232 offset:57344
MM v[0:3],a[200:203],v[192:195],v[0:3]
)ASM"
R"ASM(
s_nop 0
TR a[134:135],v232 offset:61440
TR a[136:137],v233 offset:49152
MM v[0:3],a[204:207],v[208:211],v[0:3]
s_nop 0
TR a[138:139],v233 offset:53248
v_add_f32 v247, v236, v237
MM v[4:7],a[220:223],v[208:211],v[128:131]
LPG8_%=:
s_cbranch_execz LPG12_%=
TR a[140:141],v233 offset:57344
v_cmp_nge_f32 vcc, s71, v247
MM v[4:7],a[216:219],v[192:195],v[4:7]
s_cbranch_vccnz LSITE11_%=
LBACK11_%=:
MM v[4:7],a[212:215],v[176:179],v[4:7]
s_mov_b32 m0, s67
TR a[142:143],v233 offset:61440
TR a[144:145],v232 offset:49664
MM v[4:7],a[208:211],v[160:163],v[4:7]
buffer_load_dwordx4 v252, s[36:39], s66 offen offset:0 lds
MM v[12:15],a[208:211],v[164:167],v[132:135]
s_add_u32 s66, s66, 55296
EX v0,v0
MM v[12:15],a[212:215],v[180:183],v[12:15]
s_min_u32 s66, s66, s85
EX v1,v1
MM v[12:15],a[216:219],v[196:199],v[12:15]
s_add_u32 s67, s67, 1024
TR a[146:147],v232 offset:53760
CV v0,v0,v1
MM v[12:15],a[220:223],v[212:215],v[12:15]
s_min_u32 s67, s67, s86
EX v2,v2
MM v[8:11],a[204:207],v[212:215],v[132:135]
EX v3,v3
MM v[8:11],a[200:203],v[196:199],v[8:11]
TR a[148:149],v232 offset:57856
CV v1,v2,v3
MM v[8:11],a[196:199],v[180:183],v[8:11]
EX v4,v4
MM v[8:11],a[192:195],v[164:167],v[8:11]
TR a[150:151],v232 offset:61952
TR a[152:153],v233 offset:49664
MM v[16:19],a[192:195],v[168:171],v[136:139]
EX v5,v5
MM v[16:19],a[196:199],v[184:187],v[16:19]
TR a[154:155],v233 offset:53760
CV v2,v4,v5
MM v[16:19],a[200:203],v[200:203],v[16:19]
EX v6,v6
MM v[16:19],a[204:207],v[216:219],v[16:19]
EX v7,v7
MM v[20:23],a[220:223],v[216:219],v[136:139]
TR a[156:157],v233 offset:57856
CV v3,v6,v7
MM v[20:23],a[216:219],v[200:203],v[20:23]
EX v8,v8
MM v[20:23],a[212:215],v[184:187],v[20:23]
TR a[158:159],v233 offset:61952
TR a[160:161],v232 offset:50176
MM v[20:23],a[208:211],v[168:171],v[20:23]
EX v9,v9
MM v[28:31],a[208:211],v[172:175],v[140:143]
TR a[162:163],v232 offset:54272
CV v8,v8,v9
MM v[28:31],a[212:215],v[188:191],v[28:31]
EX v10,v10
MM v[28:31],a[216:219],v[204:207],v[28:31]
EX v11,v11
MM v[28:31],a[220:223],v[220:223],v[28:31]
TR a[164:165],v232 offset:58368
CV v9,v10,v11
MM v[24:27],a[204:207],v[220:223],v[140:143]
EX v12,v12
MM v[24:27],a[200:203],v[204:207],v[24:27]
TR a[166:167],v232 offset:62464
TR a[168:169],v233 offset:50176
MM v[24:27],a[196:199],v[188:191],v[24:27]
EX v13,v13
MM v[24:27],a[192:195],v[172:175],v[24:27]
TR a[170:171],v233 offset:54272
CV v10,v12,v13
MM v[32:35],a[224:227],v[160:163],v[128:131]
EX v14,v14
MM v[32:35],a[228:231],v[176:179],v[32:35]
EX v15,v15
MM v[32:35],a[232:235],v[192:195],v[32:35]
TR a[172:173],v233 offset:58368
CV v11,v14,v15
MM v[32:35],a[236:239],v[208:211],v[32:35]
TR a[174:175],v233 offset:62464
TR a[176:177],v232 offset:50688
MM v[36:39],a[252:255],v[208:211],v[128:131]
EX v16,v16
MM v[36:39],a[248:251],v[192:195],v[36:39]
EX v17,v17
MM v[36:39],a[244:247],v[176:179],v[36:39]
TR a[178:179],v232 offset:54784
CV v16,v16,v17
MM v[36:39],a[240:243],v[160:163],v[36:39]
EX v18,v18
MM v[44:47],a[240:243],v[164:167],v[132:135]
TR a[180:181],v232 offset:58880
TR a[182:183],v232 offset:62976
MM v[44:47],a[244:247],v[180:183],v[44:47]
EX v19,v19
MM v[44:47],a[248:251],v[196:199],v[44:47]
TR a[184:185],v233 offset:50688
CV v17,v18,v19
MM v[44:47],a[252:255],v[212:215],v[44:47]
EX v20,v20
MM v[40:43],a[236:239],v[212:215],v[132:135]
EX v21,v21
MM v[40:43],a[232:235],v[196:199],v[40:43]
TR a[186:187],v233 offset:54784
CV v18,v20,v21
MM v[40:43],a[228:231],v[180:183],v[40:43]
EX v22,v22
MM v[40:43],a[224:227],v[164:167],v[40:43]
TR a[188:189],v233 offset:58880
TR a[190:191],v233 offset:62976
MM v[48:51],a[224:227],v[168:171],v[136:139]
EX v23,v23
MM v[48:51],a[228:231],v[184:187],v[48:51]
CV v19,v22,v23
MM v[48:51],a[232:235],v[200:203],v[48:51]
EX v24,v24
MM v[48:51],a[236:239],v[216:219],v[48:51]
EX v25,v25
MM v[52:55],a[252:255],v[216:219],v[136:139]
EX v26,v26
MM v[52:55],a[248:251],v[200:203],v[52:55]
EX v27,v27
MM v[52:55],a[244:247],v[184:187],v[52:55]
EX v28,v28
MM v[52:55],a[240:243],v[168:171],v[52:55]
EX v29,v29
MM v[60:63],a[240:243],v[172:175],v[140:143]
EX v30,v30
MM v[60:63],a[244:247],v[188:191],v[60:63]
EX v31,v31
MM v[60:63],a[248:251],v[204:207],v[60:63]
EX v32,v32
MM v[60:63],a[252:255],v[220:223],v[60:63]
EX v33,v33
MM v[56:59],a[236:239],v[220:223],v[140:143]
EX v34,v34
MM v[56:59],a[232:235],v[204:207],v[56:59]
EX v35,v35
MM v[56:59],a[228:231],v[188:191],v[56:59]
EX v36,v36
MM v[56:59],a[224:227],v[172:175],v[56:59]
s_waitcnt vmcnt(13) lgkmcnt(0)
s_barrier
MM a[0:3],a[128:131],v[64:67],a[0:3]
s_add_u32 m0, s65, 49152
CV v24,v24,v25
CV v25,v26,v27
MM a[0:3],a[132:135],v[68:71],a[0:3]
RD a[192:195],v224 offset:16384
CV v26,v28,v29
MM a[8:11],a[132:135],v[76:79],a[8:11]
RD a[196:199],v225 offset:16384
CV v27,v30,v31
MM a[8:11],a[128:131],v[72:75],a[8:11]
buffer_load_dwordx4 v228, s[8:11], s64 offen offset:0 lds
MM a[16:19],a[128:131],v[80:83],a[16:19]
RD a[200:203],v226 offset:16384
CV v4,v32,v33
MM a[16:19],a[132:135],v[84:87],a[16:19]
RD a[204:207],v227 offset:16384
CV v5,v34,v35
MM a[24:27],a[132:135],v[92:95],a[24:27]
buffer_load_dwordx4 v229, s[8:11], s64 offen offset:1024 lds
MM a[24:27],a[128:131],v[88:91],a[24:27]
RD a[208:211],v224 offset:20480
RD a[212:215],v225 offset:20480
MM a[28:31],a[136:139],v[88:91],a[28:31]
buffer_load_dwordx4 v230, s[8:11], s64 offen offset:2048 lds
MM a[28:31],a[140:143],v[92:95],a[28:31]
RD a[216:219],v226 offset:20480
RD a[220:223],v227 offset:20480
MM a[20:23],a[140:143],v[84:87],a[20:23]
EX v37,v37
MM a[20:23],a[136:139],v[80:83],a[20:23]
RD a[224:227],v224 offset:24576
CV v6,v36,v37
MM a[12:15],a[136:139],v[72:75],a[12:15]
EX v38,v38
MM a[12:15],a[140:143],v[76:79],a[12:15]
buffer_load_dwordx4 v231, s[8:11], s64 offen offset:3072 lds
MM a[4:7],a[140:143],v[68:71],a[4:7]
s_add_u32 m0, s65, 114688
EX v39,v39
MM a[4:7],a[136:139],v[64:67],a[4:7]
RD a[228:231],v225 offset:24576
CV v7,v38,v39
MM a[32:35],a[144:147],v[64:67],a[32:35]
EX v40,v40
MM a[32:35],a[148:151],v[68:71],a[32:35]
EX v41,v41
MM a[40:43],a[148:151],v[76:79],a[40:43]
RD a[232:235],v226 offset:24576
CV v12,v40,v41
MM a[40:43],a[144:147],v[72:75],a[40:43]
EX v42,v42
MM a[48:51],a[144:147],v[80:83],a[48:51]
buffer_load_dwordx4 v235, s[12:15], s81 offen offset:0 lds
MM a[48:51],a[148:151],v[84:87],a[48:51]
EX v43,v43
MM a[56:59],a[148:151],v[92:95],a[56:59]
RD a[236:239],v227 offset:24576
CV v13,v42,v43
MM a[56:59],a[144:147],v[88:91],a[56:59]
EX v44,v44
MM a[60:63],a[152:155],v[88:91],a[60:63]
EX v45,v45
MM a[60:63],a[156:159],v[92:95],a[60:63]
RD a[240:243],v224 offset:28672
CV v14,v44,v45
MM a[52:55],a[156:159],v[84:87],a[52:55]
EX v46,v46
MM a[52:55],a[152:155],v[80:83],a[52:55]
buffer_load_dwordx4 v235, s[12:15], s64 offen offset:1024 lds
MM a[44:47],a[152:155],v[72:75],a[44:47]
EX v47,v47
MM a[44:47],a[156:159],v[76:79],a[44:47]
RD a[244:247],v225 offset:28672
CV v15,v46,v47
MM a[36:39],a[156:159],v[68:71],a[36:39]
EX v48,v48
MM a[36:39],a[152:155],v[64:67],a[36:39]
EX v49,v49
MM a[64:67],a[160:163],v[64:67],a[64:67]
RD a[248:251],v226 offset:28672
CV v20,v48,v49
MM a[64:67],a[164:167],v[68:71],a[64:67]
EX v50,v50
MM a[72:75],a[164:167],v[76:79],a[72:75]
buffer_load_dwordx4 v235, s[12:15], s81 offen offset:2048 lds
MM a[72:75],a[160:163],v[72:75],a[72:75]
EX v51,v51
MM a[80:83],a[160:163],v[80:83],a[80:83]
RD a[252:255],v227 offset:28672
CV v21,v50,v51
MM a[80:83],a[164:167],v[84:87],a[80:83]
EX v52,v52
MM a[88:91],a[164:167],v[92:95],a[88:91]
EX v53,v53
MM a[88:91],a[160:163],v[88:91],a[88:91]
buffer_load_dwordx4 v235, s[12:15], s64 offen offset:3072 lds
MM a[92:95],a[168:171],v[88:91],a[92:95]
EX v54,v54
MM a[92:95],a[172:175],v[92:95],a[92:95]
EX v55,v55
MM a[84:87],a[172:175],v[84:87],a[84:87]
EX v56,v56
MM a[84:87],a[168:171],v[80:83],a[84:87]
EX v57,v57
MM a[76:79],a[168:171],v[72:75],a[76:79]
EX v58,v58
MM a[76:79],a[172:175],v[76:79],a[76:79]
EX v59,v59
MM a[68:71],a[172:175],v[68:71],a[68:71]
EX v60,v60
MM a[68:71],a[168:171],v[64:67],a[68:71]
EX v61,v61
MM a[96:99],a[176:179],v[64:67],a[96:99]
EX v62,v62
MM a[96:99],a[180:183],v[68:71],a[96:99]
EX v63,v63
MM a[104:107],a[180:183],v[76:79],a[104:107]
CV v22,v52,v53
CV v23,v54,v55
MM a[104:107],a[176:179],v[72:75],a[104:107]
CV v28,v56,v57
CV v29,v58,v59
MM a[112:115],a[176:179],v[80:83],a[112:115]
CV v30,v60,v61
CV v31,v62,v63
MM a[112:115],a[180:183],v[84:87],a[112:115]
MM a[120:123],a[180:183],v[92:95],a[120:123]
MM a[120:123],a[176:179],v[88:91],a[120:123]
MM a[124:127],a[184:187],v[88:91],a[124:127]
MM a[124:127],a[188:191],v[92:95],a[124:127]
MM a[116:119],a[188:191],v[84:87],a[116:119]
MM a[116:119],a[184:187],v[80:83],a[116:119]
MM a[108:111],a[184:187],v[72:75],a[108:111]
MM a[108:111],a[188:191],v[76:79],a[108:111]
MM a[100:103],a[188:191],v[68:71],a[100:103]
MM a[100:103],a[184:187],v[64:67],a[100:103]
v_smfmac_f32_16x16x64_bf16 v[236:239], v[144:147], v[0:7], v234
v_smfmac_f32_16x16x64_bf16 v[236:239], v[148:151], v[8:15], v234
v_smfmac_f32_16x16x64_bf16 v[236:239], v[152:155], v[16:23], v234
v_smfmac_f32_16x16x64_bf16 v[236:239], v[156:159], v[24:31], v234
s_waitcnt lgkmcnt(0)
s_sub_u32 s70, s70, 1
s_cmp_lg_u32 s70, 0
s_cbranch_scc1 LLA_%=
MM v[64:67],a[192:195],v[160:163],v[128:131]
s_add_u32 s64, s64, 16384
TR a[128:129],v232 offset:0
s_add_u32 s81, s64, 896
MM v[64:67],a[196:199],v[176:179],v[64:67]
TR a[130:131],v232 offset:4096
TR a[132:133],v232 offset:8192
MM v[64:67],a[200:203],v[192:195],v[64:67]
TR a[134:135],v232 offset:12288
TR a[136:137],v233 offset:0
MM v[64:67],a[204:207],v[208:211],v[64:67]
TR a[138:139],v233 offset:4096
v_add_f32 v246, v236, v237
MM v[68:71],a[220:223],v[208:211],v[128:131]
TR a[140:141],v233 offset:8192
v_cmp_nge_f32 vcc, s71, v246
MM v[68:71],a[216:219],v[192:195],v[68:71]
s_cbranch_vccnz LSITE12_%=
LBACK12_%=:
MM v[68:71],a[212:215],v[176:179],v[68:71]
v_lshrrev_b32 v52, 1, v248
v_and_b32 v52, 56, v52
MM v[68:71],a[208:211],v[160:163],v[68:71]
s_mov_b32 s90, 86016
TR a[142:143],v233 offset:12288
TR a[144:145],v232 offset:512
MM v[76:79],a[208:211],v[164:167],v[132:135]
buffer_load_dwordx2 v[48:49], v52, s[20:23], s90 offen offset:0 sc1
MM v[76:79],a[212:215],v[180:183],v[76:79]
EX v64,v64
MM v[76:79],a[216:219],v[196:199],v[76:79]
EX v65,v65
MM v[76:79],a[220:223],v[212:215],v[76:79]
TR a[146:147],v232 offset:4608
CV v64,v64,v65
MM v[72:75],a[204:207],v[212:215],v[132:135]
EX v66,v66
MM v[72:75],a[200:203],v[196:199],v[72:75]
EX v67,v67
MM v[72:75],a[196:199],v[180:183],v[72:75]
TR a[148:149],v232 offset:8704
CV v65,v66,v67
MM v[72:75],a[192:195],v[164:167],v[72:75]
EX v68,v68
MM v[80:83],a[192:195],v[168:171],v[136:139]
EX v69,v69
MM v[80:83],a[196:199],v[184:187],v[80:83]
TR a[150:151],v232 offset:12800
CV v66,v68,v69
MM v[80:83],a[200:203],v[200:203],v[80:83]
EX v70,v70
MM v[80:83],a[204:207],v[216:219],v[80:83]
TR a[152:153],v233 offset:512
TR a[154:155],v233 offset:4608
MM v[84:87],a[220:223],v[216:219],v[136:139]
EX v71,v71
MM v[84:87],a[216:219],v[200:203],v[84:87]
TR a[156:157],v233 offset:8704
CV v67,v70,v71
MM v[84:87],a[212:215],v[184:187],v[84:87]
EX v72,v72
MM v[84:87],a[208:211],v[168:171],v[84:87]
TR a[158:159],v233 offset:12800
TR a[160:161],v232 offset:1024
MM v[92:95],a[208:211],v[172:175],v[140:143]
EX v73,v73
MM v[92:95],a[212:215],v[188:191],v[92:95]
TR a[162:163],v232 offset:5120
CV v72,v72,v73
MM v[92:95],a[216:219],v[204:207],v[92:95]
EX v74,v74
MM v[92:95],a[220:223],v[220:223],v[92:95]
EX v75,v75
MM v[88:91],a[204:207],v[220:223],v[140:143]
TR a[164:165],v232 offset:9216
CV v73,v74,v75
MM v[88:91],a[200:203],v[204:207],v[88:91]
EX v76,v76
MM v[88:91],a[196:199],v[188:191],v[88:91]
TR a[166:167],v232 offset:13312
TR a[168:169],v233 offset:1024
MM v[88:91],a[192:195],v[172:175],v[88:91]
EX v77,v77
MM v[96:99],a[224:227],v[160:163],v[128:131]
TR a[170:171],v233 offset:5120
CV v74,v76,v77
MM v[96:99],a[228:231],v[176:179],v[96:99]
EX v78,v78
MM v[96:99],a[232:235],v[192:195],v[96:99]
EX v79,v79
MM v[96:99],a[236:239],v[208:211],v[96:99]
TR a[172:173],v233 offset:9216
CV v75,v78,v79
MM v[100:103],a[252:255],v[208:211],v[128:131]
)ASM"
R"ASM(
EX v80,v80
MM v[100:103],a[248:251],v[192:195],v[100:103]
TR a[174:175],v233 offset:13312
TR a[176:177],v232 offset:1536
MM v[100:103],a[244:247],v[176:179],v[100:103]
EX v81,v81
MM v[100:103],a[240:243],v[160:163],v[100:103]
TR a[178:179],v232 offset:5632
CV v80,v80,v81
MM v[108:111],a[240:243],v[164:167],v[132:135]
EX v82,v82
MM v[108:111],a[244:247],v[180:183],v[108:111]
EX v83,v83
MM v[108:111],a[248:251],v[196:199],v[108:111]
TR a[180:181],v232 offset:9728
CV v81,v82,v83
MM v[108:111],a[252:255],v[212:215],v[108:111]
EX v84,v84
MM v[104:107],a[236:239],v[212:215],v[132:135]
TR a[182:183],v232 offset:13824
TR a[184:185],v233 offset:1536
MM v[104:107],a[232:235],v[196:199],v[104:107]
EX v85,v85
MM v[104:107],a[228:231],v[180:183],v[104:107]
TR a[186:187],v233 offset:5632
CV v82,v84,v85
MM v[104:107],a[224:227],v[164:167],v[104:107]
EX v86,v86
MM v[112:115],a[224:227],v[168:171],v[136:139]
TR a[188:189],v233 offset:9728
TR a[190:191],v233 offset:13824
MM v[112:115],a[228:231],v[184:187],v[112:115]
EX v87,v87
MM v[112:115],a[232:235],v[200:203],v[112:115]
CV v83,v86,v87
MM v[112:115],a[236:239],v[216:219],v[112:115]
EX v88,v88
MM v[116:119],a[252:255],v[216:219],v[136:139]
EX v89,v89
MM v[116:119],a[248:251],v[200:203],v[116:119]
EX v90,v90
MM v[116:119],a[244:247],v[184:187],v[116:119]
EX v91,v91
MM v[116:119],a[240:243],v[168:171],v[116:119]
EX v92,v92
MM v[124:127],a[240:243],v[172:175],v[140:143]
EX v93,v93
MM v[124:127],a[244:247],v[188:191],v[124:127]
EX v94,v94
MM v[124:127],a[248:251],v[204:207],v[124:127]
EX v95,v95
MM v[124:127],a[252:255],v[220:223],v[124:127]
EX v96,v96
MM v[120:123],a[236:239],v[220:223],v[140:143]
EX v97,v97
MM v[120:123],a[232:235],v[204:207],v[120:123]
EX v98,v98
MM v[120:123],a[228:231],v[188:191],v[120:123]
EX v99,v99
MM v[120:123],a[224:227],v[172:175],v[120:123]
s_waitcnt vmcnt(14) lgkmcnt(0)
s_barrier
MM a[0:3],a[128:131],v[0:3],a[0:3]
s_waitcnt vmcnt(0)
v_cmp_ne_u32 s[76:77], s69, v49
CV v88,v88,v89
MM a[0:3],a[132:135],v[4:7],a[0:3]
s_add_u32 m0, s65, 0
CV v89,v90,v91
CV v90,v92,v93
MM a[8:11],a[132:135],v[12:15],a[8:11]
s_cmp_lg_u64 s[76:77], 0
s_cbranch_scc1 LRIX_%=
LRIXb_%=:
v_readlane_b32 s52, v48, 0
MM a[8:11],a[128:131],v[8:11],a[8:11]
v_readlane_b32 s53, v48, 1
v_readlane_b32 s54, v48, 2
MM a[16:19],a[128:131],v[16:19],a[16:19]
v_readlane_b32 s55, v48, 3
v_readlane_b32 s56, v48, 4
MM a[16:19],a[132:135],v[20:23],a[16:19]
v_readlane_b32 s57, v48, 5
v_readlane_b32 s58, v48, 6
MM a[24:27],a[132:135],v[28:31],a[24:27]
RD a[192:195],v224 offset:32768
v_readlane_b32 s59, v48, 7
MM a[24:27],a[128:131],v[24:27],a[24:27]
RD a[196:199],v225 offset:32768
CV v91,v94,v95
MM a[28:31],a[136:139],v[24:27],a[28:31]
buffer_load_dwordx4 v228, s[8:11], s64 offen offset:0 lds
MM a[28:31],a[140:143],v[28:31],a[28:31]
s_lshl_b32 s90, %[swave], 1
RD a[200:203],v226 offset:32768
CV v68,v96,v97
MM a[20:23],a[140:143],v[20:23],a[20:23]
s_add_u32 s91, s90, 1
v_readlane_b32 s66, v48, s90
v_readlane_b32 s67, v48, s91
MM a[20:23],a[136:139],v[16:19],a[20:23]
s_mul_i32 s91, %[swave], 640
CV v69,v98,v99
MM a[12:15],a[136:139],v[8:11],a[12:15]
EX v100,v100
MM a[12:15],a[140:143],v[12:15],a[12:15]
buffer_load_dwordx4 v229, s[8:11], s64 offen offset:1024 lds
MM a[4:7],a[140:143],v[4:7],a[4:7]
s_add_u32 s91, s91, %[slds]
EX v101,v101
MM a[4:7],a[136:139],v[0:3],a[4:7]
s_bfe_u32 s90, s48, 131077
RD a[204:207],v227 offset:32768
CV v70,v100,v101
MM a[32:35],a[144:147],v[0:3],a[32:35]
s_mul_i32 s90, s90, 320
EX v102,v102
MM a[32:35],a[148:151],v[4:7],a[32:35]
s_add_u32 s90, s90, 131072
EX v103,v103
MM a[40:43],a[148:151],v[12:15],a[40:43]
s_mul_i32 s66, s66, 1280
RD a[208:211],v224 offset:36864
CV v71,v102,v103
MM a[40:43],a[144:147],v[8:11],a[40:43]
s_add_u32 s66, s66, s90
s_add_u32 m0, s91, 151552
s_mov_b64 exec, 0xfffff
buffer_load_dwordx4 v248, s[20:23], s66 offen offset:0 sc1 lds
s_mov_b64 exec, -1
MM a[48:51],a[144:147],v[16:19],a[48:51]
s_mul_i32 s67, s67, 1280
EX v104,v104
MM a[48:51],a[148:151],v[20:23],a[48:51]
s_add_u32 s67, s67, s90
s_add_u32 m0, s91, 151872
s_mov_b64 exec, 0xfffff
buffer_load_dwordx4 v248, s[20:23], s67 offen offset:0 sc1 lds
s_mov_b64 exec, -1
MM a[56:59],a[148:151],v[28:31],a[56:59]
s_add_u32 m0, s65, 0
EX v105,v105
MM a[56:59],a[144:147],v[24:27],a[56:59]
buffer_load_dwordx4 v230, s[8:11], s64 offen offset:2048 lds
MM a[60:63],a[152:155],v[24:27],a[60:63]
EX v106,v106
MM a[60:63],a[156:159],v[28:31],a[60:63]
RD a[212:215],v225 offset:36864
CV v76,v104,v105
MM a[52:55],a[156:159],v[20:23],a[52:55]
EX v107,v107
MM a[52:55],a[152:155],v[16:19],a[52:55]
EX v108,v108
MM a[44:47],a[152:155],v[8:11],a[44:47]
RD a[216:219],v226 offset:36864
CV v77,v106,v107
MM a[44:47],a[156:159],v[12:15],a[44:47]
EX v109,v109
MM a[36:39],a[156:159],v[4:7],a[36:39]
buffer_load_dwordx4 v231, s[8:11], s64 offen offset:3072 lds
MM a[36:39],a[152:155],v[0:3],a[36:39]
s_add_u32 m0, s65, 65536
EX v110,v110
MM a[64:67],a[160:163],v[0:3],a[64:67]
RD a[220:223],v227 offset:36864
CV v78,v108,v109
MM a[64:67],a[164:167],v[4:7],a[64:67]
EX v111,v111
MM a[72:75],a[164:167],v[12:15],a[72:75]
EX v112,v112
MM a[72:75],a[160:163],v[8:11],a[72:75]
RD a[224:227],v224 offset:40960
CV v79,v110,v111
MM a[80:83],a[160:163],v[16:19],a[80:83]
EX v113,v113
MM a[80:83],a[164:167],v[20:23],a[80:83]
buffer_load_dwordx4 v235, s[12:15], s81 offen offset:0 lds
MM a[88:91],a[164:167],v[28:31],a[88:91]
EX v114,v114
MM a[88:91],a[160:163],v[24:27],a[88:91]
RD a[228:231],v225 offset:40960
CV v84,v112,v113
MM a[92:95],a[168:171],v[24:27],a[92:95]
EX v115,v115
MM a[92:95],a[172:175],v[28:31],a[92:95]
RD a[232:235],v226 offset:40960
CV v85,v114,v115
MM a[84:87],a[172:175],v[20:23],a[84:87]
EX v116,v116
MM a[84:87],a[168:171],v[16:19],a[84:87]
EX v117,v117
MM a[76:79],a[168:171],v[8:11],a[76:79]
RD a[236:239],v227 offset:40960
CV v86,v116,v117
MM a[76:79],a[172:175],v[12:15],a[76:79]
EX v118,v118
MM a[68:71],a[172:175],v[4:7],a[68:71]
buffer_load_dwordx4 v235, s[12:15], s64 offen offset:1024 lds
MM a[68:71],a[168:171],v[0:3],a[68:71]
EX v119,v119
MM a[96:99],a[176:179],v[0:3],a[96:99]
RD a[240:243],v224 offset:45056
CV v87,v118,v119
MM a[96:99],a[180:183],v[4:7],a[96:99]
EX v120,v120
MM a[104:107],a[180:183],v[12:15],a[104:107]
LPG9_%=:
s_cbranch_execz LTR0_%=
EX v121,v121
MM a[104:107],a[176:179],v[8:11],a[104:107]
RD a[244:247],v225 offset:45056
CV v92,v120,v121
MM a[112:115],a[176:179],v[16:19],a[112:115]
buffer_load_dwordx4 v235, s[12:15], s81 offen offset:2048 lds
MM a[112:115],a[180:183],v[20:23],a[112:115]
EX v122,v122
MM a[120:123],a[180:183],v[28:31],a[120:123]
EX v123,v123
MM a[120:123],a[176:179],v[24:27],a[120:123]
RD a[248:251],v226 offset:45056
CV v93,v122,v123
MM a[124:127],a[184:187],v[24:27],a[124:127]
EX v124,v124
MM a[124:127],a[188:191],v[28:31],a[124:127]
EX v125,v125
MM a[116:119],a[188:191],v[20:23],a[116:119]
RD a[252:255],v227 offset:45056
CV v94,v124,v125
MM a[116:119],a[184:187],v[16:19],a[116:119]
buffer_load_dwordx4 v235, s[12:15], s64 offen offset:3072 lds
MM a[108:111],a[184:187],v[8:11],a[108:111]
EX v126,v126
MM a[108:111],a[188:191],v[12:15],a[108:111]
EX v127,v127
MM a[100:103],a[188:191],v[4:7],a[100:103]
CV v95,v126,v127
MM a[100:103],a[184:187],v[0:3],a[100:103]
v_smfmac_f32_16x16x64_bf16 v[236:239], v[144:147], v[64:71], v234
v_smfmac_f32_16x16x64_bf16 v[236:239], v[148:151], v[72:79], v234
v_smfmac_f32_16x16x64_bf16 v[236:239], v[152:155], v[80:87], v234
v_smfmac_f32_16x16x64_bf16 v[236:239], v[156:159], v[88:95], v234
s_waitcnt lgkmcnt(0)
MM v[0:3],a[192:195],v[160:163],v[128:131]
s_add_u32 s64, s64, 16384
TR a[128:129],v232 offset:16384
s_add_u32 s81, s64, 896
MM v[0:3],a[196:199],v[176:179],v[0:3]
TR a[130:131],v232 offset:20480
TR a[132:133],v232 offset:24576
MM v[0:3],a[200:203],v[192:195],v[0:3]
TR a[134:135],v232 offset:28672
TR a[136:137],v233 offset:16384
MM v[0:3],a[204:207],v[208:211],v[0:3]
TR a[138:139],v233 offset:20480
v_add_f32 v247, v236, v237
MM v[4:7],a[220:223],v[208:211],v[128:131]
TR a[140:141],v233 offset:24576
v_cmp_nge_f32 vcc, s71, v247
MM v[4:7],a[216:219],v[192:195],v[4:7]
s_cbranch_vccnz LSITE13_%=
LBACK13_%=:
MM v[4:7],a[212:215],v[176:179],v[4:7]
v_lshrrev_b32 v127, 8, v248
v_lshlrev_b32 v124, 9, v127
MM v[4:7],a[208:211],v[160:163],v[4:7]
v_and_b32 v126, 192, v248
v_add_u32 v124, v124, v126
MM v[12:15],a[208:211],v[164:167],v[132:135]
v_bfe_u32 v126, v248, 4, 2
v_lshl_add_u32 v124, v126, 3, v124
MM v[12:15],a[212:215],v[180:183],v[12:15]
s_lshr_b32 s76, %[swave], 1
EX v0,v0
MM v[12:15],a[216:219],v[196:199],v[12:15]
s_mul_i32 s76, s76, 10240
EX v1,v1
MM v[12:15],a[220:223],v[212:215],v[12:15]
s_and_b32 s77, %[swave], 1
TR a[142:143],v233 offset:28672
CV v0,v0,v1
MM v[8:11],a[204:207],v[212:215],v[132:135]
s_lshl_b32 s77, s77, 5
EX v2,v2
MM v[8:11],a[200:203],v[196:199],v[8:11]
s_add_u32 s76, s76, s77
TR a[144:145],v232 offset:16896
TR a[146:147],v232 offset:20992
MM v[8:11],a[196:199],v[180:183],v[8:11]
s_add_u32 s76, s76, %[slds]
EX v3,v3
MM v[8:11],a[192:195],v[164:167],v[8:11]
s_add_u32 s76, s76, 131072
v_add_u32 v124, s76, v124
v_bfe_u32 v126, v248, 4, 3
MM v[16:19],a[192:195],v[168:171],v[136:139]
v_mul_u32_u24 v125, 320, v126
v_lshl_add_u32 v125, v127, 4, v125
MM v[16:19],a[196:199],v[184:187],v[16:19]
s_add_u32 s76, %[slds], 151552
v_add_u32 v125, s76, v125
v_lshl_add_u32 v126, v127, 3, v126
MM v[16:19],a[200:203],v[200:203],v[16:19]
v_lshlrev_b32 v126, 4, v126
CV v1,v2,v3
MM v[16:19],a[204:207],v[216:219],v[16:19]
s_lshr_b32 s76, s48, 7
EX v4,v4
MM v[20:23],a[220:223],v[216:219],v[136:139]
s_lshl_b32 s76, s76, 5
EX v5,v5
MM v[20:23],a[216:219],v[200:203],v[20:23]
s_and_b32 s77, s48, 16
TR a[148:149],v232 offset:25088
TR a[150:151],v232 offset:29184
MM v[20:23],a[212:215],v[184:187],v[20:23]
s_add_u32 s76, s76, s77
EX v6,v6
MM v[20:23],a[208:211],v[168:171],v[20:23]
s_bfe_u32 s77, s48, 131077
EX v7,v7
MM v[28:31],a[208:211],v[172:175],v[140:143]
s_lshl_b32 s77, s77, 2
TR a[152:153],v233 offset:16896
CV v2,v4,v5
MM v[28:31],a[212:215],v[188:191],v[28:31]
s_add_u32 s76, s76, s77
EX v8,v8
MM v[28:31],a[216:219],v[204:207],v[28:31]
s_add_u32 s76, s76, %[swave]
TR a[154:155],v233 offset:20992
CV v3,v6,v7
MM v[28:31],a[220:223],v[220:223],v[28:31]
s_lshl_b32 s76, s76, 9
EX v9,v9
MM v[24:27],a[204:207],v[220:223],v[140:143]
s_add_u32 s76, s76, 22183936
TR a[156:157],v233 offset:25088
TR a[158:159],v233 offset:29184
MM v[24:27],a[200:203],v[204:207],v[24:27]
EX v10,v10
MM v[24:27],a[196:199],v[188:191],v[24:27]
EX v11,v11
MM v[24:27],a[192:195],v[172:175],v[24:27]
TR a[160:161],v232 offset:17408
CV v8,v8,v9
MM v[32:35],a[224:227],v[160:163],v[128:131]
EX v12,v12
MM v[32:35],a[228:231],v[176:179],v[32:35]
TR a[162:163],v232 offset:21504
CV v9,v10,v11
MM v[32:35],a[232:235],v[192:195],v[32:35]
EX v13,v13
MM v[32:35],a[236:239],v[208:211],v[32:35]
TR a[164:165],v232 offset:25600
TR a[166:167],v232 offset:29696
MM v[36:39],a[252:255],v[208:211],v[128:131]
EX v14,v14
MM v[36:39],a[248:251],v[192:195],v[36:39]
EX v15,v15
MM v[36:39],a[244:247],v[176:179],v[36:39]
TR a[168:169],v233 offset:17408
CV v10,v12,v13
MM v[36:39],a[240:243],v[160:163],v[36:39]
EX v16,v16
MM v[44:47],a[240:243],v[164:167],v[132:135]
TR a[170:171],v233 offset:21504
CV v11,v14,v15
MM v[44:47],a[244:247],v[180:183],v[44:47]
EX v17,v17
MM v[44:47],a[248:251],v[196:199],v[44:47]
TR a[172:173],v233 offset:25600
TR a[174:175],v233 offset:29696
MM v[44:47],a[252:255],v[212:215],v[44:47]
EX v18,v18
MM v[40:43],a[236:239],v[212:215],v[132:135]
TR a[176:177],v232 offset:17920
CV v16,v16,v17
MM v[40:43],a[232:235],v[196:199],v[40:43]
EX v19,v19
MM v[40:43],a[228:231],v[180:183],v[40:43]
TR a[178:179],v232 offset:22016
CV v17,v18,v19
MM v[40:43],a[224:227],v[164:167],v[40:43]
EX v20,v20
MM v[48:51],a[224:227],v[168:171],v[136:139]
EX v21,v21
MM v[48:51],a[228:231],v[184:187],v[48:51]
TR a[180:181],v232 offset:26112
TR a[182:183],v232 offset:30208
MM v[48:51],a[232:235],v[200:203],v[48:51]
EX v22,v22
MM v[48:51],a[236:239],v[216:219],v[48:51]
TR a[184:185],v233 offset:17920
)ASM"
R"ASM(
CV v18,v20,v21
MM v[52:55],a[252:255],v[216:219],v[136:139]
EX v23,v23
MM v[52:55],a[248:251],v[200:203],v[52:55]
TR a[186:187],v233 offset:22016
CV v19,v22,v23
MM v[52:55],a[244:247],v[184:187],v[52:55]
EX v24,v24
MM v[52:55],a[240:243],v[168:171],v[52:55]
TR a[188:189],v233 offset:26112
TR a[190:191],v233 offset:30208
MM v[60:63],a[240:243],v[172:175],v[140:143]
EX v25,v25
MM v[60:63],a[244:247],v[188:191],v[60:63]
EX v26,v26
MM v[60:63],a[248:251],v[204:207],v[60:63]
EX v27,v27
MM v[60:63],a[252:255],v[220:223],v[60:63]
EX v28,v28
MM v[56:59],a[236:239],v[220:223],v[140:143]
EX v29,v29
MM v[56:59],a[232:235],v[204:207],v[56:59]
EX v30,v30
MM v[56:59],a[228:231],v[188:191],v[56:59]
EX v31,v31
MM v[56:59],a[224:227],v[172:175],v[56:59]
s_waitcnt vmcnt(6) lgkmcnt(0)
s_barrier
MM a[0:3],a[128:131],v[64:67],a[0:3]
s_add_u32 m0, s65, 16384
CV v24,v24,v25
CV v25,v26,v27
MM a[0:3],a[132:135],v[68:71],a[0:3]
RD a[192:195],v224 offset:49152
CV v26,v28,v29
MM a[8:11],a[132:135],v[76:79],a[8:11]
RD a[196:199],v225 offset:49152
CV v27,v30,v31
MM a[8:11],a[128:131],v[72:75],a[8:11]
buffer_load_dwordx4 v228, s[8:11], s64 offen offset:0 lds
MM a[16:19],a[128:131],v[80:83],a[16:19]
EX v32,v32
MM a[16:19],a[132:135],v[84:87],a[16:19]
EX v33,v33
MM a[24:27],a[132:135],v[92:95],a[24:27]
RD a[200:203],v226 offset:49152
CV v4,v32,v33
MM a[24:27],a[128:131],v[88:91],a[24:27]
EX v34,v34
MM a[28:31],a[136:139],v[88:91],a[28:31]
buffer_load_dwordx4 v229, s[8:11], s64 offen offset:1024 lds
MM a[28:31],a[140:143],v[92:95],a[28:31]
EX v35,v35
MM a[20:23],a[140:143],v[84:87],a[20:23]
RD a[204:207],v227 offset:49152
CV v5,v34,v35
MM a[20:23],a[136:139],v[80:83],a[20:23]
EX v36,v36
MM a[12:15],a[136:139],v[72:75],a[12:15]
TR v[96:97],v124 offset:0
TR v[98:99],v124 offset:256
MM a[12:15],a[140:143],v[76:79],a[12:15]
RD v[108:111],v125 offset:0
TR v[100:101],v124 offset:2048
MM a[4:7],a[140:143],v[68:71],a[4:7]
TR v[102:103],v124 offset:2304
RD v[112:115],v125 offset:64
MM a[4:7],a[136:139],v[64:67],a[4:7]
TR v[104:105],v124 offset:4096
TR v[106:107],v124 offset:4352
MM a[32:35],a[144:147],v[64:67],a[32:35]
RD v[116:119],v125 offset:128
RD a[208:211],v224 offset:53248
MM a[32:35],a[148:151],v[68:71],a[32:35]
EX v37,v37
MM a[40:43],a[148:151],v[76:79],a[40:43]
EX v38,v38
MM a[40:43],a[144:147],v[72:75],a[40:43]
EX v39,v39
MM a[48:51],a[144:147],v[80:83],a[48:51]
CV v6,v36,v37
CV v7,v38,v39
MM a[48:51],a[148:151],v[84:87],a[48:51]
EX v40,v40
MM a[56:59],a[148:151],v[92:95],a[56:59]
EX v41,v41
MM a[56:59],a[144:147],v[88:91],a[56:59]
buffer_load_dwordx4 v230, s[8:11], s64 offen offset:2048 lds
MM a[60:63],a[152:155],v[88:91],a[60:63]
s_waitcnt lgkmcnt(1)
EX v42,v42
MM a[60:63],a[156:159],v[92:95],a[60:63]
RD a[212:215],v225 offset:53248
CV v12,v40,v41
MM v[120:123],v[96:99],v[108:111],0
MM v[120:123],v[100:103],v[112:115],v[120:123]
MM v[120:123],v[104:107],v[116:119],v[120:123]
MM a[52:55],a[156:159],v[84:87],a[52:55]
TR v[96:97],v124 offset:6144
TR v[98:99],v124 offset:6400
MM a[52:55],a[152:155],v[80:83],a[52:55]
RD v[108:111],v125 offset:192
TR v[100:101],v124 offset:8192
MM a[44:47],a[152:155],v[72:75],a[44:47]
TR v[102:103],v124 offset:8448
RD v[112:115],v125 offset:256
EX v43,v43
MM a[44:47],a[156:159],v[76:79],a[44:47]
EX v44,v44
MM a[36:39],a[156:159],v[68:71],a[36:39]
EX v45,v45
MM a[36:39],a[152:155],v[64:67],a[36:39]
RD a[216:219],v226 offset:53248
CV v13,v42,v43
MM a[64:67],a[160:163],v[64:67],a[64:67]
EX v46,v46
MM a[64:67],a[164:167],v[68:71],a[64:67]
EX v47,v47
MM a[72:75],a[164:167],v[76:79],a[72:75]
buffer_load_dwordx4 v231, s[8:11], s64 offen offset:3072 lds
MM a[72:75],a[160:163],v[72:75],a[72:75]
s_add_u32 m0, s65, 81920
RD a[220:223],v227 offset:53248
CV v14,v44,v45
MM a[80:83],a[160:163],v[80:83],a[80:83]
s_waitcnt lgkmcnt(2)
EX v48,v48
MM a[80:83],a[164:167],v[84:87],a[80:83]
EX v49,v49
MM v[120:123],v[96:99],v[108:111],v[120:123]
MM v[120:123],v[100:103],v[112:115],v[120:123]
MM a[88:91],a[164:167],v[92:95],a[88:91]
RD a[224:227],v224 offset:57344
CV v15,v46,v47
MM a[88:91],a[160:163],v[88:91],a[88:91]
EX v50,v50
MM a[92:95],a[168:171],v[88:91],a[92:95]
EX v51,v51
MM a[92:95],a[172:175],v[92:95],a[92:95]
buffer_load_dwordx4 v235, s[12:15], s81 offen offset:0 lds
MM a[84:87],a[172:175],v[84:87],a[84:87]
RD a[228:231],v225 offset:57344
CV v20,v48,v49
MM a[84:87],a[168:171],v[80:83],a[84:87]
EX v52,v52
MM a[76:79],a[168:171],v[72:75],a[76:79]
s_mov_b32 exec_lo, 0x00ff00ff
s_mov_b32 exec_hi, 0x00ff00ff
buffer_store_dwordx4 v[120:123], v126, s[20:23], s76 offen offset:0 sc1
s_mov_b64 exec, -1
s_nop 0
MM a[76:79],a[172:175],v[76:79],a[76:79]
s_lshl_b32 s76, s50, 2
EX v53,v53
MM a[68:71],a[172:175],v[68:71],a[68:71]
s_add_u32 s76, s76, s84
RD a[232:235],v226 offset:57344
CV v21,v50,v51
MM a[68:71],a[168:171],v[64:67],a[68:71]
s_lshl_b32 s76, s76, 3
EX v54,v54
MM a[96:99],a[176:179],v[64:67],a[96:99]
s_lshr_b32 s77, s49, 2
EX v55,v55
MM a[96:99],a[180:183],v[68:71],a[96:99]
RD a[236:239],v227 offset:57344
CV v22,v52,v53
MM a[104:107],a[180:183],v[76:79],a[104:107]
EX v56,v56
MM a[104:107],a[176:179],v[72:75],a[104:107]
buffer_load_dwordx4 v235, s[12:15], s64 offen offset:1024 lds
MM a[112:115],a[176:179],v[80:83],a[112:115]
s_add_u32 s76, s76, s77
EX v57,v57
MM a[112:115],a[180:183],v[84:87],a[112:115]
s_mul_i32 s95, s76, 82944
RD a[240:243],v224 offset:61440
CV v23,v54,v55
MM a[120:123],a[180:183],v[92:95],a[120:123]
s_mul_i32 s76, %[swave], 20480
EX v58,v58
MM a[120:123],a[176:179],v[88:91],a[120:123]
s_add_u32 s95, s95, s76
EX v59,v59
MM a[124:127],a[184:187],v[88:91],a[124:127]
s_add_u32 s95, s95, 22708224
v_mov_b32 v240, -1
v_mov_b32 v241, -1
MM a[124:127],a[188:191],v[92:95],a[124:127]
v_mov_b32 v242, -1
v_mov_b32 v243, -1
MM a[116:119],a[188:191],v[84:87],a[116:119]
EX v60,v60
MM a[116:119],a[184:187],v[80:83],a[116:119]
RD a[244:247],v225 offset:61440
CV v28,v56,v57
MM a[108:111],a[184:187],v[72:75],a[108:111]
EX v61,v61
MM a[108:111],a[188:191],v[76:79],a[108:111]
buffer_load_dwordx4 v235, s[12:15], s81 offen offset:2048 lds
MM a[100:103],a[188:191],v[68:71],a[100:103]
EX v62,v62
MM a[100:103],a[184:187],v[64:67],a[100:103]
RD a[248:251],v226 offset:61440
CV v29,v58,v59
v_smfmac_f32_16x16x64_bf16 v[236:239], v[144:147], v[0:7], v234
RD a[252:255],v227 offset:61440
EX v63,v63
v_smfmac_f32_16x16x64_bf16 v[236:239], v[148:151], v[8:15], v234
CV v30,v60,v61
v_smfmac_f32_16x16x64_bf16 v[236:239], v[152:155], v[16:23], v234
CV v31,v62,v63
s_nop 1
v_smfmac_f32_16x16x64_bf16 v[236:239], v[156:159], v[24:31], v234
s_waitcnt lgkmcnt(0)
buffer_load_dwordx4 v235, s[12:15], s64 offen offset:3072 lds
MM v[64:67],a[192:195],v[160:163],v[128:131]
s_add_u32 s64, s64, 16384
TR a[128:129],v232 offset:32768
s_add_u32 s81, s64, 896
MM v[64:67],a[196:199],v[176:179],v[64:67]
TR a[130:131],v232 offset:36864
TR a[132:133],v232 offset:40960
MM v[64:67],a[200:203],v[192:195],v[64:67]
TR a[134:135],v232 offset:45056
TR a[136:137],v233 offset:32768
MM v[64:67],a[204:207],v[208:211],v[64:67]
TR a[138:139],v233 offset:36864
v_add_f32 v246, v236, v237
MM v[68:71],a[220:223],v[208:211],v[128:131]
TR a[140:141],v233 offset:40960
v_cmp_nge_f32 vcc, s71, v246
MM v[68:71],a[216:219],v[192:195],v[68:71]
s_cbranch_vccnz LSITE14_%=
LBACK14_%=:
MM v[68:71],a[212:215],v[176:179],v[68:71]
s_lshr_b32 s77, %[sb], 3
TR a[142:143],v233 offset:45056
TR a[144:145],v232 offset:33280
MM v[68:71],a[208:211],v[160:163],v[68:71]
s_lshl_b32 s77, s77, 2
TR a[146:147],v232 offset:37376
TR a[148:149],v232 offset:41472
MM v[76:79],a[208:211],v[164:167],v[132:135]
s_add_u32 s77, s77, %[swave]
EX v64,v64
MM v[76:79],a[212:215],v[180:183],v[76:79]
s_lshl_b32 s77, s77, 9
EX v65,v65
MM v[76:79],a[216:219],v[196:199],v[76:79]
s_add_u32 s77, s77, 0
v_lshrrev_b32 v253, 1, v248
v_add_u32 v253, s77, v253
MM v[76:79],a[220:223],v[212:215],v[76:79]
s_mov_b32 s77, LKEND_ka-LPD0_%=-16
v_min_u32 v253, s77, v253
CV v64,v64,v65
MM v[72:75],a[204:207],v[212:215],v[132:135]
s_getpc_b64 s[98:99]
LPD0_%=:
global_load_dword v253, v253, s[98:99]
MM v[72:75],a[200:203],v[196:199],v[72:75]
EX v66,v66
MM v[72:75],a[196:199],v[180:183],v[72:75]
EX v67,v67
MM v[72:75],a[192:195],v[164:167],v[72:75]
TR a[150:151],v232 offset:45568
CV v65,v66,v67
MM v[80:83],a[192:195],v[168:171],v[136:139]
EX v68,v68
MM v[80:83],a[196:199],v[184:187],v[80:83]
EX v69,v69
MM v[80:83],a[200:203],v[200:203],v[80:83]
TR a[152:153],v233 offset:33280
CV v66,v68,v69
MM v[80:83],a[204:207],v[216:219],v[80:83]
EX v70,v70
MM v[84:87],a[220:223],v[216:219],v[136:139]
EX v71,v71
MM v[84:87],a[216:219],v[200:203],v[84:87]
TR a[154:155],v233 offset:37376
CV v67,v70,v71
MM v[84:87],a[212:215],v[184:187],v[84:87]
EX v72,v72
MM v[84:87],a[208:211],v[168:171],v[84:87]
TR a[156:157],v233 offset:41472
TR a[158:159],v233 offset:45568
MM v[92:95],a[208:211],v[172:175],v[140:143]
EX v73,v73
MM v[92:95],a[212:215],v[188:191],v[92:95]
TR a[160:161],v232 offset:33792
CV v72,v72,v73
MM v[92:95],a[216:219],v[204:207],v[92:95]
EX v74,v74
MM v[92:95],a[220:223],v[220:223],v[92:95]
EX v75,v75
MM v[88:91],a[204:207],v[220:223],v[140:143]
TR a[162:163],v232 offset:37888
CV v73,v74,v75
MM v[88:91],a[200:203],v[204:207],v[88:91]
TR a[164:165],v232 offset:41984
TR a[166:167],v232 offset:46080
MM v[88:91],a[196:199],v[188:191],v[88:91]
EX v76,v76
MM v[88:91],a[192:195],v[172:175],v[88:91]
EX v77,v77
MM v[96:99],a[224:227],v[160:163],v[128:131]
TR a[168:169],v233 offset:33792
CV v74,v76,v77
MM v[96:99],a[228:231],v[176:179],v[96:99]
EX v78,v78
MM v[96:99],a[232:235],v[192:195],v[96:99]
TR a[170:171],v233 offset:37888
TR a[172:173],v233 offset:41984
MM v[96:99],a[236:239],v[208:211],v[96:99]
EX v79,v79
MM v[100:103],a[252:255],v[208:211],v[128:131]
TR a[174:175],v233 offset:46080
CV v75,v78,v79
MM v[100:103],a[248:251],v[192:195],v[100:103]
EX v80,v80
MM v[100:103],a[244:247],v[176:179],v[100:103]
EX v81,v81
MM v[100:103],a[240:243],v[160:163],v[100:103]
TR a[176:177],v232 offset:34304
CV v80,v80,v81
MM v[108:111],a[240:243],v[164:167],v[132:135]
s_add_u32 s67, s95, 10240
EX v82,v82
MM v[108:111],a[244:247],v[180:183],v[108:111]
buffer_store_dwordx4 v[240:243], v248, s[20:23], s67 offen offset:0 sc1
MM v[108:111],a[248:251],v[196:199],v[108:111]
TR a[178:179],v232 offset:38400
TR a[180:181],v232 offset:42496
MM v[108:111],a[252:255],v[212:215],v[108:111]
EX v83,v83
MM v[104:107],a[236:239],v[212:215],v[132:135]
TR a[182:183],v232 offset:46592
CV v81,v82,v83
MM v[104:107],a[232:235],v[196:199],v[104:107]
EX v84,v84
MM v[104:107],a[228:231],v[180:183],v[104:107]
EX v85,v85
MM v[104:107],a[224:227],v[164:167],v[104:107]
TR a[184:185],v233 offset:34304
CV v82,v84,v85
MM v[112:115],a[224:227],v[168:171],v[136:139]
EX v86,v86
MM v[112:115],a[228:231],v[184:187],v[112:115]
TR a[186:187],v233 offset:38400
TR a[188:189],v233 offset:42496
MM v[112:115],a[232:235],v[200:203],v[112:115]
EX v87,v87
MM v[112:115],a[236:239],v[216:219],v[112:115]
TR a[190:191],v233 offset:46592
CV v83,v86,v87
MM v[116:119],a[252:255],v[216:219],v[136:139]
EX v88,v88
MM v[116:119],a[248:251],v[200:203],v[116:119]
EX v89,v89
MM v[116:119],a[244:247],v[184:187],v[116:119]
EX v90,v90
MM v[116:119],a[240:243],v[168:171],v[116:119]
EX v91,v91
MM v[124:127],a[240:243],v[172:175],v[140:143]
EX v92,v92
MM v[124:127],a[244:247],v[188:191],v[124:127]
EX v93,v93
MM v[124:127],a[248:251],v[204:207],v[124:127]
EX v94,v94
MM v[124:127],a[252:255],v[220:223],v[124:127]
EX v95,v95
MM v[120:123],a[236:239],v[220:223],v[140:143]
EX v96,v96
MM v[120:123],a[232:235],v[204:207],v[120:123]
EX v97,v97
MM v[120:123],a[228:231],v[188:191],v[120:123]
EX v98,v98
MM v[120:123],a[224:227],v[172:175],v[120:123]
s_waitcnt vmcnt(15) lgkmcnt(0)
s_barrier
MM a[0:3],a[128:131],v[0:3],a[0:3]
s_add_u32 m0, s65, 32768
CV v88,v88,v89
CV v89,v90,v91
MM a[0:3],a[132:135],v[4:7],a[0:3]
RD a[192:195],v224 offset:0
CV v90,v92,v93
MM a[8:11],a[132:135],v[12:15],a[8:11]
RD a[196:199],v225 offset:0
CV v91,v94,v95
MM a[8:11],a[128:131],v[8:11],a[8:11]
buffer_load_dwordx4 v228, s[8:11], s64 offen offset:0 lds
)ASM"
R"ASM(
MM a[16:19],a[128:131],v[16:19],a[16:19]
RD a[200:203],v226 offset:0
CV v68,v96,v97
MM a[16:19],a[132:135],v[20:23],a[16:19]
buffer_load_dwordx4 v229, s[8:11], s64 offen offset:1024 lds
MM a[24:27],a[132:135],v[28:31],a[24:27]
RD a[204:207],v227 offset:0
RD a[208:211],v224 offset:4096
MM a[24:27],a[128:131],v[24:27],a[24:27]
EX v99,v99
MM a[28:31],a[136:139],v[24:27],a[28:31]
RD a[212:215],v225 offset:4096
CV v69,v98,v99
MM a[28:31],a[140:143],v[28:31],a[28:31]
EX v100,v100
MM a[20:23],a[140:143],v[20:23],a[20:23]
buffer_load_dwordx4 v230, s[8:11], s64 offen offset:2048 lds
MM a[20:23],a[136:139],v[16:19],a[20:23]
LPG10_%=:
s_cbranch_execz LTR0_%=
EX v101,v101
MM a[12:15],a[136:139],v[8:11],a[12:15]
RD a[216:219],v226 offset:4096
CV v70,v100,v101
MM a[12:15],a[140:143],v[12:15],a[12:15]
EX v102,v102
MM a[4:7],a[140:143],v[4:7],a[4:7]
EX v103,v103
MM a[4:7],a[136:139],v[0:3],a[4:7]
RD a[220:223],v227 offset:4096
CV v71,v102,v103
MM a[32:35],a[144:147],v[0:3],a[32:35]
EX v104,v104
MM a[32:35],a[148:151],v[4:7],a[32:35]
buffer_load_dwordx4 v231, s[8:11], s64 offen offset:3072 lds
MM a[40:43],a[148:151],v[12:15],a[40:43]
s_add_u32 m0, s65, 98304
EX v105,v105
MM a[40:43],a[144:147],v[8:11],a[40:43]
RD a[224:227],v224 offset:8192
CV v76,v104,v105
MM a[48:51],a[144:147],v[16:19],a[48:51]
EX v106,v106
MM a[48:51],a[148:151],v[20:23],a[48:51]
EX v107,v107
MM a[56:59],a[148:151],v[28:31],a[56:59]
RD a[228:231],v225 offset:8192
CV v77,v106,v107
MM a[56:59],a[144:147],v[24:27],a[56:59]
EX v108,v108
MM a[60:63],a[152:155],v[24:27],a[60:63]
buffer_load_dwordx4 v235, s[12:15], s81 offen offset:0 lds
MM a[60:63],a[156:159],v[28:31],a[60:63]
EX v109,v109
MM a[52:55],a[156:159],v[20:23],a[52:55]
RD a[232:235],v226 offset:8192
CV v78,v108,v109
MM a[52:55],a[152:155],v[16:19],a[52:55]
EX v110,v110
MM a[44:47],a[152:155],v[8:11],a[44:47]
EX v111,v111
MM a[44:47],a[156:159],v[12:15],a[44:47]
RD a[236:239],v227 offset:8192
CV v79,v110,v111
MM a[36:39],a[156:159],v[4:7],a[36:39]
buffer_load_dwordx4 v235, s[12:15], s64 offen offset:1024 lds
MM a[36:39],a[152:155],v[0:3],a[36:39]
EX v112,v112
MM a[64:67],a[160:163],v[0:3],a[64:67]
EX v113,v113
MM a[64:67],a[164:167],v[4:7],a[64:67]
RD a[240:243],v224 offset:12288
CV v84,v112,v113
MM a[72:75],a[164:167],v[12:15],a[72:75]
EX v114,v114
MM a[72:75],a[160:163],v[8:11],a[72:75]
RD a[244:247],v225 offset:12288
RD a[248:251],v226 offset:12288
MM a[80:83],a[160:163],v[16:19],a[80:83]
EX v115,v115
MM a[80:83],a[164:167],v[20:23],a[80:83]
RD a[252:255],v227 offset:12288
CV v85,v114,v115
MM a[88:91],a[164:167],v[28:31],a[88:91]
EX v116,v116
MM a[88:91],a[160:163],v[24:27],a[88:91]
buffer_load_dwordx4 v235, s[12:15], s81 offen offset:2048 lds
MM a[92:95],a[168:171],v[24:27],a[92:95]
EX v117,v117
MM a[92:95],a[172:175],v[28:31],a[92:95]
EX v118,v118
MM a[84:87],a[172:175],v[20:23],a[84:87]
buffer_load_dwordx4 v235, s[12:15], s64 offen offset:3072 lds
MM a[84:87],a[168:171],v[16:19],a[84:87]
EX v119,v119
MM a[76:79],a[168:171],v[8:11],a[76:79]
EX v120,v120
MM a[76:79],a[172:175],v[12:15],a[76:79]
EX v121,v121
MM a[68:71],a[172:175],v[4:7],a[68:71]
EX v122,v122
MM a[68:71],a[168:171],v[0:3],a[68:71]
EX v123,v123
MM a[96:99],a[176:179],v[0:3],a[96:99]
EX v124,v124
MM a[96:99],a[180:183],v[4:7],a[96:99]
EX v125,v125
MM a[104:107],a[180:183],v[12:15],a[104:107]
EX v126,v126
MM a[104:107],a[176:179],v[8:11],a[104:107]
EX v127,v127
MM a[112:115],a[176:179],v[16:19],a[112:115]
CV v86,v116,v117
CV v87,v118,v119
MM a[112:115],a[180:183],v[20:23],a[112:115]
CV v92,v120,v121
CV v93,v122,v123
MM a[120:123],a[180:183],v[28:31],a[120:123]
CV v94,v124,v125
CV v95,v126,v127
MM a[120:123],a[176:179],v[24:27],a[120:123]
MM a[124:127],a[184:187],v[24:27],a[124:127]
MM a[124:127],a[188:191],v[28:31],a[124:127]
MM a[116:119],a[188:191],v[20:23],a[116:119]
MM a[116:119],a[184:187],v[16:19],a[116:119]
MM a[108:111],a[184:187],v[8:11],a[108:111]
MM a[108:111],a[188:191],v[12:15],a[108:111]
MM a[100:103],a[188:191],v[4:7],a[100:103]
MM a[100:103],a[184:187],v[0:3],a[100:103]
v_smfmac_f32_16x16x64_bf16 v[236:239], v[144:147], v[64:71], v234
v_smfmac_f32_16x16x64_bf16 v[236:239], v[148:151], v[72:79], v234
v_smfmac_f32_16x16x64_bf16 v[236:239], v[152:155], v[80:87], v234
v_smfmac_f32_16x16x64_bf16 v[236:239], v[156:159], v[88:95], v234
s_waitcnt lgkmcnt(0)
MM v[0:3],a[192:195],v[160:163],v[128:131]
s_add_u32 s64, s64, 16384
TR a[128:129],v232 offset:49152
s_add_u32 s81, s64, 896
MM v[0:3],a[196:199],v[176:179],v[0:3]
TR a[130:131],v232 offset:53248
TR a[132:133],v232 offset:57344
MM v[0:3],a[200:203],v[192:195],v[0:3]
TR a[134:135],v232 offset:61440
TR a[136:137],v233 offset:49152
MM v[0:3],a[204:207],v[208:211],v[0:3]
TR a[138:139],v233 offset:53248
v_add_f32 v247, v236, v237
MM v[4:7],a[220:223],v[208:211],v[128:131]
TR a[140:141],v233 offset:57344
v_cmp_nge_f32 vcc, s71, v247
MM v[4:7],a[216:219],v[192:195],v[4:7]
s_cbranch_vccnz LSITE15_%=
LBACK15_%=:
MM v[4:7],a[212:215],v[176:179],v[4:7]
s_add_u32 s67, s95, 11264
TR a[142:143],v233 offset:61440
TR a[144:145],v232 offset:49664
MM v[4:7],a[208:211],v[160:163],v[4:7]
buffer_store_dwordx4 v[240:243], v248, s[20:23], s67 offen offset:0 sc1
MM v[12:15],a[208:211],v[164:167],v[132:135]
EX v0,v0
MM v[12:15],a[212:215],v[180:183],v[12:15]
EX v1,v1
MM v[12:15],a[216:219],v[196:199],v[12:15]
TR a[146:147],v232 offset:53760
CV v0,v0,v1
MM v[12:15],a[220:223],v[212:215],v[12:15]
EX v2,v2
MM v[8:11],a[204:207],v[212:215],v[132:135]
EX v3,v3
MM v[8:11],a[200:203],v[196:199],v[8:11]
TR a[148:149],v232 offset:57856
CV v1,v2,v3
MM v[8:11],a[196:199],v[180:183],v[8:11]
EX v4,v4
MM v[8:11],a[192:195],v[164:167],v[8:11]
TR a[150:151],v232 offset:61952
TR a[152:153],v233 offset:49664
MM v[16:19],a[192:195],v[168:171],v[136:139]
EX v5,v5
MM v[16:19],a[196:199],v[184:187],v[16:19]
TR a[154:155],v233 offset:53760
CV v2,v4,v5
MM v[16:19],a[200:203],v[200:203],v[16:19]
EX v6,v6
MM v[16:19],a[204:207],v[216:219],v[16:19]
EX v7,v7
MM v[20:23],a[220:223],v[216:219],v[136:139]
TR a[156:157],v233 offset:57856
CV v3,v6,v7
MM v[20:23],a[216:219],v[200:203],v[20:23]
EX v8,v8
MM v[20:23],a[212:215],v[184:187],v[20:23]
TR a[158:159],v233 offset:61952
TR a[160:161],v232 offset:50176
MM v[20:23],a[208:211],v[168:171],v[20:23]
EX v9,v9
MM v[28:31],a[208:211],v[172:175],v[140:143]
TR a[162:163],v232 offset:54272
CV v8,v8,v9
MM v[28:31],a[212:215],v[188:191],v[28:31]
EX v10,v10
MM v[28:31],a[216:219],v[204:207],v[28:31]
EX v11,v11
MM v[28:31],a[220:223],v[220:223],v[28:31]
TR a[164:165],v232 offset:58368
CV v9,v10,v11
MM v[24:27],a[204:207],v[220:223],v[140:143]
EX v12,v12
MM v[24:27],a[200:203],v[204:207],v[24:27]
TR a[166:167],v232 offset:62464
TR a[168:169],v233 offset:50176
MM v[24:27],a[196:199],v[188:191],v[24:27]
EX v13,v13
MM v[24:27],a[192:195],v[172:175],v[24:27]
TR a[170:171],v233 offset:54272
CV v10,v12,v13
MM v[32:35],a[224:227],v[160:163],v[128:131]
EX v14,v14
MM v[32:35],a[228:231],v[176:179],v[32:35]
EX v15,v15
MM v[32:35],a[232:235],v[192:195],v[32:35]
TR a[172:173],v233 offset:58368
CV v11,v14,v15
MM v[32:35],a[236:239],v[208:211],v[32:35]
TR a[174:175],v233 offset:62464
TR a[176:177],v232 offset:50688
MM v[36:39],a[252:255],v[208:211],v[128:131]
EX v16,v16
MM v[36:39],a[248:251],v[192:195],v[36:39]
EX v17,v17
MM v[36:39],a[244:247],v[176:179],v[36:39]
TR a[178:179],v232 offset:54784
CV v16,v16,v17
MM v[36:39],a[240:243],v[160:163],v[36:39]
EX v18,v18
MM v[44:47],a[240:243],v[164:167],v[132:135]
s_add_u32 s67, s95, 12288
TR a[180:181],v232 offset:58880
TR a[182:183],v232 offset:62976
MM v[44:47],a[244:247],v[180:183],v[44:47]
buffer_store_dwordx4 v[240:243], v248, s[20:23], s67 offen offset:0 sc1
MM v[44:47],a[248:251],v[196:199],v[44:47]
EX v19,v19
MM v[44:47],a[252:255],v[212:215],v[44:47]
TR a[184:185],v233 offset:50688
CV v17,v18,v19
MM v[40:43],a[236:239],v[212:215],v[132:135]
EX v20,v20
MM v[40:43],a[232:235],v[196:199],v[40:43]
EX v21,v21
MM v[40:43],a[228:231],v[180:183],v[40:43]
TR a[186:187],v233 offset:54784
CV v18,v20,v21
MM v[40:43],a[224:227],v[164:167],v[40:43]
EX v22,v22
MM v[48:51],a[224:227],v[168:171],v[136:139]
TR a[188:189],v233 offset:58880
TR a[190:191],v233 offset:62976
MM v[48:51],a[228:231],v[184:187],v[48:51]
EX v23,v23
MM v[48:51],a[232:235],v[200:203],v[48:51]
CV v19,v22,v23
MM v[48:51],a[236:239],v[216:219],v[48:51]
EX v24,v24
MM v[52:55],a[252:255],v[216:219],v[136:139]
EX v25,v25
MM v[52:55],a[248:251],v[200:203],v[52:55]
EX v26,v26
MM v[52:55],a[244:247],v[184:187],v[52:55]
EX v27,v27
MM v[52:55],a[240:243],v[168:171],v[52:55]
EX v28,v28
MM v[60:63],a[240:243],v[172:175],v[140:143]
EX v29,v29
MM v[60:63],a[244:247],v[188:191],v[60:63]
EX v30,v30
MM v[60:63],a[248:251],v[204:207],v[60:63]
EX v31,v31
MM v[60:63],a[252:255],v[220:223],v[60:63]
EX v32,v32
MM v[56:59],a[236:239],v[220:223],v[140:143]
EX v33,v33
MM v[56:59],a[232:235],v[204:207],v[56:59]
EX v34,v34
MM v[56:59],a[228:231],v[188:191],v[56:59]
EX v35,v35
MM v[56:59],a[224:227],v[172:175],v[56:59]
s_waitcnt vmcnt(17) lgkmcnt(0)
s_barrier
MM a[0:3],a[128:131],v[64:67],a[0:3]
s_lshr_b32 s91, s48, 7
CV v24,v24,v25
CV v25,v26,v27
MM a[0:3],a[132:135],v[68:71],a[0:3]
s_lshl_b32 s91, s91, 5
RD a[192:195],v224 offset:16384
CV v26,v28,v29
MM a[8:11],a[132:135],v[76:79],a[8:11]
s_and_b32 s77, s48, 16
s_add_u32 m0, s65, 49152
CV v27,v30,v31
MM a[8:11],a[128:131],v[72:75],a[8:11]
buffer_load_dwordx4 v228, s[8:11], s64 offen offset:0 lds
MM a[16:19],a[128:131],v[80:83],a[16:19]
s_add_u32 s91, s91, s77
RD a[196:199],v225 offset:16384
CV v4,v32,v33
MM a[16:19],a[132:135],v[84:87],a[16:19]
s_bfe_u32 s77, s48, 131077
RD a[200:203],v226 offset:16384
CV v5,v34,v35
MM a[24:27],a[132:135],v[92:95],a[24:27]
s_lshl_b32 s77, s77, 2
RD a[204:207],v227 offset:16384
MM a[24:27],a[128:131],v[88:91],a[24:27]
buffer_load_dwordx4 v229, s[8:11], s64 offen offset:1024 lds
MM a[28:31],a[136:139],v[88:91],a[28:31]
s_add_u32 s91, s91, s77
RD a[208:211],v224 offset:20480
MM a[28:31],a[140:143],v[92:95],a[28:31]
s_add_u32 s91, s91, %[swave]
EX v36,v36
MM a[20:23],a[140:143],v[84:87],a[20:23]
buffer_load_dwordx4 v230, s[8:11], s64 offen offset:2048 lds
MM a[20:23],a[136:139],v[80:83],a[20:23]
s_lshl_b32 s91, s91, 2
EX v37,v37
MM a[12:15],a[136:139],v[72:75],a[12:15]
s_add_u32 s91, s91, 77824
RD a[212:215],v225 offset:20480
CV v6,v36,v37
MM a[12:15],a[140:143],v[76:79],a[12:15]
s_waitcnt vmcnt(18)
v_mov_b32 v250, s69
s_mov_b64 exec, 1
buffer_store_dword v250, off, s[20:23], s91 offset:0 sc1
s_mov_b64 exec, -1
MM a[4:7],a[140:143],v[68:71],a[4:7]
EX v38,v38
MM a[4:7],a[136:139],v[64:67],a[4:7]
EX v39,v39
MM a[32:35],a[144:147],v[64:67],a[32:35]
RD a[216:219],v226 offset:20480
CV v7,v38,v39
MM a[32:35],a[148:151],v[68:71],a[32:35]
EX v40,v40
MM a[40:43],a[148:151],v[76:79],a[40:43]
RD a[220:223],v227 offset:20480
RD a[224:227],v224 offset:24576
MM a[40:43],a[144:147],v[72:75],a[40:43]
EX v41,v41
MM a[48:51],a[144:147],v[80:83],a[48:51]
RD a[228:231],v225 offset:24576
CV v12,v40,v41
MM a[48:51],a[148:151],v[84:87],a[48:51]
EX v42,v42
MM a[56:59],a[148:151],v[92:95],a[56:59]
buffer_load_dwordx4 v231, s[8:11], s64 offen offset:3072 lds
MM a[56:59],a[144:147],v[88:91],a[56:59]
s_add_u32 m0, s65, 114688
EX v43,v43
MM a[60:63],a[152:155],v[88:91],a[60:63]
RD a[232:235],v226 offset:24576
CV v13,v42,v43
MM a[60:63],a[156:159],v[92:95],a[60:63]
EX v44,v44
MM a[52:55],a[156:159],v[84:87],a[52:55]
EX v45,v45
MM a[52:55],a[152:155],v[80:83],a[52:55]
RD a[236:239],v227 offset:24576
CV v14,v44,v45
MM a[44:47],a[152:155],v[72:75],a[44:47]
EX v46,v46
MM a[44:47],a[156:159],v[76:79],a[44:47]
buffer_load_dwordx4 v235, s[12:15], s81 offen offset:0 lds
MM a[36:39],a[156:159],v[68:71],a[36:39]
EX v47,v47
MM a[36:39],a[152:155],v[64:67],a[36:39]
RD a[240:243],v224 offset:28672
CV v15,v46,v47
MM a[64:67],a[160:163],v[64:67],a[64:67]
EX v48,v48
MM a[64:67],a[164:167],v[68:71],a[64:67]
EX v49,v49
MM a[72:75],a[164:167],v[76:79],a[72:75]
RD a[244:247],v225 offset:28672
CV v20,v48,v49
MM a[72:75],a[160:163],v[72:75],a[72:75]
buffer_load_dwordx4 v235, s[12:15], s64 offen offset:1024 lds
)ASM"
R"ASM(
MM a[80:83],a[160:163],v[80:83],a[80:83]
EX v50,v50
MM a[80:83],a[164:167],v[84:87],a[80:83]
EX v51,v51
MM a[88:91],a[164:167],v[92:95],a[88:91]
RD a[248:251],v226 offset:28672
CV v21,v50,v51
MM a[88:91],a[160:163],v[88:91],a[88:91]
EX v52,v52
MM a[92:95],a[168:171],v[88:91],a[92:95]
EX v53,v53
MM a[92:95],a[172:175],v[92:95],a[92:95]
RD a[252:255],v227 offset:28672
CV v22,v52,v53
MM a[84:87],a[172:175],v[84:87],a[84:87]
buffer_load_dwordx4 v235, s[12:15], s81 offen offset:2048 lds
MM a[84:87],a[168:171],v[80:83],a[84:87]
EX v54,v54
MM a[76:79],a[168:171],v[72:75],a[76:79]
EX v55,v55
MM a[76:79],a[172:175],v[76:79],a[76:79]
CV v23,v54,v55
MM a[68:71],a[172:175],v[68:71],a[68:71]
EX v56,v56
MM a[68:71],a[168:171],v[64:67],a[68:71]
EX v57,v57
MM a[96:99],a[176:179],v[64:67],a[96:99]
buffer_load_dwordx4 v235, s[12:15], s64 offen offset:3072 lds
MM a[96:99],a[180:183],v[68:71],a[96:99]
EX v58,v58
MM a[104:107],a[180:183],v[76:79],a[104:107]
EX v59,v59
MM a[104:107],a[176:179],v[72:75],a[104:107]
EX v60,v60
MM a[112:115],a[176:179],v[80:83],a[112:115]
EX v61,v61
MM a[112:115],a[180:183],v[84:87],a[112:115]
EX v62,v62
MM a[120:123],a[180:183],v[92:95],a[120:123]
EX v63,v63
MM a[120:123],a[176:179],v[88:91],a[120:123]
CV v28,v56,v57
CV v29,v58,v59
MM a[124:127],a[184:187],v[88:91],a[124:127]
CV v30,v60,v61
CV v31,v62,v63
MM a[124:127],a[188:191],v[92:95],a[124:127]
MM a[116:119],a[188:191],v[84:87],a[116:119]
MM a[116:119],a[184:187],v[80:83],a[116:119]
MM a[108:111],a[184:187],v[72:75],a[108:111]
MM a[108:111],a[188:191],v[76:79],a[108:111]
MM a[100:103],a[188:191],v[68:71],a[100:103]
MM a[100:103],a[184:187],v[64:67],a[100:103]
v_smfmac_f32_16x16x64_bf16 v[236:239], v[144:147], v[0:7], v234
v_smfmac_f32_16x16x64_bf16 v[236:239], v[148:151], v[8:15], v234
v_smfmac_f32_16x16x64_bf16 v[236:239], v[152:155], v[16:23], v234
v_smfmac_f32_16x16x64_bf16 v[236:239], v[156:159], v[24:31], v234
s_waitcnt lgkmcnt(0)
MM v[64:67],a[192:195],v[160:163],v[128:131]
s_add_u32 s64, s64, 16384
TR a[128:129],v232 offset:0
s_add_u32 s81, s64, 896
MM v[64:67],a[196:199],v[176:179],v[64:67]
TR a[130:131],v232 offset:4096
TR a[132:133],v232 offset:8192
MM v[64:67],a[200:203],v[192:195],v[64:67]
TR a[134:135],v232 offset:12288
TR a[136:137],v233 offset:0
MM v[64:67],a[204:207],v[208:211],v[64:67]
TR a[138:139],v233 offset:4096
v_add_f32 v246, v236, v237
MM v[68:71],a[220:223],v[208:211],v[128:131]
TR a[140:141],v233 offset:8192
v_cmp_nge_f32 vcc, s71, v246
MM v[68:71],a[216:219],v[192:195],v[68:71]
s_cbranch_vccnz LSITE16_%=
LBACK16_%=:
MM v[68:71],a[212:215],v[176:179],v[68:71]
s_add_u32 s67, s95, 13312
TR a[142:143],v233 offset:12288
TR a[144:145],v232 offset:512
MM v[68:71],a[208:211],v[160:163],v[68:71]
buffer_store_dwordx4 v[240:243], v248, s[20:23], s67 offen offset:0 sc1
MM v[76:79],a[208:211],v[164:167],v[132:135]
EX v64,v64
MM v[76:79],a[212:215],v[180:183],v[76:79]
EX v65,v65
MM v[76:79],a[216:219],v[196:199],v[76:79]
TR a[146:147],v232 offset:4608
CV v64,v64,v65
MM v[76:79],a[220:223],v[212:215],v[76:79]
EX v66,v66
MM v[72:75],a[204:207],v[212:215],v[132:135]
EX v67,v67
MM v[72:75],a[200:203],v[196:199],v[72:75]
TR a[148:149],v232 offset:8704
CV v65,v66,v67
MM v[72:75],a[196:199],v[180:183],v[72:75]
EX v68,v68
MM v[72:75],a[192:195],v[164:167],v[72:75]
TR a[150:151],v232 offset:12800
TR a[152:153],v233 offset:512
MM v[80:83],a[192:195],v[168:171],v[136:139]
EX v69,v69
MM v[80:83],a[196:199],v[184:187],v[80:83]
TR a[154:155],v233 offset:4608
CV v66,v68,v69
MM v[80:83],a[200:203],v[200:203],v[80:83]
EX v70,v70
MM v[80:83],a[204:207],v[216:219],v[80:83]
EX v71,v71
MM v[84:87],a[220:223],v[216:219],v[136:139]
TR a[156:157],v233 offset:8704
CV v67,v70,v71
MM v[84:87],a[216:219],v[200:203],v[84:87]
EX v72,v72
MM v[84:87],a[212:215],v[184:187],v[84:87]
TR a[158:159],v233 offset:12800
TR a[160:161],v232 offset:1024
MM v[84:87],a[208:211],v[168:171],v[84:87]
EX v73,v73
MM v[92:95],a[208:211],v[172:175],v[140:143]
s_and_b32 s44, s50, 7
TR a[162:163],v232 offset:5120
CV v72,v72,v73
MM v[92:95],a[212:215],v[188:191],v[92:95]
s_lshl_b32 s44, s44, 2
EX v74,v74
MM v[92:95],a[216:219],v[204:207],v[92:95]
s_add_u32 s44, s44, s84
EX v75,v75
MM v[92:95],a[220:223],v[220:223],v[92:95]
s_lshl_b32 s44, s44, 2
TR a[164:165],v232 offset:9216
CV v73,v74,v75
MM v[88:91],a[204:207],v[220:223],v[140:143]
s_add_u32 s44, s44, %[swave]
EX v76,v76
MM v[88:91],a[200:203],v[204:207],v[88:91]
s_mul_i32 s44, s44, 5120
TR a[166:167],v232 offset:13312
TR a[168:169],v233 offset:1024
MM v[88:91],a[196:199],v[188:191],v[88:91]
s_mul_i32 s45, s84, 320
EX v77,v77
MM v[88:91],a[192:195],v[172:175],v[88:91]
s_sub_u32 s44, s44, s45
TR a[170:171],v233 offset:5120
CV v74,v76,v77
MM v[96:99],a[224:227],v[160:163],v[128:131]
s_lshl_b32 s45, %[swave], 10
EX v78,v78
MM v[96:99],a[228:231],v[176:179],v[96:99]
s_add_u32 s45, s45, %[slds]
s_add_u32 m0, s45, 147456
s_add_u32 s44, s44, 0
buffer_load_dwordx4 v248, s[24:27], s44 offen offset:0 lds
MM v[96:99],a[232:235],v[192:195],v[96:99]
EX v79,v79
MM v[96:99],a[236:239],v[208:211],v[96:99]
TR a[172:173],v233 offset:9216
CV v75,v78,v79
MM v[100:103],a[252:255],v[208:211],v[128:131]
EX v80,v80
MM v[100:103],a[248:251],v[192:195],v[100:103]
TR a[174:175],v233 offset:13312
TR a[176:177],v232 offset:1536
MM v[100:103],a[244:247],v[176:179],v[100:103]
EX v81,v81
MM v[100:103],a[240:243],v[160:163],v[100:103]
TR a[178:179],v232 offset:5632
CV v80,v80,v81
MM v[108:111],a[240:243],v[164:167],v[132:135]
s_add_u32 s67, s95, 14336
EX v82,v82
MM v[108:111],a[244:247],v[180:183],v[108:111]
buffer_store_dwordx4 v[240:243], v248, s[20:23], s67 offen offset:0 sc1
MM v[108:111],a[248:251],v[196:199],v[108:111]
EX v83,v83
MM v[108:111],a[252:255],v[212:215],v[108:111]
TR a[180:181],v232 offset:9728
CV v81,v82,v83
MM v[104:107],a[236:239],v[212:215],v[132:135]
EX v84,v84
MM v[104:107],a[232:235],v[196:199],v[104:107]
TR a[182:183],v232 offset:13824
TR a[184:185],v233 offset:1536
MM v[104:107],a[228:231],v[180:183],v[104:107]
EX v85,v85
MM v[104:107],a[224:227],v[164:167],v[104:107]
TR a[186:187],v233 offset:5632
CV v82,v84,v85
MM v[112:115],a[224:227],v[168:171],v[136:139]
EX v86,v86
MM v[112:115],a[228:231],v[184:187],v[112:115]
EX v87,v87
MM v[112:115],a[232:235],v[200:203],v[112:115]
TR a[188:189],v233 offset:9728
CV v83,v86,v87
MM v[112:115],a[236:239],v[216:219],v[112:115]
EX v88,v88
MM v[116:119],a[252:255],v[216:219],v[136:139]
EX v89,v89
MM v[116:119],a[248:251],v[200:203],v[116:119]
TR a[190:191],v233 offset:13824
CV v88,v88,v89
MM v[116:119],a[244:247],v[184:187],v[116:119]
EX v90,v90
MM v[116:119],a[240:243],v[168:171],v[116:119]
EX v91,v91
MM v[124:127],a[240:243],v[172:175],v[140:143]
EX v92,v92
MM v[124:127],a[244:247],v[188:191],v[124:127]
EX v93,v93
MM v[124:127],a[248:251],v[204:207],v[124:127]
EX v94,v94
MM v[124:127],a[252:255],v[220:223],v[124:127]
EX v95,v95
MM v[120:123],a[236:239],v[220:223],v[140:143]
EX v96,v96
MM v[120:123],a[232:235],v[204:207],v[120:123]
LPG11_%=:
s_cbranch_execz LTR0_%=
EX v97,v97
MM v[120:123],a[228:231],v[188:191],v[120:123]
EX v98,v98
MM v[120:123],a[224:227],v[172:175],v[120:123]
s_waitcnt vmcnt(18) lgkmcnt(0)
s_barrier
MM a[0:3],a[128:131],v[0:3],a[0:3]
s_add_u32 m0, s65, 0
CV v89,v90,v91
CV v90,v92,v93
MM a[0:3],a[132:135],v[4:7],a[0:3]
RD a[192:195],v224 offset:32768
CV v91,v94,v95
MM a[8:11],a[132:135],v[12:15],a[8:11]
buffer_load_dwordx4 v228, s[8:11], s64 offen offset:0 lds
MM a[8:11],a[128:131],v[8:11],a[8:11]
RD a[196:199],v225 offset:32768
CV v68,v96,v97
MM a[16:19],a[128:131],v[16:19],a[16:19]
RD a[200:203],v226 offset:32768
RD a[204:207],v227 offset:32768
MM a[16:19],a[132:135],v[20:23],a[16:19]
buffer_load_dwordx4 v229, s[8:11], s64 offen offset:1024 lds
MM a[24:27],a[132:135],v[28:31],a[24:27]
RD a[208:211],v224 offset:36864
RD a[212:215],v225 offset:36864
MM a[24:27],a[128:131],v[24:27],a[24:27]
EX v99,v99
MM a[28:31],a[136:139],v[24:27],a[28:31]
RD a[216:219],v226 offset:36864
CV v69,v98,v99
MM a[28:31],a[140:143],v[28:31],a[28:31]
EX v100,v100
MM a[20:23],a[140:143],v[20:23],a[20:23]
buffer_load_dwordx4 v230, s[8:11], s64 offen offset:2048 lds
MM a[20:23],a[136:139],v[16:19],a[20:23]
EX v101,v101
MM a[12:15],a[136:139],v[8:11],a[12:15]
RD a[220:223],v227 offset:36864
CV v70,v100,v101
MM a[12:15],a[140:143],v[12:15],a[12:15]
EX v102,v102
MM a[4:7],a[140:143],v[4:7],a[4:7]
EX v103,v103
MM a[4:7],a[136:139],v[0:3],a[4:7]
RD a[224:227],v224 offset:40960
CV v71,v102,v103
MM a[32:35],a[144:147],v[0:3],a[32:35]
EX v104,v104
MM a[32:35],a[148:151],v[4:7],a[32:35]
buffer_load_dwordx4 v231, s[8:11], s64 offen offset:3072 lds
MM a[40:43],a[148:151],v[12:15],a[40:43]
s_add_u32 m0, s65, 65536
EX v105,v105
MM a[40:43],a[144:147],v[8:11],a[40:43]
RD a[228:231],v225 offset:40960
CV v76,v104,v105
MM a[48:51],a[144:147],v[16:19],a[48:51]
EX v106,v106
MM a[48:51],a[148:151],v[20:23],a[48:51]
EX v107,v107
MM a[56:59],a[148:151],v[28:31],a[56:59]
RD a[232:235],v226 offset:40960
CV v77,v106,v107
MM a[56:59],a[144:147],v[24:27],a[56:59]
EX v108,v108
MM a[60:63],a[152:155],v[24:27],a[60:63]
buffer_load_dwordx4 v235, s[12:15], s81 offen offset:0 lds
MM a[60:63],a[156:159],v[28:31],a[60:63]
EX v109,v109
MM a[52:55],a[156:159],v[20:23],a[52:55]
RD a[236:239],v227 offset:40960
CV v78,v108,v109
MM a[52:55],a[152:155],v[16:19],a[52:55]
EX v110,v110
MM a[44:47],a[152:155],v[8:11],a[44:47]
EX v111,v111
MM a[44:47],a[156:159],v[12:15],a[44:47]
RD a[240:243],v224 offset:45056
CV v79,v110,v111
MM a[36:39],a[156:159],v[4:7],a[36:39]
buffer_load_dwordx4 v235, s[12:15], s64 offen offset:1024 lds
MM a[36:39],a[152:155],v[0:3],a[36:39]
EX v112,v112
MM a[64:67],a[160:163],v[0:3],a[64:67]
EX v113,v113
MM a[64:67],a[164:167],v[4:7],a[64:67]
RD a[244:247],v225 offset:45056
CV v84,v112,v113
MM a[72:75],a[164:167],v[12:15],a[72:75]
EX v114,v114
MM a[72:75],a[160:163],v[8:11],a[72:75]
EX v115,v115
MM a[80:83],a[160:163],v[16:19],a[80:83]
RD a[248:251],v226 offset:45056
CV v85,v114,v115
MM a[80:83],a[164:167],v[20:23],a[80:83]
buffer_load_dwordx4 v235, s[12:15], s81 offen offset:2048 lds
MM a[88:91],a[164:167],v[28:31],a[88:91]
EX v116,v116
MM a[88:91],a[160:163],v[24:27],a[88:91]
EX v117,v117
MM a[92:95],a[168:171],v[24:27],a[92:95]
RD a[252:255],v227 offset:45056
CV v86,v116,v117
MM a[92:95],a[172:175],v[28:31],a[92:95]
EX v118,v118
MM a[84:87],a[172:175],v[20:23],a[84:87]
buffer_load_dwordx4 v235, s[12:15], s64 offen offset:3072 lds
MM a[84:87],a[168:171],v[16:19],a[84:87]
EX v119,v119
MM a[76:79],a[168:171],v[8:11],a[76:79]
CV v87,v118,v119
MM a[76:79],a[172:175],v[12:15],a[76:79]
EX v120,v120
MM a[68:71],a[172:175],v[4:7],a[68:71]
EX v121,v121
MM a[68:71],a[168:171],v[0:3],a[68:71]
EX v122,v122
MM a[96:99],a[176:179],v[0:3],a[96:99]
EX v123,v123
MM a[96:99],a[180:183],v[4:7],a[96:99]
EX v124,v124
MM a[104:107],a[180:183],v[12:15],a[104:107]
EX v125,v125
MM a[104:107],a[176:179],v[8:11],a[104:107]
EX v126,v126
MM a[112:115],a[176:179],v[16:19],a[112:115]
EX v127,v127
MM a[112:115],a[180:183],v[20:23],a[112:115]
CV v92,v120,v121
CV v93,v122,v123
MM a[120:123],a[180:183],v[28:31],a[120:123]
CV v94,v124,v125
CV v95,v126,v127
MM a[120:123],a[176:179],v[24:27],a[120:123]
MM a[124:127],a[184:187],v[24:27],a[124:127]
MM a[124:127],a[188:191],v[28:31],a[124:127]
MM a[116:119],a[188:191],v[20:23],a[116:119]
MM a[116:119],a[184:187],v[16:19],a[116:119]
MM a[108:111],a[184:187],v[8:11],a[108:111]
MM a[108:111],a[188:191],v[12:15],a[108:111]
MM a[100:103],a[188:191],v[4:7],a[100:103]
MM a[100:103],a[184:187],v[0:3],a[100:103]
v_smfmac_f32_16x16x64_bf16 v[236:239], v[144:147], v[64:71], v234
v_smfmac_f32_16x16x64_bf16 v[236:239], v[148:151], v[72:79], v234
v_smfmac_f32_16x16x64_bf16 v[236:239], v[152:155], v[80:87], v234
v_smfmac_f32_16x16x64_bf16 v[236:239], v[156:159], v[88:95], v234
s_waitcnt lgkmcnt(0)
MM v[0:3],a[192:195],v[160:163],v[128:131]
s_add_u32 s64, s64, 16384
TR a[128:129],v232 offset:16384
s_add_u32 s81, s64, 896
MM v[0:3],a[196:199],v[176:179],v[0:3]
TR a[130:131],v232 offset:20480
TR a[132:133],v232 offset:24576
)ASM"
R"ASM(
MM v[0:3],a[200:203],v[192:195],v[0:3]
TR a[134:135],v232 offset:28672
TR a[136:137],v233 offset:16384
MM v[0:3],a[204:207],v[208:211],v[0:3]
TR a[138:139],v233 offset:20480
v_add_f32 v247, v236, v237
MM v[4:7],a[220:223],v[208:211],v[128:131]
TR a[140:141],v233 offset:24576
v_cmp_nge_f32 vcc, s71, v247
MM v[4:7],a[216:219],v[192:195],v[4:7]
s_cbranch_vccnz LSITE17_%=
LBACK17_%=:
MM v[4:7],a[212:215],v[176:179],v[4:7]
TR a[142:143],v233 offset:28672
TR a[144:145],v232 offset:16896
MM v[4:7],a[208:211],v[160:163],v[4:7]
TR a[146:147],v232 offset:20992
TR a[148:149],v232 offset:25088
MM v[12:15],a[208:211],v[164:167],v[132:135]
EX v0,v0
MM v[12:15],a[212:215],v[180:183],v[12:15]
EX v1,v1
MM v[12:15],a[216:219],v[196:199],v[12:15]
TR a[150:151],v232 offset:29184
CV v0,v0,v1
MM v[12:15],a[220:223],v[212:215],v[12:15]
EX v2,v2
MM v[8:11],a[204:207],v[212:215],v[132:135]
EX v3,v3
MM v[8:11],a[200:203],v[196:199],v[8:11]
TR a[152:153],v233 offset:16896
CV v1,v2,v3
MM v[8:11],a[196:199],v[180:183],v[8:11]
EX v4,v4
MM v[8:11],a[192:195],v[164:167],v[8:11]
EX v5,v5
MM v[16:19],a[192:195],v[168:171],v[136:139]
TR a[154:155],v233 offset:20992
CV v2,v4,v5
MM v[16:19],a[196:199],v[184:187],v[16:19]
EX v6,v6
MM v[16:19],a[200:203],v[200:203],v[16:19]
EX v7,v7
MM v[16:19],a[204:207],v[216:219],v[16:19]
TR a[156:157],v233 offset:25088
CV v3,v6,v7
MM v[20:23],a[220:223],v[216:219],v[136:139]
EX v8,v8
MM v[20:23],a[216:219],v[200:203],v[20:23]
TR a[158:159],v233 offset:29184
TR a[160:161],v232 offset:17408
MM v[20:23],a[212:215],v[184:187],v[20:23]
EX v9,v9
MM v[20:23],a[208:211],v[168:171],v[20:23]
TR a[162:163],v232 offset:21504
CV v8,v8,v9
MM v[28:31],a[208:211],v[172:175],v[140:143]
s_add_u32 m0, s45, 147456
s_add_u32 s44, s44, 1024
buffer_load_dwordx4 v248, s[24:27], s44 offen offset:0 lds
MM v[28:31],a[212:215],v[188:191],v[28:31]
EX v10,v10
MM v[28:31],a[216:219],v[204:207],v[28:31]
EX v11,v11
MM v[28:31],a[220:223],v[220:223],v[28:31]
TR a[164:165],v232 offset:25600
CV v9,v10,v11
MM v[24:27],a[204:207],v[220:223],v[140:143]
EX v12,v12
MM v[24:27],a[200:203],v[204:207],v[24:27]
TR a[166:167],v232 offset:29696
TR a[168:169],v233 offset:17408
MM v[24:27],a[196:199],v[188:191],v[24:27]
EX v13,v13
MM v[24:27],a[192:195],v[172:175],v[24:27]
TR a[170:171],v233 offset:21504
CV v10,v12,v13
MM v[32:35],a[224:227],v[160:163],v[128:131]
EX v14,v14
MM v[32:35],a[228:231],v[176:179],v[32:35]
EX v15,v15
MM v[32:35],a[232:235],v[192:195],v[32:35]
TR a[172:173],v233 offset:25600
CV v11,v14,v15
MM v[32:35],a[236:239],v[208:211],v[32:35]
TR a[174:175],v233 offset:29696
TR a[176:177],v232 offset:17920
MM v[36:39],a[252:255],v[208:211],v[128:131]
EX v16,v16
MM v[36:39],a[248:251],v[192:195],v[36:39]
EX v17,v17
MM v[36:39],a[244:247],v[176:179],v[36:39]
TR a[178:179],v232 offset:22016
CV v16,v16,v17
MM v[36:39],a[240:243],v[160:163],v[36:39]
EX v18,v18
MM v[44:47],a[240:243],v[164:167],v[132:135]
TR a[180:181],v232 offset:26112
TR a[182:183],v232 offset:30208
MM v[44:47],a[244:247],v[180:183],v[44:47]
EX v19,v19
MM v[44:47],a[248:251],v[196:199],v[44:47]
TR a[184:185],v233 offset:17920
CV v17,v18,v19
MM v[44:47],a[252:255],v[212:215],v[44:47]
EX v20,v20
MM v[40:43],a[236:239],v[212:215],v[132:135]
EX v21,v21
MM v[40:43],a[232:235],v[196:199],v[40:43]
TR a[186:187],v233 offset:22016
CV v18,v20,v21
MM v[40:43],a[228:231],v[180:183],v[40:43]
EX v22,v22
MM v[40:43],a[224:227],v[164:167],v[40:43]
TR a[188:189],v233 offset:26112
TR a[190:191],v233 offset:30208
MM v[48:51],a[224:227],v[168:171],v[136:139]
EX v23,v23
MM v[48:51],a[228:231],v[184:187],v[48:51]
CV v19,v22,v23
MM v[48:51],a[232:235],v[200:203],v[48:51]
EX v24,v24
MM v[48:51],a[236:239],v[216:219],v[48:51]
EX v25,v25
MM v[52:55],a[252:255],v[216:219],v[136:139]
EX v26,v26
MM v[52:55],a[248:251],v[200:203],v[52:55]
EX v27,v27
MM v[52:55],a[244:247],v[184:187],v[52:55]
EX v28,v28
MM v[52:55],a[240:243],v[168:171],v[52:55]
EX v29,v29
MM v[60:63],a[240:243],v[172:175],v[140:143]
EX v30,v30
MM v[60:63],a[244:247],v[188:191],v[60:63]
EX v31,v31
MM v[60:63],a[248:251],v[204:207],v[60:63]
EX v32,v32
MM v[60:63],a[252:255],v[220:223],v[60:63]
EX v33,v33
MM v[56:59],a[236:239],v[220:223],v[140:143]
EX v34,v34
MM v[56:59],a[232:235],v[204:207],v[56:59]
EX v35,v35
MM v[56:59],a[228:231],v[188:191],v[56:59]
EX v36,v36
MM v[56:59],a[224:227],v[172:175],v[56:59]
s_waitcnt vmcnt(16) lgkmcnt(0)
s_barrier
MM a[0:3],a[128:131],v[64:67],a[0:3]
s_lshl_b32 s60, s50, 2
CV v24,v24,v25
CV v25,v26,v27
MM a[0:3],a[132:135],v[68:71],a[0:3]
s_add_u32 s60, s60, %[swave]
RD a[192:195],v224 offset:49152
CV v26,v28,v29
MM a[8:11],a[132:135],v[76:79],a[8:11]
s_lshl_b32 s60, s60, 2
s_add_u32 m0, s65, 16384
CV v27,v30,v31
MM a[8:11],a[128:131],v[72:75],a[8:11]
buffer_load_dwordx4 v228, s[8:11], s64 offen offset:0 lds
MM a[16:19],a[128:131],v[80:83],a[16:19]
s_bfe_u32 s62, s49, 65538
RD a[196:199],v225 offset:49152
CV v4,v32,v33
MM a[16:19],a[132:135],v[84:87],a[16:19]
s_add_u32 s93, s60, s62
RD a[200:203],v226 offset:49152
CV v5,v34,v35
MM a[24:27],a[132:135],v[92:95],a[24:27]
s_lshl_b32 s93, s93, 2
RD a[204:207],v227 offset:49152
MM a[24:27],a[128:131],v[88:91],a[24:27]
buffer_load_dwordx4 v229, s[8:11], s64 offen offset:1024 lds
MM a[28:31],a[136:139],v[88:91],a[28:31]
s_lshr_b32 s61, s49, 3
RD a[208:211],v224 offset:53248
MM a[28:31],a[140:143],v[92:95],a[28:31]
RD a[212:215],v225 offset:53248
RD a[216:219],v226 offset:53248
MM a[20:23],a[140:143],v[84:87],a[20:23]
buffer_load_dwordx4 v230, s[8:11], s64 offen offset:2048 lds
MM a[20:23],a[136:139],v[80:83],a[20:23]
s_add_u32 s93, s93, s61
EX v37,v37
MM a[12:15],a[136:139],v[72:75],a[12:15]
s_mul_i32 s18, s93, 5120
CV v6,v36,v37
MM a[12:15],a[140:143],v[76:79],a[12:15]
s_mul_i32 s60, s84, 320
EX v38,v38
MM a[4:7],a[140:143],v[68:71],a[4:7]
s_add_u32 s18, s18, s60
EX v39,v39
MM a[4:7],a[136:139],v[64:67],a[4:7]
s_and_b32 s60, s84, 1
RD a[220:223],v227 offset:53248
CV v7,v38,v39
MM a[32:35],a[144:147],v[64:67],a[32:35]
EX v40,v40
MM a[32:35],a[148:151],v[68:71],a[32:35]
buffer_load_dwordx4 v231, s[8:11], s64 offen offset:3072 lds
MM a[40:43],a[148:151],v[76:79],a[40:43]
s_xor_b32 s60, s60, 1
EX v41,v41
MM a[40:43],a[144:147],v[72:75],a[40:43]
s_lshl_b32 s60, s60, 8
RD a[224:227],v224 offset:57344
CV v12,v40,v41
MM a[48:51],a[144:147],v[80:83],a[48:51]
s_add_u32 s63, s18, s60
EX v42,v42
MM a[48:51],a[148:151],v[84:87],a[48:51]
s_and_b32 s60, s84, 1
EX v43,v43
MM a[56:59],a[148:151],v[92:95],a[56:59]
s_lshl_b32 s60, s60, 6
s_add_u32 m0, s65, 81920
CV v13,v42,v43
MM a[56:59],a[144:147],v[88:91],a[56:59]
s_add_u32 s18, s18, s60
RD a[228:231],v225 offset:57344
RD a[232:235],v226 offset:57344
MM a[60:63],a[152:155],v[88:91],a[60:63]
EX v44,v44
MM a[60:63],a[156:159],v[92:95],a[60:63]
buffer_load_dwordx4 v235, s[12:15], s81 offen offset:0 lds
MM a[52:55],a[156:159],v[84:87],a[52:55]
s_lshl_b32 s60, s50, 2
EX v45,v45
MM a[52:55],a[152:155],v[80:83],a[52:55]
s_add_u32 s60, s60, s84
RD a[236:239],v227 offset:57344
CV v14,v44,v45
MM a[44:47],a[152:155],v[72:75],a[44:47]
s_mul_i32 s16, s60, 663552
EX v46,v46
MM a[44:47],a[156:159],v[76:79],a[44:47]
s_lshl_b32 s62, s62, 2
EX v47,v47
MM a[36:39],a[156:159],v[68:71],a[36:39]
s_add_u32 s62, s62, s61
CV v15,v46,v47
MM a[36:39],a[152:155],v[64:67],a[36:39]
EX v48,v48
MM a[64:67],a[160:163],v[64:67],a[64:67]
buffer_load_dwordx4 v235, s[12:15], s64 offen offset:1024 lds
MM a[64:67],a[164:167],v[68:71],a[64:67]
s_lshl_b32 s62, s62, 8
EX v49,v49
MM a[72:75],a[164:167],v[76:79],a[72:75]
s_add_u32 s16, s16, s62
RD a[240:243],v224 offset:61440
CV v20,v48,v49
MM a[72:75],a[160:163],v[72:75],a[72:75]
s_mul_i32 s60, %[swave], 20480
EX v50,v50
MM a[80:83],a[160:163],v[80:83],a[80:83]
s_add_u32 s16, s16, s60
EX v51,v51
MM a[80:83],a[164:167],v[84:87],a[80:83]
s_lshr_b32 s61, s49, 4
RD a[244:247],v225 offset:61440
CV v21,v50,v51
MM a[88:91],a[164:167],v[92:95],a[88:91]
s_mul_i32 s61, s61, 331776
EX v52,v52
MM a[88:91],a[160:163],v[88:91],a[88:91]
s_add_u32 s16, s16, s61
RD a[248:251],v226 offset:61440
MM a[92:95],a[168:171],v[88:91],a[92:95]
EX v53,v53
MM a[92:95],a[172:175],v[92:95],a[92:95]
RD a[252:255],v227 offset:61440
CV v22,v52,v53
MM a[84:87],a[172:175],v[84:87],a[84:87]
buffer_load_dwordx4 v235, s[12:15], s81 offen offset:2048 lds
MM a[84:87],a[168:171],v[80:83],a[84:87]
s_add_u32 s16, s16, 22708224
EX v54,v54
MM a[76:79],a[168:171],v[72:75],a[76:79]
s_mov_b32 s17, 31
EX v55,v55
MM a[76:79],a[172:175],v[76:79],a[76:79]
s_mov_b32 s72, 0
CV v23,v54,v55
MM a[68:71],a[172:175],v[68:71],a[68:71]
EX v56,v56
MM a[68:71],a[168:171],v[64:67],a[68:71]
EX v57,v57
MM a[96:99],a[176:179],v[64:67],a[96:99]
buffer_load_dwordx4 v235, s[12:15], s64 offen offset:3072 lds
MM a[96:99],a[180:183],v[68:71],a[96:99]
s_lshl_b32 s60, s50, 2
EX v58,v58
MM a[104:107],a[180:183],v[76:79],a[104:107]
s_add_u32 s60, s60, s84
EX v59,v59
MM a[104:107],a[176:179],v[72:75],a[104:107]
s_lshl_b32 s60, s60, 2
EX v60,v60
MM a[112:115],a[176:179],v[80:83],a[112:115]
s_add_u32 s60, s60, %[swave]
EX v61,v61
MM a[112:115],a[180:183],v[84:87],a[112:115]
s_lshl_b32 s60, s60, 5
EX v62,v62
MM a[120:123],a[180:183],v[92:95],a[120:123]
s_and_b32 s61, s49, 28
EX v63,v63
MM a[120:123],a[176:179],v[88:91],a[120:123]
s_add_u32 s19, s60, s61
CV v28,v56,v57
CV v29,v58,v59
MM a[124:127],a[184:187],v[88:91],a[124:127]
s_lshl_b32 s19, s19, 2
CV v30,v60,v61
CV v31,v62,v63
MM a[124:127],a[188:191],v[92:95],a[124:127]
s_add_u32 s19, s19, 16384
s_and_b32 s68, s19, 0xffffffc0
s_mov_b32 s82, 0
MM a[116:119],a[188:191],v[84:87],a[116:119]
s_lshr_b32 s61, s52, 2
s_sub_u32 s61, s61, s93
MM a[116:119],a[184:187],v[80:83],a[116:119]
s_cmp_eq_u32 s61, 0
s_cselect_b32 s60, 1, 0
s_or_b32 s82, s82, s60
MM a[108:111],a[184:187],v[72:75],a[108:111]
s_cmp_eq_u32 s61, 8
s_cselect_b32 s60, 256, 0
s_or_b32 s82, s82, s60
MM a[108:111],a[188:191],v[76:79],a[108:111]
s_lshr_b32 s61, s53, 2
s_sub_u32 s61, s61, s93
MM a[100:103],a[188:191],v[68:71],a[100:103]
s_cmp_eq_u32 s61, 0
s_cselect_b32 s60, 2, 0
s_or_b32 s82, s82, s60
MM a[100:103],a[184:187],v[64:67],a[100:103]
s_cmp_eq_u32 s61, 8
s_cselect_b32 s60, 512, 0
s_or_b32 s82, s82, s60
v_smfmac_f32_16x16x64_bf16 v[236:239], v[144:147], v[0:7], v234
s_lshr_b32 s61, s54, 2
s_sub_u32 s61, s61, s93
v_smfmac_f32_16x16x64_bf16 v[236:239], v[148:151], v[8:15], v234
s_cmp_eq_u32 s61, 0
s_cselect_b32 s60, 4, 0
v_smfmac_f32_16x16x64_bf16 v[236:239], v[152:155], v[16:23], v234
s_or_b32 s82, s82, s60
v_smfmac_f32_16x16x64_bf16 v[236:239], v[156:159], v[24:31], v234
s_waitcnt lgkmcnt(0)
MM v[64:67],a[192:195],v[160:163],v[128:131]
s_add_u32 s64, s64, 16384
s_cmp_eq_u32 s61, 8
s_cselect_b32 s60, 1024, 0
MM v[64:67],a[196:199],v[176:179],v[64:67]
s_or_b32 s82, s82, s60
TR a[128:129],v232 offset:32768
TR a[130:131],v232 offset:36864
MM v[64:67],a[200:203],v[192:195],v[64:67]
s_lshr_b32 s61, s55, 2
s_add_u32 s81, s64, 896
TR a[132:133],v232 offset:40960
MM v[64:67],a[204:207],v[208:211],v[64:67]
s_sub_u32 s61, s61, s93
TR a[134:135],v232 offset:45056
v_add_f32 v246, v236, v237
MM v[68:71],a[220:223],v[208:211],v[128:131]
s_cmp_eq_u32 s61, 0
s_cselect_b32 s60, 8, 0
v_cmp_nge_f32 vcc, s71, v246
MM v[68:71],a[216:219],v[192:195],v[68:71]
s_cbranch_vccnz LSITE18_%=
LBACK18_%=:
MM v[68:71],a[212:215],v[176:179],v[68:71]
v_bfe_u32 v253, v248, 4, 4
v_lshlrev_b32 v253, 2, v253
MM v[68:71],a[208:211],v[160:163],v[68:71]
s_and_b32 s76, s48, 0xffffffc0
TR a[136:137],v233 offset:32768
TR a[138:139],v233 offset:36864
MM v[76:79],a[208:211],v[164:167],v[132:135]
s_add_u32 s76, s76, 77824
EX v64,v64
MM v[76:79],a[212:215],v[180:183],v[76:79]
buffer_load_dword v252, v253, s[20:23], s76 offen offset:0 sc1
MM v[76:79],a[216:219],v[196:199],v[76:79]
s_or_b32 s82, s82, s60
s_cmp_eq_u32 s61, 8
s_cselect_b32 s60, 2048, 0
MM v[76:79],a[220:223],v[212:215],v[76:79]
s_or_b32 s82, s82, s60
EX v65,v65
MM v[72:75],a[204:207],v[212:215],v[132:135]
s_lshr_b32 s61, s56, 2
TR a[140:141],v233 offset:40960
CV v64,v64,v65
MM v[72:75],a[200:203],v[196:199],v[72:75]
s_sub_u32 s61, s61, s93
)ASM"
R"ASM(
s_cmp_eq_u32 s61, 0
s_cselect_b32 s60, 16, 0
MM v[72:75],a[196:199],v[180:183],v[72:75]
s_or_b32 s82, s82, s60
s_cmp_eq_u32 s61, 8
s_cselect_b32 s60, 4096, 0
MM v[72:75],a[192:195],v[164:167],v[72:75]
s_or_b32 s82, s82, s60
EX v66,v66
MM v[80:83],a[192:195],v[168:171],v[136:139]
s_lshr_b32 s61, s57, 2
EX v67,v67
MM v[80:83],a[196:199],v[184:187],v[80:83]
s_sub_u32 s61, s61, s93
s_cmp_eq_u32 s61, 0
s_cselect_b32 s60, 32, 0
MM v[80:83],a[200:203],v[200:203],v[80:83]
s_or_b32 s82, s82, s60
s_cmp_eq_u32 s61, 8
s_cselect_b32 s60, 8192, 0
MM v[80:83],a[204:207],v[216:219],v[80:83]
s_or_b32 s82, s82, s60
EX v68,v68
MM v[84:87],a[220:223],v[216:219],v[136:139]
s_lshr_b32 s61, s58, 2
TR a[142:143],v233 offset:45056
TR a[144:145],v232 offset:33280
MM v[84:87],a[216:219],v[200:203],v[84:87]
s_sub_u32 s61, s61, s93
s_cmp_eq_u32 s61, 0
s_cselect_b32 s60, 64, 0
EX v69,v69
MM v[84:87],a[212:215],v[184:187],v[84:87]
s_or_b32 s82, s82, s60
s_cmp_eq_u32 s61, 8
s_cselect_b32 s60, 16384, 0
MM v[84:87],a[208:211],v[168:171],v[84:87]
s_or_b32 s82, s82, s60
TR a[146:147],v232 offset:37376
EX v70,v70
MM v[92:95],a[208:211],v[172:175],v[140:143]
s_lshr_b32 s61, s59, 2
TR a[148:149],v232 offset:41472
TR a[150:151],v232 offset:45568
MM v[92:95],a[212:215],v[188:191],v[92:95]
s_sub_u32 s61, s61, s93
s_cmp_eq_u32 s61, 0
s_cselect_b32 s60, 128, 0
EX v71,v71
MM v[92:95],a[216:219],v[204:207],v[92:95]
s_or_b32 s82, s82, s60
s_cmp_eq_u32 s61, 8
s_cselect_b32 s60, 32768, 0
MM v[92:95],a[220:223],v[220:223],v[92:95]
s_or_b32 s82, s82, s60
s_and_b32 s60, s82, 255
s_cselect_b32 s51, 0, 31
EX v72,v72
LPG12_%=:
s_cbranch_execz LTR0_%=
MM v[88:91],a[204:207],v[220:223],v[140:143]
TR a[152:153],v233 offset:33280
CV v65,v66,v67
MM v[88:91],a[200:203],v[204:207],v[88:91]
EX v73,v73
MM v[88:91],a[196:199],v[188:191],v[88:91]
TR a[154:155],v233 offset:37376
CV v66,v68,v69
MM v[88:91],a[192:195],v[172:175],v[88:91]
EX v74,v74
MM v[96:99],a[224:227],v[160:163],v[128:131]
TR a[156:157],v233 offset:41472
TR a[158:159],v233 offset:45568
EX v75,v75
MM v[96:99],a[228:231],v[176:179],v[96:99]
TR a[160:161],v232 offset:33792
CV v67,v70,v71
MM v[96:99],a[232:235],v[192:195],v[96:99]
EX v76,v76
MM v[96:99],a[236:239],v[208:211],v[96:99]
EX v77,v77
MM v[100:103],a[252:255],v[208:211],v[128:131]
TR a[162:163],v232 offset:37888
CV v72,v72,v73
MM v[100:103],a[248:251],v[192:195],v[100:103]
EX v78,v78
MM v[100:103],a[244:247],v[176:179],v[100:103]
TR a[164:165],v232 offset:41984
CV v73,v74,v75
MM v[100:103],a[240:243],v[160:163],v[100:103]
EX v79,v79
MM v[108:111],a[240:243],v[164:167],v[132:135]
TR a[166:167],v232 offset:46080
TR a[168:169],v233 offset:33792
MM v[108:111],a[244:247],v[180:183],v[108:111]
EX v80,v80
MM v[108:111],a[248:251],v[196:199],v[108:111]
EX v81,v81
MM v[108:111],a[252:255],v[212:215],v[108:111]
TR a[170:171],v233 offset:37888
CV v74,v76,v77
MM v[104:107],a[236:239],v[212:215],v[132:135]
EX v82,v82
MM v[104:107],a[232:235],v[196:199],v[104:107]
TR a[172:173],v233 offset:41984
CV v75,v78,v79
MM v[104:107],a[228:231],v[180:183],v[104:107]
EX v83,v83
MM v[104:107],a[224:227],v[164:167],v[104:107]
TR a[174:175],v233 offset:46080
TR a[176:177],v232 offset:34304
MM v[112:115],a[224:227],v[168:171],v[136:139]
EX v84,v84
MM v[112:115],a[228:231],v[184:187],v[112:115]
TR a[178:179],v232 offset:38400
CV v80,v80,v81
MM v[112:115],a[232:235],v[200:203],v[112:115]
EX v85,v85
MM v[112:115],a[236:239],v[216:219],v[112:115]
TR a[180:181],v232 offset:42496
CV v81,v82,v83
MM v[116:119],a[252:255],v[216:219],v[136:139]
EX v86,v86
MM v[116:119],a[248:251],v[200:203],v[116:119]
TR a[182:183],v232 offset:46592
TR a[184:185],v233 offset:34304
MM v[116:119],a[244:247],v[184:187],v[116:119]
EX v87,v87
MM v[116:119],a[240:243],v[168:171],v[116:119]
EX v88,v88
MM v[124:127],a[240:243],v[172:175],v[140:143]
TR a[186:187],v233 offset:38400
CV v82,v84,v85
MM v[124:127],a[244:247],v[188:191],v[124:127]
EX v89,v89
MM v[124:127],a[248:251],v[204:207],v[124:127]
TR a[188:189],v233 offset:42496
TR a[190:191],v233 offset:46592
MM v[124:127],a[252:255],v[220:223],v[124:127]
EX v90,v90
MM v[120:123],a[236:239],v[220:223],v[140:143]
CV v83,v86,v87
CV v88,v88,v89
MM v[120:123],a[232:235],v[204:207],v[120:123]
EX v91,v91
MM v[120:123],a[228:231],v[188:191],v[120:123]
s_getpc_b64 s[98:99]
LTA2_%=:
s_lshl_b32 s73, %[swave], 12
s_add_u32 s73, s73, LPG13_%=-LTA2_%=
s_add_u32 s98, s98, s73
s_addc_u32 s99, s99, 0
s_mov_b64 exec, 0
s_setpc_b64 s[98:99]
LTR2_%=:
s_mov_b64 exec, -1
s_nop 0
CV v89,v90,v91
MM v[120:123],a[224:227],v[172:175],v[120:123]
s_waitcnt vmcnt(14) lgkmcnt(0)
s_barrier
MM a[0:3],a[128:131],v[0:3],a[0:3]
s_waitcnt vmcnt(0)
RD a[192:195],v224 offset:0
v_cmp_ne_u32 s[90:91], s69, v252
MM a[0:3],a[132:135],v[4:7],a[0:3]
s_add_u32 m0, s65, 32768
EX v92,v92
MM a[8:11],a[132:135],v[12:15],a[8:11]
s_cmp_lg_u64 s[90:91], 0
s_cbranch_scc1 LRFG_%=
LRFGb_%=:
v_lshrrev_b32 v253, 8, v248
MM a[8:11],a[128:131],v[8:11],a[8:11]
v_lshlrev_b32 v253, 10, v253
v_and_b32 v252, 240, v248
EX v93,v93
MM a[16:19],a[128:131],v[16:19],a[16:19]
v_add_u32 v253, v253, v252
CV v90,v92,v93
EX v94,v94
MM a[16:19],a[132:135],v[20:23],a[16:19]
EX v95,v95
MM a[24:27],a[132:135],v[28:31],a[24:27]
s_lshr_b32 s76, s48, 6
EX v96,v96
MM a[24:27],a[128:131],v[24:27],a[24:27]
s_lshl_b32 s76, s76, 13
EX v97,v97
MM a[28:31],a[136:139],v[24:27],a[28:31]
RD a[196:199],v225 offset:0
CV v91,v94,v95
MM a[28:31],a[140:143],v[28:31],a[28:31]
EX v98,v98
MM a[20:23],a[140:143],v[20:23],a[20:23]
buffer_load_dwordx4 v228, s[8:11], s64 offen offset:0 lds
MM a[20:23],a[136:139],v[16:19],a[20:23]
s_bfe_u32 s90, s48, 131076
EX v99,v99
MM a[12:15],a[136:139],v[8:11],a[12:15]
s_lshl_b32 s90, s90, 8
RD a[200:203],v226 offset:0
CV v68,v96,v97
MM a[12:15],a[140:143],v[12:15],a[12:15]
s_add_u32 s76, s76, s90
EX v100,v100
MM a[4:7],a[140:143],v[4:7],a[4:7]
s_add_u32 s76, s76, 22183936
EX v101,v101
MM a[4:7],a[136:139],v[0:3],a[4:7]
s_lshl_b32 s90, %[swave], 11
CV v69,v98,v99
MM a[32:35],a[144:147],v[0:3],a[32:35]
EX v102,v102
MM a[32:35],a[148:151],v[4:7],a[32:35]
buffer_load_dwordx4 v229, s[8:11], s64 offen offset:1024 lds
MM a[40:43],a[148:151],v[12:15],a[40:43]
s_add_u32 s90, s90, %[slds]
s_add_u32 m0, s90, 131072
s_nop 0
buffer_load_dwordx4 v253, s[20:23], s76 offen offset:0 sc1 lds
MM a[40:43],a[144:147],v[8:11],a[40:43]
s_add_u32 m0, s65, 32768
EX v103,v103
MM a[48:51],a[144:147],v[16:19],a[48:51]
s_add_u32 m0, s90, 132096
s_add_u32 s76, s76, 4096
buffer_load_dwordx4 v253, s[20:23], s76 offen offset:0 sc1 lds
MM a[48:51],a[148:151],v[20:23],a[48:51]
EX v104,v104
MM a[56:59],a[148:151],v[28:31],a[56:59]
RD a[204:207],v227 offset:0
CV v70,v100,v101
MM a[56:59],a[144:147],v[24:27],a[56:59]
EX v105,v105
MM a[60:63],a[152:155],v[24:27],a[60:63]
RD a[208:211],v224 offset:4096
CV v71,v102,v103
MM a[60:63],a[156:159],v[28:31],a[60:63]
EX v106,v106
MM a[52:55],a[156:159],v[20:23],a[52:55]
EX v107,v107
MM a[52:55],a[152:155],v[16:19],a[52:55]
s_add_u32 m0, s65, 32768
s_nop 0
buffer_load_dwordx4 v230, s[8:11], s64 offen offset:2048 lds
MM a[44:47],a[152:155],v[8:11],a[44:47]
EX v108,v108
MM a[44:47],a[156:159],v[12:15],a[44:47]
RD a[212:215],v225 offset:4096
CV v76,v104,v105
MM a[36:39],a[156:159],v[4:7],a[36:39]
EX v109,v109
MM a[36:39],a[152:155],v[0:3],a[36:39]
RD a[216:219],v226 offset:4096
CV v77,v106,v107
MM a[64:67],a[160:163],v[0:3],a[64:67]
EX v110,v110
MM a[64:67],a[164:167],v[4:7],a[64:67]
EX v111,v111
MM a[72:75],a[164:167],v[12:15],a[72:75]
buffer_load_dwordx4 v231, s[8:11], s64 offen offset:3072 lds
MM a[72:75],a[160:163],v[8:11],a[72:75]
s_add_u32 m0, s65, 98304
RD a[220:223],v227 offset:4096
CV v78,v108,v109
MM a[80:83],a[160:163],v[16:19],a[80:83]
EX v112,v112
MM a[80:83],a[164:167],v[20:23],a[80:83]
EX v113,v113
MM a[88:91],a[164:167],v[28:31],a[88:91]
RD a[224:227],v224 offset:8192
CV v79,v110,v111
MM a[88:91],a[160:163],v[24:27],a[88:91]
EX v114,v114
MM a[92:95],a[168:171],v[24:27],a[92:95]
EX v115,v115
MM a[92:95],a[172:175],v[28:31],a[92:95]
buffer_load_dwordx4 v235, s[12:15], s81 offen offset:0 lds
MM a[84:87],a[172:175],v[20:23],a[84:87]
RD a[228:231],v225 offset:8192
CV v84,v112,v113
MM a[84:87],a[168:171],v[16:19],a[84:87]
EX v116,v116
MM a[76:79],a[168:171],v[8:11],a[76:79]
EX v117,v117
MM a[76:79],a[172:175],v[12:15],a[76:79]
RD a[232:235],v226 offset:8192
CV v85,v114,v115
MM a[68:71],a[172:175],v[4:7],a[68:71]
EX v118,v118
MM a[68:71],a[168:171],v[0:3],a[68:71]
RD a[236:239],v227 offset:8192
CV v86,v116,v117
MM a[96:99],a[176:179],v[0:3],a[96:99]
EX v119,v119
MM a[96:99],a[180:183],v[4:7],a[96:99]
buffer_load_dwordx4 v235, s[12:15], s64 offen offset:1024 lds
MM a[104:107],a[180:183],v[12:15],a[104:107]
EX v120,v120
MM a[104:107],a[176:179],v[8:11],a[104:107]
RD a[240:243],v224 offset:12288
CV v87,v118,v119
MM a[112:115],a[176:179],v[16:19],a[112:115]
EX v121,v121
MM a[112:115],a[180:183],v[20:23],a[112:115]
EX v122,v122
MM a[120:123],a[180:183],v[28:31],a[120:123]
RD a[244:247],v225 offset:12288
CV v92,v120,v121
MM a[120:123],a[176:179],v[24:27],a[120:123]
EX v123,v123
MM a[124:127],a[184:187],v[24:27],a[124:127]
buffer_load_dwordx4 v235, s[12:15], s81 offen offset:2048 lds
MM a[124:127],a[188:191],v[28:31],a[124:127]
EX v124,v124
MM a[116:119],a[188:191],v[20:23],a[116:119]
RD a[248:251],v226 offset:12288
CV v93,v122,v123
MM a[116:119],a[184:187],v[16:19],a[116:119]
EX v125,v125
MM a[108:111],a[184:187],v[8:11],a[108:111]
EX v126,v126
MM a[108:111],a[188:191],v[12:15],a[108:111]
RD a[252:255],v227 offset:12288
CV v94,v124,v125
MM a[100:103],a[188:191],v[4:7],a[100:103]
EX v127,v127
MM a[100:103],a[184:187],v[0:3],a[100:103]
buffer_load_dwordx4 v235, s[12:15], s64 offen offset:3072 lds
v_smfmac_f32_16x16x64_bf16 v[236:239], v[144:147], v[64:71], v234
CV v95,v126,v127
v_smfmac_f32_16x16x64_bf16 v[236:239], v[148:151], v[72:79], v234
v_smfmac_f32_16x16x64_bf16 v[236:239], v[152:155], v[80:87], v234
v_smfmac_f32_16x16x64_bf16 v[236:239], v[156:159], v[88:95], v234
s_waitcnt lgkmcnt(0)
MM v[0:3],a[192:195],v[160:163],v[128:131]
s_add_u32 s64, s64, 16384
TR a[128:129],v232 offset:49152
s_add_u32 s81, s64, 896
MM v[0:3],a[196:199],v[176:179],v[0:3]
TR a[130:131],v232 offset:53248
TR a[132:133],v232 offset:57344
MM v[0:3],a[200:203],v[192:195],v[0:3]
TR a[134:135],v232 offset:61440
TR a[136:137],v233 offset:49152
MM v[0:3],a[204:207],v[208:211],v[0:3]
TR a[138:139],v233 offset:53248
v_add_f32 v247, v236, v237
MM v[4:7],a[220:223],v[208:211],v[128:131]
TR a[140:141],v233 offset:57344
v_cmp_nge_f32 vcc, s71, v247
MM v[4:7],a[216:219],v[192:195],v[4:7]
s_cbranch_vccnz LSITE19_%=
LBACK19_%=:
MM v[4:7],a[212:215],v[176:179],v[4:7]
v_lshrrev_b32 v253, 7, v248
v_mul_u32_u24 v253, 1280, v253
MM v[4:7],a[208:211],v[160:163],v[4:7]
v_and_b32 v252, 112, v248
v_add_u32 v253, v253, v252
MM v[12:15],a[208:211],v[164:167],v[132:135]
s_mul_i32 s90, %[swave], 30720
EX v0,v0
MM v[12:15],a[212:215],v[180:183],v[12:15]
s_mul_i32 s91, %[swave], 3072
EX v1,v1
MM v[12:15],a[216:219],v[196:199],v[12:15]
s_add_u32 s91, s91, %[slds]
s_add_u32 m0, s91, 151552
s_add_u32 s77, s90, 0
buffer_load_dwordx4 v253, s[28:31], s77 offen offset:0 lds
MM v[12:15],a[220:223],v[212:215],v[12:15]
TR a[142:143],v233 offset:61440
CV v0,v0,v1
MM v[8:11],a[204:207],v[212:215],v[132:135]
EX v2,v2
MM v[8:11],a[200:203],v[196:199],v[8:11]
s_add_u32 m0, s91, 152576
s_add_u32 s77, s90, 10240
buffer_load_dwordx4 v253, s[28:31], s77 offen offset:0 lds
MM v[8:11],a[196:199],v[180:183],v[8:11]
EX v3,v3
MM v[8:11],a[192:195],v[164:167],v[8:11]
TR a[144:145],v232 offset:49664
CV v1,v2,v3
MM v[16:19],a[192:195],v[168:171],v[136:139]
s_add_u32 m0, s91, 153600
s_add_u32 s77, s90, 20480
buffer_load_dwordx4 v253, s[28:31], s77 offen offset:0 lds
MM v[16:19],a[196:199],v[184:187],v[16:19]
EX v4,v4
MM v[16:19],a[200:203],v[200:203],v[16:19]
TR a[146:147],v232 offset:53760
TR a[148:149],v232 offset:57856
MM v[16:19],a[204:207],v[216:219],v[16:19]
EX v5,v5
MM v[20:23],a[220:223],v[216:219],v[136:139]
EX v6,v6
MM v[20:23],a[216:219],v[200:203],v[20:23]
)ASM"
R"ASM(
TR a[150:151],v232 offset:61952
CV v2,v4,v5
MM v[20:23],a[212:215],v[184:187],v[20:23]
EX v7,v7
MM v[20:23],a[208:211],v[168:171],v[20:23]
TR a[152:153],v233 offset:49664
CV v3,v6,v7
MM v[28:31],a[208:211],v[172:175],v[140:143]
EX v8,v8
MM v[28:31],a[212:215],v[188:191],v[28:31]
TR a[154:155],v233 offset:53760
TR a[156:157],v233 offset:57856
MM v[28:31],a[216:219],v[204:207],v[28:31]
EX v9,v9
MM v[28:31],a[220:223],v[220:223],v[28:31]
EX v10,v10
MM v[24:27],a[204:207],v[220:223],v[140:143]
TR a[158:159],v233 offset:61952
CV v8,v8,v9
MM v[24:27],a[200:203],v[204:207],v[24:27]
EX v11,v11
MM v[24:27],a[196:199],v[188:191],v[24:27]
TR a[160:161],v232 offset:50176
CV v9,v10,v11
MM v[24:27],a[192:195],v[172:175],v[24:27]
EX v12,v12
MM v[32:35],a[224:227],v[160:163],v[128:131]
TR a[162:163],v232 offset:54272
TR a[164:165],v232 offset:58368
MM v[32:35],a[228:231],v[176:179],v[32:35]
EX v13,v13
MM v[32:35],a[232:235],v[192:195],v[32:35]
TR a[166:167],v232 offset:62464
CV v10,v12,v13
MM v[32:35],a[236:239],v[208:211],v[32:35]
EX v14,v14
MM v[36:39],a[252:255],v[208:211],v[128:131]
TR a[168:169],v233 offset:50176
TR a[170:171],v233 offset:54272
MM v[36:39],a[248:251],v[192:195],v[36:39]
EX v15,v15
MM v[36:39],a[244:247],v[176:179],v[36:39]
TR a[172:173],v233 offset:58368
CV v11,v14,v15
MM v[36:39],a[240:243],v[160:163],v[36:39]
EX v16,v16
MM v[44:47],a[240:243],v[164:167],v[132:135]
EX v17,v17
MM v[44:47],a[244:247],v[180:183],v[44:47]
TR a[174:175],v233 offset:62464
CV v16,v16,v17
MM v[44:47],a[248:251],v[196:199],v[44:47]
EX v18,v18
MM v[44:47],a[252:255],v[212:215],v[44:47]
TR a[176:177],v232 offset:50688
TR a[178:179],v232 offset:54784
MM v[40:43],a[236:239],v[212:215],v[132:135]
EX v19,v19
MM v[40:43],a[232:235],v[196:199],v[40:43]
TR a[180:181],v232 offset:58880
CV v17,v18,v19
MM v[40:43],a[228:231],v[180:183],v[40:43]
EX v20,v20
MM v[40:43],a[224:227],v[164:167],v[40:43]
EX v21,v21
MM v[48:51],a[224:227],v[168:171],v[136:139]
TR a[182:183],v232 offset:62976
CV v18,v20,v21
MM v[48:51],a[228:231],v[184:187],v[48:51]
EX v22,v22
MM v[48:51],a[232:235],v[200:203],v[48:51]
TR a[184:185],v233 offset:50688
TR a[186:187],v233 offset:54784
MM v[48:51],a[236:239],v[216:219],v[48:51]
EX v23,v23
MM v[52:55],a[252:255],v[216:219],v[136:139]
TR a[188:189],v233 offset:58880
CV v19,v22,v23
MM v[52:55],a[248:251],v[200:203],v[52:55]
EX v24,v24
MM v[52:55],a[244:247],v[184:187],v[52:55]
EX v25,v25
MM v[52:55],a[240:243],v[168:171],v[52:55]
TR a[190:191],v233 offset:62976
CV v24,v24,v25
MM v[60:63],a[240:243],v[172:175],v[140:143]
EX v26,v26
MM v[60:63],a[244:247],v[188:191],v[60:63]
EX v27,v27
MM v[60:63],a[248:251],v[204:207],v[60:63]
EX v28,v28
MM v[60:63],a[252:255],v[220:223],v[60:63]
EX v29,v29
MM v[56:59],a[236:239],v[220:223],v[140:143]
EX v30,v30
MM v[56:59],a[232:235],v[204:207],v[56:59]
EX v31,v31
MM v[56:59],a[228:231],v[188:191],v[56:59]
EX v32,v32
MM v[56:59],a[224:227],v[172:175],v[56:59]
s_waitcnt lgkmcnt(0)
s_barrier
MM a[0:3],a[128:131],v[64:67],a[0:3]
s_add_u32 m0, s65, 49152
CV v25,v26,v27
CV v26,v28,v29
MM a[0:3],a[132:135],v[68:71],a[0:3]
RD a[192:195],v224 offset:16384
CV v27,v30,v31
MM a[8:11],a[132:135],v[76:79],a[8:11]
buffer_load_dwordx4 v228, s[8:11], s64 offen offset:0 lds
MM a[8:11],a[128:131],v[72:75],a[8:11]
RD a[196:199],v225 offset:16384
RD a[200:203],v226 offset:16384
MM a[16:19],a[128:131],v[80:83],a[16:19]
EX v33,v33
MM a[16:19],a[132:135],v[84:87],a[16:19]
RD a[204:207],v227 offset:16384
CV v4,v32,v33
MM a[24:27],a[132:135],v[92:95],a[24:27]
buffer_load_dwordx4 v229, s[8:11], s64 offen offset:1024 lds
MM a[24:27],a[128:131],v[88:91],a[24:27]
EX v34,v34
MM a[28:31],a[136:139],v[88:91],a[28:31]
EX v35,v35
MM a[28:31],a[140:143],v[92:95],a[28:31]
RD a[208:211],v224 offset:20480
CV v5,v34,v35
MM a[20:23],a[140:143],v[84:87],a[20:23]
EX v36,v36
MM a[20:23],a[136:139],v[80:83],a[20:23]
EX v37,v37
MM a[12:15],a[136:139],v[72:75],a[12:15]
RD a[212:215],v225 offset:20480
CV v6,v36,v37
MM a[12:15],a[140:143],v[76:79],a[12:15]
buffer_load_dwordx4 v230, s[8:11], s64 offen offset:2048 lds
MM a[4:7],a[140:143],v[68:71],a[4:7]
EX v38,v38
MM a[4:7],a[136:139],v[64:67],a[4:7]
EX v39,v39
MM a[32:35],a[144:147],v[64:67],a[32:35]
RD a[216:219],v226 offset:20480
CV v7,v38,v39
MM a[32:35],a[148:151],v[68:71],a[32:35]
EX v40,v40
MM a[40:43],a[148:151],v[76:79],a[40:43]
RD a[220:223],v227 offset:20480
RD a[224:227],v224 offset:24576
MM a[40:43],a[144:147],v[72:75],a[40:43]
EX v41,v41
MM a[48:51],a[144:147],v[80:83],a[48:51]
RD a[228:231],v225 offset:24576
CV v12,v40,v41
MM a[48:51],a[148:151],v[84:87],a[48:51]
EX v42,v42
MM a[56:59],a[148:151],v[92:95],a[56:59]
buffer_load_dwordx4 v231, s[8:11], s64 offen offset:3072 lds
MM a[56:59],a[144:147],v[88:91],a[56:59]
s_add_u32 m0, s65, 114688
EX v43,v43
MM a[60:63],a[152:155],v[88:91],a[60:63]
RD a[232:235],v226 offset:24576
CV v13,v42,v43
MM a[60:63],a[156:159],v[92:95],a[60:63]
EX v44,v44
MM a[52:55],a[156:159],v[84:87],a[52:55]
EX v45,v45
MM a[52:55],a[152:155],v[80:83],a[52:55]
RD a[236:239],v227 offset:24576
CV v14,v44,v45
MM a[44:47],a[152:155],v[72:75],a[44:47]
EX v46,v46
MM a[44:47],a[156:159],v[76:79],a[44:47]
buffer_load_dwordx4 v235, s[12:15], s81 offen offset:0 lds
MM a[36:39],a[156:159],v[68:71],a[36:39]
EX v47,v47
MM a[36:39],a[152:155],v[64:67],a[36:39]
RD a[240:243],v224 offset:28672
CV v15,v46,v47
MM a[64:67],a[160:163],v[64:67],a[64:67]
EX v48,v48
MM a[64:67],a[164:167],v[68:71],a[64:67]
EX v49,v49
MM a[72:75],a[164:167],v[76:79],a[72:75]
RD a[244:247],v225 offset:28672
CV v20,v48,v49
MM a[72:75],a[160:163],v[72:75],a[72:75]
buffer_load_dwordx4 v235, s[12:15], s64 offen offset:1024 lds
MM a[80:83],a[160:163],v[80:83],a[80:83]
EX v50,v50
MM a[80:83],a[164:167],v[84:87],a[80:83]
EX v51,v51
MM a[88:91],a[164:167],v[92:95],a[88:91]
RD a[248:251],v226 offset:28672
CV v21,v50,v51
MM a[88:91],a[160:163],v[88:91],a[88:91]
EX v52,v52
MM a[92:95],a[168:171],v[88:91],a[92:95]
EX v53,v53
MM a[92:95],a[172:175],v[92:95],a[92:95]
s_nop 0
RD a[252:255],v227 offset:28672
CV v22,v52,v53
MM a[84:87],a[172:175],v[84:87],a[84:87]
buffer_load_dwordx4 v235, s[12:15], s81 offen offset:2048 lds
MM a[84:87],a[168:171],v[80:83],a[84:87]
s_nop 0
EX v54,v54
MM a[76:79],a[168:171],v[72:75],a[76:79]
s_nop 0
EX v55,v55
MM a[76:79],a[172:175],v[76:79],a[76:79]
s_nop 0
CV v23,v54,v55
MM a[68:71],a[172:175],v[68:71],a[68:71]
s_nop 0
EX v56,v56
MM a[68:71],a[168:171],v[64:67],a[68:71]
s_nop 0
EX v57,v57
MM a[96:99],a[176:179],v[64:67],a[96:99]
buffer_load_dwordx4 v235, s[12:15], s64 offen offset:3072 lds
MM a[96:99],a[180:183],v[68:71],a[96:99]
s_nop 0
EX v58,v58
MM a[104:107],a[180:183],v[76:79],a[104:107]
s_nop 0
EX v59,v59
MM a[104:107],a[176:179],v[72:75],a[104:107]
s_nop 0
EX v60,v60
MM a[112:115],a[176:179],v[80:83],a[112:115]
s_nop 0
EX v61,v61
MM a[112:115],a[180:183],v[84:87],a[112:115]
s_nop 0
EX v62,v62
MM a[120:123],a[180:183],v[92:95],a[120:123]
s_nop 0
EX v63,v63
MM a[120:123],a[176:179],v[88:91],a[120:123]
s_nop 0
CV v28,v56,v57
CV v29,v58,v59
MM a[124:127],a[184:187],v[88:91],a[124:127]
s_nop 0
CV v30,v60,v61
CV v31,v62,v63
MM a[124:127],a[188:191],v[92:95],a[124:127]
s_nop 0
MM a[116:119],a[188:191],v[84:87],a[116:119]
s_nop 0
MM a[116:119],a[184:187],v[80:83],a[116:119]
s_nop 0
MM a[108:111],a[184:187],v[72:75],a[108:111]
s_nop 0
MM a[108:111],a[188:191],v[76:79],a[108:111]
s_nop 0
MM a[100:103],a[188:191],v[68:71],a[100:103]
s_nop 0
MM a[100:103],a[184:187],v[64:67],a[100:103]
LPG13_%=:
s_cbranch_execz LTR2_%=
v_smfmac_f32_16x16x64_bf16 v[236:239], v[144:147], v[0:7], v234
v_smfmac_f32_16x16x64_bf16 v[236:239], v[148:151], v[8:15], v234
v_smfmac_f32_16x16x64_bf16 v[236:239], v[152:155], v[16:23], v234
v_smfmac_f32_16x16x64_bf16 v[236:239], v[156:159], v[24:31], v234
s_waitcnt lgkmcnt(0)
MM v[64:67],a[192:195],v[160:163],v[128:131]
s_add_u32 s64, s64, 16384
TR a[128:129],v232 offset:0
s_add_u32 s81, s64, 896
MM v[64:67],a[196:199],v[176:179],v[64:67]
TR a[130:131],v232 offset:4096
TR a[132:133],v232 offset:8192
MM v[64:67],a[200:203],v[192:195],v[64:67]
TR a[134:135],v232 offset:12288
TR a[136:137],v233 offset:0
MM v[64:67],a[204:207],v[208:211],v[64:67]
TR a[138:139],v233 offset:4096
v_add_f32 v246, v236, v237
MM v[68:71],a[220:223],v[208:211],v[128:131]
TR a[140:141],v233 offset:8192
v_cmp_nge_f32 vcc, s71, v246
MM v[68:71],a[216:219],v[192:195],v[68:71]
s_cbranch_vccnz LSITE20_%=
LBACK20_%=:
MM v[68:71],a[212:215],v[176:179],v[68:71]
v_lshrrev_b32 v43, 7, v248
v_bfe_u32 v42, v248, 6, 1
MM v[68:71],a[208:211],v[160:163],v[68:71]
v_lshl_add_u32 v40, v42, 3, v43
v_lshlrev_b32 v40, 4, v40
MM v[76:79],a[208:211],v[164:167],v[132:135]
v_bfe_u32 v42, v248, 4, 2
v_lshl_add_u32 v40, v42, 2, v40
MM v[76:79],a[212:215],v[180:183],v[76:79]
s_lshl_b32 s76, %[swave], 11
EX v64,v64
MM v[76:79],a[216:219],v[196:199],v[76:79]
s_add_u32 s76, s76, %[slds]
EX v65,v65
MM v[76:79],a[220:223],v[212:215],v[76:79]
s_add_u32 s76, s76, 131072
v_add_u32 v40, s76, v40
CV v64,v64,v65
MM v[72:75],a[204:207],v[212:215],v[132:135]
s_waitcnt vmcnt(17)
ds_read_b32 v32, v40 offset:0
ds_read_b32 v36, v40 offset:256
MM v[72:75],a[200:203],v[196:199],v[72:75]
ds_read_b32 v33, v40 offset:512
ds_read_b32 v37, v40 offset:768
MM v[72:75],a[196:199],v[180:183],v[72:75]
ds_read_b32 v34, v40 offset:1024
ds_read_b32 v38, v40 offset:1280
MM v[72:75],a[192:195],v[164:167],v[72:75]
ds_read_b32 v35, v40 offset:1536
ds_read_b32 v39, v40 offset:1792
MM v[80:83],a[192:195],v[168:171],v[136:139]
v_mul_u32_u24 v41, 3456, v43
v_bfe_u32 v42, v248, 4, 3
MM v[80:83],a[196:199],v[184:187],v[80:83]
TR a[142:143],v233 offset:12288
v_lshl_add_u32 v41, v42, 1, v41
MM v[80:83],a[200:203],v[200:203],v[80:83]
s_add_u32 s76, s48, 21626880
EX v66,v66
MM v[80:83],a[204:207],v[216:219],v[80:83]
s_lshl_b32 s77, %[swave], 4
EX v67,v67
MM v[84:87],a[220:223],v[216:219],v[136:139]
EX v68,v68
MM v[84:87],a[216:219],v[200:203],v[84:87]
EX v69,v69
MM v[84:87],a[212:215],v[184:187],v[84:87]
TR a[144:145],v232 offset:512
TR a[146:147],v232 offset:4608
MM v[84:87],a[208:211],v[168:171],v[84:87]
EX v70,v70
MM v[92:95],a[208:211],v[172:175],v[140:143]
TR a[148:149],v232 offset:8704
TR a[150:151],v232 offset:12800
MM v[92:95],a[212:215],v[188:191],v[92:95]
EX v71,v71
MM v[92:95],a[216:219],v[204:207],v[92:95]
EX v72,v72
MM v[92:95],a[220:223],v[220:223],v[92:95]
s_waitcnt lgkmcnt(5)
v_add_f32 v32, v32, v33
v_add_f32 v36, v36, v37
MM v[88:91],a[204:207],v[220:223],v[140:143]
v_add_f32 v34, v34, v35
v_add_f32 v38, v38, v39
MM v[88:91],a[200:203],v[204:207],v[88:91]
TR a[152:153],v233 offset:512
v_add_f32 v32, v32, v34
v_add_f32 v36, v36, v38
EX v73,v73
CV v65,v66,v67
MM v[88:91],a[196:199],v[188:191],v[88:91]
TR a[154:155],v233 offset:4608
CV v42,v32,v36
v_lshlrev_b32 v32, 16, v42
MM v[88:91],a[192:195],v[172:175],v[88:91]
v_and_b32 v36, 0xffff0000, v42
v_mul_f32 v42, 0xbfb8aa3b, v32
EX v74,v74
MM v[96:99],a[224:227],v[160:163],v[128:131]
TR a[156:157],v233 offset:8704
EX v42,v42
EX v75,v75
CV v66,v68,v69
MM v[96:99],a[228:231],v[176:179],v[96:99]
TR a[158:159],v233 offset:12800
v_add_f32 v42, 1.0, v42
MM v[96:99],a[232:235],v[192:195],v[96:99]
v_rcp_f32 v42, v42
EX v76,v76
MM v[96:99],a[236:239],v[208:211],v[96:99]
v_mul_f32 v42, v32, v42
CV v42,v42,v42
MM v[100:103],a[252:255],v[208:211],v[128:131]
TR a[160:161],v232 offset:1024
v_lshlrev_b32 v42, 16, v42
v_mul_f32 v32, v42, v36
EX v77,v77
CV v67,v70,v71
MM v[100:103],a[248:251],v[192:195],v[100:103]
TR a[162:163],v232 offset:5120
CV v43,v32,v32
MM v[100:103],a[244:247],v[176:179],v[100:103]
s_lshl_b64 exec, 0xffff, s77
buffer_store_short v43, v41, s[20:23], s76 offen offset:0 sc1
s_mov_b64 exec, -1
s_nop 0
EX v78,v78
MM v[100:103],a[240:243],v[160:163],v[100:103]
TR a[164:165],v232 offset:9216
CV v72,v72,v73
MM v[108:111],a[240:243],v[164:167],v[132:135]
EX v79,v79
MM v[108:111],a[244:247],v[180:183],v[108:111]
TR a[166:167],v232 offset:13312
CV v73,v74,v75
MM v[108:111],a[248:251],v[196:199],v[108:111]
EX v80,v80
MM v[108:111],a[252:255],v[212:215],v[108:111]
TR a[168:169],v233 offset:1024
)ASM"
R"ASM(
TR a[170:171],v233 offset:5120
EX v81,v81
MM v[104:107],a[236:239],v[212:215],v[132:135]
TR a[172:173],v233 offset:9216
CV v74,v76,v77
MM v[104:107],a[232:235],v[196:199],v[104:107]
EX v82,v82
MM v[104:107],a[228:231],v[180:183],v[104:107]
EX v83,v83
MM v[104:107],a[224:227],v[164:167],v[104:107]
TR a[174:175],v233 offset:13312
CV v75,v78,v79
MM v[112:115],a[224:227],v[168:171],v[136:139]
EX v84,v84
MM v[112:115],a[228:231],v[184:187],v[112:115]
TR a[176:177],v232 offset:1536
TR a[178:179],v232 offset:5632
MM v[112:115],a[232:235],v[200:203],v[112:115]
EX v85,v85
MM v[112:115],a[236:239],v[216:219],v[112:115]
TR a[180:181],v232 offset:9728
CV v80,v80,v81
MM v[116:119],a[252:255],v[216:219],v[136:139]
EX v86,v86
MM v[116:119],a[248:251],v[200:203],v[116:119]
TR a[182:183],v232 offset:13824
CV v81,v82,v83
MM v[116:119],a[244:247],v[184:187],v[116:119]
EX v87,v87
MM v[116:119],a[240:243],v[168:171],v[116:119]
TR a[184:185],v233 offset:1536
CV v82,v84,v85
MM v[124:127],a[240:243],v[172:175],v[140:143]
EX v88,v88
MM v[124:127],a[244:247],v[188:191],v[124:127]
TR a[186:187],v233 offset:5632
TR a[188:189],v233 offset:9728
MM v[124:127],a[248:251],v[204:207],v[124:127]
TR a[190:191],v233 offset:13824
CV v83,v86,v87
MM v[124:127],a[252:255],v[220:223],v[124:127]
EX v89,v89
MM v[120:123],a[236:239],v[220:223],v[140:143]
EX v90,v90
MM v[120:123],a[232:235],v[204:207],v[120:123]
EX v91,v91
MM v[120:123],a[228:231],v[188:191],v[120:123]
EX v92,v92
MM v[120:123],a[224:227],v[172:175],v[120:123]
s_waitcnt vmcnt(16) lgkmcnt(0)
s_barrier
MM a[0:3],a[128:131],v[0:3],a[0:3]
s_add_u32 m0, s65, 0
CV v88,v88,v89
CV v89,v90,v91
MM a[0:3],a[132:135],v[4:7],a[0:3]
EX v93,v93
MM a[8:11],a[132:135],v[12:15],a[8:11]
EX v94,v94
MM a[8:11],a[128:131],v[8:11],a[8:11]
EX v95,v95
MM a[16:19],a[128:131],v[16:19],a[16:19]
RD a[192:195],v224 offset:32768
CV v90,v92,v93
MM a[16:19],a[132:135],v[20:23],a[16:19]
EX v96,v96
MM a[24:27],a[132:135],v[28:31],a[24:27]
RD a[196:199],v225 offset:32768
CV v91,v94,v95
MM a[24:27],a[128:131],v[24:27],a[24:27]
EX v97,v97
MM a[28:31],a[136:139],v[24:27],a[28:31]
buffer_load_dwordx4 v228, s[8:11], s64 offen offset:0 lds
MM a[28:31],a[140:143],v[28:31],a[28:31]
EX v98,v98
MM a[20:23],a[140:143],v[20:23],a[20:23]
EX v99,v99
MM a[20:23],a[136:139],v[16:19],a[20:23]
RD a[200:203],v226 offset:32768
CV v68,v96,v97
MM a[12:15],a[136:139],v[8:11],a[12:15]
EX v100,v100
MM a[12:15],a[140:143],v[12:15],a[12:15]
RD a[204:207],v227 offset:32768
CV v69,v98,v99
MM a[4:7],a[140:143],v[4:7],a[4:7]
EX v101,v101
MM a[4:7],a[136:139],v[0:3],a[4:7]
buffer_load_dwordx4 v229, s[8:11], s64 offen offset:1024 lds
MM a[32:35],a[144:147],v[0:3],a[32:35]
EX v102,v102
MM a[32:35],a[148:151],v[4:7],a[32:35]
RD a[208:211],v224 offset:36864
CV v70,v100,v101
MM a[40:43],a[148:151],v[12:15],a[40:43]
EX v103,v103
MM a[40:43],a[144:147],v[8:11],a[40:43]
RD a[212:215],v225 offset:36864
CV v71,v102,v103
MM a[48:51],a[144:147],v[16:19],a[48:51]
EX v104,v104
MM a[48:51],a[148:151],v[20:23],a[48:51]
EX v105,v105
MM a[56:59],a[148:151],v[28:31],a[56:59]
buffer_load_dwordx4 v230, s[8:11], s64 offen offset:2048 lds
MM a[56:59],a[144:147],v[24:27],a[56:59]
EX v106,v106
MM a[60:63],a[152:155],v[24:27],a[60:63]
RD a[216:219],v226 offset:36864
CV v76,v104,v105
MM a[60:63],a[156:159],v[28:31],a[60:63]
EX v107,v107
MM a[52:55],a[156:159],v[20:23],a[52:55]
RD a[220:223],v227 offset:36864
CV v77,v106,v107
MM a[52:55],a[152:155],v[16:19],a[52:55]
EX v108,v108
MM a[44:47],a[152:155],v[8:11],a[44:47]
EX v109,v109
MM a[44:47],a[156:159],v[12:15],a[44:47]
buffer_load_dwordx4 v231, s[8:11], s64 offen offset:3072 lds
MM a[36:39],a[156:159],v[4:7],a[36:39]
s_add_u32 m0, s65, 65536
RD a[224:227],v224 offset:40960
CV v78,v108,v109
MM a[36:39],a[152:155],v[0:3],a[36:39]
EX v110,v110
MM a[64:67],a[160:163],v[0:3],a[64:67]
EX v111,v111
MM a[64:67],a[164:167],v[4:7],a[64:67]
RD a[228:231],v225 offset:40960
CV v79,v110,v111
MM a[72:75],a[164:167],v[12:15],a[72:75]
EX v112,v112
MM a[72:75],a[160:163],v[8:11],a[72:75]
buffer_load_dwordx4 v235, s[12:15], s81 offen offset:0 lds
MM a[80:83],a[160:163],v[16:19],a[80:83]
EX v113,v113
MM a[80:83],a[164:167],v[20:23],a[80:83]
RD a[232:235],v226 offset:40960
CV v84,v112,v113
MM a[88:91],a[164:167],v[28:31],a[88:91]
EX v114,v114
MM a[88:91],a[160:163],v[24:27],a[88:91]
EX v115,v115
MM a[92:95],a[168:171],v[24:27],a[92:95]
RD a[236:239],v227 offset:40960
CV v85,v114,v115
MM a[92:95],a[172:175],v[28:31],a[92:95]
EX v116,v116
MM a[84:87],a[172:175],v[20:23],a[84:87]
buffer_load_dwordx4 v235, s[12:15], s64 offen offset:1024 lds
MM a[84:87],a[168:171],v[16:19],a[84:87]
EX v117,v117
MM a[76:79],a[168:171],v[8:11],a[76:79]
RD a[240:243],v224 offset:45056
CV v86,v116,v117
MM a[76:79],a[172:175],v[12:15],a[76:79]
EX v118,v118
MM a[68:71],a[172:175],v[4:7],a[68:71]
EX v119,v119
MM a[68:71],a[168:171],v[0:3],a[68:71]
RD a[244:247],v225 offset:45056
CV v87,v118,v119
MM a[96:99],a[176:179],v[0:3],a[96:99]
EX v120,v120
MM a[96:99],a[180:183],v[4:7],a[96:99]
buffer_load_dwordx4 v235, s[12:15], s81 offen offset:2048 lds
MM a[104:107],a[180:183],v[12:15],a[104:107]
EX v121,v121
MM a[104:107],a[176:179],v[8:11],a[104:107]
RD a[248:251],v226 offset:45056
CV v92,v120,v121
MM a[112:115],a[176:179],v[16:19],a[112:115]
EX v122,v122
MM a[112:115],a[180:183],v[20:23],a[112:115]
EX v123,v123
MM a[120:123],a[180:183],v[28:31],a[120:123]
RD a[252:255],v227 offset:45056
CV v93,v122,v123
MM a[120:123],a[176:179],v[24:27],a[120:123]
EX v124,v124
MM a[124:127],a[184:187],v[24:27],a[124:127]
buffer_load_dwordx4 v235, s[12:15], s64 offen offset:3072 lds
MM a[124:127],a[188:191],v[28:31],a[124:127]
EX v125,v125
MM a[116:119],a[188:191],v[20:23],a[116:119]
EX v126,v126
MM a[116:119],a[184:187],v[16:19],a[116:119]
EX v127,v127
MM a[108:111],a[184:187],v[8:11],a[108:111]
CV v94,v124,v125
CV v95,v126,v127
MM a[108:111],a[188:191],v[12:15],a[108:111]
MM a[100:103],a[188:191],v[4:7],a[100:103]
MM a[100:103],a[184:187],v[0:3],a[100:103]
v_smfmac_f32_16x16x64_bf16 v[236:239], v[144:147], v[64:71], v234
v_smfmac_f32_16x16x64_bf16 v[236:239], v[148:151], v[72:79], v234
v_smfmac_f32_16x16x64_bf16 v[236:239], v[152:155], v[80:87], v234
v_smfmac_f32_16x16x64_bf16 v[236:239], v[156:159], v[88:95], v234
s_waitcnt lgkmcnt(0)
MM v[0:3],a[192:195],v[160:163],v[128:131]
s_add_u32 s64, s64, 16384
TR a[128:129],v232 offset:16384
s_add_u32 s81, s64, 896
MM v[0:3],a[196:199],v[176:179],v[0:3]
TR a[130:131],v232 offset:20480
TR a[132:133],v232 offset:24576
MM v[0:3],a[200:203],v[192:195],v[0:3]
TR a[134:135],v232 offset:28672
TR a[136:137],v233 offset:16384
MM v[0:3],a[204:207],v[208:211],v[0:3]
TR a[138:139],v233 offset:20480
v_add_f32 v247, v236, v237
MM v[4:7],a[220:223],v[208:211],v[128:131]
TR a[140:141],v233 offset:24576
v_cmp_nge_f32 vcc, s71, v247
MM v[4:7],a[216:219],v[192:195],v[4:7]
s_cbranch_vccnz LSITE21_%=
LBACK21_%=:
MM v[4:7],a[212:215],v[176:179],v[4:7]
s_lshr_b32 s77, %[sb], 3
TR a[142:143],v233 offset:28672
TR a[144:145],v232 offset:16896
MM v[4:7],a[208:211],v[160:163],v[4:7]
s_lshl_b32 s77, s77, 2
TR a[146:147],v232 offset:20992
TR a[148:149],v232 offset:25088
MM v[12:15],a[208:211],v[164:167],v[132:135]
s_add_u32 s77, s77, %[swave]
EX v0,v0
MM v[12:15],a[212:215],v[180:183],v[12:15]
s_lshl_b32 s77, s77, 9
EX v1,v1
MM v[12:15],a[216:219],v[196:199],v[12:15]
s_add_u32 s77, s77, 0
v_lshrrev_b32 v253, 1, v248
v_add_u32 v253, s77, v253
MM v[12:15],a[220:223],v[212:215],v[12:15]
s_mov_b32 s77, LKEND_ka-LPD1_%=-16
v_min_u32 v253, s77, v253
CV v0,v0,v1
MM v[8:11],a[204:207],v[212:215],v[132:135]
s_getpc_b64 s[98:99]
LPD1_%=:
global_load_dword v253, v253, s[98:99]
MM v[8:11],a[200:203],v[196:199],v[8:11]
EX v2,v2
MM v[8:11],a[196:199],v[180:183],v[8:11]
EX v3,v3
MM v[8:11],a[192:195],v[164:167],v[8:11]
TR a[150:151],v232 offset:29184
CV v1,v2,v3
MM v[16:19],a[192:195],v[168:171],v[136:139]
EX v4,v4
MM v[16:19],a[196:199],v[184:187],v[16:19]
EX v5,v5
MM v[16:19],a[200:203],v[200:203],v[16:19]
TR a[152:153],v233 offset:16896
CV v2,v4,v5
MM v[16:19],a[204:207],v[216:219],v[16:19]
EX v6,v6
MM v[20:23],a[220:223],v[216:219],v[136:139]
EX v7,v7
MM v[20:23],a[216:219],v[200:203],v[20:23]
TR a[154:155],v233 offset:20992
CV v3,v6,v7
MM v[20:23],a[212:215],v[184:187],v[20:23]
EX v8,v8
MM v[20:23],a[208:211],v[168:171],v[20:23]
TR a[156:157],v233 offset:25088
TR a[158:159],v233 offset:29184
MM v[28:31],a[208:211],v[172:175],v[140:143]
s_add_u32 m0, s45, 147456
s_add_u32 s44, s44, 1024
buffer_load_dwordx4 v248, s[24:27], s44 offen offset:0 lds
MM v[28:31],a[212:215],v[188:191],v[28:31]
EX v9,v9
MM v[28:31],a[216:219],v[204:207],v[28:31]
TR a[160:161],v232 offset:17408
CV v8,v8,v9
MM v[28:31],a[220:223],v[220:223],v[28:31]
EX v10,v10
MM v[24:27],a[204:207],v[220:223],v[140:143]
EX v11,v11
MM v[24:27],a[200:203],v[204:207],v[24:27]
TR a[162:163],v232 offset:21504
CV v9,v10,v11
MM v[24:27],a[196:199],v[188:191],v[24:27]
EX v12,v12
MM v[24:27],a[192:195],v[172:175],v[24:27]
TR a[164:165],v232 offset:25600
TR a[166:167],v232 offset:29696
MM v[32:35],a[224:227],v[160:163],v[128:131]
EX v13,v13
MM v[32:35],a[228:231],v[176:179],v[32:35]
TR a[168:169],v233 offset:17408
CV v10,v12,v13
MM v[32:35],a[232:235],v[192:195],v[32:35]
EX v14,v14
MM v[32:35],a[236:239],v[208:211],v[32:35]
EX v15,v15
MM v[36:39],a[252:255],v[208:211],v[128:131]
TR a[170:171],v233 offset:21504
CV v11,v14,v15
MM v[36:39],a[248:251],v[192:195],v[36:39]
EX v16,v16
MM v[36:39],a[244:247],v[176:179],v[36:39]
TR a[172:173],v233 offset:25600
TR a[174:175],v233 offset:29696
MM v[36:39],a[240:243],v[160:163],v[36:39]
EX v17,v17
MM v[44:47],a[240:243],v[164:167],v[132:135]
s_add_u32 s67, s95, 15360
TR a[176:177],v232 offset:17920
CV v16,v16,v17
MM v[44:47],a[244:247],v[180:183],v[44:47]
buffer_store_dwordx4 v[240:243], v248, s[20:23], s67 offen offset:0 sc1
MM v[44:47],a[248:251],v[196:199],v[44:47]
EX v18,v18
MM v[44:47],a[252:255],v[212:215],v[44:47]
EX v19,v19
MM v[40:43],a[236:239],v[212:215],v[132:135]
TR a[178:179],v232 offset:22016
CV v17,v18,v19
MM v[40:43],a[232:235],v[196:199],v[40:43]
EX v20,v20
MM v[40:43],a[228:231],v[180:183],v[40:43]
TR a[180:181],v232 offset:26112
TR a[182:183],v232 offset:30208
MM v[40:43],a[224:227],v[164:167],v[40:43]
EX v21,v21
MM v[48:51],a[224:227],v[168:171],v[136:139]
TR a[184:185],v233 offset:17920
CV v18,v20,v21
MM v[48:51],a[228:231],v[184:187],v[48:51]
EX v22,v22
MM v[48:51],a[232:235],v[200:203],v[48:51]
EX v23,v23
MM v[48:51],a[236:239],v[216:219],v[48:51]
TR a[186:187],v233 offset:22016
CV v19,v22,v23
MM v[52:55],a[252:255],v[216:219],v[136:139]
EX v24,v24
MM v[52:55],a[248:251],v[200:203],v[52:55]
TR a[188:189],v233 offset:26112
TR a[190:191],v233 offset:30208
MM v[52:55],a[244:247],v[184:187],v[52:55]
EX v25,v25
MM v[52:55],a[240:243],v[168:171],v[52:55]
EX v26,v26
MM v[60:63],a[240:243],v[172:175],v[140:143]
EX v27,v27
MM v[60:63],a[244:247],v[188:191],v[60:63]
EX v28,v28
MM v[60:63],a[248:251],v[204:207],v[60:63]
EX v29,v29
MM v[60:63],a[252:255],v[220:223],v[60:63]
EX v30,v30
MM v[56:59],a[236:239],v[220:223],v[140:143]
EX v31,v31
MM v[56:59],a[232:235],v[204:207],v[56:59]
EX v32,v32
MM v[56:59],a[228:231],v[188:191],v[56:59]
EX v33,v33
MM v[56:59],a[224:227],v[172:175],v[56:59]
s_waitcnt vmcnt(16) lgkmcnt(0)
s_barrier
MM a[0:3],a[128:131],v[64:67],a[0:3]
s_add_u32 m0, s65, 16384
CV v24,v24,v25
CV v25,v26,v27
MM a[0:3],a[132:135],v[68:71],a[0:3]
RD a[192:195],v224 offset:49152
CV v26,v28,v29
MM a[8:11],a[132:135],v[76:79],a[8:11]
RD a[196:199],v225 offset:49152
CV v27,v30,v31
MM a[8:11],a[128:131],v[72:75],a[8:11]
buffer_load_dwordx4 v228, s[8:11], s64 offen offset:0 lds
MM a[16:19],a[128:131],v[80:83],a[16:19]
RD a[200:203],v226 offset:49152
CV v4,v32,v33
MM a[16:19],a[132:135],v[84:87],a[16:19]
buffer_load_dwordx4 v229, s[8:11], s64 offen offset:1024 lds
MM a[24:27],a[132:135],v[92:95],a[24:27]
EX v34,v34
MM a[24:27],a[128:131],v[88:91],a[24:27]
RD a[204:207],v227 offset:49152
RD a[208:211],v224 offset:53248
MM a[28:31],a[136:139],v[88:91],a[28:31]
)ASM"
R"ASM(
EX v35,v35
MM a[28:31],a[140:143],v[92:95],a[28:31]
RD a[212:215],v225 offset:53248
CV v5,v34,v35
MM a[20:23],a[140:143],v[84:87],a[20:23]
EX v36,v36
MM a[20:23],a[136:139],v[80:83],a[20:23]
EX v37,v37
MM a[12:15],a[136:139],v[72:75],a[12:15]
RD a[216:219],v226 offset:53248
CV v6,v36,v37
MM a[12:15],a[140:143],v[76:79],a[12:15]
buffer_load_dwordx4 v230, s[8:11], s64 offen offset:2048 lds
MM a[4:7],a[140:143],v[68:71],a[4:7]
EX v38,v38
MM a[4:7],a[136:139],v[64:67],a[4:7]
EX v39,v39
MM a[32:35],a[144:147],v[64:67],a[32:35]
RD a[220:223],v227 offset:53248
CV v7,v38,v39
MM a[32:35],a[148:151],v[68:71],a[32:35]
EX v40,v40
MM a[40:43],a[148:151],v[76:79],a[40:43]
EX v41,v41
MM a[40:43],a[144:147],v[72:75],a[40:43]
RD a[224:227],v224 offset:57344
CV v12,v40,v41
MM a[48:51],a[144:147],v[80:83],a[48:51]
buffer_load_dwordx4 v231, s[8:11], s64 offen offset:3072 lds
MM a[48:51],a[148:151],v[84:87],a[48:51]
s_add_u32 m0, s65, 81920
EX v42,v42
MM a[56:59],a[148:151],v[92:95],a[56:59]
EX v43,v43
MM a[56:59],a[144:147],v[88:91],a[56:59]
RD a[228:231],v225 offset:57344
CV v13,v42,v43
MM a[60:63],a[152:155],v[88:91],a[60:63]
EX v44,v44
MM a[60:63],a[156:159],v[92:95],a[60:63]
buffer_load_dwordx4 v235, s[12:15], s81 offen offset:0 lds
MM a[52:55],a[156:159],v[84:87],a[52:55]
EX v45,v45
MM a[52:55],a[152:155],v[80:83],a[52:55]
s_nop 0
RD a[232:235],v226 offset:57344
CV v14,v44,v45
MM a[44:47],a[152:155],v[72:75],a[44:47]
s_nop 0
EX v46,v46
MM a[44:47],a[156:159],v[76:79],a[44:47]
s_nop 0
EX v47,v47
MM a[36:39],a[156:159],v[68:71],a[36:39]
s_nop 0
RD a[236:239],v227 offset:57344
CV v15,v46,v47
MM a[36:39],a[152:155],v[64:67],a[36:39]
LPG14_%=:
s_cbranch_execz LTR2_%=
EX v48,v48
MM a[64:67],a[160:163],v[64:67],a[64:67]
buffer_load_dwordx4 v235, s[12:15], s64 offen offset:1024 lds
MM a[64:67],a[164:167],v[68:71],a[64:67]
EX v49,v49
MM a[72:75],a[164:167],v[76:79],a[72:75]
RD a[240:243],v224 offset:61440
CV v20,v48,v49
MM a[72:75],a[160:163],v[72:75],a[72:75]
EX v50,v50
MM a[80:83],a[160:163],v[80:83],a[80:83]
EX v51,v51
MM a[80:83],a[164:167],v[84:87],a[80:83]
RD a[244:247],v225 offset:61440
CV v21,v50,v51
MM a[88:91],a[164:167],v[92:95],a[88:91]
EX v52,v52
MM a[88:91],a[160:163],v[88:91],a[88:91]
buffer_load_dwordx4 v235, s[12:15], s81 offen offset:2048 lds
MM a[92:95],a[168:171],v[88:91],a[92:95]
EX v53,v53
MM a[92:95],a[172:175],v[92:95],a[92:95]
RD a[248:251],v226 offset:61440
CV v22,v52,v53
MM a[84:87],a[172:175],v[84:87],a[84:87]
EX v54,v54
MM a[84:87],a[168:171],v[80:83],a[84:87]
EX v55,v55
MM a[76:79],a[168:171],v[72:75],a[76:79]
RD a[252:255],v227 offset:61440
CV v23,v54,v55
MM a[76:79],a[172:175],v[76:79],a[76:79]
EX v56,v56
MM a[68:71],a[172:175],v[68:71],a[68:71]
buffer_load_dwordx4 v235, s[12:15], s64 offen offset:3072 lds
MM a[68:71],a[168:171],v[64:67],a[68:71]
EX v57,v57
MM a[96:99],a[176:179],v[64:67],a[96:99]
EX v58,v58
MM a[96:99],a[180:183],v[68:71],a[96:99]
EX v59,v59
MM a[104:107],a[180:183],v[76:79],a[104:107]
EX v60,v60
MM a[104:107],a[176:179],v[72:75],a[104:107]
EX v61,v61
MM a[112:115],a[176:179],v[80:83],a[112:115]
EX v62,v62
MM a[112:115],a[180:183],v[84:87],a[112:115]
EX v63,v63
MM a[120:123],a[180:183],v[92:95],a[120:123]
CV v28,v56,v57
CV v29,v58,v59
MM a[120:123],a[176:179],v[88:91],a[120:123]
CV v30,v60,v61
CV v31,v62,v63
MM a[124:127],a[184:187],v[88:91],a[124:127]
MM a[124:127],a[188:191],v[92:95],a[124:127]
MM a[116:119],a[188:191],v[84:87],a[116:119]
MM a[116:119],a[184:187],v[80:83],a[116:119]
MM a[108:111],a[184:187],v[72:75],a[108:111]
MM a[108:111],a[188:191],v[76:79],a[108:111]
MM a[100:103],a[188:191],v[68:71],a[100:103]
MM a[100:103],a[184:187],v[64:67],a[100:103]
v_smfmac_f32_16x16x64_bf16 v[236:239], v[144:147], v[0:7], v234
v_smfmac_f32_16x16x64_bf16 v[236:239], v[148:151], v[8:15], v234
v_smfmac_f32_16x16x64_bf16 v[236:239], v[152:155], v[16:23], v234
v_smfmac_f32_16x16x64_bf16 v[236:239], v[156:159], v[24:31], v234
s_waitcnt lgkmcnt(0)
MM v[64:67],a[192:195],v[160:163],v[128:131]
s_add_u32 s64, s64, 16384
TR a[128:129],v232 offset:32768
s_add_u32 s81, s64, 896
MM v[64:67],a[196:199],v[176:179],v[64:67]
TR a[130:131],v232 offset:36864
TR a[132:133],v232 offset:40960
MM v[64:67],a[200:203],v[192:195],v[64:67]
TR a[134:135],v232 offset:45056
TR a[136:137],v233 offset:32768
MM v[64:67],a[204:207],v[208:211],v[64:67]
TR a[138:139],v233 offset:36864
v_add_f32 v246, v236, v237
MM v[68:71],a[220:223],v[208:211],v[128:131]
TR a[140:141],v233 offset:40960
v_cmp_nge_f32 vcc, s71, v246
MM v[68:71],a[216:219],v[192:195],v[68:71]
s_cbranch_vccnz LSITE22_%=
LBACK22_%=:
MM v[68:71],a[212:215],v[176:179],v[68:71]
s_add_u32 s67, s95, 16384
TR a[142:143],v233 offset:45056
TR a[144:145],v232 offset:33280
MM v[68:71],a[208:211],v[160:163],v[68:71]
buffer_store_dwordx4 v[240:243], v248, s[20:23], s67 offen offset:0 sc1
MM v[76:79],a[208:211],v[164:167],v[132:135]
EX v64,v64
MM v[76:79],a[212:215],v[180:183],v[76:79]
EX v65,v65
MM v[76:79],a[216:219],v[196:199],v[76:79]
TR a[146:147],v232 offset:37376
CV v64,v64,v65
MM v[76:79],a[220:223],v[212:215],v[76:79]
EX v66,v66
MM v[72:75],a[204:207],v[212:215],v[132:135]
EX v67,v67
MM v[72:75],a[200:203],v[196:199],v[72:75]
TR a[148:149],v232 offset:41472
CV v65,v66,v67
MM v[72:75],a[196:199],v[180:183],v[72:75]
EX v68,v68
MM v[72:75],a[192:195],v[164:167],v[72:75]
TR a[150:151],v232 offset:45568
TR a[152:153],v233 offset:33280
MM v[80:83],a[192:195],v[168:171],v[136:139]
EX v69,v69
MM v[80:83],a[196:199],v[184:187],v[80:83]
TR a[154:155],v233 offset:37376
CV v66,v68,v69
MM v[80:83],a[200:203],v[200:203],v[80:83]
EX v70,v70
MM v[80:83],a[204:207],v[216:219],v[80:83]
EX v71,v71
MM v[84:87],a[220:223],v[216:219],v[136:139]
TR a[156:157],v233 offset:41472
CV v67,v70,v71
MM v[84:87],a[216:219],v[200:203],v[84:87]
EX v72,v72
MM v[84:87],a[212:215],v[184:187],v[84:87]
TR a[158:159],v233 offset:45568
TR a[160:161],v232 offset:33792
MM v[84:87],a[208:211],v[168:171],v[84:87]
EX v73,v73
MM v[92:95],a[208:211],v[172:175],v[140:143]
TR a[162:163],v232 offset:37888
CV v72,v72,v73
MM v[92:95],a[212:215],v[188:191],v[92:95]
EX v74,v74
MM v[92:95],a[216:219],v[204:207],v[92:95]
EX v75,v75
MM v[92:95],a[220:223],v[220:223],v[92:95]
TR a[164:165],v232 offset:41984
CV v73,v74,v75
MM v[88:91],a[204:207],v[220:223],v[140:143]
EX v76,v76
MM v[88:91],a[200:203],v[204:207],v[88:91]
TR a[166:167],v232 offset:46080
TR a[168:169],v233 offset:33792
MM v[88:91],a[196:199],v[188:191],v[88:91]
EX v77,v77
MM v[88:91],a[192:195],v[172:175],v[88:91]
TR a[170:171],v233 offset:37888
CV v74,v76,v77
MM v[96:99],a[224:227],v[160:163],v[128:131]
EX v78,v78
MM v[96:99],a[228:231],v[176:179],v[96:99]
EX v79,v79
MM v[96:99],a[232:235],v[192:195],v[96:99]
TR a[172:173],v233 offset:41984
CV v75,v78,v79
MM v[96:99],a[236:239],v[208:211],v[96:99]
TR a[174:175],v233 offset:46080
TR a[176:177],v232 offset:34304
MM v[100:103],a[252:255],v[208:211],v[128:131]
EX v80,v80
MM v[100:103],a[248:251],v[192:195],v[100:103]
EX v81,v81
MM v[100:103],a[244:247],v[176:179],v[100:103]
TR a[178:179],v232 offset:38400
CV v80,v80,v81
MM v[100:103],a[240:243],v[160:163],v[100:103]
EX v82,v82
MM v[108:111],a[240:243],v[164:167],v[132:135]
s_add_u32 s67, s95, 17408
TR a[180:181],v232 offset:42496
TR a[182:183],v232 offset:46592
MM v[108:111],a[244:247],v[180:183],v[108:111]
buffer_store_dwordx4 v[240:243], v248, s[20:23], s67 offen offset:0 sc1
MM v[108:111],a[248:251],v[196:199],v[108:111]
EX v83,v83
MM v[108:111],a[252:255],v[212:215],v[108:111]
TR a[184:185],v233 offset:34304
CV v81,v82,v83
MM v[104:107],a[236:239],v[212:215],v[132:135]
EX v84,v84
MM v[104:107],a[232:235],v[196:199],v[104:107]
EX v85,v85
MM v[104:107],a[228:231],v[180:183],v[104:107]
TR a[186:187],v233 offset:38400
CV v82,v84,v85
MM v[104:107],a[224:227],v[164:167],v[104:107]
EX v86,v86
MM v[112:115],a[224:227],v[168:171],v[136:139]
TR a[188:189],v233 offset:42496
TR a[190:191],v233 offset:46592
MM v[112:115],a[228:231],v[184:187],v[112:115]
EX v87,v87
MM v[112:115],a[232:235],v[200:203],v[112:115]
CV v83,v86,v87
MM v[112:115],a[236:239],v[216:219],v[112:115]
EX v88,v88
MM v[116:119],a[252:255],v[216:219],v[136:139]
EX v89,v89
MM v[116:119],a[248:251],v[200:203],v[116:119]
EX v90,v90
MM v[116:119],a[244:247],v[184:187],v[116:119]
EX v91,v91
MM v[116:119],a[240:243],v[168:171],v[116:119]
EX v92,v92
MM v[124:127],a[240:243],v[172:175],v[140:143]
EX v93,v93
MM v[124:127],a[244:247],v[188:191],v[124:127]
EX v94,v94
MM v[124:127],a[248:251],v[204:207],v[124:127]
EX v95,v95
MM v[124:127],a[252:255],v[220:223],v[124:127]
EX v96,v96
MM v[120:123],a[236:239],v[220:223],v[140:143]
EX v97,v97
MM v[120:123],a[232:235],v[204:207],v[120:123]
EX v98,v98
MM v[120:123],a[228:231],v[188:191],v[120:123]
EX v99,v99
MM v[120:123],a[224:227],v[172:175],v[120:123]
s_waitcnt vmcnt(17) lgkmcnt(0)
s_barrier
MM a[0:3],a[128:131],v[0:3],a[0:3]
s_lshl_b32 s91, %[swave], 2
CV v88,v88,v89
CV v89,v90,v91
MM a[0:3],a[132:135],v[4:7],a[0:3]
s_add_u32 s91, s91, s48
RD a[192:195],v224 offset:0
CV v90,v92,v93
MM a[8:11],a[132:135],v[12:15],a[8:11]
s_add_u32 s91, s91, 69632
s_add_u32 m0, s65, 32768
CV v91,v94,v95
MM a[8:11],a[128:131],v[8:11],a[8:11]
buffer_load_dwordx4 v228, s[8:11], s64 offen offset:0 lds
MM a[16:19],a[128:131],v[16:19],a[16:19]
v_mov_b32 v250, s69
s_mov_b64 exec, 1
buffer_store_dword v250, off, s[20:23], s91 offset:0 sc1
s_mov_b64 exec, -1
MM a[16:19],a[132:135],v[20:23],a[16:19]
RD a[196:199],v225 offset:0
CV v68,v96,v97
MM a[24:27],a[132:135],v[28:31],a[24:27]
RD a[200:203],v226 offset:0
CV v69,v98,v99
MM a[24:27],a[128:131],v[24:27],a[24:27]
buffer_load_dwordx4 v229, s[8:11], s64 offen offset:1024 lds
MM a[28:31],a[136:139],v[24:27],a[28:31]
RD a[204:207],v227 offset:0
RD a[208:211],v224 offset:4096
MM a[28:31],a[140:143],v[28:31],a[28:31]
EX v100,v100
MM a[20:23],a[140:143],v[20:23],a[20:23]
buffer_load_dwordx4 v230, s[8:11], s64 offen offset:2048 lds
MM a[20:23],a[136:139],v[16:19],a[20:23]
EX v101,v101
MM a[12:15],a[136:139],v[8:11],a[12:15]
RD a[212:215],v225 offset:4096
CV v70,v100,v101
MM a[12:15],a[140:143],v[12:15],a[12:15]
EX v102,v102
MM a[4:7],a[140:143],v[4:7],a[4:7]
RD a[216:219],v226 offset:4096
RD a[220:223],v227 offset:4096
MM a[4:7],a[136:139],v[0:3],a[4:7]
EX v103,v103
MM a[32:35],a[144:147],v[0:3],a[32:35]
RD a[224:227],v224 offset:8192
CV v71,v102,v103
MM a[32:35],a[148:151],v[4:7],a[32:35]
EX v104,v104
MM a[40:43],a[148:151],v[12:15],a[40:43]
EX v105,v105
MM a[40:43],a[144:147],v[8:11],a[40:43]
RD a[228:231],v225 offset:8192
CV v76,v104,v105
MM a[48:51],a[144:147],v[16:19],a[48:51]
buffer_load_dwordx4 v231, s[8:11], s64 offen offset:3072 lds
MM a[48:51],a[148:151],v[20:23],a[48:51]
s_add_u32 m0, s65, 98304
EX v106,v106
MM a[56:59],a[148:151],v[28:31],a[56:59]
EX v107,v107
MM a[56:59],a[144:147],v[24:27],a[56:59]
RD a[232:235],v226 offset:8192
CV v77,v106,v107
MM a[60:63],a[152:155],v[24:27],a[60:63]
EX v108,v108
MM a[60:63],a[156:159],v[28:31],a[60:63]
buffer_load_dwordx4 v235, s[12:15], s81 offen offset:0 lds
MM a[52:55],a[156:159],v[20:23],a[52:55]
EX v109,v109
MM a[52:55],a[152:155],v[16:19],a[52:55]
RD a[236:239],v227 offset:8192
CV v78,v108,v109
MM a[44:47],a[152:155],v[8:11],a[44:47]
EX v110,v110
MM a[44:47],a[156:159],v[12:15],a[44:47]
EX v111,v111
MM a[36:39],a[156:159],v[4:7],a[36:39]
RD a[240:243],v224 offset:12288
CV v79,v110,v111
MM a[36:39],a[152:155],v[0:3],a[36:39]
EX v112,v112
MM a[64:67],a[160:163],v[0:3],a[64:67]
buffer_load_dwordx4 v235, s[12:15], s64 offen offset:1024 lds
MM a[64:67],a[164:167],v[4:7],a[64:67]
EX v113,v113
MM a[72:75],a[164:167],v[12:15],a[72:75]
RD a[244:247],v225 offset:12288
CV v84,v112,v113
MM a[72:75],a[160:163],v[8:11],a[72:75]
EX v114,v114
MM a[80:83],a[160:163],v[16:19],a[80:83]
EX v115,v115
MM a[80:83],a[164:167],v[20:23],a[80:83]
RD a[248:251],v226 offset:12288
CV v85,v114,v115
MM a[88:91],a[164:167],v[28:31],a[88:91]
EX v116,v116
MM a[88:91],a[160:163],v[24:27],a[88:91]
buffer_load_dwordx4 v235, s[12:15], s81 offen offset:2048 lds
MM a[92:95],a[168:171],v[24:27],a[92:95]
)ASM"
R"ASM(
EX v117,v117
MM a[92:95],a[172:175],v[28:31],a[92:95]
RD a[252:255],v227 offset:12288
CV v86,v116,v117
MM a[84:87],a[172:175],v[20:23],a[84:87]
EX v118,v118
MM a[84:87],a[168:171],v[16:19],a[84:87]
EX v119,v119
MM a[76:79],a[168:171],v[8:11],a[76:79]
CV v87,v118,v119
MM a[76:79],a[172:175],v[12:15],a[76:79]
EX v120,v120
MM a[68:71],a[172:175],v[4:7],a[68:71]
buffer_load_dwordx4 v235, s[12:15], s64 offen offset:3072 lds
MM a[68:71],a[168:171],v[0:3],a[68:71]
EX v121,v121
MM a[96:99],a[176:179],v[0:3],a[96:99]
EX v122,v122
MM a[96:99],a[180:183],v[4:7],a[96:99]
EX v123,v123
MM a[104:107],a[180:183],v[12:15],a[104:107]
EX v124,v124
MM a[104:107],a[176:179],v[8:11],a[104:107]
EX v125,v125
MM a[112:115],a[176:179],v[16:19],a[112:115]
EX v126,v126
MM a[112:115],a[180:183],v[20:23],a[112:115]
EX v127,v127
MM a[120:123],a[180:183],v[28:31],a[120:123]
CV v92,v120,v121
CV v93,v122,v123
MM a[120:123],a[176:179],v[24:27],a[120:123]
CV v94,v124,v125
CV v95,v126,v127
MM a[124:127],a[184:187],v[24:27],a[124:127]
MM a[124:127],a[188:191],v[28:31],a[124:127]
MM a[116:119],a[188:191],v[20:23],a[116:119]
MM a[116:119],a[184:187],v[16:19],a[116:119]
MM a[108:111],a[184:187],v[8:11],a[108:111]
MM a[108:111],a[188:191],v[12:15],a[108:111]
MM a[100:103],a[188:191],v[4:7],a[100:103]
MM a[100:103],a[184:187],v[0:3],a[100:103]
v_smfmac_f32_16x16x64_bf16 v[236:239], v[144:147], v[64:71], v234
v_smfmac_f32_16x16x64_bf16 v[236:239], v[148:151], v[72:79], v234
v_smfmac_f32_16x16x64_bf16 v[236:239], v[152:155], v[80:87], v234
v_smfmac_f32_16x16x64_bf16 v[236:239], v[156:159], v[88:95], v234
s_waitcnt lgkmcnt(0)
MM v[0:3],a[192:195],v[160:163],v[128:131]
s_add_u32 s64, s64, 16384
TR a[128:129],v232 offset:49152
s_add_u32 s81, s64, 896
MM v[0:3],a[196:199],v[176:179],v[0:3]
TR a[130:131],v232 offset:53248
TR a[132:133],v232 offset:57344
MM v[0:3],a[200:203],v[192:195],v[0:3]
TR a[134:135],v232 offset:61440
TR a[136:137],v233 offset:49152
MM v[0:3],a[204:207],v[208:211],v[0:3]
TR a[138:139],v233 offset:53248
v_add_f32 v247, v236, v237
MM v[4:7],a[220:223],v[208:211],v[128:131]
TR a[140:141],v233 offset:57344
v_cmp_nge_f32 vcc, s71, v247
MM v[4:7],a[216:219],v[192:195],v[4:7]
s_cbranch_vccnz LSITE23_%=
LBACK23_%=:
MM v[4:7],a[212:215],v[176:179],v[4:7]
s_add_u32 s67, s95, 18432
TR a[142:143],v233 offset:61440
TR a[144:145],v232 offset:49664
MM v[4:7],a[208:211],v[160:163],v[4:7]
buffer_store_dwordx4 v[240:243], v248, s[20:23], s67 offen offset:0 sc1
MM v[12:15],a[208:211],v[164:167],v[132:135]
EX v0,v0
MM v[12:15],a[212:215],v[180:183],v[12:15]
EX v1,v1
MM v[12:15],a[216:219],v[196:199],v[12:15]
TR a[146:147],v232 offset:53760
CV v0,v0,v1
MM v[12:15],a[220:223],v[212:215],v[12:15]
EX v2,v2
MM v[8:11],a[204:207],v[212:215],v[132:135]
EX v3,v3
MM v[8:11],a[200:203],v[196:199],v[8:11]
TR a[148:149],v232 offset:57856
CV v1,v2,v3
MM v[8:11],a[196:199],v[180:183],v[8:11]
EX v4,v4
MM v[8:11],a[192:195],v[164:167],v[8:11]
TR a[150:151],v232 offset:61952
TR a[152:153],v233 offset:49664
MM v[16:19],a[192:195],v[168:171],v[136:139]
EX v5,v5
MM v[16:19],a[196:199],v[184:187],v[16:19]
TR a[154:155],v233 offset:53760
CV v2,v4,v5
MM v[16:19],a[200:203],v[200:203],v[16:19]
EX v6,v6
MM v[16:19],a[204:207],v[216:219],v[16:19]
EX v7,v7
MM v[20:23],a[220:223],v[216:219],v[136:139]
TR a[156:157],v233 offset:57856
CV v3,v6,v7
MM v[20:23],a[216:219],v[200:203],v[20:23]
EX v8,v8
MM v[20:23],a[212:215],v[184:187],v[20:23]
TR a[158:159],v233 offset:61952
TR a[160:161],v232 offset:50176
MM v[20:23],a[208:211],v[168:171],v[20:23]
EX v9,v9
MM v[28:31],a[208:211],v[172:175],v[140:143]
s_add_u32 m0, s45, 147456
s_add_u32 s44, s44, 1024
buffer_load_dwordx4 v248, s[24:27], s44 offen offset:0 lds
MM v[28:31],a[212:215],v[188:191],v[28:31]
TR a[162:163],v232 offset:54272
CV v8,v8,v9
MM v[28:31],a[216:219],v[204:207],v[28:31]
EX v10,v10
MM v[28:31],a[220:223],v[220:223],v[28:31]
EX v11,v11
MM v[24:27],a[204:207],v[220:223],v[140:143]
TR a[164:165],v232 offset:58368
CV v9,v10,v11
MM v[24:27],a[200:203],v[204:207],v[24:27]
EX v12,v12
MM v[24:27],a[196:199],v[188:191],v[24:27]
TR a[166:167],v232 offset:62464
TR a[168:169],v233 offset:50176
MM v[24:27],a[192:195],v[172:175],v[24:27]
EX v13,v13
MM v[32:35],a[224:227],v[160:163],v[128:131]
TR a[170:171],v233 offset:54272
CV v10,v12,v13
MM v[32:35],a[228:231],v[176:179],v[32:35]
EX v14,v14
MM v[32:35],a[232:235],v[192:195],v[32:35]
EX v15,v15
MM v[32:35],a[236:239],v[208:211],v[32:35]
TR a[172:173],v233 offset:58368
CV v11,v14,v15
MM v[36:39],a[252:255],v[208:211],v[128:131]
EX v16,v16
MM v[36:39],a[248:251],v[192:195],v[36:39]
TR a[174:175],v233 offset:62464
TR a[176:177],v232 offset:50688
MM v[36:39],a[244:247],v[176:179],v[36:39]
EX v17,v17
MM v[36:39],a[240:243],v[160:163],v[36:39]
TR a[178:179],v232 offset:54784
CV v16,v16,v17
MM v[44:47],a[240:243],v[164:167],v[132:135]
s_add_u32 s67, s95, 19456
EX v18,v18
MM v[44:47],a[244:247],v[180:183],v[44:47]
buffer_store_dwordx4 v[240:243], v248, s[20:23], s67 offen offset:0 sc1
MM v[44:47],a[248:251],v[196:199],v[44:47]
EX v19,v19
MM v[44:47],a[252:255],v[212:215],v[44:47]
TR a[180:181],v232 offset:58880
CV v17,v18,v19
MM v[40:43],a[236:239],v[212:215],v[132:135]
EX v20,v20
MM v[40:43],a[232:235],v[196:199],v[40:43]
TR a[182:183],v232 offset:62976
TR a[184:185],v233 offset:50688
MM v[40:43],a[228:231],v[180:183],v[40:43]
EX v21,v21
MM v[40:43],a[224:227],v[164:167],v[40:43]
TR a[186:187],v233 offset:54784
CV v18,v20,v21
MM v[48:51],a[224:227],v[168:171],v[136:139]
EX v22,v22
MM v[48:51],a[228:231],v[184:187],v[48:51]
EX v23,v23
MM v[48:51],a[232:235],v[200:203],v[48:51]
TR a[188:189],v233 offset:58880
CV v19,v22,v23
MM v[48:51],a[236:239],v[216:219],v[48:51]
EX v24,v24
MM v[52:55],a[252:255],v[216:219],v[136:139]
EX v25,v25
MM v[52:55],a[248:251],v[200:203],v[52:55]
TR a[190:191],v233 offset:62976
CV v24,v24,v25
MM v[52:55],a[244:247],v[184:187],v[52:55]
EX v26,v26
MM v[52:55],a[240:243],v[168:171],v[52:55]
EX v27,v27
MM v[60:63],a[240:243],v[172:175],v[140:143]
EX v28,v28
MM v[60:63],a[244:247],v[188:191],v[60:63]
EX v29,v29
MM v[60:63],a[248:251],v[204:207],v[60:63]
EX v30,v30
MM v[60:63],a[252:255],v[220:223],v[60:63]
EX v31,v31
MM v[56:59],a[236:239],v[220:223],v[140:143]
EX v32,v32
MM v[56:59],a[232:235],v[204:207],v[56:59]
s_nop 0
EX v33,v33
MM v[56:59],a[228:231],v[188:191],v[56:59]
s_nop 0
EX v34,v34
MM v[56:59],a[224:227],v[172:175],v[56:59]
s_waitcnt vmcnt(18) lgkmcnt(0)
s_barrier
MM a[0:3],a[128:131],v[64:67],a[0:3]
s_add_u32 m0, s65, 49152
CV v25,v26,v27
CV v26,v28,v29
MM a[0:3],a[132:135],v[68:71],a[0:3]
s_nop 0
RD a[192:195],v224 offset:16384
CV v27,v30,v31
MM a[8:11],a[132:135],v[76:79],a[8:11]
buffer_load_dwordx4 v228, s[8:11], s64 offen offset:0 lds
MM a[8:11],a[128:131],v[72:75],a[8:11]
s_nop 0
RD a[196:199],v225 offset:16384
CV v4,v32,v33
MM a[16:19],a[128:131],v[80:83],a[16:19]
s_nop 0
RD a[200:203],v226 offset:16384
RD a[204:207],v227 offset:16384
MM a[16:19],a[132:135],v[84:87],a[16:19]
buffer_load_dwordx4 v229, s[8:11], s64 offen offset:1024 lds
MM a[24:27],a[132:135],v[92:95],a[24:27]
s_nop 0
RD a[208:211],v224 offset:20480
RD a[212:215],v225 offset:20480
MM a[24:27],a[128:131],v[88:91],a[24:27]
s_nop 0
EX v35,v35
MM a[28:31],a[136:139],v[88:91],a[28:31]
s_nop 0
RD a[216:219],v226 offset:20480
CV v5,v34,v35
MM a[28:31],a[140:143],v[92:95],a[28:31]
s_nop 0
EX v36,v36
MM a[20:23],a[140:143],v[84:87],a[20:23]
buffer_load_dwordx4 v230, s[8:11], s64 offen offset:2048 lds
MM a[20:23],a[136:139],v[80:83],a[20:23]
s_nop 0
EX v37,v37
MM a[12:15],a[136:139],v[72:75],a[12:15]
s_nop 0
RD a[220:223],v227 offset:20480
CV v6,v36,v37
MM a[12:15],a[140:143],v[76:79],a[12:15]
s_nop 0
EX v38,v38
MM a[4:7],a[140:143],v[68:71],a[4:7]
s_nop 0
EX v39,v39
MM a[4:7],a[136:139],v[64:67],a[4:7]
s_nop 0
RD a[224:227],v224 offset:24576
CV v7,v38,v39
MM a[32:35],a[144:147],v[64:67],a[32:35]
LPG15_%=:
s_cbranch_execz LTR2_%=
EX v40,v40
MM a[32:35],a[148:151],v[68:71],a[32:35]
buffer_load_dwordx4 v231, s[8:11], s64 offen offset:3072 lds
MM a[40:43],a[148:151],v[76:79],a[40:43]
s_add_u32 m0, s65, 114688
EX v41,v41
MM a[40:43],a[144:147],v[72:75],a[40:43]
RD a[228:231],v225 offset:24576
CV v12,v40,v41
MM a[48:51],a[144:147],v[80:83],a[48:51]
EX v42,v42
MM a[48:51],a[148:151],v[84:87],a[48:51]
EX v43,v43
MM a[56:59],a[148:151],v[92:95],a[56:59]
RD a[232:235],v226 offset:24576
CV v13,v42,v43
MM a[56:59],a[144:147],v[88:91],a[56:59]
EX v44,v44
MM a[60:63],a[152:155],v[88:91],a[60:63]
buffer_load_dwordx4 v235, s[12:15], s81 offen offset:0 lds
MM a[60:63],a[156:159],v[92:95],a[60:63]
EX v45,v45
MM a[52:55],a[156:159],v[84:87],a[52:55]
RD a[236:239],v227 offset:24576
CV v14,v44,v45
MM a[52:55],a[152:155],v[80:83],a[52:55]
EX v46,v46
MM a[44:47],a[152:155],v[72:75],a[44:47]
EX v47,v47
MM a[44:47],a[156:159],v[76:79],a[44:47]
RD a[240:243],v224 offset:28672
CV v15,v46,v47
MM a[36:39],a[156:159],v[68:71],a[36:39]
buffer_load_dwordx4 v235, s[12:15], s64 offen offset:1024 lds
MM a[36:39],a[152:155],v[64:67],a[36:39]
EX v48,v48
MM a[64:67],a[160:163],v[64:67],a[64:67]
EX v49,v49
MM a[64:67],a[164:167],v[68:71],a[64:67]
RD a[244:247],v225 offset:28672
CV v20,v48,v49
MM a[72:75],a[164:167],v[76:79],a[72:75]
EX v50,v50
MM a[72:75],a[160:163],v[72:75],a[72:75]
EX v51,v51
MM a[80:83],a[160:163],v[80:83],a[80:83]
RD a[248:251],v226 offset:28672
CV v21,v50,v51
MM a[80:83],a[164:167],v[84:87],a[80:83]
buffer_load_dwordx4 v235, s[12:15], s81 offen offset:2048 lds
MM a[88:91],a[164:167],v[92:95],a[88:91]
EX v52,v52
MM a[88:91],a[160:163],v[88:91],a[88:91]
EX v53,v53
MM a[92:95],a[168:171],v[88:91],a[92:95]
RD a[252:255],v227 offset:28672
CV v22,v52,v53
MM a[92:95],a[172:175],v[92:95],a[92:95]
EX v54,v54
MM a[84:87],a[172:175],v[84:87],a[84:87]
buffer_load_dwordx4 v235, s[12:15], s64 offen offset:3072 lds
MM a[84:87],a[168:171],v[80:83],a[84:87]
EX v55,v55
MM a[76:79],a[168:171],v[72:75],a[76:79]
CV v23,v54,v55
MM a[76:79],a[172:175],v[76:79],a[76:79]
EX v56,v56
MM a[68:71],a[172:175],v[68:71],a[68:71]
EX v57,v57
MM a[68:71],a[168:171],v[64:67],a[68:71]
EX v58,v58
MM a[96:99],a[176:179],v[64:67],a[96:99]
EX v59,v59
MM a[96:99],a[180:183],v[68:71],a[96:99]
EX v60,v60
MM a[104:107],a[180:183],v[76:79],a[104:107]
EX v61,v61
MM a[104:107],a[176:179],v[72:75],a[104:107]
EX v62,v62
MM a[112:115],a[176:179],v[80:83],a[112:115]
EX v63,v63
MM a[112:115],a[180:183],v[84:87],a[112:115]
CV v28,v56,v57
CV v29,v58,v59
MM a[120:123],a[180:183],v[92:95],a[120:123]
CV v30,v60,v61
CV v31,v62,v63
MM a[120:123],a[176:179],v[88:91],a[120:123]
MM a[124:127],a[184:187],v[88:91],a[124:127]
MM a[124:127],a[188:191],v[92:95],a[124:127]
MM a[116:119],a[188:191],v[84:87],a[116:119]
MM a[116:119],a[184:187],v[80:83],a[116:119]
MM a[108:111],a[184:187],v[72:75],a[108:111]
MM a[108:111],a[188:191],v[76:79],a[108:111]
MM a[100:103],a[188:191],v[68:71],a[100:103]
MM a[100:103],a[184:187],v[64:67],a[100:103]
v_smfmac_f32_16x16x64_bf16 v[236:239], v[144:147], v[0:7], v234
v_smfmac_f32_16x16x64_bf16 v[236:239], v[148:151], v[8:15], v234
v_smfmac_f32_16x16x64_bf16 v[236:239], v[152:155], v[16:23], v234
v_smfmac_f32_16x16x64_bf16 v[236:239], v[156:159], v[24:31], v234
s_waitcnt lgkmcnt(0)
MM v[64:67],a[192:195],v[160:163],v[128:131]
s_add_u32 s64, s64, 16384
TR a[128:129],v232 offset:0
s_add_u32 s81, s64, 896
MM v[64:67],a[196:199],v[176:179],v[64:67]
TR a[130:131],v232 offset:4096
TR a[132:133],v232 offset:8192
MM v[64:67],a[200:203],v[192:195],v[64:67]
TR a[134:135],v232 offset:12288
TR a[136:137],v233 offset:0
MM v[64:67],a[204:207],v[208:211],v[64:67]
TR a[138:139],v233 offset:4096
v_add_f32 v246, v236, v237
MM v[68:71],a[220:223],v[208:211],v[128:131]
TR a[140:141],v233 offset:8192
v_cmp_nge_f32 vcc, s71, v246
MM v[68:71],a[216:219],v[192:195],v[68:71]
s_cbranch_vccnz LSITE24_%=
LBACK24_%=:
MM v[68:71],a[212:215],v[176:179],v[68:71]
v_bfe_u32 v242, v248, 6, 3
v_mul_u32_u24 v242, 1280, v242
MM v[68:71],a[208:211],v[160:163],v[68:71]
v_and_b32 v240, 48, v248
v_bfe_u32 v243, v248, 8, 1
MM v[76:79],a[208:211],v[164:167],v[132:135]
)ASM"
R"ASM(
v_lshlrev_b32 v243, 5, v243
v_xor_b32 v240, v240, v243
MM v[76:79],a[212:215],v[180:183],v[76:79]
v_add_u32 v240, v240, v242
v_lshrrev_b32 v243, 9, v248
MM v[76:79],a[216:219],v[196:199],v[76:79]
v_mul_u32_u24 v242, 10240, v243
v_add_u32 v241, v240, v242
MM v[76:79],a[220:223],v[212:215],v[76:79]
TR a[142:143],v233 offset:12288
v_lshl_add_u32 v240, v243, 6, v240
MM v[72:75],a[204:207],v[212:215],v[132:135]
s_and_b32 s66, s84, 1
EX v64,v64
MM v[72:75],a[200:203],v[196:199],v[72:75]
s_lshl_b32 s94, s66, 6
TR a[144:145],v232 offset:512
v_add_u32 v240, s94, v240
MM v[72:75],a[196:199],v[180:183],v[72:75]
s_xor_b32 s66, s66, 1
EX v65,v65
MM v[72:75],a[192:195],v[164:167],v[72:75]
s_lshl_b32 s66, s66, 8
v_add_u32 v241, s66, v241
CV v64,v64,v65
MM v[80:83],a[192:195],v[168:171],v[136:139]
s_mul_i32 s89, %[swave], 20480
EX v66,v66
MM v[80:83],a[196:199],v[184:187],v[80:83]
s_lshl_b32 s94, %[swave], 10
EX v67,v67
MM v[80:83],a[200:203],v[200:203],v[80:83]
s_add_u32 s94, s94, %[slds]
EX v68,v68
MM v[80:83],a[204:207],v[216:219],v[80:83]
s_xor_b32 s66, s84, 0
EX v69,v69
MM v[84:87],a[220:223],v[216:219],v[136:139]
s_mul_i32 s66, s66, 163840
TR a[146:147],v232 offset:4608
CV v65,v66,v67
MM v[84:87],a[216:219],v[200:203],v[84:87]
s_add_u32 s85, s66, s89
TR a[148:149],v232 offset:8704
TR a[150:151],v232 offset:12800
MM v[84:87],a[212:215],v[184:187],v[84:87]
s_xor_b32 s66, s84, 1
EX v70,v70
MM v[84:87],a[208:211],v[168:171],v[84:87]
s_mul_i32 s66, s66, 163840
EX v71,v71
MM v[92:95],a[208:211],v[172:175],v[140:143]
s_add_u32 s86, s66, s89
s_add_u32 m0, s45, 147456
s_add_u32 s44, s44, 1024
buffer_load_dwordx4 v248, s[24:27], s44 offen offset:0 lds
MM v[92:95],a[212:215],v[188:191],v[92:95]
s_xor_b32 s66, s84, 2
EX v72,v72
MM v[92:95],a[216:219],v[204:207],v[92:95]
s_mul_i32 s66, s66, 163840
TR a[152:153],v233 offset:512
CV v66,v68,v69
MM v[92:95],a[220:223],v[220:223],v[92:95]
s_add_u32 s87, s66, s89
EX v73,v73
MM v[88:91],a[204:207],v[220:223],v[140:143]
s_xor_b32 s66, s84, 3
TR a[154:155],v233 offset:4608
TR a[156:157],v233 offset:8704
MM v[88:91],a[200:203],v[204:207],v[88:91]
s_mul_i32 s66, s66, 163840
EX v74,v74
MM v[88:91],a[196:199],v[188:191],v[88:91]
s_add_u32 s88, s66, s89
TR a[158:159],v233 offset:12800
CV v67,v70,v71
MM v[88:91],a[192:195],v[172:175],v[88:91]
s_lshl_b32 s89, %[swave], 11
EX v75,v75
MM v[96:99],a[224:227],v[160:163],v[128:131]
s_add_u32 s89, s89, %[slds]
TR a[160:161],v232 offset:1024
CV v72,v72,v73
MM v[96:99],a[228:231],v[176:179],v[96:99]
s_lshr_b32 s96, s49, 2
EX v76,v76
MM v[96:99],a[232:235],v[192:195],v[96:99]
s_lshl_b32 s97, s50, 3
TR a[162:163],v232 offset:5120
CV v73,v74,v75
MM v[96:99],a[236:239],v[208:211],v[96:99]
s_add_u32 s96, s96, s97
EX v77,v77
MM v[100:103],a[252:255],v[208:211],v[128:131]
s_lshl_b32 s96, s96, 2
TR a[164:165],v232 offset:9216
TR a[166:167],v232 offset:13312
MM v[100:103],a[248:251],v[192:195],v[100:103]
s_add_u32 s96, s96, %[swave]
EX v78,v78
MM v[100:103],a[244:247],v[176:179],v[100:103]
s_lshl_b32 s96, s96, 6
EX v79,v79
MM v[100:103],a[240:243],v[160:163],v[100:103]
s_add_u32 s96, s96, 0
TR a[168:169],v233 offset:1024
CV v74,v76,v77
MM v[108:111],a[240:243],v[164:167],v[132:135]
buffer_load_dword v242, off, s[20:23], s96 sc1
MM v[108:111],a[244:247],v[180:183],v[108:111]
buffer_load_dword v243, off, s[20:23], s19 sc1
MM v[108:111],a[248:251],v[196:199],v[108:111]
EX v80,v80
MM v[108:111],a[252:255],v[212:215],v[108:111]
EX v81,v81
MM v[104:107],a[236:239],v[212:215],v[132:135]
TR a[170:171],v233 offset:5120
TR a[172:173],v233 offset:9216
MM v[104:107],a[232:235],v[196:199],v[104:107]
EX v82,v82
MM v[104:107],a[228:231],v[180:183],v[104:107]
EX v83,v83
MM v[104:107],a[224:227],v[164:167],v[104:107]
TR a[174:175],v233 offset:13312
CV v75,v78,v79
MM v[112:115],a[224:227],v[168:171],v[136:139]
EX v84,v84
MM v[112:115],a[228:231],v[184:187],v[112:115]
TR a[176:177],v232 offset:1536
TR a[178:179],v232 offset:5632
MM v[112:115],a[232:235],v[200:203],v[112:115]
EX v85,v85
MM v[112:115],a[236:239],v[216:219],v[112:115]
TR a[180:181],v232 offset:9728
CV v80,v80,v81
MM v[116:119],a[252:255],v[216:219],v[136:139]
EX v86,v86
MM v[116:119],a[248:251],v[200:203],v[116:119]
TR a[182:183],v232 offset:13824
CV v81,v82,v83
MM v[116:119],a[244:247],v[184:187],v[116:119]
EX v87,v87
MM v[116:119],a[240:243],v[168:171],v[116:119]
TR a[184:185],v233 offset:1536
CV v82,v84,v85
MM v[124:127],a[240:243],v[172:175],v[140:143]
EX v88,v88
MM v[124:127],a[244:247],v[188:191],v[124:127]
TR a[186:187],v233 offset:5632
TR a[188:189],v233 offset:9728
MM v[124:127],a[248:251],v[204:207],v[124:127]
TR a[190:191],v233 offset:13824
CV v83,v86,v87
MM v[124:127],a[252:255],v[220:223],v[124:127]
EX v89,v89
MM v[120:123],a[236:239],v[220:223],v[140:143]
EX v90,v90
MM v[120:123],a[232:235],v[204:207],v[120:123]
EX v91,v91
MM v[120:123],a[228:231],v[188:191],v[120:123]
EX v92,v92
MM v[120:123],a[224:227],v[172:175],v[120:123]
s_waitcnt vmcnt(18) lgkmcnt(0)
s_barrier
MM a[0:3],a[128:131],v[0:3],a[0:3]
s_add_u32 m0, s65, 0
CV v88,v88,v89
CV v89,v90,v91
MM a[0:3],a[132:135],v[4:7],a[0:3]
EX v93,v93
MM a[8:11],a[132:135],v[12:15],a[8:11]
EX v94,v94
MM a[8:11],a[128:131],v[8:11],a[8:11]
EX v95,v95
MM a[16:19],a[128:131],v[16:19],a[16:19]
RD a[192:195],v224 offset:32768
CV v90,v92,v93
MM a[16:19],a[132:135],v[20:23],a[16:19]
EX v96,v96
MM a[24:27],a[132:135],v[28:31],a[24:27]
RD a[196:199],v225 offset:32768
CV v91,v94,v95
MM a[24:27],a[128:131],v[24:27],a[24:27]
EX v97,v97
MM a[28:31],a[136:139],v[24:27],a[28:31]
buffer_load_dwordx4 v228, s[8:11], s64 offen offset:0 lds
MM a[28:31],a[140:143],v[28:31],a[28:31]
EX v98,v98
MM a[20:23],a[140:143],v[20:23],a[20:23]
EX v99,v99
MM a[20:23],a[136:139],v[16:19],a[20:23]
RD a[200:203],v226 offset:32768
CV v68,v96,v97
MM a[12:15],a[136:139],v[8:11],a[12:15]
EX v100,v100
MM a[12:15],a[140:143],v[12:15],a[12:15]
RD a[204:207],v227 offset:32768
CV v69,v98,v99
MM a[4:7],a[140:143],v[4:7],a[4:7]
EX v101,v101
MM a[4:7],a[136:139],v[0:3],a[4:7]
buffer_load_dwordx4 v229, s[8:11], s64 offen offset:1024 lds
MM a[32:35],a[144:147],v[0:3],a[32:35]
EX v102,v102
MM a[32:35],a[148:151],v[4:7],a[32:35]
RD a[208:211],v224 offset:36864
CV v70,v100,v101
MM a[40:43],a[148:151],v[12:15],a[40:43]
EX v103,v103
MM a[40:43],a[144:147],v[8:11],a[40:43]
RD a[212:215],v225 offset:36864
CV v71,v102,v103
MM a[48:51],a[144:147],v[16:19],a[48:51]
EX v104,v104
MM a[48:51],a[148:151],v[20:23],a[48:51]
EX v105,v105
MM a[56:59],a[148:151],v[28:31],a[56:59]
buffer_load_dwordx4 v230, s[8:11], s64 offen offset:2048 lds
MM a[56:59],a[144:147],v[24:27],a[56:59]
EX v106,v106
MM a[60:63],a[152:155],v[24:27],a[60:63]
RD a[216:219],v226 offset:36864
CV v76,v104,v105
MM a[60:63],a[156:159],v[28:31],a[60:63]
EX v107,v107
MM a[52:55],a[156:159],v[20:23],a[52:55]
RD a[220:223],v227 offset:36864
CV v77,v106,v107
MM a[52:55],a[152:155],v[16:19],a[52:55]
EX v108,v108
MM a[44:47],a[152:155],v[8:11],a[44:47]
EX v109,v109
MM a[44:47],a[156:159],v[12:15],a[44:47]
buffer_load_dwordx4 v231, s[8:11], s64 offen offset:3072 lds
MM a[36:39],a[156:159],v[4:7],a[36:39]
s_add_u32 m0, s65, 65536
RD a[224:227],v224 offset:40960
CV v78,v108,v109
MM a[36:39],a[152:155],v[0:3],a[36:39]
EX v110,v110
MM a[64:67],a[160:163],v[0:3],a[64:67]
EX v111,v111
MM a[64:67],a[164:167],v[4:7],a[64:67]
RD a[228:231],v225 offset:40960
CV v79,v110,v111
MM a[72:75],a[164:167],v[12:15],a[72:75]
EX v112,v112
MM a[72:75],a[160:163],v[8:11],a[72:75]
buffer_load_dwordx4 v235, s[12:15], s81 offen offset:0 lds
MM a[80:83],a[160:163],v[16:19],a[80:83]
EX v113,v113
MM a[80:83],a[164:167],v[20:23],a[80:83]
RD a[232:235],v226 offset:40960
CV v84,v112,v113
MM a[88:91],a[164:167],v[28:31],a[88:91]
EX v114,v114
MM a[88:91],a[160:163],v[24:27],a[88:91]
EX v115,v115
MM a[92:95],a[168:171],v[24:27],a[92:95]
RD a[236:239],v227 offset:40960
CV v85,v114,v115
MM a[92:95],a[172:175],v[28:31],a[92:95]
EX v116,v116
MM a[84:87],a[172:175],v[20:23],a[84:87]
buffer_load_dwordx4 v235, s[12:15], s64 offen offset:1024 lds
MM a[84:87],a[168:171],v[16:19],a[84:87]
EX v117,v117
MM a[76:79],a[168:171],v[8:11],a[76:79]
RD a[240:243],v224 offset:45056
CV v86,v116,v117
MM a[76:79],a[172:175],v[12:15],a[76:79]
EX v118,v118
MM a[68:71],a[172:175],v[4:7],a[68:71]
EX v119,v119
MM a[68:71],a[168:171],v[0:3],a[68:71]
RD a[244:247],v225 offset:45056
CV v87,v118,v119
MM a[96:99],a[176:179],v[0:3],a[96:99]
EX v120,v120
MM a[96:99],a[180:183],v[4:7],a[96:99]
buffer_load_dwordx4 v235, s[12:15], s81 offen offset:2048 lds
MM a[104:107],a[180:183],v[12:15],a[104:107]
EX v121,v121
MM a[104:107],a[176:179],v[8:11],a[104:107]
RD a[248:251],v226 offset:45056
CV v92,v120,v121
MM a[112:115],a[176:179],v[16:19],a[112:115]
EX v122,v122
MM a[112:115],a[180:183],v[20:23],a[112:115]
EX v123,v123
MM a[120:123],a[180:183],v[28:31],a[120:123]
RD a[252:255],v227 offset:45056
CV v93,v122,v123
MM a[120:123],a[176:179],v[24:27],a[120:123]
EX v124,v124
MM a[124:127],a[184:187],v[24:27],a[124:127]
buffer_load_dwordx4 v235, s[12:15], s64 offen offset:3072 lds
MM a[124:127],a[188:191],v[28:31],a[124:127]
EX v125,v125
MM a[116:119],a[188:191],v[20:23],a[116:119]
EX v126,v126
MM a[116:119],a[184:187],v[16:19],a[116:119]
EX v127,v127
MM a[108:111],a[184:187],v[8:11],a[108:111]
CV v94,v124,v125
CV v95,v126,v127
MM a[108:111],a[188:191],v[12:15],a[108:111]
MM a[100:103],a[188:191],v[4:7],a[100:103]
MM a[100:103],a[184:187],v[0:3],a[100:103]
v_smfmac_f32_16x16x64_bf16 v[236:239], v[144:147], v[64:71], v234
v_smfmac_f32_16x16x64_bf16 v[236:239], v[148:151], v[72:79], v234
v_smfmac_f32_16x16x64_bf16 v[236:239], v[152:155], v[80:87], v234
v_smfmac_f32_16x16x64_bf16 v[236:239], v[156:159], v[88:95], v234
s_waitcnt lgkmcnt(0)
MM v[0:3],a[192:195],v[160:163],v[128:131]
s_add_u32 s64, s64, 16384
TR a[128:129],v232 offset:16384
s_add_u32 s81, s64, 896
MM v[0:3],a[196:199],v[176:179],v[0:3]
TR a[130:131],v232 offset:20480
TR a[132:133],v232 offset:24576
MM v[0:3],a[200:203],v[192:195],v[0:3]
TR a[134:135],v232 offset:28672
TR a[136:137],v233 offset:16384
MM v[0:3],a[204:207],v[208:211],v[0:3]
TR a[138:139],v233 offset:20480
v_add_f32 v247, v236, v237
MM v[4:7],a[220:223],v[208:211],v[128:131]
TR a[140:141],v233 offset:24576
v_cmp_nge_f32 vcc, s71, v247
MM v[4:7],a[216:219],v[192:195],v[4:7]
s_cbranch_vccnz LSITE25_%=
LBACK25_%=:
MM v[4:7],a[212:215],v[176:179],v[4:7]
TR a[142:143],v233 offset:28672
TR a[144:145],v232 offset:16896
MM v[4:7],a[208:211],v[160:163],v[4:7]
s_mul_i32 s76, s46, 192
TR a[146:147],v232 offset:20992
TR a[148:149],v232 offset:25088
MM v[12:15],a[208:211],v[164:167],v[132:135]
s_add_u32 s76, s76, 69632
v_lshrrev_b32 v253, 2, v248
v_min_u32 v253, 188, v253
MM v[12:15],a[212:215],v[180:183],v[12:15]
buffer_load_dword v252, v253, s[20:23], s76 offen offset:0 sc1
MM v[12:15],a[216:219],v[196:199],v[12:15]
EX v0,v0
MM v[12:15],a[220:223],v[212:215],v[12:15]
EX v1,v1
MM v[8:11],a[204:207],v[212:215],v[132:135]
EX v2,v2
MM v[8:11],a[200:203],v[196:199],v[8:11]
TR a[150:151],v232 offset:29184
CV v0,v0,v1
MM v[8:11],a[196:199],v[180:183],v[8:11]
EX v3,v3
MM v[8:11],a[192:195],v[164:167],v[8:11]
TR a[152:153],v233 offset:16896
CV v1,v2,v3
MM v[16:19],a[192:195],v[168:171],v[136:139]
EX v4,v4
MM v[16:19],a[196:199],v[184:187],v[16:19]
EX v5,v5
MM v[16:19],a[200:203],v[200:203],v[16:19]
TR a[154:155],v233 offset:20992
CV v2,v4,v5
MM v[16:19],a[204:207],v[216:219],v[16:19]
EX v6,v6
MM v[20:23],a[220:223],v[216:219],v[136:139]
EX v7,v7
MM v[20:23],a[216:219],v[200:203],v[20:23]
TR a[156:157],v233 offset:25088
CV v3,v6,v7
MM v[20:23],a[212:215],v[184:187],v[20:23]
EX v8,v8
MM v[20:23],a[208:211],v[168:171],v[20:23]
TR a[158:159],v233 offset:29184
TR a[160:161],v232 offset:17408
MM v[28:31],a[208:211],v[172:175],v[140:143]
EX v9,v9
MM v[28:31],a[212:215],v[188:191],v[28:31]
TR a[162:163],v232 offset:21504
CV v8,v8,v9
MM v[28:31],a[216:219],v[204:207],v[28:31]
EX v10,v10
MM v[28:31],a[220:223],v[220:223],v[28:31]
EX v11,v11
MM v[24:27],a[204:207],v[220:223],v[140:143]
TR a[164:165],v232 offset:25600
)ASM"
R"ASM(
CV v9,v10,v11
MM v[24:27],a[200:203],v[204:207],v[24:27]
EX v12,v12
MM v[24:27],a[196:199],v[188:191],v[24:27]
TR a[166:167],v232 offset:29696
TR a[168:169],v233 offset:17408
MM v[24:27],a[192:195],v[172:175],v[24:27]
EX v13,v13
MM v[32:35],a[224:227],v[160:163],v[128:131]
TR a[170:171],v233 offset:21504
CV v10,v12,v13
MM v[32:35],a[228:231],v[176:179],v[32:35]
EX v14,v14
MM v[32:35],a[232:235],v[192:195],v[32:35]
EX v15,v15
MM v[32:35],a[236:239],v[208:211],v[32:35]
TR a[172:173],v233 offset:25600
CV v11,v14,v15
MM v[36:39],a[252:255],v[208:211],v[128:131]
EX v16,v16
MM v[36:39],a[248:251],v[192:195],v[36:39]
TR a[174:175],v233 offset:29696
TR a[176:177],v232 offset:17920
MM v[36:39],a[244:247],v[176:179],v[36:39]
EX v17,v17
MM v[36:39],a[240:243],v[160:163],v[36:39]
TR a[178:179],v232 offset:22016
CV v16,v16,v17
MM v[44:47],a[240:243],v[164:167],v[132:135]
EX v18,v18
MM v[44:47],a[244:247],v[180:183],v[44:47]
EX v19,v19
MM v[44:47],a[248:251],v[196:199],v[44:47]
TR a[180:181],v232 offset:26112
CV v17,v18,v19
MM v[44:47],a[252:255],v[212:215],v[44:47]
EX v20,v20
MM v[40:43],a[236:239],v[212:215],v[132:135]
TR a[182:183],v232 offset:30208
TR a[184:185],v233 offset:17920
MM v[40:43],a[232:235],v[196:199],v[40:43]
EX v21,v21
MM v[40:43],a[228:231],v[180:183],v[40:43]
TR a[186:187],v233 offset:22016
CV v18,v20,v21
MM v[40:43],a[224:227],v[164:167],v[40:43]
EX v22,v22
MM v[48:51],a[224:227],v[168:171],v[136:139]
TR a[188:189],v233 offset:26112
TR a[190:191],v233 offset:30208
MM v[48:51],a[228:231],v[184:187],v[48:51]
EX v23,v23
MM v[48:51],a[232:235],v[200:203],v[48:51]
CV v19,v22,v23
MM v[48:51],a[236:239],v[216:219],v[48:51]
EX v24,v24
MM v[52:55],a[252:255],v[216:219],v[136:139]
EX v25,v25
MM v[52:55],a[248:251],v[200:203],v[52:55]
EX v26,v26
MM v[52:55],a[244:247],v[184:187],v[52:55]
EX v27,v27
MM v[52:55],a[240:243],v[168:171],v[52:55]
EX v28,v28
MM v[60:63],a[240:243],v[172:175],v[140:143]
EX v29,v29
MM v[60:63],a[244:247],v[188:191],v[60:63]
EX v30,v30
MM v[60:63],a[248:251],v[204:207],v[60:63]
EX v31,v31
MM v[60:63],a[252:255],v[220:223],v[60:63]
s_nop 0
EX v32,v32
MM v[56:59],a[236:239],v[220:223],v[140:143]
s_nop 0
EX v33,v33
MM v[56:59],a[232:235],v[204:207],v[56:59]
s_nop 0
EX v34,v34
MM v[56:59],a[228:231],v[188:191],v[56:59]
LPG16_%=:
s_cbranch_execz LTR2_%=
EX v35,v35
MM v[56:59],a[224:227],v[172:175],v[56:59]
s_waitcnt vmcnt(16) lgkmcnt(0)
s_barrier
MM a[0:3],a[128:131],v[64:67],a[0:3]
s_waitcnt vmcnt(0)
v_cmp_ne_u32 s[90:91], s69, v252
CV v24,v24,v25
MM a[0:3],a[132:135],v[68:71],a[0:3]
s_add_u32 m0, s65, 16384
CV v25,v26,v27
CV v26,v28,v29
MM a[8:11],a[132:135],v[76:79],a[8:11]
s_cmp_lg_u64 s[90:91], 0
s_cbranch_scc1 LRFH_%=
LRFHb_%=:
v_lshrrev_b32 v253, 7, v248
MM a[8:11],a[128:131],v[72:75],a[8:11]
v_mul_u32_u24 v253, 3328, v253
v_add_u32 v253, v253, v248
MM a[16:19],a[128:131],v[80:83],a[16:19]
v_lshrrev_b32 v252, 6, v248
v_mul_u32_u24 v252, 3392, v252
MM a[16:19],a[132:135],v[84:87],a[16:19]
RD a[192:195],v224 offset:49152
v_add_u32 v252, v252, v248
MM a[24:27],a[132:135],v[92:95],a[24:27]
RD a[196:199],v225 offset:49152
CV v27,v30,v31
MM a[24:27],a[128:131],v[88:91],a[24:27]
buffer_load_dwordx4 v228, s[8:11], s64 offen offset:0 lds
MM a[28:31],a[136:139],v[88:91],a[28:31]
s_add_u32 s76, s76, 21557248
RD a[200:203],v226 offset:49152
CV v4,v32,v33
MM a[28:31],a[140:143],v[92:95],a[28:31]
s_lshl_b32 s90, %[swave], 11
CV v5,v34,v35
MM a[20:23],a[140:143],v[84:87],a[20:23]
buffer_load_dwordx4 v229, s[8:11], s64 offen offset:1024 lds
MM a[20:23],a[136:139],v[80:83],a[20:23]
s_add_u32 s90, s90, %[slds]
s_add_u32 m0, s90, 139264
s_nop 0
buffer_load_dwordx4 v253, s[20:23], s76 offen offset:0 sc1 lds
MM a[12:15],a[136:139],v[72:75],a[12:15]
s_add_u32 m0, s65, 16384
EX v36,v36
MM a[12:15],a[140:143],v[76:79],a[12:15]
s_add_u32 s76, s76, 128
s_add_u32 m0, s90, 140288
s_mov_b32 exec_hi, 0
buffer_load_dwordx4 v252, s[20:23], s76 offen offset:0 sc1 lds
s_mov_b64 exec, -1
MM a[4:7],a[140:143],v[68:71],a[4:7]
EX v37,v37
MM a[4:7],a[136:139],v[64:67],a[4:7]
RD a[204:207],v227 offset:49152
CV v6,v36,v37
MM a[32:35],a[144:147],v[64:67],a[32:35]
EX v38,v38
MM a[32:35],a[148:151],v[68:71],a[32:35]
EX v39,v39
MM a[40:43],a[148:151],v[76:79],a[40:43]
RD a[208:211],v224 offset:53248
CV v7,v38,v39
MM a[40:43],a[144:147],v[72:75],a[40:43]
EX v40,v40
MM a[48:51],a[144:147],v[80:83],a[48:51]
s_add_u32 m0, s65, 16384
s_nop 0
buffer_load_dwordx4 v230, s[8:11], s64 offen offset:2048 lds
MM a[48:51],a[148:151],v[84:87],a[48:51]
EX v41,v41
MM a[56:59],a[148:151],v[92:95],a[56:59]
RD a[212:215],v225 offset:53248
CV v12,v40,v41
MM a[56:59],a[144:147],v[88:91],a[56:59]
EX v42,v42
MM a[60:63],a[152:155],v[88:91],a[60:63]
EX v43,v43
MM a[60:63],a[156:159],v[92:95],a[60:63]
RD a[216:219],v226 offset:53248
CV v13,v42,v43
MM a[52:55],a[156:159],v[84:87],a[52:55]
EX v44,v44
MM a[52:55],a[152:155],v[80:83],a[52:55]
buffer_load_dwordx4 v231, s[8:11], s64 offen offset:3072 lds
MM a[44:47],a[152:155],v[72:75],a[44:47]
s_add_u32 m0, s65, 81920
EX v45,v45
MM a[44:47],a[156:159],v[76:79],a[44:47]
RD a[220:223],v227 offset:53248
CV v14,v44,v45
MM a[36:39],a[156:159],v[68:71],a[36:39]
EX v46,v46
MM a[36:39],a[152:155],v[64:67],a[36:39]
RD a[224:227],v224 offset:57344
RD a[228:231],v225 offset:57344
MM a[64:67],a[160:163],v[64:67],a[64:67]
EX v47,v47
MM a[64:67],a[164:167],v[68:71],a[64:67]
RD a[232:235],v226 offset:57344
CV v15,v46,v47
MM a[72:75],a[164:167],v[76:79],a[72:75]
EX v48,v48
MM a[72:75],a[160:163],v[72:75],a[72:75]
buffer_load_dwordx4 v235, s[12:15], s81 offen offset:0 lds
MM a[80:83],a[160:163],v[80:83],a[80:83]
EX v49,v49
MM a[80:83],a[164:167],v[84:87],a[80:83]
RD a[236:239],v227 offset:57344
CV v20,v48,v49
MM a[88:91],a[164:167],v[92:95],a[88:91]
EX v50,v50
MM a[88:91],a[160:163],v[88:91],a[88:91]
EX v51,v51
MM a[92:95],a[168:171],v[88:91],a[92:95]
RD a[240:243],v224 offset:61440
CV v21,v50,v51
MM a[92:95],a[172:175],v[92:95],a[92:95]
EX v52,v52
MM a[84:87],a[172:175],v[84:87],a[84:87]
buffer_load_dwordx4 v235, s[12:15], s64 offen offset:1024 lds
MM a[84:87],a[168:171],v[80:83],a[84:87]
EX v53,v53
MM a[76:79],a[168:171],v[72:75],a[76:79]
RD a[244:247],v225 offset:61440
CV v22,v52,v53
MM a[76:79],a[172:175],v[76:79],a[76:79]
EX v54,v54
MM a[68:71],a[172:175],v[68:71],a[68:71]
EX v55,v55
MM a[68:71],a[168:171],v[64:67],a[68:71]
RD a[248:251],v226 offset:61440
CV v23,v54,v55
MM a[96:99],a[176:179],v[64:67],a[96:99]
EX v56,v56
MM a[96:99],a[180:183],v[68:71],a[96:99]
buffer_load_dwordx4 v235, s[12:15], s81 offen offset:2048 lds
MM a[104:107],a[180:183],v[76:79],a[104:107]
EX v57,v57
MM a[104:107],a[176:179],v[72:75],a[104:107]
RD a[252:255],v227 offset:61440
CV v28,v56,v57
MM a[112:115],a[176:179],v[80:83],a[112:115]
EX v58,v58
MM a[112:115],a[180:183],v[84:87],a[112:115]
EX v59,v59
MM a[120:123],a[180:183],v[92:95],a[120:123]
buffer_load_dwordx4 v235, s[12:15], s64 offen offset:3072 lds
MM a[120:123],a[176:179],v[88:91],a[120:123]
EX v60,v60
MM a[124:127],a[184:187],v[88:91],a[124:127]
EX v61,v61
MM a[124:127],a[188:191],v[92:95],a[124:127]
EX v62,v62
MM a[116:119],a[188:191],v[84:87],a[116:119]
EX v63,v63
MM a[116:119],a[184:187],v[80:83],a[116:119]
CV v29,v58,v59
CV v30,v60,v61
MM a[108:111],a[184:187],v[72:75],a[108:111]
CV v31,v62,v63
MM a[108:111],a[188:191],v[76:79],a[108:111]
MM a[100:103],a[188:191],v[68:71],a[100:103]
MM a[100:103],a[184:187],v[64:67],a[100:103]
v_smfmac_f32_16x16x64_bf16 v[236:239], v[144:147], v[0:7], v234
v_smfmac_f32_16x16x64_bf16 v[236:239], v[148:151], v[8:15], v234
v_smfmac_f32_16x16x64_bf16 v[236:239], v[152:155], v[16:23], v234
v_smfmac_f32_16x16x64_bf16 v[236:239], v[156:159], v[24:31], v234
s_waitcnt lgkmcnt(0)
MM v[64:67],a[192:195],v[160:163],v[128:131]
s_add_u32 s64, s64, 16384
TR a[128:129],v232 offset:32768
s_add_u32 s81, s64, 896
MM v[64:67],a[196:199],v[176:179],v[64:67]
TR a[130:131],v232 offset:36864
TR a[132:133],v232 offset:40960
MM v[64:67],a[200:203],v[192:195],v[64:67]
TR a[134:135],v232 offset:45056
TR a[136:137],v233 offset:32768
MM v[64:67],a[204:207],v[208:211],v[64:67]
TR a[138:139],v233 offset:36864
v_add_f32 v246, v236, v237
MM v[68:71],a[220:223],v[208:211],v[128:131]
TR a[140:141],v233 offset:40960
v_cmp_nge_f32 vcc, s71, v246
MM v[68:71],a[216:219],v[192:195],v[68:71]
s_cbranch_vccnz LSITE26_%=
LBACK26_%=:
MM v[68:71],a[212:215],v[176:179],v[68:71]
s_add_u32 m0, s94, 131072
s_add_u32 s66, s85, 0
buffer_load_dwordx4 v241, s[24:27], s66 offen offset:0 lds
MM v[68:71],a[208:211],v[160:163],v[68:71]
TR a[142:143],v233 offset:45056
TR a[144:145],v232 offset:33280
MM v[76:79],a[208:211],v[164:167],v[132:135]
EX v64,v64
MM v[76:79],a[212:215],v[180:183],v[76:79]
EX v65,v65
MM v[76:79],a[216:219],v[196:199],v[76:79]
TR a[146:147],v232 offset:37376
CV v64,v64,v65
MM v[76:79],a[220:223],v[212:215],v[76:79]
EX v66,v66
MM v[72:75],a[204:207],v[212:215],v[132:135]
EX v67,v67
MM v[72:75],a[200:203],v[196:199],v[72:75]
TR a[148:149],v232 offset:41472
CV v65,v66,v67
MM v[72:75],a[196:199],v[180:183],v[72:75]
s_add_u32 m0, s94, 135168
s_add_u32 s66, s85, 81920
buffer_load_dwordx4 v241, s[24:27], s66 offen offset:0 lds
MM v[72:75],a[192:195],v[164:167],v[72:75]
EX v68,v68
MM v[80:83],a[192:195],v[168:171],v[136:139]
EX v69,v69
MM v[80:83],a[196:199],v[184:187],v[80:83]
TR a[150:151],v232 offset:45568
CV v66,v68,v69
MM v[80:83],a[200:203],v[200:203],v[80:83]
EX v70,v70
MM v[80:83],a[204:207],v[216:219],v[80:83]
TR a[152:153],v233 offset:33280
TR a[154:155],v233 offset:37376
MM v[84:87],a[220:223],v[216:219],v[136:139]
EX v71,v71
MM v[84:87],a[216:219],v[200:203],v[84:87]
TR a[156:157],v233 offset:41472
CV v67,v70,v71
MM v[84:87],a[212:215],v[184:187],v[84:87]
s_add_u32 m0, s94, 147456
s_add_u32 s66, s87, 0
buffer_load_dwordx4 v241, s[24:27], s66 offen offset:0 lds
MM v[84:87],a[208:211],v[168:171],v[84:87]
EX v72,v72
MM v[92:95],a[208:211],v[172:175],v[140:143]
EX v73,v73
MM v[92:95],a[212:215],v[188:191],v[92:95]
TR a[158:159],v233 offset:45568
CV v72,v72,v73
MM v[92:95],a[216:219],v[204:207],v[92:95]
EX v74,v74
MM v[92:95],a[220:223],v[220:223],v[92:95]
TR a[160:161],v232 offset:33792
TR a[162:163],v232 offset:37888
MM v[88:91],a[204:207],v[220:223],v[140:143]
EX v75,v75
MM v[88:91],a[200:203],v[204:207],v[88:91]
TR a[164:165],v232 offset:41984
CV v73,v74,v75
MM v[88:91],a[196:199],v[188:191],v[88:91]
EX v76,v76
MM v[88:91],a[192:195],v[172:175],v[88:91]
EX v77,v77
MM v[96:99],a[224:227],v[160:163],v[128:131]
TR a[166:167],v232 offset:46080
CV v74,v76,v77
MM v[96:99],a[228:231],v[176:179],v[96:99]
EX v78,v78
MM v[96:99],a[232:235],v[192:195],v[96:99]
TR a[168:169],v233 offset:33792
TR a[170:171],v233 offset:37888
MM v[96:99],a[236:239],v[208:211],v[96:99]
EX v79,v79
MM v[100:103],a[252:255],v[208:211],v[128:131]
TR a[172:173],v233 offset:41984
CV v75,v78,v79
MM v[100:103],a[248:251],v[192:195],v[100:103]
EX v80,v80
MM v[100:103],a[244:247],v[176:179],v[100:103]
TR a[174:175],v233 offset:46080
TR a[176:177],v232 offset:34304
MM v[100:103],a[240:243],v[160:163],v[100:103]
EX v81,v81
MM v[108:111],a[240:243],v[164:167],v[132:135]
TR a[178:179],v232 offset:38400
CV v80,v80,v81
MM v[108:111],a[244:247],v[180:183],v[108:111]
EX v82,v82
MM v[108:111],a[248:251],v[196:199],v[108:111]
EX v83,v83
MM v[108:111],a[252:255],v[212:215],v[108:111]
TR a[180:181],v232 offset:42496
CV v81,v82,v83
MM v[104:107],a[236:239],v[212:215],v[132:135]
EX v84,v84
MM v[104:107],a[232:235],v[196:199],v[104:107]
TR a[182:183],v232 offset:46592
TR a[184:185],v233 offset:34304
MM v[104:107],a[228:231],v[180:183],v[104:107]
EX v85,v85
MM v[104:107],a[224:227],v[164:167],v[104:107]
TR a[186:187],v233 offset:38400
CV v82,v84,v85
MM v[112:115],a[224:227],v[168:171],v[136:139]
EX v86,v86
MM v[112:115],a[228:231],v[184:187],v[112:115]
EX v87,v87
MM v[112:115],a[232:235],v[200:203],v[112:115]
TR a[188:189],v233 offset:42496
CV v83,v86,v87
MM v[112:115],a[236:239],v[216:219],v[112:115]
EX v88,v88
MM v[116:119],a[252:255],v[216:219],v[136:139]
EX v89,v89
MM v[116:119],a[248:251],v[200:203],v[116:119]
)ASM"
R"ASM(
TR a[190:191],v233 offset:46592
CV v88,v88,v89
MM v[116:119],a[244:247],v[184:187],v[116:119]
EX v90,v90
MM v[116:119],a[240:243],v[168:171],v[116:119]
EX v91,v91
MM v[124:127],a[240:243],v[172:175],v[140:143]
EX v92,v92
MM v[124:127],a[244:247],v[188:191],v[124:127]
EX v93,v93
MM v[124:127],a[248:251],v[204:207],v[124:127]
EX v94,v94
MM v[124:127],a[252:255],v[220:223],v[124:127]
EX v95,v95
MM v[120:123],a[236:239],v[220:223],v[140:143]
EX v96,v96
MM v[120:123],a[232:235],v[204:207],v[120:123]
EX v97,v97
MM v[120:123],a[228:231],v[188:191],v[120:123]
s_getpc_b64 s[98:99]
LTA3_%=:
s_lshl_b32 s73, %[swave], 12
s_add_u32 s73, s73, LPG17_%=-LTA3_%=
s_add_u32 s98, s98, s73
s_addc_u32 s99, s99, 0
s_mov_b64 exec, 0
s_setpc_b64 s[98:99]
LTR3_%=:
s_mov_b64 exec, -1
s_nop 0
CV v89,v90,v91
MM v[120:123],a[224:227],v[172:175],v[120:123]
s_waitcnt lgkmcnt(0)
s_barrier
MM a[0:3],a[128:131],v[0:3],a[0:3]
s_add_u32 m0, s65, 32768
RD a[192:195],v224 offset:0
CV v90,v92,v93
MM a[0:3],a[132:135],v[4:7],a[0:3]
RD a[196:199],v225 offset:0
CV v91,v94,v95
MM a[8:11],a[132:135],v[12:15],a[8:11]
buffer_load_dwordx4 v228, s[8:11], s64 offen offset:0 lds
MM a[8:11],a[128:131],v[8:11],a[8:11]
RD a[200:203],v226 offset:0
CV v68,v96,v97
MM a[16:19],a[128:131],v[16:19],a[16:19]
buffer_load_dwordx4 v229, s[8:11], s64 offen offset:1024 lds
MM a[16:19],a[132:135],v[20:23],a[16:19]
RD a[204:207],v227 offset:0
RD a[208:211],v224 offset:4096
MM a[24:27],a[132:135],v[28:31],a[24:27]
EX v98,v98
MM a[24:27],a[128:131],v[24:27],a[24:27]
EX v99,v99
MM a[28:31],a[136:139],v[24:27],a[28:31]
RD a[212:215],v225 offset:4096
CV v69,v98,v99
MM a[28:31],a[140:143],v[28:31],a[28:31]
EX v100,v100
MM a[20:23],a[140:143],v[20:23],a[20:23]
buffer_load_dwordx4 v230, s[8:11], s64 offen offset:2048 lds
MM a[20:23],a[136:139],v[16:19],a[20:23]
EX v101,v101
MM a[12:15],a[136:139],v[8:11],a[12:15]
RD a[216:219],v226 offset:4096
CV v70,v100,v101
MM a[12:15],a[140:143],v[12:15],a[12:15]
EX v102,v102
MM a[4:7],a[140:143],v[4:7],a[4:7]
EX v103,v103
MM a[4:7],a[136:139],v[0:3],a[4:7]
RD a[220:223],v227 offset:4096
CV v71,v102,v103
MM a[32:35],a[144:147],v[0:3],a[32:35]
EX v104,v104
MM a[32:35],a[148:151],v[4:7],a[32:35]
buffer_load_dwordx4 v231, s[8:11], s64 offen offset:3072 lds
MM a[40:43],a[148:151],v[12:15],a[40:43]
s_add_u32 m0, s65, 98304
EX v105,v105
MM a[40:43],a[144:147],v[8:11],a[40:43]
RD a[224:227],v224 offset:8192
CV v76,v104,v105
MM a[48:51],a[144:147],v[16:19],a[48:51]
EX v106,v106
MM a[48:51],a[148:151],v[20:23],a[48:51]
EX v107,v107
MM a[56:59],a[148:151],v[28:31],a[56:59]
RD a[228:231],v225 offset:8192
CV v77,v106,v107
MM a[56:59],a[144:147],v[24:27],a[56:59]
EX v108,v108
MM a[60:63],a[152:155],v[24:27],a[60:63]
buffer_load_dwordx4 v235, s[12:15], s81 offen offset:0 lds
MM a[60:63],a[156:159],v[28:31],a[60:63]
EX v109,v109
MM a[52:55],a[156:159],v[20:23],a[52:55]
RD a[232:235],v226 offset:8192
CV v78,v108,v109
MM a[52:55],a[152:155],v[16:19],a[52:55]
EX v110,v110
MM a[44:47],a[152:155],v[8:11],a[44:47]
EX v111,v111
MM a[44:47],a[156:159],v[12:15],a[44:47]
RD a[236:239],v227 offset:8192
CV v79,v110,v111
MM a[36:39],a[156:159],v[4:7],a[36:39]
buffer_load_dwordx4 v235, s[12:15], s64 offen offset:1024 lds
MM a[36:39],a[152:155],v[0:3],a[36:39]
EX v112,v112
MM a[64:67],a[160:163],v[0:3],a[64:67]
EX v113,v113
MM a[64:67],a[164:167],v[4:7],a[64:67]
RD a[240:243],v224 offset:12288
CV v84,v112,v113
MM a[72:75],a[164:167],v[12:15],a[72:75]
EX v114,v114
MM a[72:75],a[160:163],v[8:11],a[72:75]
RD a[244:247],v225 offset:12288
RD a[248:251],v226 offset:12288
MM a[80:83],a[160:163],v[16:19],a[80:83]
EX v115,v115
MM a[80:83],a[164:167],v[20:23],a[80:83]
RD a[252:255],v227 offset:12288
CV v85,v114,v115
MM a[88:91],a[164:167],v[28:31],a[88:91]
EX v116,v116
MM a[88:91],a[160:163],v[24:27],a[88:91]
buffer_load_dwordx4 v235, s[12:15], s81 offen offset:2048 lds
MM a[92:95],a[168:171],v[24:27],a[92:95]
EX v117,v117
MM a[92:95],a[172:175],v[28:31],a[92:95]
EX v118,v118
MM a[84:87],a[172:175],v[20:23],a[84:87]
buffer_load_dwordx4 v235, s[12:15], s64 offen offset:3072 lds
MM a[84:87],a[168:171],v[16:19],a[84:87]
EX v119,v119
MM a[76:79],a[168:171],v[8:11],a[76:79]
EX v120,v120
MM a[76:79],a[172:175],v[12:15],a[76:79]
EX v121,v121
MM a[68:71],a[172:175],v[4:7],a[68:71]
EX v122,v122
MM a[68:71],a[168:171],v[0:3],a[68:71]
EX v123,v123
MM a[96:99],a[176:179],v[0:3],a[96:99]
EX v124,v124
MM a[96:99],a[180:183],v[4:7],a[96:99]
EX v125,v125
MM a[104:107],a[180:183],v[12:15],a[104:107]
EX v126,v126
MM a[104:107],a[176:179],v[8:11],a[104:107]
EX v127,v127
MM a[112:115],a[176:179],v[16:19],a[112:115]
CV v86,v116,v117
CV v87,v118,v119
MM a[112:115],a[180:183],v[20:23],a[112:115]
CV v92,v120,v121
CV v93,v122,v123
MM a[120:123],a[180:183],v[28:31],a[120:123]
CV v94,v124,v125
CV v95,v126,v127
MM a[120:123],a[176:179],v[24:27],a[120:123]
MM a[124:127],a[184:187],v[24:27],a[124:127]
MM a[124:127],a[188:191],v[28:31],a[124:127]
MM a[116:119],a[188:191],v[20:23],a[116:119]
MM a[116:119],a[184:187],v[16:19],a[116:119]
MM a[108:111],a[184:187],v[8:11],a[108:111]
MM a[108:111],a[188:191],v[12:15],a[108:111]
MM a[100:103],a[188:191],v[4:7],a[100:103]
MM a[100:103],a[184:187],v[0:3],a[100:103]
v_smfmac_f32_16x16x64_bf16 v[236:239], v[144:147], v[64:71], v234
v_smfmac_f32_16x16x64_bf16 v[236:239], v[148:151], v[72:79], v234
v_smfmac_f32_16x16x64_bf16 v[236:239], v[152:155], v[80:87], v234
v_smfmac_f32_16x16x64_bf16 v[236:239], v[156:159], v[88:95], v234
s_waitcnt lgkmcnt(0)
MM v[0:3],a[192:195],v[160:163],v[128:131]
s_add_u32 s64, s64, 16384
TR a[128:129],v232 offset:49152
s_add_u32 s81, s64, 896
MM v[0:3],a[196:199],v[176:179],v[0:3]
TR a[130:131],v232 offset:53248
TR a[132:133],v232 offset:57344
MM v[0:3],a[200:203],v[192:195],v[0:3]
TR a[134:135],v232 offset:61440
TR a[136:137],v233 offset:49152
MM v[0:3],a[204:207],v[208:211],v[0:3]
TR a[138:139],v233 offset:53248
v_add_f32 v247, v236, v237
MM v[4:7],a[220:223],v[208:211],v[128:131]
TR a[140:141],v233 offset:57344
v_cmp_nge_f32 vcc, s71, v247
MM v[4:7],a[216:219],v[192:195],v[4:7]
s_cbranch_vccnz LSITE27_%=
LBACK27_%=:
MM v[4:7],a[212:215],v[176:179],v[4:7]
v_lshrrev_b32 v125, 8, v248
v_lshlrev_b32 v124, 10, v125
MM v[4:7],a[208:211],v[160:163],v[4:7]
v_bfe_u32 v126, v248, 6, 2
v_lshl_add_u32 v124, v126, 7, v124
MM v[12:15],a[208:211],v[164:167],v[132:135]
v_bfe_u32 v126, v248, 4, 2
v_lshl_add_u32 v124, v126, 3, v124
MM v[12:15],a[212:215],v[180:183],v[12:15]
s_lshl_b32 s91, %[swave], 5
EX v0,v0
MM v[12:15],a[216:219],v[196:199],v[12:15]
s_add_u32 s91, s91, %[slds]
EX v1,v1
MM v[12:15],a[220:223],v[212:215],v[12:15]
s_add_u32 s91, s91, 151552
v_add_u32 v124, s91, v124
v_and_b32 v126, 112, v248
MM v[8:11],a[204:207],v[212:215],v[132:135]
s_lshl_b32 s91, %[swave], 11
EX v2,v2
MM v[8:11],a[200:203],v[196:199],v[8:11]
s_add_u32 s91, s91, %[slds]
TR a[142:143],v233 offset:61440
CV v0,v0,v1
MM v[8:11],a[196:199],v[180:183],v[8:11]
s_add_u32 s91, s91, 139264
v_lshl_add_u32 v127, v125, 4, s91
v_lshl_add_u32 v127, v126, 2, v127
MM v[8:11],a[192:195],v[164:167],v[8:11]
TR a[144:145],v232 offset:49664
v_lshl_add_u32 v126, v126, 2, v127
MM v[16:19],a[192:195],v[168:171],v[136:139]
s_waitcnt vmcnt(17)
TR v[96:97],v124 offset:0
TR v[98:99],v124 offset:512
MM v[16:19],a[196:199],v[184:187],v[16:19]
RD v[108:111],v126 offset:0
TR v[100:101],v124 offset:4096
MM v[16:19],a[200:203],v[200:203],v[16:19]
TR v[102:103],v124 offset:4608
RD v[112:115],v126 offset:64
MM v[16:19],a[204:207],v[216:219],v[16:19]
TR v[104:105],v124 offset:8192
TR v[106:107],v124 offset:8704
EX v3,v3
MM v[20:23],a[220:223],v[216:219],v[136:139]
RD v[116:119],v127 offset:1024
v_bfe_u32 v127, v248, 4, 3
EX v4,v4
MM v[20:23],a[216:219],v[200:203],v[20:23]
s_nop 0
v_mul_u32_u24 v127, 2560, v127
v_lshl_add_u32 v127, v125, 4, v127
EX v5,v5
MM v[20:23],a[212:215],v[184:187],v[20:23]
s_lshl_b32 s90, %[swave], 6
TR a[146:147],v232 offset:53760
TR a[148:149],v232 offset:57856
MM v[20:23],a[208:211],v[168:171],v[20:23]
s_lshl_b32 s91, s47, 8
EX v6,v6
MM v[28:31],a[208:211],v[172:175],v[140:143]
s_add_u32 s90, s90, s91
EX v7,v7
MM v[28:31],a[212:215],v[188:191],v[28:31]
s_mul_i32 s91, s46, 17920
TR a[150:151],v232 offset:61952
CV v1,v2,v3
MM v[28:31],a[216:219],v[204:207],v[28:31]
s_add_u32 s90, s90, s91
EX v8,v8
MM v[28:31],a[220:223],v[220:223],v[28:31]
s_add_u32 s90, s90, 21659648
TR a[152:153],v233 offset:49664
CV v2,v4,v5
MM v[24:27],a[204:207],v[220:223],v[140:143]
s_nop 0
EX v9,v9
MM v[24:27],a[200:203],v[204:207],v[24:27]
s_nop 0
TR a[154:155],v233 offset:53760
TR a[156:157],v233 offset:57856
MM v[24:27],a[196:199],v[188:191],v[24:27]
s_nop 0
EX v10,v10
MM v[24:27],a[192:195],v[172:175],v[24:27]
LPG17_%=:
s_cbranch_execz LPG21_%=
EX v11,v11
MM v[32:35],a[224:227],v[160:163],v[128:131]
TR a[158:159],v233 offset:61952
CV v3,v6,v7
MM v[32:35],a[228:231],v[176:179],v[32:35]
EX v12,v12
MM v[32:35],a[232:235],v[192:195],v[32:35]
TR a[160:161],v232 offset:50176
CV v8,v8,v9
MM v[32:35],a[236:239],v[208:211],v[32:35]
s_waitcnt lgkmcnt(8)
EX v13,v13
MM v[36:39],a[252:255],v[208:211],v[128:131]
TR a[162:163],v232 offset:54272
TR a[164:165],v232 offset:58368
MM v[120:123],v[96:99],v[108:111],0
MM v[120:123],v[100:103],v[112:115],v[120:123]
MM v[120:123],v[104:107],v[116:119],v[120:123]
MM v[36:39],a[248:251],v[192:195],v[36:39]
EX v14,v14
MM v[36:39],a[244:247],v[176:179],v[36:39]
EX v15,v15
MM v[36:39],a[240:243],v[160:163],v[36:39]
TR a[166:167],v232 offset:62464
CV v9,v10,v11
MM v[44:47],a[240:243],v[164:167],v[132:135]
TR a[168:169],v233 offset:50176
CV v10,v12,v13
MM v[44:47],a[244:247],v[180:183],v[44:47]
EX v16,v16
MM v[44:47],a[248:251],v[196:199],v[44:47]
EX v17,v17
MM v[44:47],a[252:255],v[212:215],v[44:47]
s_mov_b32 exec_lo, 0x00ff00ff
s_mov_b32 exec_hi, 0x00ff00ff
buffer_store_dwordx4 v[120:123], v127, s[20:23], s90 offen offset:0 sc1
s_mov_b64 exec, -1
s_nop 0
MM v[40:43],a[236:239],v[212:215],v[132:135]
TR a[170:171],v233 offset:54272
TR a[172:173],v233 offset:58368
MM v[40:43],a[232:235],v[196:199],v[40:43]
EX v18,v18
MM v[40:43],a[228:231],v[180:183],v[40:43]
EX v19,v19
MM v[40:43],a[224:227],v[164:167],v[40:43]
TR a[174:175],v233 offset:62464
CV v11,v14,v15
MM v[48:51],a[224:227],v[168:171],v[136:139]
EX v20,v20
MM v[48:51],a[228:231],v[184:187],v[48:51]
TR a[176:177],v232 offset:50688
TR a[178:179],v232 offset:54784
MM v[48:51],a[232:235],v[200:203],v[48:51]
EX v21,v21
MM v[48:51],a[236:239],v[216:219],v[48:51]
TR a[180:181],v232 offset:58880
CV v16,v16,v17
MM v[52:55],a[252:255],v[216:219],v[136:139]
EX v22,v22
MM v[52:55],a[248:251],v[200:203],v[52:55]
TR a[182:183],v232 offset:62976
CV v17,v18,v19
MM v[52:55],a[244:247],v[184:187],v[52:55]
EX v23,v23
MM v[52:55],a[240:243],v[168:171],v[52:55]
TR a[184:185],v233 offset:50688
CV v18,v20,v21
MM v[60:63],a[240:243],v[172:175],v[140:143]
EX v24,v24
MM v[60:63],a[244:247],v[188:191],v[60:63]
TR a[186:187],v233 offset:54784
TR a[188:189],v233 offset:58880
MM v[60:63],a[248:251],v[204:207],v[60:63]
TR a[190:191],v233 offset:62976
CV v19,v22,v23
MM v[60:63],a[252:255],v[220:223],v[60:63]
EX v25,v25
MM v[56:59],a[236:239],v[220:223],v[140:143]
EX v26,v26
MM v[56:59],a[232:235],v[204:207],v[56:59]
EX v27,v27
MM v[56:59],a[228:231],v[188:191],v[56:59]
EX v28,v28
MM v[56:59],a[224:227],v[172:175],v[56:59]
s_waitcnt vmcnt(16) lgkmcnt(0)
s_barrier
MM a[0:3],a[128:131],v[64:67],a[0:3]
s_add_u32 m0, s65, 49152
CV v24,v24,v25
CV v25,v26,v27
MM a[0:3],a[132:135],v[68:71],a[0:3]
EX v29,v29
MM a[8:11],a[132:135],v[76:79],a[8:11]
EX v30,v30
MM a[8:11],a[128:131],v[72:75],a[8:11]
EX v31,v31
MM a[16:19],a[128:131],v[80:83],a[16:19]
RD a[192:195],v224 offset:16384
CV v26,v28,v29
MM a[16:19],a[132:135],v[84:87],a[16:19]
EX v32,v32
MM a[24:27],a[132:135],v[92:95],a[24:27]
RD a[196:199],v225 offset:16384
CV v27,v30,v31
MM a[24:27],a[128:131],v[88:91],a[24:27]
EX v33,v33
MM a[28:31],a[136:139],v[88:91],a[28:31]
buffer_load_dwordx4 v228, s[8:11], s64 offen offset:0 lds
MM a[28:31],a[140:143],v[92:95],a[28:31]
EX v34,v34
MM a[20:23],a[140:143],v[84:87],a[20:23]
)ASM"
R"ASM(
EX v35,v35
MM a[20:23],a[136:139],v[80:83],a[20:23]
RD a[200:203],v226 offset:16384
CV v4,v32,v33
MM a[12:15],a[136:139],v[72:75],a[12:15]
EX v36,v36
MM a[12:15],a[140:143],v[76:79],a[12:15]
RD a[204:207],v227 offset:16384
CV v5,v34,v35
MM a[4:7],a[140:143],v[68:71],a[4:7]
EX v37,v37
MM a[4:7],a[136:139],v[64:67],a[4:7]
buffer_load_dwordx4 v229, s[8:11], s64 offen offset:1024 lds
MM a[32:35],a[144:147],v[64:67],a[32:35]
EX v38,v38
MM a[32:35],a[148:151],v[68:71],a[32:35]
RD a[208:211],v224 offset:20480
CV v6,v36,v37
MM a[40:43],a[148:151],v[76:79],a[40:43]
EX v39,v39
MM a[40:43],a[144:147],v[72:75],a[40:43]
RD a[212:215],v225 offset:20480
CV v7,v38,v39
MM a[48:51],a[144:147],v[80:83],a[48:51]
EX v40,v40
MM a[48:51],a[148:151],v[84:87],a[48:51]
EX v41,v41
MM a[56:59],a[148:151],v[92:95],a[56:59]
buffer_load_dwordx4 v230, s[8:11], s64 offen offset:2048 lds
MM a[56:59],a[144:147],v[88:91],a[56:59]
EX v42,v42
MM a[60:63],a[152:155],v[88:91],a[60:63]
RD a[216:219],v226 offset:20480
CV v12,v40,v41
MM a[60:63],a[156:159],v[92:95],a[60:63]
EX v43,v43
MM a[52:55],a[156:159],v[84:87],a[52:55]
RD a[220:223],v227 offset:20480
CV v13,v42,v43
MM a[52:55],a[152:155],v[80:83],a[52:55]
EX v44,v44
MM a[44:47],a[152:155],v[72:75],a[44:47]
EX v45,v45
MM a[44:47],a[156:159],v[76:79],a[44:47]
buffer_load_dwordx4 v231, s[8:11], s64 offen offset:3072 lds
MM a[36:39],a[156:159],v[68:71],a[36:39]
s_add_u32 m0, s65, 114688
RD a[224:227],v224 offset:24576
CV v14,v44,v45
MM a[36:39],a[152:155],v[64:67],a[36:39]
EX v46,v46
MM a[64:67],a[160:163],v[64:67],a[64:67]
EX v47,v47
MM a[64:67],a[164:167],v[68:71],a[64:67]
RD a[228:231],v225 offset:24576
CV v15,v46,v47
MM a[72:75],a[164:167],v[76:79],a[72:75]
EX v48,v48
MM a[72:75],a[160:163],v[72:75],a[72:75]
buffer_load_dwordx4 v235, s[12:15], s81 offen offset:0 lds
MM a[80:83],a[160:163],v[80:83],a[80:83]
EX v49,v49
MM a[80:83],a[164:167],v[84:87],a[80:83]
RD a[232:235],v226 offset:24576
CV v20,v48,v49
MM a[88:91],a[164:167],v[92:95],a[88:91]
EX v50,v50
MM a[88:91],a[160:163],v[88:91],a[88:91]
EX v51,v51
MM a[92:95],a[168:171],v[88:91],a[92:95]
RD a[236:239],v227 offset:24576
CV v21,v50,v51
MM a[92:95],a[172:175],v[92:95],a[92:95]
EX v52,v52
MM a[84:87],a[172:175],v[84:87],a[84:87]
buffer_load_dwordx4 v235, s[12:15], s64 offen offset:1024 lds
MM a[84:87],a[168:171],v[80:83],a[84:87]
EX v53,v53
MM a[76:79],a[168:171],v[72:75],a[76:79]
RD a[240:243],v224 offset:28672
CV v22,v52,v53
MM a[76:79],a[172:175],v[76:79],a[76:79]
EX v54,v54
MM a[68:71],a[172:175],v[68:71],a[68:71]
EX v55,v55
MM a[68:71],a[168:171],v[64:67],a[68:71]
RD a[244:247],v225 offset:28672
CV v23,v54,v55
MM a[96:99],a[176:179],v[64:67],a[96:99]
EX v56,v56
MM a[96:99],a[180:183],v[68:71],a[96:99]
buffer_load_dwordx4 v235, s[12:15], s81 offen offset:2048 lds
MM a[104:107],a[180:183],v[76:79],a[104:107]
EX v57,v57
MM a[104:107],a[176:179],v[72:75],a[104:107]
RD a[248:251],v226 offset:28672
CV v28,v56,v57
MM a[112:115],a[176:179],v[80:83],a[112:115]
EX v58,v58
MM a[112:115],a[180:183],v[84:87],a[112:115]
EX v59,v59
MM a[120:123],a[180:183],v[92:95],a[120:123]
RD a[252:255],v227 offset:28672
CV v29,v58,v59
MM a[120:123],a[176:179],v[88:91],a[120:123]
EX v60,v60
MM a[124:127],a[184:187],v[88:91],a[124:127]
buffer_load_dwordx4 v235, s[12:15], s64 offen offset:3072 lds
MM a[124:127],a[188:191],v[92:95],a[124:127]
EX v61,v61
MM a[116:119],a[188:191],v[84:87],a[116:119]
EX v62,v62
MM a[116:119],a[184:187],v[80:83],a[116:119]
EX v63,v63
MM a[108:111],a[184:187],v[72:75],a[108:111]
CV v30,v60,v61
CV v31,v62,v63
MM a[108:111],a[188:191],v[76:79],a[108:111]
MM a[100:103],a[188:191],v[68:71],a[100:103]
MM a[100:103],a[184:187],v[64:67],a[100:103]
v_smfmac_f32_16x16x64_bf16 v[236:239], v[144:147], v[0:7], v234
v_smfmac_f32_16x16x64_bf16 v[236:239], v[148:151], v[8:15], v234
v_smfmac_f32_16x16x64_bf16 v[236:239], v[152:155], v[16:23], v234
v_smfmac_f32_16x16x64_bf16 v[236:239], v[156:159], v[24:31], v234
s_waitcnt lgkmcnt(0)
MM v[64:67],a[192:195],v[160:163],v[128:131]
s_sub_u32 s64, s64, 507904
TR a[128:129],v232 offset:0
s_add_u32 s81, s64, 896
MM v[64:67],a[196:199],v[176:179],v[64:67]
TR a[130:131],v232 offset:4096
TR a[132:133],v232 offset:8192
MM v[64:67],a[200:203],v[192:195],v[64:67]
TR a[134:135],v232 offset:12288
TR a[136:137],v233 offset:0
MM v[64:67],a[204:207],v[208:211],v[64:67]
TR a[138:139],v233 offset:4096
v_add_f32 v246, v236, v237
MM v[68:71],a[220:223],v[208:211],v[128:131]
TR a[140:141],v233 offset:8192
v_cmp_nge_f32 vcc, s71, v246
MM v[68:71],a[216:219],v[192:195],v[68:71]
s_cbranch_vccnz LSITE28_%=
LBACK28_%=:
MM v[68:71],a[212:215],v[176:179],v[68:71]
TR a[142:143],v233 offset:12288
TR a[144:145],v232 offset:512
MM v[68:71],a[208:211],v[160:163],v[68:71]
TR a[146:147],v232 offset:4608
TR a[148:149],v232 offset:8704
MM v[76:79],a[208:211],v[164:167],v[132:135]
EX v64,v64
MM v[76:79],a[212:215],v[180:183],v[76:79]
EX v65,v65
MM v[76:79],a[216:219],v[196:199],v[76:79]
TR a[150:151],v232 offset:12800
CV v64,v64,v65
MM v[76:79],a[220:223],v[212:215],v[76:79]
EX v66,v66
MM v[72:75],a[204:207],v[212:215],v[132:135]
EX v67,v67
MM v[72:75],a[200:203],v[196:199],v[72:75]
TR a[152:153],v233 offset:512
CV v65,v66,v67
MM v[72:75],a[196:199],v[180:183],v[72:75]
EX v68,v68
MM v[72:75],a[192:195],v[164:167],v[72:75]
EX v69,v69
MM v[80:83],a[192:195],v[168:171],v[136:139]
TR a[154:155],v233 offset:4608
CV v66,v68,v69
MM v[80:83],a[196:199],v[184:187],v[80:83]
EX v70,v70
MM v[80:83],a[200:203],v[200:203],v[80:83]
EX v71,v71
MM v[80:83],a[204:207],v[216:219],v[80:83]
TR a[156:157],v233 offset:8704
CV v67,v70,v71
MM v[84:87],a[220:223],v[216:219],v[136:139]
EX v72,v72
MM v[84:87],a[216:219],v[200:203],v[84:87]
TR a[158:159],v233 offset:12800
TR a[160:161],v232 offset:1024
MM v[84:87],a[212:215],v[184:187],v[84:87]
EX v73,v73
MM v[84:87],a[208:211],v[168:171],v[84:87]
TR a[162:163],v232 offset:5120
CV v72,v72,v73
MM v[92:95],a[208:211],v[172:175],v[140:143]
EX v74,v74
MM v[92:95],a[212:215],v[188:191],v[92:95]
TR a[164:165],v232 offset:9216
TR a[166:167],v232 offset:13312
MM v[92:95],a[216:219],v[204:207],v[92:95]
EX v75,v75
MM v[92:95],a[220:223],v[220:223],v[92:95]
TR a[168:169],v233 offset:1024
CV v73,v74,v75
MM v[88:91],a[204:207],v[220:223],v[140:143]
EX v76,v76
MM v[88:91],a[200:203],v[204:207],v[88:91]
EX v77,v77
MM v[88:91],a[196:199],v[188:191],v[88:91]
TR a[170:171],v233 offset:5120
CV v74,v76,v77
MM v[88:91],a[192:195],v[172:175],v[88:91]
EX v78,v78
MM v[96:99],a[224:227],v[160:163],v[128:131]
TR a[172:173],v233 offset:9216
TR a[174:175],v233 offset:13312
MM v[96:99],a[228:231],v[176:179],v[96:99]
EX v79,v79
MM v[96:99],a[232:235],v[192:195],v[96:99]
TR a[176:177],v232 offset:1536
CV v75,v78,v79
MM v[96:99],a[236:239],v[208:211],v[96:99]
EX v80,v80
MM v[100:103],a[252:255],v[208:211],v[128:131]
EX v81,v81
MM v[100:103],a[248:251],v[192:195],v[100:103]
TR a[178:179],v232 offset:5632
CV v80,v80,v81
MM v[100:103],a[244:247],v[176:179],v[100:103]
EX v82,v82
MM v[100:103],a[240:243],v[160:163],v[100:103]
TR a[180:181],v232 offset:9728
TR a[182:183],v232 offset:13824
MM v[108:111],a[240:243],v[164:167],v[132:135]
EX v83,v83
MM v[108:111],a[244:247],v[180:183],v[108:111]
TR a[184:185],v233 offset:1536
CV v81,v82,v83
MM v[108:111],a[248:251],v[196:199],v[108:111]
EX v84,v84
MM v[108:111],a[252:255],v[212:215],v[108:111]
EX v85,v85
MM v[104:107],a[236:239],v[212:215],v[132:135]
TR a[186:187],v233 offset:5632
CV v82,v84,v85
MM v[104:107],a[232:235],v[196:199],v[104:107]
EX v86,v86
MM v[104:107],a[228:231],v[180:183],v[104:107]
TR a[188:189],v233 offset:9728
TR a[190:191],v233 offset:13824
MM v[104:107],a[224:227],v[164:167],v[104:107]
EX v87,v87
MM v[112:115],a[224:227],v[168:171],v[136:139]
CV v83,v86,v87
MM v[112:115],a[228:231],v[184:187],v[112:115]
EX v88,v88
MM v[112:115],a[232:235],v[200:203],v[112:115]
EX v89,v89
MM v[112:115],a[236:239],v[216:219],v[112:115]
EX v90,v90
MM v[116:119],a[252:255],v[216:219],v[136:139]
EX v91,v91
MM v[116:119],a[248:251],v[200:203],v[116:119]
EX v92,v92
MM v[116:119],a[244:247],v[184:187],v[116:119]
EX v93,v93
MM v[116:119],a[240:243],v[168:171],v[116:119]
EX v94,v94
MM v[124:127],a[240:243],v[172:175],v[140:143]
EX v95,v95
MM v[124:127],a[244:247],v[188:191],v[124:127]
EX v96,v96
MM v[124:127],a[248:251],v[204:207],v[124:127]
EX v97,v97
MM v[124:127],a[252:255],v[220:223],v[124:127]
EX v98,v98
MM v[120:123],a[236:239],v[220:223],v[140:143]
EX v99,v99
MM v[120:123],a[232:235],v[204:207],v[120:123]
EX v100,v100
MM v[120:123],a[228:231],v[188:191],v[120:123]
EX v101,v101
MM v[120:123],a[224:227],v[172:175],v[120:123]
s_waitcnt vmcnt(13) lgkmcnt(0)
s_barrier
MM a[0:3],a[128:131],v[0:3],a[0:3]
s_add_u32 m0, s65, 0
s_add_u32 m0, s94, 139264
s_add_u32 s66, s86, 0
buffer_load_dwordx4 v241, s[24:27], s66 offen offset:0 lds
MM a[0:3],a[132:135],v[4:7],a[0:3]
CV v88,v88,v89
CV v89,v90,v91
MM a[8:11],a[132:135],v[12:15],a[8:11]
RD a[192:195],v224 offset:32768
CV v90,v92,v93
MM a[8:11],a[128:131],v[8:11],a[8:11]
RD a[196:199],v225 offset:32768
CV v91,v94,v95
MM a[16:19],a[128:131],v[16:19],a[16:19]
s_add_u32 m0, s65, 0
s_nop 0
buffer_load_dwordx4 v228, s[8:11], s64 offen offset:0 lds
MM a[16:19],a[132:135],v[20:23],a[16:19]
RD a[200:203],v226 offset:32768
CV v68,v96,v97
MM a[24:27],a[132:135],v[28:31],a[24:27]
RD a[204:207],v227 offset:32768
CV v69,v98,v99
MM a[24:27],a[128:131],v[24:27],a[24:27]
buffer_load_dwordx4 v229, s[8:11], s64 offen offset:1024 lds
MM a[28:31],a[136:139],v[24:27],a[28:31]
s_add_u32 m0, s94, 143360
s_add_u32 s66, s86, 81920
buffer_load_dwordx4 v241, s[24:27], s66 offen offset:0 lds
MM a[28:31],a[140:143],v[28:31],a[28:31]
RD a[208:211],v224 offset:36864
CV v70,v100,v101
MM a[20:23],a[140:143],v[20:23],a[20:23]
s_add_u32 m0, s65, 0
s_nop 0
buffer_load_dwordx4 v230, s[8:11], s64 offen offset:2048 lds
MM a[20:23],a[136:139],v[16:19],a[20:23]
RD a[212:215],v225 offset:36864
RD a[216:219],v226 offset:36864
MM a[12:15],a[136:139],v[8:11],a[12:15]
EX v102,v102
MM a[12:15],a[140:143],v[12:15],a[12:15]
RD a[220:223],v227 offset:36864
RD a[224:227],v224 offset:40960
MM a[4:7],a[140:143],v[4:7],a[4:7]
EX v103,v103
MM a[4:7],a[136:139],v[0:3],a[4:7]
RD a[228:231],v225 offset:40960
CV v71,v102,v103
MM a[32:35],a[144:147],v[0:3],a[32:35]
EX v104,v104
MM a[32:35],a[148:151],v[4:7],a[32:35]
buffer_load_dwordx4 v231, s[8:11], s64 offen offset:3072 lds
MM a[40:43],a[148:151],v[12:15],a[40:43]
s_add_u32 m0, s65, 65536
EX v105,v105
MM a[40:43],a[144:147],v[8:11],a[40:43]
RD a[232:235],v226 offset:40960
CV v76,v104,v105
MM a[48:51],a[144:147],v[16:19],a[48:51]
EX v106,v106
MM a[48:51],a[148:151],v[20:23],a[48:51]
EX v107,v107
MM a[56:59],a[148:151],v[28:31],a[56:59]
RD a[236:239],v227 offset:40960
CV v77,v106,v107
MM a[56:59],a[144:147],v[24:27],a[56:59]
EX v108,v108
MM a[60:63],a[152:155],v[24:27],a[60:63]
buffer_load_dwordx4 v235, s[12:15], s81 offen offset:0 lds
MM a[60:63],a[156:159],v[28:31],a[60:63]
EX v109,v109
MM a[52:55],a[156:159],v[20:23],a[52:55]
RD a[240:243],v224 offset:45056
CV v78,v108,v109
MM a[52:55],a[152:155],v[16:19],a[52:55]
EX v110,v110
MM a[44:47],a[152:155],v[8:11],a[44:47]
EX v111,v111
MM a[44:47],a[156:159],v[12:15],a[44:47]
RD a[244:247],v225 offset:45056
CV v79,v110,v111
MM a[36:39],a[156:159],v[4:7],a[36:39]
buffer_load_dwordx4 v235, s[12:15], s64 offen offset:1024 lds
MM a[36:39],a[152:155],v[0:3],a[36:39]
EX v112,v112
MM a[64:67],a[160:163],v[0:3],a[64:67]
EX v113,v113
MM a[64:67],a[164:167],v[4:7],a[64:67]
RD a[248:251],v226 offset:45056
CV v84,v112,v113
MM a[72:75],a[164:167],v[12:15],a[72:75]
EX v114,v114
MM a[72:75],a[160:163],v[8:11],a[72:75]
EX v115,v115
MM a[80:83],a[160:163],v[16:19],a[80:83]
RD a[252:255],v227 offset:45056
CV v85,v114,v115
MM a[80:83],a[164:167],v[20:23],a[80:83]
buffer_load_dwordx4 v235, s[12:15], s81 offen offset:2048 lds
MM a[88:91],a[164:167],v[28:31],a[88:91]
EX v116,v116
MM a[88:91],a[160:163],v[24:27],a[88:91]
EX v117,v117
MM a[92:95],a[168:171],v[24:27],a[92:95]
EX v118,v118
MM a[92:95],a[172:175],v[28:31],a[92:95]
)ASM"
R"ASM(
buffer_load_dwordx4 v235, s[12:15], s64 offen offset:3072 lds
MM a[84:87],a[172:175],v[20:23],a[84:87]
EX v119,v119
MM a[84:87],a[168:171],v[16:19],a[84:87]
EX v120,v120
MM a[76:79],a[168:171],v[8:11],a[76:79]
EX v121,v121
MM a[76:79],a[172:175],v[12:15],a[76:79]
EX v122,v122
MM a[68:71],a[172:175],v[4:7],a[68:71]
EX v123,v123
MM a[68:71],a[168:171],v[0:3],a[68:71]
EX v124,v124
MM a[96:99],a[176:179],v[0:3],a[96:99]
EX v125,v125
MM a[96:99],a[180:183],v[4:7],a[96:99]
EX v126,v126
MM a[104:107],a[180:183],v[12:15],a[104:107]
EX v127,v127
MM a[104:107],a[176:179],v[8:11],a[104:107]
CV v86,v116,v117
CV v87,v118,v119
MM a[112:115],a[176:179],v[16:19],a[112:115]
CV v92,v120,v121
CV v93,v122,v123
MM a[112:115],a[180:183],v[20:23],a[112:115]
CV v94,v124,v125
CV v95,v126,v127
MM a[120:123],a[180:183],v[28:31],a[120:123]
MM a[120:123],a[176:179],v[24:27],a[120:123]
MM a[124:127],a[184:187],v[24:27],a[124:127]
MM a[124:127],a[188:191],v[28:31],a[124:127]
MM a[116:119],a[188:191],v[20:23],a[116:119]
MM a[116:119],a[184:187],v[16:19],a[116:119]
MM a[108:111],a[184:187],v[8:11],a[108:111]
MM a[108:111],a[188:191],v[12:15],a[108:111]
MM a[100:103],a[188:191],v[4:7],a[100:103]
MM a[100:103],a[184:187],v[0:3],a[100:103]
v_smfmac_f32_16x16x64_bf16 v[236:239], v[144:147], v[64:71], v234
v_smfmac_f32_16x16x64_bf16 v[236:239], v[148:151], v[72:79], v234
v_smfmac_f32_16x16x64_bf16 v[236:239], v[152:155], v[80:87], v234
v_smfmac_f32_16x16x64_bf16 v[236:239], v[156:159], v[88:95], v234
s_waitcnt lgkmcnt(0)
MM v[0:3],a[192:195],v[160:163],v[128:131]
s_add_u32 s64, s64, 16384
TR a[128:129],v232 offset:16384
s_add_u32 s81, s64, 896
MM v[0:3],a[196:199],v[176:179],v[0:3]
TR a[130:131],v232 offset:20480
TR a[132:133],v232 offset:24576
MM v[0:3],a[200:203],v[192:195],v[0:3]
TR a[134:135],v232 offset:28672
TR a[136:137],v233 offset:16384
MM v[0:3],a[204:207],v[208:211],v[0:3]
TR a[138:139],v233 offset:20480
v_add_f32 v247, v236, v237
MM v[4:7],a[220:223],v[208:211],v[128:131]
TR a[140:141],v233 offset:24576
v_cmp_nge_f32 vcc, s71, v247
MM v[4:7],a[216:219],v[192:195],v[4:7]
s_cbranch_vccnz LSITE29_%=
LBACK29_%=:
MM v[4:7],a[212:215],v[176:179],v[4:7]
s_add_u32 m0, s94, 151552
s_add_u32 s66, s87, 81920
buffer_load_dwordx4 v241, s[24:27], s66 offen offset:0 lds
MM v[4:7],a[208:211],v[160:163],v[4:7]
v_bfe_u32 v96, v248, 6, 2
v_mul_u32_u24 v96, 1280, v96
MM v[12:15],a[208:211],v[164:167],v[132:135]
v_bfe_u32 v97, v248, 8, 1
v_mul_u32_u24 v97, 40960, v97
MM v[12:15],a[212:215],v[180:183],v[12:15]
v_add_u32 v96, v96, v97
v_and_b32 v97, 48, v248
MM v[12:15],a[216:219],v[196:199],v[12:15]
s_nop 0
v_mul_u32_u24 v97, 6, v97
v_add_u32 v96, v96, v97
MM v[12:15],a[220:223],v[212:215],v[12:15]
s_min_u32 s60, s18, s63
EX v0,v0
MM v[8:11],a[204:207],v[212:215],v[132:135]
s_add_u32 s61, s60, 131072
s_mov_b32 exec_hi, 0
buffer_load_dword v100, v96, s[32:35], s60 offen offset:0
s_mov_b64 exec, -1
MM v[8:11],a[200:203],v[196:199],v[8:11]
LPG18_%=:
s_cbranch_execz LPG22_%=
EX v1,v1
MM v[8:11],a[196:199],v[180:183],v[8:11]
s_add_u32 m0, s94, 155648
s_add_u32 s66, s88, 0
buffer_load_dwordx4 v241, s[24:27], s66 offen offset:0 lds
MM v[8:11],a[192:195],v[164:167],v[8:11]
s_mov_b32 exec_hi, 0
buffer_load_dword v101, v96, s[20:23], s61 offen offset:0 sc1
s_mov_b64 exec, -1
MM v[16:19],a[192:195],v[168:171],v[136:139]
EX v2,v2
MM v[16:19],a[196:199],v[184:187],v[16:19]
EX v3,v3
MM v[16:19],a[200:203],v[200:203],v[16:19]
TR a[142:143],v233 offset:28672
TR a[144:145],v232 offset:16896
MM v[16:19],a[204:207],v[216:219],v[16:19]
EX v4,v4
MM v[20:23],a[220:223],v[216:219],v[136:139]
EX v5,v5
MM v[20:23],a[216:219],v[200:203],v[20:23]
TR a[146:147],v232 offset:20992
CV v0,v0,v1
MM v[20:23],a[212:215],v[184:187],v[20:23]
s_add_u32 m0, s94, 159744
s_add_u32 s66, s88, 81920
buffer_load_dwordx4 v241, s[24:27], s66 offen offset:0 lds
MM v[20:23],a[208:211],v[168:171],v[20:23]
EX v6,v6
MM v[28:31],a[208:211],v[172:175],v[140:143]
TR a[148:149],v232 offset:25088
TR a[150:151],v232 offset:29184
MM v[28:31],a[212:215],v[188:191],v[28:31]
EX v7,v7
MM v[28:31],a[216:219],v[204:207],v[28:31]
EX v8,v8
MM v[28:31],a[220:223],v[220:223],v[28:31]
TR a[152:153],v233 offset:16896
CV v1,v2,v3
MM v[24:27],a[204:207],v[220:223],v[140:143]
EX v9,v9
MM v[24:27],a[200:203],v[204:207],v[24:27]
TR a[154:155],v233 offset:20992
CV v2,v4,v5
MM v[24:27],a[196:199],v[188:191],v[24:27]
EX v10,v10
MM v[24:27],a[192:195],v[172:175],v[24:27]
TR a[156:157],v233 offset:25088
CV v3,v6,v7
MM v[32:35],a[224:227],v[160:163],v[128:131]
EX v11,v11
MM v[32:35],a[228:231],v[176:179],v[32:35]
TR a[158:159],v233 offset:29184
TR a[160:161],v232 offset:17408
MM v[32:35],a[232:235],v[192:195],v[32:35]
EX v12,v12
MM v[32:35],a[236:239],v[208:211],v[32:35]
EX v13,v13
MM v[36:39],a[252:255],v[208:211],v[128:131]
TR a[162:163],v232 offset:21504
CV v8,v8,v9
MM v[36:39],a[248:251],v[192:195],v[36:39]
EX v14,v14
MM v[36:39],a[244:247],v[176:179],v[36:39]
TR a[164:165],v232 offset:25600
CV v9,v10,v11
MM v[36:39],a[240:243],v[160:163],v[36:39]
EX v15,v15
MM v[44:47],a[240:243],v[164:167],v[132:135]
TR a[166:167],v232 offset:29696
TR a[168:169],v233 offset:17408
MM v[44:47],a[244:247],v[180:183],v[44:47]
EX v16,v16
MM v[44:47],a[248:251],v[196:199],v[44:47]
EX v17,v17
MM v[44:47],a[252:255],v[212:215],v[44:47]
TR a[170:171],v233 offset:21504
CV v10,v12,v13
MM v[40:43],a[236:239],v[212:215],v[132:135]
EX v18,v18
MM v[40:43],a[232:235],v[196:199],v[40:43]
TR a[172:173],v233 offset:25600
CV v11,v14,v15
MM v[40:43],a[228:231],v[180:183],v[40:43]
EX v19,v19
MM v[40:43],a[224:227],v[164:167],v[40:43]
TR a[174:175],v233 offset:29696
TR a[176:177],v232 offset:17920
MM v[48:51],a[224:227],v[168:171],v[136:139]
EX v20,v20
MM v[48:51],a[228:231],v[184:187],v[48:51]
TR a[178:179],v232 offset:22016
CV v16,v16,v17
MM v[48:51],a[232:235],v[200:203],v[48:51]
EX v21,v21
MM v[48:51],a[236:239],v[216:219],v[48:51]
TR a[180:181],v232 offset:26112
CV v17,v18,v19
MM v[52:55],a[252:255],v[216:219],v[136:139]
EX v22,v22
MM v[52:55],a[248:251],v[200:203],v[52:55]
TR a[182:183],v232 offset:30208
TR a[184:185],v233 offset:17920
MM v[52:55],a[244:247],v[184:187],v[52:55]
EX v23,v23
MM v[52:55],a[240:243],v[168:171],v[52:55]
EX v24,v24
MM v[60:63],a[240:243],v[172:175],v[140:143]
TR a[186:187],v233 offset:22016
CV v18,v20,v21
MM v[60:63],a[244:247],v[188:191],v[60:63]
EX v25,v25
MM v[60:63],a[248:251],v[204:207],v[60:63]
TR a[188:189],v233 offset:26112
TR a[190:191],v233 offset:30208
MM v[60:63],a[252:255],v[220:223],v[60:63]
EX v26,v26
MM v[56:59],a[236:239],v[220:223],v[140:143]
CV v19,v22,v23
CV v24,v24,v25
MM v[56:59],a[232:235],v[204:207],v[56:59]
EX v27,v27
MM v[56:59],a[228:231],v[188:191],v[56:59]
EX v28,v28
MM v[56:59],a[224:227],v[172:175],v[56:59]
s_waitcnt vmcnt(19) lgkmcnt(0)
s_barrier
MM a[0:3],a[128:131],v[64:67],a[0:3]
EX v29,v29
MM a[0:3],a[132:135],v[68:71],a[0:3]
s_lshl_b32 s91, s47, 2
CV v25,v26,v27
CV v26,v28,v29
MM a[8:11],a[132:135],v[76:79],a[8:11]
s_add_u32 s91, s91, %[swave]
EX v30,v30
MM a[8:11],a[128:131],v[72:75],a[8:11]
s_lshl_b32 s91, s91, 2
EX v31,v31
MM a[16:19],a[128:131],v[80:83],a[16:19]
s_add_u32 s91, s91, 73728
RD a[192:195],v224 offset:49152
CV v27,v30,v31
MM a[16:19],a[132:135],v[84:87],a[16:19]
v_mov_b32 v250, s69
s_mov_b64 exec, 1
buffer_store_dword v250, off, s[20:23], s91 offset:0 sc1
s_mov_b64 exec, -1
MM a[24:27],a[132:135],v[92:95],a[24:27]
s_lshr_b32 s77, %[sb], 3
EX v32,v32
MM a[24:27],a[128:131],v[88:91],a[24:27]
s_lshl_b32 s77, s77, 2
EX v33,v33
MM a[28:31],a[136:139],v[88:91],a[28:31]
s_add_u32 s77, s77, %[swave]
s_add_u32 m0, s65, 16384
RD a[196:199],v225 offset:49152
MM a[28:31],a[140:143],v[92:95],a[28:31]
EX v34,v34
MM a[20:23],a[140:143],v[84:87],a[20:23]
buffer_load_dwordx4 v228, s[8:11], s64 offen offset:0 lds
MM a[20:23],a[136:139],v[80:83],a[20:23]
s_lshl_b32 s77, s77, 9
EX v35,v35
MM a[12:15],a[136:139],v[72:75],a[12:15]
s_add_u32 s77, s77, 0
v_lshrrev_b32 v253, 1, v248
v_add_u32 v253, s77, v253
MM a[12:15],a[140:143],v[76:79],a[12:15]
s_mov_b32 s77, LKEND_ka-LPD2_%=-16
v_min_u32 v253, s77, v253
CV v4,v32,v33
MM a[4:7],a[140:143],v[68:71],a[4:7]
s_getpc_b64 s[98:99]
LPD2_%=:
global_load_dword v253, v253, s[98:99]
MM a[4:7],a[136:139],v[64:67],a[4:7]
EX v36,v36
MM a[32:35],a[144:147],v[64:67],a[32:35]
EX v37,v37
MM a[32:35],a[148:151],v[68:71],a[32:35]
EX v38,v38
MM a[40:43],a[148:151],v[76:79],a[40:43]
RD a[200:203],v226 offset:49152
CV v5,v34,v35
MM a[40:43],a[144:147],v[72:75],a[40:43]
EX v39,v39
MM a[48:51],a[144:147],v[80:83],a[48:51]
buffer_load_dwordx4 v229, s[8:11], s64 offen offset:1024 lds
MM a[48:51],a[148:151],v[84:87],a[48:51]
EX v40,v40
MM a[56:59],a[148:151],v[92:95],a[56:59]
RD a[204:207],v227 offset:49152
CV v6,v36,v37
MM a[56:59],a[144:147],v[88:91],a[56:59]
EX v41,v41
MM a[60:63],a[152:155],v[88:91],a[60:63]
RD a[208:211],v224 offset:53248
CV v7,v38,v39
MM a[60:63],a[156:159],v[92:95],a[60:63]
EX v42,v42
MM a[52:55],a[156:159],v[84:87],a[52:55]
EX v43,v43
MM a[52:55],a[152:155],v[80:83],a[52:55]
buffer_load_dwordx4 v230, s[8:11], s64 offen offset:2048 lds
MM a[44:47],a[152:155],v[72:75],a[44:47]
EX v44,v44
MM a[44:47],a[156:159],v[76:79],a[44:47]
RD a[212:215],v225 offset:53248
CV v12,v40,v41
MM a[36:39],a[156:159],v[68:71],a[36:39]
EX v45,v45
MM a[36:39],a[152:155],v[64:67],a[36:39]
RD a[216:219],v226 offset:53248
CV v13,v42,v43
MM a[64:67],a[160:163],v[64:67],a[64:67]
EX v46,v46
MM a[64:67],a[164:167],v[68:71],a[64:67]
EX v47,v47
MM a[72:75],a[164:167],v[76:79],a[72:75]
buffer_load_dwordx4 v231, s[8:11], s64 offen offset:3072 lds
MM a[72:75],a[160:163],v[72:75],a[72:75]
s_add_u32 m0, s65, 81920
RD a[220:223],v227 offset:53248
CV v14,v44,v45
MM a[80:83],a[160:163],v[80:83],a[80:83]
EX v48,v48
MM a[80:83],a[164:167],v[84:87],a[80:83]
EX v49,v49
MM a[88:91],a[164:167],v[92:95],a[88:91]
RD a[224:227],v224 offset:57344
CV v15,v46,v47
MM a[88:91],a[160:163],v[88:91],a[88:91]
EX v50,v50
MM a[92:95],a[168:171],v[88:91],a[92:95]
EX v51,v51
MM a[92:95],a[172:175],v[92:95],a[92:95]
buffer_load_dwordx4 v235, s[12:15], s81 offen offset:0 lds
MM a[84:87],a[172:175],v[84:87],a[84:87]
RD a[228:231],v225 offset:57344
CV v20,v48,v49
MM a[84:87],a[168:171],v[80:83],a[84:87]
EX v52,v52
MM a[76:79],a[168:171],v[72:75],a[76:79]
EX v53,v53
MM a[76:79],a[172:175],v[76:79],a[76:79]
RD a[232:235],v226 offset:57344
CV v21,v50,v51
MM a[68:71],a[172:175],v[68:71],a[68:71]
EX v54,v54
MM a[68:71],a[168:171],v[64:67],a[68:71]
RD a[236:239],v227 offset:57344
CV v22,v52,v53
MM a[96:99],a[176:179],v[64:67],a[96:99]
EX v55,v55
MM a[96:99],a[180:183],v[68:71],a[96:99]
buffer_load_dwordx4 v235, s[12:15], s64 offen offset:1024 lds
MM a[104:107],a[180:183],v[76:79],a[104:107]
EX v56,v56
MM a[104:107],a[176:179],v[72:75],a[104:107]
RD a[240:243],v224 offset:61440
CV v23,v54,v55
MM a[112:115],a[176:179],v[80:83],a[112:115]
EX v57,v57
MM a[112:115],a[180:183],v[84:87],a[112:115]
EX v58,v58
MM a[120:123],a[180:183],v[92:95],a[120:123]
RD a[244:247],v225 offset:61440
CV v28,v56,v57
MM a[120:123],a[176:179],v[88:91],a[120:123]
EX v59,v59
MM a[124:127],a[184:187],v[88:91],a[124:127]
buffer_load_dwordx4 v235, s[12:15], s81 offen offset:2048 lds
MM a[124:127],a[188:191],v[92:95],a[124:127]
s_waitcnt vmcnt(10)
EX v60,v60
MM a[116:119],a[188:191],v[84:87],a[116:119]
RD a[248:251],v226 offset:61440
CV v29,v58,v59
MM a[116:119],a[184:187],v[80:83],a[116:119]
EX v61,v61
MM a[108:111],a[184:187],v[72:75],a[108:111]
EX v62,v62
MM a[108:111],a[188:191],v[76:79],a[108:111]
RD a[252:255],v227 offset:61440
CV v30,v60,v61
MM a[100:103],a[188:191],v[68:71],a[100:103]
EX v63,v63
MM a[100:103],a[184:187],v[64:67],a[100:103]
buffer_load_dwordx4 v235, s[12:15], s64 offen offset:3072 lds
v_smfmac_f32_16x16x64_bf16 v[236:239], v[144:147], v[0:7], v234
CV v31,v62,v63
v_smfmac_f32_16x16x64_bf16 v[236:239], v[148:151], v[8:15], v234
v_smfmac_f32_16x16x64_bf16 v[236:239], v[152:155], v[16:23], v234
v_smfmac_f32_16x16x64_bf16 v[236:239], v[156:159], v[24:31], v234
s_waitcnt lgkmcnt(0)
MM v[64:67],a[192:195],v[160:163],v[128:131]
)ASM"
R"ASM(
s_add_u32 s64, s64, 16384
TR a[128:129],v232 offset:32768
s_add_u32 s81, s64, 896
MM v[64:67],a[196:199],v[176:179],v[64:67]
TR a[130:131],v232 offset:36864
TR a[132:133],v232 offset:40960
MM v[64:67],a[200:203],v[192:195],v[64:67]
TR a[134:135],v232 offset:45056
TR a[136:137],v233 offset:32768
MM v[64:67],a[204:207],v[208:211],v[64:67]
TR a[138:139],v233 offset:36864
v_add_f32 v246, v236, v237
MM v[68:71],a[220:223],v[208:211],v[128:131]
TR a[140:141],v233 offset:40960
v_cmp_nge_f32 vcc, s71, v246
MM v[68:71],a[216:219],v[192:195],v[68:71]
s_cbranch_vccnz LSITE30_%=
LBACK30_%=:
MM v[68:71],a[212:215],v[176:179],v[68:71]
TR a[142:143],v233 offset:45056
TR a[144:145],v232 offset:33280
MM v[68:71],a[208:211],v[160:163],v[68:71]
TR a[146:147],v232 offset:37376
TR a[148:149],v232 offset:41472
MM v[76:79],a[208:211],v[164:167],v[132:135]
EX v64,v64
MM v[76:79],a[212:215],v[180:183],v[76:79]
EX v65,v65
MM v[76:79],a[216:219],v[196:199],v[76:79]
TR a[150:151],v232 offset:45568
CV v64,v64,v65
MM v[76:79],a[220:223],v[212:215],v[76:79]
EX v66,v66
MM v[72:75],a[204:207],v[212:215],v[132:135]
EX v67,v67
MM v[72:75],a[200:203],v[196:199],v[72:75]
TR a[152:153],v233 offset:33280
CV v65,v66,v67
MM v[72:75],a[196:199],v[180:183],v[72:75]
EX v68,v68
MM v[72:75],a[192:195],v[164:167],v[72:75]
EX v69,v69
MM v[80:83],a[192:195],v[168:171],v[136:139]
TR a[154:155],v233 offset:37376
CV v66,v68,v69
MM v[80:83],a[196:199],v[184:187],v[80:83]
EX v70,v70
MM v[80:83],a[200:203],v[200:203],v[80:83]
EX v71,v71
MM v[80:83],a[204:207],v[216:219],v[80:83]
TR a[156:157],v233 offset:41472
CV v67,v70,v71
MM v[84:87],a[220:223],v[216:219],v[136:139]
EX v72,v72
MM v[84:87],a[216:219],v[200:203],v[84:87]
TR a[158:159],v233 offset:45568
TR a[160:161],v232 offset:33792
MM v[84:87],a[212:215],v[184:187],v[84:87]
EX v73,v73
MM v[84:87],a[208:211],v[168:171],v[84:87]
TR a[162:163],v232 offset:37888
CV v72,v72,v73
MM v[92:95],a[208:211],v[172:175],v[140:143]
EX v74,v74
MM v[92:95],a[212:215],v[188:191],v[92:95]
TR a[164:165],v232 offset:41984
TR a[166:167],v232 offset:46080
MM v[92:95],a[216:219],v[204:207],v[92:95]
EX v75,v75
MM v[92:95],a[220:223],v[220:223],v[92:95]
TR a[168:169],v233 offset:33792
CV v73,v74,v75
MM v[88:91],a[204:207],v[220:223],v[140:143]
EX v76,v76
MM v[88:91],a[200:203],v[204:207],v[88:91]
EX v77,v77
MM v[88:91],a[196:199],v[188:191],v[88:91]
TR a[170:171],v233 offset:37888
CV v74,v76,v77
MM v[88:91],a[192:195],v[172:175],v[88:91]
EX v78,v78
MM v[96:99],a[224:227],v[160:163],v[128:131]
TR a[172:173],v233 offset:41984
TR a[174:175],v233 offset:46080
MM v[96:99],a[228:231],v[176:179],v[96:99]
EX v79,v79
MM v[96:99],a[232:235],v[192:195],v[96:99]
TR a[176:177],v232 offset:34304
CV v75,v78,v79
MM v[96:99],a[236:239],v[208:211],v[96:99]
EX v80,v80
MM v[100:103],a[252:255],v[208:211],v[128:131]
EX v81,v81
MM v[100:103],a[248:251],v[192:195],v[100:103]
TR a[178:179],v232 offset:38400
CV v80,v80,v81
MM v[100:103],a[244:247],v[176:179],v[100:103]
EX v82,v82
MM v[100:103],a[240:243],v[160:163],v[100:103]
TR a[180:181],v232 offset:42496
TR a[182:183],v232 offset:46592
MM v[108:111],a[240:243],v[164:167],v[132:135]
EX v83,v83
MM v[108:111],a[244:247],v[180:183],v[108:111]
TR a[184:185],v233 offset:34304
CV v81,v82,v83
MM v[108:111],a[248:251],v[196:199],v[108:111]
EX v84,v84
MM v[108:111],a[252:255],v[212:215],v[108:111]
EX v85,v85
MM v[104:107],a[236:239],v[212:215],v[132:135]
TR a[186:187],v233 offset:38400
CV v82,v84,v85
MM v[104:107],a[232:235],v[196:199],v[104:107]
EX v86,v86
MM v[104:107],a[228:231],v[180:183],v[104:107]
TR a[188:189],v233 offset:42496
TR a[190:191],v233 offset:46592
MM v[104:107],a[224:227],v[164:167],v[104:107]
EX v87,v87
MM v[112:115],a[224:227],v[168:171],v[136:139]
CV v83,v86,v87
MM v[112:115],a[228:231],v[184:187],v[112:115]
EX v88,v88
MM v[112:115],a[232:235],v[200:203],v[112:115]
EX v89,v89
MM v[112:115],a[236:239],v[216:219],v[112:115]
EX v90,v90
MM v[116:119],a[252:255],v[216:219],v[136:139]
EX v91,v91
MM v[116:119],a[248:251],v[200:203],v[116:119]
EX v92,v92
MM v[116:119],a[244:247],v[184:187],v[116:119]
EX v93,v93
MM v[116:119],a[240:243],v[168:171],v[116:119]
EX v94,v94
MM v[124:127],a[240:243],v[172:175],v[140:143]
EX v95,v95
MM v[124:127],a[244:247],v[188:191],v[124:127]
EX v96,v96
MM v[124:127],a[248:251],v[204:207],v[124:127]
EX v97,v97
MM v[124:127],a[252:255],v[220:223],v[124:127]
EX v98,v98
MM v[120:123],a[236:239],v[220:223],v[140:143]
EX v99,v99
MM v[120:123],a[232:235],v[204:207],v[120:123]
EX v100,v100
MM v[120:123],a[228:231],v[188:191],v[120:123]
EX v101,v101
MM v[120:123],a[224:227],v[172:175],v[120:123]
s_waitcnt lgkmcnt(0)
s_barrier
MM a[0:3],a[128:131],v[0:3],a[0:3]
s_add_u32 m0, s65, 32768
CV v88,v88,v89
CV v89,v90,v91
MM a[0:3],a[132:135],v[4:7],a[0:3]
RD a[192:195],v224 offset:0
CV v90,v92,v93
MM a[8:11],a[132:135],v[12:15],a[8:11]
RD a[196:199],v225 offset:0
CV v91,v94,v95
MM a[8:11],a[128:131],v[8:11],a[8:11]
buffer_load_dwordx4 v228, s[8:11], s64 offen offset:0 lds
MM a[16:19],a[128:131],v[16:19],a[16:19]
RD a[200:203],v226 offset:0
CV v68,v96,v97
MM a[16:19],a[132:135],v[20:23],a[16:19]
RD a[204:207],v227 offset:0
CV v69,v98,v99
MM a[24:27],a[132:135],v[28:31],a[24:27]
buffer_load_dwordx4 v229, s[8:11], s64 offen offset:1024 lds
MM a[24:27],a[128:131],v[24:27],a[24:27]
RD a[208:211],v224 offset:4096
CV v70,v100,v101
MM a[28:31],a[136:139],v[24:27],a[28:31]
RD a[212:215],v225 offset:4096
RD a[216:219],v226 offset:4096
MM a[28:31],a[140:143],v[28:31],a[28:31]
buffer_load_dwordx4 v230, s[8:11], s64 offen offset:2048 lds
MM a[20:23],a[140:143],v[20:23],a[20:23]
RD a[220:223],v227 offset:4096
RD a[224:227],v224 offset:8192
MM a[20:23],a[136:139],v[16:19],a[20:23]
EX v102,v102
MM a[12:15],a[136:139],v[8:11],a[12:15]
buffer_load_dwordx4 v231, s[8:11], s64 offen offset:3072 lds
MM a[12:15],a[140:143],v[12:15],a[12:15]
s_add_u32 m0, s65, 98304
EX v103,v103
MM a[4:7],a[140:143],v[4:7],a[4:7]
RD a[228:231],v225 offset:8192
CV v71,v102,v103
MM a[4:7],a[136:139],v[0:3],a[4:7]
EX v104,v104
MM a[32:35],a[144:147],v[0:3],a[32:35]
EX v105,v105
MM a[32:35],a[148:151],v[4:7],a[32:35]
RD a[232:235],v226 offset:8192
CV v76,v104,v105
MM a[40:43],a[148:151],v[12:15],a[40:43]
EX v106,v106
MM a[40:43],a[144:147],v[8:11],a[40:43]
buffer_load_dwordx4 v235, s[12:15], s81 offen offset:0 lds
MM a[48:51],a[144:147],v[16:19],a[48:51]
EX v107,v107
MM a[48:51],a[148:151],v[20:23],a[48:51]
RD a[236:239],v227 offset:8192
CV v77,v106,v107
MM a[56:59],a[148:151],v[28:31],a[56:59]
EX v108,v108
MM a[56:59],a[144:147],v[24:27],a[56:59]
EX v109,v109
MM a[60:63],a[152:155],v[24:27],a[60:63]
RD a[240:243],v224 offset:12288
CV v78,v108,v109
MM a[60:63],a[156:159],v[28:31],a[60:63]
buffer_load_dwordx4 v235, s[12:15], s64 offen offset:1024 lds
MM a[52:55],a[156:159],v[20:23],a[52:55]
EX v110,v110
MM a[52:55],a[152:155],v[16:19],a[52:55]
EX v111,v111
MM a[44:47],a[152:155],v[8:11],a[44:47]
RD a[244:247],v225 offset:12288
CV v79,v110,v111
MM a[44:47],a[156:159],v[12:15],a[44:47]
EX v112,v112
MM a[36:39],a[156:159],v[4:7],a[36:39]
EX v113,v113
MM a[36:39],a[152:155],v[0:3],a[36:39]
RD a[248:251],v226 offset:12288
CV v84,v112,v113
MM a[64:67],a[160:163],v[0:3],a[64:67]
buffer_load_dwordx4 v235, s[12:15], s81 offen offset:2048 lds
MM a[64:67],a[164:167],v[4:7],a[64:67]
EX v114,v114
MM a[72:75],a[164:167],v[12:15],a[72:75]
EX v115,v115
MM a[72:75],a[160:163],v[8:11],a[72:75]
RD a[252:255],v227 offset:12288
CV v85,v114,v115
MM a[80:83],a[160:163],v[16:19],a[80:83]
EX v116,v116
MM a[80:83],a[164:167],v[20:23],a[80:83]
buffer_load_dwordx4 v235, s[12:15], s64 offen offset:3072 lds
MM a[88:91],a[164:167],v[28:31],a[88:91]
EX v117,v117
MM a[88:91],a[160:163],v[24:27],a[88:91]
EX v118,v118
MM a[92:95],a[168:171],v[24:27],a[92:95]
EX v119,v119
MM a[92:95],a[172:175],v[28:31],a[92:95]
EX v120,v120
MM a[84:87],a[172:175],v[20:23],a[84:87]
EX v121,v121
MM a[84:87],a[168:171],v[16:19],a[84:87]
EX v122,v122
MM a[76:79],a[168:171],v[8:11],a[76:79]
EX v123,v123
MM a[76:79],a[172:175],v[12:15],a[76:79]
EX v124,v124
MM a[68:71],a[172:175],v[4:7],a[68:71]
EX v125,v125
MM a[68:71],a[168:171],v[0:3],a[68:71]
EX v126,v126
MM a[96:99],a[176:179],v[0:3],a[96:99]
EX v127,v127
MM a[96:99],a[180:183],v[4:7],a[96:99]
CV v86,v116,v117
CV v87,v118,v119
MM a[104:107],a[180:183],v[12:15],a[104:107]
CV v92,v120,v121
CV v93,v122,v123
MM a[104:107],a[176:179],v[8:11],a[104:107]
CV v94,v124,v125
CV v95,v126,v127
MM a[112:115],a[176:179],v[16:19],a[112:115]
MM a[112:115],a[180:183],v[20:23],a[112:115]
MM a[120:123],a[180:183],v[28:31],a[120:123]
MM a[120:123],a[176:179],v[24:27],a[120:123]
MM a[124:127],a[184:187],v[24:27],a[124:127]
s_nop 0
MM a[124:127],a[188:191],v[28:31],a[124:127]
s_nop 0
MM a[116:119],a[188:191],v[20:23],a[116:119]
s_nop 0
MM a[116:119],a[184:187],v[16:19],a[116:119]
s_nop 0
MM a[108:111],a[184:187],v[8:11],a[108:111]
s_nop 0
MM a[108:111],a[188:191],v[12:15],a[108:111]
s_nop 0
MM a[100:103],a[188:191],v[4:7],a[100:103]
s_nop 0
MM a[100:103],a[184:187],v[0:3],a[100:103]
LPG19_%=:
s_cbranch_execz LPG23_%=
v_smfmac_f32_16x16x64_bf16 v[236:239], v[144:147], v[64:71], v234
v_smfmac_f32_16x16x64_bf16 v[236:239], v[148:151], v[72:79], v234
v_smfmac_f32_16x16x64_bf16 v[236:239], v[152:155], v[80:87], v234
v_smfmac_f32_16x16x64_bf16 v[236:239], v[156:159], v[88:95], v234
s_waitcnt lgkmcnt(0)
MM v[16:19],a[192:195],v[168:171],v[136:139]
TR a[128:129],v232 offset:49152
TR a[130:131],v232 offset:53248
MM v[16:19],a[196:199],v[184:187],v[16:19]
TR a[132:133],v232 offset:57344
TR a[134:135],v232 offset:61440
MM v[16:19],a[200:203],v[200:203],v[16:19]
TR a[136:137],v233 offset:49152
TR a[138:139],v233 offset:53248
MM v[16:19],a[204:207],v[216:219],v[16:19]
TR a[140:141],v233 offset:57344
v_add_f32 v247, v236, v237
MM v[20:23],a[220:223],v[216:219],v[136:139]
TR a[142:143],v233 offset:61440
v_cmp_nge_f32 vcc, s71, v247
MM v[20:23],a[216:219],v[200:203],v[20:23]
s_cbranch_vccnz LSITE31_%=
LBACK31_%=:
TR a[144:145],v232 offset:49664
TR a[146:147],v232 offset:53760
MM v[20:23],a[212:215],v[184:187],v[20:23]
v_rcp_f32 v145, v247
MM v[20:23],a[208:211],v[168:171],v[20:23]
TR a[148:149],v232 offset:57856
TR a[150:151],v232 offset:61952
MM v[28:31],a[208:211],v[172:175],v[140:143]
EX v16,v16
MM v[28:31],a[212:215],v[188:191],v[28:31]
TR a[152:153],v233 offset:49664
TR a[154:155],v233 offset:53760
MM v[28:31],a[216:219],v[204:207],v[28:31]
EX v17,v17
MM v[28:31],a[220:223],v[220:223],v[28:31]
TR a[156:157],v233 offset:57856
CV v16,v16,v17
MM v[24:27],a[204:207],v[220:223],v[140:143]
TR a[158:159],v233 offset:61952
TR a[160:161],v232 offset:50176
MM v[24:27],a[200:203],v[204:207],v[24:27]
TR a[162:163],v232 offset:54272
TR a[164:165],v232 offset:58368
MM v[24:27],a[196:199],v[188:191],v[24:27]
EX v18,v18
MM v[24:27],a[192:195],v[172:175],v[24:27]
TR a[166:167],v232 offset:62464
TR a[168:169],v233 offset:50176
MM v[48:51],a[224:227],v[168:171],v[136:139]
EX v19,v19
MM v[48:51],a[228:231],v[184:187],v[48:51]
TR a[170:171],v233 offset:54272
CV v17,v18,v19
MM v[48:51],a[232:235],v[200:203],v[48:51]
TR a[172:173],v233 offset:58368
TR a[174:175],v233 offset:62464
MM v[48:51],a[236:239],v[216:219],v[48:51]
TR a[176:177],v232 offset:50688
TR a[178:179],v232 offset:54784
MM v[52:55],a[252:255],v[216:219],v[136:139]
EX v20,v20
MM v[52:55],a[248:251],v[200:203],v[52:55]
TR a[180:181],v232 offset:58880
TR a[182:183],v232 offset:62976
MM v[52:55],a[244:247],v[184:187],v[52:55]
EX v21,v21
MM v[52:55],a[240:243],v[168:171],v[52:55]
TR a[184:185],v233 offset:50688
TR a[186:187],v233 offset:54784
MM v[60:63],a[240:243],v[172:175],v[140:143]
TR a[188:189],v233 offset:58880
CV v18,v20,v21
MM v[60:63],a[244:247],v[188:191],v[60:63]
TR a[190:191],v233 offset:62976
v_mov_b32 v144, v145
MM v[60:63],a[248:251],v[204:207],v[60:63]
EX v22,v22
MM v[60:63],a[252:255],v[220:223],v[60:63]
EX v23,v23
MM v[56:59],a[236:239],v[220:223],v[140:143]
CV v19,v22,v23
v_permlane32_swap_b32 v145, v144
MM v[56:59],a[232:235],v[204:207],v[56:59]
EX v24,v24
MM v[56:59],a[228:231],v[188:191],v[56:59]
)ASM"
R"ASM(
EX v25,v25
MM v[56:59],a[224:227],v[172:175],v[56:59]
s_waitcnt vmcnt(12) lgkmcnt(0)
s_barrier
CV v24,v24,v25
MM a[0:3],a[128:131],v[64:67],a[0:3]
s_add_u32 m0, s89, 49152
RD a[192:195],v224 offset:16384
RD a[196:199],v225 offset:16384
MM a[0:3],a[132:135],v[68:71],a[0:3]
s_add_u32 s66, s85, 0
EX v26,v26
MM a[8:11],a[132:135],v[76:79],a[8:11]
buffer_load_dwordx4 v240, s[24:27], s66 offen offset:0 lds
MM a[8:11],a[128:131],v[72:75],a[8:11]
s_add_u32 m0, s89, 50176
RD a[200:203],v226 offset:16384
RD a[204:207],v227 offset:16384
MM a[12:15],a[136:139],v[72:75],a[12:15]
s_add_u32 s66, s85, 10240
EX v27,v27
MM a[12:15],a[140:143],v[76:79],a[12:15]
RD a[208:211],v224 offset:20480
CV v25,v26,v27
MM a[4:7],a[140:143],v[68:71],a[4:7]
buffer_load_dwordx4 v240, s[24:27], s66 offen offset:0 lds
MM a[4:7],a[136:139],v[64:67],a[4:7]
s_add_u32 m0, s89, 57344
EX v28,v28
MM a[32:35],a[144:147],v[64:67],a[32:35]
s_add_u32 s66, s85, 81920
EX v29,v29
MM a[32:35],a[148:151],v[68:71],a[32:35]
CV v26,v28,v29
v_mov_b32 v149, v145
MM a[40:43],a[148:151],v[76:79],a[40:43]
RD a[212:215],v225 offset:20480
v_permlane16_swap_b32 v145, v149
MM a[40:43],a[144:147],v[72:75],a[40:43]
s_mul_i32 s78, s49, 17408
s_add_u32 s78, s83, s78
MM a[44:47],a[152:155],v[72:75],a[44:47]
s_add_u32 s78, s78, 0
s_add_u32 s79, s78, 4096
MM a[44:47],a[156:159],v[76:79],a[44:47]
AR v0,a0
AR v1,a1
MM a[36:39],a[156:159],v[68:71],a[36:39]
AR v2,a2
AR v3,a3
MM a[36:39],a[152:155],v[64:67],a[36:39]
AR v4,a4
AR v5,a5
MM a[64:67],a[160:163],v[64:67],a[64:67]
AR v6,a6
AR v7,a7
MM a[64:67],a[164:167],v[68:71],a[64:67]
v_mul_f32 v0, v0, v145
v_mul_f32 v1, v1, v145
MM a[72:75],a[164:167],v[76:79],a[72:75]
v_mul_f32 v2, v2, v145
v_mul_f32 v3, v3, v145
MM a[72:75],a[160:163],v[72:75],a[72:75]
EX v30,v30
MM a[76:79],a[168:171],v[72:75],a[76:79]
v_mul_f32 v4, v4, v145
v_mul_f32 v5, v5, v145
MM a[76:79],a[172:175],v[76:79],a[76:79]
v_mul_f32 v6, v6, v145
v_mul_f32 v7, v7, v145
EX v31,v31
MM a[68:71],a[172:175],v[68:71],a[68:71]
CV v160,v0,v1
CV v161,v2,v3
MM a[68:71],a[168:171],v[64:67],a[68:71]
CV v162,v4,v5
CV v163,v6,v7
MM a[96:99],a[176:179],v[64:67],a[96:99]
buffer_store_dwordx4 v[160:163], v248, s[20:23], s78 offen offset:0 sc1
MM a[96:99],a[180:183],v[68:71],a[96:99]
AR v8,a8
AR v9,a9
EX v48,v48
MM a[104:107],a[180:183],v[76:79],a[104:107]
AR v10,a10
AR v11,a11
MM a[104:107],a[176:179],v[72:75],a[104:107]
AR v12,a12
AR v13,a13
EX v49,v49
MM a[108:111],a[184:187],v[72:75],a[108:111]
AR v14,a14
AR v15,a15
MM a[108:111],a[188:191],v[76:79],a[108:111]
v_mul_f32 v8, v8, v149
v_mul_f32 v9, v9, v149
EX v50,v50
MM a[100:103],a[188:191],v[68:71],a[100:103]
v_mul_f32 v10, v10, v149
v_mul_f32 v11, v11, v149
MM a[100:103],a[184:187],v[64:67],a[100:103]
v_mul_f32 v12, v12, v149
v_mul_f32 v13, v13, v149
MM a[16:19],a[128:131],v[80:83],a[16:19]
v_mul_f32 v14, v14, v149
v_mul_f32 v15, v15, v149
EX v51,v51
MM a[16:19],a[132:135],v[84:87],a[16:19]
buffer_load_dwordx4 v240, s[24:27], s66 offen offset:0 lds
MM a[24:27],a[132:135],v[92:95],a[24:27]
s_add_u32 m0, s89, 58368
CV v164,v8,v9
CV v165,v10,v11
CV v166,v12,v13
MM a[24:27],a[128:131],v[88:91],a[24:27]
s_add_u32 s66, s85, 92160
CV v167,v14,v15
CV v27,v30,v31
EX v52,v52
MM a[28:31],a[136:139],v[88:91],a[28:31]
buffer_store_dwordx4 v[164:167], v248, s[20:23], s79 offen offset:0 sc1
MM a[28:31],a[140:143],v[92:95],a[28:31]
AR v32,a32
AR v33,a33
EX v53,v53
AR v34,a34
MM a[20:23],a[140:143],v[84:87],a[20:23]
AR v35,a35
AR v36,a36
MM a[20:23],a[136:139],v[80:83],a[20:23]
RD a[216:219],v226 offset:20480
AR v37,a37
AR v38,a38
MM a[48:51],a[144:147],v[80:83],a[48:51]
AR v39,a39
v_mul_f32 v32, v32, v145
EX v54,v54
MM a[48:51],a[148:151],v[84:87],a[48:51]
buffer_load_dwordx4 v240, s[24:27], s66 offen offset:0 lds
MM a[56:59],a[148:151],v[92:95],a[56:59]
s_add_u32 m0, s89, 114688
RD a[220:223],v227 offset:20480
v_mul_f32 v33, v33, v145
v_mul_f32 v34, v34, v145
v_mul_f32 v35, v35, v145
CV v20,v48,v49
EX v55,v55
MM a[56:59],a[144:147],v[88:91],a[56:59]
s_add_u32 s66, s86, 0
v_mul_f32 v36, v36, v145
v_mul_f32 v37, v37, v145
MM a[60:63],a[152:155],v[88:91],a[60:63]
v_mul_f32 v38, v38, v145
v_mul_f32 v39, v39, v145
MM a[60:63],a[156:159],v[92:95],a[60:63]
CV v176,v32,v33
CV v177,v34,v35
EX v56,v56
MM a[52:55],a[156:159],v[84:87],a[52:55]
RD a[224:227],v224 offset:24576
CV v178,v36,v37
CV v179,v38,v39
CV v21,v50,v51
MM a[52:55],a[152:155],v[80:83],a[52:55]
buffer_store_dwordx4 v[176:179], v248, s[20:23], s78 offen offset:1024 sc1
MM a[80:83],a[160:163],v[80:83],a[80:83]
AR v40,a40
AR v41,a41
EX v57,v57
MM a[80:83],a[164:167],v[84:87],a[80:83]
AR v42,a42
AR v43,a43
MM a[88:91],a[164:167],v[92:95],a[88:91]
buffer_load_dwordx4 v240, s[24:27], s66 offen offset:0 lds
MM a[88:91],a[160:163],v[88:91],a[88:91]
s_add_u32 m0, s89, 115712
RD a[228:231],v225 offset:24576
AR v44,a44
AR v45,a45
AR v46,a46
EX v58,v58
CV v22,v52,v53
MM a[92:95],a[168:171],v[88:91],a[92:95]
s_add_u32 s66, s86, 10240
AR v47,a47
v_mul_f32 v40, v40, v149
MM a[92:95],a[172:175],v[92:95],a[92:95]
v_mul_f32 v41, v41, v149
v_mul_f32 v42, v42, v149
EX v59,v59
MM a[84:87],a[172:175],v[84:87],a[84:87]
RD a[232:235],v226 offset:24576
v_mul_f32 v43, v43, v149
v_mul_f32 v44, v44, v149
MM a[84:87],a[168:171],v[80:83],a[84:87]
v_mul_f32 v45, v45, v149
v_mul_f32 v46, v46, v149
MM a[112:115],a[176:179],v[80:83],a[112:115]
v_mul_f32 v47, v47, v149
CV v180,v40,v41
EX v60,v60
CV v23,v54,v55
MM a[112:115],a[180:183],v[84:87],a[112:115]
CV v181,v42,v43
CV v182,v44,v45
MM a[120:123],a[180:183],v[92:95],a[120:123]
buffer_load_dwordx4 v240, s[24:27], s66 offen offset:0 lds
MM a[120:123],a[176:179],v[88:91],a[120:123]
s_add_u32 m0, s89, 122880
RD a[236:239],v227 offset:24576
CV v183,v46,v47
EX v61,v61
MM a[124:127],a[184:187],v[88:91],a[124:127]
buffer_store_dwordx4 v[180:183], v248, s[20:23], s79 offen offset:1024 sc1
MM a[124:127],a[188:191],v[92:95],a[124:127]
s_add_u32 s66, s86, 81920
AR v0,a64
AR v1,a65
AR v2,a66
AR v3,a67
EX v62,v62
AR v4,a68
MM a[116:119],a[188:191],v[84:87],a[116:119]
RD a[240:243],v224 offset:28672
RD a[244:247],v225 offset:28672
RD a[248:251],v226 offset:28672
RD a[252:255],v227 offset:28672
EX v63,v63
AR v5,a69
CV v28,v56,v57
MM a[116:119],a[184:187],v[80:83],a[116:119]
AR v6,a70
AR v7,a71
v_smfmac_f32_16x16x64_bf16 v[236:239], v[152:155], v[16:23], v234
buffer_load_dwordx4 v240, s[24:27], s66 offen offset:0 lds
CV v29,v58,v59
CV v30,v60,v61
CV v31,v62,v63
s_nop 1
v_smfmac_f32_16x16x64_bf16 v[236:239], v[156:159], v[24:31], v234
s_waitcnt lgkmcnt(0)
s_add_u32 m0, s89, 123904
v_mul_f32 v0, v0, v145
v_mul_f32 v1, v1, v145
v_mul_f32 v2, v2, v145
v_mul_f32 v3, v3, v145
s_add_u32 s66, s86, 92160
buffer_load_dwordx4 v240, s[24:27], s66 offen offset:0 lds
MM v[80:83],a[192:195],v[168:171],v[136:139]
v_mul_f32 v4, v4, v145
v_mul_f32 v5, v5, v145
MM v[80:83],a[196:199],v[184:187],v[80:83]
v_mul_f32 v6, v6, v145
v_mul_f32 v7, v7, v145
MM v[80:83],a[200:203],v[200:203],v[80:83]
CV v192,v0,v1
CV v193,v2,v3
MM v[80:83],a[204:207],v[216:219],v[80:83]
TR a[128:129],v232 offset:0
v_add_f32 v246, v236, v237
CV v194,v4,v5
CV v195,v6,v7
MM v[84:87],a[220:223],v[216:219],v[136:139]
TR a[130:131],v232 offset:4096
TR a[132:133],v232 offset:8192
v_cmp_nge_f32 vcc, s71, v246
MM v[84:87],a[216:219],v[200:203],v[84:87]
s_cbranch_vccnz LSITE32_%=
LBACK32_%=:
buffer_store_dwordx4 v[192:195], v248, s[20:23], s78 offen offset:2048 sc1
MM v[84:87],a[212:215],v[184:187],v[84:87]
TR a[134:135],v232 offset:12288
TR a[136:137],v233 offset:0
AR v8,a72
AR v9,a73
AR v10,a74
AR v11,a75
MM v[84:87],a[208:211],v[168:171],v[84:87]
TR a[138:139],v233 offset:4096
AR v12,a76
MM v[92:95],a[208:211],v[172:175],v[140:143]
TR a[140:141],v233 offset:8192
TR a[142:143],v233 offset:12288
AR v13,a77
AR v14,a78
MM v[92:95],a[212:215],v[188:191],v[92:95]
TR a[144:145],v232 offset:512
AR v15,a79
v_mul_f32 v8, v8, v149
MM v[92:95],a[216:219],v[204:207],v[92:95]
TR a[146:147],v232 offset:4608
v_mul_f32 v9, v9, v149
v_mul_f32 v10, v10, v149
MM v[92:95],a[220:223],v[220:223],v[92:95]
TR a[148:149],v232 offset:8704
v_mul_f32 v11, v11, v149
v_mul_f32 v12, v12, v149
MM v[88:91],a[204:207],v[220:223],v[140:143]
TR a[150:151],v232 offset:12800
v_mul_f32 v13, v13, v149
EX v80,v80
v_mul_f32 v14, v14, v149
MM v[88:91],a[200:203],v[204:207],v[88:91]
TR a[152:153],v233 offset:512
TR a[154:155],v233 offset:4608
v_mul_f32 v15, v15, v149
CV v196,v8,v9
EX v81,v81
MM v[88:91],a[196:199],v[188:191],v[88:91]
TR a[156:157],v233 offset:8704
CV v197,v10,v11
CV v198,v12,v13
MM v[88:91],a[192:195],v[172:175],v[88:91]
TR a[158:159],v233 offset:12800
CV v199,v14,v15
EX v82,v82
MM v[112:115],a[224:227],v[168:171],v[136:139]
buffer_store_dwordx4 v[196:199], v248, s[20:23], s79 offen offset:2048 sc1
MM v[112:115],a[228:231],v[184:187],v[112:115]
TR a[160:161],v232 offset:1024
TR a[162:163],v232 offset:5120
TR a[164:165],v232 offset:9216
AR v32,a96
EX v83,v83
AR v33,a97
AR v34,a98
MM v[112:115],a[232:235],v[200:203],v[112:115]
TR a[166:167],v232 offset:13312
AR v35,a99
EX v84,v84
MM v[112:115],a[236:239],v[216:219],v[112:115]
TR a[168:169],v233 offset:1024
AR v36,a100
AR v37,a101
MM v[116:119],a[252:255],v[216:219],v[136:139]
TR a[170:171],v233 offset:5120
AR v38,a102
EX v85,v85
AR v39,a103
MM v[116:119],a[248:251],v[200:203],v[116:119]
TR a[172:173],v233 offset:9216
TR a[174:175],v233 offset:13312
v_mul_f32 v32, v32, v145
v_mul_f32 v33, v33, v145
MM v[116:119],a[244:247],v[184:187],v[116:119]
TR a[176:177],v232 offset:1536
v_mul_f32 v34, v34, v145
v_mul_f32 v35, v35, v145
EX v86,v86
MM v[116:119],a[240:243],v[168:171],v[116:119]
TR a[178:179],v232 offset:5632
v_mul_f32 v36, v36, v145
v_mul_f32 v37, v37, v145
EX v87,v87
MM v[124:127],a[240:243],v[172:175],v[140:143]
TR a[180:181],v232 offset:9728
v_mul_f32 v38, v38, v145
v_mul_f32 v39, v39, v145
CV v80,v80,v81
MM v[124:127],a[244:247],v[188:191],v[124:127]
TR a[182:183],v232 offset:13824
CV v208,v32,v33
EX v88,v88
CV v209,v34,v35
MM v[124:127],a[248:251],v[204:207],v[124:127]
TR a[184:185],v233 offset:1536
TR a[186:187],v233 offset:5632
TR a[188:189],v233 offset:9728
TR a[190:191],v233 offset:13824
CV v210,v36,v37
CV v211,v38,v39
EX v89,v89
MM v[124:127],a[252:255],v[220:223],v[124:127]
buffer_store_dwordx4 v[208:211], v248, s[20:23], s78 offen offset:3072 sc1
MM v[120:123],a[236:239],v[220:223],v[140:143]
AR v40,a104
CV v81,v82,v83
EX v90,v90
AR v41,a105
MM v[120:123],a[232:235],v[204:207],v[120:123]
AR v42,a106
AR v43,a107
MM v[120:123],a[228:231],v[188:191],v[120:123]
EX v91,v91
AR v44,a108
CV v82,v84,v85
MM v[120:123],a[224:227],v[172:175],v[120:123]
s_waitcnt vmcnt(19) lgkmcnt(0)
s_barrier
AR v45,a109
AR v46,a110
EX v92,v92
MM a[16:19],a[128:131],v[16:19],a[16:19]
s_add_u32 m0, s89, 0
AR v47,a111
v_mul_f32 v40, v40, v149
MM a[16:19],a[132:135],v[20:23],a[16:19]
s_add_u32 s66, s87, 0
v_mul_f32 v41, v41, v149
v_mul_f32 v42, v42, v149
EX v93,v93
CV v83,v86,v87
MM a[24:27],a[132:135],v[28:31],a[24:27]
v_mul_f32 v43, v43, v149
v_mul_f32 v44, v44, v149
MM a[24:27],a[128:131],v[24:27],a[24:27]
v_mul_f32 v45, v45, v149
v_mul_f32 v46, v46, v149
EX v94,v94
MM a[28:31],a[136:139],v[24:27],a[28:31]
v_mul_f32 v47, v47, v149
CV v212,v40,v41
EX v95,v95
MM a[28:31],a[140:143],v[28:31],a[28:31]
CV v213,v42,v43
CV v88,v88,v89
CV v214,v44,v45
MM a[20:23],a[140:143],v[20:23],a[20:23]
CV v215,v46,v47
CV v89,v90,v91
EX v112,v112
MM a[20:23],a[136:139],v[16:19],a[20:23]
buffer_store_dwordx4 v[212:215], v248, s[20:23], s79 offen offset:3072 sc1
MM a[48:51],a[144:147],v[16:19],a[48:51]
RD a[192:195],v224 offset:32768
EX v113,v113
MM a[48:51],a[148:151],v[20:23],a[48:51]
buffer_load_dwordx4 v240, s[24:27], s66 offen offset:0 lds
MM a[56:59],a[148:151],v[28:31],a[56:59]
s_add_u32 m0, s89, 1024
RD a[196:199],v225 offset:32768
EX v114,v114
MM a[56:59],a[144:147],v[24:27],a[56:59]
s_add_u32 s66, s87, 10240
RD a[200:203],v226 offset:32768
EX v115,v115
CV v90,v92,v93
MM a[60:63],a[152:155],v[24:27],a[60:63]
buffer_load_dwordx4 v240, s[24:27], s66 offen offset:0 lds
MM a[60:63],a[156:159],v[28:31],a[60:63]
s_add_u32 m0, s89, 8192
)ASM"
R"ASM(
RD a[204:207],v227 offset:32768
EX v116,v116
MM a[52:55],a[156:159],v[20:23],a[52:55]
s_add_u32 s66, s87, 81920
EX v117,v117
CV v91,v94,v95
MM a[52:55],a[152:155],v[16:19],a[52:55]
RD a[208:211],v224 offset:36864
RD a[212:215],v225 offset:36864
MM a[80:83],a[160:163],v[16:19],a[80:83]
buffer_load_dwordx4 v240, s[24:27], s66 offen offset:0 lds
MM a[80:83],a[164:167],v[20:23],a[80:83]
s_add_u32 m0, s89, 9216
EX v118,v118
MM a[88:91],a[164:167],v[28:31],a[88:91]
s_add_u32 s66, s87, 92160
EX v119,v119
CV v84,v112,v113
MM a[88:91],a[160:163],v[24:27],a[88:91]
RD a[216:219],v226 offset:36864
CV v85,v114,v115
EX v120,v120
MM a[92:95],a[168:171],v[24:27],a[92:95]
buffer_load_dwordx4 v240, s[24:27], s66 offen offset:0 lds
MM a[92:95],a[172:175],v[28:31],a[92:95]
s_add_u32 m0, s89, 65536
RD a[220:223],v227 offset:36864
EX v121,v121
MM a[84:87],a[172:175],v[20:23],a[84:87]
s_add_u32 s66, s88, 0
RD a[224:227],v224 offset:40960
CV v86,v116,v117
MM a[84:87],a[168:171],v[16:19],a[84:87]
EX v122,v122
MM a[112:115],a[176:179],v[16:19],a[112:115]
buffer_load_dwordx4 v240, s[24:27], s66 offen offset:0 lds
MM a[112:115],a[180:183],v[20:23],a[112:115]
s_add_u32 m0, s89, 66560
RD a[228:231],v225 offset:40960
EX v123,v123
MM a[120:123],a[180:183],v[28:31],a[120:123]
s_add_u32 s66, s88, 10240
RD a[232:235],v226 offset:40960
EX v124,v124
MM a[120:123],a[176:179],v[24:27],a[120:123]
buffer_load_dwordx4 v240, s[24:27], s66 offen offset:0 lds
MM a[124:127],a[184:187],v[24:27],a[124:127]
s_add_u32 m0, s89, 73728
RD a[236:239],v227 offset:40960
EX v125,v125
CV v87,v118,v119
MM a[124:127],a[188:191],v[28:31],a[124:127]
s_add_u32 s66, s88, 81920
EX v126,v126
MM a[116:119],a[188:191],v[20:23],a[116:119]
RD a[240:243],v224 offset:45056
RD a[244:247],v225 offset:45056
RD a[248:251],v226 offset:45056
RD a[252:255],v227 offset:45056
EX v127,v127
MM a[116:119],a[184:187],v[16:19],a[116:119]
LPG20_%=:
s_cbranch_execz LPG24_%=
buffer_load_dwordx4 v240, s[24:27], s66 offen offset:0 lds
v_smfmac_f32_16x16x64_bf16 v[236:239], v[152:155], v[80:87], v234
s_add_u32 m0, s89, 74752
CV v92,v120,v121
CV v93,v122,v123
CV v94,v124,v125
CV v95,v126,v127
s_nop 1
v_smfmac_f32_16x16x64_bf16 v[236:239], v[156:159], v[88:95], v234
s_waitcnt lgkmcnt(0)
s_add_u32 s66, s88, 92160
buffer_load_dwordx4 v240, s[24:27], s66 offen offset:0 lds
v_lshrrev_b32 v230, 9, v248
v_lshlrev_b32 v230, 9, v230
v_sub_u32 v244, v232, v230
v_sub_u32 v235, v233, v230
v_sub_u32 v244, v244, v230
v_sub_u32 v235, v235, v230
v_sub_u32 v228, v244, v230
v_sub_u32 v229, v235, v230
v_add_u32 v228, 65536, v228
v_add_u32 v229, 65536, v229
v_subrev_u32 v230, 65536, v244
v_subrev_u32 v231, 65536, v235
MM v[16:19],a[192:195],v[168:171],v[136:139]
TR a[128:129],v232 offset:16384
TR a[130:131],v232 offset:20480
MM v[16:19],a[196:199],v[184:187],v[16:19]
TR a[132:133],v232 offset:24576
TR a[134:135],v232 offset:28672
MM v[16:19],a[200:203],v[200:203],v[16:19]
TR a[136:137],v233 offset:16384
TR a[138:139],v233 offset:20480
MM v[16:19],a[204:207],v[216:219],v[16:19]
TR a[140:141],v233 offset:24576
v_add_f32 v247, v236, v237
MM v[20:23],a[220:223],v[216:219],v[136:139]
TR a[142:143],v233 offset:28672
v_cmp_nge_f32 vcc, s71, v247
MM v[20:23],a[216:219],v[200:203],v[20:23]
s_cbranch_vccnz LSITE33_%=
LBACK33_%=:
TR a[144:145],v232 offset:16896
TR a[146:147],v232 offset:20992
MM v[20:23],a[212:215],v[184:187],v[20:23]
TR a[148:149],v232 offset:25088
TR a[150:151],v232 offset:29184
MM v[20:23],a[208:211],v[168:171],v[20:23]
TR a[152:153],v233 offset:16896
TR a[154:155],v233 offset:20992
MM v[28:31],a[208:211],v[172:175],v[140:143]
EX v16,v16
MM v[28:31],a[212:215],v[188:191],v[28:31]
EX v17,v17
MM v[28:31],a[216:219],v[204:207],v[28:31]
s_lshl_b32 s97, s84, 2
s_add_u32 s97, s97, s96
s_add_u32 s97, s97, 0
MM v[28:31],a[220:223],v[220:223],v[28:31]
s_waitcnt vmcnt(8)
v_mov_b32 v242, s69
s_mov_b64 exec, 1
buffer_store_dword v242, off, s[20:23], s97 offset:0 sc1
s_mov_b64 exec, -1
MM v[24:27],a[204:207],v[220:223],v[140:143]
TR a[156:157],v233 offset:25088
TR a[158:159],v233 offset:29184
MM v[24:27],a[200:203],v[204:207],v[24:27]
EX v18,v18
MM v[24:27],a[196:199],v[188:191],v[24:27]
TR a[160:161],v232 offset:17408
TR a[162:163],v232 offset:21504
MM v[24:27],a[192:195],v[172:175],v[24:27]
EX v19,v19
MM v[48:51],a[224:227],v[168:171],v[136:139]
TR a[164:165],v232 offset:25600
TR a[166:167],v232 offset:29696
MM v[48:51],a[228:231],v[184:187],v[48:51]
EX v20,v20
MM v[48:51],a[232:235],v[200:203],v[48:51]
TR a[168:169],v233 offset:17408
TR a[170:171],v233 offset:21504
MM v[48:51],a[236:239],v[216:219],v[48:51]
EX v21,v21
MM v[52:55],a[252:255],v[216:219],v[136:139]
TR a[172:173],v233 offset:25600
CV v16,v16,v17
MM v[52:55],a[248:251],v[200:203],v[52:55]
TR a[174:175],v233 offset:29696
TR a[176:177],v232 offset:17920
MM v[52:55],a[244:247],v[184:187],v[52:55]
EX v22,v22
MM v[52:55],a[240:243],v[168:171],v[52:55]
TR a[178:179],v232 offset:22016
TR a[180:181],v232 offset:26112
EX v23,v23
MM v[60:63],a[240:243],v[172:175],v[140:143]
TR a[182:183],v232 offset:30208
CV v17,v18,v19
MM v[60:63],a[244:247],v[188:191],v[60:63]
EX v24,v24
MM v[60:63],a[248:251],v[204:207],v[60:63]
TR a[184:185],v233 offset:17920
TR a[186:187],v233 offset:22016
TR a[188:189],v233 offset:26112
TR a[190:191],v233 offset:30208
EX v25,v25
MM v[60:63],a[252:255],v[220:223],v[60:63]
EX v26,v26
MM v[56:59],a[236:239],v[220:223],v[140:143]
EX v27,v27
MM v[56:59],a[232:235],v[204:207],v[56:59]
EX v28,v28
MM v[56:59],a[228:231],v[188:191],v[56:59]
EX v29,v29
CV v18,v20,v21
MM v[56:59],a[224:227],v[172:175],v[56:59]
s_waitcnt lgkmcnt(0)
s_barrier
CV v19,v22,v23
MM a[16:19],a[128:131],v[80:83],a[16:19]
s_add_u32 m0, s89, 16384
EX v30,v30
MM a[16:19],a[132:135],v[84:87],a[16:19]
s_add_u32 s66, s85, 128
EX v31,v31
MM a[24:27],a[132:135],v[92:95],a[24:27]
buffer_load_dwordx4 v240, s[24:27], s66 offen offset:0 lds
MM a[24:27],a[128:131],v[88:91],a[24:27]
s_add_u32 m0, s89, 17408
EX v48,v48
MM a[28:31],a[136:139],v[88:91],a[28:31]
s_add_u32 s66, s85, 10368
CV v24,v24,v25
CV v25,v26,v27
MM a[28:31],a[140:143],v[92:95],a[28:31]
CV v26,v28,v29
CV v27,v30,v31
MM a[20:23],a[140:143],v[84:87],a[20:23]
EX v49,v49
MM a[20:23],a[136:139],v[80:83],a[20:23]
EX v50,v50
MM a[48:51],a[144:147],v[80:83],a[48:51]
buffer_load_dwordx4 v240, s[24:27], s66 offen offset:0 lds
MM a[48:51],a[148:151],v[84:87],a[48:51]
s_add_u32 m0, s89, 24576
EX v51,v51
MM a[56:59],a[148:151],v[92:95],a[56:59]
s_add_u32 s66, s85, 82048
TR a[128:129],v232 offset:32768
CV v20,v48,v49
MM a[56:59],a[144:147],v[88:91],a[56:59]
EX v52,v52
MM a[60:63],a[152:155],v[88:91],a[60:63]
TR a[130:131],v232 offset:36864
TR a[132:133],v232 offset:40960
MM a[60:63],a[156:159],v[92:95],a[60:63]
EX v53,v53
MM a[52:55],a[156:159],v[84:87],a[52:55]
TR a[134:135],v232 offset:45056
TR a[136:137],v233 offset:32768
MM a[52:55],a[152:155],v[80:83],a[52:55]
buffer_load_dwordx4 v240, s[24:27], s66 offen offset:0 lds
MM a[80:83],a[160:163],v[80:83],a[80:83]
s_add_u32 m0, s89, 25600
TR a[138:139],v233 offset:36864
TR a[140:141],v233 offset:40960
EX v54,v54
MM a[80:83],a[164:167],v[84:87],a[80:83]
s_add_u32 s66, s85, 92288
TR a[142:143],v233 offset:45056
TR a[144:145],v232 offset:33280
MM a[88:91],a[164:167],v[92:95],a[88:91]
EX v55,v55
MM a[88:91],a[160:163],v[88:91],a[88:91]
TR a[146:147],v232 offset:37376
TR a[148:149],v232 offset:41472
EX v56,v56
MM a[92:95],a[168:171],v[88:91],a[92:95]
buffer_load_dwordx4 v240, s[24:27], s66 offen offset:0 lds
MM a[92:95],a[172:175],v[92:95],a[92:95]
s_add_u32 m0, s89, 81920
TR a[150:151],v232 offset:45568
TR a[152:153],v233 offset:33280
EX v57,v57
CV v21,v50,v51
MM a[84:87],a[172:175],v[84:87],a[84:87]
s_add_u32 s66, s86, 128
TR a[154:155],v233 offset:37376
TR a[156:157],v233 offset:41472
MM a[84:87],a[168:171],v[80:83],a[84:87]
TR a[158:159],v233 offset:45568
EX v58,v58
MM a[112:115],a[176:179],v[80:83],a[112:115]
buffer_load_dwordx4 v240, s[24:27], s66 offen offset:0 lds
MM a[112:115],a[180:183],v[84:87],a[112:115]
s_add_u32 m0, s89, 82944
TR a[160:161],v232 offset:33792
TR a[162:163],v232 offset:37888
TR a[164:165],v232 offset:41984
EX v59,v59
CV v22,v52,v53
MM a[120:123],a[180:183],v[92:95],a[120:123]
s_add_u32 s66, s86, 10368
TR a[166:167],v232 offset:46080
CV v23,v54,v55
EX v60,v60
MM a[120:123],a[176:179],v[88:91],a[120:123]
TR a[168:169],v233 offset:33792
TR a[170:171],v233 offset:37888
MM a[124:127],a[184:187],v[88:91],a[124:127]
TR a[172:173],v233 offset:41984
v_lshrrev_b32 v243, 2, v248
v_min_u32 v243, 12, v243
EX v61,v61
MM a[124:127],a[188:191],v[92:95],a[124:127]
buffer_load_dword v242, v243, s[20:23], s96 offen offset:0 sc1
MM a[116:119],a[188:191],v[84:87],a[116:119]
TR a[174:175],v233 offset:46080
TR a[176:177],v232 offset:34304
TR a[178:179],v232 offset:38400
EX v62,v62
EX v63,v63
MM a[116:119],a[184:187],v[80:83],a[116:119]
buffer_load_dwordx4 v240, s[24:27], s66 offen offset:0 lds
v_smfmac_f32_16x16x64_bf16 v[236:239], v[152:155], v[16:23], v234
s_add_u32 m0, s89, 90112
TR a[180:181],v232 offset:42496
TR a[182:183],v232 offset:46592
CV v28,v56,v57
CV v29,v58,v59
CV v30,v60,v61
CV v31,v62,v63
s_nop 1
v_smfmac_f32_16x16x64_bf16 v[236:239], v[156:159], v[24:31], v234
TR a[184:185],v233 offset:34304
TR a[186:187],v233 offset:38400
TR a[188:189],v233 offset:42496
TR a[190:191],v233 offset:46592
s_waitcnt lgkmcnt(0)
s_add_u32 s66, s86, 82048
buffer_load_dwordx4 v240, s[24:27], s66 offen offset:0 lds
s_add_u32 m0, s89, 91136
s_add_u32 s66, s86, 92288
buffer_load_dwordx4 v240, s[24:27], s66 offen offset:0 lds
s_nop 6
v_add_f32 v246, v236, v237
v_cmp_nge_f32 vcc, s71, v246
s_cbranch_vccnz LSITE34_%=
LBACK34_%=:
s_waitcnt lgkmcnt(0)
MM a[16:19],a[128:131],v[16:19],a[16:19]
s_waitcnt vmcnt(10)
s_barrier
MM a[16:19],a[132:135],v[20:23],a[16:19]
v_rcp_f32 v152, v246
MM a[24:27],a[132:135],v[28:31],a[24:27]
v_mov_b32 v153, v152
MM a[24:27],a[128:131],v[24:27],a[24:27]
s_nop 0
v_permlane32_swap_b32 v152, v153
MM a[28:31],a[136:139],v[24:27],a[28:31]
v_mov_b32 v157, v153
MM a[28:31],a[140:143],v[28:31],a[28:31]
s_nop 0
v_permlane16_swap_b32 v153, v157
MM a[20:23],a[140:143],v[20:23],a[20:23]
s_mul_i32 s78, s49, 17408
s_add_u32 s78, s83, s78
s_add_u32 s78, s78, 8192
MM a[20:23],a[136:139],v[16:19],a[20:23]
s_add_u32 s79, s78, 4096
MM a[48:51],a[144:147],v[16:19],a[48:51]
TR a[128:129],v230 offset:49152
TR a[130:131],v230 offset:51200
MM a[48:51],a[148:151],v[20:23],a[48:51]
MM a[56:59],a[148:151],v[28:31],a[56:59]
TR a[132:133],v230 offset:53248
TR a[134:135],v230 offset:55296
MM a[56:59],a[144:147],v[24:27],a[56:59]
AR v80,a16
AR v81,a17
MM a[60:63],a[152:155],v[24:27],a[60:63]
TR a[136:137],v230 offset:57344
TR a[138:139],v230 offset:59392
MM a[60:63],a[156:159],v[28:31],a[60:63]
AR v82,a18
AR v83,a19
AR v84,a20
MM a[52:55],a[156:159],v[20:23],a[52:55]
TR a[140:141],v230 offset:61440
TR a[142:143],v230 offset:63488
AR v85,a21
AR v86,a22
MM a[52:55],a[152:155],v[16:19],a[52:55]
AR v87,a23
v_mul_f32 v80, v80, v153
v_mul_f32 v81, v81, v153
MM a[80:83],a[160:163],v[16:19],a[80:83]
TR a[144:145],v231 offset:49152
TR a[146:147],v231 offset:51200
v_mul_f32 v82, v82, v153
v_mul_f32 v83, v83, v153
MM a[80:83],a[164:167],v[20:23],a[80:83]
v_mul_f32 v84, v84, v153
v_mul_f32 v85, v85, v153
v_mul_f32 v86, v86, v153
MM a[88:91],a[164:167],v[28:31],a[88:91]
TR a[148:149],v231 offset:53248
TR a[150:151],v231 offset:55296
v_mul_f32 v87, v87, v153
CV v168,v80,v81
MM a[88:91],a[160:163],v[24:27],a[88:91]
s_waitcnt vmcnt(3)
v_cmp_ne_u32 s[74:75], s69, v242
CV v169,v82,v83
CV v170,v84,v85
CV v171,v86,v87
MM a[92:95],a[168:171],v[24:27],a[92:95]
TR a[152:153],v231 offset:57344
TR a[154:155],v231 offset:59392
MM a[92:95],a[172:175],v[28:31],a[92:95]
s_cmp_lg_u64 s[74:75], 0
s_cbranch_scc1 LRPO0_%=
LRPO0b_%=:
s_xor_b32 s92, s49, 1
MM a[84:87],a[172:175],v[20:23],a[84:87]
s_mul_i32 s92, s92, 17408
TR a[156:157],v231 offset:61440
TR a[158:159],v231 offset:63488
MM a[84:87],a[168:171],v[16:19],a[84:87]
buffer_store_dwordx4 v[168:171], v248, s[20:23], s78 offen offset:0 sc1
AR v88,a24
)ASM"
R"ASM(
AR v89,a25
MM a[112:115],a[176:179],v[16:19],a[112:115]
s_add_u32 s92, s83, s92
TR a[160:161],v230 offset:49664
TR a[162:163],v230 offset:51712
AR v90,a26
AR v91,a27
AR v92,a28
AR v93,a29
AR v94,a30
AR v95,a31
v_mul_f32 v88, v88, v157
v_mul_f32 v89, v89, v157
v_mul_f32 v90, v90, v157
MM a[112:115],a[180:183],v[20:23],a[112:115]
buffer_load_dwordx4 a[0:3], v248, s[20:23], s92 offen offset:0 sc1
MM a[120:123],a[180:183],v[28:31],a[120:123]
TR a[164:165],v230 offset:53760
TR a[166:167],v230 offset:55808
v_mul_f32 v91, v91, v157
v_mul_f32 v92, v92, v157
v_mul_f32 v93, v93, v157
v_mul_f32 v94, v94, v157
v_mul_f32 v95, v95, v157
MM a[120:123],a[176:179],v[24:27],a[120:123]
buffer_load_dwordx4 a[32:35], v248, s[20:23], s92 offen offset:1024 sc1
MM a[124:127],a[184:187],v[24:27],a[124:127]
TR a[168:169],v230 offset:57856
TR a[170:171],v230 offset:59904
CV v172,v88,v89
CV v173,v90,v91
CV v174,v92,v93
CV v175,v94,v95
MM a[124:127],a[188:191],v[28:31],a[124:127]
buffer_load_dwordx4 a[64:67], v248, s[20:23], s92 offen offset:2048 sc1
MM a[116:119],a[188:191],v[20:23],a[116:119]
TR a[172:173],v230 offset:61952
TR a[174:175],v230 offset:64000
MM a[116:119],a[184:187],v[16:19],a[116:119]
s_waitcnt lgkmcnt(15)
MM v[0:3],a[128:131],v[160:163],0
buffer_store_dwordx4 v[172:175], v248, s[20:23], s79 offen offset:0 sc1
TR a[176:177],v231 offset:49664
TR a[178:179],v231 offset:51712
AR v112,a48
AR v113,a49
AR v114,a50
MM v[0:3],a[132:135],v[176:179],v[0:3]
v_bfe_u32 v249, v248, 6, 2
v_lshlrev_b32 v249, 8, v249
AR v115,a51
AR v116,a52
AR v117,a53
AR v118,a54
AR v119,a55
v_mul_f32 v112, v112, v153
v_mul_f32 v113, v113, v153
v_mul_f32 v114, v114, v153
v_mul_f32 v115, v115, v153
MM v[0:3],a[136:139],v[192:195],v[0:3]
TR a[180:181],v231 offset:53760
TR a[182:183],v231 offset:55808
v_mul_f32 v116, v116, v153
v_mul_f32 v117, v117, v153
MM v[0:3],a[140:143],v[208:211],v[0:3]
v_lshrrev_b32 v250, 8, v248
v_lshl_add_u32 v249, v250, 6, v249
v_mul_f32 v118, v118, v153
v_mul_f32 v119, v119, v153
CV v184,v112,v113
MM v[8:11],a[140:143],v[212:215],0
TR a[184:185],v231 offset:57856
TR a[186:187],v231 offset:59904
CV v185,v114,v115
CV v186,v116,v117
MM v[8:11],a[136:139],v[196:199],v[8:11]
buffer_load_dwordx4 a[96:99], v248, s[20:23], s92 offen offset:3072 sc1
MM v[8:11],a[132:135],v[180:183],v[8:11]
s_add_u32 s92, s92, 4096
TR a[188:189],v231 offset:61952
TR a[190:191],v231 offset:64000
CV v187,v118,v119
v_and_b32 v251, 48, v248
MM v[8:11],a[128:131],v[164:167],v[8:11]
s_waitcnt lgkmcnt(15)
buffer_store_dwordx4 v[184:187], v248, s[20:23], s78 offen offset:1024 sc1
v_add_u32 v249, v249, v251
s_lshr_b32 s45, s49, 3
MM v[4:7],a[144:147],v[160:163],0
TR a[128:129],v228 offset:0
TR a[130:131],v228 offset:1024
AR v120,a56
AR v121,a57
AR v122,a58
AR v123,a59
AR v124,a60
AR v125,a61
AR v126,a62
MM v[4:7],a[148:151],v[176:179],v[4:7]
v_bfe_u32 v245, v248, 6, 2
v_xor_b32 v251, s45, v245
AR v127,a63
v_mul_f32 v120, v120, v157
v_mul_f32 v121, v121, v157
MM v[4:7],a[152:155],v[192:195],v[4:7]
TR a[132:133],v228 offset:2048
TR a[134:135],v228 offset:3072
v_mul_f32 v122, v122, v157
v_mul_f32 v123, v123, v157
v_mul_f32 v124, v124, v157
MM v[4:7],a[156:159],v[208:211],v[4:7]
buffer_load_dwordx4 a[4:7], v248, s[20:23], s92 offen offset:0 sc1
MM v[12:15],a[156:159],v[212:215],0
TR a[136:137],v228 offset:4096
TR a[138:139],v228 offset:5120
v_mul_f32 v125, v125, v157
v_mul_f32 v126, v126, v157
v_mul_f32 v127, v127, v157
CV v188,v120,v121
CV v189,v122,v123
v_add_u32 v251, -1, v251
MM v[12:15],a[152:155],v[196:199],v[12:15]
v_and_b32 v251, 0x40000000, v251
v_lshlrev_b32 v245, 6, v245
CV v190,v124,v125
CV v191,v126,v127
MM v[12:15],a[148:151],v[180:183],v[12:15]
TR a[140:141],v228 offset:6144
TR a[142:143],v228 offset:7168
MM v[12:15],a[144:147],v[164:167],v[12:15]
s_waitcnt lgkmcnt(15)
buffer_load_dwordx4 a[36:39], v248, s[20:23], s92 offen offset:1024 sc1
MM v[32:35],a[160:163],v[160:163],0
TR a[144:145],v229 offset:0
TR a[146:147],v229 offset:1024
v_lshl_add_u32 v245, s45, 4, v245
MM v[32:35],a[164:167],v[176:179],v[32:35]
v_mov_b32 v234, v249
v_add_u32 v249, v249, v251
MM v[32:35],a[168:171],v[192:195],v[32:35]
buffer_store_dwordx4 v[188:191], v248, s[20:23], s79 offen offset:1024 sc1
TR a[148:149],v229 offset:2048
TR a[150:151],v229 offset:3072
AR v80,a80
AR v81,a81
AR v82,a82
AR v83,a83
AR v84,a84
MM v[32:35],a[172:175],v[208:211],v[32:35]
v_bfe_u32 v251, v248, 4, 2
v_lshl_add_u32 v245, v251, 2, v245
AR v85,a85
AR v86,a86
AR v87,a87
v_mul_f32 v80, v80, v153
v_mul_f32 v81, v81, v153
v_mul_f32 v82, v82, v153
v_mul_f32 v83, v83, v153
v_mul_f32 v84, v84, v153
MM v[40:43],a[172:175],v[212:215],0
s_lshl_b32 s47, %[sb], 10
TR a[152:153],v229 offset:4096
TR a[154:155],v229 offset:5120
v_mul_f32 v85, v85, v153
v_mul_f32 v86, v86, v153
v_mul_f32 v87, v87, v153
MM v[40:43],a[168:171],v[196:199],v[40:43]
s_and_b32 s47, s47, 1024
s_add_u32 s46, s95, s47
s_sub_u32 s47, s95, s47
CV v200,v80,v81
CV v201,v82,v83
CV v202,v84,v85
MM v[40:43],a[164:167],v[180:183],v[40:43]
TR a[156:157],v229 offset:6144
TR a[158:159],v229 offset:7168
CV v203,v86,v87
MM v[40:43],a[160:163],v[164:167],v[40:43]
s_waitcnt lgkmcnt(15)
s_bitcmp1_b32 s49, 4
s_cselect_b32 s44, -1, 1
s_waitcnt vmcnt(10)
s_barrier
MM v[36:39],a[176:179],v[160:163],0
s_mul_i32 s44, s44, 331776
TR a[160:161],v230 offset:16384
TR a[162:163],v230 offset:18432
MM v[36:39],a[180:183],v[176:179],v[36:39]
s_add_u32 s44, s44, s16
s_xor_b32 s45, s68, 64
MM v[36:39],a[184:187],v[192:195],v[36:39]
s_bfe_u32 s80, s49, 131074
TR a[164:165],v230 offset:20480
TR a[166:167],v230 offset:22528
v_xor_b32 v250, s80, v250
v_mul_u32_u24 v250, 82944, v250
v_and_b32 v252, 240, v248
v_add_u32 v250, v250, v252
v_lshrrev_b32 v251, 8, v248
v_mul_u32_u24 v251, 1280, v251
v_bfe_u32 v252, v248, 4, 3
v_lshl_add_u32 v251, v252, 3, v251
v_bfe_u32 v252, v248, 7, 1
v_mul_u32_u24 v254, 40960, v252
v_add_u32 v254, v254, v251
v_lshl_add_u32 v251, v252, 6, v251
v_lshrrev_b32 v253, 8, v248
v_and_b32 v252, 240, v248
v_lshl_add_u32 v232, v253, 2, v252
v_xor_b32 v253, s80, v253
MM v[36:39],a[188:191],v[208:211],v[36:39]
buffer_load_dwordx4 a[68:71], v248, s[20:23], s92 offen offset:2048 sc1
MM v[44:47],a[188:191],v[212:215],0
TR a[168:169],v230 offset:24576
TR a[170:171],v230 offset:26624
v_lshlrev_b32 v253, 4, v253
v_mov_b32 v233, s69
MM v[44:47],a[184:187],v[196:199],v[44:47]
s_add_u32 s80, s18, 131072
MM v[44:47],a[180:183],v[180:183],v[44:47]
TR a[172:173],v230 offset:28672
TR a[174:175],v230 offset:30720
MM v[44:47],a[176:179],v[164:167],v[44:47]
s_waitcnt lgkmcnt(15)
buffer_load_dwordx4 a[100:103], v248, s[20:23], s92 offen offset:3072 sc1
MM v[128:131],a[128:131],v[160:163],0
TR a[176:177],v231 offset:16384
TR a[178:179],v231 offset:18432
MM v[128:131],a[132:135],v[176:179],v[128:131]
buffer_load_dwordx2 v[224:225], v251, s[20:23], s80 offen offset:0 sc1
MM v[128:131],a[136:139],v[192:195],v[128:131]
TR a[180:181],v231 offset:20480
TR a[182:183],v231 offset:22528
MM v[128:131],a[140:143],v[208:211],v[128:131]
buffer_store_dwordx4 v[200:203], v248, s[20:23], s78 offen offset:2048 sc1
AR v88,a88
AR v89,a89
AR v90,a90
AR v91,a91
AR v92,a92
AR v93,a93
AR v94,a94
AR v95,a95
v_mul_f32 v88, v88, v157
v_mul_f32 v89, v89, v157
v_mul_f32 v90, v90, v157
v_mul_f32 v91, v91, v157
v_mul_f32 v92, v92, v157
v_mul_f32 v93, v93, v157
v_mul_f32 v94, v94, v157
v_mul_f32 v95, v95, v157
CV v204,v88,v89
CV v205,v90,v91
CV v206,v92,v93
CV v207,v94,v95
MM v[136:139],a[140:143],v[212:215],0
s_add_u32 s80, s18, 131200
TR a[184:185],v231 offset:24576
TR a[186:187],v231 offset:26624
MM v[136:139],a[136:139],v[196:199],v[136:139]
buffer_load_dwordx2 v[226:227], v251, s[20:23], s80 offen offset:0 sc1
LPG21_%=:
s_cbranch_execz LTR3_%=
MM v[136:139],a[132:135],v[180:183],v[136:139]
TR a[188:189],v231 offset:28672
TR a[190:191],v231 offset:30720
MM v[136:139],a[128:131],v[164:167],v[136:139]
s_waitcnt lgkmcnt(15)
s_add_u32 s80, s18, 172032
MM v[132:135],a[144:147],v[160:163],0
buffer_load_dwordx2 v[236:237], v251, s[20:23], s80 offen offset:0 sc1
TR a[128:129],v230 offset:16896
TR a[130:131],v230 offset:18944
MM v[132:135],a[148:151],v[176:179],v[132:135]
MM v[132:135],a[152:155],v[192:195],v[132:135]
s_add_u32 s80, s18, 172160
TR a[132:133],v230 offset:20992
TR a[134:135],v230 offset:23040
MM v[132:135],a[156:159],v[208:211],v[132:135]
buffer_load_dwordx2 v[238:239], v251, s[20:23], s80 offen offset:0 sc1
MM v[140:143],a[156:159],v[212:215],0
buffer_store_dwordx4 v[204:207], v248, s[20:23], s79 offen offset:2048 sc1
TR a[136:137],v230 offset:25088
TR a[138:139],v230 offset:27136
AR v112,a112
AR v113,a113
AR v114,a114
AR v115,a115
AR v116,a116
AR v117,a117
AR v118,a118
AR v119,a119
v_mul_f32 v112, v112, v153
v_mul_f32 v113, v113, v153
v_mul_f32 v114, v114, v153
v_mul_f32 v115, v115, v153
v_mul_f32 v116, v116, v153
v_mul_f32 v117, v117, v153
v_mul_f32 v118, v118, v153
v_mul_f32 v119, v119, v153
CV v216,v112,v113
CV v217,v114,v115
CV v218,v116,v117
CV v219,v118,v119
MM v[140:143],a[152:155],v[196:199],v[140:143]
s_add_u32 s80, s63, 131072
MM v[140:143],a[148:151],v[180:183],v[140:143]
buffer_load_dwordx2 v[246:247], v254, s[20:23], s80 offen offset:0 sc1
TR a[140:141],v230 offset:29184
TR a[142:143],v230 offset:31232
MM v[140:143],a[144:147],v[164:167],v[140:143]
s_waitcnt lgkmcnt(15)
MM v[64:67],a[160:163],v[160:163],0
TR a[144:145],v231 offset:16896
TR a[146:147],v231 offset:18944
MM v[64:67],a[164:167],v[176:179],v[64:67]
MM v[64:67],a[168:171],v[192:195],v[64:67]
TR a[148:149],v231 offset:20992
TR a[150:151],v231 offset:23040
MM v[64:67],a[172:175],v[208:211],v[64:67]
MM v[72:75],a[172:175],v[212:215],0
TR a[152:153],v231 offset:25088
TR a[154:155],v231 offset:27136
MM v[72:75],a[168:171],v[196:199],v[72:75]
buffer_store_dwordx4 v[216:219], v248, s[20:23], s78 offen offset:3072 sc1
AR v120,a120
AR v121,a121
AR v122,a122
AR v123,a123
AR v124,a124
AR v125,a125
AR v126,a126
AR v127,a127
v_mul_f32 v120, v120, v157
v_mul_f32 v121, v121, v157
v_mul_f32 v122, v122, v157
v_mul_f32 v123, v123, v157
v_mul_f32 v124, v124, v157
v_mul_f32 v125, v125, v157
v_mul_f32 v126, v126, v157
v_mul_f32 v127, v127, v157
CV v220,v120,v121
CV v221,v122,v123
CV v222,v124,v125
CV v223,v126,v127
MM v[72:75],a[164:167],v[180:183],v[72:75]
TR a[156:157],v231 offset:29184
TR a[158:159],v231 offset:31232
MM v[72:75],a[160:163],v[164:167],v[72:75]
s_waitcnt lgkmcnt(15)
MM v[68:71],a[176:179],v[160:163],0
TR a[160:161],v244 offset:49152
TR a[162:163],v244 offset:51200
MM v[68:71],a[180:183],v[176:179],v[68:71]
MM v[68:71],a[184:187],v[192:195],v[68:71]
TR a[164:165],v244 offset:53248
TR a[166:167],v244 offset:55296
MM v[68:71],a[188:191],v[208:211],v[68:71]
MM v[76:79],a[188:191],v[212:215],0
TR a[168:169],v244 offset:57344
TR a[170:171],v244 offset:59392
MM v[76:79],a[184:187],v[196:199],v[76:79]
MM v[76:79],a[180:183],v[180:183],v[76:79]
buffer_store_dwordx4 v[220:223], v248, s[20:23], s79 offen offset:3072 sc1
TR a[172:173],v244 offset:61440
TR a[174:175],v244 offset:63488
MM v[76:79],a[176:179],v[164:167],v[76:79]
s_waitcnt lgkmcnt(15)
MM v[96:99],a[128:131],v[160:163],0
s_xor_b32 s92, s49, 2
TR a[176:177],v235 offset:49152
TR a[178:179],v235 offset:51200
MM v[96:99],a[132:135],v[176:179],v[96:99]
s_mul_i32 s92, s92, 17408
s_add_u32 s92, s83, s92
MM v[96:99],a[136:139],v[192:195],v[96:99]
TR a[180:181],v235 offset:53248
TR a[182:183],v235 offset:55296
MM v[96:99],a[140:143],v[208:211],v[96:99]
buffer_load_dwordx4 a[8:11], v248, s[20:23], s92 offen offset:0 sc1
MM v[104:107],a[140:143],v[212:215],0
TR a[184:185],v235 offset:57344
TR a[186:187],v235 offset:59392
MM v[104:107],a[136:139],v[196:199],v[104:107]
buffer_load_dwordx4 a[40:43], v248, s[20:23], s92 offen offset:1024 sc1
MM v[104:107],a[132:135],v[180:183],v[104:107]
TR a[188:189],v235 offset:61440
TR a[190:191],v235 offset:63488
MM v[104:107],a[128:131],v[164:167],v[104:107]
s_waitcnt lgkmcnt(15)
buffer_load_dwordx4 a[72:75], v248, s[20:23], s92 offen offset:2048 sc1
MM v[100:103],a[144:147],v[160:163],0
TR a[128:129],v244 offset:49664
)ASM"
R"ASM(
TR a[130:131],v244 offset:51712
MM v[100:103],a[148:151],v[176:179],v[100:103]
buffer_load_dwordx4 a[104:107], v248, s[20:23], s92 offen offset:3072 sc1
MM v[100:103],a[152:155],v[192:195],v[100:103]
s_add_u32 s92, s92, 4096
TR a[132:133],v244 offset:53760
TR a[134:135],v244 offset:55808
MM v[100:103],a[156:159],v[208:211],v[100:103]
MM v[108:111],a[156:159],v[212:215],0
TR a[136:137],v244 offset:57856
TR a[138:139],v244 offset:59904
MM v[108:111],a[152:155],v[196:199],v[108:111]
buffer_load_dwordx4 a[12:15], v248, s[20:23], s92 offen offset:0 sc1
MM v[108:111],a[148:151],v[180:183],v[108:111]
TR a[140:141],v244 offset:61952
TR a[142:143],v244 offset:64000
MM v[108:111],a[144:147],v[164:167],v[108:111]
s_waitcnt lgkmcnt(15)
buffer_load_dwordx4 a[44:47], v248, s[20:23], s92 offen offset:1024 sc1
s_waitcnt vmcnt(21)
MM v[0:3],a[160:163],a[0:3],v[0:3]
TR a[144:145],v235 offset:49664
TR a[146:147],v235 offset:51712
MM v[0:3],a[164:167],a[32:35],v[0:3]
MM v[0:3],a[168:171],a[64:67],v[0:3]
TR a[148:149],v235 offset:53760
TR a[150:151],v235 offset:55808
MM v[0:3],a[172:175],a[96:99],v[0:3]
s_waitcnt vmcnt(15)
buffer_load_dwordx4 a[76:79], v248, s[20:23], s92 offen offset:2048 sc1
MM v[8:11],a[172:175],a[100:103],v[8:11]
TR a[152:153],v235 offset:57856
TR a[154:155],v235 offset:59904
MM v[8:11],a[168:171],a[68:71],v[8:11]
buffer_load_dwordx4 a[108:111], v248, s[20:23], s92 offen offset:3072 sc1
MM v[8:11],a[164:167],a[36:39],v[8:11]
TR a[156:157],v235 offset:61952
TR a[158:159],v235 offset:64000
MM v[8:11],a[160:163],a[4:7],v[8:11]
s_waitcnt lgkmcnt(15)
MM v[4:7],a[176:179],a[0:3],v[4:7]
TR a[160:161],v228 offset:8192
TR a[162:163],v228 offset:9216
MM v[4:7],a[180:183],a[32:35],v[4:7]
MM v[4:7],a[184:187],a[64:67],v[4:7]
TR a[164:165],v228 offset:10240
TR a[166:167],v228 offset:11264
MM v[4:7],a[188:191],a[96:99],v[4:7]
MM v[12:15],a[188:191],a[100:103],v[12:15]
TR a[168:169],v228 offset:12288
TR a[170:171],v228 offset:13312
MM v[12:15],a[184:187],a[68:71],v[12:15]
MM v[12:15],a[180:183],a[36:39],v[12:15]
TR a[172:173],v228 offset:14336
TR a[174:175],v228 offset:15360
MM v[12:15],a[176:179],a[4:7],v[12:15]
s_waitcnt lgkmcnt(15)
MM v[32:35],a[128:131],a[0:3],v[32:35]
TR a[176:177],v229 offset:8192
TR a[178:179],v229 offset:9216
MM v[32:35],a[132:135],a[32:35],v[32:35]
MM v[32:35],a[136:139],a[64:67],v[32:35]
TR a[180:181],v229 offset:10240
TR a[182:183],v229 offset:11264
MM v[32:35],a[140:143],a[96:99],v[32:35]
MM v[40:43],a[140:143],a[100:103],v[40:43]
TR a[184:185],v229 offset:12288
TR a[186:187],v229 offset:13312
MM v[40:43],a[136:139],a[68:71],v[40:43]
MM v[40:43],a[132:135],a[36:39],v[40:43]
TR a[188:189],v229 offset:14336
TR a[190:191],v229 offset:15360
MM v[40:43],a[128:131],a[4:7],v[40:43]
s_waitcnt lgkmcnt(15)
MM v[36:39],a[144:147],a[0:3],v[36:39]
s_waitcnt vmcnt(10)
TR a[128:129],v230 offset:0
TR a[130:131],v230 offset:2048
MM v[36:39],a[148:151],a[32:35],v[36:39]
v_lshlrev_b32 v112, 16, v224
v_and_b32 v113, 0xffff0000, v224
MM v[36:39],a[152:155],a[64:67],v[36:39]
TR a[132:133],v230 offset:4096
TR a[134:135],v230 offset:6144
MM v[36:39],a[156:159],a[96:99],v[36:39]
v_lshlrev_b32 v114, 16, v225
v_and_b32 v115, 0xffff0000, v225
MM v[44:47],a[156:159],a[100:103],v[44:47]
TR a[136:137],v230 offset:8192
TR a[138:139],v230 offset:10240
MM v[44:47],a[152:155],a[68:71],v[44:47]
v_mul_f32 v116, 0xbfb8aa3b, v112
v_mul_f32 v117, 0xbfb8aa3b, v113
MM v[44:47],a[148:151],a[36:39],v[44:47]
TR a[140:141],v230 offset:12288
TR a[142:143],v230 offset:14336
MM v[44:47],a[144:147],a[4:7],v[44:47]
s_waitcnt lgkmcnt(15)
v_mul_f32 v118, 0xbfb8aa3b, v114
v_mul_f32 v119, 0xbfb8aa3b, v115
MM v[128:131],a[160:163],a[0:3],v[128:131]
TR a[144:145],v231 offset:0
TR a[146:147],v231 offset:2048
MM v[128:131],a[164:167],a[32:35],v[128:131]
EX v116,v116
MM v[128:131],a[168:171],a[64:67],v[128:131]
TR a[148:149],v231 offset:4096
TR a[150:151],v231 offset:6144
MM v[128:131],a[172:175],a[96:99],v[128:131]
EX v117,v117
MM v[136:139],a[172:175],a[100:103],v[136:139]
TR a[152:153],v231 offset:8192
TR a[154:155],v231 offset:10240
MM v[136:139],a[168:171],a[68:71],v[136:139]
EX v118,v118
MM v[136:139],a[164:167],a[36:39],v[136:139]
TR a[156:157],v231 offset:12288
TR a[158:159],v231 offset:14336
MM v[136:139],a[160:163],a[4:7],v[136:139]
s_waitcnt lgkmcnt(15)
EX v119,v119
MM v[132:135],a[176:179],a[0:3],v[132:135]
s_xor_b32 s92, s49, 3
TR a[160:161],v230 offset:512
TR a[162:163],v230 offset:2560
MM v[132:135],a[180:183],a[32:35],v[132:135]
s_mul_i32 s92, s92, 17408
s_add_u32 s92, s83, s92
v_add_f32 v116, 1.0, v116
MM v[132:135],a[184:187],a[64:67],v[132:135]
TR a[164:165],v230 offset:4608
TR a[166:167],v230 offset:6656
MM v[132:135],a[188:191],a[96:99],v[132:135]
buffer_load_dwordx4 a[192:195], v248, s[20:23], s92 offen offset:0 sc1
MM v[140:143],a[188:191],a[100:103],v[140:143]
TR a[168:169],v230 offset:8704
TR a[170:171],v230 offset:10752
MM v[140:143],a[184:187],a[68:71],v[140:143]
buffer_load_dwordx4 a[208:211], v248, s[20:23], s92 offen offset:1024 sc1
MM v[140:143],a[180:183],a[36:39],v[140:143]
TR a[172:173],v230 offset:12800
TR a[174:175],v230 offset:14848
MM v[140:143],a[176:179],a[4:7],v[140:143]
s_waitcnt lgkmcnt(15)
buffer_load_dwordx4 a[224:227], v248, s[20:23], s92 offen offset:2048 sc1
s_waitcnt vmcnt(7)
MM v[0:3],a[128:131],a[8:11],v[0:3]
TR a[176:177],v231 offset:512
TR a[178:179],v231 offset:2560
MM v[0:3],a[132:135],a[40:43],v[0:3]
buffer_load_dwordx4 a[240:243], v248, s[20:23], s92 offen offset:3072 sc1
MM v[0:3],a[136:139],a[72:75],v[0:3]
s_add_u32 s92, s92, 4096
TR a[180:181],v231 offset:4608
TR a[182:183],v231 offset:6656
MM v[0:3],a[140:143],a[104:107],v[0:3]
s_waitcnt vmcnt(4)
v_add_f32 v117, 1.0, v117
v_add_f32 v118, 1.0, v118
MM v[8:11],a[140:143],a[108:111],v[8:11]
TR a[184:185],v231 offset:8704
TR a[186:187],v231 offset:10752
MM v[8:11],a[136:139],a[76:79],v[8:11]
buffer_load_dwordx4 a[196:199], v248, s[20:23], s92 offen offset:0 sc1
MM v[8:11],a[132:135],a[44:47],v[8:11]
TR a[188:189],v231 offset:12800
TR a[190:191],v231 offset:14848
MM v[8:11],a[128:131],a[12:15],v[8:11]
s_waitcnt lgkmcnt(15)
buffer_load_dwordx4 a[212:215], v248, s[20:23], s92 offen offset:1024 sc1
MM v[4:7],a[144:147],a[8:11],v[4:7]
TR a[128:129],v228 offset:16384
TR a[130:131],v228 offset:17408
MM v[4:7],a[148:151],a[40:43],v[4:7]
v_add_f32 v119, 1.0, v119
MM v[4:7],a[152:155],a[72:75],v[4:7]
TR a[132:133],v228 offset:18432
TR a[134:135],v228 offset:19456
MM v[4:7],a[156:159],a[104:107],v[4:7]
buffer_load_dwordx4 a[228:231], v248, s[20:23], s92 offen offset:2048 sc1
MM v[12:15],a[156:159],a[108:111],v[12:15]
TR a[136:137],v228 offset:20480
TR a[138:139],v228 offset:21504
MM v[12:15],a[152:155],a[76:79],v[12:15]
buffer_load_dwordx4 a[244:247], v248, s[20:23], s92 offen offset:3072 sc1
MM v[12:15],a[148:151],a[44:47],v[12:15]
TR a[140:141],v228 offset:22528
TR a[142:143],v228 offset:23552
MM v[12:15],a[144:147],a[12:15],v[12:15]
s_waitcnt lgkmcnt(15)
v_rcp_f32 v116, v116
MM v[32:35],a[160:163],a[8:11],v[32:35]
s_add_u32 m0, s89, 32768
TR a[144:145],v229 offset:16384
TR a[146:147],v229 offset:17408
MM v[32:35],a[164:167],a[40:43],v[32:35]
s_add_u32 s66, s87, 128
v_rcp_f32 v117, v117
MM v[32:35],a[168:171],a[72:75],v[32:35]
TR a[148:149],v229 offset:18432
TR a[150:151],v229 offset:19456
MM v[32:35],a[172:175],a[104:107],v[32:35]
buffer_load_dwordx4 v240, s[24:27], s66 offen offset:0 lds
MM v[40:43],a[172:175],a[108:111],v[40:43]
s_add_u32 m0, s89, 33792
TR a[152:153],v229 offset:20480
TR a[154:155],v229 offset:21504
MM v[40:43],a[168:171],a[76:79],v[40:43]
s_add_u32 s66, s87, 10368
v_rcp_f32 v118, v118
MM v[40:43],a[164:167],a[44:47],v[40:43]
TR a[156:157],v229 offset:22528
TR a[158:159],v229 offset:23552
MM v[40:43],a[160:163],a[12:15],v[40:43]
s_waitcnt lgkmcnt(15)
buffer_load_dwordx4 v240, s[24:27], s66 offen offset:0 lds
MM v[36:39],a[176:179],a[8:11],v[36:39]
s_add_u32 m0, s89, 40960
TR a[160:161],v244 offset:0
TR a[162:163],v244 offset:2048
MM v[36:39],a[180:183],a[40:43],v[36:39]
s_add_u32 s66, s87, 82048
v_rcp_f32 v119, v119
MM v[36:39],a[184:187],a[72:75],v[36:39]
TR a[164:165],v244 offset:4096
TR a[166:167],v244 offset:6144
MM v[36:39],a[188:191],a[104:107],v[36:39]
buffer_load_dwordx4 v240, s[24:27], s66 offen offset:0 lds
MM v[44:47],a[188:191],a[108:111],v[44:47]
s_add_u32 m0, s89, 41984
TR a[168:169],v244 offset:8192
TR a[170:171],v244 offset:10240
MM v[44:47],a[184:187],a[76:79],v[44:47]
s_add_u32 s66, s87, 92288
v_mul_f32 v116, v112, v116
v_mul_f32 v117, v113, v117
MM v[44:47],a[180:183],a[44:47],v[44:47]
TR a[172:173],v244 offset:12288
TR a[174:175],v244 offset:14336
MM v[44:47],a[176:179],a[12:15],v[44:47]
s_waitcnt lgkmcnt(15)
buffer_load_dwordx4 v240, s[24:27], s66 offen offset:0 lds
MM v[128:131],a[128:131],a[8:11],v[128:131]
s_add_u32 m0, s89, 98304
TR a[176:177],v235 offset:0
TR a[178:179],v235 offset:2048
MM v[128:131],a[132:135],a[40:43],v[128:131]
s_add_u32 s66, s88, 128
v_mul_f32 v118, v114, v118
v_mul_f32 v119, v115, v119
MM v[128:131],a[136:139],a[72:75],v[128:131]
TR a[180:181],v235 offset:4096
TR a[182:183],v235 offset:6144
MM v[128:131],a[140:143],a[104:107],v[128:131]
buffer_load_dwordx4 v240, s[24:27], s66 offen offset:0 lds
MM v[136:139],a[140:143],a[108:111],v[136:139]
s_add_u32 m0, s89, 99328
TR a[184:185],v235 offset:8192
TR a[186:187],v235 offset:10240
MM v[136:139],a[136:139],a[76:79],v[136:139]
s_add_u32 s66, s88, 10368
MM v[136:139],a[132:135],a[44:47],v[136:139]
TR a[188:189],v235 offset:12288
TR a[190:191],v235 offset:14336
MM v[136:139],a[128:131],a[12:15],v[136:139]
s_waitcnt lgkmcnt(15)
buffer_load_dwordx4 v240, s[24:27], s66 offen offset:0 lds
MM v[132:135],a[144:147],a[8:11],v[132:135]
s_lshl_b32 s97, s84, 2
TR a[128:129],v244 offset:512
TR a[130:131],v244 offset:2560
MM v[132:135],a[148:151],a[40:43],v[132:135]
s_add_u32 s97, s97, s96
s_add_u32 s97, s97, 16
MM v[132:135],a[152:155],a[72:75],v[132:135]
s_add_u32 m0, s89, 106496
TR a[132:133],v244 offset:4608
TR a[134:135],v244 offset:6656
MM v[132:135],a[156:159],a[104:107],v[132:135]
s_add_u32 s66, s88, 82048
v_mov_b32 v242, s69
s_mov_b64 exec, 1
buffer_store_dword v242, off, s[20:23], s97 offset:0 sc1
s_mov_b64 exec, -1
MM v[140:143],a[156:159],a[108:111],v[140:143]
TR a[136:137],v244 offset:8704
TR a[138:139],v244 offset:10752
MM v[140:143],a[152:155],a[76:79],v[140:143]
buffer_load_dwordx4 v240, s[24:27], s66 offen offset:0 lds
MM v[140:143],a[148:151],a[44:47],v[140:143]
s_add_u32 m0, s89, 107520
TR a[140:141],v244 offset:12800
TR a[142:143],v244 offset:14848
MM v[140:143],a[144:147],a[12:15],v[140:143]
s_waitcnt lgkmcnt(15)
s_add_u32 s66, s88, 92288
s_waitcnt vmcnt(12)
MM v[0:3],a[160:163],a[192:195],v[0:3]
TR a[144:145],v235 offset:512
TR a[146:147],v235 offset:2560
MM v[0:3],a[164:167],a[208:211],v[0:3]
buffer_load_dwordx4 v240, s[24:27], s66 offen offset:0 lds
MM v[0:3],a[168:171],a[224:227],v[0:3]
TR a[148:149],v235 offset:4608
TR a[150:151],v235 offset:6656
MM v[0:3],a[172:175],a[240:243],v[0:3]
s_waitcnt vmcnt(9)
MM v[8:11],a[172:175],a[244:247],v[8:11]
TR a[152:153],v235 offset:8704
TR a[154:155],v235 offset:10752
MM v[8:11],a[168:171],a[228:231],v[8:11]
MM v[8:11],a[164:167],a[212:215],v[8:11]
TR a[156:157],v235 offset:12800
TR a[158:159],v235 offset:14848
MM v[8:11],a[160:163],a[196:199],v[8:11]
s_waitcnt lgkmcnt(15)
MM v[4:7],a[176:179],a[192:195],v[4:7]
TR a[160:161],v228 offset:24576
TR a[162:163],v228 offset:25600
MM v[4:7],a[180:183],a[208:211],v[4:7]
MM v[4:7],a[184:187],a[224:227],v[4:7]
TR a[164:165],v228 offset:26624
TR a[166:167],v228 offset:27648
MM v[4:7],a[188:191],a[240:243],v[4:7]
MM v[12:15],a[188:191],a[244:247],v[12:15]
TR a[168:169],v228 offset:28672
TR a[170:171],v228 offset:29696
MM v[12:15],a[184:187],a[228:231],v[12:15]
MM v[12:15],a[180:183],a[212:215],v[12:15]
TR a[172:173],v228 offset:30720
TR a[174:175],v228 offset:31744
MM v[12:15],a[176:179],a[196:199],v[12:15]
s_waitcnt lgkmcnt(15)
MM v[32:35],a[128:131],a[192:195],v[32:35]
TR a[176:177],v229 offset:24576
TR a[178:179],v229 offset:25600
)ASM"
R"ASM(
MM v[32:35],a[132:135],a[208:211],v[32:35]
MM v[32:35],a[136:139],a[224:227],v[32:35]
TR a[180:181],v229 offset:26624
TR a[182:183],v229 offset:27648
MM v[32:35],a[140:143],a[240:243],v[32:35]
s_add_u32 s67, s46, 0
CV v0,v0,v1
CV v1,v2,v3
MM v[40:43],a[140:143],a[244:247],v[40:43]
TR a[184:185],v229 offset:28672
TR a[186:187],v229 offset:29696
MM v[40:43],a[136:139],a[228:231],v[40:43]
CV v2,v4,v5
CV v3,v6,v7
MM v[40:43],a[132:135],a[212:215],v[40:43]
TR a[188:189],v229 offset:30720
TR a[190:191],v229 offset:31744
MM v[40:43],a[128:131],a[196:199],v[40:43]
s_waitcnt lgkmcnt(15)
buffer_store_dwordx4 v[0:3], v249, s[20:23], s67 offen offset:0 sc1
MM v[36:39],a[144:147],a[192:195],v[36:39]
s_add_u32 s67, s47, 0
TR a[128:129],v244 offset:16384
TR a[130:131],v244 offset:18432
MM v[36:39],a[148:151],a[208:211],v[36:39]
CV v8,v8,v9
CV v9,v10,v11
MM v[36:39],a[152:155],a[224:227],v[36:39]
TR a[132:133],v244 offset:20480
TR a[134:135],v244 offset:22528
MM v[36:39],a[156:159],a[240:243],v[36:39]
CV v10,v12,v13
CV v11,v14,v15
MM v[44:47],a[156:159],a[244:247],v[44:47]
TR a[136:137],v244 offset:24576
TR a[138:139],v244 offset:26624
MM v[44:47],a[152:155],a[228:231],v[44:47]
buffer_store_dwordx4 v[8:11], v234, s[20:23], s67 offen offset:1024 sc1
MM v[44:47],a[148:151],a[212:215],v[44:47]
TR a[140:141],v244 offset:28672
TR a[142:143],v244 offset:30720
MM v[44:47],a[144:147],a[196:199],v[44:47]
s_waitcnt lgkmcnt(15)
ds_bpermute_b32 v160, v245, v0
MM v[128:131],a[160:163],a[192:195],v[128:131]
TR a[144:145],v235 offset:16384
TR a[146:147],v235 offset:18432
MM v[128:131],a[164:167],a[208:211],v[128:131]
ds_bpermute_b32 v161, v245, v1
MM v[128:131],a[168:171],a[224:227],v[128:131]
TR a[148:149],v235 offset:20480
TR a[150:151],v235 offset:22528
MM v[128:131],a[172:175],a[240:243],v[128:131]
ds_bpermute_b32 v162, v245, v2
MM v[136:139],a[172:175],a[244:247],v[136:139]
TR a[152:153],v235 offset:24576
TR a[154:155],v235 offset:26624
MM v[136:139],a[168:171],a[228:231],v[136:139]
s_nop 0
ds_bpermute_b32 v163, v245, v3
MM v[136:139],a[164:167],a[212:215],v[136:139]
s_nop 0
TR a[156:157],v235 offset:28672
TR a[158:159],v235 offset:30720
MM v[136:139],a[160:163],a[196:199],v[136:139]
s_waitcnt lgkmcnt(15)
CV v0,v116,v117
CV v1,v118,v119
s_waitcnt vmcnt(2)
s_barrier
MM v[132:135],a[176:179],a[192:195],v[132:135]
s_add_u32 s67, s46, 0
TR a[160:161],v230 offset:32768
TR a[162:163],v230 offset:34816
MM v[132:135],a[180:183],a[208:211],v[132:135]
s_nop 0
CV v32,v32,v33
CV v33,v34,v35
MM v[132:135],a[184:187],a[224:227],v[132:135]
s_nop 0
TR a[164:165],v230 offset:36864
TR a[166:167],v230 offset:38912
v_lshrrev_b32 v243, 2, v248
MM v[132:135],a[188:191],a[240:243],v[132:135]
s_nop 0
CV v34,v36,v37
CV v35,v38,v39
MM v[140:143],a[188:191],a[244:247],v[140:143]
LPG22_%=:
s_cbranch_execz LTR3_%=
TR a[168:169],v230 offset:40960
TR a[170:171],v230 offset:43008
MM v[140:143],a[184:187],a[228:231],v[140:143]
buffer_store_dwordx4 v[32:35], v249, s[20:23], s67 offen offset:2048 sc1
MM v[140:143],a[180:183],a[212:215],v[140:143]
s_add_u32 s67, s47, 0
TR a[172:173],v230 offset:45056
TR a[174:175],v230 offset:47104
v_min_u32 v243, 12, v243
MM v[140:143],a[176:179],a[196:199],v[140:143]
s_waitcnt lgkmcnt(15)
CV v40,v40,v41
CV v41,v42,v43
MM v[64:67],a[128:131],a[0:3],v[64:67]
TR a[176:177],v231 offset:32768
TR a[178:179],v231 offset:34816
MM v[64:67],a[132:135],a[32:35],v[64:67]
buffer_load_dword v242, v243, s[20:23], s96 offen offset:16 sc1
MM v[64:67],a[136:139],a[64:67],v[64:67]
TR a[180:181],v231 offset:36864
TR a[182:183],v231 offset:38912
CV v42,v44,v45
MM v[64:67],a[140:143],a[96:99],v[64:67]
CV v43,v46,v47
v_lshlrev_b32 v112, 16, v226
MM v[72:75],a[140:143],a[100:103],v[72:75]
TR a[184:185],v231 offset:40960
TR a[186:187],v231 offset:43008
MM v[72:75],a[136:139],a[68:71],v[72:75]
buffer_store_dwordx4 v[40:43], v234, s[20:23], s67 offen offset:3072 sc1
MM v[72:75],a[132:135],a[36:39],v[72:75]
TR a[188:189],v231 offset:45056
TR a[190:191],v231 offset:47104
MM v[72:75],a[128:131],a[4:7],v[72:75]
s_waitcnt lgkmcnt(15)
ds_bpermute_b32 v176, v245, v32
MM v[68:71],a[144:147],a[0:3],v[68:71]
TR a[128:129],v244 offset:32768
TR a[130:131],v244 offset:34816
MM v[68:71],a[148:151],a[32:35],v[68:71]
ds_bpermute_b32 v177, v245, v33
MM v[68:71],a[152:155],a[64:67],v[68:71]
TR a[132:133],v244 offset:36864
TR a[134:135],v244 offset:38912
MM v[68:71],a[156:159],a[96:99],v[68:71]
ds_bpermute_b32 v178, v245, v34
MM v[76:79],a[156:159],a[100:103],v[76:79]
TR a[136:137],v244 offset:40960
TR a[138:139],v244 offset:43008
MM v[76:79],a[152:155],a[68:71],v[76:79]
ds_bpermute_b32 v179, v245, v35
MM v[76:79],a[148:151],a[36:39],v[76:79]
TR a[140:141],v244 offset:45056
TR a[142:143],v244 offset:47104
MM v[76:79],a[144:147],a[4:7],v[76:79]
s_waitcnt lgkmcnt(15)
v_and_b32 v113, 0xffff0000, v226
v_lshlrev_b32 v114, 16, v227
MM v[64:67],a[160:163],a[8:11],v[64:67]
s_add_u32 s67, s46, 8192
TR a[144:145],v235 offset:32768
TR a[146:147],v235 offset:34816
MM v[64:67],a[164:167],a[40:43],v[64:67]
CV v128,v128,v129
CV v129,v130,v131
MM v[64:67],a[168:171],a[72:75],v[64:67]
TR a[148:149],v235 offset:36864
TR a[150:151],v235 offset:38912
MM v[64:67],a[172:175],a[104:107],v[64:67]
CV v130,v132,v133
CV v131,v134,v135
MM v[72:75],a[172:175],a[108:111],v[72:75]
TR a[152:153],v235 offset:40960
TR a[154:155],v235 offset:43008
MM v[72:75],a[168:171],a[76:79],v[72:75]
buffer_store_dwordx4 v[128:131], v249, s[20:23], s67 offen offset:0 sc1
MM v[72:75],a[164:167],a[44:47],v[72:75]
s_add_u32 s67, s47, 8192
TR a[156:157],v235 offset:45056
TR a[158:159],v235 offset:47104
MM v[72:75],a[160:163],a[12:15],v[72:75]
s_waitcnt lgkmcnt(15)
CV v136,v136,v137
CV v137,v138,v139
MM v[68:71],a[176:179],a[8:11],v[68:71]
TR a[160:161],v244 offset:16896
TR a[162:163],v244 offset:18944
MM v[68:71],a[180:183],a[40:43],v[68:71]
CV v138,v140,v141
CV v139,v142,v143
MM v[68:71],a[184:187],a[72:75],v[68:71]
TR a[164:165],v244 offset:20992
TR a[166:167],v244 offset:23040
MM v[68:71],a[188:191],a[104:107],v[68:71]
buffer_store_dwordx4 v[136:139], v234, s[20:23], s67 offen offset:1024 sc1
MM v[76:79],a[188:191],a[108:111],v[76:79]
TR a[168:169],v244 offset:25088
TR a[170:171],v244 offset:27136
MM v[76:79],a[184:187],a[76:79],v[76:79]
ds_bpermute_b32 v164, v245, v128
MM v[76:79],a[180:183],a[44:47],v[76:79]
TR a[172:173],v244 offset:29184
TR a[174:175],v244 offset:31232
MM v[76:79],a[176:179],a[12:15],v[76:79]
s_waitcnt lgkmcnt(15)
ds_bpermute_b32 v165, v245, v129
MM v[64:67],a[128:131],a[192:195],v[64:67]
TR a[176:177],v235 offset:16896
TR a[178:179],v235 offset:18944
MM v[64:67],a[132:135],a[208:211],v[64:67]
ds_bpermute_b32 v166, v245, v130
MM v[64:67],a[136:139],a[224:227],v[64:67]
TR a[180:181],v235 offset:20992
TR a[182:183],v235 offset:23040
MM v[64:67],a[140:143],a[240:243],v[64:67]
ds_bpermute_b32 v167, v245, v131
MM v[72:75],a[140:143],a[244:247],v[72:75]
TR a[184:185],v235 offset:25088
TR a[186:187],v235 offset:27136
MM v[72:75],a[136:139],a[228:231],v[72:75]
v_and_b32 v115, 0xffff0000, v227
v_mul_f32 v116, 0xbfb8aa3b, v112
MM v[72:75],a[132:135],a[212:215],v[72:75]
TR a[188:189],v235 offset:29184
TR a[190:191],v235 offset:31232
MM v[72:75],a[128:131],a[196:199],v[72:75]
s_waitcnt lgkmcnt(15)
v_mul_f32 v117, 0xbfb8aa3b, v113
v_mul_f32 v118, 0xbfb8aa3b, v114
MM v[68:71],a[144:147],a[192:195],v[68:71]
TR a[128:129],v230 offset:33280
TR a[130:131],v230 offset:35328
MM v[68:71],a[148:151],a[208:211],v[68:71]
v_mul_f32 v119, 0xbfb8aa3b, v115
MM v[68:71],a[152:155],a[224:227],v[68:71]
TR a[132:133],v230 offset:37376
TR a[134:135],v230 offset:39424
MM v[68:71],a[156:159],a[240:243],v[68:71]
EX v116,v116
MM v[76:79],a[156:159],a[244:247],v[76:79]
TR a[136:137],v230 offset:41472
TR a[138:139],v230 offset:43520
MM v[76:79],a[152:155],a[228:231],v[76:79]
EX v117,v117
MM v[76:79],a[148:151],a[212:215],v[76:79]
TR a[140:141],v230 offset:45568
TR a[142:143],v230 offset:47616
MM v[76:79],a[144:147],a[196:199],v[76:79]
s_waitcnt lgkmcnt(15)
EX v118,v118
MM v[96:99],a[160:163],a[0:3],v[96:99]
s_waitcnt vmcnt(3)
TR a[144:145],v231 offset:33280
TR a[146:147],v231 offset:35328
MM v[96:99],a[164:167],a[32:35],v[96:99]
EX v119,v119
MM v[96:99],a[168:171],a[64:67],v[96:99]
TR a[148:149],v231 offset:37376
TR a[150:151],v231 offset:39424
v_cmp_ne_u32 s[74:75], s69, v242
MM v[96:99],a[172:175],a[96:99],v[96:99]
s_add_u32 s67, s46, 4096
CV v64,v64,v65
CV v65,v66,v67
MM v[104:107],a[172:175],a[100:103],v[104:107]
TR a[152:153],v231 offset:41472
TR a[154:155],v231 offset:43520
MM v[104:107],a[168:171],a[68:71],v[104:107]
CV v66,v68,v69
CV v67,v70,v71
MM v[104:107],a[164:167],a[36:39],v[104:107]
s_cmp_lg_u64 s[74:75], 0
s_cbranch_scc1 LRPO1_%=
LRPO1b_%=:
TR a[156:157],v231 offset:45568
TR a[158:159],v231 offset:47616
MM v[104:107],a[160:163],a[4:7],v[104:107]
s_waitcnt lgkmcnt(15)
buffer_store_dwordx4 v[64:67], v249, s[20:23], s67 offen offset:0 sc1
MM v[100:103],a[176:179],a[0:3],v[100:103]
s_add_u32 s67, s47, 4096
TR a[160:161],v244 offset:33280
TR a[162:163],v244 offset:35328
MM v[100:103],a[180:183],a[32:35],v[100:103]
s_xor_b32 s92, s49, 1
CV v72,v72,v73
CV v73,v74,v75
MM v[100:103],a[184:187],a[64:67],v[100:103]
s_mul_i32 s92, s92, 17408
TR a[164:165],v244 offset:37376
TR a[166:167],v244 offset:39424
MM v[100:103],a[188:191],a[96:99],v[100:103]
s_add_u32 s92, s83, s92
CV v74,v76,v77
CV v75,v78,v79
MM v[108:111],a[188:191],a[100:103],v[108:111]
s_add_u32 s92, s92, 8192
TR a[168:169],v244 offset:41472
TR a[170:171],v244 offset:43520
MM v[108:111],a[184:187],a[68:71],v[108:111]
buffer_store_dwordx4 v[72:75], v234, s[20:23], s67 offen offset:1024 sc1
MM v[108:111],a[180:183],a[36:39],v[108:111]
s_cmp_eq_u32 s82, 0
s_cbranch_scc1 LY0_%=
v_lshrrev_b32 v135, 4, v248
v_min_u32 v135, 59, v135
v_mul_u32_u24 v132, 205, v135
v_lshrrev_b32 v132, 11, v132
v_mul_u32_u24 v132, 30, v132
v_add_lshl_u32 v135, v135, v132, 2
s_mul_i32 s78, s84, 40
s_add_u32 s78, s78, 73728
buffer_load_dword v132, v135, s[20:23], s78 offen offset:0 sc1
buffer_load_dword v133, v135, s[20:23], s78 offen offset:960 sc1
buffer_load_dword v134, v135, s[20:23], s78 offen offset:1920 sc1
LY0_%=:
TR a[172:173],v244 offset:45568
TR a[174:175],v244 offset:47616
MM v[108:111],a[176:179],a[4:7],v[108:111]
s_waitcnt lgkmcnt(15)
buffer_load_dwordx4 a[16:19], v248, s[20:23], s92 offen offset:0 sc1
MM v[96:99],a[128:131],a[8:11],v[96:99]
ds_bpermute_b32 v192, v245, v64
TR a[176:177],v235 offset:33280
TR a[178:179],v235 offset:35328
MM v[96:99],a[132:135],a[40:43],v[96:99]
buffer_load_dwordx4 a[48:51], v248, s[20:23], s92 offen offset:1024 sc1
MM v[96:99],a[136:139],a[72:75],v[96:99]
TR a[180:181],v235 offset:37376
TR a[182:183],v235 offset:39424
MM v[96:99],a[140:143],a[104:107],v[96:99]
ds_bpermute_b32 v193, v245, v65
MM v[104:107],a[140:143],a[108:111],v[104:107]
TR a[184:185],v235 offset:41472
TR a[186:187],v235 offset:43520
MM v[104:107],a[136:139],a[76:79],v[104:107]
buffer_load_dwordx4 a[80:83], v248, s[20:23], s92 offen offset:2048 sc1
MM v[104:107],a[132:135],a[44:47],v[104:107]
ds_bpermute_b32 v194, v245, v66
TR a[188:189],v235 offset:45568
TR a[190:191],v235 offset:47616
MM v[104:107],a[128:131],a[12:15],v[104:107]
s_waitcnt lgkmcnt(15)
buffer_load_dwordx4 a[112:115], v248, s[20:23], s92 offen offset:3072 sc1
MM v[100:103],a[144:147],a[8:11],v[100:103]
s_add_u32 s92, s92, 4096
TR a[128:129],v230 offset:49152
TR a[130:131],v230 offset:51200
MM v[100:103],a[148:151],a[40:43],v[100:103]
ds_bpermute_b32 v195, v245, v67
MM v[100:103],a[152:155],a[72:75],v[100:103]
TR a[132:133],v230 offset:53248
TR a[134:135],v230 offset:55296
MM v[100:103],a[156:159],a[104:107],v[100:103]
buffer_load_dwordx4 a[20:23], v248, s[20:23], s92 offen offset:0 sc1
MM v[108:111],a[156:159],a[108:111],v[108:111]
TR a[136:137],v230 offset:57344
TR a[138:139],v230 offset:59392
MM v[108:111],a[152:155],a[76:79],v[108:111]
buffer_load_dwordx4 a[52:55], v248, s[20:23], s92 offen offset:1024 sc1
MM v[108:111],a[148:151],a[44:47],v[108:111]
TR a[140:141],v230 offset:61440
TR a[142:143],v230 offset:63488
)ASM"
R"ASM(
MM v[108:111],a[144:147],a[12:15],v[108:111]
s_waitcnt lgkmcnt(15)
buffer_load_dwordx4 a[84:87], v248, s[20:23], s92 offen offset:2048 sc1
MM v[96:99],a[160:163],a[192:195],v[96:99]
TR a[144:145],v231 offset:49152
TR a[146:147],v231 offset:51200
MM v[96:99],a[164:167],a[208:211],v[96:99]
v_add_f32 v116, 1.0, v116
v_add_f32 v117, 1.0, v117
MM v[96:99],a[168:171],a[224:227],v[96:99]
TR a[148:149],v231 offset:53248
TR a[150:151],v231 offset:55296
MM v[96:99],a[172:175],a[240:243],v[96:99]
buffer_load_dwordx4 a[116:119], v248, s[20:23], s92 offen offset:3072 sc1
MM v[104:107],a[172:175],a[244:247],v[104:107]
s_waitcnt vmcnt(12)
TR a[152:153],v231 offset:57344
TR a[154:155],v231 offset:59392
MM v[104:107],a[168:171],a[228:231],v[104:107]
v_mov_b32 v252, s69
s_mov_b64 exec, 1
buffer_store_dword v252, off, s[20:23], s19 offset:0 sc1
s_mov_b64 exec, -1
MM v[104:107],a[164:167],a[212:215],v[104:107]
TR a[156:157],v231 offset:61440
TR a[158:159],v231 offset:63488
MM v[104:107],a[160:163],a[196:199],v[104:107]
s_waitcnt lgkmcnt(15)
v_add_f32 v118, 1.0, v118
v_add_f32 v119, 1.0, v119
MM v[100:103],a[176:179],a[192:195],v[100:103]
TR a[160:161],v230 offset:49664
TR a[162:163],v230 offset:51712
MM v[100:103],a[180:183],a[208:211],v[100:103]
v_rcp_f32 v116, v116
MM v[100:103],a[184:187],a[224:227],v[100:103]
TR a[164:165],v230 offset:53760
TR a[166:167],v230 offset:55808
MM v[100:103],a[188:191],a[240:243],v[100:103]
v_rcp_f32 v117, v117
MM v[108:111],a[188:191],a[244:247],v[108:111]
TR a[168:169],v230 offset:57856
TR a[170:171],v230 offset:59904
MM v[108:111],a[184:187],a[228:231],v[108:111]
v_rcp_f32 v118, v118
MM v[108:111],a[180:183],a[212:215],v[108:111]
TR a[172:173],v230 offset:61952
TR a[174:175],v230 offset:64000
MM v[108:111],a[176:179],a[196:199],v[108:111]
s_waitcnt lgkmcnt(15)
v_rcp_f32 v119, v119
MM v[16:19],a[128:131],v[168:171],0
TR a[176:177],v231 offset:49664
TR a[178:179],v231 offset:51712
MM v[16:19],a[132:135],v[184:187],v[16:19]
AW a3,-1
AW a7,-1
MM v[16:19],a[136:139],v[200:203],v[16:19]
TR a[180:181],v231 offset:53760
TR a[182:183],v231 offset:55808
MM v[16:19],a[140:143],v[216:219],v[16:19]
s_add_u32 s67, s46, 4096
CV v96,v96,v97
CV v97,v98,v99
AW a11,-1
MM v[24:27],a[140:143],v[220:223],0
TR a[184:185],v231 offset:57856
TR a[186:187],v231 offset:59904
MM v[24:27],a[136:139],v[204:207],v[24:27]
CV v98,v100,v101
CV v99,v102,v103
MM v[24:27],a[132:135],v[188:191],v[24:27]
TR a[188:189],v231 offset:61952
TR a[190:191],v231 offset:64000
AW a15,-1
MM v[24:27],a[128:131],v[172:175],v[24:27]
s_waitcnt lgkmcnt(15)
buffer_store_dwordx4 v[96:99], v249, s[20:23], s67 offen offset:2048 sc1
MM v[20:23],a[144:147],v[168:171],0
s_add_u32 s67, s47, 4096
TR a[128:129],v230 offset:16384
TR a[130:131],v230 offset:18432
MM v[20:23],a[148:151],v[184:187],v[20:23]
CV v104,v104,v105
CV v105,v106,v107
AW a35,-1
MM v[20:23],a[152:155],v[200:203],v[20:23]
TR a[132:133],v230 offset:20480
TR a[134:135],v230 offset:22528
MM v[20:23],a[156:159],v[216:219],v[20:23]
CV v106,v108,v109
CV v107,v110,v111
s_mov_b64 exec, 0xffff
AW a0,v160
AW a1,v161
AW a2,v162
AW a3,v163
s_mov_b64 exec, -1
MM v[28:31],a[156:159],v[220:223],0
TR a[136:137],v230 offset:24576
TR a[138:139],v230 offset:26624
AW a39,-1
MM v[28:31],a[152:155],v[204:207],v[28:31]
buffer_store_dwordx4 v[104:107], v234, s[20:23], s67 offen offset:3072 sc1
MM v[28:31],a[148:151],v[188:191],v[28:31]
TR a[140:141],v230 offset:28672
TR a[142:143],v230 offset:30720
s_mov_b64 exec, 0xffff
AW a8,v176
AW a9,v177
AW a10,v178
AW a11,v179
s_mov_b64 exec, -1
MM v[28:31],a[144:147],v[172:175],v[28:31]
s_waitcnt lgkmcnt(15)
ds_bpermute_b32 v208, v245, v96
AW a43,-1
MM v[48:51],a[160:163],v[168:171],0
TR a[144:145],v231 offset:16384
TR a[146:147],v231 offset:18432
MM v[48:51],a[164:167],v[184:187],v[48:51]
ds_bpermute_b32 v209, v245, v97
MM v[48:51],a[168:171],v[200:203],v[48:51]
TR a[148:149],v231 offset:20480
TR a[150:151],v231 offset:22528
AW a47,-1
MM v[48:51],a[172:175],v[216:219],v[48:51]
ds_bpermute_b32 v210, v245, v98
MM v[56:59],a[172:175],v[220:223],0
TR a[152:153],v231 offset:24576
TR a[154:155],v231 offset:26624
MM v[56:59],a[168:171],v[204:207],v[56:59]
ds_bpermute_b32 v211, v245, v99
AW a71,-1
MM v[56:59],a[164:167],v[188:191],v[56:59]
TR a[156:157],v231 offset:28672
TR a[158:159],v231 offset:30720
MM v[56:59],a[160:163],v[172:175],v[56:59]
s_waitcnt lgkmcnt(15)
AW a79,-1
AW a103,-1
MM v[52:55],a[176:179],v[168:171],0
TR a[160:161],v230 offset:16896
TR a[162:163],v230 offset:18944
MM v[52:55],a[180:183],v[184:187],v[52:55]
AW a111,-1
AW a195,-1
MM v[52:55],a[184:187],v[200:203],v[52:55]
TR a[164:165],v230 offset:20992
TR a[166:167],v230 offset:23040
MM v[52:55],a[188:191],v[216:219],v[52:55]
AW a199,-1
AW a215,-1
MM v[60:63],a[188:191],v[220:223],0
TR a[168:169],v230 offset:25088
TR a[170:171],v230 offset:27136
MM v[60:63],a[184:187],v[204:207],v[60:63]
s_mov_b64 exec, 0xffff
AW a32,v192
AW a33,v193
AW a34,v194
AW a35,v195
s_mov_b64 exec, -1
MM v[60:63],a[180:183],v[188:191],v[60:63]
s_waitcnt lgkmcnt(8)
TR a[172:173],v230 offset:29184
TR a[174:175],v230 offset:31232
MM v[60:63],a[176:179],v[172:175],v[60:63]
s_mov_b64 exec, 0xffff
AW a40,v208
AW a41,v209
AW a42,v210
AW a43,v211
s_mov_b64 exec, -1
MM v[80:83],a[128:131],v[168:171],0
TR a[176:177],v231 offset:16896
TR a[178:179],v231 offset:18944
MM v[80:83],a[132:135],v[184:187],v[80:83]
s_mov_b64 exec, 0xffff
AW a192,v164
AW a193,v165
AW a194,v166
AW a195,v167
s_mov_b64 exec, -1
MM v[80:83],a[136:139],v[200:203],v[80:83]
TR a[180:181],v231 offset:20992
TR a[182:183],v231 offset:23040
MM v[80:83],a[140:143],v[216:219],v[80:83]
v_lshrrev_b32 v104, 8, v248
v_min_u32 v104, 1, v104
MM v[88:91],a[140:143],v[220:223],0
TR a[184:185],v231 offset:25088
TR a[186:187],v231 offset:27136
MM v[88:91],a[136:139],v[204:207],v[88:91]
v_sub_u32 v104, 0, v104
v_mul_f32 v116, v112, v116
MM v[88:91],a[132:135],v[188:191],v[88:91]
TR a[188:189],v231 offset:29184
TR a[190:191],v231 offset:31232
MM v[88:91],a[128:131],v[172:175],v[88:91]
s_waitcnt lgkmcnt(15)
buffer_load_dword v240, v253, s[20:23], s68 offen offset:0 sc1
buffer_load_dword v140, v253, s[20:23], s45 offen offset:0 sc1
MM v[84:87],a[144:147],v[168:171],0
TR a[128:129],v228 offset:0
TR a[130:131],v228 offset:1024
MM v[84:87],a[148:151],v[184:187],v[84:87]
v_mul_f32 v117, v113, v117
v_mul_f32 v118, v114, v118
MM v[84:87],a[152:155],v[200:203],v[84:87]
TR a[132:133],v228 offset:2048
TR a[134:135],v228 offset:3072
MM v[84:87],a[156:159],v[216:219],v[84:87]
v_mul_f32 v119, v115, v119
CV v2,v116,v117
MM v[92:95],a[156:159],v[220:223],0
TR a[136:137],v228 offset:4096
TR a[138:139],v228 offset:5120
MM v[92:95],a[152:155],v[204:207],v[92:95]
CV v3,v118,v119
v_lshlrev_b32 v10, 16, v236
MM v[92:95],a[148:151],v[188:191],v[92:95]
TR a[140:141],v228 offset:6144
TR a[142:143],v228 offset:7168
MM v[92:95],a[144:147],v[172:175],v[92:95]
s_waitcnt lgkmcnt(15)
v_and_b32 v11, 0xffff0000, v236
v_lshlrev_b32 v12, 16, v237
MM v[112:115],a[160:163],v[168:171],0
s_xor_b32 s92, s49, 2
TR a[144:145],v229 offset:0
TR a[146:147],v229 offset:1024
MM v[112:115],a[164:167],v[184:187],v[112:115]
s_mul_i32 s92, s92, 17408
s_add_u32 s92, s83, s92
s_add_u32 s92, s92, 8192
MM v[112:115],a[168:171],v[200:203],v[112:115]
TR a[148:149],v229 offset:2048
TR a[150:151],v229 offset:3072
MM v[112:115],a[172:175],v[216:219],v[112:115]
buffer_load_dwordx4 a[24:27], v248, s[20:23], s92 offen offset:0 sc1
MM v[120:123],a[172:175],v[220:223],0
TR a[152:153],v229 offset:4096
TR a[154:155],v229 offset:5120
MM v[120:123],a[168:171],v[204:207],v[120:123]
buffer_load_dwordx4 a[56:59], v248, s[20:23], s92 offen offset:1024 sc1
MM v[120:123],a[164:167],v[188:191],v[120:123]
TR a[156:157],v229 offset:6144
TR a[158:159],v229 offset:7168
MM v[120:123],a[160:163],v[172:175],v[120:123]
s_waitcnt lgkmcnt(15)
buffer_load_dwordx4 a[88:91], v248, s[20:23], s92 offen offset:2048 sc1
MM v[116:119],a[176:179],v[168:171],0
s_nop 0
TR a[160:161],v244 offset:49152
TR a[162:163],v244 offset:51200
MM v[116:119],a[180:183],v[184:187],v[116:119]
buffer_load_dwordx4 a[120:123], v248, s[20:23], s92 offen offset:3072 sc1
MM v[116:119],a[184:187],v[200:203],v[116:119]
s_add_u32 s92, s92, 4096
TR a[164:165],v244 offset:53248
TR a[166:167],v244 offset:55296
MM v[116:119],a[188:191],v[216:219],v[116:119]
s_nop 0
v_and_b32 v13, 0xffff0000, v237
v_mul_f32 v14, 0xbfb8aa3b, v10
MM v[124:127],a[188:191],v[220:223],0
s_nop 0
TR a[168:169],v244 offset:57344
TR a[170:171],v244 offset:59392
MM v[124:127],a[184:187],v[204:207],v[124:127]
buffer_load_dwordx4 a[28:31], v248, s[20:23], s92 offen offset:0 sc1
MM v[124:127],a[180:183],v[188:191],v[124:127]
s_nop 0
TR a[172:173],v244 offset:61440
TR a[174:175],v244 offset:63488
MM v[124:127],a[176:179],v[172:175],v[124:127]
s_waitcnt lgkmcnt(15)
buffer_load_dwordx4 a[60:63], v248, s[20:23], s92 offen offset:1024 sc1
MM v[144:147],a[128:131],v[168:171],0
s_nop 0
TR a[176:177],v235 offset:49152
TR a[178:179],v235 offset:51200
MM v[144:147],a[132:135],v[184:187],v[144:147]
s_nop 0
v_mul_f32 v15, 0xbfb8aa3b, v11
v_mul_f32 v42, 0xbfb8aa3b, v12
MM v[144:147],a[136:139],v[200:203],v[144:147]
LPG23_%=:
s_cbranch_execz LTR3_%=
TR a[180:181],v235 offset:53248
TR a[182:183],v235 offset:55296
MM v[144:147],a[140:143],v[216:219],v[144:147]
buffer_load_dwordx4 a[92:95], v248, s[20:23], s92 offen offset:2048 sc1
MM v[152:155],a[140:143],v[220:223],0
TR a[184:185],v235 offset:57344
TR a[186:187],v235 offset:59392
MM v[152:155],a[136:139],v[204:207],v[152:155]
buffer_load_dwordx4 a[124:127], v248, s[20:23], s92 offen offset:3072 sc1
MM v[152:155],a[132:135],v[188:191],v[152:155]
s_xor_b32 s92, s49, 3
TR a[188:189],v235 offset:61440
TR a[190:191],v235 offset:63488
MM v[152:155],a[128:131],v[172:175],v[152:155]
s_waitcnt lgkmcnt(15)
s_mul_i32 s92, s92, 17408
s_add_u32 s92, s83, s92
MM v[148:151],a[144:147],v[168:171],0
s_add_u32 s92, s92, 8192
TR a[128:129],v244 offset:49664
TR a[130:131],v244 offset:51712
MM v[148:151],a[148:151],v[184:187],v[148:151]
buffer_load_dwordx4 a[200:203], v248, s[20:23], s92 offen offset:0 sc1
MM v[148:151],a[152:155],v[200:203],v[148:151]
TR a[132:133],v244 offset:53760
TR a[134:135],v244 offset:55808
MM v[148:151],a[156:159],v[216:219],v[148:151]
buffer_load_dwordx4 a[216:219], v248, s[20:23], s92 offen offset:1024 sc1
MM v[156:159],a[156:159],v[220:223],0
TR a[136:137],v244 offset:57856
TR a[138:139],v244 offset:59904
MM v[156:159],a[152:155],v[204:207],v[156:159]
v_mul_f32 v43, 0xbfb8aa3b, v13
MM v[156:159],a[148:151],v[188:191],v[156:159]
TR a[140:141],v244 offset:61952
TR a[142:143],v244 offset:64000
MM v[156:159],a[144:147],v[172:175],v[156:159]
s_waitcnt lgkmcnt(15)
buffer_load_dwordx4 a[232:235], v248, s[20:23], s92 offen offset:2048 sc1
s_waitcnt vmcnt(20)
MM v[16:19],a[160:163],a[16:19],v[16:19]
TR a[144:145],v235 offset:49664
TR a[146:147],v235 offset:51712
MM v[16:19],a[164:167],a[48:51],v[16:19]
buffer_load_dwordx4 a[248:251], v248, s[20:23], s92 offen offset:3072 sc1
MM v[16:19],a[168:171],a[80:83],v[16:19]
s_add_u32 s92, s92, 4096
TR a[148:149],v235 offset:53760
TR a[150:151],v235 offset:55808
MM v[16:19],a[172:175],a[112:115],v[16:19]
s_waitcnt vmcnt(17)
EX v14,v14
MM v[24:27],a[172:175],a[116:119],v[24:27]
TR a[152:153],v235 offset:57856
TR a[154:155],v235 offset:59904
MM v[24:27],a[168:171],a[84:87],v[24:27]
buffer_load_dwordx4 a[204:207], v248, s[20:23], s92 offen offset:0 sc1
MM v[24:27],a[164:167],a[52:55],v[24:27]
TR a[156:157],v235 offset:61952
TR a[158:159],v235 offset:64000
MM v[24:27],a[160:163],a[20:23],v[24:27]
s_waitcnt lgkmcnt(15)
buffer_load_dwordx4 a[220:223], v248, s[20:23], s92 offen offset:1024 sc1
MM v[20:23],a[176:179],a[16:19],v[20:23]
TR a[160:161],v244 offset:16384
TR a[162:163],v244 offset:18432
MM v[20:23],a[180:183],a[48:51],v[20:23]
s_waitcnt vmcnt(14)
v_cmp_eq_u32 s[36:37], s69, v240
v_cmp_eq_u32 s[38:39], s69, v140
MM v[20:23],a[184:187],a[80:83],v[20:23]
TR a[164:165],v244 offset:20480
TR a[166:167],v244 offset:22528
MM v[20:23],a[188:191],a[112:115],v[20:23]
)ASM"
R"ASM(
buffer_load_dwordx4 a[236:239], v248, s[20:23], s92 offen offset:2048 sc1
MM v[28:31],a[188:191],a[116:119],v[28:31]
s_nop 0
TR a[168:169],v244 offset:24576
TR a[170:171],v244 offset:26624
MM v[28:31],a[184:187],a[84:87],v[28:31]
buffer_load_dwordx4 a[252:255], v248, s[20:23], s92 offen offset:3072 sc1
MM v[28:31],a[180:183],a[52:55],v[28:31]
s_and_b32 s36, s36, 0xffff0000
TR a[172:173],v244 offset:28672
TR a[174:175],v244 offset:30720
MM v[28:31],a[176:179],a[20:23],v[28:31]
s_waitcnt lgkmcnt(15)
s_add_u32 s66, s16, 0
MM v[48:51],a[128:131],a[16:19],v[48:51]
TR a[176:177],v235 offset:16384
TR a[178:179],v235 offset:18432
MM v[48:51],a[132:135],a[48:51],v[48:51]
v_mov_b32 v252, s69
s_mov_b64 exec, 1
buffer_store_dword v252, off, s[20:23], s19 offset:4 sc1
s_mov_b64 exec, -1
MM v[48:51],a[136:139],a[80:83],v[48:51]
s_mov_b64 exec, s[36:37]
buffer_load_dwordx4 a[0:3], v250, s[20:23], s66 offen offset:0 sc1
s_mov_b64 exec, -1
TR a[180:181],v235 offset:20480
TR a[182:183],v235 offset:22528
MM v[48:51],a[140:143],a[112:115],v[48:51]
s_add_u32 s66, s44, 0
s_mov_b64 exec, s[38:39]
buffer_load_dwordx4 a[4:7], v250, s[20:23], s66 offen offset:0 sc1
s_mov_b64 exec, -1
MM v[56:59],a[140:143],a[116:119],v[56:59]
s_add_u32 s66, s16, 2048
TR a[184:185],v235 offset:24576
TR a[186:187],v235 offset:26624
MM v[56:59],a[136:139],a[84:87],v[56:59]
EX v15,v15
MM v[56:59],a[132:135],a[52:55],v[56:59]
TR a[188:189],v235 offset:28672
TR a[190:191],v235 offset:30720
MM v[56:59],a[128:131],a[20:23],v[56:59]
s_waitcnt lgkmcnt(15)
s_mov_b64 exec, s[36:37]
buffer_load_dwordx4 a[8:11], v250, s[20:23], s66 offen offset:0 sc1
s_mov_b64 exec, -1
MM v[52:55],a[144:147],a[16:19],v[52:55]
s_add_u32 s66, s44, 2048
TR a[128:129],v244 offset:16896
TR a[130:131],v244 offset:18944
MM v[52:55],a[148:151],a[48:51],v[52:55]
s_mov_b64 exec, s[38:39]
buffer_load_dwordx4 a[12:15], v250, s[20:23], s66 offen offset:0 sc1
s_mov_b64 exec, -1
MM v[52:55],a[152:155],a[80:83],v[52:55]
TR a[132:133],v244 offset:20992
TR a[134:135],v244 offset:23040
MM v[52:55],a[156:159],a[112:115],v[52:55]
EX v42,v42
MM v[60:63],a[156:159],a[116:119],v[60:63]
TR a[136:137],v244 offset:25088
TR a[138:139],v244 offset:27136
MM v[60:63],a[152:155],a[84:87],v[60:63]
EX v43,v43
MM v[60:63],a[148:151],a[52:55],v[60:63]
TR a[140:141],v244 offset:29184
TR a[142:143],v244 offset:31232
MM v[60:63],a[144:147],a[20:23],v[60:63]
s_waitcnt lgkmcnt(15)
v_add_f32 v14, 1.0, v14
v_add_f32 v15, 1.0, v15
MM v[80:83],a[160:163],a[16:19],v[80:83]
TR a[144:145],v235 offset:16896
TR a[146:147],v235 offset:18944
MM v[80:83],a[164:167],a[48:51],v[80:83]
v_add_f32 v42, 1.0, v42
v_add_f32 v43, 1.0, v43
MM v[80:83],a[168:171],a[80:83],v[80:83]
TR a[148:149],v235 offset:20992
TR a[150:151],v235 offset:23040
MM v[80:83],a[172:175],a[112:115],v[80:83]
v_rcp_f32 v14, v14
MM v[88:91],a[172:175],a[116:119],v[88:91]
TR a[152:153],v235 offset:25088
TR a[154:155],v235 offset:27136
MM v[88:91],a[168:171],a[84:87],v[88:91]
v_rcp_f32 v15, v15
MM v[88:91],a[164:167],a[52:55],v[88:91]
TR a[156:157],v235 offset:29184
TR a[158:159],v235 offset:31232
MM v[88:91],a[160:163],a[20:23],v[88:91]
s_waitcnt lgkmcnt(15)
v_rcp_f32 v42, v42
MM v[84:87],a[176:179],a[16:19],v[84:87]
TR a[160:161],v228 offset:8192
TR a[162:163],v228 offset:9216
MM v[84:87],a[180:183],a[48:51],v[84:87]
v_rcp_f32 v43, v43
MM v[84:87],a[184:187],a[80:83],v[84:87]
TR a[164:165],v228 offset:10240
TR a[166:167],v228 offset:11264
MM v[84:87],a[188:191],a[112:115],v[84:87]
v_mul_f32 v14, v10, v14
v_mul_f32 v15, v11, v15
MM v[92:95],a[188:191],a[116:119],v[92:95]
TR a[168:169],v228 offset:12288
TR a[170:171],v228 offset:13312
MM v[92:95],a[184:187],a[84:87],v[92:95]
v_mul_f32 v42, v12, v42
v_mul_f32 v43, v13, v43
MM v[92:95],a[180:183],a[52:55],v[92:95]
TR a[172:173],v228 offset:14336
TR a[174:175],v228 offset:15360
MM v[92:95],a[176:179],a[20:23],v[92:95]
s_waitcnt lgkmcnt(15)
CV v4,v14,v15
CV v5,v42,v43
MM v[112:115],a[128:131],a[16:19],v[112:115]
TR a[176:177],v229 offset:8192
TR a[178:179],v229 offset:9216
MM v[112:115],a[132:135],a[48:51],v[112:115]
v_lshlrev_b32 v10, 16, v238
v_and_b32 v11, 0xffff0000, v238
MM v[112:115],a[136:139],a[80:83],v[112:115]
TR a[180:181],v229 offset:10240
TR a[182:183],v229 offset:11264
MM v[112:115],a[140:143],a[112:115],v[112:115]
v_lshlrev_b32 v12, 16, v239
v_and_b32 v13, 0xffff0000, v239
MM v[120:123],a[140:143],a[116:119],v[120:123]
TR a[184:185],v229 offset:12288
TR a[186:187],v229 offset:13312
MM v[120:123],a[136:139],a[84:87],v[120:123]
v_mul_f32 v14, 0xbfb8aa3b, v10
v_mul_f32 v15, 0xbfb8aa3b, v11
MM v[120:123],a[132:135],a[52:55],v[120:123]
TR a[188:189],v229 offset:14336
TR a[190:191],v229 offset:15360
MM v[120:123],a[128:131],a[20:23],v[120:123]
s_waitcnt lgkmcnt(15)
v_mul_f32 v42, 0xbfb8aa3b, v12
v_mul_f32 v43, 0xbfb8aa3b, v13
MM v[116:119],a[144:147],a[16:19],v[116:119]
TR a[128:129],v230 offset:0
TR a[130:131],v230 offset:2048
MM v[116:119],a[148:151],a[48:51],v[116:119]
EX v14,v14
MM v[116:119],a[152:155],a[80:83],v[116:119]
TR a[132:133],v230 offset:4096
TR a[134:135],v230 offset:6144
MM v[116:119],a[156:159],a[112:115],v[116:119]
EX v15,v15
MM v[124:127],a[156:159],a[116:119],v[124:127]
TR a[136:137],v230 offset:8192
TR a[138:139],v230 offset:10240
MM v[124:127],a[152:155],a[84:87],v[124:127]
EX v42,v42
MM v[124:127],a[148:151],a[52:55],v[124:127]
TR a[140:141],v230 offset:12288
TR a[142:143],v230 offset:14336
MM v[124:127],a[144:147],a[20:23],v[124:127]
s_waitcnt lgkmcnt(15)
EX v43,v43
MM v[144:147],a[160:163],a[16:19],v[144:147]
TR a[144:145],v244 offset:0
TR a[146:147],v244 offset:2048
MM v[144:147],a[164:167],a[48:51],v[144:147]
v_add_f32 v14, 1.0, v14
v_add_f32 v15, 1.0, v15
MM v[144:147],a[168:171],a[80:83],v[144:147]
TR a[148:149],v244 offset:4096
TR a[150:151],v244 offset:6144
MM v[144:147],a[172:175],a[112:115],v[144:147]
buffer_load_dword v241, v253, s[20:23], s68 offen offset:4 sc1
buffer_load_dword v141, v253, s[20:23], s45 offen offset:4 sc1
MM v[152:155],a[172:175],a[116:119],v[152:155]
TR a[152:153],v244 offset:8192
TR a[154:155],v244 offset:10240
MM v[152:155],a[168:171],a[84:87],v[152:155]
v_add_f32 v42, 1.0, v42
v_add_f32 v43, 1.0, v43
MM v[152:155],a[164:167],a[52:55],v[152:155]
TR a[156:157],v244 offset:12288
TR a[158:159],v244 offset:14336
MM v[152:155],a[160:163],a[20:23],v[152:155]
s_waitcnt lgkmcnt(15)
v_rcp_f32 v14, v14
MM v[148:151],a[176:179],a[16:19],v[148:151]
TR a[160:161],v231 offset:0
TR a[162:163],v231 offset:2048
MM v[148:151],a[180:183],a[48:51],v[148:151]
v_rcp_f32 v15, v15
MM v[148:151],a[184:187],a[80:83],v[148:151]
TR a[164:165],v231 offset:4096
TR a[166:167],v231 offset:6144
MM v[148:151],a[188:191],a[112:115],v[148:151]
v_rcp_f32 v42, v42
MM v[156:159],a[188:191],a[116:119],v[156:159]
TR a[168:169],v231 offset:8192
TR a[170:171],v231 offset:10240
MM v[156:159],a[184:187],a[84:87],v[156:159]
v_rcp_f32 v43, v43
MM v[156:159],a[180:183],a[52:55],v[156:159]
TR a[172:173],v231 offset:12288
TR a[174:175],v231 offset:14336
MM v[156:159],a[176:179],a[20:23],v[156:159]
s_waitcnt lgkmcnt(15)
v_mul_f32 v14, v10, v14
v_mul_f32 v15, v11, v15
s_waitcnt vmcnt(19)
MM v[16:19],a[128:131],a[24:27],v[16:19]
TR a[176:177],v235 offset:0
TR a[178:179],v235 offset:2048
MM v[16:19],a[132:135],a[56:59],v[16:19]
v_mul_f32 v42, v12, v42
v_mul_f32 v43, v13, v43
MM v[16:19],a[136:139],a[88:91],v[16:19]
TR a[180:181],v235 offset:4096
TR a[182:183],v235 offset:6144
MM v[16:19],a[140:143],a[120:123],v[16:19]
s_waitcnt vmcnt(15)
CV v6,v14,v15
CV v7,v42,v43
MM v[24:27],a[140:143],a[124:127],v[24:27]
s_add_u32 s80, s18, 0
TR a[184:185],v235 offset:8192
TR a[186:187],v235 offset:10240
MM v[24:27],a[136:139],a[92:95],v[24:27]
buffer_load_dwordx2 v[32:33], v251, s[32:35], s80 offen offset:0
MM v[24:27],a[132:135],a[60:63],v[24:27]
s_add_u32 s80, s18, 128
TR a[188:189],v235 offset:12288
TR a[190:191],v235 offset:14336
MM v[24:27],a[128:131],a[28:31],v[24:27]
s_waitcnt lgkmcnt(15)
buffer_load_dwordx2 v[34:35], v251, s[32:35], s80 offen offset:0
s_waitcnt vmcnt(13)
MM v[16:19],a[144:147],a[200:203],v[16:19]
s_add_u32 s80, s18, 40960
TR a[128:129],v230 offset:512
TR a[130:131],v230 offset:2560
MM v[16:19],a[148:151],a[216:219],v[16:19]
buffer_load_dwordx2 v[36:37], v251, s[32:35], s80 offen offset:0
MM v[16:19],a[152:155],a[232:235],v[16:19]
s_add_u32 s80, s18, 41088
TR a[132:133],v230 offset:4608
TR a[134:135],v230 offset:6656
MM v[16:19],a[156:159],a[248:251],v[16:19]
s_waitcnt vmcnt(10)
buffer_load_dwordx2 v[38:39], v251, s[32:35], s80 offen offset:0
MM v[24:27],a[156:159],a[252:255],v[24:27]
s_add_u32 s80, s63, 0
TR a[136:137],v230 offset:8704
TR a[138:139],v230 offset:10752
MM v[24:27],a[152:155],a[236:239],v[24:27]
buffer_load_dwordx2 v[40:41], v254, s[32:35], s80 offen offset:0
MM v[24:27],a[148:151],a[220:223],v[24:27]
TR a[140:141],v230 offset:12800
TR a[142:143],v230 offset:14848
MM v[24:27],a[144:147],a[204:207],v[24:27]
s_waitcnt lgkmcnt(15)
v_mov_b32 v77, 0x3f80
v_and_b32 v76, 16, v248
MM v[20:23],a[160:163],a[24:27],v[20:23]
TR a[144:145],v244 offset:512
TR a[146:147],v244 offset:2560
MM v[20:23],a[164:167],a[56:59],v[20:23]
v_lshlrev_b32 v76, v76, v77
s_mov_b32 s28, 0x30003
MM v[20:23],a[168:171],a[88:91],v[20:23]
s_mov_b32 s29, s28
TR a[148:149],v244 offset:4608
TR a[150:151],v244 offset:6656
MM v[20:23],a[172:175],a[120:123],v[20:23]
v_cndmask_b32 v64, 0, v76, s[28:29]
s_lshl_b64 s[28:29], s[28:29], 2
MM v[28:31],a[172:175],a[124:127],v[28:31]
TR a[152:153],v244 offset:8704
TR a[154:155],v244 offset:10752
MM v[28:31],a[168:171],a[92:95],v[28:31]
v_cndmask_b32 v65, 0, v76, s[28:29]
s_lshl_b64 s[28:29], s[28:29], 2
MM v[28:31],a[164:167],a[60:63],v[28:31]
TR a[156:157],v244 offset:12800
TR a[158:159],v244 offset:14848
MM v[28:31],a[160:163],a[28:31],v[28:31]
s_waitcnt lgkmcnt(15)
v_cndmask_b32 v66, 0, v76, s[28:29]
s_lshl_b64 s[28:29], s[28:29], 2
MM v[28:31],a[188:191],a[252:255],v[28:31]
TR a[160:161],v231 offset:512
TR a[162:163],v231 offset:2560
MM v[28:31],a[184:187],a[236:239],v[28:31]
v_cndmask_b32 v67, 0, v76, s[28:29]
s_lshl_b64 s[28:29], s[28:29], 2
MM v[28:31],a[180:183],a[220:223],v[28:31]
TR a[164:165],v231 offset:4608
TR a[166:167],v231 offset:6656
MM v[28:31],a[176:179],a[204:207],v[28:31]
v_cndmask_b32 v68, 0, v76, s[28:29]
s_lshl_b64 s[28:29], s[28:29], 2
MM v[20:23],a[176:179],a[200:203],v[20:23]
TR a[168:169],v231 offset:8704
TR a[170:171],v231 offset:10752
MM v[20:23],a[180:183],a[216:219],v[20:23]
v_cndmask_b32 v69, 0, v76, s[28:29]
s_lshl_b64 s[28:29], s[28:29], 2
MM v[20:23],a[184:187],a[232:235],v[20:23]
TR a[172:173],v231 offset:12800
TR a[174:175],v231 offset:14848
MM v[20:23],a[188:191],a[248:251],v[20:23]
s_waitcnt lgkmcnt(15)
v_cndmask_b32 v70, 0, v76, s[28:29]
s_lshl_b64 s[28:29], s[28:29], 2
MM v[48:51],a[128:131],a[24:27],v[48:51]
TR a[176:177],v235 offset:512
TR a[178:179],v235 offset:2560
MM v[48:51],a[132:135],a[56:59],v[48:51]
v_cndmask_b32 v71, 0, v76, s[28:29]
MM v[48:51],a[136:139],a[88:91],v[48:51]
TR a[180:181],v235 offset:4608
TR a[182:183],v235 offset:6656
MM v[48:51],a[140:143],a[120:123],v[48:51]
s_add_u32 s67, s46, 8192
CV v16,v16,v17
CV v17,v18,v19
MM v[56:59],a[140:143],a[124:127],v[56:59]
TR a[184:185],v235 offset:8704
TR a[186:187],v235 offset:10752
MM v[56:59],a[136:139],a[92:95],v[56:59]
s_waitcnt vmcnt(5)
CV v18,v20,v21
CV v19,v22,v23
MM v[56:59],a[132:135],a[60:63],v[56:59]
TR a[188:189],v235 offset:12800
TR a[190:191],v235 offset:14848
MM v[56:59],a[128:131],a[28:31],v[56:59]
s_waitcnt lgkmcnt(15)
buffer_store_dwordx4 v[16:19], v249, s[20:23], s67 offen offset:2048 sc1
MM v[56:59],a[156:159],a[252:255],v[56:59]
s_add_u32 s67, s47, 8192
TR a[128:129],v230 offset:32768
TR a[130:131],v230 offset:34816
v_cmp_eq_u32 s[8:9], s69, v241
MM v[56:59],a[152:155],a[236:239],v[56:59]
CV v24,v24,v25
CV v25,v26,v27
MM v[56:59],a[148:151],a[220:223],v[56:59]
TR a[132:133],v230 offset:36864
TR a[134:135],v230 offset:38912
MM v[56:59],a[144:147],a[204:207],v[56:59]
CV v26,v28,v29
CV v27,v30,v31
v_cmp_eq_u32 s[10:11], s69, v141
)ASM"
R"ASM(
MM v[48:51],a[144:147],a[200:203],v[48:51]
TR a[136:137],v230 offset:40960
TR a[138:139],v230 offset:43008
MM v[48:51],a[148:151],a[216:219],v[48:51]
buffer_store_dwordx4 v[24:27], v234, s[20:23], s67 offen offset:3072 sc1
MM v[48:51],a[152:155],a[232:235],v[48:51]
s_nop 0
TR a[140:141],v230 offset:45056
TR a[142:143],v230 offset:47104
MM v[48:51],a[156:159],a[248:251],v[48:51]
s_waitcnt lgkmcnt(15)
s_and_b32 s8, s8, 0xffff0000
MM v[52:55],a[160:163],a[24:27],v[52:55]
s_add_u32 s66, s16, 4096
TR a[144:145],v244 offset:32768
TR a[146:147],v244 offset:34816
MM v[52:55],a[164:167],a[56:59],v[52:55]
s_mov_b64 exec, s[8:9]
buffer_load_dwordx4 a[32:35], v250, s[20:23], s66 offen offset:0 sc1
s_mov_b64 exec, -1
ds_bpermute_b32 v16, v245, v16
MM v[52:55],a[168:171],a[88:91],v[52:55]
s_add_u32 s66, s44, 4096
TR a[148:149],v244 offset:36864
TR a[150:151],v244 offset:38912
MM v[52:55],a[172:175],a[120:123],v[52:55]
s_mov_b64 exec, s[10:11]
buffer_load_dwordx4 a[36:39], v250, s[20:23], s66 offen offset:0 sc1
s_mov_b64 exec, -1
MM v[60:63],a[172:175],a[124:127],v[60:63]
s_add_u32 s66, s16, 6144
ds_bpermute_b32 v17, v245, v17
TR a[152:153],v244 offset:40960
TR a[154:155],v244 offset:43008
MM v[60:63],a[168:171],a[92:95],v[60:63]
ds_bpermute_b32 v18, v245, v18
MM v[60:63],a[164:167],a[60:63],v[60:63]
TR a[156:157],v244 offset:45056
TR a[158:159],v244 offset:47104
MM v[60:63],a[160:163],a[28:31],v[60:63]
s_waitcnt lgkmcnt(15)
s_mov_b64 exec, s[8:9]
buffer_load_dwordx4 a[40:43], v250, s[20:23], s66 offen offset:0 sc1
s_mov_b64 exec, -1
ds_bpermute_b32 v19, v245, v19
MM v[60:63],a[188:191],a[252:255],v[60:63]
s_add_u32 s66, s44, 6144
TR a[160:161],v231 offset:32768
TR a[162:163],v231 offset:34816
MM v[60:63],a[184:187],a[236:239],v[60:63]
MM v[60:63],a[180:183],a[220:223],v[60:63]
TR a[164:165],v231 offset:36864
TR a[166:167],v231 offset:38912
MM v[60:63],a[176:179],a[204:207],v[60:63]
MM v[52:55],a[176:179],a[200:203],v[52:55]
TR a[168:169],v231 offset:40960
TR a[170:171],v231 offset:43008
MM v[52:55],a[180:183],a[216:219],v[52:55]
s_waitcnt lgkmcnt(6)
s_mov_b64 exec, s[10:11]
buffer_load_dwordx4 a[44:47], v250, s[20:23], s66 offen offset:0 sc1
s_mov_b64 exec, -1
MM v[52:55],a[184:187],a[232:235],v[52:55]
s_add_u32 s66, s16, 8192
TR a[172:173],v231 offset:45056
TR a[174:175],v231 offset:47104
MM v[52:55],a[188:191],a[248:251],v[52:55]
v_or_b32 v19, v104, v19
MM v[80:83],a[128:131],a[24:27],v[80:83]
TR a[176:177],v235 offset:32768
TR a[178:179],v235 offset:34816
MM v[80:83],a[132:135],a[56:59],v[80:83]
v_mov_b32 v252, s69
s_mov_b64 exec, 1
buffer_store_dword v252, off, s[20:23], s19 offset:8 sc1
s_mov_b64 exec, -1
MM v[80:83],a[136:139],a[88:91],v[80:83]
TR a[180:181],v235 offset:36864
TR a[182:183],v235 offset:38912
MM v[80:83],a[140:143],a[120:123],v[80:83]
s_add_u32 s67, s46, 12288
CV v48,v48,v49
CV v49,v50,v51
MM v[88:91],a[140:143],a[124:127],v[88:91]
TR a[184:185],v235 offset:40960
TR a[186:187],v235 offset:43008
MM v[88:91],a[136:139],a[92:95],v[88:91]
s_mov_b64 exec, s[8:9]
buffer_load_dwordx4 a[192:195], v250, s[20:23], s66 offen offset:0 sc1
s_mov_b64 exec, -1
CV v50,v52,v53
CV v51,v54,v55
MM v[88:91],a[132:135],a[60:63],v[88:91]
s_add_u32 s66, s44, 8192
TR a[188:189],v235 offset:45056
TR a[190:191],v235 offset:47104
MM v[88:91],a[128:131],a[28:31],v[88:91]
buffer_store_dwordx4 v[48:51], v249, s[20:23], s67 offen offset:0 sc1
MM v[88:91],a[156:159],a[252:255],v[88:91]
s_add_u32 s67, s47, 12288
TR a[128:129],v230 offset:33280
TR a[130:131],v230 offset:35328
MM v[88:91],a[152:155],a[236:239],v[88:91]
CV v56,v56,v57
CV v57,v58,v59
MM v[88:91],a[148:151],a[220:223],v[88:91]
TR a[132:133],v230 offset:37376
TR a[134:135],v230 offset:39424
MM v[88:91],a[144:147],a[204:207],v[88:91]
CV v58,v60,v61
CV v59,v62,v63
MM v[80:83],a[144:147],a[200:203],v[80:83]
TR a[136:137],v230 offset:41472
TR a[138:139],v230 offset:43520
MM v[80:83],a[148:151],a[216:219],v[80:83]
MM v[80:83],a[152:155],a[232:235],v[80:83]
TR a[140:141],v230 offset:45568
TR a[142:143],v230 offset:47616
MM v[80:83],a[156:159],a[248:251],v[80:83]
s_waitcnt lgkmcnt(15)
MM v[84:87],a[160:163],a[24:27],v[84:87]
TR a[144:145],v244 offset:33280
TR a[146:147],v244 offset:35328
MM v[84:87],a[164:167],a[56:59],v[84:87]
buffer_store_dwordx4 v[56:59], v234, s[20:23], s67 offen offset:1024 sc1
MM v[84:87],a[168:171],a[88:91],v[84:87]
s_nop 0
TR a[148:149],v244 offset:37376
TR a[150:151],v244 offset:39424
MM v[84:87],a[172:175],a[120:123],v[84:87]
s_nop 0
ds_bpermute_b32 v48, v245, v48
MM v[92:95],a[172:175],a[124:127],v[92:95]
s_nop 0
TR a[152:153],v244 offset:41472
TR a[154:155],v244 offset:43520
MM v[92:95],a[168:171],a[92:95],v[92:95]
LPG24_%=:
s_cbranch_execz LTR3_%=
ds_bpermute_b32 v49, v245, v49
MM v[92:95],a[164:167],a[60:63],v[92:95]
TR a[156:157],v244 offset:45568
TR a[158:159],v244 offset:47616
MM v[92:95],a[160:163],a[28:31],v[92:95]
s_waitcnt lgkmcnt(15)
ds_bpermute_b32 v50, v245, v50
MM v[92:95],a[188:191],a[252:255],v[92:95]
TR a[160:161],v231 offset:33280
TR a[162:163],v231 offset:35328
MM v[92:95],a[184:187],a[236:239],v[92:95]
ds_bpermute_b32 v51, v245, v51
MM v[92:95],a[180:183],a[220:223],v[92:95]
TR a[164:165],v231 offset:37376
TR a[166:167],v231 offset:39424
MM v[92:95],a[176:179],a[204:207],v[92:95]
MM v[84:87],a[176:179],a[200:203],v[84:87]
TR a[168:169],v231 offset:41472
TR a[170:171],v231 offset:43520
MM v[84:87],a[180:183],a[216:219],v[84:87]
s_waitcnt vmcnt(10)
MM v[84:87],a[184:187],a[232:235],v[84:87]
TR a[172:173],v231 offset:45568
TR a[174:175],v231 offset:47616
MM v[72:75],v[64:67],a[0:3],0
MM v[72:75],v[64:67],a[4:7],v[72:75]
MM v[72:75],v[68:71],a[8:11],v[72:75]
MM v[72:75],v[68:71],a[12:15],v[72:75]
MM v[84:87],a[188:191],a[248:251],v[84:87]
s_waitcnt lgkmcnt(15)
s_waitcnt lgkmcnt(6)
v_or_b32 v51, v104, v51
MM v[112:115],a[128:131],a[24:27],v[112:115]
s_cmp_eq_u32 s82, 0
s_cbranch_scc1 LY1_%=
v_xor_b32 v132, s69, v132
v_xor_b32 v133, s69, v133
v_xor_b32 v134, s69, v134
v_or3_b32 v139, v132, v133, v134
v_cmp_ne_u32 vcc, 0, v139
s_cbranch_vccnz LRYF_%=
LRYFb_%=:
s_ff1_i32_b32 s76, s82
s_and_b32 s76, s76, 7
s_mov_b32 s79, s52
s_cmp_eq_u32 s76, 1
s_cselect_b32 s79, s53, s79
s_cmp_eq_u32 s76, 2
s_cselect_b32 s79, s54, s79
s_cmp_eq_u32 s76, 3
s_cselect_b32 s79, s55, s79
s_cmp_eq_u32 s76, 4
s_cselect_b32 s79, s56, s79
s_cmp_eq_u32 s76, 5
s_cselect_b32 s79, s57, s79
s_cmp_eq_u32 s76, 6
s_cselect_b32 s79, s58, s79
s_cmp_eq_u32 s76, 7
s_cselect_b32 s79, s59, s79
s_mul_i32 s76, s76, 2560
s_mul_i32 s90, s84, 640
s_add_u32 s76, s76, s90
s_add_u32 s76, s76, 21659648
s_lshl_b32 s90, s79, 2
s_add_u32 s90, s90, 53248
buffer_load_dword v138, off, s[20:23], s90 sc1
s_mul_i32 s90, s79, 1280
s_mul_i32 s77, s84, 320
s_add_u32 s90, s90, s77
s_add_u32 s90, s90, 131072
v_lshrrev_b32 v139, 1, v248
s_mov_b32 exec_lo, -1
s_mov_b32 exec_hi, 255
buffer_load_dwordx2 v[136:137], v139, s[20:23], s90 offen offset:0 sc1
s_mov_b32 exec_lo, -1
s_mov_b32 exec_hi, 255
buffer_load_dwordx4 v[160:163], v248, s[20:23], s76 offen offset:0 sc1
s_add_u32 s76, s76, 20480
buffer_load_dwordx4 v[164:167], v248, s[20:23], s76 offen offset:0 sc1
s_add_u32 s76, s76, 20480
buffer_load_dwordx4 v[168:171], v248, s[20:23], s76 offen offset:0 sc1
s_add_u32 s76, s76, 20480
buffer_load_dwordx4 v[172:175], v248, s[20:23], s76 offen offset:0 sc1
s_add_u32 s76, s76, 20480
buffer_load_dwordx4 v[176:179], v248, s[20:23], s76 offen offset:0 sc1
s_add_u32 s76, s76, 20480
buffer_load_dwordx4 v[180:183], v248, s[20:23], s76 offen offset:0 sc1
s_add_u32 s76, s76, 20480
s_mov_b64 exec, -1
LY1_%=:
TR a[176:177],v235 offset:33280
TR a[178:179],v235 offset:35328
MM v[112:115],a[132:135],a[56:59],v[112:115]
s_mov_b64 exec, s[10:11]
buffer_load_dwordx4 a[196:199], v250, s[20:23], s66 offen offset:0 sc1
s_mov_b64 exec, -1
MM v[112:115],a[136:139],a[88:91],v[112:115]
TR a[180:181],v235 offset:37376
TR a[182:183],v235 offset:39424
MM v[112:115],a[140:143],a[120:123],v[112:115]
s_add_u32 s67, s46, 12288
CV v80,v80,v81
CV v81,v82,v83
MM v[120:123],a[140:143],a[124:127],v[120:123]
TR a[184:185],v235 offset:41472
TR a[186:187],v235 offset:43520
MM v[120:123],a[136:139],a[92:95],v[120:123]
CV v82,v84,v85
CV v83,v86,v87
MM v[120:123],a[132:135],a[60:63],v[120:123]
TR a[188:189],v235 offset:45568
TR a[190:191],v235 offset:47616
MM v[120:123],a[128:131],a[28:31],v[120:123]
buffer_store_dwordx4 v[80:83], v249, s[20:23], s67 offen offset:2048 sc1
MM v[120:123],a[156:159],a[252:255],v[120:123]
s_add_u32 s67, s47, 12288
TR a[128:129],v228 offset:16384
TR a[130:131],v228 offset:17408
MM v[120:123],a[152:155],a[236:239],v[120:123]
CV v88,v88,v89
CV v89,v90,v91
MM v[120:123],a[148:151],a[220:223],v[120:123]
TR a[132:133],v228 offset:18432
TR a[134:135],v228 offset:19456
MM v[120:123],a[144:147],a[204:207],v[120:123]
CV v90,v92,v93
CV v91,v94,v95
MM v[112:115],a[144:147],a[200:203],v[112:115]
TR a[136:137],v228 offset:20480
TR a[138:139],v228 offset:21504
MM v[112:115],a[148:151],a[216:219],v[112:115]
v_cmp_u_f32 s[30:31], v72, v72
CV v76,v72,v73
MM v[112:115],a[152:155],a[232:235],v[112:115]
TR a[140:141],v228 offset:22528
TR a[142:143],v228 offset:23552
MM v[112:115],a[156:159],a[248:251],v[112:115]
s_waitcnt lgkmcnt(15)
buffer_store_dwordx4 v[88:91], v234, s[20:23], s67 offen offset:3072 sc1
MM v[116:119],a[160:163],a[24:27],v[116:119]
TR a[144:145],v228 offset:24576
TR a[146:147],v228 offset:25600
MM v[116:119],a[164:167],a[56:59],v[116:119]
buffer_load_dword v242, v253, s[20:23], s68 offen offset:8 sc1
buffer_load_dword v142, v253, s[20:23], s45 offen offset:8 sc1
MM v[116:119],a[168:171],a[88:91],v[116:119]
TR a[148:149],v228 offset:26624
TR a[150:151],v228 offset:27648
MM v[116:119],a[172:175],a[120:123],v[116:119]
ds_bpermute_b32 v80, v245, v80
MM v[124:127],a[172:175],a[124:127],v[124:127]
TR a[152:153],v228 offset:28672
TR a[154:155],v228 offset:29696
MM v[124:127],a[168:171],a[92:95],v[124:127]
ds_bpermute_b32 v81, v245, v81
MM v[124:127],a[164:167],a[60:63],v[124:127]
TR a[156:157],v228 offset:30720
TR a[158:159],v228 offset:31744
MM v[124:127],a[160:163],a[28:31],v[124:127]
s_waitcnt lgkmcnt(15)
ds_bpermute_b32 v82, v245, v82
MM v[124:127],a[188:191],a[252:255],v[124:127]
s_cmp_eq_u32 s82, 0
s_cbranch_scc1 LY2_%=
s_mov_b32 exec_lo, -1
s_mov_b32 exec_hi, 255
buffer_load_dwordx4 v[184:187], v248, s[20:23], s76 offen offset:0 sc1
s_add_u32 s76, s76, 20480
buffer_load_dwordx4 v[188:191], v248, s[20:23], s76 offen offset:0 sc1
s_add_u32 s76, s76, 20480
buffer_load_dwordx4 v[192:195], v248, s[20:23], s76 offen offset:0 sc1
s_add_u32 s76, s76, 20480
buffer_load_dwordx4 v[196:199], v248, s[20:23], s76 offen offset:0 sc1
s_add_u32 s76, s76, 20480
buffer_load_dwordx4 v[200:203], v248, s[20:23], s76 offen offset:0 sc1
s_add_u32 s76, s76, 20480
buffer_load_dwordx4 v[204:207], v248, s[20:23], s76 offen offset:0 sc1
s_add_u32 s76, s76, 20480
s_mov_b64 exec, -1
LY2_%=:
TR a[160:161],v229 offset:16384
TR a[162:163],v229 offset:17408
MM v[124:127],a[184:187],a[236:239],v[124:127]
ds_bpermute_b32 v83, v245, v83
MM v[124:127],a[180:183],a[220:223],v[124:127]
TR a[164:165],v229 offset:18432
TR a[166:167],v229 offset:19456
MM v[124:127],a[176:179],a[204:207],v[124:127]
CV v77,v74,v75
MM v[116:119],a[176:179],a[200:203],v[116:119]
TR a[168:169],v229 offset:20480
TR a[170:171],v229 offset:21504
MM v[116:119],a[180:183],a[216:219],v[116:119]
ds_bpermute_b32 v76, v232, v76
MM v[116:119],a[184:187],a[232:235],v[116:119]
TR a[172:173],v229 offset:22528
TR a[174:175],v229 offset:23552
MM v[116:119],a[188:191],a[248:251],v[116:119]
s_waitcnt lgkmcnt(15)
s_waitcnt lgkmcnt(7)
v_or_b32 v83, v104, v83
MM v[144:147],a[128:131],a[24:27],v[144:147]
TR a[176:177],v229 offset:24576
TR a[178:179],v229 offset:25600
MM v[144:147],a[132:135],a[56:59],v[144:147]
ds_bpermute_b32 v77, v232, v77
MM v[144:147],a[136:139],a[88:91],v[144:147]
TR a[180:181],v229 offset:26624
TR a[182:183],v229 offset:27648
MM v[144:147],a[140:143],a[120:123],v[144:147]
s_add_u32 s67, s46, 16384
CV v112,v112,v113
CV v113,v114,v115
MM v[152:155],a[140:143],a[124:127],v[152:155]
)ASM"
R"ASM(
TR a[184:185],v229 offset:28672
TR a[186:187],v229 offset:29696
MM v[152:155],a[136:139],a[92:95],v[152:155]
CV v114,v116,v117
CV v115,v118,v119
MM v[152:155],a[132:135],a[60:63],v[152:155]
TR a[188:189],v229 offset:30720
TR a[190:191],v229 offset:31744
MM v[152:155],a[128:131],a[28:31],v[152:155]
buffer_store_dwordx4 v[112:115], v249, s[20:23], s67 offen offset:0 sc1
MM v[152:155],a[156:159],a[252:255],v[152:155]
s_add_u32 s67, s47, 16384
CV v120,v120,v121
CV v121,v122,v123
MM v[152:155],a[152:155],a[236:239],v[152:155]
CV v122,v124,v125
CV v123,v126,v127
MM v[152:155],a[148:151],a[220:223],v[152:155]
buffer_store_dwordx4 v[120:123], v234, s[20:23], s67 offen offset:1024 sc1
MM v[152:155],a[144:147],a[204:207],v[152:155]
v_lshlrev_b32 v10, 16, v0
v_and_b32 v11, 0xffff0000, v0
MM v[144:147],a[144:147],a[200:203],v[144:147]
ds_bpermute_b32 v112, v245, v112
MM v[144:147],a[148:151],a[216:219],v[144:147]
ds_bpermute_b32 v113, v245, v113
MM v[144:147],a[152:155],a[232:235],v[144:147]
ds_bpermute_b32 v114, v245, v114
MM v[144:147],a[156:159],a[248:251],v[144:147]
s_waitcnt lgkmcnt(12)
ds_bpermute_b32 v115, v245, v115
MM v[148:151],a[160:163],a[24:27],v[148:151]
s_cmp_eq_u32 s82, 0
s_cbranch_scc1 LY3_%=
s_mov_b32 exec_lo, -1
s_mov_b32 exec_hi, 255
buffer_load_dwordx4 v[208:211], v248, s[20:23], s76 offen offset:0 sc1
s_add_u32 s76, s76, 20480
buffer_load_dwordx4 v[212:215], v248, s[20:23], s76 offen offset:0 sc1
s_add_u32 s76, s76, 20480
buffer_load_dwordx4 v[216:219], v248, s[20:23], s76 offen offset:0 sc1
s_add_u32 s76, s76, 20480
buffer_load_dwordx4 v[220:223], v248, s[20:23], s76 offen offset:0 sc1
s_add_u32 s76, s76, 20480
buffer_load_dwordx4 v[96:99], v248, s[20:23], s76 offen offset:0 sc1
s_add_u32 s76, s76, 20480
buffer_load_dwordx4 v[100:103], v248, s[20:23], s76 offen offset:0 sc1
s_mov_b64 exec, -1
LY3_%=:
v_lshlrev_b32 v12, 16, v1
v_and_b32 v13, 0xffff0000, v1
MM v[148:151],a[164:167],a[56:59],v[148:151]
v_lshlrev_b32 v14, 16, v32
v_and_b32 v15, 0xffff0000, v32
MM v[148:151],a[168:171],a[88:91],v[148:151]
v_lshlrev_b32 v42, 16, v33
v_and_b32 v43, 0xffff0000, v33
MM v[148:151],a[172:175],a[120:123],v[148:151]
s_add_u32 s80, s18, 0
s_waitcnt lgkmcnt(10)
v_lshlrev_b32 v72, 16, v76
MM v[156:159],a[172:175],a[124:127],v[156:159]
v_and_b32 v73, 0xffff0000, v76
v_lshlrev_b32 v74, 16, v77
MM v[156:159],a[168:171],a[92:95],v[156:159]
s_waitcnt lgkmcnt(0)
v_or_b32 v115, v104, v115
v_and_b32 v75, 0xffff0000, v77
MM v[156:159],a[164:167],a[60:63],v[156:159]
v_mul_f32 v72, v72, v10
v_mul_f32 v73, v73, v11
MM v[156:159],a[160:163],a[28:31],v[156:159]
v_mul_f32 v74, v74, v12
v_mul_f32 v75, v75, v13
MM v[156:159],a[188:191],a[252:255],v[156:159]
CV v76,v72,v73
CV v77,v74,v75
MM v[156:159],a[184:187],a[236:239],v[156:159]
v_lshlrev_b32 v72, 16, v76
v_and_b32 v73, 0xffff0000, v76
MM v[156:159],a[180:183],a[220:223],v[156:159]
v_lshlrev_b32 v74, 16, v77
v_and_b32 v75, 0xffff0000, v77
MM v[156:159],a[176:179],a[204:207],v[156:159]
v_add_f32 v72, v72, v14
v_add_f32 v73, v73, v15
MM v[148:151],a[176:179],a[200:203],v[148:151]
v_add_f32 v74, v74, v42
v_add_f32 v75, v75, v43
MM v[148:151],a[180:183],a[216:219],v[148:151]
CV v44,v72,v73
CV v45,v74,v75
MM v[148:151],a[184:187],a[232:235],v[148:151]
s_cmp_eq_u64 s[30:31], 0
s_cselect_b32 s94, s51, 0
s_and_b32 s94, s94, 1
MM v[148:151],a[188:191],a[248:251],v[148:151]
s_xor_b32 s17, s17, s94
s_cmp_lg_u32 s94, 0
s_cselect_b64 exec, -1, 0
buffer_store_dwordx2 v[44:45], v251, s[40:43], s80 offen offset:0 nt
s_mov_b64 exec, -1
s_nop 5
s_add_u32 s67, s46, 16384
CV v144,v144,v145
CV v145,v146,v147
CV v146,v148,v149
CV v147,v150,v151
buffer_store_dwordx4 v[144:147], v249, s[20:23], s67 offen offset:2048 sc1
s_add_u32 s67, s47, 16384
CV v152,v152,v153
CV v153,v154,v155
CV v154,v156,v157
CV v155,v158,v159
buffer_store_dwordx4 v[152:155], v234, s[20:23], s67 offen offset:3072 sc1
ds_bpermute_b32 v144, v245, v144
ds_bpermute_b32 v145, v245, v145
ds_bpermute_b32 v146, v245, v146
ds_bpermute_b32 v147, v245, v147
s_waitcnt vmcnt(5)
v_cmp_eq_u32 s[12:13], s69, v242
v_cmp_eq_u32 s[14:15], s69, v142
s_nop 0
s_and_b32 s12, s12, 0xffff0000
s_nop 0
s_add_u32 s66, s16, 10240
s_mov_b64 exec, s[12:13]
buffer_load_dwordx4 v[16:19], v250, s[20:23], s66 offen offset:0 sc1
s_mov_b64 exec, -1
s_add_u32 s66, s44, 10240
s_mov_b64 exec, s[14:15]
buffer_load_dwordx4 a[68:71], v250, s[20:23], s66 offen offset:0 sc1
s_mov_b64 exec, -1
s_add_u32 s66, s16, 12288
s_mov_b64 exec, s[12:13]
buffer_load_dwordx4 v[48:51], v250, s[20:23], s66 offen offset:0 sc1
s_mov_b64 exec, -1
s_add_u32 s66, s44, 12288
s_mov_b64 exec, s[14:15]
buffer_load_dwordx4 a[76:79], v250, s[20:23], s66 offen offset:0 sc1
s_mov_b64 exec, -1
s_add_u32 s66, s16, 14336
s_mov_b64 exec, s[12:13]
buffer_load_dwordx4 v[80:83], v250, s[20:23], s66 offen offset:0 sc1
s_mov_b64 exec, -1
s_add_u32 s66, s44, 14336
s_mov_b64 exec, s[14:15]
buffer_load_dwordx4 a[100:103], v250, s[20:23], s66 offen offset:0 sc1
s_mov_b64 exec, -1
s_add_u32 s66, s16, 16384
s_mov_b64 exec, s[12:13]
buffer_load_dwordx4 v[112:115], v250, s[20:23], s66 offen offset:0 sc1
s_mov_b64 exec, -1
s_add_u32 s66, s44, 16384
s_mov_b64 exec, s[14:15]
buffer_load_dwordx4 a[108:111], v250, s[20:23], s66 offen offset:0 sc1
s_mov_b64 exec, -1
s_waitcnt lgkmcnt(0)
v_or_b32 v147, v104, v147
s_add_u32 s66, s16, 18432
s_mov_b64 exec, s[12:13]
buffer_load_dwordx4 v[144:147], v250, s[20:23], s66 offen offset:0 sc1
s_mov_b64 exec, -1
s_add_u32 s66, s44, 18432
s_mov_b64 exec, s[14:15]
buffer_load_dwordx4 a[212:215], v250, s[20:23], s66 offen offset:0 sc1
s_mov_b64 exec, -1
buffer_load_dwordx4 v[240:243], v253, s[20:23], s68 offen offset:0 sc1
buffer_load_dwordx4 v[140:143], v253, s[20:23], s45 offen offset:0 sc1
v_lshlrev_b32 v10, 16, v246
v_and_b32 v11, 0xffff0000, v246
v_lshlrev_b32 v12, 16, v247
v_and_b32 v13, 0xffff0000, v247
v_mul_f32 v14, 0xbfb8aa3b, v10
v_mul_f32 v15, 0xbfb8aa3b, v11
v_mul_f32 v42, 0xbfb8aa3b, v12
v_mul_f32 v43, 0xbfb8aa3b, v13
EX v14,v14
EX v15,v15
EX v42,v42
EX v43,v43
v_add_f32 v14, 1.0, v14
v_add_f32 v15, 1.0, v15
v_add_f32 v42, 1.0, v42
v_add_f32 v43, 1.0, v43
v_rcp_f32 v14, v14
v_rcp_f32 v15, v15
v_rcp_f32 v42, v42
v_rcp_f32 v43, v43
v_mul_f32 v14, v10, v14
v_mul_f32 v15, v11, v15
v_mul_f32 v42, v12, v42
v_mul_f32 v43, v13, v43
CV v8,v14,v15
CV v9,v42,v43
s_cmp_lg_u32 s82, 0
s_cbranch_scc1 LYS_%=
LYE_%=:
s_mov_b32 s73, 30001
s_mov_b32 s65, 1
s_bitcmp0_b32 s17, 0
s_cbranch_scc1 LSA0_%=
MM v[72:75],v[64:67],a[0:3],0
MM v[72:75],v[64:67],a[4:7],v[72:75]
MM v[72:75],v[68:71],a[8:11],v[72:75]
MM v[72:75],v[68:71],a[12:15],v[72:75]
s_nop 7
v_cmp_u_f32 s[30:31], v72, v72
s_nop 1
s_cmp_lg_u64 s[30:31], 0
s_cbranch_scc1 LSA0_%=
s_bitset0_b32 s17, 0
CV v76,v72,v73
CV v77,v74,v75
ds_bpermute_b32 v76, v232, v76
ds_bpermute_b32 v77, v232, v77
v_lshlrev_b32 v10, 16, v0
v_and_b32 v11, 0xffff0000, v0
v_lshlrev_b32 v12, 16, v1
v_and_b32 v13, 0xffff0000, v1
v_lshlrev_b32 v14, 16, v32
v_and_b32 v15, 0xffff0000, v32
v_lshlrev_b32 v42, 16, v33
v_and_b32 v43, 0xffff0000, v33
s_add_u32 s80, s18, 0
s_waitcnt lgkmcnt(0)
v_lshlrev_b32 v72, 16, v76
v_and_b32 v73, 0xffff0000, v76
v_lshlrev_b32 v74, 16, v77
v_and_b32 v75, 0xffff0000, v77
v_mul_f32 v72, v72, v10
v_mul_f32 v73, v73, v11
v_mul_f32 v74, v74, v12
v_mul_f32 v75, v75, v13
CV v76,v72,v73
CV v77,v74,v75
v_lshlrev_b32 v72, 16, v76
v_and_b32 v73, 0xffff0000, v76
v_lshlrev_b32 v74, 16, v77
v_and_b32 v75, 0xffff0000, v77
v_add_f32 v72, v72, v14
v_add_f32 v73, v73, v15
v_add_f32 v74, v74, v42
v_add_f32 v75, v75, v43
CV v44,v72,v73
CV v45,v74,v75
buffer_store_dwordx2 v[44:45], v251, s[40:43], s80 offen offset:0 nt
s_nop 1
LSA0_%=:
s_bitcmp0_b32 s17, 1
s_cbranch_scc1 LSA1_%=
MM v[72:75],v[64:67],a[32:35],0
MM v[72:75],v[64:67],a[36:39],v[72:75]
MM v[72:75],v[68:71],a[40:43],v[72:75]
MM v[72:75],v[68:71],a[44:47],v[72:75]
s_nop 7
v_cmp_u_f32 s[30:31], v72, v72
s_nop 1
s_cmp_lg_u64 s[30:31], 0
s_cbranch_scc1 LSA1_%=
s_bitset0_b32 s17, 1
CV v76,v72,v73
CV v77,v74,v75
ds_bpermute_b32 v76, v232, v76
ds_bpermute_b32 v77, v232, v77
v_lshlrev_b32 v10, 16, v2
v_and_b32 v11, 0xffff0000, v2
v_lshlrev_b32 v12, 16, v3
v_and_b32 v13, 0xffff0000, v3
v_lshlrev_b32 v14, 16, v34
v_and_b32 v15, 0xffff0000, v34
v_lshlrev_b32 v42, 16, v35
v_and_b32 v43, 0xffff0000, v35
s_add_u32 s80, s18, 128
s_waitcnt lgkmcnt(0)
v_lshlrev_b32 v72, 16, v76
v_and_b32 v73, 0xffff0000, v76
v_lshlrev_b32 v74, 16, v77
v_and_b32 v75, 0xffff0000, v77
v_mul_f32 v72, v72, v10
v_mul_f32 v73, v73, v11
v_mul_f32 v74, v74, v12
v_mul_f32 v75, v75, v13
CV v76,v72,v73
CV v77,v74,v75
v_lshlrev_b32 v72, 16, v76
v_and_b32 v73, 0xffff0000, v76
v_lshlrev_b32 v74, 16, v77
v_and_b32 v75, 0xffff0000, v77
v_add_f32 v72, v72, v14
v_add_f32 v73, v73, v15
v_add_f32 v74, v74, v42
v_add_f32 v75, v75, v43
CV v46,v72,v73
CV v47,v74,v75
buffer_store_dwordx2 v[46:47], v251, s[40:43], s80 offen offset:0 nt
s_nop 1
LSA1_%=:
s_waitcnt vmcnt(0)
s_mov_b64 exec, 1
buffer_store_dword v233, off, s[20:23], s19 offset:12 sc1
s_mov_b64 exec, -1
MM v[72:75],v[64:67],a[192:195],0
MM v[100:103],v[64:67],v[80:83],0
MM v[96:99],v[64:67],v[16:19],0
MM v[72:75],v[64:67],a[196:199],v[72:75]
MM v[100:103],v[64:67],a[100:103],v[100:103]
MM v[96:99],v[64:67],a[68:71],v[96:99]
MM v[72:75],v[68:71],v[144:147],v[72:75]
MM v[100:103],v[68:71],v[112:115],v[100:103]
MM v[96:99],v[68:71],v[48:51],v[96:99]
MM v[72:75],v[68:71],a[212:215],v[72:75]
MM v[100:103],v[68:71],a[108:111],v[100:103]
MM v[96:99],v[68:71],a[76:79],v[96:99]
s_mov_b32 s65, 0
s_mov_b32 s90, 250
s_nop 5
v_cmp_u_f32 vcc, v72, v100
v_cmp_u_f32 s[30:31], v96, v96
s_nop 1
s_or_b64 vcc, vcc, s[30:31]
s_cbranch_scc1 LQS_%=
CV v76,v72,v73
CV v77,v74,v75
CV v108,v100,v101
CV v109,v102,v103
CV v78,v96,v97
CV v79,v98,v99
ds_bpermute_b32 v76, v232, v76
ds_bpermute_b32 v77, v232, v77
ds_bpermute_b32 v108, v232, v108
ds_bpermute_b32 v109, v232, v109
ds_bpermute_b32 v78, v232, v78
ds_bpermute_b32 v79, v232, v79
v_lshlrev_b32 v10, 16, v8
v_and_b32 v11, 0xffff0000, v8
v_lshlrev_b32 v12, 16, v9
v_and_b32 v13, 0xffff0000, v9
v_lshlrev_b32 v14, 16, v40
v_and_b32 v15, 0xffff0000, v40
v_lshlrev_b32 v42, 16, v41
v_and_b32 v43, 0xffff0000, v41
s_add_u32 s80, s63, 0
s_waitcnt lgkmcnt(4)
v_lshlrev_b32 v72, 16, v76
v_and_b32 v73, 0xffff0000, v76
v_lshlrev_b32 v74, 16, v77
v_and_b32 v75, 0xffff0000, v77
v_mul_f32 v72, v72, v10
v_mul_f32 v73, v73, v11
v_mul_f32 v74, v74, v12
v_mul_f32 v75, v75, v13
CV v76,v72,v73
CV v77,v74,v75
v_lshlrev_b32 v72, 16, v76
v_and_b32 v73, 0xffff0000, v76
v_lshlrev_b32 v74, 16, v77
v_and_b32 v75, 0xffff0000, v77
v_add_f32 v72, v72, v14
v_add_f32 v73, v73, v15
v_add_f32 v74, v74, v42
v_add_f32 v75, v75, v43
CV v44,v72,v73
CV v45,v74,v75
buffer_store_dwordx2 v[44:45], v254, s[40:43], s80 offen offset:0 nt
v_lshlrev_b32 v10, 16, v6
v_and_b32 v11, 0xffff0000, v6
v_lshlrev_b32 v12, 16, v7
v_and_b32 v13, 0xffff0000, v7
v_lshlrev_b32 v14, 16, v38
v_and_b32 v15, 0xffff0000, v38
v_lshlrev_b32 v42, 16, v39
v_and_b32 v43, 0xffff0000, v39
s_add_u32 s80, s18, 41088
s_waitcnt lgkmcnt(2)
v_lshlrev_b32 v100, 16, v108
v_and_b32 v101, 0xffff0000, v108
v_lshlrev_b32 v102, 16, v109
v_and_b32 v103, 0xffff0000, v109
v_mul_f32 v100, v100, v10
v_mul_f32 v101, v101, v11
v_mul_f32 v102, v102, v12
v_mul_f32 v103, v103, v13
CV v108,v100,v101
CV v109,v102,v103
v_lshlrev_b32 v100, 16, v108
v_and_b32 v101, 0xffff0000, v108
v_lshlrev_b32 v102, 16, v109
v_and_b32 v103, 0xffff0000, v109
v_add_f32 v100, v100, v14
v_add_f32 v101, v101, v15
v_add_f32 v102, v102, v42
v_add_f32 v103, v103, v43
CV v46,v100,v101
CV v47,v102,v103
buffer_store_dwordx2 v[46:47], v251, s[40:43], s80 offen offset:0 nt
v_lshlrev_b32 v10, 16, v4
v_and_b32 v11, 0xffff0000, v4
v_lshlrev_b32 v12, 16, v5
v_and_b32 v13, 0xffff0000, v5
v_lshlrev_b32 v14, 16, v36
v_and_b32 v15, 0xffff0000, v36
v_lshlrev_b32 v42, 16, v37
v_and_b32 v43, 0xffff0000, v37
s_add_u32 s80, s18, 40960
s_waitcnt lgkmcnt(0)
v_lshlrev_b32 v96, 16, v78
v_and_b32 v97, 0xffff0000, v78
v_lshlrev_b32 v98, 16, v79
v_and_b32 v99, 0xffff0000, v79
v_mul_f32 v96, v96, v10
)ASM"
R"ASM(
v_mul_f32 v97, v97, v11
v_mul_f32 v98, v98, v12
v_mul_f32 v99, v99, v13
CV v78,v96,v97
CV v79,v98,v99
v_lshlrev_b32 v96, 16, v78
v_and_b32 v97, 0xffff0000, v78
v_lshlrev_b32 v98, 16, v79
v_and_b32 v99, 0xffff0000, v79
v_add_f32 v96, v96, v14
v_add_f32 v97, v97, v15
v_add_f32 v98, v98, v42
LPG25_%=:
s_nop 0
v_add_f32 v99, v99, v43
CV v44,v96,v97
CV v45,v98,v99
buffer_store_dwordx2 v[44:45], v251, s[40:43], s80 offen offset:0 nt
s_and_b32 s17, s17, 3
s_cbranch_scc0 LQE_%=
LQS_%=:
v_cmp_eq_u32 s[12:13], s69, v242
v_cmp_eq_u32 s[14:15], s69, v142
v_cmp_eq_u32 s[8:9], s69, v241
v_cmp_eq_u32 s[10:11], s69, v141
v_cmp_eq_u32 s[36:37], s69, v240
v_cmp_eq_u32 s[38:39], s69, v140
s_mov_b32 s94, 0
s_sub_u32 s90, s90, 1
s_cbranch_scc1 LSW_%=
s_and_b64 vcc, s[12:13], s[14:15]
s_and_b32 s12, s12, 0xffff0000
s_and_b32 s8, s8, 0xffff0000
s_and_b32 s36, s36, 0xffff0000
s_bitcmp0_b32 s17, 4
s_cbranch_scc1 LQG4_%=
AR v108,a195
AR v109,a199
AR v111,a215
v_cmp_eq_u32 s[24:25], -1, v108
v_cmp_eq_u32 s[26:27], -1, v109
v_cmp_eq_u32 s[28:29], -1, v147
v_cmp_eq_u32 s[46:47], -1, v111
s_nop 0
s_or_b64 s[30:31], s[24:25], s[26:27]
s_or_b64 s[30:31], s[30:31], s[28:29]
s_or_b64 s[30:31], s[30:31], s[46:47]
s_cselect_b32 s76, 256, 16
s_or_b32 s94, s94, s76
s_bitcmp1_b32 s94, 4
s_cbranch_scc1 LQG4_%=
s_and_b64 exec, s[46:47], s[14:15]
s_cbranch_scc0 LQK43_%=
s_add_u32 s66, s44, 18432
buffer_load_dwordx4 a[212:215], v250, s[20:23], s66 offen offset:0 sc1
LQK43_%=:
s_and_b64 exec, s[28:29], s[12:13]
s_cbranch_scc0 LQK42_%=
s_add_u32 s66, s16, 18432
buffer_load_dwordx4 v[144:147], v250, s[20:23], s66 offen offset:0 sc1
LQK42_%=:
s_and_b64 exec, s[26:27], s[10:11]
s_cbranch_scc0 LQK41_%=
s_add_u32 s66, s44, 8192
buffer_load_dwordx4 a[196:199], v250, s[20:23], s66 offen offset:0 sc1
LQK41_%=:
s_and_b64 exec, s[24:25], s[8:9]
s_cbranch_scc0 LQK40_%=
s_add_u32 s66, s16, 8192
buffer_load_dwordx4 a[192:195], v250, s[20:23], s66 offen offset:0 sc1
LQK40_%=:
s_mov_b64 exec, -1
LQG4_%=:
s_bitcmp0_b32 s17, 3
s_cbranch_scc1 LQG3_%=
AR v109,a103
AR v111,a111
v_cmp_eq_u32 s[24:25], -1, v83
v_cmp_eq_u32 s[26:27], -1, v109
v_cmp_eq_u32 s[28:29], -1, v115
v_cmp_eq_u32 s[46:47], -1, v111
s_nop 0
s_or_b64 s[30:31], s[24:25], s[26:27]
s_or_b64 s[30:31], s[30:31], s[28:29]
s_or_b64 s[30:31], s[30:31], s[46:47]
s_cselect_b32 s76, 256, 8
s_or_b32 s94, s94, s76
s_bitcmp1_b32 s94, 3
s_cbranch_scc1 LQG3_%=
s_and_b64 exec, s[46:47], s[14:15]
s_cbranch_scc0 LQK33_%=
s_add_u32 s66, s44, 16384
buffer_load_dwordx4 a[108:111], v250, s[20:23], s66 offen offset:0 sc1
LQK33_%=:
s_and_b64 exec, s[28:29], s[12:13]
s_cbranch_scc0 LQK32_%=
s_add_u32 s66, s16, 16384
buffer_load_dwordx4 v[112:115], v250, s[20:23], s66 offen offset:0 sc1
LQK32_%=:
s_and_b64 exec, s[26:27], s[14:15]
s_cbranch_scc0 LQK31_%=
s_add_u32 s66, s44, 14336
buffer_load_dwordx4 a[100:103], v250, s[20:23], s66 offen offset:0 sc1
LQK31_%=:
s_and_b64 exec, s[24:25], s[12:13]
s_cbranch_scc0 LQK30_%=
s_add_u32 s66, s16, 14336
buffer_load_dwordx4 v[80:83], v250, s[20:23], s66 offen offset:0 sc1
LQK30_%=:
s_mov_b64 exec, -1
LQG3_%=:
s_bitcmp0_b32 s17, 2
s_cbranch_scc1 LQG2_%=
AR v109,a71
AR v111,a79
v_cmp_eq_u32 s[24:25], -1, v19
v_cmp_eq_u32 s[26:27], -1, v109
v_cmp_eq_u32 s[28:29], -1, v51
v_cmp_eq_u32 s[46:47], -1, v111
s_nop 0
s_or_b64 s[30:31], s[24:25], s[26:27]
s_or_b64 s[30:31], s[30:31], s[28:29]
s_or_b64 s[30:31], s[30:31], s[46:47]
s_cselect_b32 s76, 256, 4
s_or_b32 s94, s94, s76
s_bitcmp1_b32 s94, 2
s_cbranch_scc1 LQG2_%=
s_and_b64 exec, s[46:47], s[14:15]
s_cbranch_scc0 LQK23_%=
s_add_u32 s66, s44, 12288
buffer_load_dwordx4 a[76:79], v250, s[20:23], s66 offen offset:0 sc1
LQK23_%=:
s_and_b64 exec, s[28:29], s[12:13]
s_cbranch_scc0 LQK22_%=
s_add_u32 s66, s16, 12288
buffer_load_dwordx4 v[48:51], v250, s[20:23], s66 offen offset:0 sc1
LQK22_%=:
s_and_b64 exec, s[26:27], s[14:15]
s_cbranch_scc0 LQK21_%=
s_add_u32 s66, s44, 10240
buffer_load_dwordx4 a[68:71], v250, s[20:23], s66 offen offset:0 sc1
LQK21_%=:
s_and_b64 exec, s[24:25], s[12:13]
s_cbranch_scc0 LQK20_%=
s_add_u32 s66, s16, 10240
buffer_load_dwordx4 v[16:19], v250, s[20:23], s66 offen offset:0 sc1
LQK20_%=:
s_mov_b64 exec, -1
LQG2_%=:
s_and_b32 s76, s17, 3
s_cbranch_scc1 LQA_%=
LQB_%=:
s_cmp_eq_u64 vcc, -1
s_cbranch_scc1 LQF_%=
buffer_load_dwordx4 v[240:243], v253, s[20:23], s68 offen offset:0 sc1
buffer_load_dwordx4 v[140:143], v253, s[20:23], s45 offen offset:0 sc1
LQF_%=:
s_and_b32 s76, s94, 3
s_cbranch_scc1 LQC_%=
LQD_%=:
s_bitcmp0_b32 s94, 2
s_cbranch_scc1 LQN2_%=
MM v[72:75],v[64:67],v[16:19],0
MM v[72:75],v[64:67],a[68:71],v[72:75]
MM v[72:75],v[68:71],v[48:51],v[72:75]
MM v[72:75],v[68:71],a[76:79],v[72:75]
s_bitset0_b32 s17, 2
s_nop 6
CV v76,v72,v73
CV v77,v74,v75
ds_bpermute_b32 v76, v232, v76
ds_bpermute_b32 v77, v232, v77
v_lshlrev_b32 v10, 16, v4
v_and_b32 v11, 0xffff0000, v4
v_lshlrev_b32 v12, 16, v5
v_and_b32 v13, 0xffff0000, v5
v_lshlrev_b32 v14, 16, v36
v_and_b32 v15, 0xffff0000, v36
v_lshlrev_b32 v42, 16, v37
v_and_b32 v43, 0xffff0000, v37
s_add_u32 s80, s18, 40960
s_waitcnt lgkmcnt(0)
v_lshlrev_b32 v72, 16, v76
v_and_b32 v73, 0xffff0000, v76
v_lshlrev_b32 v74, 16, v77
v_and_b32 v75, 0xffff0000, v77
v_mul_f32 v72, v72, v10
v_mul_f32 v73, v73, v11
v_mul_f32 v74, v74, v12
v_mul_f32 v75, v75, v13
CV v76,v72,v73
CV v77,v74,v75
v_lshlrev_b32 v72, 16, v76
v_and_b32 v73, 0xffff0000, v76
v_lshlrev_b32 v74, 16, v77
v_and_b32 v75, 0xffff0000, v77
v_add_f32 v72, v72, v14
v_add_f32 v73, v73, v15
v_add_f32 v74, v74, v42
v_add_f32 v75, v75, v43
CV v44,v72,v73
CV v45,v74,v75
buffer_store_dwordx2 v[44:45], v251, s[40:43], s80 offen offset:0 nt
s_bitcmp0_b32 s94, 8
s_cbranch_scc1 LQN2_%=
s_waitcnt vmcnt(1)
s_branch LQS_%=
LQN2_%=:
s_bitcmp0_b32 s94, 3
s_cbranch_scc1 LQN3_%=
MM v[72:75],v[64:67],v[80:83],0
MM v[72:75],v[64:67],a[100:103],v[72:75]
MM v[72:75],v[68:71],v[112:115],v[72:75]
MM v[72:75],v[68:71],a[108:111],v[72:75]
s_bitset0_b32 s17, 3
s_nop 6
CV v76,v72,v73
CV v77,v74,v75
ds_bpermute_b32 v76, v232, v76
ds_bpermute_b32 v77, v232, v77
v_lshlrev_b32 v10, 16, v6
v_and_b32 v11, 0xffff0000, v6
v_lshlrev_b32 v12, 16, v7
v_and_b32 v13, 0xffff0000, v7
v_lshlrev_b32 v14, 16, v38
v_and_b32 v15, 0xffff0000, v38
v_lshlrev_b32 v42, 16, v39
v_and_b32 v43, 0xffff0000, v39
s_add_u32 s80, s18, 41088
s_waitcnt lgkmcnt(0)
v_lshlrev_b32 v72, 16, v76
v_and_b32 v73, 0xffff0000, v76
v_lshlrev_b32 v74, 16, v77
v_and_b32 v75, 0xffff0000, v77
v_mul_f32 v72, v72, v10
v_mul_f32 v73, v73, v11
v_mul_f32 v74, v74, v12
v_mul_f32 v75, v75, v13
CV v76,v72,v73
CV v77,v74,v75
v_lshlrev_b32 v72, 16, v76
v_and_b32 v73, 0xffff0000, v76
v_lshlrev_b32 v74, 16, v77
v_and_b32 v75, 0xffff0000, v77
v_add_f32 v72, v72, v14
v_add_f32 v73, v73, v15
v_add_f32 v74, v74, v42
v_add_f32 v75, v75, v43
CV v46,v72,v73
CV v47,v74,v75
buffer_store_dwordx2 v[46:47], v251, s[40:43], s80 offen offset:0 nt
s_bitcmp0_b32 s94, 8
s_cbranch_scc1 LQN3_%=
s_waitcnt vmcnt(1)
s_branch LQS_%=
LQN3_%=:
s_bitcmp0_b32 s94, 4
s_cbranch_scc1 LQN4_%=
MM v[72:75],v[64:67],a[192:195],0
MM v[72:75],v[64:67],a[196:199],v[72:75]
MM v[72:75],v[68:71],v[144:147],v[72:75]
MM v[72:75],v[68:71],a[212:215],v[72:75]
s_bitset0_b32 s17, 4
s_nop 6
CV v76,v72,v73
CV v77,v74,v75
ds_bpermute_b32 v76, v232, v76
ds_bpermute_b32 v77, v232, v77
v_lshlrev_b32 v10, 16, v8
v_and_b32 v11, 0xffff0000, v8
v_lshlrev_b32 v12, 16, v9
v_and_b32 v13, 0xffff0000, v9
v_lshlrev_b32 v14, 16, v40
v_and_b32 v15, 0xffff0000, v40
v_lshlrev_b32 v42, 16, v41
v_and_b32 v43, 0xffff0000, v41
s_add_u32 s80, s63, 0
s_waitcnt lgkmcnt(0)
v_lshlrev_b32 v72, 16, v76
v_and_b32 v73, 0xffff0000, v76
v_lshlrev_b32 v74, 16, v77
v_and_b32 v75, 0xffff0000, v77
v_mul_f32 v72, v72, v10
v_mul_f32 v73, v73, v11
v_mul_f32 v74, v74, v12
v_mul_f32 v75, v75, v13
CV v76,v72,v73
CV v77,v74,v75
v_lshlrev_b32 v72, 16, v76
v_and_b32 v73, 0xffff0000, v76
v_lshlrev_b32 v74, 16, v77
v_and_b32 v75, 0xffff0000, v77
v_add_f32 v72, v72, v14
v_add_f32 v73, v73, v15
v_add_f32 v74, v74, v42
v_add_f32 v75, v75, v43
CV v44,v72,v73
CV v45,v74,v75
buffer_store_dwordx2 v[44:45], v254, s[40:43], s80 offen offset:0 nt
s_bitcmp0_b32 s94, 8
s_cbranch_scc1 LQN4_%=
s_waitcnt vmcnt(1)
s_branch LQS_%=
LQN4_%=:
s_cmp_eq_u32 s17, 0
s_cbranch_scc1 LQE_%=
s_waitcnt vmcnt(0)
s_branch LQS_%=
LQE_%=:
s_endpgm
LYS_%=:
s_waitcnt vmcnt(15)
LYT_%=:
v_ashrrev_i32 v139, 31, v138
v_not_b32 v139, v139
v_or_b32 v139, 0x80000000, v139
v_xor_b32 v138, v138, v139
v_and_b32 v138, 0xffff0000, v138
v_mul_f32 v138, 0xbfb8aa3b, v138
EX v138,v138
v_pk_add_f32 v[160:161], v[160:161], v[196:197]
v_pk_add_f32 v[162:163], v[162:163], v[198:199]
v_pk_add_f32 v[164:165], v[164:165], v[200:201]
v_pk_add_f32 v[166:167], v[166:167], v[202:203]
v_pk_add_f32 v[168:169], v[168:169], v[204:205]
v_pk_add_f32 v[170:171], v[170:171], v[206:207]
v_pk_add_f32 v[172:173], v[172:173], v[208:209]
v_pk_add_f32 v[174:175], v[174:175], v[210:211]
v_pk_add_f32 v[176:177], v[176:177], v[212:213]
v_pk_add_f32 v[178:179], v[178:179], v[214:215]
v_pk_add_f32 v[180:181], v[180:181], v[216:217]
v_pk_add_f32 v[182:183], v[182:183], v[218:219]
v_pk_add_f32 v[184:185], v[184:185], v[220:221]
v_pk_add_f32 v[186:187], v[186:187], v[222:223]
v_pk_add_f32 v[188:189], v[188:189], v[96:97]
v_pk_add_f32 v[190:191], v[190:191], v[98:99]
v_pk_add_f32 v[192:193], v[192:193], v[100:101]
v_pk_add_f32 v[194:195], v[194:195], v[102:103]
v_pk_add_f32 v[160:161], v[160:161], v[192:193]
v_pk_add_f32 v[162:163], v[162:163], v[194:195]
v_add_f32 v138, 1.0, v138
v_rcp_f32 v138, v138
v_pk_add_f32 v[160:161], v[160:161], v[176:177]
v_pk_add_f32 v[162:163], v[162:163], v[178:179]
v_pk_add_f32 v[164:165], v[164:165], v[180:181]
v_pk_add_f32 v[166:167], v[166:167], v[182:183]
v_pk_add_f32 v[168:169], v[168:169], v[184:185]
v_pk_add_f32 v[170:171], v[170:171], v[186:187]
v_pk_add_f32 v[172:173], v[172:173], v[188:189]
v_pk_add_f32 v[174:175], v[174:175], v[190:191]
CV v138,v138,v138
v_and_b32 v138, 0xffff0000, v138
v_pk_add_f32 v[160:161], v[160:161], v[168:169]
v_pk_add_f32 v[162:163], v[162:163], v[170:171]
v_pk_add_f32 v[164:165], v[164:165], v[172:173]
v_pk_add_f32 v[166:167], v[166:167], v[174:175]
v_pk_add_f32 v[160:161], v[160:161], v[164:165]
v_pk_add_f32 v[162:163], v[162:163], v[166:167]
CV v164,v160,v161
CV v165,v162,v163
v_lshlrev_b32 v160, 16, v164
v_and_b32 v161, 0xffff0000, v164
v_lshlrev_b32 v162, 16, v165
v_and_b32 v163, 0xffff0000, v165
v_mul_f32 v160, v160, v138
v_mul_f32 v161, v161, v138
v_mul_f32 v162, v162, v138
v_mul_f32 v163, v163, v138
CV v164,v160,v161
CV v165,v162,v163
v_lshlrev_b32 v160, 16, v164
v_and_b32 v161, 0xffff0000, v164
v_lshlrev_b32 v162, 16, v165
v_and_b32 v163, 0xffff0000, v165
v_lshlrev_b32 v168, 16, v136
v_and_b32 v169, 0xffff0000, v136
v_lshlrev_b32 v170, 16, v137
v_and_b32 v171, 0xffff0000, v137
v_add_f32 v160, v160, v168
v_add_f32 v161, v161, v169
v_add_f32 v162, v162, v170
v_add_f32 v163, v163, v171
CV v164,v160,v161
CV v165,v162,v163
v_lshlrev_b32 v160, 16, v164
v_and_b32 v161, 0xffff0000, v164
v_lshlrev_b32 v162, 16, v165
v_and_b32 v163, 0xffff0000, v165
v_mul_f32 v168, 0xbfb8aa3b, v160
v_mul_f32 v169, 0xbfb8aa3b, v161
v_mul_f32 v170, 0xbfb8aa3b, v162
v_mul_f32 v171, 0xbfb8aa3b, v163
EX v168,v168
EX v169,v169
EX v170,v170
EX v171,v171
v_bfe_u32 v139, v248, 2, 6
v_and_b32 v132, 28, v139
v_add_f32 v168, 1.0, v168
v_add_f32 v169, 1.0, v169
v_add_f32 v170, 1.0, v170
v_add_f32 v171, 1.0, v171
v_rcp_f32 v168, v168
v_rcp_f32 v169, v169
v_rcp_f32 v170, v170
v_rcp_f32 v171, v171
s_and_b32 s77, s84, 1
s_lshl_b32 s76, s77, 5
v_add_u32 v139, s76, v139
s_xor_b32 s77, s77, 1
s_lshl_b32 s77, s77, 7
v_add_u32 v132, s77, v132
v_mul_f32 v168, v160, v168
v_mul_f32 v169, v161, v169
v_mul_f32 v170, v162, v170
v_mul_f32 v171, v163, v171
CV v172,v168,v169
CV v173,v170,v171
ds_bpermute_b32 v176, v139, v172 offset:0
ds_bpermute_b32 v177, v139, v173 offset:0
ds_bpermute_b32 v180, v139, v172 offset:64
ds_bpermute_b32 v181, v139, v173 offset:64
ds_bpermute_b32 v184, v132, v172 offset:0
ds_bpermute_b32 v185, v132, v173 offset:0
s_ff1_i32_b32 s76, s82
)ASM"
R"ASM(
s_bitset0_b32 s82, s76
s_and_b32 s77, s79, 3
s_lshl_b32 s77, s77, 4
s_and_b32 s76, s76, 8
s_waitcnt lgkmcnt(0)
s_lshl_b64 vcc, 0xffff, s77
s_cmp_eq_u32 s76, 0
s_cselect_b64 exec, vcc, 0
v_mov_b32 v0, v176
v_mov_b32 v1, v177
v_mov_b32 v2, v180
v_mov_b32 v3, v181
s_cselect_b64 exec, 0, vcc
v_mov_b32 v4, v176
v_mov_b32 v5, v177
v_mov_b32 v6, v180
v_mov_b32 v7, v181
s_add_u32 s77, s77, s76
s_lshl_b64 exec, 0xff, s77
v_mov_b32 v8, v184
v_mov_b32 v9, v185
s_mov_b64 exec, -1
s_cmp_eq_u32 s82, 0
s_cbranch_scc1 LYE_%=
s_ff1_i32_b32 s76, s82
s_and_b32 s76, s76, 7
s_mov_b32 s79, s52
s_cmp_eq_u32 s76, 1
s_cselect_b32 s79, s53, s79
s_cmp_eq_u32 s76, 2
s_cselect_b32 s79, s54, s79
s_cmp_eq_u32 s76, 3
s_cselect_b32 s79, s55, s79
s_cmp_eq_u32 s76, 4
s_cselect_b32 s79, s56, s79
s_cmp_eq_u32 s76, 5
s_cselect_b32 s79, s57, s79
s_cmp_eq_u32 s76, 6
s_cselect_b32 s79, s58, s79
s_cmp_eq_u32 s76, 7
s_cselect_b32 s79, s59, s79
s_mul_i32 s76, s76, 2560
s_mul_i32 s90, s84, 640
s_add_u32 s76, s76, s90
s_add_u32 s76, s76, 21659648
s_lshl_b32 s90, s79, 2
s_add_u32 s90, s90, 53248
buffer_load_dword v138, off, s[20:23], s90 sc1
s_mul_i32 s90, s79, 1280
s_mul_i32 s77, s84, 320
s_add_u32 s90, s90, s77
s_add_u32 s90, s90, 131072
v_lshrrev_b32 v139, 1, v248
s_mov_b32 exec_lo, -1
s_mov_b32 exec_hi, 255
buffer_load_dwordx2 v[136:137], v139, s[20:23], s90 offen offset:0 sc1
s_mov_b32 exec_lo, -1
s_mov_b32 exec_hi, 255
buffer_load_dwordx4 v[160:163], v248, s[20:23], s76 offen offset:0 sc1
s_add_u32 s76, s76, 20480
buffer_load_dwordx4 v[164:167], v248, s[20:23], s76 offen offset:0 sc1
s_add_u32 s76, s76, 20480
buffer_load_dwordx4 v[168:171], v248, s[20:23], s76 offen offset:0 sc1
s_add_u32 s76, s76, 20480
buffer_load_dwordx4 v[172:175], v248, s[20:23], s76 offen offset:0 sc1
s_add_u32 s76, s76, 20480
buffer_load_dwordx4 v[176:179], v248, s[20:23], s76 offen offset:0 sc1
s_add_u32 s76, s76, 20480
buffer_load_dwordx4 v[180:183], v248, s[20:23], s76 offen offset:0 sc1
s_add_u32 s76, s76, 20480
buffer_load_dwordx4 v[184:187], v248, s[20:23], s76 offen offset:0 sc1
s_add_u32 s76, s76, 20480
buffer_load_dwordx4 v[188:191], v248, s[20:23], s76 offen offset:0 sc1
s_add_u32 s76, s76, 20480
buffer_load_dwordx4 v[192:195], v248, s[20:23], s76 offen offset:0 sc1
s_add_u32 s76, s76, 20480
buffer_load_dwordx4 v[196:199], v248, s[20:23], s76 offen offset:0 sc1
s_add_u32 s76, s76, 20480
buffer_load_dwordx4 v[200:203], v248, s[20:23], s76 offen offset:0 sc1
s_add_u32 s76, s76, 20480
buffer_load_dwordx4 v[204:207], v248, s[20:23], s76 offen offset:0 sc1
s_add_u32 s76, s76, 20480
buffer_load_dwordx4 v[208:211], v248, s[20:23], s76 offen offset:0 sc1
s_add_u32 s76, s76, 20480
buffer_load_dwordx4 v[212:215], v248, s[20:23], s76 offen offset:0 sc1
s_add_u32 s76, s76, 20480
buffer_load_dwordx4 v[216:219], v248, s[20:23], s76 offen offset:0 sc1
s_add_u32 s76, s76, 20480
buffer_load_dwordx4 v[220:223], v248, s[20:23], s76 offen offset:0 sc1
s_add_u32 s76, s76, 20480
buffer_load_dwordx4 v[96:99], v248, s[20:23], s76 offen offset:0 sc1
s_add_u32 s76, s76, 20480
buffer_load_dwordx4 v[100:103], v248, s[20:23], s76 offen offset:0 sc1
s_mov_b64 exec, -1
s_waitcnt vmcnt(0)
s_branch LYT_%=
.fill 302, 4, 0xbf800000
LXP_%=:
LPG26_%=:
s_cbranch_execz LTR1_%=
v_mov_b32 v250, s69
s_mov_b32 s76, 32768
s_mov_b64 exec, 1
buffer_store_dword v250, off, s[20:23], s76 sc1
s_mov_b64 exec, -1
s_add_u32 s79, %[slds], 154112
s_lshl_b32 s80, %[swave], 2
MM v[64:67],a[192:195],v[160:163],v[128:131]
s_add_u32 s64, s64, 16384
TR a[128:129],v232 offset:0
s_add_u32 s81, s64, 896
MM v[64:67],a[196:199],v[176:179],v[64:67]
TR a[130:131],v232 offset:4096
TR a[132:133],v232 offset:8192
MM v[64:67],a[200:203],v[192:195],v[64:67]
TR a[134:135],v232 offset:12288
TR a[136:137],v233 offset:0
MM v[64:67],a[204:207],v[208:211],v[64:67]
TR a[138:139],v233 offset:4096
v_add_f32 v246, v236, v237
MM v[68:71],a[220:223],v[208:211],v[128:131]
TR a[140:141],v233 offset:8192
v_cmp_nge_f32 vcc, s71, v246
MM v[68:71],a[216:219],v[192:195],v[68:71]
s_cbranch_vccnz LSITE4_%=
LBACK4_%=:
MM v[68:71],a[212:215],v[176:179],v[68:71]
s_mov_b32 m0, s67
TR a[142:143],v233 offset:12288
TR a[144:145],v232 offset:512
MM v[68:71],a[208:211],v[160:163],v[68:71]
buffer_load_dwordx4 v252, s[36:39], s66 offen offset:0 lds
MM v[76:79],a[208:211],v[164:167],v[132:135]
s_add_u32 s66, s66, 55296
EX v64,v64
MM v[76:79],a[212:215],v[180:183],v[76:79]
s_min_u32 s66, s66, s85
EX v65,v65
MM v[76:79],a[216:219],v[196:199],v[76:79]
s_add_u32 s67, s67, 1024
TR a[146:147],v232 offset:4608
CV v64,v64,v65
MM v[76:79],a[220:223],v[212:215],v[76:79]
s_min_u32 s67, s67, s86
EX v66,v66
MM v[72:75],a[204:207],v[212:215],v[132:135]
EX v67,v67
MM v[72:75],a[200:203],v[196:199],v[72:75]
TR a[148:149],v232 offset:8704
CV v65,v66,v67
MM v[72:75],a[196:199],v[180:183],v[72:75]
EX v68,v68
MM v[72:75],a[192:195],v[164:167],v[72:75]
TR a[150:151],v232 offset:12800
TR a[152:153],v233 offset:512
MM v[80:83],a[192:195],v[168:171],v[136:139]
EX v69,v69
MM v[80:83],a[196:199],v[184:187],v[80:83]
TR a[154:155],v233 offset:4608
CV v66,v68,v69
MM v[80:83],a[200:203],v[200:203],v[80:83]
EX v70,v70
MM v[80:83],a[204:207],v[216:219],v[80:83]
EX v71,v71
MM v[84:87],a[220:223],v[216:219],v[136:139]
TR a[156:157],v233 offset:8704
CV v67,v70,v71
MM v[84:87],a[216:219],v[200:203],v[84:87]
EX v72,v72
MM v[84:87],a[212:215],v[184:187],v[84:87]
TR a[158:159],v233 offset:12800
TR a[160:161],v232 offset:1024
MM v[84:87],a[208:211],v[168:171],v[84:87]
EX v73,v73
MM v[92:95],a[208:211],v[172:175],v[140:143]
TR a[162:163],v232 offset:5120
CV v72,v72,v73
MM v[92:95],a[212:215],v[188:191],v[92:95]
EX v74,v74
MM v[92:95],a[216:219],v[204:207],v[92:95]
EX v75,v75
MM v[92:95],a[220:223],v[220:223],v[92:95]
TR a[164:165],v232 offset:9216
CV v73,v74,v75
MM v[88:91],a[204:207],v[220:223],v[140:143]
EX v76,v76
MM v[88:91],a[200:203],v[204:207],v[88:91]
TR a[166:167],v232 offset:13312
TR a[168:169],v233 offset:1024
MM v[88:91],a[196:199],v[188:191],v[88:91]
EX v77,v77
MM v[88:91],a[192:195],v[172:175],v[88:91]
TR a[170:171],v233 offset:5120
CV v74,v76,v77
MM v[96:99],a[224:227],v[160:163],v[128:131]
EX v78,v78
MM v[96:99],a[228:231],v[176:179],v[96:99]
EX v79,v79
MM v[96:99],a[232:235],v[192:195],v[96:99]
TR a[172:173],v233 offset:9216
CV v75,v78,v79
MM v[96:99],a[236:239],v[208:211],v[96:99]
TR a[174:175],v233 offset:13312
TR a[176:177],v232 offset:1536
MM v[100:103],a[252:255],v[208:211],v[128:131]
EX v80,v80
MM v[100:103],a[248:251],v[192:195],v[100:103]
EX v81,v81
MM v[100:103],a[244:247],v[176:179],v[100:103]
TR a[178:179],v232 offset:5632
CV v80,v80,v81
MM v[100:103],a[240:243],v[160:163],v[100:103]
EX v82,v82
MM v[108:111],a[240:243],v[164:167],v[132:135]
v_add_u32 v48, s78, v248
v_mov_b32 v49, s79
MM v[108:111],a[244:247],v[180:183],v[108:111]
EX v83,v83
MM v[108:111],a[248:251],v[196:199],v[108:111]
TR a[180:181],v232 offset:9728
CV v81,v82,v83
MM v[108:111],a[252:255],v[212:215],v[108:111]
EX v84,v84
MM v[104:107],a[236:239],v[212:215],v[132:135]
TR a[182:183],v232 offset:13824
TR a[184:185],v233 offset:1536
MM v[104:107],a[232:235],v[196:199],v[104:107]
EX v85,v85
MM v[104:107],a[228:231],v[180:183],v[104:107]
TR a[186:187],v233 offset:5632
CV v82,v84,v85
MM v[104:107],a[224:227],v[164:167],v[104:107]
EX v86,v86
MM v[112:115],a[224:227],v[168:171],v[136:139]
TR a[188:189],v233 offset:9728
TR a[190:191],v233 offset:13824
MM v[112:115],a[228:231],v[184:187],v[112:115]
EX v87,v87
MM v[112:115],a[232:235],v[200:203],v[112:115]
CV v83,v86,v87
MM v[112:115],a[236:239],v[216:219],v[112:115]
EX v88,v88
MM v[116:119],a[252:255],v[216:219],v[136:139]
EX v89,v89
MM v[116:119],a[248:251],v[200:203],v[116:119]
EX v90,v90
MM v[116:119],a[244:247],v[184:187],v[116:119]
EX v91,v91
MM v[116:119],a[240:243],v[168:171],v[116:119]
EX v92,v92
MM v[124:127],a[240:243],v[172:175],v[140:143]
EX v93,v93
MM v[124:127],a[244:247],v[188:191],v[124:127]
EX v94,v94
MM v[124:127],a[248:251],v[204:207],v[124:127]
EX v95,v95
MM v[124:127],a[252:255],v[220:223],v[124:127]
EX v96,v96
MM v[120:123],a[236:239],v[220:223],v[140:143]
EX v97,v97
MM v[120:123],a[232:235],v[204:207],v[120:123]
EX v98,v98
MM v[120:123],a[228:231],v[188:191],v[120:123]
EX v99,v99
MM v[120:123],a[224:227],v[172:175],v[120:123]
s_waitcnt vmcnt(13) lgkmcnt(0)
s_barrier
MM a[0:3],a[128:131],v[0:3],a[0:3]
s_add_u32 m0, s65, 0
RD v[32:35],v48 offset:0
RD v[36:39],v48 offset:1024
MM a[0:3],a[132:135],v[4:7],a[0:3]
s_mov_b32 s92, -1
CV v88,v88,v89
CV v89,v90,v91
MM a[8:11],a[132:135],v[12:15],a[8:11]
RD a[192:195],v224 offset:32768
CV v90,v92,v93
MM a[8:11],a[128:131],v[8:11],a[8:11]
RD a[196:199],v225 offset:32768
CV v91,v94,v95
MM a[16:19],a[128:131],v[16:19],a[16:19]
buffer_load_dwordx4 v228, s[8:11], s64 offen offset:0 lds
MM a[16:19],a[132:135],v[20:23],a[16:19]
s_waitcnt lgkmcnt(2)
v_sub_u32 v32, s92, v32
v_sub_u32 v33, s92, v33
MM a[24:27],a[132:135],v[28:31],a[24:27]
v_sub_u32 v34, s92, v34
v_sub_u32 v35, s92, v35
MM a[24:27],a[128:131],v[24:27],a[24:27]
v_sub_u32 v36, s92, v36
v_sub_u32 v37, s92, v37
MM a[28:31],a[136:139],v[24:27],a[28:31]
v_sub_u32 v38, s92, v38
v_sub_u32 v39, s92, v39
MM a[28:31],a[140:143],v[28:31],a[28:31]
v_min3_u32 v52, v32, v33, v34
v_min3_u32 v51, v35, v36, v37
MM a[20:23],a[140:143],v[20:23],a[20:23]
v_min3_u32 v52, v52, v38, v39
v_min_u32 v52, v52, v51
MM a[20:23],a[136:139],v[16:19],a[20:23]
s_nop 0
v_min_u32_dpp v52, v52, v52 row_shr:1 row_mask:0xf bank_mask:0xf
CV v68,v96,v97
MM a[12:15],a[136:139],v[8:11],a[12:15]
RD a[200:203],v226 offset:32768
v_min_u32_dpp v52, v52, v52 row_shr:2 row_mask:0xf bank_mask:0xf
MM a[12:15],a[140:143],v[12:15],a[12:15]
s_nop 0
v_min_u32_dpp v52, v52, v52 row_shr:4 row_mask:0xf bank_mask:0xf
CV v69,v98,v99
MM a[4:7],a[140:143],v[4:7],a[4:7]
RD a[204:207],v227 offset:32768
v_min_u32_dpp v52, v52, v52 row_shr:8 row_mask:0xf bank_mask:0xf
MM a[4:7],a[136:139],v[0:3],a[4:7]
RD a[208:211],v224 offset:36864
v_min_u32_dpp v52, v52, v52 row_bcast:15 row_mask:0xa bank_mask:0xf
MM a[32:35],a[144:147],v[0:3],a[32:35]
RD a[212:215],v225 offset:36864
v_min_u32_dpp v52, v52, v52 row_bcast:31 row_mask:0xc bank_mask:0xf
EX v100,v100
MM a[32:35],a[148:151],v[4:7],a[32:35]
v_readlane_b32 s76, v52, 63
v_lshl_add_u32 v49, s80, 1, v49
EX v101,v101
MM a[40:43],a[148:151],v[12:15],a[40:43]
EX v102,v102
MM a[40:43],a[144:147],v[8:11],a[40:43]
EX v103,v103
MM a[48:51],a[144:147],v[16:19],a[48:51]
buffer_load_dwordx4 v229, s[8:11], s64 offen offset:1024 lds
MM a[48:51],a[148:151],v[20:23],a[48:51]
s_add_u32 s76, s76, 1
v_subrev_u32 v32, s76, v32
v_subrev_u32 v33, s76, v33
MM a[56:59],a[148:151],v[28:31],a[56:59]
v_subrev_u32 v34, s76, v34
v_subrev_u32 v35, s76, v35
EX v104,v104
MM a[56:59],a[144:147],v[24:27],a[56:59]
v_subrev_u32 v36, s76, v36
v_subrev_u32 v37, s76, v37
MM a[60:63],a[152:155],v[24:27],a[60:63]
v_subrev_u32 v38, s76, v38
v_subrev_u32 v39, s76, v39
EX v105,v105
MM a[60:63],a[156:159],v[28:31],a[60:63]
v_min3_u32 v53, v32, v33, v34
v_min3_u32 v51, v35, v36, v37
MM a[52:55],a[156:159],v[20:23],a[52:55]
v_min3_u32 v53, v53, v38, v39
v_min_u32 v53, v53, v51
EX v106,v106
MM a[52:55],a[152:155],v[16:19],a[52:55]
v_min_u32_dpp v53, v53, v53 row_shr:1 row_mask:0xf bank_mask:0xf
CV v70,v100,v101
MM a[44:47],a[152:155],v[8:11],a[44:47]
v_min_u32_dpp v53, v53, v53 row_shr:2 row_mask:0xf bank_mask:0xf
CV v71,v102,v103
EX v107,v107
MM a[44:47],a[156:159],v[12:15],a[44:47]
v_min_u32_dpp v53, v53, v53 row_shr:4 row_mask:0xf bank_mask:0xf
CV v76,v104,v105
MM a[36:39],a[156:159],v[4:7],a[36:39]
v_min_u32_dpp v53, v53, v53 row_shr:8 row_mask:0xf bank_mask:0xf
CV v77,v106,v107
EX v108,v108
MM a[36:39],a[152:155],v[0:3],a[36:39]
RD a[216:219],v226 offset:36864
v_min_u32_dpp v53, v53, v53 row_bcast:15 row_mask:0xa bank_mask:0xf
MM a[64:67],a[160:163],v[0:3],a[64:67]
RD a[220:223],v227 offset:36864
v_min_u32_dpp v53, v53, v53 row_bcast:31 row_mask:0xc bank_mask:0xf
EX v109,v109
MM a[64:67],a[164:167],v[4:7],a[64:67]
v_add_u32 v53, s76, v53
CV v78,v108,v109
MM a[72:75],a[164:167],v[12:15],a[72:75]
)ASM"
R"ASM(
s_mov_b32 exec_lo, 0
s_mov_b32 exec_hi, 0x80000000
ds_write_b64 v49, v[52:53] offset:0
s_mov_b64 exec, -1
EX v110,v110
MM a[72:75],a[160:163],v[8:11],a[72:75]
buffer_load_dwordx4 v230, s[8:11], s64 offen offset:2048 lds
MM a[80:83],a[160:163],v[16:19],a[80:83]
EX v111,v111
MM a[80:83],a[164:167],v[20:23],a[80:83]
EX v112,v112
MM a[88:91],a[164:167],v[28:31],a[88:91]
EX v113,v113
MM a[88:91],a[160:163],v[24:27],a[88:91]
buffer_load_dwordx4 v231, s[8:11], s64 offen offset:3072 lds
MM a[92:95],a[168:171],v[24:27],a[92:95]
s_add_u32 m0, s65, 65536
EX v114,v114
MM a[92:95],a[172:175],v[28:31],a[92:95]
RD a[224:227],v224 offset:40960
CV v79,v110,v111
MM a[84:87],a[172:175],v[20:23],a[84:87]
EX v115,v115
MM a[84:87],a[168:171],v[16:19],a[84:87]
EX v116,v116
MM a[76:79],a[168:171],v[8:11],a[76:79]
buffer_load_dwordx4 v235, s[12:15], s81 offen offset:0 lds
MM a[76:79],a[172:175],v[12:15],a[76:79]
EX v117,v117
MM a[68:71],a[172:175],v[4:7],a[68:71]
RD a[228:231],v225 offset:40960
CV v84,v112,v113
MM a[68:71],a[168:171],v[0:3],a[68:71]
EX v118,v118
MM a[96:99],a[176:179],v[0:3],a[96:99]
RD a[232:235],v226 offset:40960
CV v85,v114,v115
MM a[96:99],a[180:183],v[4:7],a[96:99]
EX v119,v119
MM a[104:107],a[180:183],v[12:15],a[104:107]
RD a[236:239],v227 offset:40960
CV v86,v116,v117
MM a[104:107],a[176:179],v[8:11],a[104:107]
EX v120,v120
MM a[112:115],a[176:179],v[16:19],a[112:115]
buffer_load_dwordx4 v235, s[12:15], s64 offen offset:1024 lds
MM a[112:115],a[180:183],v[20:23],a[112:115]
EX v121,v121
MM a[120:123],a[180:183],v[28:31],a[120:123]
EX v122,v122
MM a[120:123],a[176:179],v[24:27],a[120:123]
RD a[240:243],v224 offset:45056
CV v87,v118,v119
MM a[124:127],a[184:187],v[24:27],a[124:127]
EX v123,v123
MM a[124:127],a[188:191],v[28:31],a[124:127]
RD a[244:247],v225 offset:45056
CV v92,v120,v121
MM a[116:119],a[188:191],v[20:23],a[116:119]
EX v124,v124
MM a[116:119],a[184:187],v[16:19],a[116:119]
buffer_load_dwordx4 v235, s[12:15], s81 offen offset:2048 lds
MM a[108:111],a[184:187],v[8:11],a[108:111]
EX v125,v125
MM a[108:111],a[188:191],v[12:15],a[108:111]
RD a[248:251],v226 offset:45056
CV v93,v122,v123
MM a[100:103],a[188:191],v[4:7],a[100:103]
EX v126,v126
MM a[100:103],a[184:187],v[0:3],a[100:103]
EX v127,v127
v_smfmac_f32_16x16x64_bf16 v[236:239], v[144:147], v[64:71], v234
RD a[252:255],v227 offset:45056
v_smfmac_f32_16x16x64_bf16 v[236:239], v[148:151], v[72:79], v234
CV v94,v124,v125
v_smfmac_f32_16x16x64_bf16 v[236:239], v[152:155], v[80:87], v234
CV v95,v126,v127
s_nop 1
v_smfmac_f32_16x16x64_bf16 v[236:239], v[156:159], v[88:95], v234
s_waitcnt lgkmcnt(0)
buffer_load_dwordx4 v235, s[12:15], s64 offen offset:3072 lds
MM v[0:3],a[192:195],v[160:163],v[128:131]
s_add_u32 s64, s64, 16384
TR a[128:129],v232 offset:16384
s_add_u32 s81, s64, 896
MM v[0:3],a[196:199],v[176:179],v[0:3]
TR a[130:131],v232 offset:20480
TR a[132:133],v232 offset:24576
MM v[0:3],a[200:203],v[192:195],v[0:3]
TR a[134:135],v232 offset:28672
TR a[136:137],v233 offset:16384
MM v[0:3],a[204:207],v[208:211],v[0:3]
TR a[138:139],v233 offset:20480
v_add_f32 v247, v236, v237
MM v[4:7],a[220:223],v[208:211],v[128:131]
TR a[140:141],v233 offset:24576
v_cmp_nge_f32 vcc, s71, v247
MM v[4:7],a[216:219],v[192:195],v[4:7]
s_cbranch_vccnz LSITE5_%=
LBACK5_%=:
MM v[4:7],a[212:215],v[176:179],v[4:7]
s_mov_b32 m0, s67
TR a[142:143],v233 offset:28672
TR a[144:145],v232 offset:16896
MM v[4:7],a[208:211],v[160:163],v[4:7]
buffer_load_dwordx4 v252, s[36:39], s66 offen offset:0 lds
MM v[12:15],a[208:211],v[164:167],v[132:135]
s_add_u32 s66, s66, 55296
EX v0,v0
MM v[12:15],a[212:215],v[180:183],v[12:15]
s_min_u32 s66, s66, s85
EX v1,v1
MM v[12:15],a[216:219],v[196:199],v[12:15]
s_add_u32 s67, s67, 1024
TR a[146:147],v232 offset:20992
CV v0,v0,v1
MM v[12:15],a[220:223],v[212:215],v[12:15]
s_min_u32 s67, s67, s86
EX v2,v2
MM v[8:11],a[204:207],v[212:215],v[132:135]
EX v3,v3
MM v[8:11],a[200:203],v[196:199],v[8:11]
TR a[148:149],v232 offset:25088
CV v1,v2,v3
MM v[8:11],a[196:199],v[180:183],v[8:11]
EX v4,v4
MM v[8:11],a[192:195],v[164:167],v[8:11]
TR a[150:151],v232 offset:29184
TR a[152:153],v233 offset:16896
MM v[16:19],a[192:195],v[168:171],v[136:139]
EX v5,v5
MM v[16:19],a[196:199],v[184:187],v[16:19]
TR a[154:155],v233 offset:20992
CV v2,v4,v5
MM v[16:19],a[200:203],v[200:203],v[16:19]
EX v6,v6
MM v[16:19],a[204:207],v[216:219],v[16:19]
EX v7,v7
MM v[20:23],a[220:223],v[216:219],v[136:139]
TR a[156:157],v233 offset:25088
CV v3,v6,v7
MM v[20:23],a[216:219],v[200:203],v[20:23]
EX v8,v8
MM v[20:23],a[212:215],v[184:187],v[20:23]
TR a[158:159],v233 offset:29184
TR a[160:161],v232 offset:17408
MM v[20:23],a[208:211],v[168:171],v[20:23]
EX v9,v9
MM v[28:31],a[208:211],v[172:175],v[140:143]
TR a[162:163],v232 offset:21504
CV v8,v8,v9
MM v[28:31],a[212:215],v[188:191],v[28:31]
EX v10,v10
MM v[28:31],a[216:219],v[204:207],v[28:31]
EX v11,v11
MM v[28:31],a[220:223],v[220:223],v[28:31]
TR a[164:165],v232 offset:25600
CV v9,v10,v11
MM v[24:27],a[204:207],v[220:223],v[140:143]
EX v12,v12
MM v[24:27],a[200:203],v[204:207],v[24:27]
TR a[166:167],v232 offset:29696
TR a[168:169],v233 offset:17408
MM v[24:27],a[196:199],v[188:191],v[24:27]
EX v13,v13
MM v[24:27],a[192:195],v[172:175],v[24:27]
TR a[170:171],v233 offset:21504
CV v10,v12,v13
MM v[32:35],a[224:227],v[160:163],v[128:131]
EX v14,v14
MM v[32:35],a[228:231],v[176:179],v[32:35]
EX v15,v15
MM v[32:35],a[232:235],v[192:195],v[32:35]
TR a[172:173],v233 offset:25600
CV v11,v14,v15
MM v[32:35],a[236:239],v[208:211],v[32:35]
TR a[174:175],v233 offset:29696
TR a[176:177],v232 offset:17920
MM v[36:39],a[252:255],v[208:211],v[128:131]
EX v16,v16
MM v[36:39],a[248:251],v[192:195],v[36:39]
EX v17,v17
MM v[36:39],a[244:247],v[176:179],v[36:39]
TR a[178:179],v232 offset:22016
CV v16,v16,v17
MM v[36:39],a[240:243],v[160:163],v[36:39]
EX v18,v18
MM v[44:47],a[240:243],v[164:167],v[132:135]
v_add_u32 v112, s78, v248
v_mov_b32 v113, s79
MM v[44:47],a[244:247],v[180:183],v[44:47]
EX v19,v19
MM v[44:47],a[248:251],v[196:199],v[44:47]
TR a[180:181],v232 offset:26112
CV v17,v18,v19
MM v[44:47],a[252:255],v[212:215],v[44:47]
EX v20,v20
MM v[40:43],a[236:239],v[212:215],v[132:135]
TR a[182:183],v232 offset:30208
TR a[184:185],v233 offset:17920
MM v[40:43],a[232:235],v[196:199],v[40:43]
EX v21,v21
MM v[40:43],a[228:231],v[180:183],v[40:43]
TR a[186:187],v233 offset:22016
CV v18,v20,v21
MM v[40:43],a[224:227],v[164:167],v[40:43]
EX v22,v22
MM v[48:51],a[224:227],v[168:171],v[136:139]
TR a[188:189],v233 offset:26112
TR a[190:191],v233 offset:30208
MM v[48:51],a[228:231],v[184:187],v[48:51]
EX v23,v23
MM v[48:51],a[232:235],v[200:203],v[48:51]
CV v19,v22,v23
MM v[48:51],a[236:239],v[216:219],v[48:51]
EX v24,v24
MM v[52:55],a[252:255],v[216:219],v[136:139]
EX v25,v25
MM v[52:55],a[248:251],v[200:203],v[52:55]
EX v26,v26
MM v[52:55],a[244:247],v[184:187],v[52:55]
EX v27,v27
MM v[52:55],a[240:243],v[168:171],v[52:55]
EX v28,v28
MM v[60:63],a[240:243],v[172:175],v[140:143]
EX v29,v29
MM v[60:63],a[244:247],v[188:191],v[60:63]
EX v30,v30
MM v[60:63],a[248:251],v[204:207],v[60:63]
EX v31,v31
MM v[60:63],a[252:255],v[220:223],v[60:63]
EX v32,v32
MM v[56:59],a[236:239],v[220:223],v[140:143]
EX v33,v33
MM v[56:59],a[232:235],v[204:207],v[56:59]
EX v34,v34
MM v[56:59],a[228:231],v[188:191],v[56:59]
EX v35,v35
MM v[56:59],a[224:227],v[172:175],v[56:59]
s_waitcnt vmcnt(13) lgkmcnt(0)
s_barrier
MM a[0:3],a[128:131],v[64:67],a[0:3]
s_add_u32 m0, s65, 16384
RD v[104:107],v113 offset:0
RD v[108:111],v113 offset:16
MM a[0:3],a[132:135],v[68:71],a[0:3]
RD v[96:99],v112 offset:0
RD v[100:103],v112 offset:1024
MM a[8:11],a[132:135],v[76:79],a[8:11]
CV v24,v24,v25
CV v25,v26,v27
MM a[8:11],a[128:131],v[72:75],a[8:11]
RD a[192:195],v224 offset:49152
CV v26,v28,v29
MM a[16:19],a[128:131],v[80:83],a[16:19]
RD a[196:199],v225 offset:49152
CV v27,v30,v31
MM a[16:19],a[132:135],v[84:87],a[16:19]
buffer_load_dwordx4 v228, s[8:11], s64 offen offset:0 lds
MM a[24:27],a[132:135],v[92:95],a[24:27]
s_waitcnt lgkmcnt(4)
v_min3_u32 v114, v104, v105, v106
v_min3_u32 v115, v107, v108, v109
MM a[24:27],a[128:131],v[88:91],a[24:27]
v_min3_u32 v114, v114, v110, v111
v_min_u32 v114, v114, v115
MM a[28:31],a[136:139],v[88:91],a[28:31]
v_readfirstlane_b32 s76, v114
CV v4,v32,v33
MM a[28:31],a[140:143],v[92:95],a[28:31]
s_sub_u32 s92, s92, s76
RD a[200:203],v226 offset:49152
CV v5,v34,v35
MM a[20:23],a[140:143],v[84:87],a[20:23]
buffer_load_dwordx4 v229, s[8:11], s64 offen offset:1024 lds
MM a[20:23],a[136:139],v[80:83],a[20:23]
s_add_u32 s76, s76, 1
v_subrev_u32 v104, s76, v104
v_subrev_u32 v105, s76, v105
MM a[12:15],a[136:139],v[72:75],a[12:15]
v_subrev_u32 v106, s76, v106
v_subrev_u32 v107, s76, v107
MM a[12:15],a[140:143],v[76:79],a[12:15]
v_subrev_u32 v108, s76, v108
v_subrev_u32 v109, s76, v109
MM a[4:7],a[140:143],v[68:71],a[4:7]
v_subrev_u32 v110, s76, v110
v_subrev_u32 v111, s76, v111
MM a[4:7],a[136:139],v[64:67],a[4:7]
s_and_b32 s77, s92, 65535
EX v36,v36
MM a[32:35],a[144:147],v[64:67],a[32:35]
s_sub_u32 s52, 65535, s77
v_min3_u32 v114, v104, v105, v106
v_min3_u32 v115, v107, v108, v109
MM a[32:35],a[148:151],v[68:71],a[32:35]
v_min3_u32 v114, v114, v110, v111
v_min_u32 v114, v114, v115
EX v37,v37
MM a[40:43],a[148:151],v[76:79],a[40:43]
RD a[204:207],v227 offset:49152
v_readfirstlane_b32 s76, v114
MM a[40:43],a[144:147],v[72:75],a[40:43]
s_sub_u32 s92, s92, s76
EX v38,v38
MM a[48:51],a[144:147],v[80:83],a[48:51]
s_sub_u32 s92, s92, 1
EX v39,v39
MM a[48:51],a[148:151],v[84:87],a[48:51]
s_and_b32 s77, s92, 65535
EX v40,v40
MM a[56:59],a[148:151],v[92:95],a[56:59]
s_sub_u32 s53, 65535, s77
CV v6,v36,v37
CV v7,v38,v39
MM a[56:59],a[144:147],v[88:91],a[56:59]
s_sub_u32 s92, s92, 1
EX v41,v41
MM a[60:63],a[152:155],v[88:91],a[60:63]
s_waitcnt lgkmcnt(4)
v_sub_u32 v96, s92, v96
v_sub_u32 v97, s92, v97
MM a[60:63],a[156:159],v[92:95],a[60:63]
v_sub_u32 v98, s92, v98
LPG27_%=:
s_cbranch_execz LTR1_%=
v_sub_u32 v99, s92, v99
MM a[52:55],a[156:159],v[84:87],a[52:55]
v_sub_u32 v100, s92, v100
v_sub_u32 v101, s92, v101
EX v42,v42
MM a[52:55],a[152:155],v[80:83],a[52:55]
v_sub_u32 v102, s92, v102
v_sub_u32 v103, s92, v103
MM a[44:47],a[152:155],v[72:75],a[44:47]
v_min3_u32 v116, v96, v97, v98
v_min3_u32 v115, v99, v100, v101
EX v43,v43
MM a[44:47],a[156:159],v[76:79],a[44:47]
v_min3_u32 v116, v116, v102, v103
v_min_u32 v116, v116, v115
MM a[36:39],a[156:159],v[68:71],a[36:39]
RD a[208:211],v224 offset:53248
v_min_u32_dpp v116, v116, v116 row_shr:1 row_mask:0xf bank_mask:0xf
EX v44,v44
MM a[36:39],a[152:155],v[64:67],a[36:39]
v_min_u32_dpp v116, v116, v116 row_shr:2 row_mask:0xf bank_mask:0xf
CV v12,v40,v41
MM a[64:67],a[160:163],v[64:67],a[64:67]
RD a[212:215],v225 offset:53248
v_min_u32_dpp v116, v116, v116 row_shr:4 row_mask:0xf bank_mask:0xf
EX v45,v45
MM a[64:67],a[164:167],v[68:71],a[64:67]
v_min_u32_dpp v116, v116, v116 row_shr:8 row_mask:0xf bank_mask:0xf
CV v13,v42,v43
MM a[72:75],a[164:167],v[76:79],a[72:75]
RD a[216:219],v226 offset:53248
v_min_u32_dpp v116, v116, v116 row_bcast:15 row_mask:0xa bank_mask:0xf
EX v46,v46
MM a[72:75],a[160:163],v[72:75],a[72:75]
v_min_u32_dpp v116, v116, v116 row_bcast:31 row_mask:0xc bank_mask:0xf
CV v14,v44,v45
EX v47,v47
MM a[80:83],a[160:163],v[80:83],a[80:83]
v_readlane_b32 s76, v116, 63
v_lshl_add_u32 v113, s80, 1, v113
MM a[80:83],a[164:167],v[84:87],a[80:83]
buffer_load_dwordx4 v230, s[8:11], s64 offen offset:2048 lds
MM a[88:91],a[164:167],v[92:95],a[88:91]
s_add_u32 s76, s76, 1
v_subrev_u32 v96, s76, v96
v_subrev_u32 v97, s76, v97
EX v48,v48
MM a[88:91],a[160:163],v[88:91],a[88:91]
v_subrev_u32 v98, s76, v98
v_subrev_u32 v99, s76, v99
EX v49,v49
MM a[92:95],a[168:171],v[88:91],a[92:95]
v_subrev_u32 v100, s76, v100
v_subrev_u32 v101, s76, v101
MM a[92:95],a[172:175],v[92:95],a[92:95]
v_subrev_u32 v102, s76, v102
v_subrev_u32 v103, s76, v103
EX v50,v50
MM a[84:87],a[172:175],v[84:87],a[84:87]
v_min3_u32 v117, v96, v97, v98
v_min3_u32 v115, v99, v100, v101
MM a[84:87],a[168:171],v[80:83],a[84:87]
v_min3_u32 v117, v117, v102, v103
)ASM"
R"ASM(
v_min_u32 v117, v117, v115
EX v51,v51
MM a[76:79],a[168:171],v[72:75],a[76:79]
RD a[220:223],v227 offset:53248
v_min_u32_dpp v117, v117, v117 row_shr:1 row_mask:0xf bank_mask:0xf
MM a[76:79],a[172:175],v[76:79],a[76:79]
s_nop 0
v_min_u32_dpp v117, v117, v117 row_shr:2 row_mask:0xf bank_mask:0xf
CV v15,v46,v47
EX v52,v52
MM a[68:71],a[172:175],v[68:71],a[68:71]
RD a[224:227],v224 offset:57344
v_min_u32_dpp v117, v117, v117 row_shr:4 row_mask:0xf bank_mask:0xf
EX v53,v53
MM a[68:71],a[168:171],v[64:67],a[68:71]
v_min_u32_dpp v117, v117, v117 row_shr:8 row_mask:0xf bank_mask:0xf
CV v20,v48,v49
MM a[96:99],a[176:179],v[64:67],a[96:99]
RD a[228:231],v225 offset:57344
v_min_u32_dpp v117, v117, v117 row_bcast:15 row_mask:0xa bank_mask:0xf
EX v54,v54
MM a[96:99],a[180:183],v[68:71],a[96:99]
v_min_u32_dpp v117, v117, v117 row_bcast:31 row_mask:0xc bank_mask:0xf
CV v21,v50,v51
MM a[104:107],a[180:183],v[76:79],a[104:107]
RD a[232:235],v226 offset:57344
v_add_u32 v117, s76, v117
EX v55,v55
MM a[104:107],a[176:179],v[72:75],a[104:107]
s_mov_b32 exec_lo, 0
s_mov_b32 exec_hi, 0x80000000
ds_write_b64 v113, v[116:117] offset:32
s_mov_b64 exec, -1
MM a[112:115],a[176:179],v[80:83],a[112:115]
buffer_load_dwordx4 v231, s[8:11], s64 offen offset:3072 lds
MM a[112:115],a[180:183],v[84:87],a[112:115]
s_add_u32 m0, s65, 81920
EX v56,v56
MM a[120:123],a[180:183],v[92:95],a[120:123]
buffer_load_dwordx4 v235, s[12:15], s81 offen offset:0 lds
MM a[120:123],a[176:179],v[88:91],a[120:123]
EX v57,v57
MM a[124:127],a[184:187],v[88:91],a[124:127]
EX v58,v58
MM a[124:127],a[188:191],v[92:95],a[124:127]
RD a[236:239],v227 offset:57344
CV v22,v52,v53
MM a[116:119],a[188:191],v[84:87],a[116:119]
EX v59,v59
MM a[116:119],a[184:187],v[80:83],a[116:119]
buffer_load_dwordx4 v235, s[12:15], s64 offen offset:1024 lds
MM a[108:111],a[184:187],v[72:75],a[108:111]
EX v60,v60
CV v23,v54,v55
MM a[108:111],a[188:191],v[76:79],a[108:111]
EX v61,v61
MM a[100:103],a[188:191],v[68:71],a[100:103]
RD a[240:243],v224 offset:61440
CV v28,v56,v57
MM a[100:103],a[184:187],v[64:67],a[100:103]
EX v62,v62
v_smfmac_f32_16x16x64_bf16 v[236:239], v[144:147], v[0:7], v234
RD a[244:247],v225 offset:61440
RD a[248:251],v226 offset:61440
RD a[252:255],v227 offset:61440
EX v63,v63
v_smfmac_f32_16x16x64_bf16 v[236:239], v[148:151], v[8:15], v234
CV v29,v58,v59
v_smfmac_f32_16x16x64_bf16 v[236:239], v[152:155], v[16:23], v234
CV v30,v60,v61
CV v31,v62,v63
s_nop 1
v_smfmac_f32_16x16x64_bf16 v[236:239], v[156:159], v[24:31], v234
s_waitcnt lgkmcnt(0)
buffer_load_dwordx4 v235, s[12:15], s81 offen offset:2048 lds
buffer_load_dwordx4 v235, s[12:15], s64 offen offset:3072 lds
MM v[64:67],a[192:195],v[160:163],v[128:131]
s_add_u32 s64, s64, 16384
TR a[128:129],v232 offset:32768
s_add_u32 s81, s64, 896
MM v[64:67],a[196:199],v[176:179],v[64:67]
TR a[130:131],v232 offset:36864
TR a[132:133],v232 offset:40960
MM v[64:67],a[200:203],v[192:195],v[64:67]
TR a[134:135],v232 offset:45056
TR a[136:137],v233 offset:32768
MM v[64:67],a[204:207],v[208:211],v[64:67]
TR a[138:139],v233 offset:36864
v_add_f32 v246, v236, v237
MM v[68:71],a[220:223],v[208:211],v[128:131]
TR a[140:141],v233 offset:40960
v_cmp_nge_f32 vcc, s71, v246
MM v[68:71],a[216:219],v[192:195],v[68:71]
s_cbranch_vccnz LSITE6_%=
LBACK6_%=:
MM v[68:71],a[212:215],v[176:179],v[68:71]
s_mov_b32 m0, s67
TR a[142:143],v233 offset:45056
TR a[144:145],v232 offset:33280
MM v[68:71],a[208:211],v[160:163],v[68:71]
buffer_load_dwordx4 v252, s[36:39], s66 offen offset:0 lds
MM v[76:79],a[208:211],v[164:167],v[132:135]
s_add_u32 s66, s66, 55296
EX v64,v64
MM v[76:79],a[212:215],v[180:183],v[76:79]
s_min_u32 s66, s66, s85
EX v65,v65
MM v[76:79],a[216:219],v[196:199],v[76:79]
s_add_u32 s67, s67, 1024
TR a[146:147],v232 offset:37376
CV v64,v64,v65
MM v[76:79],a[220:223],v[212:215],v[76:79]
s_min_u32 s67, s67, s86
EX v66,v66
MM v[72:75],a[204:207],v[212:215],v[132:135]
EX v67,v67
MM v[72:75],a[200:203],v[196:199],v[72:75]
TR a[148:149],v232 offset:41472
CV v65,v66,v67
MM v[72:75],a[196:199],v[180:183],v[72:75]
EX v68,v68
MM v[72:75],a[192:195],v[164:167],v[72:75]
TR a[150:151],v232 offset:45568
TR a[152:153],v233 offset:33280
MM v[80:83],a[192:195],v[168:171],v[136:139]
EX v69,v69
MM v[80:83],a[196:199],v[184:187],v[80:83]
TR a[154:155],v233 offset:37376
CV v66,v68,v69
MM v[80:83],a[200:203],v[200:203],v[80:83]
EX v70,v70
MM v[80:83],a[204:207],v[216:219],v[80:83]
EX v71,v71
MM v[84:87],a[220:223],v[216:219],v[136:139]
TR a[156:157],v233 offset:41472
CV v67,v70,v71
MM v[84:87],a[216:219],v[200:203],v[84:87]
EX v72,v72
MM v[84:87],a[212:215],v[184:187],v[84:87]
TR a[158:159],v233 offset:45568
TR a[160:161],v232 offset:33792
MM v[84:87],a[208:211],v[168:171],v[84:87]
EX v73,v73
MM v[92:95],a[208:211],v[172:175],v[140:143]
TR a[162:163],v232 offset:37888
CV v72,v72,v73
MM v[92:95],a[212:215],v[188:191],v[92:95]
EX v74,v74
MM v[92:95],a[216:219],v[204:207],v[92:95]
EX v75,v75
MM v[92:95],a[220:223],v[220:223],v[92:95]
TR a[164:165],v232 offset:41984
CV v73,v74,v75
MM v[88:91],a[204:207],v[220:223],v[140:143]
EX v76,v76
MM v[88:91],a[200:203],v[204:207],v[88:91]
TR a[166:167],v232 offset:46080
TR a[168:169],v233 offset:33792
MM v[88:91],a[196:199],v[188:191],v[88:91]
EX v77,v77
MM v[88:91],a[192:195],v[172:175],v[88:91]
TR a[170:171],v233 offset:37888
CV v74,v76,v77
MM v[96:99],a[224:227],v[160:163],v[128:131]
EX v78,v78
MM v[96:99],a[228:231],v[176:179],v[96:99]
EX v79,v79
MM v[96:99],a[232:235],v[192:195],v[96:99]
TR a[172:173],v233 offset:41984
CV v75,v78,v79
MM v[96:99],a[236:239],v[208:211],v[96:99]
TR a[174:175],v233 offset:46080
TR a[176:177],v232 offset:34304
MM v[100:103],a[252:255],v[208:211],v[128:131]
EX v80,v80
MM v[100:103],a[248:251],v[192:195],v[100:103]
EX v81,v81
MM v[100:103],a[244:247],v[176:179],v[100:103]
TR a[178:179],v232 offset:38400
CV v80,v80,v81
MM v[100:103],a[240:243],v[160:163],v[100:103]
EX v82,v82
MM v[108:111],a[240:243],v[164:167],v[132:135]
v_add_u32 v48, s78, v248
v_mov_b32 v49, s79
MM v[108:111],a[244:247],v[180:183],v[108:111]
EX v83,v83
MM v[108:111],a[248:251],v[196:199],v[108:111]
TR a[180:181],v232 offset:42496
CV v81,v82,v83
MM v[108:111],a[252:255],v[212:215],v[108:111]
EX v84,v84
MM v[104:107],a[236:239],v[212:215],v[132:135]
TR a[182:183],v232 offset:46592
TR a[184:185],v233 offset:34304
MM v[104:107],a[232:235],v[196:199],v[104:107]
EX v85,v85
MM v[104:107],a[228:231],v[180:183],v[104:107]
TR a[186:187],v233 offset:38400
CV v82,v84,v85
MM v[104:107],a[224:227],v[164:167],v[104:107]
EX v86,v86
MM v[112:115],a[224:227],v[168:171],v[136:139]
TR a[188:189],v233 offset:42496
TR a[190:191],v233 offset:46592
MM v[112:115],a[228:231],v[184:187],v[112:115]
EX v87,v87
MM v[112:115],a[232:235],v[200:203],v[112:115]
CV v83,v86,v87
MM v[112:115],a[236:239],v[216:219],v[112:115]
EX v88,v88
MM v[116:119],a[252:255],v[216:219],v[136:139]
EX v89,v89
MM v[116:119],a[248:251],v[200:203],v[116:119]
EX v90,v90
MM v[116:119],a[244:247],v[184:187],v[116:119]
EX v91,v91
MM v[116:119],a[240:243],v[168:171],v[116:119]
EX v92,v92
MM v[124:127],a[240:243],v[172:175],v[140:143]
EX v93,v93
MM v[124:127],a[244:247],v[188:191],v[124:127]
EX v94,v94
MM v[124:127],a[248:251],v[204:207],v[124:127]
EX v95,v95
MM v[124:127],a[252:255],v[220:223],v[124:127]
EX v96,v96
MM v[120:123],a[236:239],v[220:223],v[140:143]
EX v97,v97
MM v[120:123],a[232:235],v[204:207],v[120:123]
EX v98,v98
MM v[120:123],a[228:231],v[188:191],v[120:123]
EX v99,v99
MM v[120:123],a[224:227],v[172:175],v[120:123]
s_waitcnt vmcnt(13) lgkmcnt(0)
s_barrier
MM a[0:3],a[128:131],v[0:3],a[0:3]
s_add_u32 m0, s65, 32768
RD v[40:43],v49 offset:32
RD v[44:47],v49 offset:48
MM a[0:3],a[132:135],v[4:7],a[0:3]
RD v[32:35],v48 offset:0
RD v[36:39],v48 offset:1024
MM a[8:11],a[132:135],v[12:15],a[8:11]
CV v88,v88,v89
CV v89,v90,v91
MM a[8:11],a[128:131],v[8:11],a[8:11]
RD a[192:195],v224 offset:0
CV v90,v92,v93
MM a[16:19],a[128:131],v[16:19],a[16:19]
RD a[196:199],v225 offset:0
CV v91,v94,v95
MM a[16:19],a[132:135],v[20:23],a[16:19]
buffer_load_dwordx4 v228, s[8:11], s64 offen offset:0 lds
MM a[24:27],a[132:135],v[28:31],a[24:27]
s_waitcnt lgkmcnt(4)
v_min3_u32 v50, v40, v41, v42
v_min3_u32 v51, v43, v44, v45
MM a[24:27],a[128:131],v[24:27],a[24:27]
v_min3_u32 v50, v50, v46, v47
v_min_u32 v50, v50, v51
MM a[28:31],a[136:139],v[24:27],a[28:31]
v_readfirstlane_b32 s76, v50
CV v68,v96,v97
MM a[28:31],a[140:143],v[28:31],a[28:31]
s_sub_u32 s92, s92, s76
RD a[200:203],v226 offset:0
CV v69,v98,v99
MM a[20:23],a[140:143],v[20:23],a[20:23]
buffer_load_dwordx4 v229, s[8:11], s64 offen offset:1024 lds
MM a[20:23],a[136:139],v[16:19],a[20:23]
s_add_u32 s76, s76, 1
v_subrev_u32 v40, s76, v40
v_subrev_u32 v41, s76, v41
MM a[12:15],a[136:139],v[8:11],a[12:15]
v_subrev_u32 v42, s76, v42
v_subrev_u32 v43, s76, v43
MM a[12:15],a[140:143],v[12:15],a[12:15]
v_subrev_u32 v44, s76, v44
v_subrev_u32 v45, s76, v45
MM a[4:7],a[140:143],v[4:7],a[4:7]
v_subrev_u32 v46, s76, v46
v_subrev_u32 v47, s76, v47
MM a[4:7],a[136:139],v[0:3],a[4:7]
s_and_b32 s77, s92, 65535
EX v100,v100
MM a[32:35],a[144:147],v[0:3],a[32:35]
s_sub_u32 s54, 65535, s77
v_min3_u32 v50, v40, v41, v42
v_min3_u32 v51, v43, v44, v45
MM a[32:35],a[148:151],v[4:7],a[32:35]
v_min3_u32 v50, v50, v46, v47
v_min_u32 v50, v50, v51
EX v101,v101
MM a[40:43],a[148:151],v[12:15],a[40:43]
RD a[204:207],v227 offset:0
v_readfirstlane_b32 s76, v50
MM a[40:43],a[144:147],v[8:11],a[40:43]
s_sub_u32 s92, s92, s76
EX v102,v102
MM a[48:51],a[144:147],v[16:19],a[48:51]
s_sub_u32 s92, s92, 1
EX v103,v103
MM a[48:51],a[148:151],v[20:23],a[48:51]
s_and_b32 s77, s92, 65535
EX v104,v104
MM a[56:59],a[148:151],v[28:31],a[56:59]
s_sub_u32 s55, 65535, s77
CV v70,v100,v101
CV v71,v102,v103
MM a[56:59],a[144:147],v[24:27],a[56:59]
s_sub_u32 s92, s92, 1
EX v105,v105
MM a[60:63],a[152:155],v[24:27],a[60:63]
s_waitcnt lgkmcnt(4)
v_sub_u32 v32, s92, v32
v_sub_u32 v33, s92, v33
MM a[60:63],a[156:159],v[28:31],a[60:63]
v_sub_u32 v34, s92, v34
v_sub_u32 v35, s92, v35
MM a[52:55],a[156:159],v[20:23],a[52:55]
v_sub_u32 v36, s92, v36
v_sub_u32 v37, s92, v37
EX v106,v106
MM a[52:55],a[152:155],v[16:19],a[52:55]
v_sub_u32 v38, s92, v38
v_sub_u32 v39, s92, v39
MM a[44:47],a[152:155],v[8:11],a[44:47]
v_min3_u32 v52, v32, v33, v34
v_min3_u32 v51, v35, v36, v37
EX v107,v107
MM a[44:47],a[156:159],v[12:15],a[44:47]
v_min3_u32 v52, v52, v38, v39
v_min_u32 v52, v52, v51
MM a[36:39],a[156:159],v[4:7],a[36:39]
RD a[208:211],v224 offset:4096
v_min_u32_dpp v52, v52, v52 row_shr:1 row_mask:0xf bank_mask:0xf
EX v108,v108
MM a[36:39],a[152:155],v[0:3],a[36:39]
v_min_u32_dpp v52, v52, v52 row_shr:2 row_mask:0xf bank_mask:0xf
CV v76,v104,v105
MM a[64:67],a[160:163],v[0:3],a[64:67]
RD a[212:215],v225 offset:4096
v_min_u32_dpp v52, v52, v52 row_shr:4 row_mask:0xf bank_mask:0xf
EX v109,v109
MM a[64:67],a[164:167],v[4:7],a[64:67]
v_min_u32_dpp v52, v52, v52 row_shr:8 row_mask:0xf bank_mask:0xf
CV v77,v106,v107
MM a[72:75],a[164:167],v[12:15],a[72:75]
RD a[216:219],v226 offset:4096
v_min_u32_dpp v52, v52, v52 row_bcast:15 row_mask:0xa bank_mask:0xf
EX v110,v110
MM a[72:75],a[160:163],v[8:11],a[72:75]
v_min_u32_dpp v52, v52, v52 row_bcast:31 row_mask:0xc bank_mask:0xf
CV v78,v108,v109
EX v111,v111
MM a[80:83],a[160:163],v[16:19],a[80:83]
v_readlane_b32 s76, v52, 63
v_lshl_add_u32 v49, s80, 1, v49
MM a[80:83],a[164:167],v[20:23],a[80:83]
buffer_load_dwordx4 v230, s[8:11], s64 offen offset:2048 lds
MM a[88:91],a[164:167],v[28:31],a[88:91]
s_add_u32 s76, s76, 1
v_subrev_u32 v32, s76, v32
v_subrev_u32 v33, s76, v33
EX v112,v112
MM a[88:91],a[160:163],v[24:27],a[88:91]
v_subrev_u32 v34, s76, v34
v_subrev_u32 v35, s76, v35
EX v113,v113
MM a[92:95],a[168:171],v[24:27],a[92:95]
v_subrev_u32 v36, s76, v36
v_subrev_u32 v37, s76, v37
MM a[92:95],a[172:175],v[28:31],a[92:95]
v_subrev_u32 v38, s76, v38
v_subrev_u32 v39, s76, v39
EX v114,v114
MM a[84:87],a[172:175],v[20:23],a[84:87]
v_min3_u32 v53, v32, v33, v34
v_min3_u32 v51, v35, v36, v37
MM a[84:87],a[168:171],v[16:19],a[84:87]
v_min3_u32 v53, v53, v38, v39
)ASM"
R"ASM(
v_min_u32 v53, v53, v51
EX v115,v115
MM a[76:79],a[168:171],v[8:11],a[76:79]
RD a[220:223],v227 offset:4096
v_min_u32_dpp v53, v53, v53 row_shr:1 row_mask:0xf bank_mask:0xf
MM a[76:79],a[172:175],v[12:15],a[76:79]
s_nop 0
v_min_u32_dpp v53, v53, v53 row_shr:2 row_mask:0xf bank_mask:0xf
CV v79,v110,v111
EX v116,v116
MM a[68:71],a[172:175],v[4:7],a[68:71]
RD a[224:227],v224 offset:8192
v_min_u32_dpp v53, v53, v53 row_shr:4 row_mask:0xf bank_mask:0xf
EX v117,v117
MM a[68:71],a[168:171],v[0:3],a[68:71]
v_min_u32_dpp v53, v53, v53 row_shr:8 row_mask:0xf bank_mask:0xf
CV v84,v112,v113
MM a[96:99],a[176:179],v[0:3],a[96:99]
RD a[228:231],v225 offset:8192
v_min_u32_dpp v53, v53, v53 row_bcast:15 row_mask:0xa bank_mask:0xf
EX v118,v118
MM a[96:99],a[180:183],v[4:7],a[96:99]
v_min_u32_dpp v53, v53, v53 row_bcast:31 row_mask:0xc bank_mask:0xf
CV v85,v114,v115
MM a[104:107],a[180:183],v[12:15],a[104:107]
RD a[232:235],v226 offset:8192
v_add_u32 v53, s76, v53
EX v119,v119
MM a[104:107],a[176:179],v[8:11],a[104:107]
s_mov_b32 exec_lo, 0
s_mov_b32 exec_hi, 0x80000000
ds_write_b64 v49, v[52:53] offset:0
s_mov_b64 exec, -1
MM a[112:115],a[176:179],v[16:19],a[112:115]
buffer_load_dwordx4 v231, s[8:11], s64 offen offset:3072 lds
MM a[112:115],a[180:183],v[20:23],a[112:115]
s_add_u32 m0, s65, 98304
EX v120,v120
MM a[120:123],a[180:183],v[28:31],a[120:123]
buffer_load_dwordx4 v235, s[12:15], s81 offen offset:0 lds
MM a[120:123],a[176:179],v[24:27],a[120:123]
EX v121,v121
MM a[124:127],a[184:187],v[24:27],a[124:127]
EX v122,v122
MM a[124:127],a[188:191],v[28:31],a[124:127]
RD a[236:239],v227 offset:8192
CV v86,v116,v117
MM a[116:119],a[188:191],v[20:23],a[116:119]
EX v123,v123
MM a[116:119],a[184:187],v[16:19],a[116:119]
buffer_load_dwordx4 v235, s[12:15], s64 offen offset:1024 lds
MM a[108:111],a[184:187],v[8:11],a[108:111]
EX v124,v124
CV v87,v118,v119
MM a[108:111],a[188:191],v[12:15],a[108:111]
EX v125,v125
MM a[100:103],a[188:191],v[4:7],a[100:103]
RD a[240:243],v224 offset:12288
CV v92,v120,v121
MM a[100:103],a[184:187],v[0:3],a[100:103]
EX v126,v126
v_smfmac_f32_16x16x64_bf16 v[236:239], v[144:147], v[64:71], v234
RD a[244:247],v225 offset:12288
RD a[248:251],v226 offset:12288
RD a[252:255],v227 offset:12288
EX v127,v127
v_smfmac_f32_16x16x64_bf16 v[236:239], v[148:151], v[72:79], v234
CV v93,v122,v123
v_smfmac_f32_16x16x64_bf16 v[236:239], v[152:155], v[80:87], v234
CV v94,v124,v125
CV v95,v126,v127
s_nop 1
v_smfmac_f32_16x16x64_bf16 v[236:239], v[156:159], v[88:95], v234
s_waitcnt lgkmcnt(0)
buffer_load_dwordx4 v235, s[12:15], s81 offen offset:2048 lds
buffer_load_dwordx4 v235, s[12:15], s64 offen offset:3072 lds
MM v[0:3],a[192:195],v[160:163],v[128:131]
s_add_u32 s64, s64, 16384
TR a[128:129],v232 offset:49152
s_add_u32 s81, s64, 896
MM v[0:3],a[196:199],v[176:179],v[0:3]
TR a[130:131],v232 offset:53248
TR a[132:133],v232 offset:57344
MM v[0:3],a[200:203],v[192:195],v[0:3]
TR a[134:135],v232 offset:61440
TR a[136:137],v233 offset:49152
MM v[0:3],a[204:207],v[208:211],v[0:3]
TR a[138:139],v233 offset:53248
v_add_f32 v247, v236, v237
MM v[4:7],a[220:223],v[208:211],v[128:131]
TR a[140:141],v233 offset:57344
v_cmp_nge_f32 vcc, s71, v247
MM v[4:7],a[216:219],v[192:195],v[4:7]
s_cbranch_vccnz LSITE7_%=
LBACK7_%=:
MM v[4:7],a[212:215],v[176:179],v[4:7]
s_mov_b32 m0, s67
TR a[142:143],v233 offset:61440
TR a[144:145],v232 offset:49664
MM v[4:7],a[208:211],v[160:163],v[4:7]
buffer_load_dwordx4 v252, s[36:39], s66 offen offset:0 lds
MM v[12:15],a[208:211],v[164:167],v[132:135]
s_add_u32 s66, s66, 55296
EX v0,v0
MM v[12:15],a[212:215],v[180:183],v[12:15]
s_min_u32 s66, s66, s85
EX v1,v1
MM v[12:15],a[216:219],v[196:199],v[12:15]
s_add_u32 s67, s67, 1024
TR a[146:147],v232 offset:53760
CV v0,v0,v1
MM v[12:15],a[220:223],v[212:215],v[12:15]
s_min_u32 s67, s67, s86
EX v2,v2
MM v[8:11],a[204:207],v[212:215],v[132:135]
EX v3,v3
MM v[8:11],a[200:203],v[196:199],v[8:11]
TR a[148:149],v232 offset:57856
CV v1,v2,v3
MM v[8:11],a[196:199],v[180:183],v[8:11]
EX v4,v4
MM v[8:11],a[192:195],v[164:167],v[8:11]
TR a[150:151],v232 offset:61952
TR a[152:153],v233 offset:49664
MM v[16:19],a[192:195],v[168:171],v[136:139]
EX v5,v5
MM v[16:19],a[196:199],v[184:187],v[16:19]
TR a[154:155],v233 offset:53760
CV v2,v4,v5
MM v[16:19],a[200:203],v[200:203],v[16:19]
EX v6,v6
MM v[16:19],a[204:207],v[216:219],v[16:19]
EX v7,v7
MM v[20:23],a[220:223],v[216:219],v[136:139]
TR a[156:157],v233 offset:57856
CV v3,v6,v7
MM v[20:23],a[216:219],v[200:203],v[20:23]
EX v8,v8
MM v[20:23],a[212:215],v[184:187],v[20:23]
TR a[158:159],v233 offset:61952
TR a[160:161],v232 offset:50176
MM v[20:23],a[208:211],v[168:171],v[20:23]
EX v9,v9
MM v[28:31],a[208:211],v[172:175],v[140:143]
TR a[162:163],v232 offset:54272
CV v8,v8,v9
MM v[28:31],a[212:215],v[188:191],v[28:31]
EX v10,v10
MM v[28:31],a[216:219],v[204:207],v[28:31]
EX v11,v11
MM v[28:31],a[220:223],v[220:223],v[28:31]
TR a[164:165],v232 offset:58368
CV v9,v10,v11
MM v[24:27],a[204:207],v[220:223],v[140:143]
EX v12,v12
MM v[24:27],a[200:203],v[204:207],v[24:27]
TR a[166:167],v232 offset:62464
TR a[168:169],v233 offset:50176
MM v[24:27],a[196:199],v[188:191],v[24:27]
LPG28_%=:
s_cbranch_execz LTR1_%=
EX v13,v13
MM v[24:27],a[192:195],v[172:175],v[24:27]
TR a[170:171],v233 offset:54272
CV v10,v12,v13
MM v[32:35],a[224:227],v[160:163],v[128:131]
EX v14,v14
MM v[32:35],a[228:231],v[176:179],v[32:35]
EX v15,v15
MM v[32:35],a[232:235],v[192:195],v[32:35]
TR a[172:173],v233 offset:58368
CV v11,v14,v15
MM v[32:35],a[236:239],v[208:211],v[32:35]
TR a[174:175],v233 offset:62464
TR a[176:177],v232 offset:50688
MM v[36:39],a[252:255],v[208:211],v[128:131]
EX v16,v16
MM v[36:39],a[248:251],v[192:195],v[36:39]
EX v17,v17
MM v[36:39],a[244:247],v[176:179],v[36:39]
TR a[178:179],v232 offset:54784
CV v16,v16,v17
MM v[36:39],a[240:243],v[160:163],v[36:39]
EX v18,v18
MM v[44:47],a[240:243],v[164:167],v[132:135]
v_add_u32 v112, s78, v248
v_mov_b32 v113, s79
MM v[44:47],a[244:247],v[180:183],v[44:47]
EX v19,v19
MM v[44:47],a[248:251],v[196:199],v[44:47]
TR a[180:181],v232 offset:58880
CV v17,v18,v19
MM v[44:47],a[252:255],v[212:215],v[44:47]
EX v20,v20
MM v[40:43],a[236:239],v[212:215],v[132:135]
TR a[182:183],v232 offset:62976
TR a[184:185],v233 offset:50688
MM v[40:43],a[232:235],v[196:199],v[40:43]
EX v21,v21
MM v[40:43],a[228:231],v[180:183],v[40:43]
TR a[186:187],v233 offset:54784
CV v18,v20,v21
MM v[40:43],a[224:227],v[164:167],v[40:43]
EX v22,v22
MM v[48:51],a[224:227],v[168:171],v[136:139]
TR a[188:189],v233 offset:58880
TR a[190:191],v233 offset:62976
MM v[48:51],a[228:231],v[184:187],v[48:51]
EX v23,v23
MM v[48:51],a[232:235],v[200:203],v[48:51]
CV v19,v22,v23
MM v[48:51],a[236:239],v[216:219],v[48:51]
EX v24,v24
MM v[52:55],a[252:255],v[216:219],v[136:139]
EX v25,v25
MM v[52:55],a[248:251],v[200:203],v[52:55]
EX v26,v26
MM v[52:55],a[244:247],v[184:187],v[52:55]
EX v27,v27
MM v[52:55],a[240:243],v[168:171],v[52:55]
EX v28,v28
MM v[60:63],a[240:243],v[172:175],v[140:143]
EX v29,v29
MM v[60:63],a[244:247],v[188:191],v[60:63]
EX v30,v30
MM v[60:63],a[248:251],v[204:207],v[60:63]
EX v31,v31
MM v[60:63],a[252:255],v[220:223],v[60:63]
EX v32,v32
MM v[56:59],a[236:239],v[220:223],v[140:143]
EX v33,v33
MM v[56:59],a[232:235],v[204:207],v[56:59]
EX v34,v34
MM v[56:59],a[228:231],v[188:191],v[56:59]
EX v35,v35
MM v[56:59],a[224:227],v[172:175],v[56:59]
s_waitcnt vmcnt(13) lgkmcnt(0)
s_barrier
MM a[0:3],a[128:131],v[64:67],a[0:3]
s_add_u32 m0, s65, 49152
RD v[104:107],v113 offset:0
RD v[108:111],v113 offset:16
MM a[0:3],a[132:135],v[68:71],a[0:3]
RD v[96:99],v112 offset:0
RD v[100:103],v112 offset:1024
MM a[8:11],a[132:135],v[76:79],a[8:11]
CV v24,v24,v25
CV v25,v26,v27
MM a[8:11],a[128:131],v[72:75],a[8:11]
RD a[192:195],v224 offset:16384
CV v26,v28,v29
MM a[16:19],a[128:131],v[80:83],a[16:19]
RD a[196:199],v225 offset:16384
CV v27,v30,v31
MM a[16:19],a[132:135],v[84:87],a[16:19]
buffer_load_dwordx4 v228, s[8:11], s64 offen offset:0 lds
MM a[24:27],a[132:135],v[92:95],a[24:27]
s_waitcnt lgkmcnt(4)
v_min3_u32 v114, v104, v105, v106
v_min3_u32 v115, v107, v108, v109
MM a[24:27],a[128:131],v[88:91],a[24:27]
v_min3_u32 v114, v114, v110, v111
v_min_u32 v114, v114, v115
MM a[28:31],a[136:139],v[88:91],a[28:31]
v_readfirstlane_b32 s76, v114
CV v4,v32,v33
MM a[28:31],a[140:143],v[92:95],a[28:31]
s_sub_u32 s92, s92, s76
RD a[200:203],v226 offset:16384
CV v5,v34,v35
MM a[20:23],a[140:143],v[84:87],a[20:23]
buffer_load_dwordx4 v229, s[8:11], s64 offen offset:1024 lds
MM a[20:23],a[136:139],v[80:83],a[20:23]
s_add_u32 s76, s76, 1
v_subrev_u32 v104, s76, v104
v_subrev_u32 v105, s76, v105
MM a[12:15],a[136:139],v[72:75],a[12:15]
v_subrev_u32 v106, s76, v106
v_subrev_u32 v107, s76, v107
MM a[12:15],a[140:143],v[76:79],a[12:15]
v_subrev_u32 v108, s76, v108
v_subrev_u32 v109, s76, v109
MM a[4:7],a[140:143],v[68:71],a[4:7]
v_subrev_u32 v110, s76, v110
v_subrev_u32 v111, s76, v111
MM a[4:7],a[136:139],v[64:67],a[4:7]
s_and_b32 s77, s92, 65535
EX v36,v36
MM a[32:35],a[144:147],v[64:67],a[32:35]
s_sub_u32 s56, 65535, s77
v_min3_u32 v114, v104, v105, v106
v_min3_u32 v115, v107, v108, v109
MM a[32:35],a[148:151],v[68:71],a[32:35]
v_min3_u32 v114, v114, v110, v111
v_min_u32 v114, v114, v115
EX v37,v37
MM a[40:43],a[148:151],v[76:79],a[40:43]
RD a[204:207],v227 offset:16384
v_readfirstlane_b32 s76, v114
MM a[40:43],a[144:147],v[72:75],a[40:43]
s_sub_u32 s92, s92, s76
EX v38,v38
MM a[48:51],a[144:147],v[80:83],a[48:51]
s_sub_u32 s92, s92, 1
EX v39,v39
MM a[48:51],a[148:151],v[84:87],a[48:51]
s_and_b32 s77, s92, 65535
EX v40,v40
MM a[56:59],a[148:151],v[92:95],a[56:59]
s_sub_u32 s57, 65535, s77
CV v6,v36,v37
CV v7,v38,v39
MM a[56:59],a[144:147],v[88:91],a[56:59]
s_sub_u32 s92, s92, 1
EX v41,v41
MM a[60:63],a[152:155],v[88:91],a[60:63]
s_waitcnt lgkmcnt(4)
v_sub_u32 v96, s92, v96
v_sub_u32 v97, s92, v97
MM a[60:63],a[156:159],v[92:95],a[60:63]
v_sub_u32 v98, s92, v98
v_sub_u32 v99, s92, v99
MM a[52:55],a[156:159],v[84:87],a[52:55]
v_sub_u32 v100, s92, v100
v_sub_u32 v101, s92, v101
EX v42,v42
MM a[52:55],a[152:155],v[80:83],a[52:55]
v_sub_u32 v102, s92, v102
v_sub_u32 v103, s92, v103
MM a[44:47],a[152:155],v[72:75],a[44:47]
v_min3_u32 v116, v96, v97, v98
v_min3_u32 v115, v99, v100, v101
EX v43,v43
MM a[44:47],a[156:159],v[76:79],a[44:47]
v_min3_u32 v116, v116, v102, v103
v_min_u32 v116, v116, v115
MM a[36:39],a[156:159],v[68:71],a[36:39]
RD a[208:211],v224 offset:20480
v_min_u32_dpp v116, v116, v116 row_shr:1 row_mask:0xf bank_mask:0xf
EX v44,v44
MM a[36:39],a[152:155],v[64:67],a[36:39]
v_min_u32_dpp v116, v116, v116 row_shr:2 row_mask:0xf bank_mask:0xf
CV v12,v40,v41
MM a[64:67],a[160:163],v[64:67],a[64:67]
RD a[212:215],v225 offset:20480
v_min_u32_dpp v116, v116, v116 row_shr:4 row_mask:0xf bank_mask:0xf
EX v45,v45
MM a[64:67],a[164:167],v[68:71],a[64:67]
v_min_u32_dpp v116, v116, v116 row_shr:8 row_mask:0xf bank_mask:0xf
CV v13,v42,v43
MM a[72:75],a[164:167],v[76:79],a[72:75]
RD a[216:219],v226 offset:20480
v_min_u32_dpp v116, v116, v116 row_bcast:15 row_mask:0xa bank_mask:0xf
EX v46,v46
MM a[72:75],a[160:163],v[72:75],a[72:75]
v_min_u32_dpp v116, v116, v116 row_bcast:31 row_mask:0xc bank_mask:0xf
CV v14,v44,v45
EX v47,v47
MM a[80:83],a[160:163],v[80:83],a[80:83]
v_readlane_b32 s76, v116, 63
v_lshl_add_u32 v113, s80, 1, v113
MM a[80:83],a[164:167],v[84:87],a[80:83]
buffer_load_dwordx4 v230, s[8:11], s64 offen offset:2048 lds
MM a[88:91],a[164:167],v[92:95],a[88:91]
s_add_u32 s76, s76, 1
v_subrev_u32 v96, s76, v96
v_subrev_u32 v97, s76, v97
EX v48,v48
MM a[88:91],a[160:163],v[88:91],a[88:91]
v_subrev_u32 v98, s76, v98
v_subrev_u32 v99, s76, v99
EX v49,v49
MM a[92:95],a[168:171],v[88:91],a[92:95]
v_subrev_u32 v100, s76, v100
v_subrev_u32 v101, s76, v101
MM a[92:95],a[172:175],v[92:95],a[92:95]
v_subrev_u32 v102, s76, v102
v_subrev_u32 v103, s76, v103
EX v50,v50
MM a[84:87],a[172:175],v[84:87],a[84:87]
v_min3_u32 v117, v96, v97, v98
v_min3_u32 v115, v99, v100, v101
MM a[84:87],a[168:171],v[80:83],a[84:87]
v_min3_u32 v117, v117, v102, v103
)ASM"
R"ASM(
v_min_u32 v117, v117, v115
EX v51,v51
MM a[76:79],a[168:171],v[72:75],a[76:79]
RD a[220:223],v227 offset:20480
v_min_u32_dpp v117, v117, v117 row_shr:1 row_mask:0xf bank_mask:0xf
MM a[76:79],a[172:175],v[76:79],a[76:79]
s_nop 0
v_min_u32_dpp v117, v117, v117 row_shr:2 row_mask:0xf bank_mask:0xf
CV v15,v46,v47
EX v52,v52
MM a[68:71],a[172:175],v[68:71],a[68:71]
RD a[224:227],v224 offset:24576
v_min_u32_dpp v117, v117, v117 row_shr:4 row_mask:0xf bank_mask:0xf
EX v53,v53
MM a[68:71],a[168:171],v[64:67],a[68:71]
v_min_u32_dpp v117, v117, v117 row_shr:8 row_mask:0xf bank_mask:0xf
CV v20,v48,v49
MM a[96:99],a[176:179],v[64:67],a[96:99]
RD a[228:231],v225 offset:24576
v_min_u32_dpp v117, v117, v117 row_bcast:15 row_mask:0xa bank_mask:0xf
EX v54,v54
MM a[96:99],a[180:183],v[68:71],a[96:99]
v_min_u32_dpp v117, v117, v117 row_bcast:31 row_mask:0xc bank_mask:0xf
CV v21,v50,v51
MM a[104:107],a[180:183],v[76:79],a[104:107]
RD a[232:235],v226 offset:24576
v_add_u32 v117, s76, v117
EX v55,v55
MM a[104:107],a[176:179],v[72:75],a[104:107]
s_mov_b32 exec_lo, 0
s_mov_b32 exec_hi, 0x80000000
ds_write_b64 v113, v[116:117] offset:32
s_mov_b64 exec, -1
MM a[112:115],a[176:179],v[80:83],a[112:115]
buffer_load_dwordx4 v231, s[8:11], s64 offen offset:3072 lds
MM a[112:115],a[180:183],v[84:87],a[112:115]
s_add_u32 m0, s65, 114688
EX v56,v56
MM a[120:123],a[180:183],v[92:95],a[120:123]
buffer_load_dwordx4 v235, s[12:15], s81 offen offset:0 lds
MM a[120:123],a[176:179],v[88:91],a[120:123]
EX v57,v57
MM a[124:127],a[184:187],v[88:91],a[124:127]
EX v58,v58
MM a[124:127],a[188:191],v[92:95],a[124:127]
RD a[236:239],v227 offset:24576
CV v22,v52,v53
MM a[116:119],a[188:191],v[84:87],a[116:119]
EX v59,v59
MM a[116:119],a[184:187],v[80:83],a[116:119]
buffer_load_dwordx4 v235, s[12:15], s64 offen offset:1024 lds
MM a[108:111],a[184:187],v[72:75],a[108:111]
EX v60,v60
CV v23,v54,v55
MM a[108:111],a[188:191],v[76:79],a[108:111]
EX v61,v61
MM a[100:103],a[188:191],v[68:71],a[100:103]
RD a[240:243],v224 offset:28672
CV v28,v56,v57
MM a[100:103],a[184:187],v[64:67],a[100:103]
EX v62,v62
v_smfmac_f32_16x16x64_bf16 v[236:239], v[144:147], v[0:7], v234
RD a[244:247],v225 offset:28672
RD a[248:251],v226 offset:28672
RD a[252:255],v227 offset:28672
EX v63,v63
v_smfmac_f32_16x16x64_bf16 v[236:239], v[148:151], v[8:15], v234
CV v29,v58,v59
v_smfmac_f32_16x16x64_bf16 v[236:239], v[152:155], v[16:23], v234
CV v30,v60,v61
CV v31,v62,v63
s_nop 1
v_smfmac_f32_16x16x64_bf16 v[236:239], v[156:159], v[24:31], v234
s_waitcnt lgkmcnt(0)
buffer_load_dwordx4 v235, s[12:15], s81 offen offset:2048 lds
buffer_load_dwordx4 v235, s[12:15], s64 offen offset:3072 lds
s_waitcnt lgkmcnt(0)
s_barrier
v_mov_b32 v113, s79
RD v[104:107],v113 offset:32
RD v[108:111],v113 offset:48
s_waitcnt lgkmcnt(0)
v_min3_u32 v114, v104, v105, v106
v_min3_u32 v115, v107, v108, v109
v_min3_u32 v114, v114, v110, v111
v_min_u32 v114, v114, v115
s_nop 0
v_readfirstlane_b32 s76, v114
s_nop 0
s_sub_u32 s92, s92, s76
s_add_u32 s76, s76, 1
v_subrev_u32 v104, s76, v104
v_subrev_u32 v105, s76, v105
v_subrev_u32 v106, s76, v106
v_subrev_u32 v107, s76, v107
v_subrev_u32 v108, s76, v108
v_subrev_u32 v109, s76, v109
v_subrev_u32 v110, s76, v110
v_subrev_u32 v111, s76, v111
s_and_b32 s77, s92, 65535
s_sub_u32 s58, 65535, s77
v_min3_u32 v114, v104, v105, v106
v_min3_u32 v115, v107, v108, v109
v_min3_u32 v114, v114, v110, v111
v_min_u32 v114, v114, v115
s_nop 0
v_readfirstlane_b32 s76, v114
s_nop 0
s_sub_u32 s92, s92, s76
s_sub_u32 s92, s92, 1
s_and_b32 s77, s92, 65535
s_sub_u32 s59, 65535, s77
s_sub_u32 s92, s92, 1
s_lshl_b32 s90, %[swave], 8
s_add_u32 s90, s90, %[sb]
s_cmp_lg_u32 s90, 0
s_cbranch_scc1 LIX_%=
v_writelane_b32 v112, s52, 0
v_writelane_b32 v112, s53, 1
v_writelane_b32 v112, s54, 2
v_writelane_b32 v112, s55, 3
v_writelane_b32 v112, s56, 4
v_writelane_b32 v112, s57, 5
v_writelane_b32 v112, s58, 6
v_writelane_b32 v112, s59, 7
v_mov_b32 v113, s69
v_lshrrev_b32 v116, 1, v248
s_mov_b32 s90, 86016
s_mov_b64 exec, 0xff
buffer_store_dwordx2 v[112:113], v116, s[20:23], s90 offen offset:0 sc1
s_mov_b64 exec, -1
s_nop 1
LIX_%=:
s_mov_b32 s70, 1
s_branch LLA_%=
.fill 443, 4, 0xbf800000
LPG29_%=:
s_cbranch_execz LTR1_%=
s_nop 0
LRG1o_%=:
s_mov_b32 s70, 30001
LRG1ol_%=:
s_sleep 2
buffer_load_dwordx4 v[200:203], v208, s[20:23], s93 offen offset:0 sc1
buffer_load_dwordx4 v[204:207], v208, s[20:23], s93 offen offset:256 sc1
s_waitcnt vmcnt(0)
v_xor_b32 v201, s69, v201
v_xor_b32 v203, s69, v203
v_xor_b32 v205, s69, v205
v_xor_b32 v207, s69, v207
v_or3_b32 v209, v201, v203, v205
v_or_b32 v209, v209, v207
v_cmp_ne_u32 vcc, 0, v209
s_cbranch_vccz LRG1ob_%=
s_sub_u32 s70, s70, 1
s_cbranch_scc0 LRG1ol_%=
s_branch LRG1ob_%=
LRFR_%=:
s_mov_b32 s91, 30001
LRFRl_%=:
s_sleep 2
buffer_load_dwordx4 v[48:51], v248, s[20:23], s90 offen offset:0 sc1
s_add_u32 s78, s90, 8192
buffer_load_dwordx4 v[56:59], v248, s[20:23], s78 offen offset:0 sc1
s_waitcnt vmcnt(0)
v_xor_b32 v48, s69, v48
v_xor_b32 v49, s69, v49
v_xor_b32 v50, s69, v50
v_xor_b32 v51, s69, v51
v_xor_b32 v56, s69, v56
v_xor_b32 v57, s69, v57
v_xor_b32 v58, s69, v58
v_xor_b32 v59, s69, v59
v_or3_b32 v52, v48, v49, v50
v_or3_b32 v52, v52, v51, v56
v_or3_b32 v52, v52, v57, v58
v_or_b32 v52, v52, v59
v_cmp_ne_u32 vcc, 0, v52
s_cbranch_vccz LRFRb_%=
s_sub_u32 s91, s91, 1
s_cbranch_scc0 LRFRl_%=
s_branch LRFRb_%=
LRIX_%=:
s_mov_b32 s91, 30001
LRIXl_%=:
s_sleep 2
buffer_load_dwordx2 v[48:49], v52, s[20:23], s90 offen offset:0 sc1
s_waitcnt vmcnt(0)
v_cmp_ne_u32 vcc, s69, v49
s_cbranch_vccz LRIXb_%=
s_sub_u32 s91, s91, 1
s_cbranch_scc0 LRIXl_%=
s_branch LRIXb_%=
LRFG_%=:
s_mov_b32 s91, 30001
LRFGl_%=:
s_sleep 2
buffer_load_dword v252, v253, s[20:23], s76 offen offset:0 sc1
s_waitcnt vmcnt(0)
v_cmp_ne_u32 vcc, s69, v252
s_cbranch_vccz LRFGb_%=
s_sub_u32 s91, s91, 1
s_cbranch_scc0 LRFGl_%=
s_branch LRFGb_%=
LRFH_%=:
s_mov_b32 s91, 30001
LRFHl_%=:
s_sleep 2
buffer_load_dword v252, v253, s[20:23], s76 offen offset:0 sc1
s_waitcnt vmcnt(0)
v_cmp_ne_u32 vcc, s69, v252
s_cbranch_vccz LRFHb_%=
s_sub_u32 s91, s91, 1
s_cbranch_scc0 LRFHl_%=
s_branch LRFHb_%=
LRPO0_%=:
s_mov_b32 s97, 30001
LRPO0l_%=:
s_sleep 2
buffer_load_dword v242, v243, s[20:23], s96 offen offset:0 sc1
s_waitcnt vmcnt(0)
v_cmp_ne_u32 vcc, s69, v242
s_cbranch_vccz LRPO0b_%=
s_sub_u32 s97, s97, 1
s_cbranch_scc0 LRPO0l_%=
s_branch LRPO0b_%=
LRPO1_%=:
s_mov_b32 s97, 30001
LRPO1l_%=:
s_sleep 2
buffer_load_dword v242, v243, s[20:23], s96 offen offset:16 sc1
s_waitcnt vmcnt(0)
v_cmp_ne_u32 vcc, s69, v242
s_cbranch_vccz LRPO1b_%=
s_sub_u32 s97, s97, 1
s_cbranch_scc0 LRPO1l_%=
s_branch LRPO1b_%=
LRYF_%=:
s_mov_b32 s91, 30001
LRYFl_%=:
s_sleep 2
buffer_load_dword v132, v135, s[20:23], s78 offen offset:0 sc1
buffer_load_dword v133, v135, s[20:23], s78 offen offset:960 sc1
buffer_load_dword v134, v135, s[20:23], s78 offen offset:1920 sc1
s_waitcnt vmcnt(0)
v_xor_b32 v132, s69, v132
v_xor_b32 v133, s69, v133
v_xor_b32 v134, s69, v134
v_or3_b32 v139, v132, v133, v134
v_cmp_ne_u32 vcc, 0, v139
s_cbranch_vccz LRYFb_%=
s_sub_u32 s91, s91, 1
s_cbranch_scc0 LRYFl_%=
s_branch LRYFb_%=
LQA_%=:
s_bitcmp0_b32 s17, 1
s_cbranch_scc1 LQG1_%=
AR v108,a35
AR v109,a39
AR v110,a43
AR v111,a47
v_cmp_eq_u32 s[24:25], -1, v108
v_cmp_eq_u32 s[26:27], -1, v109
v_cmp_eq_u32 s[28:29], -1, v110
v_cmp_eq_u32 s[46:47], -1, v111
s_nop 0
s_or_b64 s[30:31], s[24:25], s[26:27]
s_or_b64 s[30:31], s[30:31], s[28:29]
s_or_b64 s[30:31], s[30:31], s[46:47]
s_cselect_b32 s76, 256, 2
s_or_b32 s94, s94, s76
s_bitcmp1_b32 s94, 1
s_cbranch_scc1 LQG1_%=
s_and_b64 exec, s[46:47], s[10:11]
s_cbranch_scc0 LQK13_%=
s_add_u32 s66, s44, 6144
buffer_load_dwordx4 a[44:47], v250, s[20:23], s66 offen offset:0 sc1
LQK13_%=:
s_and_b64 exec, s[28:29], s[8:9]
s_cbranch_scc0 LQK12_%=
s_add_u32 s66, s16, 6144
buffer_load_dwordx4 a[40:43], v250, s[20:23], s66 offen offset:0 sc1
LQK12_%=:
s_and_b64 exec, s[26:27], s[10:11]
s_cbranch_scc0 LQK11_%=
s_add_u32 s66, s44, 4096
buffer_load_dwordx4 a[36:39], v250, s[20:23], s66 offen offset:0 sc1
LQK11_%=:
s_and_b64 exec, s[24:25], s[8:9]
s_cbranch_scc0 LQK10_%=
s_add_u32 s66, s16, 4096
buffer_load_dwordx4 a[32:35], v250, s[20:23], s66 offen offset:0 sc1
LQK10_%=:
s_mov_b64 exec, -1
LQG1_%=:
s_bitcmp0_b32 s17, 0
s_cbranch_scc1 LQG0_%=
AR v108,a3
AR v109,a7
AR v110,a11
AR v111,a15
v_cmp_eq_u32 s[24:25], -1, v108
v_cmp_eq_u32 s[26:27], -1, v109
v_cmp_eq_u32 s[28:29], -1, v110
v_cmp_eq_u32 s[46:47], -1, v111
s_nop 0
s_or_b64 s[30:31], s[24:25], s[26:27]
s_or_b64 s[30:31], s[30:31], s[28:29]
s_or_b64 s[30:31], s[30:31], s[46:47]
s_cselect_b32 s76, 256, 1
s_or_b32 s94, s94, s76
s_bitcmp1_b32 s94, 0
s_cbranch_scc1 LQG0_%=
s_and_b64 exec, s[46:47], s[38:39]
s_cbranch_scc0 LQK03_%=
s_add_u32 s66, s44, 2048
buffer_load_dwordx4 a[12:15], v250, s[20:23], s66 offen offset:0 sc1
LQK03_%=:
s_and_b64 exec, s[28:29], s[36:37]
s_cbranch_scc0 LQK02_%=
s_add_u32 s66, s16, 2048
buffer_load_dwordx4 a[8:11], v250, s[20:23], s66 offen offset:0 sc1
LQK02_%=:
s_and_b64 exec, s[26:27], s[38:39]
s_cbranch_scc0 LQK01_%=
s_add_u32 s66, s44, 0
buffer_load_dwordx4 a[4:7], v250, s[20:23], s66 offen offset:0 sc1
LQK01_%=:
s_and_b64 exec, s[24:25], s[36:37]
s_cbranch_scc0 LQK00_%=
s_add_u32 s66, s16, 0
buffer_load_dwordx4 a[0:3], v250, s[20:23], s66 offen offset:0 sc1
LQK00_%=:
s_mov_b64 exec, -1
LQG0_%=:
s_branch LQB_%=
LQC_%=:
s_bitcmp0_b32 s94, 0
s_cbranch_scc1 LQN0_%=
MM v[72:75],v[64:67],a[0:3],0
MM v[72:75],v[64:67],a[4:7],v[72:75]
MM v[72:75],v[68:71],a[8:11],v[72:75]
MM v[72:75],v[68:71],a[12:15],v[72:75]
s_bitset0_b32 s17, 0
s_nop 6
CV v76,v72,v73
CV v77,v74,v75
ds_bpermute_b32 v76, v232, v76
ds_bpermute_b32 v77, v232, v77
v_lshlrev_b32 v10, 16, v0
v_and_b32 v11, 0xffff0000, v0
v_lshlrev_b32 v12, 16, v1
v_and_b32 v13, 0xffff0000, v1
v_lshlrev_b32 v14, 16, v32
v_and_b32 v15, 0xffff0000, v32
v_lshlrev_b32 v42, 16, v33
v_and_b32 v43, 0xffff0000, v33
s_add_u32 s80, s18, 0
s_waitcnt lgkmcnt(0)
v_lshlrev_b32 v72, 16, v76
v_and_b32 v73, 0xffff0000, v76
v_lshlrev_b32 v74, 16, v77
v_and_b32 v75, 0xffff0000, v77
v_mul_f32 v72, v72, v10
v_mul_f32 v73, v73, v11
v_mul_f32 v74, v74, v12
v_mul_f32 v75, v75, v13
CV v76,v72,v73
CV v77,v74,v75
v_lshlrev_b32 v72, 16, v76
v_and_b32 v73, 0xffff0000, v76
v_lshlrev_b32 v74, 16, v77
v_and_b32 v75, 0xffff0000, v77
v_add_f32 v72, v72, v14
v_add_f32 v73, v73, v15
v_add_f32 v74, v74, v42
v_add_f32 v75, v75, v43
CV v44,v72,v73
CV v45,v74,v75
buffer_store_dwordx2 v[44:45], v251, s[40:43], s80 offen offset:0 nt
s_bitcmp0_b32 s94, 8
s_cbranch_scc1 LQN0_%=
s_waitcnt vmcnt(1)
s_branch LQS_%=
LQN0_%=:
s_bitcmp0_b32 s94, 1
s_cbranch_scc1 LQN1_%=
MM v[72:75],v[64:67],a[32:35],0
MM v[72:75],v[64:67],a[36:39],v[72:75]
MM v[72:75],v[68:71],a[40:43],v[72:75]
MM v[72:75],v[68:71],a[44:47],v[72:75]
s_bitset0_b32 s17, 1
s_nop 6
CV v76,v72,v73
CV v77,v74,v75
ds_bpermute_b32 v76, v232, v76
ds_bpermute_b32 v77, v232, v77
v_lshlrev_b32 v10, 16, v2
v_and_b32 v11, 0xffff0000, v2
v_lshlrev_b32 v12, 16, v3
v_and_b32 v13, 0xffff0000, v3
v_lshlrev_b32 v14, 16, v34
v_and_b32 v15, 0xffff0000, v34
v_lshlrev_b32 v42, 16, v35
v_and_b32 v43, 0xffff0000, v35
s_add_u32 s80, s18, 128
s_waitcnt lgkmcnt(0)
v_lshlrev_b32 v72, 16, v76
v_and_b32 v73, 0xffff0000, v76
v_lshlrev_b32 v74, 16, v77
v_and_b32 v75, 0xffff0000, v77
v_mul_f32 v72, v72, v10
v_mul_f32 v73, v73, v11
v_mul_f32 v74, v74, v12
v_mul_f32 v75, v75, v13
CV v76,v72,v73
CV v77,v74,v75
v_lshlrev_b32 v72, 16, v76
v_and_b32 v73, 0xffff0000, v76
v_lshlrev_b32 v74, 16, v77
v_and_b32 v75, 0xffff0000, v77
v_add_f32 v72, v72, v14
v_add_f32 v73, v73, v15
v_add_f32 v74, v74, v42
v_add_f32 v75, v75, v43
CV v46,v72,v73
CV v47,v74,v75
buffer_store_dwordx2 v[46:47], v251, s[40:43], s80 offen offset:0 nt
s_bitcmp0_b32 s94, 8
s_cbranch_scc1 LQN1_%=
s_waitcnt vmcnt(1)
s_branch LQS_%=
LQN1_%=:
s_branch LQD_%=
LSW_%=:
s_sleep 1
s_waitcnt vmcnt(0)
s_cmp_eq_u32 s65, 0
s_cbranch_scc1 LSX_%=
s_mov_b64 exec, 1
buffer_store_dword v233, off, s[20:23], s19 offset:12 sc1
s_mov_b64 exec, -1
s_mov_b32 s65, 0
LSX_%=:
v_cmp_ne_u32 vcc, s69, v243
)ASM"
R"ASM(
v_cmp_ne_u32 s[36:37], s69, v143
s_nop 1
s_or_b64 vcc, vcc, s[36:37]
s_cselect_b32 s76, 0, 1
s_add_u32 s72, s72, s76
s_sub_u32 s73, s73, 1
s_cselect_b32 s72, 2, s72
s_bitcmp0_b32 s17, 4
s_cbranch_scc1 LSN4_%=
MM v[72:75],v[64:67],a[192:195],0
MM v[72:75],v[64:67],a[196:199],v[72:75]
MM v[72:75],v[68:71],v[144:147],v[72:75]
MM v[72:75],v[68:71],a[212:215],v[72:75]
AR v76,a195
AR v77,a199
v_mov_b32 v78, v147
AR v79,a215
s_nop 3
v_cmp_u_f32 s[30:31], v72, v72
s_nop 1
s_cmp_eq_u64 s[30:31], 0
s_cbranch_scc1 LSF4_%=
s_cmp_ge_u32 s72, 2
s_cbranch_scc1 LSF4_%=
v_cmp_eq_u32 vcc, s69, v242
v_cmp_eq_u32 s[36:37], -1, v78
s_add_u32 s66, s16, 18432
s_and_b64 vcc, vcc, s[36:37]
s_cmp_eq_u32 s72, 0
s_cselect_b64 exec, vcc, -1
s_and_b32 exec_lo, exec_lo, 0xffff0000
buffer_load_dwordx4 v[144:147], v250, s[20:23], s66 offen offset:0 sc1
s_mov_b64 exec, -1
v_cmp_eq_u32 vcc, s69, v142
v_cmp_eq_u32 s[36:37], -1, v79
s_add_u32 s66, s44, 18432
s_and_b64 vcc, vcc, s[36:37]
s_cmp_eq_u32 s72, 0
s_cselect_b64 exec, vcc, -1
buffer_load_dwordx4 a[212:215], v250, s[20:23], s66 offen offset:0 sc1
s_mov_b64 exec, -1
v_cmp_eq_u32 vcc, s69, v241
v_cmp_eq_u32 s[36:37], -1, v76
s_add_u32 s66, s16, 8192
s_and_b64 vcc, vcc, s[36:37]
s_cmp_eq_u32 s72, 0
s_cselect_b64 exec, vcc, -1
s_and_b32 exec_lo, exec_lo, 0xffff0000
buffer_load_dwordx4 a[192:195], v250, s[20:23], s66 offen offset:0 sc1
s_mov_b64 exec, -1
v_cmp_eq_u32 vcc, s69, v141
v_cmp_eq_u32 s[36:37], -1, v77
s_add_u32 s66, s44, 8192
s_and_b64 vcc, vcc, s[36:37]
s_cmp_eq_u32 s72, 0
s_cselect_b64 exec, vcc, -1
buffer_load_dwordx4 a[196:199], v250, s[20:23], s66 offen offset:0 sc1
s_mov_b64 exec, -1
s_branch LSN4_%=
LSF4_%=:
s_bitset0_b32 s17, 4
CV v76,v72,v73
CV v77,v74,v75
ds_bpermute_b32 v76, v232, v76
ds_bpermute_b32 v77, v232, v77
v_lshlrev_b32 v10, 16, v8
v_and_b32 v11, 0xffff0000, v8
v_lshlrev_b32 v12, 16, v9
v_and_b32 v13, 0xffff0000, v9
v_lshlrev_b32 v14, 16, v40
v_and_b32 v15, 0xffff0000, v40
v_lshlrev_b32 v42, 16, v41
v_and_b32 v43, 0xffff0000, v41
s_add_u32 s80, s63, 0
s_waitcnt lgkmcnt(0)
v_lshlrev_b32 v72, 16, v76
v_and_b32 v73, 0xffff0000, v76
v_lshlrev_b32 v74, 16, v77
v_and_b32 v75, 0xffff0000, v77
v_mul_f32 v72, v72, v10
v_mul_f32 v73, v73, v11
v_mul_f32 v74, v74, v12
v_mul_f32 v75, v75, v13
CV v76,v72,v73
CV v77,v74,v75
v_lshlrev_b32 v72, 16, v76
v_and_b32 v73, 0xffff0000, v76
v_lshlrev_b32 v74, 16, v77
v_and_b32 v75, 0xffff0000, v77
v_add_f32 v72, v72, v14
v_add_f32 v73, v73, v15
v_add_f32 v74, v74, v42
v_add_f32 v75, v75, v43
CV v44,v72,v73
CV v45,v74,v75
buffer_store_dwordx2 v[44:45], v254, s[40:43], s80 offen offset:0 nt
s_nop 1
LSN4_%=:
s_bitcmp0_b32 s17, 3
s_cbranch_scc1 LSN3_%=
MM v[72:75],v[64:67],v[80:83],0
MM v[72:75],v[64:67],a[100:103],v[72:75]
MM v[72:75],v[68:71],v[112:115],v[72:75]
MM v[72:75],v[68:71],a[108:111],v[72:75]
v_mov_b32 v76, v83
AR v77,a103
v_mov_b32 v78, v115
AR v79,a111
s_nop 3
v_cmp_u_f32 s[30:31], v72, v72
s_nop 1
s_cmp_eq_u64 s[30:31], 0
s_cbranch_scc1 LSF3_%=
s_cmp_ge_u32 s72, 2
s_cbranch_scc1 LSF3_%=
v_cmp_eq_u32 vcc, s69, v242
v_cmp_eq_u32 s[36:37], -1, v78
s_add_u32 s66, s16, 16384
s_and_b64 vcc, vcc, s[36:37]
s_cmp_eq_u32 s72, 0
s_cselect_b64 exec, vcc, -1
s_and_b32 exec_lo, exec_lo, 0xffff0000
buffer_load_dwordx4 v[112:115], v250, s[20:23], s66 offen offset:0 sc1
s_mov_b64 exec, -1
v_cmp_eq_u32 vcc, s69, v142
v_cmp_eq_u32 s[36:37], -1, v79
s_add_u32 s66, s44, 16384
s_and_b64 vcc, vcc, s[36:37]
s_cmp_eq_u32 s72, 0
s_cselect_b64 exec, vcc, -1
buffer_load_dwordx4 a[108:111], v250, s[20:23], s66 offen offset:0 sc1
s_mov_b64 exec, -1
v_cmp_eq_u32 vcc, s69, v242
v_cmp_eq_u32 s[36:37], -1, v76
s_add_u32 s66, s16, 14336
s_and_b64 vcc, vcc, s[36:37]
s_cmp_eq_u32 s72, 0
s_cselect_b64 exec, vcc, -1
s_and_b32 exec_lo, exec_lo, 0xffff0000
buffer_load_dwordx4 v[80:83], v250, s[20:23], s66 offen offset:0 sc1
s_mov_b64 exec, -1
v_cmp_eq_u32 vcc, s69, v142
v_cmp_eq_u32 s[36:37], -1, v77
s_add_u32 s66, s44, 14336
s_and_b64 vcc, vcc, s[36:37]
s_cmp_eq_u32 s72, 0
s_cselect_b64 exec, vcc, -1
buffer_load_dwordx4 a[100:103], v250, s[20:23], s66 offen offset:0 sc1
s_mov_b64 exec, -1
s_branch LSN3_%=
LSF3_%=:
s_bitset0_b32 s17, 3
CV v76,v72,v73
CV v77,v74,v75
ds_bpermute_b32 v76, v232, v76
ds_bpermute_b32 v77, v232, v77
v_lshlrev_b32 v10, 16, v6
v_and_b32 v11, 0xffff0000, v6
v_lshlrev_b32 v12, 16, v7
v_and_b32 v13, 0xffff0000, v7
v_lshlrev_b32 v14, 16, v38
v_and_b32 v15, 0xffff0000, v38
v_lshlrev_b32 v42, 16, v39
v_and_b32 v43, 0xffff0000, v39
s_add_u32 s80, s18, 41088
s_waitcnt lgkmcnt(0)
v_lshlrev_b32 v72, 16, v76
v_and_b32 v73, 0xffff0000, v76
v_lshlrev_b32 v74, 16, v77
v_and_b32 v75, 0xffff0000, v77
v_mul_f32 v72, v72, v10
v_mul_f32 v73, v73, v11
v_mul_f32 v74, v74, v12
v_mul_f32 v75, v75, v13
CV v76,v72,v73
CV v77,v74,v75
v_lshlrev_b32 v72, 16, v76
v_and_b32 v73, 0xffff0000, v76
v_lshlrev_b32 v74, 16, v77
v_and_b32 v75, 0xffff0000, v77
v_add_f32 v72, v72, v14
v_add_f32 v73, v73, v15
v_add_f32 v74, v74, v42
v_add_f32 v75, v75, v43
CV v46,v72,v73
CV v47,v74,v75
buffer_store_dwordx2 v[46:47], v251, s[40:43], s80 offen offset:0 nt
s_nop 1
LSN3_%=:
s_bitcmp0_b32 s17, 2
s_cbranch_scc1 LSN2_%=
MM v[72:75],v[64:67],v[16:19],0
MM v[72:75],v[64:67],a[68:71],v[72:75]
MM v[72:75],v[68:71],v[48:51],v[72:75]
MM v[72:75],v[68:71],a[76:79],v[72:75]
v_mov_b32 v76, v19
AR v77,a71
v_mov_b32 v78, v51
AR v79,a79
s_nop 3
v_cmp_u_f32 s[30:31], v72, v72
s_nop 1
s_cmp_eq_u64 s[30:31], 0
s_cbranch_scc1 LSF2_%=
s_cmp_ge_u32 s72, 2
s_cbranch_scc1 LSF2_%=
v_cmp_eq_u32 vcc, s69, v242
v_cmp_eq_u32 s[36:37], -1, v78
s_add_u32 s66, s16, 12288
s_and_b64 vcc, vcc, s[36:37]
s_cmp_eq_u32 s72, 0
s_cselect_b64 exec, vcc, -1
s_and_b32 exec_lo, exec_lo, 0xffff0000
buffer_load_dwordx4 v[48:51], v250, s[20:23], s66 offen offset:0 sc1
s_mov_b64 exec, -1
v_cmp_eq_u32 vcc, s69, v142
v_cmp_eq_u32 s[36:37], -1, v79
s_add_u32 s66, s44, 12288
s_and_b64 vcc, vcc, s[36:37]
s_cmp_eq_u32 s72, 0
s_cselect_b64 exec, vcc, -1
buffer_load_dwordx4 a[76:79], v250, s[20:23], s66 offen offset:0 sc1
s_mov_b64 exec, -1
v_cmp_eq_u32 vcc, s69, v242
v_cmp_eq_u32 s[36:37], -1, v76
s_add_u32 s66, s16, 10240
s_and_b64 vcc, vcc, s[36:37]
s_cmp_eq_u32 s72, 0
s_cselect_b64 exec, vcc, -1
s_and_b32 exec_lo, exec_lo, 0xffff0000
buffer_load_dwordx4 v[16:19], v250, s[20:23], s66 offen offset:0 sc1
s_mov_b64 exec, -1
v_cmp_eq_u32 vcc, s69, v142
v_cmp_eq_u32 s[36:37], -1, v77
s_add_u32 s66, s44, 10240
s_and_b64 vcc, vcc, s[36:37]
s_cmp_eq_u32 s72, 0
s_cselect_b64 exec, vcc, -1
buffer_load_dwordx4 a[68:71], v250, s[20:23], s66 offen offset:0 sc1
s_mov_b64 exec, -1
s_branch LSN2_%=
LSF2_%=:
s_bitset0_b32 s17, 2
CV v76,v72,v73
CV v77,v74,v75
ds_bpermute_b32 v76, v232, v76
ds_bpermute_b32 v77, v232, v77
v_lshlrev_b32 v10, 16, v4
v_and_b32 v11, 0xffff0000, v4
v_lshlrev_b32 v12, 16, v5
v_and_b32 v13, 0xffff0000, v5
v_lshlrev_b32 v14, 16, v36
v_and_b32 v15, 0xffff0000, v36
v_lshlrev_b32 v42, 16, v37
v_and_b32 v43, 0xffff0000, v37
s_add_u32 s80, s18, 40960
s_waitcnt lgkmcnt(0)
v_lshlrev_b32 v72, 16, v76
v_and_b32 v73, 0xffff0000, v76
v_lshlrev_b32 v74, 16, v77
v_and_b32 v75, 0xffff0000, v77
v_mul_f32 v72, v72, v10
v_mul_f32 v73, v73, v11
v_mul_f32 v74, v74, v12
v_mul_f32 v75, v75, v13
CV v76,v72,v73
CV v77,v74,v75
v_lshlrev_b32 v72, 16, v76
v_and_b32 v73, 0xffff0000, v76
v_lshlrev_b32 v74, 16, v77
v_and_b32 v75, 0xffff0000, v77
v_add_f32 v72, v72, v14
v_add_f32 v73, v73, v15
v_add_f32 v74, v74, v42
v_add_f32 v75, v75, v43
CV v44,v72,v73
CV v45,v74,v75
buffer_store_dwordx2 v[44:45], v251, s[40:43], s80 offen offset:0 nt
s_nop 1
LSN2_%=:
s_bitcmp0_b32 s17, 1
s_cbranch_scc1 LSN1_%=
MM v[72:75],v[64:67],a[32:35],0
MM v[72:75],v[64:67],a[36:39],v[72:75]
MM v[72:75],v[68:71],a[40:43],v[72:75]
MM v[72:75],v[68:71],a[44:47],v[72:75]
AR v76,a35
AR v77,a39
AR v78,a43
AR v79,a47
s_nop 3
v_cmp_u_f32 s[30:31], v72, v72
s_nop 1
s_cmp_eq_u64 s[30:31], 0
s_cbranch_scc1 LSF1_%=
s_cmp_ge_u32 s72, 2
s_cbranch_scc1 LSF1_%=
v_cmp_eq_u32 vcc, s69, v241
v_cmp_eq_u32 s[36:37], -1, v78
s_add_u32 s66, s16, 6144
s_and_b64 vcc, vcc, s[36:37]
s_cmp_eq_u32 s72, 0
s_cselect_b64 exec, vcc, -1
s_and_b32 exec_lo, exec_lo, 0xffff0000
buffer_load_dwordx4 a[40:43], v250, s[20:23], s66 offen offset:0 sc1
s_mov_b64 exec, -1
v_cmp_eq_u32 vcc, s69, v141
v_cmp_eq_u32 s[36:37], -1, v79
s_add_u32 s66, s44, 6144
s_and_b64 vcc, vcc, s[36:37]
s_cmp_eq_u32 s72, 0
s_cselect_b64 exec, vcc, -1
buffer_load_dwordx4 a[44:47], v250, s[20:23], s66 offen offset:0 sc1
s_mov_b64 exec, -1
v_cmp_eq_u32 vcc, s69, v241
v_cmp_eq_u32 s[36:37], -1, v76
s_add_u32 s66, s16, 4096
s_and_b64 vcc, vcc, s[36:37]
s_cmp_eq_u32 s72, 0
s_cselect_b64 exec, vcc, -1
s_and_b32 exec_lo, exec_lo, 0xffff0000
buffer_load_dwordx4 a[32:35], v250, s[20:23], s66 offen offset:0 sc1
s_mov_b64 exec, -1
v_cmp_eq_u32 vcc, s69, v141
v_cmp_eq_u32 s[36:37], -1, v77
s_add_u32 s66, s44, 4096
s_and_b64 vcc, vcc, s[36:37]
s_cmp_eq_u32 s72, 0
s_cselect_b64 exec, vcc, -1
buffer_load_dwordx4 a[36:39], v250, s[20:23], s66 offen offset:0 sc1
s_mov_b64 exec, -1
s_branch LSN1_%=
LSF1_%=:
s_bitset0_b32 s17, 1
CV v76,v72,v73
CV v77,v74,v75
ds_bpermute_b32 v76, v232, v76
ds_bpermute_b32 v77, v232, v77
v_lshlrev_b32 v10, 16, v2
v_and_b32 v11, 0xffff0000, v2
v_lshlrev_b32 v12, 16, v3
v_and_b32 v13, 0xffff0000, v3
v_lshlrev_b32 v14, 16, v34
v_and_b32 v15, 0xffff0000, v34
v_lshlrev_b32 v42, 16, v35
v_and_b32 v43, 0xffff0000, v35
s_add_u32 s80, s18, 128
s_waitcnt lgkmcnt(0)
v_lshlrev_b32 v72, 16, v76
v_and_b32 v73, 0xffff0000, v76
v_lshlrev_b32 v74, 16, v77
v_and_b32 v75, 0xffff0000, v77
v_mul_f32 v72, v72, v10
v_mul_f32 v73, v73, v11
v_mul_f32 v74, v74, v12
v_mul_f32 v75, v75, v13
CV v76,v72,v73
CV v77,v74,v75
v_lshlrev_b32 v72, 16, v76
v_and_b32 v73, 0xffff0000, v76
v_lshlrev_b32 v74, 16, v77
v_and_b32 v75, 0xffff0000, v77
v_add_f32 v72, v72, v14
v_add_f32 v73, v73, v15
v_add_f32 v74, v74, v42
v_add_f32 v75, v75, v43
CV v46,v72,v73
CV v47,v74,v75
buffer_store_dwordx2 v[46:47], v251, s[40:43], s80 offen offset:0 nt
s_nop 1
LSN1_%=:
s_bitcmp0_b32 s17, 0
s_cbranch_scc1 LSN0_%=
MM v[72:75],v[64:67],a[0:3],0
MM v[72:75],v[64:67],a[4:7],v[72:75]
MM v[72:75],v[68:71],a[8:11],v[72:75]
MM v[72:75],v[68:71],a[12:15],v[72:75]
AR v76,a3
AR v77,a7
AR v78,a11
AR v79,a15
s_nop 3
v_cmp_u_f32 s[30:31], v72, v72
s_nop 1
s_cmp_eq_u64 s[30:31], 0
s_cbranch_scc1 LSF0_%=
s_cmp_ge_u32 s72, 2
s_cbranch_scc1 LSF0_%=
v_cmp_eq_u32 vcc, s69, v240
v_cmp_eq_u32 s[36:37], -1, v78
s_add_u32 s66, s16, 2048
s_and_b64 vcc, vcc, s[36:37]
s_cmp_eq_u32 s72, 0
s_cselect_b64 exec, vcc, -1
s_and_b32 exec_lo, exec_lo, 0xffff0000
buffer_load_dwordx4 a[8:11], v250, s[20:23], s66 offen offset:0 sc1
s_mov_b64 exec, -1
v_cmp_eq_u32 vcc, s69, v140
v_cmp_eq_u32 s[36:37], -1, v79
s_add_u32 s66, s44, 2048
s_and_b64 vcc, vcc, s[36:37]
s_cmp_eq_u32 s72, 0
s_cselect_b64 exec, vcc, -1
buffer_load_dwordx4 a[12:15], v250, s[20:23], s66 offen offset:0 sc1
s_mov_b64 exec, -1
v_cmp_eq_u32 vcc, s69, v240
v_cmp_eq_u32 s[36:37], -1, v76
s_add_u32 s66, s16, 0
s_and_b64 vcc, vcc, s[36:37]
s_cmp_eq_u32 s72, 0
s_cselect_b64 exec, vcc, -1
s_and_b32 exec_lo, exec_lo, 0xffff0000
buffer_load_dwordx4 a[0:3], v250, s[20:23], s66 offen offset:0 sc1
s_mov_b64 exec, -1
v_cmp_eq_u32 vcc, s69, v140
v_cmp_eq_u32 s[36:37], -1, v77
s_add_u32 s66, s44, 0
s_and_b64 vcc, vcc, s[36:37]
s_cmp_eq_u32 s72, 0
s_cselect_b64 exec, vcc, -1
buffer_load_dwordx4 a[4:7], v250, s[20:23], s66 offen offset:0 sc1
s_mov_b64 exec, -1
s_branch LSN0_%=
LSF0_%=:
s_bitset0_b32 s17, 0
CV v76,v72,v73
CV v77,v74,v75
ds_bpermute_b32 v76, v232, v76
ds_bpermute_b32 v77, v232, v77
v_lshlrev_b32 v10, 16, v0
v_and_b32 v11, 0xffff0000, v0
v_lshlrev_b32 v12, 16, v1
v_and_b32 v13, 0xffff0000, v1
v_lshlrev_b32 v14, 16, v32
v_and_b32 v15, 0xffff0000, v32
v_lshlrev_b32 v42, 16, v33
v_and_b32 v43, 0xffff0000, v33
s_add_u32 s80, s18, 0
s_waitcnt lgkmcnt(0)
v_lshlrev_b32 v72, 16, v76
v_and_b32 v73, 0xffff0000, v76
v_lshlrev_b32 v74, 16, v77
v_and_b32 v75, 0xffff0000, v77
v_mul_f32 v72, v72, v10
v_mul_f32 v73, v73, v11
v_mul_f32 v74, v74, v12
)ASM"
R"ASM(
v_mul_f32 v75, v75, v13
CV v76,v72,v73
CV v77,v74,v75
v_lshlrev_b32 v72, 16, v76
v_and_b32 v73, 0xffff0000, v76
v_lshlrev_b32 v74, 16, v77
v_and_b32 v75, 0xffff0000, v77
v_add_f32 v72, v72, v14
v_add_f32 v73, v73, v15
v_add_f32 v74, v74, v42
v_add_f32 v75, v75, v43
CV v44,v72,v73
CV v45,v74,v75
buffer_store_dwordx2 v[44:45], v251, s[40:43], s80 offen offset:0 nt
s_nop 1
LSN0_%=:
v_cmp_ne_u32 vcc, s69, v242
v_cmp_ne_u32 s[36:37], s69, v142
s_nop 1
s_or_b64 vcc, vcc, s[36:37]
s_cselect_b32 s76, 0, 15
s_and_b32 s76, s76, s73
s_cbranch_scc1 LSU_%=
buffer_load_dwordx4 v[240:243], v253, s[20:23], s68 offen offset:0 sc1
buffer_load_dwordx4 v[140:143], v253, s[20:23], s45 offen offset:0 sc1
LSU_%=:
s_or_b32 s76, s17, s65
s_cbranch_scc1 LSW_%=
s_branch LQE_%=
LKEND_ka:
LSITE0_%=:
s_mov_b32 s73, 0
s_mov_b32 s75, 16384
s_mov_b32 s74, 0
s_branch LRARE002_%=
LSITE2_%=:
s_mov_b32 s73, 2
s_mov_b32 s75, 49152
s_mov_b32 s74, 32768
s_branch LRARE002_%=
LRARE002_%=:
s_nop 15
s_waitcnt vmcnt(0) lgkmcnt(0)
v_add_u32 v249, s74, v224
RD a[192:195],v249 offset:0
RD a[208:211],v249 offset:4096
RD a[224:227],v249 offset:8192
RD a[240:243],v249 offset:12288
v_add_u32 v250, s74, v225
RD a[196:199],v250 offset:0
RD a[212:215],v250 offset:4096
RD a[228:231],v250 offset:8192
RD a[244:247],v250 offset:12288
v_add_u32 v251, s74, v226
RD a[200:203],v251 offset:0
RD a[216:219],v251 offset:4096
RD a[232:235],v251 offset:8192
RD a[248:251],v251 offset:12288
v_add_u32 v254, s74, v227
RD a[204:207],v254 offset:0
RD a[220:223],v254 offset:4096
RD a[236:239],v254 offset:8192
RD a[252:255],v254 offset:12288
s_waitcnt lgkmcnt(0)
MM v[0:3],a[192:195],v[160:163],v[128:131]
MM v[0:3],a[196:199],v[176:179],v[0:3]
MM v[0:3],a[200:203],v[192:195],v[0:3]
MM v[0:3],a[204:207],v[208:211],v[0:3]
MM v[4:7],a[220:223],v[208:211],v[128:131]
MM v[4:7],a[216:219],v[192:195],v[4:7]
MM v[4:7],a[212:215],v[176:179],v[4:7]
MM v[4:7],a[208:211],v[160:163],v[4:7]
MM v[12:15],a[208:211],v[164:167],v[132:135]
MM v[12:15],a[212:215],v[180:183],v[12:15]
MM v[12:15],a[216:219],v[196:199],v[12:15]
MM v[12:15],a[220:223],v[212:215],v[12:15]
MM v[8:11],a[204:207],v[212:215],v[132:135]
MM v[8:11],a[200:203],v[196:199],v[8:11]
MM v[8:11],a[196:199],v[180:183],v[8:11]
MM v[8:11],a[192:195],v[164:167],v[8:11]
MM v[32:35],a[224:227],v[160:163],v[128:131]
MM v[32:35],a[228:231],v[176:179],v[32:35]
MM v[32:35],a[232:235],v[192:195],v[32:35]
MM v[32:35],a[236:239],v[208:211],v[32:35]
MM v[36:39],a[252:255],v[208:211],v[128:131]
MM v[36:39],a[248:251],v[192:195],v[36:39]
MM v[36:39],a[244:247],v[176:179],v[36:39]
MM v[36:39],a[240:243],v[160:163],v[36:39]
MM v[44:47],a[240:243],v[164:167],v[132:135]
MM v[44:47],a[244:247],v[180:183],v[44:47]
MM v[44:47],a[248:251],v[196:199],v[44:47]
MM v[44:47],a[252:255],v[212:215],v[44:47]
MM v[40:43],a[236:239],v[212:215],v[132:135]
MM v[40:43],a[232:235],v[196:199],v[40:43]
MM v[40:43],a[228:231],v[180:183],v[40:43]
MM v[40:43],a[224:227],v[164:167],v[40:43]
s_nop 15
v_add_u32 v249, s75, v224
RD a[192:195],v249 offset:0
RD a[208:211],v249 offset:4096
RD a[224:227],v249 offset:8192
RD a[240:243],v249 offset:12288
v_add_u32 v250, s75, v225
RD a[196:199],v250 offset:0
RD a[212:215],v250 offset:4096
RD a[228:231],v250 offset:8192
RD a[244:247],v250 offset:12288
v_add_u32 v251, s75, v226
RD a[200:203],v251 offset:0
RD a[216:219],v251 offset:4096
RD a[232:235],v251 offset:8192
RD a[248:251],v251 offset:12288
v_add_u32 v254, s75, v227
RD a[204:207],v254 offset:0
RD a[220:223],v254 offset:4096
RD a[236:239],v254 offset:8192
RD a[252:255],v254 offset:12288
s_waitcnt lgkmcnt(0)
v_mov_b32 v236, v247
v_mov_b32 v237, 0
v_max3_f32 v249, v0, v1, v2
v_max3_f32 v251, v32, v33, v34
v_max3_f32 v249, v249, v3, v4
v_max3_f32 v251, v251, v35, v36
v_max3_f32 v249, v249, v5, v6
v_max3_f32 v251, v251, v37, v38
v_max3_f32 v249, v249, v7, v39
v_max_f32 v249, v249, v251
v_log_f32 v250, v247
s_lshl_b64 exec, 0xffff, 0
v_max_f32 v249, v249, v250
s_mov_b64 exec, -1
v_mov_b32 v251, v249
s_nop 1
v_permlane16_swap_b32 v249, v251
v_max_f32 v249, v249, v251
v_mov_b32 v251, v249
s_nop 1
v_permlane32_swap_b32 v249, v251
v_max_f32 v249, v249, v251
v_max_f32 v249, 0, v249
v_sub_f32 v250, 0, v249
EX v250,v250
v_sub_f32 v128, v128, v249
v_sub_f32 v129, v129, v249
v_sub_f32 v130, v130, v249
v_sub_f32 v131, v131, v249
v_sub_f32 v64, v64, v249
v_sub_f32 v0, v0, v249
v_sub_f32 v65, v65, v249
v_sub_f32 v1, v1, v249
v_sub_f32 v66, v66, v249
v_sub_f32 v2, v2, v249
v_sub_f32 v67, v67, v249
v_sub_f32 v3, v3, v249
v_sub_f32 v68, v68, v249
v_sub_f32 v4, v4, v249
v_sub_f32 v69, v69, v249
v_sub_f32 v5, v5, v249
v_sub_f32 v70, v70, v249
v_sub_f32 v6, v6, v249
v_sub_f32 v71, v71, v249
v_sub_f32 v7, v7, v249
v_sub_f32 v96, v96, v249
v_sub_f32 v32, v32, v249
v_sub_f32 v97, v97, v249
v_sub_f32 v33, v33, v249
v_sub_f32 v98, v98, v249
v_sub_f32 v34, v34, v249
v_sub_f32 v99, v99, v249
v_sub_f32 v35, v35, v249
v_sub_f32 v100, v100, v249
v_sub_f32 v36, v36, v249
v_sub_f32 v101, v101, v249
v_sub_f32 v37, v37, v249
v_sub_f32 v102, v102, v249
v_sub_f32 v38, v38, v249
v_sub_f32 v103, v103, v249
v_sub_f32 v39, v39, v249
s_lshl_b64 exec, 0xffff, 0
v_mul_f32 v236, v236, v250
s_mov_b64 exec, -1
AR v254,a0
v_mul_f32 v254, v254, v250
AW a0,v254
AR v251,a1
v_mul_f32 v251, v251, v250
AW a1,v251
AR v254,a2
v_mul_f32 v254, v254, v250
AW a2,v254
AR v251,a3
v_mul_f32 v251, v251, v250
AW a3,v251
AR v254,a4
v_mul_f32 v254, v254, v250
AW a4,v254
AR v251,a5
v_mul_f32 v251, v251, v250
AW a5,v251
AR v254,a6
v_mul_f32 v254, v254, v250
AW a6,v254
AR v251,a7
v_mul_f32 v251, v251, v250
AW a7,v251
AR v254,a32
v_mul_f32 v254, v254, v250
AW a32,v254
AR v251,a33
v_mul_f32 v251, v251, v250
AW a33,v251
AR v254,a34
v_mul_f32 v254, v254, v250
AW a34,v254
AR v251,a35
v_mul_f32 v251, v251, v250
AW a35,v251
AR v254,a36
v_mul_f32 v254, v254, v250
AW a36,v254
AR v251,a37
v_mul_f32 v251, v251, v250
AW a37,v251
AR v254,a38
v_mul_f32 v254, v254, v250
AW a38,v254
AR v251,a39
v_mul_f32 v251, v251, v250
AW a39,v251
AR v254,a64
v_mul_f32 v254, v254, v250
AW a64,v254
AR v251,a65
v_mul_f32 v251, v251, v250
AW a65,v251
AR v254,a66
v_mul_f32 v254, v254, v250
AW a66,v254
AR v251,a67
v_mul_f32 v251, v251, v250
AW a67,v251
AR v254,a68
v_mul_f32 v254, v254, v250
AW a68,v254
AR v251,a69
v_mul_f32 v251, v251, v250
AW a69,v251
AR v254,a70
v_mul_f32 v254, v254, v250
AW a70,v254
AR v251,a71
v_mul_f32 v251, v251, v250
AW a71,v251
AR v254,a96
v_mul_f32 v254, v254, v250
AW a96,v254
AR v251,a97
v_mul_f32 v251, v251, v250
AW a97,v251
AR v254,a98
v_mul_f32 v254, v254, v250
AW a98,v254
AR v251,a99
v_mul_f32 v251, v251, v250
AW a99,v251
AR v254,a100
v_mul_f32 v254, v254, v250
AW a100,v254
AR v251,a101
v_mul_f32 v251, v251, v250
AW a101,v251
AR v254,a102
v_mul_f32 v254, v254, v250
AW a102,v254
AR v251,a103
v_mul_f32 v251, v251, v250
AW a103,v251
v_max3_f32 v249, v8, v9, v10
v_max3_f32 v251, v40, v41, v42
v_max3_f32 v249, v249, v11, v12
v_max3_f32 v251, v251, v43, v44
v_max3_f32 v249, v249, v13, v14
v_max3_f32 v251, v251, v45, v46
v_max3_f32 v249, v249, v15, v47
v_max_f32 v249, v249, v251
v_log_f32 v250, v247
s_lshl_b64 exec, 0xffff, 16
v_max_f32 v249, v249, v250
s_mov_b64 exec, -1
v_mov_b32 v251, v249
s_nop 1
v_permlane16_swap_b32 v249, v251
v_max_f32 v249, v249, v251
v_mov_b32 v251, v249
s_nop 1
v_permlane32_swap_b32 v249, v251
v_max_f32 v249, v249, v251
v_max_f32 v249, 0, v249
v_sub_f32 v250, 0, v249
EX v250,v250
v_sub_f32 v132, v132, v249
v_sub_f32 v133, v133, v249
v_sub_f32 v134, v134, v249
v_sub_f32 v135, v135, v249
v_sub_f32 v72, v72, v249
v_sub_f32 v8, v8, v249
v_sub_f32 v73, v73, v249
v_sub_f32 v9, v9, v249
v_sub_f32 v74, v74, v249
v_sub_f32 v10, v10, v249
v_sub_f32 v75, v75, v249
v_sub_f32 v11, v11, v249
v_sub_f32 v76, v76, v249
v_sub_f32 v12, v12, v249
v_sub_f32 v77, v77, v249
v_sub_f32 v13, v13, v249
v_sub_f32 v78, v78, v249
v_sub_f32 v14, v14, v249
v_sub_f32 v79, v79, v249
v_sub_f32 v15, v15, v249
v_sub_f32 v104, v104, v249
v_sub_f32 v40, v40, v249
v_sub_f32 v105, v105, v249
v_sub_f32 v41, v41, v249
v_sub_f32 v106, v106, v249
v_sub_f32 v42, v42, v249
v_sub_f32 v107, v107, v249
v_sub_f32 v43, v43, v249
v_sub_f32 v108, v108, v249
v_sub_f32 v44, v44, v249
v_sub_f32 v109, v109, v249
v_sub_f32 v45, v45, v249
v_sub_f32 v110, v110, v249
v_sub_f32 v46, v46, v249
v_sub_f32 v111, v111, v249
v_sub_f32 v47, v47, v249
s_lshl_b64 exec, 0xffff, 16
v_mul_f32 v236, v236, v250
s_mov_b64 exec, -1
AR v254,a8
v_mul_f32 v254, v254, v250
AW a8,v254
AR v251,a9
v_mul_f32 v251, v251, v250
AW a9,v251
AR v254,a10
v_mul_f32 v254, v254, v250
AW a10,v254
AR v251,a11
v_mul_f32 v251, v251, v250
AW a11,v251
AR v254,a12
v_mul_f32 v254, v254, v250
AW a12,v254
AR v251,a13
v_mul_f32 v251, v251, v250
AW a13,v251
AR v254,a14
v_mul_f32 v254, v254, v250
AW a14,v254
AR v251,a15
v_mul_f32 v251, v251, v250
AW a15,v251
AR v254,a40
v_mul_f32 v254, v254, v250
AW a40,v254
AR v251,a41
v_mul_f32 v251, v251, v250
AW a41,v251
AR v254,a42
v_mul_f32 v254, v254, v250
AW a42,v254
AR v251,a43
v_mul_f32 v251, v251, v250
AW a43,v251
AR v254,a44
v_mul_f32 v254, v254, v250
AW a44,v254
AR v251,a45
v_mul_f32 v251, v251, v250
AW a45,v251
AR v254,a46
v_mul_f32 v254, v254, v250
AW a46,v254
AR v251,a47
v_mul_f32 v251, v251, v250
AW a47,v251
AR v254,a72
v_mul_f32 v254, v254, v250
AW a72,v254
AR v251,a73
v_mul_f32 v251, v251, v250
AW a73,v251
AR v254,a74
v_mul_f32 v254, v254, v250
AW a74,v254
AR v251,a75
v_mul_f32 v251, v251, v250
AW a75,v251
AR v254,a76
v_mul_f32 v254, v254, v250
AW a76,v254
AR v251,a77
v_mul_f32 v251, v251, v250
AW a77,v251
AR v254,a78
v_mul_f32 v254, v254, v250
AW a78,v254
AR v251,a79
v_mul_f32 v251, v251, v250
AW a79,v251
AR v254,a104
v_mul_f32 v254, v254, v250
AW a104,v254
AR v251,a105
v_mul_f32 v251, v251, v250
AW a105,v251
AR v254,a106
v_mul_f32 v254, v254, v250
AW a106,v254
AR v251,a107
v_mul_f32 v251, v251, v250
AW a107,v251
AR v254,a108
v_mul_f32 v254, v254, v250
AW a108,v254
AR v251,a109
v_mul_f32 v251, v251, v250
AW a109,v251
AR v254,a110
v_mul_f32 v254, v254, v250
AW a110,v254
AR v251,a111
v_mul_f32 v251, v251, v250
AW a111,v251
EX v0,v0
EX v1,v1
EX v2,v2
EX v3,v3
EX v4,v4
EX v5,v5
EX v6,v6
EX v7,v7
EX v8,v8
EX v9,v9
EX v10,v10
EX v11,v11
EX v12,v12
EX v13,v13
EX v14,v14
EX v15,v15
EX v32,v32
EX v33,v33
EX v34,v34
EX v35,v35
EX v36,v36
EX v37,v37
EX v38,v38
EX v39,v39
EX v40,v40
EX v41,v41
EX v42,v42
EX v43,v43
EX v44,v44
EX v45,v45
EX v46,v46
EX v47,v47
CV v0,v0,v1
CV v1,v2,v3
CV v2,v4,v5
CV v3,v6,v7
CV v8,v8,v9
CV v9,v10,v11
CV v10,v12,v13
CV v11,v14,v15
CV v4,v32,v33
CV v5,v34,v35
CV v6,v36,v37
CV v7,v38,v39
CV v12,v40,v41
CV v13,v42,v43
CV v14,v44,v45
CV v15,v46,v47
v_smfmac_f32_16x16x64_bf16 v[236:239], v[144:147], v[0:7], v234
s_nop 0
v_smfmac_f32_16x16x64_bf16 v[236:239], v[148:151], v[8:15], v234
s_nop 9
v_add_f32 v246, v236, v237
s_cmp_eq_u32 s73, 0
s_cbranch_scc1 LBACK0_%=
s_cmp_eq_u32 s73, 2
s_cbranch_scc1 LBACK2_%=
s_endpgm
LSITE1_%=:
s_mov_b32 s73, 1
s_mov_b32 s75, 32768
s_mov_b32 s74, 16384
s_branch LRARE102_%=
LRARE102_%=:
s_nop 15
s_waitcnt vmcnt(0) lgkmcnt(0)
v_add_u32 v249, s74, v224
RD a[192:195],v249 offset:0
RD a[208:211],v249 offset:4096
RD a[224:227],v249 offset:8192
RD a[240:243],v249 offset:12288
v_add_u32 v250, s74, v225
RD a[196:199],v250 offset:0
RD a[212:215],v250 offset:4096
RD a[228:231],v250 offset:8192
RD a[244:247],v250 offset:12288
v_add_u32 v251, s74, v226
RD a[200:203],v251 offset:0
RD a[216:219],v251 offset:4096
RD a[232:235],v251 offset:8192
RD a[248:251],v251 offset:12288
v_add_u32 v254, s74, v227
RD a[204:207],v254 offset:0
RD a[220:223],v254 offset:4096
RD a[236:239],v254 offset:8192
RD a[252:255],v254 offset:12288
s_waitcnt lgkmcnt(0)
MM v[64:67],a[192:195],v[160:163],v[128:131]
MM v[64:67],a[196:199],v[176:179],v[64:67]
MM v[64:67],a[200:203],v[192:195],v[64:67]
MM v[64:67],a[204:207],v[208:211],v[64:67]
MM v[68:71],a[220:223],v[208:211],v[128:131]
MM v[68:71],a[216:219],v[192:195],v[68:71]
MM v[68:71],a[212:215],v[176:179],v[68:71]
MM v[68:71],a[208:211],v[160:163],v[68:71]
MM v[76:79],a[208:211],v[164:167],v[132:135]
MM v[76:79],a[212:215],v[180:183],v[76:79]
)ASM"
R"ASM(
MM v[76:79],a[216:219],v[196:199],v[76:79]
MM v[76:79],a[220:223],v[212:215],v[76:79]
MM v[72:75],a[204:207],v[212:215],v[132:135]
MM v[72:75],a[200:203],v[196:199],v[72:75]
MM v[72:75],a[196:199],v[180:183],v[72:75]
MM v[72:75],a[192:195],v[164:167],v[72:75]
MM v[96:99],a[224:227],v[160:163],v[128:131]
MM v[96:99],a[228:231],v[176:179],v[96:99]
MM v[96:99],a[232:235],v[192:195],v[96:99]
MM v[96:99],a[236:239],v[208:211],v[96:99]
MM v[100:103],a[252:255],v[208:211],v[128:131]
MM v[100:103],a[248:251],v[192:195],v[100:103]
MM v[100:103],a[244:247],v[176:179],v[100:103]
MM v[100:103],a[240:243],v[160:163],v[100:103]
MM v[108:111],a[240:243],v[164:167],v[132:135]
MM v[108:111],a[244:247],v[180:183],v[108:111]
MM v[108:111],a[248:251],v[196:199],v[108:111]
MM v[108:111],a[252:255],v[212:215],v[108:111]
MM v[104:107],a[236:239],v[212:215],v[132:135]
MM v[104:107],a[232:235],v[196:199],v[104:107]
MM v[104:107],a[228:231],v[180:183],v[104:107]
MM v[104:107],a[224:227],v[164:167],v[104:107]
s_nop 15
v_add_u32 v249, s75, v224
RD a[192:195],v249 offset:0
RD a[208:211],v249 offset:4096
RD a[224:227],v249 offset:8192
RD a[240:243],v249 offset:12288
v_add_u32 v250, s75, v225
RD a[196:199],v250 offset:0
RD a[212:215],v250 offset:4096
RD a[228:231],v250 offset:8192
RD a[244:247],v250 offset:12288
v_add_u32 v251, s75, v226
RD a[200:203],v251 offset:0
RD a[216:219],v251 offset:4096
RD a[232:235],v251 offset:8192
RD a[248:251],v251 offset:12288
v_add_u32 v254, s75, v227
RD a[204:207],v254 offset:0
RD a[220:223],v254 offset:4096
RD a[236:239],v254 offset:8192
RD a[252:255],v254 offset:12288
s_waitcnt lgkmcnt(0)
v_mov_b32 v236, v246
v_mov_b32 v237, 0
v_max3_f32 v249, v64, v65, v66
v_max3_f32 v251, v96, v97, v98
v_max3_f32 v249, v249, v67, v68
v_max3_f32 v251, v251, v99, v100
v_max3_f32 v249, v249, v69, v70
v_max3_f32 v251, v251, v101, v102
v_max3_f32 v249, v249, v71, v103
v_max_f32 v249, v249, v251
v_log_f32 v250, v246
s_lshl_b64 exec, 0xffff, 0
v_max_f32 v249, v249, v250
s_mov_b64 exec, -1
v_mov_b32 v251, v249
s_nop 1
v_permlane16_swap_b32 v249, v251
v_max_f32 v249, v249, v251
v_mov_b32 v251, v249
s_nop 1
v_permlane32_swap_b32 v249, v251
v_max_f32 v249, v249, v251
v_max_f32 v249, 0, v249
v_sub_f32 v250, 0, v249
EX v250,v250
v_sub_f32 v128, v128, v249
v_sub_f32 v129, v129, v249
v_sub_f32 v130, v130, v249
v_sub_f32 v131, v131, v249
v_sub_f32 v0, v0, v249
v_sub_f32 v64, v64, v249
v_sub_f32 v1, v1, v249
v_sub_f32 v65, v65, v249
v_sub_f32 v2, v2, v249
v_sub_f32 v66, v66, v249
v_sub_f32 v3, v3, v249
v_sub_f32 v67, v67, v249
v_sub_f32 v4, v4, v249
v_sub_f32 v68, v68, v249
v_sub_f32 v5, v5, v249
v_sub_f32 v69, v69, v249
v_sub_f32 v6, v6, v249
v_sub_f32 v70, v70, v249
v_sub_f32 v7, v7, v249
v_sub_f32 v71, v71, v249
v_sub_f32 v32, v32, v249
v_sub_f32 v96, v96, v249
v_sub_f32 v33, v33, v249
v_sub_f32 v97, v97, v249
v_sub_f32 v34, v34, v249
v_sub_f32 v98, v98, v249
v_sub_f32 v35, v35, v249
v_sub_f32 v99, v99, v249
v_sub_f32 v36, v36, v249
v_sub_f32 v100, v100, v249
v_sub_f32 v37, v37, v249
v_sub_f32 v101, v101, v249
v_sub_f32 v38, v38, v249
v_sub_f32 v102, v102, v249
v_sub_f32 v39, v39, v249
v_sub_f32 v103, v103, v249
s_lshl_b64 exec, 0xffff, 0
v_mul_f32 v236, v236, v250
s_mov_b64 exec, -1
AR v254,a0
v_mul_f32 v254, v254, v250
AW a0,v254
AR v251,a1
v_mul_f32 v251, v251, v250
AW a1,v251
AR v254,a2
v_mul_f32 v254, v254, v250
AW a2,v254
AR v251,a3
v_mul_f32 v251, v251, v250
AW a3,v251
AR v254,a4
v_mul_f32 v254, v254, v250
AW a4,v254
AR v251,a5
v_mul_f32 v251, v251, v250
AW a5,v251
AR v254,a6
v_mul_f32 v254, v254, v250
AW a6,v254
AR v251,a7
v_mul_f32 v251, v251, v250
AW a7,v251
AR v254,a32
v_mul_f32 v254, v254, v250
AW a32,v254
AR v251,a33
v_mul_f32 v251, v251, v250
AW a33,v251
AR v254,a34
v_mul_f32 v254, v254, v250
AW a34,v254
AR v251,a35
v_mul_f32 v251, v251, v250
AW a35,v251
AR v254,a36
v_mul_f32 v254, v254, v250
AW a36,v254
AR v251,a37
v_mul_f32 v251, v251, v250
AW a37,v251
AR v254,a38
v_mul_f32 v254, v254, v250
AW a38,v254
AR v251,a39
v_mul_f32 v251, v251, v250
AW a39,v251
AR v254,a64
v_mul_f32 v254, v254, v250
AW a64,v254
AR v251,a65
v_mul_f32 v251, v251, v250
AW a65,v251
AR v254,a66
v_mul_f32 v254, v254, v250
AW a66,v254
AR v251,a67
v_mul_f32 v251, v251, v250
AW a67,v251
AR v254,a68
v_mul_f32 v254, v254, v250
AW a68,v254
AR v251,a69
v_mul_f32 v251, v251, v250
AW a69,v251
AR v254,a70
v_mul_f32 v254, v254, v250
AW a70,v254
AR v251,a71
v_mul_f32 v251, v251, v250
AW a71,v251
AR v254,a96
v_mul_f32 v254, v254, v250
AW a96,v254
AR v251,a97
v_mul_f32 v251, v251, v250
AW a97,v251
AR v254,a98
v_mul_f32 v254, v254, v250
AW a98,v254
AR v251,a99
v_mul_f32 v251, v251, v250
AW a99,v251
AR v254,a100
v_mul_f32 v254, v254, v250
AW a100,v254
AR v251,a101
v_mul_f32 v251, v251, v250
AW a101,v251
AR v254,a102
v_mul_f32 v254, v254, v250
AW a102,v254
AR v251,a103
v_mul_f32 v251, v251, v250
AW a103,v251
v_max3_f32 v249, v72, v73, v74
v_max3_f32 v251, v104, v105, v106
v_max3_f32 v249, v249, v75, v76
v_max3_f32 v251, v251, v107, v108
v_max3_f32 v249, v249, v77, v78
v_max3_f32 v251, v251, v109, v110
v_max3_f32 v249, v249, v79, v111
v_max_f32 v249, v249, v251
v_log_f32 v250, v246
s_lshl_b64 exec, 0xffff, 16
v_max_f32 v249, v249, v250
s_mov_b64 exec, -1
v_mov_b32 v251, v249
s_nop 1
v_permlane16_swap_b32 v249, v251
v_max_f32 v249, v249, v251
v_mov_b32 v251, v249
s_nop 1
v_permlane32_swap_b32 v249, v251
v_max_f32 v249, v249, v251
v_max_f32 v249, 0, v249
v_sub_f32 v250, 0, v249
EX v250,v250
v_sub_f32 v132, v132, v249
v_sub_f32 v133, v133, v249
v_sub_f32 v134, v134, v249
v_sub_f32 v135, v135, v249
v_sub_f32 v8, v8, v249
v_sub_f32 v72, v72, v249
v_sub_f32 v9, v9, v249
v_sub_f32 v73, v73, v249
v_sub_f32 v10, v10, v249
v_sub_f32 v74, v74, v249
v_sub_f32 v11, v11, v249
v_sub_f32 v75, v75, v249
v_sub_f32 v12, v12, v249
v_sub_f32 v76, v76, v249
v_sub_f32 v13, v13, v249
v_sub_f32 v77, v77, v249
v_sub_f32 v14, v14, v249
v_sub_f32 v78, v78, v249
v_sub_f32 v15, v15, v249
v_sub_f32 v79, v79, v249
v_sub_f32 v40, v40, v249
v_sub_f32 v104, v104, v249
v_sub_f32 v41, v41, v249
v_sub_f32 v105, v105, v249
v_sub_f32 v42, v42, v249
v_sub_f32 v106, v106, v249
v_sub_f32 v43, v43, v249
v_sub_f32 v107, v107, v249
v_sub_f32 v44, v44, v249
v_sub_f32 v108, v108, v249
v_sub_f32 v45, v45, v249
v_sub_f32 v109, v109, v249
v_sub_f32 v46, v46, v249
v_sub_f32 v110, v110, v249
v_sub_f32 v47, v47, v249
v_sub_f32 v111, v111, v249
s_lshl_b64 exec, 0xffff, 16
v_mul_f32 v236, v236, v250
s_mov_b64 exec, -1
AR v254,a8
v_mul_f32 v254, v254, v250
AW a8,v254
AR v251,a9
v_mul_f32 v251, v251, v250
AW a9,v251
AR v254,a10
v_mul_f32 v254, v254, v250
AW a10,v254
AR v251,a11
v_mul_f32 v251, v251, v250
AW a11,v251
AR v254,a12
v_mul_f32 v254, v254, v250
AW a12,v254
AR v251,a13
v_mul_f32 v251, v251, v250
AW a13,v251
AR v254,a14
v_mul_f32 v254, v254, v250
AW a14,v254
AR v251,a15
v_mul_f32 v251, v251, v250
AW a15,v251
AR v254,a40
v_mul_f32 v254, v254, v250
AW a40,v254
AR v251,a41
v_mul_f32 v251, v251, v250
AW a41,v251
AR v254,a42
v_mul_f32 v254, v254, v250
AW a42,v254
AR v251,a43
v_mul_f32 v251, v251, v250
AW a43,v251
AR v254,a44
v_mul_f32 v254, v254, v250
AW a44,v254
AR v251,a45
v_mul_f32 v251, v251, v250
AW a45,v251
AR v254,a46
v_mul_f32 v254, v254, v250
AW a46,v254
AR v251,a47
v_mul_f32 v251, v251, v250
AW a47,v251
AR v254,a72
v_mul_f32 v254, v254, v250
AW a72,v254
AR v251,a73
v_mul_f32 v251, v251, v250
AW a73,v251
AR v254,a74
v_mul_f32 v254, v254, v250
AW a74,v254
AR v251,a75
v_mul_f32 v251, v251, v250
AW a75,v251
AR v254,a76
v_mul_f32 v254, v254, v250
AW a76,v254
AR v251,a77
v_mul_f32 v251, v251, v250
AW a77,v251
AR v254,a78
v_mul_f32 v254, v254, v250
AW a78,v254
AR v251,a79
v_mul_f32 v251, v251, v250
AW a79,v251
AR v254,a104
v_mul_f32 v254, v254, v250
AW a104,v254
AR v251,a105
v_mul_f32 v251, v251, v250
AW a105,v251
AR v254,a106
v_mul_f32 v254, v254, v250
AW a106,v254
AR v251,a107
v_mul_f32 v251, v251, v250
AW a107,v251
AR v254,a108
v_mul_f32 v254, v254, v250
AW a108,v254
AR v251,a109
v_mul_f32 v251, v251, v250
AW a109,v251
AR v254,a110
v_mul_f32 v254, v254, v250
AW a110,v254
AR v251,a111
v_mul_f32 v251, v251, v250
AW a111,v251
EX v64,v64
EX v65,v65
EX v66,v66
EX v67,v67
EX v68,v68
EX v69,v69
EX v70,v70
EX v71,v71
EX v72,v72
EX v73,v73
EX v74,v74
EX v75,v75
EX v76,v76
EX v77,v77
EX v78,v78
EX v79,v79
EX v96,v96
EX v97,v97
EX v98,v98
EX v99,v99
EX v100,v100
EX v101,v101
EX v102,v102
EX v103,v103
EX v104,v104
EX v105,v105
EX v106,v106
EX v107,v107
EX v108,v108
EX v109,v109
EX v110,v110
EX v111,v111
CV v64,v64,v65
CV v65,v66,v67
CV v66,v68,v69
CV v67,v70,v71
CV v72,v72,v73
CV v73,v74,v75
CV v74,v76,v77
CV v75,v78,v79
CV v68,v96,v97
CV v69,v98,v99
CV v70,v100,v101
CV v71,v102,v103
CV v76,v104,v105
CV v77,v106,v107
CV v78,v108,v109
CV v79,v110,v111
v_smfmac_f32_16x16x64_bf16 v[236:239], v[144:147], v[64:71], v234
s_nop 0
v_smfmac_f32_16x16x64_bf16 v[236:239], v[148:151], v[72:79], v234
s_nop 9
v_add_f32 v247, v236, v237
s_cmp_eq_u32 s73, 1
s_cbranch_scc1 LBACK1_%=
s_endpgm
LSITE3_%=:
s_mov_b32 s73, 3
s_mov_b32 s75, 0
s_mov_b32 s74, 49152
s_branch LRARE104_%=
LSITE5_%=:
s_mov_b32 s73, 5
s_mov_b32 s75, 32768
s_mov_b32 s74, 16384
s_branch LRARE104_%=
LSITE7_%=:
s_mov_b32 s73, 7
s_mov_b32 s75, 0
s_mov_b32 s74, 49152
s_branch LRARE104_%=
LSITE9_%=:
s_mov_b32 s73, 9
s_mov_b32 s75, 32768
s_mov_b32 s74, 16384
s_branch LRARE104_%=
LSITE11_%=:
s_mov_b32 s73, 11
s_mov_b32 s75, 0
s_mov_b32 s74, 49152
s_branch LRARE104_%=
LSITE13_%=:
s_mov_b32 s73, 13
s_mov_b32 s75, 32768
s_mov_b32 s74, 16384
s_branch LRARE104_%=
LSITE15_%=:
s_mov_b32 s73, 15
s_mov_b32 s75, 0
s_mov_b32 s74, 49152
s_branch LRARE104_%=
LSITE17_%=:
s_mov_b32 s73, 17
s_mov_b32 s75, 32768
s_mov_b32 s74, 16384
s_branch LRARE104_%=
LSITE19_%=:
s_mov_b32 s73, 19
s_mov_b32 s75, 0
s_mov_b32 s74, 49152
s_branch LRARE104_%=
LSITE21_%=:
s_mov_b32 s73, 21
s_mov_b32 s75, 32768
s_mov_b32 s74, 16384
s_branch LRARE104_%=
LSITE23_%=:
s_mov_b32 s73, 23
s_mov_b32 s75, 0
s_mov_b32 s74, 49152
s_branch LRARE104_%=
LSITE25_%=:
s_mov_b32 s73, 25
s_mov_b32 s75, 32768
s_mov_b32 s74, 16384
s_branch LRARE104_%=
LSITE27_%=:
s_mov_b32 s73, 27
s_mov_b32 s75, 0
s_mov_b32 s74, 49152
s_branch LRARE104_%=
LSITE29_%=:
s_mov_b32 s73, 29
s_mov_b32 s75, 32768
s_mov_b32 s74, 16384
s_branch LRARE104_%=
LSITE31_%=:
s_mov_b32 s73, 31
s_mov_b32 s75, 0
s_mov_b32 s74, 49152
s_branch LRARE104_%=
LRARE104_%=:
s_nop 15
s_waitcnt vmcnt(0) lgkmcnt(0)
v_add_u32 v249, s74, v224
RD a[192:195],v249 offset:0
RD a[208:211],v249 offset:4096
RD a[224:227],v249 offset:8192
RD a[240:243],v249 offset:12288
v_add_u32 v250, s74, v225
RD a[196:199],v250 offset:0
RD a[212:215],v250 offset:4096
RD a[228:231],v250 offset:8192
RD a[244:247],v250 offset:12288
v_add_u32 v251, s74, v226
RD a[200:203],v251 offset:0
RD a[216:219],v251 offset:4096
RD a[232:235],v251 offset:8192
RD a[248:251],v251 offset:12288
v_add_u32 v254, s74, v227
RD a[204:207],v254 offset:0
RD a[220:223],v254 offset:4096
RD a[236:239],v254 offset:8192
RD a[252:255],v254 offset:12288
s_waitcnt lgkmcnt(0)
MM v[64:67],a[192:195],v[160:163],v[128:131]
MM v[64:67],a[196:199],v[176:179],v[64:67]
MM v[64:67],a[200:203],v[192:195],v[64:67]
MM v[64:67],a[204:207],v[208:211],v[64:67]
MM v[68:71],a[220:223],v[208:211],v[128:131]
MM v[68:71],a[216:219],v[192:195],v[68:71]
MM v[68:71],a[212:215],v[176:179],v[68:71]
MM v[68:71],a[208:211],v[160:163],v[68:71]
MM v[76:79],a[208:211],v[164:167],v[132:135]
MM v[76:79],a[212:215],v[180:183],v[76:79]
MM v[76:79],a[216:219],v[196:199],v[76:79]
MM v[76:79],a[220:223],v[212:215],v[76:79]
MM v[72:75],a[204:207],v[212:215],v[132:135]
MM v[72:75],a[200:203],v[196:199],v[72:75]
MM v[72:75],a[196:199],v[180:183],v[72:75]
MM v[72:75],a[192:195],v[164:167],v[72:75]
MM v[80:83],a[192:195],v[168:171],v[136:139]
MM v[80:83],a[196:199],v[184:187],v[80:83]
MM v[80:83],a[200:203],v[200:203],v[80:83]
MM v[80:83],a[204:207],v[216:219],v[80:83]
MM v[84:87],a[220:223],v[216:219],v[136:139]
MM v[84:87],a[216:219],v[200:203],v[84:87]
MM v[84:87],a[212:215],v[184:187],v[84:87]
MM v[84:87],a[208:211],v[168:171],v[84:87]
MM v[92:95],a[208:211],v[172:175],v[140:143]
)ASM"
R"ASM(
MM v[92:95],a[212:215],v[188:191],v[92:95]
MM v[92:95],a[216:219],v[204:207],v[92:95]
MM v[92:95],a[220:223],v[220:223],v[92:95]
MM v[88:91],a[204:207],v[220:223],v[140:143]
MM v[88:91],a[200:203],v[204:207],v[88:91]
MM v[88:91],a[196:199],v[188:191],v[88:91]
MM v[88:91],a[192:195],v[172:175],v[88:91]
MM v[96:99],a[224:227],v[160:163],v[128:131]
MM v[96:99],a[228:231],v[176:179],v[96:99]
MM v[96:99],a[232:235],v[192:195],v[96:99]
MM v[96:99],a[236:239],v[208:211],v[96:99]
MM v[100:103],a[252:255],v[208:211],v[128:131]
MM v[100:103],a[248:251],v[192:195],v[100:103]
MM v[100:103],a[244:247],v[176:179],v[100:103]
MM v[100:103],a[240:243],v[160:163],v[100:103]
MM v[108:111],a[240:243],v[164:167],v[132:135]
MM v[108:111],a[244:247],v[180:183],v[108:111]
MM v[108:111],a[248:251],v[196:199],v[108:111]
MM v[108:111],a[252:255],v[212:215],v[108:111]
MM v[104:107],a[236:239],v[212:215],v[132:135]
MM v[104:107],a[232:235],v[196:199],v[104:107]
MM v[104:107],a[228:231],v[180:183],v[104:107]
MM v[104:107],a[224:227],v[164:167],v[104:107]
MM v[112:115],a[224:227],v[168:171],v[136:139]
MM v[112:115],a[228:231],v[184:187],v[112:115]
MM v[112:115],a[232:235],v[200:203],v[112:115]
MM v[112:115],a[236:239],v[216:219],v[112:115]
MM v[116:119],a[252:255],v[216:219],v[136:139]
MM v[116:119],a[248:251],v[200:203],v[116:119]
MM v[116:119],a[244:247],v[184:187],v[116:119]
MM v[116:119],a[240:243],v[168:171],v[116:119]
MM v[124:127],a[240:243],v[172:175],v[140:143]
MM v[124:127],a[244:247],v[188:191],v[124:127]
MM v[124:127],a[248:251],v[204:207],v[124:127]
MM v[124:127],a[252:255],v[220:223],v[124:127]
MM v[120:123],a[236:239],v[220:223],v[140:143]
MM v[120:123],a[232:235],v[204:207],v[120:123]
MM v[120:123],a[228:231],v[188:191],v[120:123]
MM v[120:123],a[224:227],v[172:175],v[120:123]
s_nop 15
v_add_u32 v249, s75, v224
RD a[192:195],v249 offset:0
RD a[208:211],v249 offset:4096
RD a[224:227],v249 offset:8192
RD a[240:243],v249 offset:12288
v_add_u32 v250, s75, v225
RD a[196:199],v250 offset:0
RD a[212:215],v250 offset:4096
RD a[228:231],v250 offset:8192
RD a[244:247],v250 offset:12288
v_add_u32 v251, s75, v226
RD a[200:203],v251 offset:0
RD a[216:219],v251 offset:4096
RD a[232:235],v251 offset:8192
RD a[248:251],v251 offset:12288
v_add_u32 v254, s75, v227
RD a[204:207],v254 offset:0
RD a[220:223],v254 offset:4096
RD a[236:239],v254 offset:8192
RD a[252:255],v254 offset:12288
s_waitcnt lgkmcnt(0)
v_mov_b32 v236, v246
v_mov_b32 v237, 0
v_max3_f32 v249, v64, v65, v66
v_max3_f32 v251, v96, v97, v98
v_max3_f32 v249, v249, v67, v68
v_max3_f32 v251, v251, v99, v100
v_max3_f32 v249, v249, v69, v70
v_max3_f32 v251, v251, v101, v102
v_max3_f32 v249, v249, v71, v103
v_max_f32 v249, v249, v251
v_log_f32 v250, v246
s_lshl_b64 exec, 0xffff, 0
v_max_f32 v249, v249, v250
s_mov_b64 exec, -1
v_mov_b32 v251, v249
s_nop 1
v_permlane16_swap_b32 v249, v251
v_max_f32 v249, v249, v251
v_mov_b32 v251, v249
s_nop 1
v_permlane32_swap_b32 v249, v251
v_max_f32 v249, v249, v251
v_max_f32 v249, 0, v249
v_sub_f32 v250, 0, v249
EX v250,v250
v_sub_f32 v128, v128, v249
v_sub_f32 v129, v129, v249
v_sub_f32 v130, v130, v249
v_sub_f32 v131, v131, v249
v_sub_f32 v0, v0, v249
v_sub_f32 v64, v64, v249
v_sub_f32 v1, v1, v249
v_sub_f32 v65, v65, v249
v_sub_f32 v2, v2, v249
v_sub_f32 v66, v66, v249
v_sub_f32 v3, v3, v249
v_sub_f32 v67, v67, v249
v_sub_f32 v4, v4, v249
v_sub_f32 v68, v68, v249
v_sub_f32 v5, v5, v249
v_sub_f32 v69, v69, v249
v_sub_f32 v6, v6, v249
v_sub_f32 v70, v70, v249
v_sub_f32 v7, v7, v249
v_sub_f32 v71, v71, v249
v_sub_f32 v32, v32, v249
v_sub_f32 v96, v96, v249
v_sub_f32 v33, v33, v249
v_sub_f32 v97, v97, v249
v_sub_f32 v34, v34, v249
v_sub_f32 v98, v98, v249
v_sub_f32 v35, v35, v249
v_sub_f32 v99, v99, v249
v_sub_f32 v36, v36, v249
v_sub_f32 v100, v100, v249
v_sub_f32 v37, v37, v249
v_sub_f32 v101, v101, v249
v_sub_f32 v38, v38, v249
v_sub_f32 v102, v102, v249
v_sub_f32 v39, v39, v249
v_sub_f32 v103, v103, v249
s_lshl_b64 exec, 0xffff, 0
v_mul_f32 v236, v236, v250
s_mov_b64 exec, -1
AR v254,a0
v_mul_f32 v254, v254, v250
AW a0,v254
AR v251,a1
v_mul_f32 v251, v251, v250
AW a1,v251
AR v254,a2
v_mul_f32 v254, v254, v250
AW a2,v254
AR v251,a3
v_mul_f32 v251, v251, v250
AW a3,v251
AR v254,a4
v_mul_f32 v254, v254, v250
AW a4,v254
AR v251,a5
v_mul_f32 v251, v251, v250
AW a5,v251
AR v254,a6
v_mul_f32 v254, v254, v250
AW a6,v254
AR v251,a7
v_mul_f32 v251, v251, v250
AW a7,v251
AR v254,a32
v_mul_f32 v254, v254, v250
AW a32,v254
AR v251,a33
v_mul_f32 v251, v251, v250
AW a33,v251
AR v254,a34
v_mul_f32 v254, v254, v250
AW a34,v254
AR v251,a35
v_mul_f32 v251, v251, v250
AW a35,v251
AR v254,a36
v_mul_f32 v254, v254, v250
AW a36,v254
AR v251,a37
v_mul_f32 v251, v251, v250
AW a37,v251
AR v254,a38
v_mul_f32 v254, v254, v250
AW a38,v254
AR v251,a39
v_mul_f32 v251, v251, v250
AW a39,v251
AR v254,a64
v_mul_f32 v254, v254, v250
AW a64,v254
AR v251,a65
v_mul_f32 v251, v251, v250
AW a65,v251
AR v254,a66
v_mul_f32 v254, v254, v250
AW a66,v254
AR v251,a67
v_mul_f32 v251, v251, v250
AW a67,v251
AR v254,a68
v_mul_f32 v254, v254, v250
AW a68,v254
AR v251,a69
v_mul_f32 v251, v251, v250
AW a69,v251
AR v254,a70
v_mul_f32 v254, v254, v250
AW a70,v254
AR v251,a71
v_mul_f32 v251, v251, v250
AW a71,v251
AR v254,a96
v_mul_f32 v254, v254, v250
AW a96,v254
AR v251,a97
v_mul_f32 v251, v251, v250
AW a97,v251
AR v254,a98
v_mul_f32 v254, v254, v250
AW a98,v254
AR v251,a99
v_mul_f32 v251, v251, v250
AW a99,v251
AR v254,a100
v_mul_f32 v254, v254, v250
AW a100,v254
AR v251,a101
v_mul_f32 v251, v251, v250
AW a101,v251
AR v254,a102
v_mul_f32 v254, v254, v250
AW a102,v254
AR v251,a103
v_mul_f32 v251, v251, v250
AW a103,v251
v_max3_f32 v249, v72, v73, v74
v_max3_f32 v251, v104, v105, v106
v_max3_f32 v249, v249, v75, v76
v_max3_f32 v251, v251, v107, v108
v_max3_f32 v249, v249, v77, v78
v_max3_f32 v251, v251, v109, v110
v_max3_f32 v249, v249, v79, v111
v_max_f32 v249, v249, v251
v_log_f32 v250, v246
s_lshl_b64 exec, 0xffff, 16
v_max_f32 v249, v249, v250
s_mov_b64 exec, -1
v_mov_b32 v251, v249
s_nop 1
v_permlane16_swap_b32 v249, v251
v_max_f32 v249, v249, v251
v_mov_b32 v251, v249
s_nop 1
v_permlane32_swap_b32 v249, v251
v_max_f32 v249, v249, v251
v_max_f32 v249, 0, v249
v_sub_f32 v250, 0, v249
EX v250,v250
v_sub_f32 v132, v132, v249
v_sub_f32 v133, v133, v249
v_sub_f32 v134, v134, v249
v_sub_f32 v135, v135, v249
v_sub_f32 v8, v8, v249
v_sub_f32 v72, v72, v249
v_sub_f32 v9, v9, v249
v_sub_f32 v73, v73, v249
v_sub_f32 v10, v10, v249
v_sub_f32 v74, v74, v249
v_sub_f32 v11, v11, v249
v_sub_f32 v75, v75, v249
v_sub_f32 v12, v12, v249
v_sub_f32 v76, v76, v249
v_sub_f32 v13, v13, v249
v_sub_f32 v77, v77, v249
v_sub_f32 v14, v14, v249
v_sub_f32 v78, v78, v249
v_sub_f32 v15, v15, v249
v_sub_f32 v79, v79, v249
v_sub_f32 v40, v40, v249
v_sub_f32 v104, v104, v249
v_sub_f32 v41, v41, v249
v_sub_f32 v105, v105, v249
v_sub_f32 v42, v42, v249
v_sub_f32 v106, v106, v249
v_sub_f32 v43, v43, v249
v_sub_f32 v107, v107, v249
v_sub_f32 v44, v44, v249
v_sub_f32 v108, v108, v249
v_sub_f32 v45, v45, v249
v_sub_f32 v109, v109, v249
v_sub_f32 v46, v46, v249
v_sub_f32 v110, v110, v249
v_sub_f32 v47, v47, v249
v_sub_f32 v111, v111, v249
s_lshl_b64 exec, 0xffff, 16
v_mul_f32 v236, v236, v250
s_mov_b64 exec, -1
AR v254,a8
v_mul_f32 v254, v254, v250
AW a8,v254
AR v251,a9
v_mul_f32 v251, v251, v250
AW a9,v251
AR v254,a10
v_mul_f32 v254, v254, v250
AW a10,v254
AR v251,a11
v_mul_f32 v251, v251, v250
AW a11,v251
AR v254,a12
v_mul_f32 v254, v254, v250
AW a12,v254
AR v251,a13
v_mul_f32 v251, v251, v250
AW a13,v251
AR v254,a14
v_mul_f32 v254, v254, v250
AW a14,v254
AR v251,a15
v_mul_f32 v251, v251, v250
AW a15,v251
AR v254,a40
v_mul_f32 v254, v254, v250
AW a40,v254
AR v251,a41
v_mul_f32 v251, v251, v250
AW a41,v251
AR v254,a42
v_mul_f32 v254, v254, v250
AW a42,v254
AR v251,a43
v_mul_f32 v251, v251, v250
AW a43,v251
AR v254,a44
v_mul_f32 v254, v254, v250
AW a44,v254
AR v251,a45
v_mul_f32 v251, v251, v250
AW a45,v251
AR v254,a46
v_mul_f32 v254, v254, v250
AW a46,v254
AR v251,a47
v_mul_f32 v251, v251, v250
AW a47,v251
AR v254,a72
v_mul_f32 v254, v254, v250
AW a72,v254
AR v251,a73
v_mul_f32 v251, v251, v250
AW a73,v251
AR v254,a74
v_mul_f32 v254, v254, v250
AW a74,v254
AR v251,a75
v_mul_f32 v251, v251, v250
AW a75,v251
AR v254,a76
v_mul_f32 v254, v254, v250
AW a76,v254
AR v251,a77
v_mul_f32 v251, v251, v250
AW a77,v251
AR v254,a78
v_mul_f32 v254, v254, v250
AW a78,v254
AR v251,a79
v_mul_f32 v251, v251, v250
AW a79,v251
AR v254,a104
v_mul_f32 v254, v254, v250
AW a104,v254
AR v251,a105
v_mul_f32 v251, v251, v250
AW a105,v251
AR v254,a106
v_mul_f32 v254, v254, v250
AW a106,v254
AR v251,a107
v_mul_f32 v251, v251, v250
AW a107,v251
AR v254,a108
v_mul_f32 v254, v254, v250
AW a108,v254
AR v251,a109
v_mul_f32 v251, v251, v250
AW a109,v251
AR v254,a110
v_mul_f32 v254, v254, v250
AW a110,v254
AR v251,a111
v_mul_f32 v251, v251, v250
AW a111,v251
v_max3_f32 v249, v80, v81, v82
v_max3_f32 v251, v112, v113, v114
v_max3_f32 v249, v249, v83, v84
v_max3_f32 v251, v251, v115, v116
v_max3_f32 v249, v249, v85, v86
v_max3_f32 v251, v251, v117, v118
v_max3_f32 v249, v249, v87, v119
v_max_f32 v249, v249, v251
v_log_f32 v250, v246
s_lshl_b64 exec, 0xffff, 32
v_max_f32 v249, v249, v250
s_mov_b64 exec, -1
v_mov_b32 v251, v249
s_nop 1
v_permlane16_swap_b32 v249, v251
v_max_f32 v249, v249, v251
v_mov_b32 v251, v249
s_nop 1
v_permlane32_swap_b32 v249, v251
v_max_f32 v249, v249, v251
v_max_f32 v249, 0, v249
v_sub_f32 v250, 0, v249
EX v250,v250
v_sub_f32 v136, v136, v249
v_sub_f32 v137, v137, v249
v_sub_f32 v138, v138, v249
v_sub_f32 v139, v139, v249
v_sub_f32 v16, v16, v249
v_sub_f32 v80, v80, v249
v_sub_f32 v17, v17, v249
v_sub_f32 v81, v81, v249
v_sub_f32 v18, v18, v249
v_sub_f32 v82, v82, v249
v_sub_f32 v19, v19, v249
v_sub_f32 v83, v83, v249
v_sub_f32 v20, v20, v249
v_sub_f32 v84, v84, v249
v_sub_f32 v21, v21, v249
v_sub_f32 v85, v85, v249
v_sub_f32 v22, v22, v249
v_sub_f32 v86, v86, v249
v_sub_f32 v23, v23, v249
v_sub_f32 v87, v87, v249
v_sub_f32 v48, v48, v249
v_sub_f32 v112, v112, v249
v_sub_f32 v49, v49, v249
v_sub_f32 v113, v113, v249
v_sub_f32 v50, v50, v249
v_sub_f32 v114, v114, v249
v_sub_f32 v51, v51, v249
v_sub_f32 v115, v115, v249
v_sub_f32 v52, v52, v249
v_sub_f32 v116, v116, v249
v_sub_f32 v53, v53, v249
v_sub_f32 v117, v117, v249
v_sub_f32 v54, v54, v249
v_sub_f32 v118, v118, v249
v_sub_f32 v55, v55, v249
v_sub_f32 v119, v119, v249
s_lshl_b64 exec, 0xffff, 32
v_mul_f32 v236, v236, v250
s_mov_b64 exec, -1
AR v254,a16
v_mul_f32 v254, v254, v250
AW a16,v254
AR v251,a17
v_mul_f32 v251, v251, v250
AW a17,v251
AR v254,a18
v_mul_f32 v254, v254, v250
AW a18,v254
AR v251,a19
v_mul_f32 v251, v251, v250
AW a19,v251
AR v254,a20
v_mul_f32 v254, v254, v250
AW a20,v254
AR v251,a21
v_mul_f32 v251, v251, v250
AW a21,v251
AR v254,a22
v_mul_f32 v254, v254, v250
AW a22,v254
AR v251,a23
v_mul_f32 v251, v251, v250
AW a23,v251
AR v254,a48
v_mul_f32 v254, v254, v250
AW a48,v254
AR v251,a49
v_mul_f32 v251, v251, v250
AW a49,v251
AR v254,a50
v_mul_f32 v254, v254, v250
AW a50,v254
AR v251,a51
v_mul_f32 v251, v251, v250
AW a51,v251
AR v254,a52
v_mul_f32 v254, v254, v250
AW a52,v254
AR v251,a53
v_mul_f32 v251, v251, v250
AW a53,v251
AR v254,a54
v_mul_f32 v254, v254, v250
AW a54,v254
AR v251,a55
v_mul_f32 v251, v251, v250
AW a55,v251
AR v254,a80
v_mul_f32 v254, v254, v250
AW a80,v254
AR v251,a81
v_mul_f32 v251, v251, v250
AW a81,v251
AR v254,a82
v_mul_f32 v254, v254, v250
AW a82,v254
AR v251,a83
v_mul_f32 v251, v251, v250
AW a83,v251
AR v254,a84
v_mul_f32 v254, v254, v250
AW a84,v254
AR v251,a85
v_mul_f32 v251, v251, v250
AW a85,v251
AR v254,a86
v_mul_f32 v254, v254, v250
AW a86,v254
AR v251,a87
v_mul_f32 v251, v251, v250
AW a87,v251
AR v254,a112
v_mul_f32 v254, v254, v250
AW a112,v254
AR v251,a113
v_mul_f32 v251, v251, v250
AW a113,v251
AR v254,a114
v_mul_f32 v254, v254, v250
AW a114,v254
AR v251,a115
v_mul_f32 v251, v251, v250
AW a115,v251
AR v254,a116
v_mul_f32 v254, v254, v250
AW a116,v254
AR v251,a117
v_mul_f32 v251, v251, v250
AW a117,v251
AR v254,a118
)ASM"
R"ASM(
v_mul_f32 v254, v254, v250
AW a118,v254
AR v251,a119
v_mul_f32 v251, v251, v250
AW a119,v251
v_max3_f32 v249, v88, v89, v90
v_max3_f32 v251, v120, v121, v122
v_max3_f32 v249, v249, v91, v92
v_max3_f32 v251, v251, v123, v124
v_max3_f32 v249, v249, v93, v94
v_max3_f32 v251, v251, v125, v126
v_max3_f32 v249, v249, v95, v127
v_max_f32 v249, v249, v251
v_log_f32 v250, v246
s_lshl_b64 exec, 0xffff, 48
v_max_f32 v249, v249, v250
s_mov_b64 exec, -1
v_mov_b32 v251, v249
s_nop 1
v_permlane16_swap_b32 v249, v251
v_max_f32 v249, v249, v251
v_mov_b32 v251, v249
s_nop 1
v_permlane32_swap_b32 v249, v251
v_max_f32 v249, v249, v251
v_max_f32 v249, 0, v249
v_sub_f32 v250, 0, v249
EX v250,v250
v_sub_f32 v140, v140, v249
v_sub_f32 v141, v141, v249
v_sub_f32 v142, v142, v249
v_sub_f32 v143, v143, v249
v_sub_f32 v24, v24, v249
v_sub_f32 v88, v88, v249
v_sub_f32 v25, v25, v249
v_sub_f32 v89, v89, v249
v_sub_f32 v26, v26, v249
v_sub_f32 v90, v90, v249
v_sub_f32 v27, v27, v249
v_sub_f32 v91, v91, v249
v_sub_f32 v28, v28, v249
v_sub_f32 v92, v92, v249
v_sub_f32 v29, v29, v249
v_sub_f32 v93, v93, v249
v_sub_f32 v30, v30, v249
v_sub_f32 v94, v94, v249
v_sub_f32 v31, v31, v249
v_sub_f32 v95, v95, v249
v_sub_f32 v56, v56, v249
v_sub_f32 v120, v120, v249
v_sub_f32 v57, v57, v249
v_sub_f32 v121, v121, v249
v_sub_f32 v58, v58, v249
v_sub_f32 v122, v122, v249
v_sub_f32 v59, v59, v249
v_sub_f32 v123, v123, v249
v_sub_f32 v60, v60, v249
v_sub_f32 v124, v124, v249
v_sub_f32 v61, v61, v249
v_sub_f32 v125, v125, v249
v_sub_f32 v62, v62, v249
v_sub_f32 v126, v126, v249
v_sub_f32 v63, v63, v249
v_sub_f32 v127, v127, v249
s_lshl_b64 exec, 0xffff, 48
v_mul_f32 v236, v236, v250
s_mov_b64 exec, -1
AR v254,a24
v_mul_f32 v254, v254, v250
AW a24,v254
AR v251,a25
v_mul_f32 v251, v251, v250
AW a25,v251
AR v254,a26
v_mul_f32 v254, v254, v250
AW a26,v254
AR v251,a27
v_mul_f32 v251, v251, v250
AW a27,v251
AR v254,a28
v_mul_f32 v254, v254, v250
AW a28,v254
AR v251,a29
v_mul_f32 v251, v251, v250
AW a29,v251
AR v254,a30
v_mul_f32 v254, v254, v250
AW a30,v254
AR v251,a31
v_mul_f32 v251, v251, v250
AW a31,v251
AR v254,a56
v_mul_f32 v254, v254, v250
AW a56,v254
AR v251,a57
v_mul_f32 v251, v251, v250
AW a57,v251
AR v254,a58
v_mul_f32 v254, v254, v250
AW a58,v254
AR v251,a59
v_mul_f32 v251, v251, v250
AW a59,v251
AR v254,a60
v_mul_f32 v254, v254, v250
AW a60,v254
AR v251,a61
v_mul_f32 v251, v251, v250
AW a61,v251
AR v254,a62
v_mul_f32 v254, v254, v250
AW a62,v254
AR v251,a63
v_mul_f32 v251, v251, v250
AW a63,v251
AR v254,a88
v_mul_f32 v254, v254, v250
AW a88,v254
AR v251,a89
v_mul_f32 v251, v251, v250
AW a89,v251
AR v254,a90
v_mul_f32 v254, v254, v250
AW a90,v254
AR v251,a91
v_mul_f32 v251, v251, v250
AW a91,v251
AR v254,a92
v_mul_f32 v254, v254, v250
AW a92,v254
AR v251,a93
v_mul_f32 v251, v251, v250
AW a93,v251
AR v254,a94
v_mul_f32 v254, v254, v250
AW a94,v254
AR v251,a95
v_mul_f32 v251, v251, v250
AW a95,v251
AR v254,a120
v_mul_f32 v254, v254, v250
AW a120,v254
AR v251,a121
v_mul_f32 v251, v251, v250
AW a121,v251
AR v254,a122
v_mul_f32 v254, v254, v250
AW a122,v254
AR v251,a123
v_mul_f32 v251, v251, v250
AW a123,v251
AR v254,a124
v_mul_f32 v254, v254, v250
AW a124,v254
AR v251,a125
v_mul_f32 v251, v251, v250
AW a125,v251
AR v254,a126
v_mul_f32 v254, v254, v250
AW a126,v254
AR v251,a127
v_mul_f32 v251, v251, v250
AW a127,v251
EX v64,v64
EX v65,v65
EX v66,v66
EX v67,v67
EX v68,v68
EX v69,v69
EX v70,v70
EX v71,v71
EX v72,v72
EX v73,v73
EX v74,v74
EX v75,v75
EX v76,v76
EX v77,v77
EX v78,v78
EX v79,v79
EX v80,v80
EX v81,v81
EX v82,v82
EX v83,v83
EX v84,v84
EX v85,v85
EX v86,v86
EX v87,v87
EX v88,v88
EX v89,v89
EX v90,v90
EX v91,v91
EX v92,v92
EX v93,v93
EX v94,v94
EX v95,v95
EX v96,v96
EX v97,v97
EX v98,v98
EX v99,v99
EX v100,v100
EX v101,v101
EX v102,v102
EX v103,v103
EX v104,v104
EX v105,v105
EX v106,v106
EX v107,v107
EX v108,v108
EX v109,v109
EX v110,v110
EX v111,v111
EX v112,v112
EX v113,v113
EX v114,v114
EX v115,v115
EX v116,v116
EX v117,v117
EX v118,v118
EX v119,v119
EX v120,v120
EX v121,v121
EX v122,v122
EX v123,v123
EX v124,v124
EX v125,v125
EX v126,v126
EX v127,v127
CV v64,v64,v65
CV v65,v66,v67
CV v66,v68,v69
CV v67,v70,v71
CV v72,v72,v73
CV v73,v74,v75
CV v74,v76,v77
CV v75,v78,v79
CV v80,v80,v81
CV v81,v82,v83
CV v82,v84,v85
CV v83,v86,v87
CV v88,v88,v89
CV v89,v90,v91
CV v90,v92,v93
CV v91,v94,v95
CV v68,v96,v97
CV v69,v98,v99
CV v70,v100,v101
CV v71,v102,v103
CV v76,v104,v105
CV v77,v106,v107
CV v78,v108,v109
CV v79,v110,v111
CV v84,v112,v113
CV v85,v114,v115
CV v86,v116,v117
CV v87,v118,v119
CV v92,v120,v121
CV v93,v122,v123
CV v94,v124,v125
CV v95,v126,v127
v_smfmac_f32_16x16x64_bf16 v[236:239], v[144:147], v[64:71], v234
v_smfmac_f32_16x16x64_bf16 v[236:239], v[148:151], v[72:79], v234
v_smfmac_f32_16x16x64_bf16 v[236:239], v[152:155], v[80:87], v234
v_smfmac_f32_16x16x64_bf16 v[236:239], v[156:159], v[88:95], v234
s_nop 9
v_add_f32 v247, v236, v237
s_cmp_eq_u32 s73, 3
s_cbranch_scc1 LBACK3_%=
s_cmp_eq_u32 s73, 5
s_cbranch_scc1 LBACK5_%=
s_cmp_eq_u32 s73, 7
s_cbranch_scc1 LBACK7_%=
s_cmp_eq_u32 s73, 9
s_cbranch_scc1 LBACK9_%=
s_cmp_eq_u32 s73, 11
s_cbranch_scc1 LBACK11_%=
s_cmp_eq_u32 s73, 13
s_cbranch_scc1 LBACK13_%=
s_cmp_eq_u32 s73, 15
s_cbranch_scc1 LBACK15_%=
s_cmp_eq_u32 s73, 17
s_cbranch_scc1 LBACK17_%=
s_cmp_eq_u32 s73, 19
s_cbranch_scc1 LBACK19_%=
s_cmp_eq_u32 s73, 21
s_cbranch_scc1 LBACK21_%=
s_cmp_eq_u32 s73, 23
s_cbranch_scc1 LBACK23_%=
s_cmp_eq_u32 s73, 25
s_cbranch_scc1 LBACK25_%=
s_cmp_eq_u32 s73, 27
s_cbranch_scc1 LBACK27_%=
s_cmp_eq_u32 s73, 29
s_cbranch_scc1 LBACK29_%=
s_cmp_eq_u32 s73, 31
s_cbranch_scc1 LBACK31_%=
s_endpgm
LSITE4_%=:
s_mov_b32 s73, 4
s_mov_b32 s75, 16384
s_mov_b32 s74, 0
s_branch LRARE004_%=
LSITE6_%=:
s_mov_b32 s73, 6
s_mov_b32 s75, 49152
s_mov_b32 s74, 32768
s_branch LRARE004_%=
LSITE8_%=:
s_mov_b32 s73, 8
s_mov_b32 s75, 16384
s_mov_b32 s74, 0
s_branch LRARE004_%=
LSITE10_%=:
s_mov_b32 s73, 10
s_mov_b32 s75, 49152
s_mov_b32 s74, 32768
s_branch LRARE004_%=
LSITE12_%=:
s_mov_b32 s73, 12
s_mov_b32 s75, 16384
s_mov_b32 s74, 0
s_branch LRARE004_%=
LSITE14_%=:
s_mov_b32 s73, 14
s_mov_b32 s75, 49152
s_mov_b32 s74, 32768
s_branch LRARE004_%=
LSITE16_%=:
s_mov_b32 s73, 16
s_mov_b32 s75, 16384
s_mov_b32 s74, 0
s_branch LRARE004_%=
LSITE18_%=:
s_mov_b32 s73, 18
s_mov_b32 s75, 49152
s_mov_b32 s74, 32768
s_branch LRARE004_%=
LSITE20_%=:
s_mov_b32 s73, 20
s_mov_b32 s75, 16384
s_mov_b32 s74, 0
s_branch LRARE004_%=
LSITE22_%=:
s_mov_b32 s73, 22
s_mov_b32 s75, 49152
s_mov_b32 s74, 32768
s_branch LRARE004_%=
LSITE24_%=:
s_mov_b32 s73, 24
s_mov_b32 s75, 16384
s_mov_b32 s74, 0
s_branch LRARE004_%=
LSITE26_%=:
s_mov_b32 s73, 26
s_mov_b32 s75, 49152
s_mov_b32 s74, 32768
s_branch LRARE004_%=
LSITE28_%=:
s_mov_b32 s73, 28
s_mov_b32 s75, 16384
s_mov_b32 s74, 0
s_branch LRARE004_%=
LSITE30_%=:
s_mov_b32 s73, 30
s_mov_b32 s75, 49152
s_mov_b32 s74, 32768
s_branch LRARE004_%=
LRARE004_%=:
s_nop 15
s_waitcnt vmcnt(0) lgkmcnt(0)
v_add_u32 v249, s74, v224
RD a[192:195],v249 offset:0
RD a[208:211],v249 offset:4096
RD a[224:227],v249 offset:8192
RD a[240:243],v249 offset:12288
v_add_u32 v250, s74, v225
RD a[196:199],v250 offset:0
RD a[212:215],v250 offset:4096
RD a[228:231],v250 offset:8192
RD a[244:247],v250 offset:12288
v_add_u32 v251, s74, v226
RD a[200:203],v251 offset:0
RD a[216:219],v251 offset:4096
RD a[232:235],v251 offset:8192
RD a[248:251],v251 offset:12288
v_add_u32 v254, s74, v227
RD a[204:207],v254 offset:0
RD a[220:223],v254 offset:4096
RD a[236:239],v254 offset:8192
RD a[252:255],v254 offset:12288
s_waitcnt lgkmcnt(0)
MM v[0:3],a[192:195],v[160:163],v[128:131]
MM v[0:3],a[196:199],v[176:179],v[0:3]
MM v[0:3],a[200:203],v[192:195],v[0:3]
MM v[0:3],a[204:207],v[208:211],v[0:3]
MM v[4:7],a[220:223],v[208:211],v[128:131]
MM v[4:7],a[216:219],v[192:195],v[4:7]
MM v[4:7],a[212:215],v[176:179],v[4:7]
MM v[4:7],a[208:211],v[160:163],v[4:7]
MM v[12:15],a[208:211],v[164:167],v[132:135]
MM v[12:15],a[212:215],v[180:183],v[12:15]
MM v[12:15],a[216:219],v[196:199],v[12:15]
MM v[12:15],a[220:223],v[212:215],v[12:15]
MM v[8:11],a[204:207],v[212:215],v[132:135]
MM v[8:11],a[200:203],v[196:199],v[8:11]
MM v[8:11],a[196:199],v[180:183],v[8:11]
MM v[8:11],a[192:195],v[164:167],v[8:11]
MM v[16:19],a[192:195],v[168:171],v[136:139]
MM v[16:19],a[196:199],v[184:187],v[16:19]
MM v[16:19],a[200:203],v[200:203],v[16:19]
MM v[16:19],a[204:207],v[216:219],v[16:19]
MM v[20:23],a[220:223],v[216:219],v[136:139]
MM v[20:23],a[216:219],v[200:203],v[20:23]
MM v[20:23],a[212:215],v[184:187],v[20:23]
MM v[20:23],a[208:211],v[168:171],v[20:23]
MM v[28:31],a[208:211],v[172:175],v[140:143]
MM v[28:31],a[212:215],v[188:191],v[28:31]
MM v[28:31],a[216:219],v[204:207],v[28:31]
MM v[28:31],a[220:223],v[220:223],v[28:31]
MM v[24:27],a[204:207],v[220:223],v[140:143]
MM v[24:27],a[200:203],v[204:207],v[24:27]
MM v[24:27],a[196:199],v[188:191],v[24:27]
MM v[24:27],a[192:195],v[172:175],v[24:27]
MM v[32:35],a[224:227],v[160:163],v[128:131]
MM v[32:35],a[228:231],v[176:179],v[32:35]
MM v[32:35],a[232:235],v[192:195],v[32:35]
MM v[32:35],a[236:239],v[208:211],v[32:35]
MM v[36:39],a[252:255],v[208:211],v[128:131]
MM v[36:39],a[248:251],v[192:195],v[36:39]
MM v[36:39],a[244:247],v[176:179],v[36:39]
MM v[36:39],a[240:243],v[160:163],v[36:39]
MM v[44:47],a[240:243],v[164:167],v[132:135]
MM v[44:47],a[244:247],v[180:183],v[44:47]
MM v[44:47],a[248:251],v[196:199],v[44:47]
MM v[44:47],a[252:255],v[212:215],v[44:47]
MM v[40:43],a[236:239],v[212:215],v[132:135]
MM v[40:43],a[232:235],v[196:199],v[40:43]
MM v[40:43],a[228:231],v[180:183],v[40:43]
MM v[40:43],a[224:227],v[164:167],v[40:43]
MM v[48:51],a[224:227],v[168:171],v[136:139]
MM v[48:51],a[228:231],v[184:187],v[48:51]
MM v[48:51],a[232:235],v[200:203],v[48:51]
MM v[48:51],a[236:239],v[216:219],v[48:51]
MM v[52:55],a[252:255],v[216:219],v[136:139]
MM v[52:55],a[248:251],v[200:203],v[52:55]
MM v[52:55],a[244:247],v[184:187],v[52:55]
MM v[52:55],a[240:243],v[168:171],v[52:55]
MM v[60:63],a[240:243],v[172:175],v[140:143]
MM v[60:63],a[244:247],v[188:191],v[60:63]
MM v[60:63],a[248:251],v[204:207],v[60:63]
MM v[60:63],a[252:255],v[220:223],v[60:63]
MM v[56:59],a[236:239],v[220:223],v[140:143]
MM v[56:59],a[232:235],v[204:207],v[56:59]
MM v[56:59],a[228:231],v[188:191],v[56:59]
MM v[56:59],a[224:227],v[172:175],v[56:59]
s_nop 15
v_add_u32 v249, s75, v224
RD a[192:195],v249 offset:0
RD a[208:211],v249 offset:4096
RD a[224:227],v249 offset:8192
RD a[240:243],v249 offset:12288
v_add_u32 v250, s75, v225
RD a[196:199],v250 offset:0
RD a[212:215],v250 offset:4096
RD a[228:231],v250 offset:8192
RD a[244:247],v250 offset:12288
v_add_u32 v251, s75, v226
RD a[200:203],v251 offset:0
RD a[216:219],v251 offset:4096
RD a[232:235],v251 offset:8192
RD a[248:251],v251 offset:12288
v_add_u32 v254, s75, v227
RD a[204:207],v254 offset:0
RD a[220:223],v254 offset:4096
RD a[236:239],v254 offset:8192
RD a[252:255],v254 offset:12288
s_waitcnt lgkmcnt(0)
v_mov_b32 v236, v247
v_mov_b32 v237, 0
v_max3_f32 v249, v0, v1, v2
v_max3_f32 v251, v32, v33, v34
v_max3_f32 v249, v249, v3, v4
v_max3_f32 v251, v251, v35, v36
v_max3_f32 v249, v249, v5, v6
v_max3_f32 v251, v251, v37, v38
v_max3_f32 v249, v249, v7, v39
v_max_f32 v249, v249, v251
v_log_f32 v250, v247
s_lshl_b64 exec, 0xffff, 0
v_max_f32 v249, v249, v250
s_mov_b64 exec, -1
v_mov_b32 v251, v249
s_nop 1
v_permlane16_swap_b32 v249, v251
v_max_f32 v249, v249, v251
v_mov_b32 v251, v249
s_nop 1
v_permlane32_swap_b32 v249, v251
v_max_f32 v249, v249, v251
v_max_f32 v249, 0, v249
v_sub_f32 v250, 0, v249
EX v250,v250
v_sub_f32 v128, v128, v249
v_sub_f32 v129, v129, v249
v_sub_f32 v130, v130, v249
v_sub_f32 v131, v131, v249
v_sub_f32 v64, v64, v249
v_sub_f32 v0, v0, v249
v_sub_f32 v65, v65, v249
v_sub_f32 v1, v1, v249
v_sub_f32 v66, v66, v249
v_sub_f32 v2, v2, v249
v_sub_f32 v67, v67, v249
v_sub_f32 v3, v3, v249
v_sub_f32 v68, v68, v249
v_sub_f32 v4, v4, v249
v_sub_f32 v69, v69, v249
v_sub_f32 v5, v5, v249
v_sub_f32 v70, v70, v249
v_sub_f32 v6, v6, v249
v_sub_f32 v71, v71, v249
v_sub_f32 v7, v7, v249
)ASM"
R"ASM(
v_sub_f32 v96, v96, v249
v_sub_f32 v32, v32, v249
v_sub_f32 v97, v97, v249
v_sub_f32 v33, v33, v249
v_sub_f32 v98, v98, v249
v_sub_f32 v34, v34, v249
v_sub_f32 v99, v99, v249
v_sub_f32 v35, v35, v249
v_sub_f32 v100, v100, v249
v_sub_f32 v36, v36, v249
v_sub_f32 v101, v101, v249
v_sub_f32 v37, v37, v249
v_sub_f32 v102, v102, v249
v_sub_f32 v38, v38, v249
v_sub_f32 v103, v103, v249
v_sub_f32 v39, v39, v249
s_lshl_b64 exec, 0xffff, 0
v_mul_f32 v236, v236, v250
s_mov_b64 exec, -1
AR v254,a0
v_mul_f32 v254, v254, v250
AW a0,v254
AR v251,a1
v_mul_f32 v251, v251, v250
AW a1,v251
AR v254,a2
v_mul_f32 v254, v254, v250
AW a2,v254
AR v251,a3
v_mul_f32 v251, v251, v250
AW a3,v251
AR v254,a4
v_mul_f32 v254, v254, v250
AW a4,v254
AR v251,a5
v_mul_f32 v251, v251, v250
AW a5,v251
AR v254,a6
v_mul_f32 v254, v254, v250
AW a6,v254
AR v251,a7
v_mul_f32 v251, v251, v250
AW a7,v251
AR v254,a32
v_mul_f32 v254, v254, v250
AW a32,v254
AR v251,a33
v_mul_f32 v251, v251, v250
AW a33,v251
AR v254,a34
v_mul_f32 v254, v254, v250
AW a34,v254
AR v251,a35
v_mul_f32 v251, v251, v250
AW a35,v251
AR v254,a36
v_mul_f32 v254, v254, v250
AW a36,v254
AR v251,a37
v_mul_f32 v251, v251, v250
AW a37,v251
AR v254,a38
v_mul_f32 v254, v254, v250
AW a38,v254
AR v251,a39
v_mul_f32 v251, v251, v250
AW a39,v251
AR v254,a64
v_mul_f32 v254, v254, v250
AW a64,v254
AR v251,a65
v_mul_f32 v251, v251, v250
AW a65,v251
AR v254,a66
v_mul_f32 v254, v254, v250
AW a66,v254
AR v251,a67
v_mul_f32 v251, v251, v250
AW a67,v251
AR v254,a68
v_mul_f32 v254, v254, v250
AW a68,v254
AR v251,a69
v_mul_f32 v251, v251, v250
AW a69,v251
AR v254,a70
v_mul_f32 v254, v254, v250
AW a70,v254
AR v251,a71
v_mul_f32 v251, v251, v250
AW a71,v251
AR v254,a96
v_mul_f32 v254, v254, v250
AW a96,v254
AR v251,a97
v_mul_f32 v251, v251, v250
AW a97,v251
AR v254,a98
v_mul_f32 v254, v254, v250
AW a98,v254
AR v251,a99
v_mul_f32 v251, v251, v250
AW a99,v251
AR v254,a100
v_mul_f32 v254, v254, v250
AW a100,v254
AR v251,a101
v_mul_f32 v251, v251, v250
AW a101,v251
AR v254,a102
v_mul_f32 v254, v254, v250
AW a102,v254
AR v251,a103
v_mul_f32 v251, v251, v250
AW a103,v251
v_max3_f32 v249, v8, v9, v10
v_max3_f32 v251, v40, v41, v42
v_max3_f32 v249, v249, v11, v12
v_max3_f32 v251, v251, v43, v44
v_max3_f32 v249, v249, v13, v14
v_max3_f32 v251, v251, v45, v46
v_max3_f32 v249, v249, v15, v47
v_max_f32 v249, v249, v251
v_log_f32 v250, v247
s_lshl_b64 exec, 0xffff, 16
v_max_f32 v249, v249, v250
s_mov_b64 exec, -1
v_mov_b32 v251, v249
s_nop 1
v_permlane16_swap_b32 v249, v251
v_max_f32 v249, v249, v251
v_mov_b32 v251, v249
s_nop 1
v_permlane32_swap_b32 v249, v251
v_max_f32 v249, v249, v251
v_max_f32 v249, 0, v249
v_sub_f32 v250, 0, v249
EX v250,v250
v_sub_f32 v132, v132, v249
v_sub_f32 v133, v133, v249
v_sub_f32 v134, v134, v249
v_sub_f32 v135, v135, v249
v_sub_f32 v72, v72, v249
v_sub_f32 v8, v8, v249
v_sub_f32 v73, v73, v249
v_sub_f32 v9, v9, v249
v_sub_f32 v74, v74, v249
v_sub_f32 v10, v10, v249
v_sub_f32 v75, v75, v249
v_sub_f32 v11, v11, v249
v_sub_f32 v76, v76, v249
v_sub_f32 v12, v12, v249
v_sub_f32 v77, v77, v249
v_sub_f32 v13, v13, v249
v_sub_f32 v78, v78, v249
v_sub_f32 v14, v14, v249
v_sub_f32 v79, v79, v249
v_sub_f32 v15, v15, v249
v_sub_f32 v104, v104, v249
v_sub_f32 v40, v40, v249
v_sub_f32 v105, v105, v249
v_sub_f32 v41, v41, v249
v_sub_f32 v106, v106, v249
v_sub_f32 v42, v42, v249
v_sub_f32 v107, v107, v249
v_sub_f32 v43, v43, v249
v_sub_f32 v108, v108, v249
v_sub_f32 v44, v44, v249
v_sub_f32 v109, v109, v249
v_sub_f32 v45, v45, v249
v_sub_f32 v110, v110, v249
v_sub_f32 v46, v46, v249
v_sub_f32 v111, v111, v249
v_sub_f32 v47, v47, v249
s_lshl_b64 exec, 0xffff, 16
v_mul_f32 v236, v236, v250
s_mov_b64 exec, -1
AR v254,a8
v_mul_f32 v254, v254, v250
AW a8,v254
AR v251,a9
v_mul_f32 v251, v251, v250
AW a9,v251
AR v254,a10
v_mul_f32 v254, v254, v250
AW a10,v254
AR v251,a11
v_mul_f32 v251, v251, v250
AW a11,v251
AR v254,a12
v_mul_f32 v254, v254, v250
AW a12,v254
AR v251,a13
v_mul_f32 v251, v251, v250
AW a13,v251
AR v254,a14
v_mul_f32 v254, v254, v250
AW a14,v254
AR v251,a15
v_mul_f32 v251, v251, v250
AW a15,v251
AR v254,a40
v_mul_f32 v254, v254, v250
AW a40,v254
AR v251,a41
v_mul_f32 v251, v251, v250
AW a41,v251
AR v254,a42
v_mul_f32 v254, v254, v250
AW a42,v254
AR v251,a43
v_mul_f32 v251, v251, v250
AW a43,v251
AR v254,a44
v_mul_f32 v254, v254, v250
AW a44,v254
AR v251,a45
v_mul_f32 v251, v251, v250
AW a45,v251
AR v254,a46
v_mul_f32 v254, v254, v250
AW a46,v254
AR v251,a47
v_mul_f32 v251, v251, v250
AW a47,v251
AR v254,a72
v_mul_f32 v254, v254, v250
AW a72,v254
AR v251,a73
v_mul_f32 v251, v251, v250
AW a73,v251
AR v254,a74
v_mul_f32 v254, v254, v250
AW a74,v254
AR v251,a75
v_mul_f32 v251, v251, v250
AW a75,v251
AR v254,a76
v_mul_f32 v254, v254, v250
AW a76,v254
AR v251,a77
v_mul_f32 v251, v251, v250
AW a77,v251
AR v254,a78
v_mul_f32 v254, v254, v250
AW a78,v254
AR v251,a79
v_mul_f32 v251, v251, v250
AW a79,v251
AR v254,a104
v_mul_f32 v254, v254, v250
AW a104,v254
AR v251,a105
v_mul_f32 v251, v251, v250
AW a105,v251
AR v254,a106
v_mul_f32 v254, v254, v250
AW a106,v254
AR v251,a107
v_mul_f32 v251, v251, v250
AW a107,v251
AR v254,a108
v_mul_f32 v254, v254, v250
AW a108,v254
AR v251,a109
v_mul_f32 v251, v251, v250
AW a109,v251
AR v254,a110
v_mul_f32 v254, v254, v250
AW a110,v254
AR v251,a111
v_mul_f32 v251, v251, v250
AW a111,v251
v_max3_f32 v249, v16, v17, v18
v_max3_f32 v251, v48, v49, v50
v_max3_f32 v249, v249, v19, v20
v_max3_f32 v251, v251, v51, v52
v_max3_f32 v249, v249, v21, v22
v_max3_f32 v251, v251, v53, v54
v_max3_f32 v249, v249, v23, v55
v_max_f32 v249, v249, v251
v_log_f32 v250, v247
s_lshl_b64 exec, 0xffff, 32
v_max_f32 v249, v249, v250
s_mov_b64 exec, -1
v_mov_b32 v251, v249
s_nop 1
v_permlane16_swap_b32 v249, v251
v_max_f32 v249, v249, v251
v_mov_b32 v251, v249
s_nop 1
v_permlane32_swap_b32 v249, v251
v_max_f32 v249, v249, v251
v_max_f32 v249, 0, v249
v_sub_f32 v250, 0, v249
EX v250,v250
v_sub_f32 v136, v136, v249
v_sub_f32 v137, v137, v249
v_sub_f32 v138, v138, v249
v_sub_f32 v139, v139, v249
v_sub_f32 v80, v80, v249
v_sub_f32 v16, v16, v249
v_sub_f32 v81, v81, v249
v_sub_f32 v17, v17, v249
v_sub_f32 v82, v82, v249
v_sub_f32 v18, v18, v249
v_sub_f32 v83, v83, v249
v_sub_f32 v19, v19, v249
v_sub_f32 v84, v84, v249
v_sub_f32 v20, v20, v249
v_sub_f32 v85, v85, v249
v_sub_f32 v21, v21, v249
v_sub_f32 v86, v86, v249
v_sub_f32 v22, v22, v249
v_sub_f32 v87, v87, v249
v_sub_f32 v23, v23, v249
v_sub_f32 v112, v112, v249
v_sub_f32 v48, v48, v249
v_sub_f32 v113, v113, v249
v_sub_f32 v49, v49, v249
v_sub_f32 v114, v114, v249
v_sub_f32 v50, v50, v249
v_sub_f32 v115, v115, v249
v_sub_f32 v51, v51, v249
v_sub_f32 v116, v116, v249
v_sub_f32 v52, v52, v249
v_sub_f32 v117, v117, v249
v_sub_f32 v53, v53, v249
v_sub_f32 v118, v118, v249
v_sub_f32 v54, v54, v249
v_sub_f32 v119, v119, v249
v_sub_f32 v55, v55, v249
s_lshl_b64 exec, 0xffff, 32
v_mul_f32 v236, v236, v250
s_mov_b64 exec, -1
AR v254,a16
v_mul_f32 v254, v254, v250
AW a16,v254
AR v251,a17
v_mul_f32 v251, v251, v250
AW a17,v251
AR v254,a18
v_mul_f32 v254, v254, v250
AW a18,v254
AR v251,a19
v_mul_f32 v251, v251, v250
AW a19,v251
AR v254,a20
v_mul_f32 v254, v254, v250
AW a20,v254
AR v251,a21
v_mul_f32 v251, v251, v250
AW a21,v251
AR v254,a22
v_mul_f32 v254, v254, v250
AW a22,v254
AR v251,a23
v_mul_f32 v251, v251, v250
AW a23,v251
AR v254,a48
v_mul_f32 v254, v254, v250
AW a48,v254
AR v251,a49
v_mul_f32 v251, v251, v250
AW a49,v251
AR v254,a50
v_mul_f32 v254, v254, v250
AW a50,v254
AR v251,a51
v_mul_f32 v251, v251, v250
AW a51,v251
AR v254,a52
v_mul_f32 v254, v254, v250
AW a52,v254
AR v251,a53
v_mul_f32 v251, v251, v250
AW a53,v251
AR v254,a54
v_mul_f32 v254, v254, v250
AW a54,v254
AR v251,a55
v_mul_f32 v251, v251, v250
AW a55,v251
AR v254,a80
v_mul_f32 v254, v254, v250
AW a80,v254
AR v251,a81
v_mul_f32 v251, v251, v250
AW a81,v251
AR v254,a82
v_mul_f32 v254, v254, v250
AW a82,v254
AR v251,a83
v_mul_f32 v251, v251, v250
AW a83,v251
AR v254,a84
v_mul_f32 v254, v254, v250
AW a84,v254
AR v251,a85
v_mul_f32 v251, v251, v250
AW a85,v251
AR v254,a86
v_mul_f32 v254, v254, v250
AW a86,v254
AR v251,a87
v_mul_f32 v251, v251, v250
AW a87,v251
AR v254,a112
v_mul_f32 v254, v254, v250
AW a112,v254
AR v251,a113
v_mul_f32 v251, v251, v250
AW a113,v251
AR v254,a114
v_mul_f32 v254, v254, v250
AW a114,v254
AR v251,a115
v_mul_f32 v251, v251, v250
AW a115,v251
AR v254,a116
v_mul_f32 v254, v254, v250
AW a116,v254
AR v251,a117
v_mul_f32 v251, v251, v250
AW a117,v251
AR v254,a118
v_mul_f32 v254, v254, v250
AW a118,v254
AR v251,a119
v_mul_f32 v251, v251, v250
AW a119,v251
v_max3_f32 v249, v24, v25, v26
v_max3_f32 v251, v56, v57, v58
v_max3_f32 v249, v249, v27, v28
v_max3_f32 v251, v251, v59, v60
v_max3_f32 v249, v249, v29, v30
v_max3_f32 v251, v251, v61, v62
v_max3_f32 v249, v249, v31, v63
v_max_f32 v249, v249, v251
v_log_f32 v250, v247
s_lshl_b64 exec, 0xffff, 48
v_max_f32 v249, v249, v250
s_mov_b64 exec, -1
v_mov_b32 v251, v249
s_nop 1
v_permlane16_swap_b32 v249, v251
v_max_f32 v249, v249, v251
v_mov_b32 v251, v249
s_nop 1
v_permlane32_swap_b32 v249, v251
v_max_f32 v249, v249, v251
v_max_f32 v249, 0, v249
v_sub_f32 v250, 0, v249
EX v250,v250
v_sub_f32 v140, v140, v249
v_sub_f32 v141, v141, v249
v_sub_f32 v142, v142, v249
v_sub_f32 v143, v143, v249
v_sub_f32 v88, v88, v249
v_sub_f32 v24, v24, v249
v_sub_f32 v89, v89, v249
v_sub_f32 v25, v25, v249
v_sub_f32 v90, v90, v249
v_sub_f32 v26, v26, v249
v_sub_f32 v91, v91, v249
v_sub_f32 v27, v27, v249
v_sub_f32 v92, v92, v249
v_sub_f32 v28, v28, v249
v_sub_f32 v93, v93, v249
v_sub_f32 v29, v29, v249
v_sub_f32 v94, v94, v249
v_sub_f32 v30, v30, v249
v_sub_f32 v95, v95, v249
v_sub_f32 v31, v31, v249
v_sub_f32 v120, v120, v249
v_sub_f32 v56, v56, v249
v_sub_f32 v121, v121, v249
v_sub_f32 v57, v57, v249
v_sub_f32 v122, v122, v249
v_sub_f32 v58, v58, v249
v_sub_f32 v123, v123, v249
v_sub_f32 v59, v59, v249
v_sub_f32 v124, v124, v249
v_sub_f32 v60, v60, v249
v_sub_f32 v125, v125, v249
v_sub_f32 v61, v61, v249
v_sub_f32 v126, v126, v249
v_sub_f32 v62, v62, v249
v_sub_f32 v127, v127, v249
v_sub_f32 v63, v63, v249
s_lshl_b64 exec, 0xffff, 48
v_mul_f32 v236, v236, v250
s_mov_b64 exec, -1
AR v254,a24
v_mul_f32 v254, v254, v250
AW a24,v254
AR v251,a25
v_mul_f32 v251, v251, v250
AW a25,v251
AR v254,a26
v_mul_f32 v254, v254, v250
AW a26,v254
AR v251,a27
v_mul_f32 v251, v251, v250
AW a27,v251
AR v254,a28
v_mul_f32 v254, v254, v250
AW a28,v254
AR v251,a29
v_mul_f32 v251, v251, v250
AW a29,v251
AR v254,a30
v_mul_f32 v254, v254, v250
AW a30,v254
AR v251,a31
v_mul_f32 v251, v251, v250
AW a31,v251
AR v254,a56
v_mul_f32 v254, v254, v250
AW a56,v254
AR v251,a57
v_mul_f32 v251, v251, v250
AW a57,v251
AR v254,a58
v_mul_f32 v254, v254, v250
AW a58,v254
AR v251,a59
v_mul_f32 v251, v251, v250
AW a59,v251
AR v254,a60
v_mul_f32 v254, v254, v250
AW a60,v254
AR v251,a61
v_mul_f32 v251, v251, v250
AW a61,v251
AR v254,a62
v_mul_f32 v254, v254, v250
AW a62,v254
AR v251,a63
v_mul_f32 v251, v251, v250
AW a63,v251
AR v254,a88
v_mul_f32 v254, v254, v250
AW a88,v254
AR v251,a89
v_mul_f32 v251, v251, v250
AW a89,v251
AR v254,a90
v_mul_f32 v254, v254, v250
AW a90,v254
AR v251,a91
v_mul_f32 v251, v251, v250
AW a91,v251
AR v254,a92
v_mul_f32 v254, v254, v250
AW a92,v254
AR v251,a93
v_mul_f32 v251, v251, v250
AW a93,v251
AR v254,a94
v_mul_f32 v254, v254, v250
AW a94,v254
AR v251,a95
v_mul_f32 v251, v251, v250
AW a95,v251
AR v254,a120
v_mul_f32 v254, v254, v250
AW a120,v254
AR v251,a121
v_mul_f32 v251, v251, v250
AW a121,v251
AR v254,a122
v_mul_f32 v254, v254, v250
AW a122,v254
AR v251,a123
v_mul_f32 v251, v251, v250
AW a123,v251
AR v254,a124
v_mul_f32 v254, v254, v250
AW a124,v254
AR v251,a125
v_mul_f32 v251, v251, v250
AW a125,v251
AR v254,a126
v_mul_f32 v254, v254, v250
AW a126,v254
AR v251,a127
v_mul_f32 v251, v251, v250
AW a127,v251
EX v0,v0
EX v1,v1
EX v2,v2
EX v3,v3
EX v4,v4
EX v5,v5
EX v6,v6
EX v7,v7
EX v8,v8
EX v9,v9
EX v10,v10
EX v11,v11
EX v12,v12
EX v13,v13
EX v14,v14
EX v15,v15
EX v16,v16
EX v17,v17
EX v18,v18
EX v19,v19
EX v20,v20
EX v21,v21
)ASM"
R"ASM(
EX v22,v22
EX v23,v23
EX v24,v24
EX v25,v25
EX v26,v26
EX v27,v27
EX v28,v28
EX v29,v29
EX v30,v30
EX v31,v31
EX v32,v32
EX v33,v33
EX v34,v34
EX v35,v35
EX v36,v36
EX v37,v37
EX v38,v38
EX v39,v39
EX v40,v40
EX v41,v41
EX v42,v42
EX v43,v43
EX v44,v44
EX v45,v45
EX v46,v46
EX v47,v47
EX v48,v48
EX v49,v49
EX v50,v50
EX v51,v51
EX v52,v52
EX v53,v53
EX v54,v54
EX v55,v55
EX v56,v56
EX v57,v57
EX v58,v58
EX v59,v59
EX v60,v60
EX v61,v61
EX v62,v62
EX v63,v63
CV v0,v0,v1
CV v1,v2,v3
CV v2,v4,v5
CV v3,v6,v7
CV v8,v8,v9
CV v9,v10,v11
CV v10,v12,v13
CV v11,v14,v15
CV v16,v16,v17
CV v17,v18,v19
CV v18,v20,v21
CV v19,v22,v23
CV v24,v24,v25
CV v25,v26,v27
CV v26,v28,v29
CV v27,v30,v31
CV v4,v32,v33
CV v5,v34,v35
CV v6,v36,v37
CV v7,v38,v39
CV v12,v40,v41
CV v13,v42,v43
CV v14,v44,v45
CV v15,v46,v47
CV v20,v48,v49
CV v21,v50,v51
CV v22,v52,v53
CV v23,v54,v55
CV v28,v56,v57
CV v29,v58,v59
CV v30,v60,v61
CV v31,v62,v63
v_smfmac_f32_16x16x64_bf16 v[236:239], v[144:147], v[0:7], v234
v_smfmac_f32_16x16x64_bf16 v[236:239], v[148:151], v[8:15], v234
v_smfmac_f32_16x16x64_bf16 v[236:239], v[152:155], v[16:23], v234
v_smfmac_f32_16x16x64_bf16 v[236:239], v[156:159], v[24:31], v234
s_nop 9
v_add_f32 v246, v236, v237
s_cmp_eq_u32 s73, 4
s_cbranch_scc1 LBACK4_%=
s_cmp_eq_u32 s73, 6
s_cbranch_scc1 LBACK6_%=
s_cmp_eq_u32 s73, 8
s_cbranch_scc1 LBACK8_%=
s_cmp_eq_u32 s73, 10
s_cbranch_scc1 LBACK10_%=
s_cmp_eq_u32 s73, 12
s_cbranch_scc1 LBACK12_%=
s_cmp_eq_u32 s73, 14
s_cbranch_scc1 LBACK14_%=
s_cmp_eq_u32 s73, 16
s_cbranch_scc1 LBACK16_%=
s_cmp_eq_u32 s73, 18
s_cbranch_scc1 LBACK18_%=
s_cmp_eq_u32 s73, 20
s_cbranch_scc1 LBACK20_%=
s_cmp_eq_u32 s73, 22
s_cbranch_scc1 LBACK22_%=
s_cmp_eq_u32 s73, 24
s_cbranch_scc1 LBACK24_%=
s_cmp_eq_u32 s73, 26
s_cbranch_scc1 LBACK26_%=
s_cmp_eq_u32 s73, 28
s_cbranch_scc1 LBACK28_%=
s_cmp_eq_u32 s73, 30
s_cbranch_scc1 LBACK30_%=
s_endpgm
LSITE32_%=:
s_mov_b32 s73, 32
s_mov_b32 s75, 16384
s_mov_b32 s74, 0
s_branch LRARE022_%=
LSITE34_%=:
s_mov_b32 s73, 34
s_mov_b32 s75, 49152
s_mov_b32 s74, 32768
s_branch LRARE022_%=
LRARE022_%=:
s_nop 15
s_waitcnt vmcnt(0) lgkmcnt(0)
v_add_u32 v249, s74, v224
RD a[192:195],v249 offset:0
RD a[208:211],v249 offset:4096
RD a[224:227],v249 offset:8192
RD a[240:243],v249 offset:12288
v_add_u32 v250, s74, v225
RD a[196:199],v250 offset:0
RD a[212:215],v250 offset:4096
RD a[228:231],v250 offset:8192
RD a[244:247],v250 offset:12288
v_add_u32 v251, s74, v226
RD a[200:203],v251 offset:0
RD a[216:219],v251 offset:4096
RD a[232:235],v251 offset:8192
RD a[248:251],v251 offset:12288
v_add_u32 v254, s74, v227
RD a[204:207],v254 offset:0
RD a[220:223],v254 offset:4096
RD a[236:239],v254 offset:8192
RD a[252:255],v254 offset:12288
s_waitcnt lgkmcnt(0)
MM v[16:19],a[192:195],v[168:171],v[136:139]
MM v[16:19],a[196:199],v[184:187],v[16:19]
MM v[16:19],a[200:203],v[200:203],v[16:19]
MM v[16:19],a[204:207],v[216:219],v[16:19]
MM v[20:23],a[220:223],v[216:219],v[136:139]
MM v[20:23],a[216:219],v[200:203],v[20:23]
MM v[20:23],a[212:215],v[184:187],v[20:23]
MM v[20:23],a[208:211],v[168:171],v[20:23]
MM v[28:31],a[208:211],v[172:175],v[140:143]
MM v[28:31],a[212:215],v[188:191],v[28:31]
MM v[28:31],a[216:219],v[204:207],v[28:31]
MM v[28:31],a[220:223],v[220:223],v[28:31]
MM v[24:27],a[204:207],v[220:223],v[140:143]
MM v[24:27],a[200:203],v[204:207],v[24:27]
MM v[24:27],a[196:199],v[188:191],v[24:27]
MM v[24:27],a[192:195],v[172:175],v[24:27]
MM v[48:51],a[224:227],v[168:171],v[136:139]
MM v[48:51],a[228:231],v[184:187],v[48:51]
MM v[48:51],a[232:235],v[200:203],v[48:51]
MM v[48:51],a[236:239],v[216:219],v[48:51]
MM v[52:55],a[252:255],v[216:219],v[136:139]
MM v[52:55],a[248:251],v[200:203],v[52:55]
MM v[52:55],a[244:247],v[184:187],v[52:55]
MM v[52:55],a[240:243],v[168:171],v[52:55]
MM v[60:63],a[240:243],v[172:175],v[140:143]
MM v[60:63],a[244:247],v[188:191],v[60:63]
MM v[60:63],a[248:251],v[204:207],v[60:63]
MM v[60:63],a[252:255],v[220:223],v[60:63]
MM v[56:59],a[236:239],v[220:223],v[140:143]
MM v[56:59],a[232:235],v[204:207],v[56:59]
MM v[56:59],a[228:231],v[188:191],v[56:59]
MM v[56:59],a[224:227],v[172:175],v[56:59]
s_nop 15
v_add_u32 v249, s75, v224
RD a[192:195],v249 offset:0
RD a[208:211],v249 offset:4096
RD a[224:227],v249 offset:8192
RD a[240:243],v249 offset:12288
v_add_u32 v250, s75, v225
RD a[196:199],v250 offset:0
RD a[212:215],v250 offset:4096
RD a[228:231],v250 offset:8192
RD a[244:247],v250 offset:12288
v_add_u32 v251, s75, v226
RD a[200:203],v251 offset:0
RD a[216:219],v251 offset:4096
RD a[232:235],v251 offset:8192
RD a[248:251],v251 offset:12288
v_add_u32 v254, s75, v227
RD a[204:207],v254 offset:0
RD a[220:223],v254 offset:4096
RD a[236:239],v254 offset:8192
RD a[252:255],v254 offset:12288
s_waitcnt lgkmcnt(0)
v_mov_b32 v236, v247
v_mov_b32 v237, 0
v_max3_f32 v249, v16, v17, v18
v_max3_f32 v251, v48, v49, v50
v_max3_f32 v249, v249, v19, v20
v_max3_f32 v251, v251, v51, v52
v_max3_f32 v249, v249, v21, v22
v_max3_f32 v251, v251, v53, v54
v_max3_f32 v249, v249, v23, v55
v_max_f32 v249, v249, v251
v_log_f32 v250, v247
s_lshl_b64 exec, 0xffff, 32
v_max_f32 v249, v249, v250
s_mov_b64 exec, -1
v_mov_b32 v251, v249
s_nop 1
v_permlane16_swap_b32 v249, v251
v_max_f32 v249, v249, v251
v_mov_b32 v251, v249
s_nop 1
v_permlane32_swap_b32 v249, v251
v_max_f32 v249, v249, v251
v_max_f32 v249, 0, v249
v_sub_f32 v250, 0, v249
EX v250,v250
v_sub_f32 v136, v136, v249
v_sub_f32 v137, v137, v249
v_sub_f32 v138, v138, v249
v_sub_f32 v139, v139, v249
v_sub_f32 v80, v80, v249
v_sub_f32 v16, v16, v249
v_sub_f32 v81, v81, v249
v_sub_f32 v17, v17, v249
v_sub_f32 v82, v82, v249
v_sub_f32 v18, v18, v249
v_sub_f32 v83, v83, v249
v_sub_f32 v19, v19, v249
v_sub_f32 v84, v84, v249
v_sub_f32 v20, v20, v249
v_sub_f32 v85, v85, v249
v_sub_f32 v21, v21, v249
v_sub_f32 v86, v86, v249
v_sub_f32 v22, v22, v249
v_sub_f32 v87, v87, v249
v_sub_f32 v23, v23, v249
v_sub_f32 v112, v112, v249
v_sub_f32 v48, v48, v249
v_sub_f32 v113, v113, v249
v_sub_f32 v49, v49, v249
v_sub_f32 v114, v114, v249
v_sub_f32 v50, v50, v249
v_sub_f32 v115, v115, v249
v_sub_f32 v51, v51, v249
v_sub_f32 v116, v116, v249
v_sub_f32 v52, v52, v249
v_sub_f32 v117, v117, v249
v_sub_f32 v53, v53, v249
v_sub_f32 v118, v118, v249
v_sub_f32 v54, v54, v249
v_sub_f32 v119, v119, v249
v_sub_f32 v55, v55, v249
s_lshl_b64 exec, 0xffff, 32
v_mul_f32 v236, v236, v250
s_mov_b64 exec, -1
AR v254,a16
v_mul_f32 v254, v254, v250
AW a16,v254
AR v251,a17
v_mul_f32 v251, v251, v250
AW a17,v251
AR v254,a18
v_mul_f32 v254, v254, v250
AW a18,v254
AR v251,a19
v_mul_f32 v251, v251, v250
AW a19,v251
AR v254,a20
v_mul_f32 v254, v254, v250
AW a20,v254
AR v251,a21
v_mul_f32 v251, v251, v250
AW a21,v251
AR v254,a22
v_mul_f32 v254, v254, v250
AW a22,v254
AR v251,a23
v_mul_f32 v251, v251, v250
AW a23,v251
AR v254,a48
v_mul_f32 v254, v254, v250
AW a48,v254
AR v251,a49
v_mul_f32 v251, v251, v250
AW a49,v251
AR v254,a50
v_mul_f32 v254, v254, v250
AW a50,v254
AR v251,a51
v_mul_f32 v251, v251, v250
AW a51,v251
AR v254,a52
v_mul_f32 v254, v254, v250
AW a52,v254
AR v251,a53
v_mul_f32 v251, v251, v250
AW a53,v251
AR v254,a54
v_mul_f32 v254, v254, v250
AW a54,v254
AR v251,a55
v_mul_f32 v251, v251, v250
AW a55,v251
AR v254,a80
v_mul_f32 v254, v254, v250
AW a80,v254
AR v251,a81
v_mul_f32 v251, v251, v250
AW a81,v251
AR v254,a82
v_mul_f32 v254, v254, v250
AW a82,v254
AR v251,a83
v_mul_f32 v251, v251, v250
AW a83,v251
AR v254,a84
v_mul_f32 v254, v254, v250
AW a84,v254
AR v251,a85
v_mul_f32 v251, v251, v250
AW a85,v251
AR v254,a86
v_mul_f32 v254, v254, v250
AW a86,v254
AR v251,a87
v_mul_f32 v251, v251, v250
AW a87,v251
AR v254,a112
v_mul_f32 v254, v254, v250
AW a112,v254
AR v251,a113
v_mul_f32 v251, v251, v250
AW a113,v251
AR v254,a114
v_mul_f32 v254, v254, v250
AW a114,v254
AR v251,a115
v_mul_f32 v251, v251, v250
AW a115,v251
AR v254,a116
v_mul_f32 v254, v254, v250
AW a116,v254
AR v251,a117
v_mul_f32 v251, v251, v250
AW a117,v251
AR v254,a118
v_mul_f32 v254, v254, v250
AW a118,v254
AR v251,a119
v_mul_f32 v251, v251, v250
AW a119,v251
v_max3_f32 v249, v24, v25, v26
v_max3_f32 v251, v56, v57, v58
v_max3_f32 v249, v249, v27, v28
v_max3_f32 v251, v251, v59, v60
v_max3_f32 v249, v249, v29, v30
v_max3_f32 v251, v251, v61, v62
v_max3_f32 v249, v249, v31, v63
v_max_f32 v249, v249, v251
v_log_f32 v250, v247
s_lshl_b64 exec, 0xffff, 48
v_max_f32 v249, v249, v250
s_mov_b64 exec, -1
v_mov_b32 v251, v249
s_nop 1
v_permlane16_swap_b32 v249, v251
v_max_f32 v249, v249, v251
v_mov_b32 v251, v249
s_nop 1
v_permlane32_swap_b32 v249, v251
v_max_f32 v249, v249, v251
v_max_f32 v249, 0, v249
v_sub_f32 v250, 0, v249
EX v250,v250
v_sub_f32 v140, v140, v249
v_sub_f32 v141, v141, v249
v_sub_f32 v142, v142, v249
v_sub_f32 v143, v143, v249
v_sub_f32 v88, v88, v249
v_sub_f32 v24, v24, v249
v_sub_f32 v89, v89, v249
v_sub_f32 v25, v25, v249
v_sub_f32 v90, v90, v249
v_sub_f32 v26, v26, v249
v_sub_f32 v91, v91, v249
v_sub_f32 v27, v27, v249
v_sub_f32 v92, v92, v249
v_sub_f32 v28, v28, v249
v_sub_f32 v93, v93, v249
v_sub_f32 v29, v29, v249
v_sub_f32 v94, v94, v249
v_sub_f32 v30, v30, v249
v_sub_f32 v95, v95, v249
v_sub_f32 v31, v31, v249
v_sub_f32 v120, v120, v249
v_sub_f32 v56, v56, v249
v_sub_f32 v121, v121, v249
v_sub_f32 v57, v57, v249
v_sub_f32 v122, v122, v249
v_sub_f32 v58, v58, v249
v_sub_f32 v123, v123, v249
v_sub_f32 v59, v59, v249
v_sub_f32 v124, v124, v249
v_sub_f32 v60, v60, v249
v_sub_f32 v125, v125, v249
v_sub_f32 v61, v61, v249
v_sub_f32 v126, v126, v249
v_sub_f32 v62, v62, v249
v_sub_f32 v127, v127, v249
v_sub_f32 v63, v63, v249
s_lshl_b64 exec, 0xffff, 48
v_mul_f32 v236, v236, v250
s_mov_b64 exec, -1
AR v254,a24
v_mul_f32 v254, v254, v250
AW a24,v254
AR v251,a25
v_mul_f32 v251, v251, v250
AW a25,v251
AR v254,a26
v_mul_f32 v254, v254, v250
AW a26,v254
AR v251,a27
v_mul_f32 v251, v251, v250
AW a27,v251
AR v254,a28
v_mul_f32 v254, v254, v250
AW a28,v254
AR v251,a29
v_mul_f32 v251, v251, v250
AW a29,v251
AR v254,a30
v_mul_f32 v254, v254, v250
AW a30,v254
AR v251,a31
v_mul_f32 v251, v251, v250
AW a31,v251
AR v254,a56
v_mul_f32 v254, v254, v250
AW a56,v254
AR v251,a57
v_mul_f32 v251, v251, v250
AW a57,v251
AR v254,a58
v_mul_f32 v254, v254, v250
AW a58,v254
AR v251,a59
v_mul_f32 v251, v251, v250
AW a59,v251
AR v254,a60
v_mul_f32 v254, v254, v250
AW a60,v254
AR v251,a61
v_mul_f32 v251, v251, v250
AW a61,v251
AR v254,a62
v_mul_f32 v254, v254, v250
AW a62,v254
AR v251,a63
v_mul_f32 v251, v251, v250
AW a63,v251
AR v254,a88
v_mul_f32 v254, v254, v250
AW a88,v254
AR v251,a89
v_mul_f32 v251, v251, v250
AW a89,v251
AR v254,a90
v_mul_f32 v254, v254, v250
AW a90,v254
AR v251,a91
v_mul_f32 v251, v251, v250
AW a91,v251
AR v254,a92
v_mul_f32 v254, v254, v250
AW a92,v254
AR v251,a93
v_mul_f32 v251, v251, v250
AW a93,v251
AR v254,a94
v_mul_f32 v254, v254, v250
AW a94,v254
AR v251,a95
v_mul_f32 v251, v251, v250
AW a95,v251
AR v254,a120
v_mul_f32 v254, v254, v250
AW a120,v254
AR v251,a121
v_mul_f32 v251, v251, v250
AW a121,v251
AR v254,a122
v_mul_f32 v254, v254, v250
AW a122,v254
AR v251,a123
v_mul_f32 v251, v251, v250
AW a123,v251
AR v254,a124
v_mul_f32 v254, v254, v250
AW a124,v254
AR v251,a125
v_mul_f32 v251, v251, v250
AW a125,v251
AR v254,a126
v_mul_f32 v254, v254, v250
AW a126,v254
AR v251,a127
v_mul_f32 v251, v251, v250
AW a127,v251
EX v16,v16
EX v17,v17
EX v18,v18
EX v19,v19
EX v20,v20
EX v21,v21
EX v22,v22
EX v23,v23
EX v24,v24
EX v25,v25
EX v26,v26
EX v27,v27
EX v28,v28
EX v29,v29
EX v30,v30
EX v31,v31
EX v48,v48
EX v49,v49
EX v50,v50
EX v51,v51
EX v52,v52
EX v53,v53
EX v54,v54
EX v55,v55
EX v56,v56
EX v57,v57
EX v58,v58
EX v59,v59
EX v60,v60
EX v61,v61
EX v62,v62
EX v63,v63
CV v16,v16,v17
CV v17,v18,v19
CV v18,v20,v21
CV v19,v22,v23
CV v24,v24,v25
CV v25,v26,v27
CV v26,v28,v29
CV v27,v30,v31
CV v20,v48,v49
CV v21,v50,v51
CV v22,v52,v53
CV v23,v54,v55
CV v28,v56,v57
CV v29,v58,v59
CV v30,v60,v61
CV v31,v62,v63
v_smfmac_f32_16x16x64_bf16 v[236:239], v[152:155], v[16:23], v234
s_nop 0
v_smfmac_f32_16x16x64_bf16 v[236:239], v[156:159], v[24:31], v234
s_nop 9
v_add_f32 v246, v236, v237
)ASM"
R"ASM(
s_cmp_eq_u32 s73, 32
s_cbranch_scc1 LBACK32_%=
s_cmp_eq_u32 s73, 34
s_cbranch_scc1 LBACK34_%=
s_endpgm
LSITE33_%=:
s_mov_b32 s73, 33
s_mov_b32 s75, 32768
s_mov_b32 s74, 16384
s_branch LRARE122_%=
LRARE122_%=:
s_nop 15
s_waitcnt vmcnt(0) lgkmcnt(0)
v_add_u32 v249, s74, v224
RD a[192:195],v249 offset:0
RD a[208:211],v249 offset:4096
RD a[224:227],v249 offset:8192
RD a[240:243],v249 offset:12288
v_add_u32 v250, s74, v225
RD a[196:199],v250 offset:0
RD a[212:215],v250 offset:4096
RD a[228:231],v250 offset:8192
RD a[244:247],v250 offset:12288
v_add_u32 v251, s74, v226
RD a[200:203],v251 offset:0
RD a[216:219],v251 offset:4096
RD a[232:235],v251 offset:8192
RD a[248:251],v251 offset:12288
v_add_u32 v254, s74, v227
RD a[204:207],v254 offset:0
RD a[220:223],v254 offset:4096
RD a[236:239],v254 offset:8192
RD a[252:255],v254 offset:12288
s_waitcnt lgkmcnt(0)
MM v[80:83],a[192:195],v[168:171],v[136:139]
MM v[80:83],a[196:199],v[184:187],v[80:83]
MM v[80:83],a[200:203],v[200:203],v[80:83]
MM v[80:83],a[204:207],v[216:219],v[80:83]
MM v[84:87],a[220:223],v[216:219],v[136:139]
MM v[84:87],a[216:219],v[200:203],v[84:87]
MM v[84:87],a[212:215],v[184:187],v[84:87]
MM v[84:87],a[208:211],v[168:171],v[84:87]
MM v[92:95],a[208:211],v[172:175],v[140:143]
MM v[92:95],a[212:215],v[188:191],v[92:95]
MM v[92:95],a[216:219],v[204:207],v[92:95]
MM v[92:95],a[220:223],v[220:223],v[92:95]
MM v[88:91],a[204:207],v[220:223],v[140:143]
MM v[88:91],a[200:203],v[204:207],v[88:91]
MM v[88:91],a[196:199],v[188:191],v[88:91]
MM v[88:91],a[192:195],v[172:175],v[88:91]
MM v[112:115],a[224:227],v[168:171],v[136:139]
MM v[112:115],a[228:231],v[184:187],v[112:115]
MM v[112:115],a[232:235],v[200:203],v[112:115]
MM v[112:115],a[236:239],v[216:219],v[112:115]
MM v[116:119],a[252:255],v[216:219],v[136:139]
MM v[116:119],a[248:251],v[200:203],v[116:119]
MM v[116:119],a[244:247],v[184:187],v[116:119]
MM v[116:119],a[240:243],v[168:171],v[116:119]
MM v[124:127],a[240:243],v[172:175],v[140:143]
MM v[124:127],a[244:247],v[188:191],v[124:127]
MM v[124:127],a[248:251],v[204:207],v[124:127]
MM v[124:127],a[252:255],v[220:223],v[124:127]
MM v[120:123],a[236:239],v[220:223],v[140:143]
MM v[120:123],a[232:235],v[204:207],v[120:123]
MM v[120:123],a[228:231],v[188:191],v[120:123]
MM v[120:123],a[224:227],v[172:175],v[120:123]
s_nop 15
v_add_u32 v249, s75, v224
RD a[192:195],v249 offset:0
RD a[208:211],v249 offset:4096
RD a[224:227],v249 offset:8192
RD a[240:243],v249 offset:12288
v_add_u32 v250, s75, v225
RD a[196:199],v250 offset:0
RD a[212:215],v250 offset:4096
RD a[228:231],v250 offset:8192
RD a[244:247],v250 offset:12288
v_add_u32 v251, s75, v226
RD a[200:203],v251 offset:0
RD a[216:219],v251 offset:4096
RD a[232:235],v251 offset:8192
RD a[248:251],v251 offset:12288
v_add_u32 v254, s75, v227
RD a[204:207],v254 offset:0
RD a[220:223],v254 offset:4096
RD a[236:239],v254 offset:8192
RD a[252:255],v254 offset:12288
s_waitcnt lgkmcnt(0)
v_mov_b32 v236, v246
v_mov_b32 v237, 0
v_max3_f32 v249, v80, v81, v82
v_max3_f32 v251, v112, v113, v114
v_max3_f32 v249, v249, v83, v84
v_max3_f32 v251, v251, v115, v116
v_max3_f32 v249, v249, v85, v86
v_max3_f32 v251, v251, v117, v118
v_max3_f32 v249, v249, v87, v119
v_max_f32 v249, v249, v251
v_log_f32 v250, v246
s_lshl_b64 exec, 0xffff, 32
v_max_f32 v249, v249, v250
s_mov_b64 exec, -1
v_mov_b32 v251, v249
s_nop 1
v_permlane16_swap_b32 v249, v251
v_max_f32 v249, v249, v251
v_mov_b32 v251, v249
s_nop 1
v_permlane32_swap_b32 v249, v251
v_max_f32 v249, v249, v251
v_max_f32 v249, 0, v249
v_sub_f32 v250, 0, v249
EX v250,v250
v_sub_f32 v136, v136, v249
v_sub_f32 v137, v137, v249
v_sub_f32 v138, v138, v249
v_sub_f32 v139, v139, v249
v_sub_f32 v16, v16, v249
v_sub_f32 v80, v80, v249
v_sub_f32 v17, v17, v249
v_sub_f32 v81, v81, v249
v_sub_f32 v18, v18, v249
v_sub_f32 v82, v82, v249
v_sub_f32 v19, v19, v249
v_sub_f32 v83, v83, v249
v_sub_f32 v20, v20, v249
v_sub_f32 v84, v84, v249
v_sub_f32 v21, v21, v249
v_sub_f32 v85, v85, v249
v_sub_f32 v22, v22, v249
v_sub_f32 v86, v86, v249
v_sub_f32 v23, v23, v249
v_sub_f32 v87, v87, v249
v_sub_f32 v48, v48, v249
v_sub_f32 v112, v112, v249
v_sub_f32 v49, v49, v249
v_sub_f32 v113, v113, v249
v_sub_f32 v50, v50, v249
v_sub_f32 v114, v114, v249
v_sub_f32 v51, v51, v249
v_sub_f32 v115, v115, v249
v_sub_f32 v52, v52, v249
v_sub_f32 v116, v116, v249
v_sub_f32 v53, v53, v249
v_sub_f32 v117, v117, v249
v_sub_f32 v54, v54, v249
v_sub_f32 v118, v118, v249
v_sub_f32 v55, v55, v249
v_sub_f32 v119, v119, v249
s_lshl_b64 exec, 0xffff, 32
v_mul_f32 v236, v236, v250
s_mov_b64 exec, -1
AR v254,a16
v_mul_f32 v254, v254, v250
AW a16,v254
AR v251,a17
v_mul_f32 v251, v251, v250
AW a17,v251
AR v254,a18
v_mul_f32 v254, v254, v250
AW a18,v254
AR v251,a19
v_mul_f32 v251, v251, v250
AW a19,v251
AR v254,a20
v_mul_f32 v254, v254, v250
AW a20,v254
AR v251,a21
v_mul_f32 v251, v251, v250
AW a21,v251
AR v254,a22
v_mul_f32 v254, v254, v250
AW a22,v254
AR v251,a23
v_mul_f32 v251, v251, v250
AW a23,v251
AR v254,a48
v_mul_f32 v254, v254, v250
AW a48,v254
AR v251,a49
v_mul_f32 v251, v251, v250
AW a49,v251
AR v254,a50
v_mul_f32 v254, v254, v250
AW a50,v254
AR v251,a51
v_mul_f32 v251, v251, v250
AW a51,v251
AR v254,a52
v_mul_f32 v254, v254, v250
AW a52,v254
AR v251,a53
v_mul_f32 v251, v251, v250
AW a53,v251
AR v254,a54
v_mul_f32 v254, v254, v250
AW a54,v254
AR v251,a55
v_mul_f32 v251, v251, v250
AW a55,v251
AR v254,a80
v_mul_f32 v254, v254, v250
AW a80,v254
AR v251,a81
v_mul_f32 v251, v251, v250
AW a81,v251
AR v254,a82
v_mul_f32 v254, v254, v250
AW a82,v254
AR v251,a83
v_mul_f32 v251, v251, v250
AW a83,v251
AR v254,a84
v_mul_f32 v254, v254, v250
AW a84,v254
AR v251,a85
v_mul_f32 v251, v251, v250
AW a85,v251
AR v254,a86
v_mul_f32 v254, v254, v250
AW a86,v254
AR v251,a87
v_mul_f32 v251, v251, v250
AW a87,v251
AR v254,a112
v_mul_f32 v254, v254, v250
AW a112,v254
AR v251,a113
v_mul_f32 v251, v251, v250
AW a113,v251
AR v254,a114
v_mul_f32 v254, v254, v250
AW a114,v254
AR v251,a115
v_mul_f32 v251, v251, v250
AW a115,v251
AR v254,a116
v_mul_f32 v254, v254, v250
AW a116,v254
AR v251,a117
v_mul_f32 v251, v251, v250
AW a117,v251
AR v254,a118
v_mul_f32 v254, v254, v250
AW a118,v254
AR v251,a119
v_mul_f32 v251, v251, v250
AW a119,v251
v_max3_f32 v249, v88, v89, v90
v_max3_f32 v251, v120, v121, v122
v_max3_f32 v249, v249, v91, v92
v_max3_f32 v251, v251, v123, v124
v_max3_f32 v249, v249, v93, v94
v_max3_f32 v251, v251, v125, v126
v_max3_f32 v249, v249, v95, v127
v_max_f32 v249, v249, v251
v_log_f32 v250, v246
s_lshl_b64 exec, 0xffff, 48
v_max_f32 v249, v249, v250
s_mov_b64 exec, -1
v_mov_b32 v251, v249
s_nop 1
v_permlane16_swap_b32 v249, v251
v_max_f32 v249, v249, v251
v_mov_b32 v251, v249
s_nop 1
v_permlane32_swap_b32 v249, v251
v_max_f32 v249, v249, v251
v_max_f32 v249, 0, v249
v_sub_f32 v250, 0, v249
EX v250,v250
v_sub_f32 v140, v140, v249
v_sub_f32 v141, v141, v249
v_sub_f32 v142, v142, v249
v_sub_f32 v143, v143, v249
v_sub_f32 v24, v24, v249
v_sub_f32 v88, v88, v249
v_sub_f32 v25, v25, v249
v_sub_f32 v89, v89, v249
v_sub_f32 v26, v26, v249
v_sub_f32 v90, v90, v249
v_sub_f32 v27, v27, v249
v_sub_f32 v91, v91, v249
v_sub_f32 v28, v28, v249
v_sub_f32 v92, v92, v249
v_sub_f32 v29, v29, v249
v_sub_f32 v93, v93, v249
v_sub_f32 v30, v30, v249
v_sub_f32 v94, v94, v249
v_sub_f32 v31, v31, v249
v_sub_f32 v95, v95, v249
v_sub_f32 v56, v56, v249
v_sub_f32 v120, v120, v249
v_sub_f32 v57, v57, v249
v_sub_f32 v121, v121, v249
v_sub_f32 v58, v58, v249
v_sub_f32 v122, v122, v249
v_sub_f32 v59, v59, v249
v_sub_f32 v123, v123, v249
v_sub_f32 v60, v60, v249
v_sub_f32 v124, v124, v249
v_sub_f32 v61, v61, v249
v_sub_f32 v125, v125, v249
v_sub_f32 v62, v62, v249
v_sub_f32 v126, v126, v249
v_sub_f32 v63, v63, v249
v_sub_f32 v127, v127, v249
s_lshl_b64 exec, 0xffff, 48
v_mul_f32 v236, v236, v250
s_mov_b64 exec, -1
AR v254,a24
v_mul_f32 v254, v254, v250
AW a24,v254
AR v251,a25
v_mul_f32 v251, v251, v250
AW a25,v251
AR v254,a26
v_mul_f32 v254, v254, v250
AW a26,v254
AR v251,a27
v_mul_f32 v251, v251, v250
AW a27,v251
AR v254,a28
v_mul_f32 v254, v254, v250
AW a28,v254
AR v251,a29
v_mul_f32 v251, v251, v250
AW a29,v251
AR v254,a30
v_mul_f32 v254, v254, v250
AW a30,v254
AR v251,a31
v_mul_f32 v251, v251, v250
AW a31,v251
AR v254,a56
v_mul_f32 v254, v254, v250
AW a56,v254
AR v251,a57
v_mul_f32 v251, v251, v250
AW a57,v251
AR v254,a58
v_mul_f32 v254, v254, v250
AW a58,v254
AR v251,a59
v_mul_f32 v251, v251, v250
AW a59,v251
AR v254,a60
v_mul_f32 v254, v254, v250
AW a60,v254
AR v251,a61
v_mul_f32 v251, v251, v250
AW a61,v251
AR v254,a62
v_mul_f32 v254, v254, v250
AW a62,v254
AR v251,a63
v_mul_f32 v251, v251, v250
AW a63,v251
AR v254,a88
v_mul_f32 v254, v254, v250
AW a88,v254
AR v251,a89
v_mul_f32 v251, v251, v250
AW a89,v251
AR v254,a90
v_mul_f32 v254, v254, v250
AW a90,v254
AR v251,a91
v_mul_f32 v251, v251, v250
AW a91,v251
AR v254,a92
v_mul_f32 v254, v254, v250
AW a92,v254
AR v251,a93
v_mul_f32 v251, v251, v250
AW a93,v251
AR v254,a94
v_mul_f32 v254, v254, v250
AW a94,v254
AR v251,a95
v_mul_f32 v251, v251, v250
AW a95,v251
AR v254,a120
v_mul_f32 v254, v254, v250
AW a120,v254
AR v251,a121
v_mul_f32 v251, v251, v250
AW a121,v251
AR v254,a122
v_mul_f32 v254, v254, v250
AW a122,v254
AR v251,a123
v_mul_f32 v251, v251, v250
AW a123,v251
AR v254,a124
v_mul_f32 v254, v254, v250
AW a124,v254
AR v251,a125
v_mul_f32 v251, v251, v250
AW a125,v251
AR v254,a126
v_mul_f32 v254, v254, v250
AW a126,v254
AR v251,a127
v_mul_f32 v251, v251, v250
AW a127,v251
EX v80,v80
EX v81,v81
EX v82,v82
EX v83,v83
EX v84,v84
EX v85,v85
EX v86,v86
EX v87,v87
EX v88,v88
EX v89,v89
EX v90,v90
EX v91,v91
EX v92,v92
EX v93,v93
EX v94,v94
EX v95,v95
EX v112,v112
EX v113,v113
EX v114,v114
EX v115,v115
EX v116,v116
EX v117,v117
EX v118,v118
EX v119,v119
EX v120,v120
EX v121,v121
EX v122,v122
EX v123,v123
EX v124,v124
EX v125,v125
EX v126,v126
EX v127,v127
CV v80,v80,v81
CV v81,v82,v83
CV v82,v84,v85
CV v83,v86,v87
CV v88,v88,v89
CV v89,v90,v91
CV v90,v92,v93
CV v91,v94,v95
CV v84,v112,v113
CV v85,v114,v115
CV v86,v116,v117
CV v87,v118,v119
CV v92,v120,v121
CV v93,v122,v123
CV v94,v124,v125
CV v95,v126,v127
v_smfmac_f32_16x16x64_bf16 v[236:239], v[152:155], v[80:87], v234
s_nop 0
v_smfmac_f32_16x16x64_bf16 v[236:239], v[156:159], v[88:95], v234
s_nop 9
v_add_f32 v247, v236, v237
s_cmp_eq_u32 s73, 33
s_cbranch_scc1 LBACK33_%=
s_endpgm
LEND_%=:
)ASM"

    :: [ka] "s"(__builtin_amdgcn_kernarg_segment_ptr()), [slds] "s"(slds), [swave] "s"(w), [sb] "s"(blockIdx.x)
    : "memory", "vcc", "scc", CLOB_V, CLOB_A, CLOB_S);
}

extern "C" void kernel_launch(void* const* d_in, const int* in_sizes, int n_in, void* d_out, int out_size, void* d_ws, size_t ws_size, hipStream_t stream) {
  hipFuncSetAttribute((const void*)ka_fused, hipFuncAttributeMaxDynamicSharedMemorySize, 163840);
  ka_fused<<<256, 256, 163840, stream>>>((const unsigned short*)d_in[0], (const unsigned short*)d_in[1], (const unsigned short*)d_in[2], (const unsigned short*)d_in[3],
                                         (const unsigned short*)d_in[4], (const unsigned short*)d_in[6], (const unsigned short*)d_in[7], (const unsigned short*)d_in[8],
                                         (const unsigned short*)d_in[9], (char*)d_ws, (unsigned short*)d_out
                                         );
}
```
